```python
import torch

def _softmax(x, axis=-1):
    return torch.softmax(x, dim=axis)
def _gelu(x):
    return torch.nn.functional.gelu(x, approximate="tanh")
def _silu(x):
    return torch.nn.functional.silu(x)
def _sigmoid(x):
    return torch.sigmoid(x)
def _layernorm(x, axis=-1, eps=1e-5):
    m = x.mean(dim=axis, keepdim=True)
    v = x.var(dim=axis, keepdim=True, unbiased=False)
    return (x - m) / torch.sqrt(v + eps)
def _scatter_add(tgt, idx, src, axis=0):
    return tgt.clone().index_add_(axis, idx, src)
def _segment_sum(src, seg, n):
    out = torch.zeros((n,) + src.shape[1:], dtype=src.dtype)
    return out.index_add_(0, seg, src)
def _pad(x, pw):
    flat = tuple(v for pair in reversed(pw) for v in pair)
    return torch.nn.functional.pad(x, flat)

kernel_name = 'sequential_preLN__ssm_conv_s4__moe_arch_mod__prompt'

def setup_inputs():

    _w = (lambda a: (lambda t: t.to(torch.bfloat16) if t.is_floating_point() else t)(torch.as_tensor(a)))
    def _A_setup():
        B, L, D, N = 1, 2048, 576, 128
        A_re = -(lambda _x: torch.exp(torch.as_tensor(_x)))((lambda *s: torch.randn(*s, dtype=torch.bfloat16) * 1.6)(D, N))
        A_im = torch.pi * (lambda *s: torch.randn(*s, dtype=torch.bfloat16) * 0.2)(D, N)
        B_re = (lambda *s: torch.randn(*s, dtype=torch.bfloat16) * 0.2)(D, N)
        B_im = (lambda *s: torch.randn(*s, dtype=torch.bfloat16) * 0.2)(D, N)
        C_re = (lambda *s: torch.randn(*s, dtype=torch.bfloat16) * 0.2)(D, N)
        C_im = (lambda *s: torch.randn(*s, dtype=torch.bfloat16) * 0.2)(D, N)
        log_dt = (lambda *s: torch.randn(*s, dtype=torch.bfloat16) * 0.2)(D) * 0.5 - 4.0
        Dskip = (lambda *s: torch.randn(*s, dtype=torch.bfloat16) * 0.2)(D)
        return {
            "u": _w((lambda *s: torch.randn(*s, dtype=torch.bfloat16) * 0.05)(B, L, D)),
            "A_re": _w(A_re),
            "A_im": _w(A_im),
            "B_re": _w(B_re),
            "B_im": _w(B_im),
            "C_re": _w(C_re),
            "C_im": _w(C_im),
            "log_dt": _w(log_dt),
            "Dskip": _w(Dskip),
        }
    _A_in = _A_setup()

    def _B_setup():
        B, S, D, F, C = 1, 2048, 576, 4608, 8
        return {
            "x": _w((lambda *s: torch.randn(*s, dtype=torch.bfloat16) * 0.05)(B, S, D)),
            "w_r": _w((lambda *s: torch.randn(*s, dtype=torch.bfloat16) * 0.2)(D)),
            "w_gate": _w((lambda *s: torch.randn(*s, dtype=torch.bfloat16) * 0.2)(D, F)),
            "w_up": _w((lambda *s: torch.randn(*s, dtype=torch.bfloat16) * 0.2)(D, F)),
            "w_down": _w((lambda *s: torch.randn(*s, dtype=torch.bfloat16) * 0.2)(F, D)),
        }
    _B_in = _B_setup()

    return {
        "A_u": _A_in["u"],
        "A_A_re": _A_in["A_re"],
        "A_A_im": _A_in["A_im"],
        "A_B_re": _A_in["B_re"],
        "A_B_im": _A_in["B_im"],
        "A_C_re": _A_in["C_re"],
        "A_C_im": _A_in["C_im"],
        "A_log_dt": _A_in["log_dt"],
        "A_Dskip": _A_in["Dskip"],
        "B_x": _B_in["x"],
        "B_w_r": _B_in["w_r"],
        "B_w_gate": _B_in["w_gate"],
        "B_w_up": _B_in["w_up"],
        "B_w_down": _B_in["w_down"],
    }

def reference(A_u, A_A_re, A_A_im, A_B_re, A_B_im, A_C_re, A_C_im, A_log_dt, A_Dskip, B_x, B_w_r, B_w_gate, B_w_up, B_w_down):
    def _A_ref(u, A_re, A_im, B_re, B_im, C_re, C_im, log_dt, Dskip):
        Bsz, L, D = u.shape
        dt = torch.exp(log_dt)[:, None]
        A = A_re + 1j * A_im
        Bc = B_re + 1j * B_im
        Cc = C_re + 1j * C_im
        logdA = dt * A
        dB = dt * Bc
        l = (lambda x, dt: x.to(dt))(torch.arange(L), A_re.dtype)
        P = torch.exp(logdA[:, :, None] * l[None, None, :])
        K = torch.einsum("dn,dnl,dn->dl", Cc, P, dB).real
        L2 = 2 * L
        Kp = _pad(K, ((0, 0), (0, L)))
        up = (lambda x, *d: x.permute(*d))(u, 0, 2, 1)
        up = _pad(up, ((0, 0), (0, 0), (0, L)))
        Kf = (lambda x, axes=(-2,-1): torch.fft.rfft2(x.float(), dim=axes))(Kp[:, None, :], axes=(-2, -1))
        Uf = (lambda x, axes=(-2,-1): torch.fft.rfft2(x.float(), dim=axes))(up[:, :, None, :], axes=(-2, -1))
        Yf = Uf * Kf[None, :, :, :]
        y = (lambda x, s, axes=(-2,-1): torch.fft.irfft2(x, s=s, dim=axes).to(torch.bfloat16))(Yf, (1, L2), axes=(-2, -1))[:, :, 0, :L]
        y = (lambda x, *d: x.permute(*d))(y, 0, 2, 1) + u * Dskip[None, None, :]
        return y

    def _B_ref(x, w_r, w_gate, w_up, w_down):
        B, S, D = x.shape
        C = 8
        r = x @ w_r
        idx = torch.argsort(-r, axis=-1)[:, :C]
        idx_e = (lambda x, n, axis: torch.repeat_interleave(x, n, dim=axis))(idx.reshape(B, C, 1), D, axis=2)
        x_sel = (lambda x, i, axis: torch.take_along_dim(x, i, dim=axis))(x, idx_e, axis=1)
        g = _silu(x_sel @ w_gate)
        u = x_sel @ w_up
        y = (g * u) @ w_down
        r_sel = _sigmoid((lambda x, i, axis: torch.take_along_dim(x, i, dim=axis))(r, idx, axis=1))
        y = y * r_sel.reshape(B, C, 1)
        b_off = (torch.arange(B) * S).reshape(B, 1)
        flat_idx = (idx + b_off).reshape(B * C)
        zero = (x * 0.0).reshape(B * S, D)
        upd = _scatter_add(zero, flat_idx, y.reshape(B * C, D), axis=0)
        return x + upd.reshape(B, S, D)

    x = A_u
    h = x + _A_ref(_layernorm(x, axis=-1), A_A_re, A_A_im, A_B_re, A_B_im, A_C_re, A_C_im, A_log_dt, A_Dskip)
    h2 = h + _B_ref(_layernorm(h, axis=-1), B_w_r, B_w_gate, B_w_up, B_w_down)
    return h2
```

```cpp
#include <hip/hip_runtime.h>
#include <hip/hip_bf16.h>
#include <stdint.h>

typedef __attribute__((ext_vector_type(8))) short bf16x8;
typedef __attribute__((ext_vector_type(4))) float f32x4;
typedef uint16_t u16;
typedef __attribute__((ext_vector_type(4))) uint32_t u32x4;
typedef __attribute__((ext_vector_type(2))) uint32_t u32x2;
#define TSD
#define TS(k, i)
#define TSC(i)
#define TSE()
#define TSP
#define TSA

#define VDENSE 0
constexpr int L_ = 2048, D_ = 576, N_ = 128, F_ = 4608, C_ = 8;
constexpr int NF = 144;
constexpr uint32_t O_EP = 0;
constexpr uint32_t O_KF = 4096;
constexpr uint32_t O_KEY = O_KF + 8192;
constexpr uint32_t O_SV = O_KEY + 32768;
constexpr uint32_t O_SU = O_SV + 8 * 320 * 128;
constexpr uint32_t O_SG = O_SU + 262144;
constexpr uint32_t O_FB = O_SG + 16384;
constexpr uint32_t O_XC = O_FB + 8 * 1152;
constexpr uint32_t SP = 9216 + 128;
constexpr uint32_t O_SL = O_XC + 1024 * 1152;
constexpr uint32_t SLU = 336;
constexpr uint32_t O_U = O_SL + 256 * SLU * 16;
constexpr int UP = 584;
constexpr uint32_t VP = 8192 + 128, VPP = 4096 + 128;
constexpr uint32_t O_V = O_U + 256 * UP * 16, O_VP = O_V + 256 * VP;
constexpr int GQP = 800;
constexpr uint32_t O_GQ = O_VP + 64 * VPP;
static_assert(O_GQ - O_V == 256 * 586 * 16 && 112 * 768 * 16 >= NF * SP && 112 * 1320 >= L_ * D_ / 8 && 256 * SLU * 16 >= NF * SP && O_SL % 128 == 0 && O_U % 128 == 0 && O_GQ % 128 == 0, "");
__host__ __device__ __forceinline__ uint32_t u_at(int tg, int d) { return O_U + 16u * (uint32_t)((tg >> 1) * (UP * 2) + (d >> 2) * 8 + (tg & 1) * 4 + (d & 3)); }
__host__ __device__ __forceinline__ uint32_t v_at(int d, int tg) {
  if (!VDENSE) return (d < 512) ? O_V + (uint32_t)(d >> 1) * VP + 16u * (uint32_t)(2 * tg + (d & 1)) : O_VP + (uint32_t)(d - 512) * VPP + 16u * (uint32_t)tg;
  return (d < 512) ? O_V + (uint32_t)(d >> 2) * (2 * VP - 128) + 16u * (uint32_t)(8 * (tg >> 1) + 2 * (d & 2) + 2 * (tg & 1) + (d & 1))
                   : O_VP + (uint32_t)((d - 512) >> 1) * (2 * VPP - 128) + 16u * (uint32_t)(8 * (tg >> 2) + 4 * (d & 1) + (tg & 3));
}
#define HAVE_VAT
__device__ __forceinline__ float bf2f(u16 v) { return __uint_as_float((uint32_t)v << 16); }
__device__ __forceinline__ float bflo(uint32_t v) { return __uint_as_float(v << 16); }
__device__ __forceinline__ float bfhi(uint32_t v) { return __uint_as_float(v & 0xffff0000u); }
__device__ __forceinline__ u16 f2bf(float f) { return __builtin_bit_cast(u16, (__bf16)f); }
__device__ __forceinline__ float rbf(float f) { return bf2f(f2bf(f)); }
typedef __attribute__((ext_vector_type(2))) float f32x2;
typedef __attribute__((ext_vector_type(2))) __bf16 bf16x2v;
__device__ __forceinline__ uint32_t pack2(float a, float b) {
  return __builtin_bit_cast(uint32_t, __builtin_convertvector((f32x2){a, b}, bf16x2v));
}

#define DPP_I(v, ctrl, rmask) __builtin_amdgcn_update_dpp(0, (v), (ctrl), (rmask), 0xf, true)
__device__ __forceinline__ float row_sum(float v) {
  v += __int_as_float(DPP_I(__float_as_int(v), 0x111, 0xf));
  v += __int_as_float(DPP_I(__float_as_int(v), 0x112, 0xf));
  v += __int_as_float(DPP_I(__float_as_int(v), 0x114, 0xf));
  v += __int_as_float(DPP_I(__float_as_int(v), 0x118, 0xf));
  return v;
}
__device__ __forceinline__ float rdl(float v, int l) { return __int_as_float(__builtin_amdgcn_readlane(__float_as_int(v), l)); }
__device__ __forceinline__ float wave_sum(float v) {
  v = row_sum(v);
  asm("s_nop 1\n\tv_add_f32_dpp %0, %0, %0 row_bcast:15 row_mask:0xa bank_mask:0xf\n\t"
      "s_nop 1\n\tv_add_f32_dpp %0, %0, %0 row_bcast:31 row_mask:0xc bank_mask:0xf\n\ts_nop 0" : "+v"(v));
  return rdl(v, 63);
}
__device__ __forceinline__ float fold32(float a, float b) {
  const auto r = __builtin_amdgcn_permlane32_swap(__float_as_uint(a), __float_as_uint(b), false, false);
  return __uint_as_float(r[0]) + __uint_as_float(r[1]);
}
__device__ __forceinline__ float fold16(float a, float b) {
  const auto r = __builtin_amdgcn_permlane16_swap(__float_as_uint(a), __float_as_uint(b), false, false);
  return __uint_as_float(r[0]) + __uint_as_float(r[1]);
}
__device__ __forceinline__ void wave_sum2(float& a, float& b) {
  float v = row_sum(fold32(a, b));
  asm("s_nop 1\n\tv_add_f32_dpp %0, %0, %0 row_bcast:15 row_mask:0xa bank_mask:0xf\n\ts_nop 0" : "+v"(v));
  a = rdl(v, 31); b = rdl(v, 63);
}
__device__ __forceinline__ void wave_sum4(float& a, float& b, float& c, float& d) {
  const float v = row_sum(fold16(fold32(a, b), fold32(c, d)));
  a = rdl(v, 15); c = rdl(v, 31); b = rdl(v, 47); d = rdl(v, 63);
}
__device__ __forceinline__ float dot2(uint32_t a, uint32_t b, float c) {
  return __builtin_amdgcn_fdot2_f32_bf16(__builtin_bit_cast(bf16x2v, a), __builtin_bit_cast(bf16x2v, b), c, false);
}
__device__ __forceinline__ uint32_t row_maxu(uint32_t v) {
  v = max(v, (uint32_t)DPP_I((int)v, 0xb1, 0xf));
  v = max(v, (uint32_t)DPP_I((int)v, 0x4e, 0xf));
  v = max(v, (uint32_t)DPP_I((int)v, 0x141, 0xf));
  v = max(v, (uint32_t)DPP_I((int)v, 0x140, 0xf));
  return v;
}
__device__ __forceinline__ uint32_t wave_maxu(uint32_t v) {
  v = max(v, (uint32_t)DPP_I((int)v, 0x111, 0xf));
  v = max(v, (uint32_t)DPP_I((int)v, 0x112, 0xf));
  v = max(v, (uint32_t)DPP_I((int)v, 0x114, 0xf));
  v = max(v, (uint32_t)DPP_I((int)v, 0x118, 0xf));
  v = max(v, (uint32_t)DPP_I((int)v, 0x142, 0xa));
  v = max(v, (uint32_t)DPP_I((int)v, 0x143, 0xc));
  return (uint32_t)__builtin_amdgcn_readlane((int)v, 63);
}
__device__ __forceinline__ void wave_lds_sync() {
  __builtin_amdgcn_s_waitcnt(0xc07f);
  __builtin_amdgcn_wave_barrier();
}

__device__ __forceinline__ int kdel1(int i) { return (i & 1) ? ((i & 2) ? 765 : 511) : 256; }
__device__ __forceinline__ int kdel2(int i) { return (i & 1) ? ((i & 4) ? 1020 : 512) : 1792; }
__device__ __forceinline__ int kbase(int e) {
  const int i = e & 15;
  int l = (i & 7) + 128 * (i >> 3);
  if (e >= 16) l += kdel1(i);
  if (e >= 32) l += kdel2(i);
  return l;
}

__device__ __forceinline__ void cexp2(float ar2, float air, float l, float& er, float& ei) {
  float m = __builtin_amdgcn_exp2f(ar2 * l);
  float t = __builtin_amdgcn_fractf(air * l);
  er = m * __builtin_amdgcn_cosf(t);
  ei = m * __builtin_amdgcn_sinf(t);
}

constexpr int PS = 36;
typedef __attribute__((ext_vector_type(8))) _Float16 f16x8;
typedef __attribute__((ext_vector_type(2))) _Float16 f16x2;
__device__ __forceinline__ uint32_t packh(f32x2 v) { return __builtin_bit_cast(uint32_t, __builtin_convertvector(v, f16x2)); }
struct KPar { float ldt, are, aim, bre, bim, cre, cim; };
template <int NS> __device__ __forceinline__ uint32_t grp_maxu(uint32_t v) {
  v = max(v, (uint32_t)DPP_I((int)v, 0x111, 0xf));
  v = max(v, (uint32_t)DPP_I((int)v, 0x112, 0xf));
  v = max(v, (uint32_t)DPP_I((int)v, 0x114, 0xf));
  if (NS > 8) v = max(v, (uint32_t)DPP_I((int)v, 0x118, 0xf));
  if (NS > 16) v = max(v, (uint32_t)DPP_I((int)v, 0x142, 0xa));
  if (NS > 32) v = max(v, (uint32_t)DPP_I((int)v, 0x143, 0xc));
  return (uint32_t)__builtin_amdgcn_readlane((int)v, NS - 1);
}
template <int NS> __device__ __forceinline__ f32x4 kgen_base(const KPar& kp, float& ox, int& eb) {
  const float dt = rbf(__expf(kp.ldt));
  const float ar = dt * kp.are, ai = dt * kp.aim;
  const float dbr = dt * kp.bre, dbi = dt * kp.bim;
  const float cr = kp.cre, ci = kp.cim;
  const float ar2 = ar * 1.4426950408889634f, air = ai * 0.15915494309189535f;
  const float pr = cr * dbr - ci * dbi, pi = cr * dbi + ci * dbr;
  int ey = (int)(grp_maxu<NS>(__float_as_uint(fmaxf(fabsf(pr), fabsf(pi)))) >> 23) - 126;
  int gx = 0;
  if (__any(ar2 > 0.f)) {
    const float g = fminf(__uint_as_float(grp_maxu<NS>(__float_as_uint(fmaxf(ar2, 0.f)))), 1.f);
    gx = min((int)ceilf(g * 2304.f), 300);
    ey += (int)ceilf(g * 120.f);
  }
  ox = (float)(15 - gx);
  eb = ey + gx;
  return (f32x4){ar2, air - __builtin_rintf(air), __builtin_amdgcn_ldexpf(pr, 14 - ey), __builtin_amdgcn_ldexpf(pi, 14 - ey)};
}
__device__ __forceinline__ float kconst(int j) {
  const uint32_t wd = (j < 3) ? (256u | (511u << 11) | (765u << 22)) : (1792u | (512u << 11) | (1020u << 22));
  return (float)((wd >> (11 * ((j < 3) ? j : (j - 3) & 3))) & 0x7ffu);
}
__device__ __forceinline__ void cexp2p(f32x2 a, f32x2 t, f32x2& er, f32x2& ei) {
  const f32x2 m = {__builtin_amdgcn_exp2f(a[0]), __builtin_amdgcn_exp2f(a[1])};
  er = m * (f32x2){__builtin_amdgcn_cosf(t[0]), __builtin_amdgcn_cosf(t[1])};
  ei = m * (f32x2){__builtin_amdgcn_sinf(t[0]), __builtin_amdgcn_sinf(t[1])};
}
__device__ __forceinline__ void kgen_pair2(const float* ps, const float* p1, const float* p2, float lx0, float ly, float ox,
                                           u32x4* xh, u32x4& yh, int q) {
  const f32x4 pa = *(const f32x4*)ps, pc = *(const f32x4*)(ps + 4);
  const f32x4 w1 = *(const f32x4*)p1, w2 = *(const f32x4*)p2;
  f32x2 er, ei, xr, xi;
  cexp2p(pa.xy * ly, pa.zw * ly, er, ei);
  cexp2p(pa.xy * lx0 + ox, pa.zw * lx0, xr, xi);
  const f32x2 yr = pc.xy * er - pc.zw * ei, yi = -(pc.xy * ei) - pc.zw * er;
  const f32x2 x1r = xr * w1.xy - xi * w1.zw, x1i = xr * w1.zw + xi * w1.xy;
  const f32x2 x2r = x1r * w2.xy - x1i * w2.zw, x2i = x1r * w2.zw + x1i * w2.xy;
  yh[q] = packh(yr); yh[q + 1] = packh(yi);
  xh[0][q] = packh(xr); xh[0][q + 1] = packh(xi);
  xh[1][q] = packh(x1r); xh[1][q + 1] = packh(x1i);
  xh[2][q] = packh(x2r); xh[2][q + 1] = packh(x2i);
}
__device__ __forceinline__ void kgen_mma(const u32x4* xh, const u32x4& yh, f32x4* acc) {
#pragma unroll
  for (int t = 0; t < 3; ++t)
    acc[t] = __builtin_amdgcn_mfma_f32_16x16x32_f16(__builtin_bit_cast(f16x8, xh[t]), __builtin_bit_cast(f16x8, yh), acc[t], 0, 0, 0);
}
__device__ __forceinline__ f32x4 ldexp4(f32x4 v, int e) {
  return (f32x4){__builtin_amdgcn_ldexpf(v[0], e), __builtin_amdgcn_ldexpf(v[1], e), __builtin_amdgcn_ldexpf(v[2], e), __builtin_amdgcn_ldexpf(v[3], e)};
}

template <int I> struct IC { static constexpr int value = I; };
template <int B, int E, class F> __device__ __forceinline__ void static_for(F&& f) {
  if constexpr (B < E) { f(IC<B>{}); static_for<B + 1, E>(f); }
}
typedef __attribute__((ext_vector_type(4))) short s16x4;
__device__ __forceinline__ u32x2 trd(uint32_t a) {
  return __builtin_bit_cast(u32x2, __builtin_amdgcn_ds_read_tr16_b64_v4i16((__attribute__((address_space(3))) s16x4*)(uintptr_t)a));
}
#define DPP_F(v, ctrl) __int_as_float(DPP_I(__float_as_int(v), (ctrl), 0xf))
__device__ __forceinline__ float s_add(float a, float b) { float r; asm("v_add_f32 %0, %1, %2" : "=v"(r) : "v"(a), "v"(b)); return r; }
__device__ __forceinline__ float s_fma(float a, float b, float c) { float r; asm("v_fma_f32 %0, %1, %2, %3" : "=v"(r) : "v"(a), "v"(b), "v"(c)); return r; }
#define BAR() asm volatile("s_waitcnt lgkmcnt(0)\n\ts_barrier" ::: "memory")
#define ST16(off, v) asm volatile("global_store_dwordx4 %0, %1, %2 sc1\n\ts_nop 1" : : "v"(off), "v"(v), "s"(ws) : "memory")
#define ST4(off, v) asm volatile("global_store_dword %0, %1, %2 sc1" : : "v"(off), "v"(v), "s"(ws) : "memory")
#define LD16(r, off) asm volatile("global_load_dwordx4 %0, %1, %2 sc1" : "=v"(r) : "v"(off), "s"(ws) : "memory")
#define LD16W(r, off) asm volatile("global_load_dwordx4 %0, %1, %2 sc1\n\ts_waitcnt vmcnt(0)" : "=&v"(r) : "v"(off), "s"(ws) : "memory")
#define DRAIN() asm volatile("s_waitcnt vmcnt(0)" ::: "memory")
#define CEX(a, b) { const uint32_t hi_ = max(a, b), lo_ = min(a, b); a = hi_; b = lo_; }
#define MMB(a_, b_, c_) __builtin_amdgcn_mfma_f32_16x16x32_bf16(__builtin_bit_cast(bf16x8, a_), __builtin_bit_cast(bf16x8, b_), c_, 0, 0, 0)
#define DPOL ""
#define XPOL ""
#define TPOL ""
constexpr int SPIN_CAP = 1 << 18;
constexpr uint32_t UMASK = 0x40074007u;
__device__ __forceinline__ uint32_t utag(uint32_t T, int j) {
  return ((T >> (3 * j)) & 0x00070007u) | (((j <= 2) ? (T << (2 - j)) : (T >> (j - 2))) & 0x40004000u);
}
__device__ __forceinline__ uint32_t max4(const u32x4& a) { return max(max(a[0], a[1]), max(a[2], a[3])); }
__device__ __forceinline__ uint32_t min4(const u32x4& a) { return min(min(a[0], a[1]), min(a[2], a[3])); }
__device__ __forceinline__ void until(uint32_t t) {
#pragma unroll 1
  for (int spin = 0; spin < 200 && (int)((uint32_t)__builtin_amdgcn_s_memrealtime() - t) < 0; ++spin) __builtin_amdgcn_s_sleep(1);
}
__device__ __forceinline__ uint32_t latest(uint32_t t) {
  const uint32_t t0 = (uint32_t)__builtin_amdgcn_readfirstlane(t) - 0x40000000u;
  return wave_maxu(t - t0) + t0;
}
__device__ __forceinline__ uint32_t gate(const char* ws, uint32_t po, uint32_t T) {
  u32x2 pv;
#pragma unroll 1
  for (int spin = 0; spin < SPIN_CAP; ++spin) {
    asm volatile("global_load_dwordx2 %0, %1, %2 sc1\n\ts_waitcnt vmcnt(0)" : "=&v"(pv) : "v"(po), "s"(ws) : "memory");
    if (!__any(pv[0] != T)) break;
    __builtin_amdgcn_s_sleep(2);
  }
  return pv[1];
}
__device__ __forceinline__ uint32_t gate2(const char* ws, uint32_t oa, uint32_t da, uint32_t ob, uint32_t T) {
  u32x2 pa, pb;
#pragma unroll 1
  for (int spin = 0; spin < SPIN_CAP; ++spin) {
    asm volatile("global_load_dwordx2 %0, %2, %4 sc1\n\tglobal_load_dwordx2 %1, %3, %4 sc1\n\ts_waitcnt vmcnt(0)" : "=&v"(pa), "=&v"(pb) : "v"(oa), "v"(ob), "s"(ws) : "memory");
    if (!__any(pa[0] != T || pb[0] != T)) break;
    __builtin_amdgcn_s_sleep(4);
  }
  pa[1] += da;
  return latest(((int)(pa[1] - pb[1]) > 0) ? pa[1] : pb[1]);
}
__device__ __forceinline__ uint32_t ahead(uint32_t c0, uint32_t t0, int dt) {
  const uint32_t c = (uint32_t)__builtin_amdgcn_s_memtime(), t = (uint32_t)__builtin_amdgcn_s_memrealtime();
  return t + (uint32_t)((float)((t - t0) * (uint32_t)(20 * dt)) * __builtin_amdgcn_rcpf((float)(c - c0)));
}
#define SIGNAL(off, pitch, dt) do { const uint32_t t_ = ahead(ck0, rt0, dt); \
    ST16((off) + (uint32_t)(lane >> 3) * (pitch) + 16u * (lane & 7), ((u32x4){T, t_, T, t_})); } while (0)
__device__ __forceinline__ void gsync(uint32_t a, uint32_t target) {
  uint32_t t, c, n = SPIN_CAP;
  uint64_t ex;
  asm volatile("s_waitcnt lgkmcnt(0)\n\ts_mov_b64 %3, exec\n\ts_mov_b64 exec, 1\n\tv_mov_b32 %0, 1\n\tds_add_u32 %4, %0\n\ts_mov_b64 exec, %3\n"
               "1:\n\ts_sleep 1\n\tds_read_b32 %0, %4\n\ts_sub_u32 %2, %2, 1\n\ts_cbranch_scc1 2f\n\ts_waitcnt lgkmcnt(0)\n\tv_readfirstlane_b32 %1, %0\n\t"
               "s_cmp_lt_u32 %1, %5\n\ts_cbranch_scc1 1b\n2:\n\ts_waitcnt lgkmcnt(0)"
               : "=&v"(t), "=&s"(c), "+s"(n), "=&s"(ex) : "v"(a), "n"(target) : "memory", "scc");
}
__device__ __forceinline__ void gsync0(uint32_t a, uint32_t target) {
  uint32_t t, c, n = SPIN_CAP;
  asm volatile("s_waitcnt lgkmcnt(0)\n\ts_mov_b64 exec, 1\n\tv_mov_b32 %0, 1\n\tds_add_u32 %3, %0\n\ts_mov_b64 exec, -1\n"
               "1:\n\tds_read_b32 %0, %3\n\ts_sub_u32 %2, %2, 1\n\ts_cbranch_scc1 2f\n\ts_waitcnt lgkmcnt(0)\n\tv_readfirstlane_b32 %1, %0\n\t"
               "s_cmp_lt_u32 %1, %4\n\ts_cbranch_scc1 1b\n2:\n\ts_waitcnt lgkmcnt(0)"
               : "=&v"(t), "=&s"(c), "+s"(n) : "v"(a), "n"(target) : "memory", "scc");
}
__device__ __forceinline__ void gwait(uint32_t a, uint32_t target) {
  uint32_t t, c, n = SPIN_CAP;
  asm volatile("1:\n\ts_sleep 2\n\tds_read_b32 %0, %3\n\ts_sub_u32 %2, %2, 1\n\ts_cbranch_scc1 2f\n\ts_waitcnt lgkmcnt(0)\n\tv_readfirstlane_b32 %1, %0\n\t"
               "s_cmp_lt_u32 %1, %4\n\ts_cbranch_scc1 1b\n2:\n\ts_waitcnt lgkmcnt(0)"
               : "=&v"(t), "=&s"(c), "+s"(n) : "v"(a), "n"(target) : "memory", "scc");
}
__device__ __forceinline__ void dma16(const void* base, uint32_t voff, uint32_t lds) {
  uint32_t keep;
  asm volatile("s_mov_b32 %0, m0\n\ts_mov_b32 m0, %3\n\ts_nop 0\n\tglobal_load_lds_dwordx4 %1, %2" DPOL "\n\ts_mov_b32 m0, %0" : "=&s"(keep) : "v"(voff), "s"(base), "s"(lds) : "memory");
}
__device__ __forceinline__ void nap(int n) {
  for (; n > 15; n -= 15) __builtin_amdgcn_s_sleep(15);
  switch (n) {
    case 1: __builtin_amdgcn_s_sleep(1); break; case 2: __builtin_amdgcn_s_sleep(2); break; case 3: __builtin_amdgcn_s_sleep(3); break;
    case 4: __builtin_amdgcn_s_sleep(4); break; case 5: __builtin_amdgcn_s_sleep(5); break; case 6: __builtin_amdgcn_s_sleep(6); break;
    case 7: __builtin_amdgcn_s_sleep(7); break; case 8: __builtin_amdgcn_s_sleep(8); break; case 9: __builtin_amdgcn_s_sleep(9); break;
    case 10: __builtin_amdgcn_s_sleep(10); break; case 11: __builtin_amdgcn_s_sleep(11); break; case 12: __builtin_amdgcn_s_sleep(12); break;
    case 13: __builtin_amdgcn_s_sleep(13); break; case 14: __builtin_amdgcn_s_sleep(14); break; case 15: __builtin_amdgcn_s_sleep(15); break;
    default: break;
  }
}
#define SLEEP(n) do { if ((n) > 0) __builtin_amdgcn_s_sleep(n); } while (0)
#define USLEEP 3
#define UPOS 1
#define QSLEEP 0
#define CSLEEP 4
#define WSLEEP 4
#define TSLEEP 0
#define VSLEEP 45
#define KSLEEP 6
#define SSLEEP 11
#define PSLEEP 2
#define PPRIO 0
#define APRIO 1
#define NTOUCH 4
#define SWZ 1
#define PREOUT 0
#define LFIRST 1
#define CBR "0"
#define WD 95
#define CD 3
#define OSLEEP 12
#define QPRIO 3
#define KPRIO 1
#define UDRAIN 0
#define GSLEEP 0
#define SIGPOS 2
#define LPRIO 3
#define TW 0
#define TPOS 0
#define R1EARLY 1
#define DPOS 1
#define NSLEEP 0
#define ASLEEP 40
#define FPRIO 1
#define PRBW 1
#define PRBPOS 1
#define TW2 6
#define DSLEEP 8
#define VD 330
#define XSLEEP 20
#define RPOS 1
#define KPRE 1
#define UD 40
#define XD 70
#define TRIG 1
#define CPF 0
#define RSLEEP 0
#define UPRIO 2
#define VD2 275
#define TWP 256
#define WDEPTH 12
#define PD 255
#define VDX 175
#define EARLY 0
#define ESLEEP 0
#define WDEPTH1 5
#define WD2 85
#define SD 35
#define KD 185
#define UFIRST 0
#define TX 1
#define QWAIT 4
#define OD 6
#define KBACK 6
#define DDEPTH 7
#define C3 1
#define XOFF 1
#define GD 105
#define XROT 2
#define WSOFF 0
#define T0 2
#define TF 192
#define DD 0
#define TDEPTH 1
#define TB1 0
#define TB1I 0
#define TB1X 2
#define B1SLEEP 0
#define DFIRST 0
#define T0SLEEP 0
#define BSLEEP 6
#define KN 3
#define TD 0
#define KE 0
#define SKIPB 0
#define NFIRST 0
#define PW1 16
#define PW2 38
#define KLATE 0
#define RPF 1
#define HSLEEP 6
#define LSLEEP 2
#define FORCESLOW 0
#define FORCEFB 0
constexpr int CP = 832;
#define CONV_ASM \
  "s_mov_b32 s92, 0\ns_brev_b32 s93, 1\ns_cmp_eq_u32 s89, 0\ns_cbranch_scc1 10f\ns_cmp_eq_u32 s89, 3\ns_cbranch_scc1 11f\n" \
  "ds_read_b64 v[40:41], v11 offset:192\nds_read_b64 v[42:43], v11 offset:224\nds_read_b64_tr_b16 v[106:107], v10 offset:1152\n" \
  "ds_read_b64_tr_b16 v[104:105], v10 offset:1280\nds_read_b64_tr_b16 v[102:103], v10 offset:1408\nds_read_b64_tr_b16 v[100:101], v10 offset:1536\n" \
  "ds_read_b64 v[36:37], v11 offset:128\nds_read_b64 v[38:39], v11 offset:160\nds_read_b64_tr_b16 v[98:99], v10 offset:1664\n" \
  "ds_read_b64_tr_b16 v[96:97], v10 offset:1792\ns_bitcmp0_b32 s89, 2\ns_cbranch_scc1 16f\nds_read_b128 v[48:51], v14 offset:2976\n" \
  "ds_read_b128 v[52:55], v14 offset:2992\ns_bitcmp1_b32 s89, 0\ns_waitcnt lgkmcnt(0)\nv_alignbit_b32 v56, v49, v48, 16\n" \
  "v_alignbit_b32 v57, v50, v49, 16\nv_alignbit_b32 v58, v51, v50, 16\nv_alignbit_b32 v59, v52, v51, 16\nv_alignbit_b32 v60, v53, v52, 16\n" \
  "v_alignbit_b32 v61, v54, v53, 16\nv_alignbit_b32 v62, v55, v54, 16\nv_pk_add_f16 v108, v57, v58\nv_pk_add_f16 v109, v58, v59\n" \
  "v_pk_add_f16 v110, v59, v60\nv_pk_add_f16 v111, v60, v61\nv_pk_add_f16 v108, v50, v108\nv_pk_add_f16 v109, v51, v109\n" \
  "v_pk_add_f16 v110, v52, v110\nv_pk_add_f16 v111, v53, v111\nds_write_b128 v14, v[108:111] offset:7120\nv_pk_add_f16 v112, v49, v51\n" \
  "v_pk_add_f16 v113, v50, v52\nv_pk_add_f16 v114, v51, v53\nv_pk_add_f16 v115, v52, v54\nv_pk_add_f16 v112, v108, v112\n" \
  "v_pk_add_f16 v113, v109, v113\nv_pk_add_f16 v114, v110, v114\nv_pk_add_f16 v115, v111, v115\nds_write_b128 v14, v[112:115] offset:10320\n" \
  "s_cbranch_scc1 17f\nv_pk_add_f16 v108, v56, v59\nv_pk_add_f16 v109, v57, v60\nv_pk_add_f16 v110, v58, v61\nv_pk_add_f16 v111, v59, v62\n" \
  "v_pk_add_f16 v108, v112, v108\nv_pk_add_f16 v109, v113, v109\nv_pk_add_f16 v110, v114, v110\nv_pk_add_f16 v111, v115, v111\n" \
  "ds_write_b128 v14, v[108:111] offset:13520\nv_pk_add_f16 v112, v48, v52\nv_pk_add_f16 v113, v49, v53\nv_pk_add_f16 v114, v50, v54\n" \
  "v_pk_add_f16 v115, v51, v55\nv_pk_add_f16 v112, v108, v112\nv_pk_add_f16 v113, v109, v113\nv_pk_add_f16 v114, v110, v114\n" \
  "v_pk_add_f16 v115, v111, v115\nds_write_b128 v14, v[112:115] offset:15712\n17:\ns_mov_b64 exec, 1\nds_inc_u32 v15, v14\ns_mov_b64 exec, -1\n16:\n" \
  "s_waitcnt lgkmcnt(4)\nv_mfma_f32_16x16x32_f16 v[48:51], v[40:43], v[104:107], 0\ns_setprio 1\nds_read_b64 v[32:33], v11 offset:64\n" \
  "ds_read_b64 v[34:35], v11 offset:96\nv_mfma_f32_16x16x32_f16 v[52:55], v[40:43], v[102:105], 0\nds_read_b64_tr_b16 v[94:95], v10 offset:1920\n" \
  "ds_read_b64_tr_b16 v[92:93], v10 offset:2048\nv_mfma_f32_16x16x32_f16 v[56:59], v[40:43], v[100:103], 0\nds_read_b64 v[28:29], v11 offset:0\n" \
  "ds_read_b64 v[30:31], v11 offset:32\ns_waitcnt lgkmcnt(6)\nv_mfma_f32_16x16x32_f16 v[48:51], v[36:39], v[100:103], v[48:51]\n" \
  "ds_read_b64_tr_b16 v[90:91], v10 offset:2176\nds_read_b64_tr_b16 v[88:89], v10 offset:2304\n" \
  "v_mfma_f32_16x16x32_f16 v[52:55], v[36:39], v[98:101], v[52:55]\nds_read_b64 v[44:45], v11 offset:256\nds_read_b64 v[46:47], v11 offset:288\n" \
  "v_mfma_f32_16x16x32_f16 v[56:59], v[36:39], v[96:99], v[56:59]\nds_read_b64_tr_b16 v[64:65], v10 offset:1024\n" \
  "ds_read_b64_tr_b16 v[66:67], v10 offset:0\ns_waitcnt lgkmcnt(8)\nv_mfma_f32_16x16x32_f16 v[48:51], v[32:35], v[96:99], v[48:51]\n" \
  "ds_read_b64_tr_b16 v[68:69], v10 offset:1152\nds_read_b64_tr_b16 v[70:71], v10 offset:128\n" \
  "v_mfma_f32_16x16x32_f16 v[52:55], v[32:35], v[94:97], v[52:55]\nds_read_b64_tr_b16 v[72:73], v10 offset:1280\n" \
  "ds_read_b64_tr_b16 v[74:75], v10 offset:256\nv_mfma_f32_16x16x32_f16 v[56:59], v[32:35], v[92:95], v[56:59]\n" \
  "ds_read_b64_tr_b16 v[86:87], v10 offset:2432\nds_read_b64_tr_b16 v[84:85], v10 offset:2560\ns_waitcnt lgkmcnt(10)\n" \
  "v_mfma_f32_16x16x32_f16 v[48:51], v[28:31], v[92:95], v[48:51]\nds_read_b64_tr_b16 v[82:83], v10 offset:2688\n" \
  "ds_read_b64_tr_b16 v[80:81], v10 offset:2816\nv_mfma_f32_16x16x32_f16 v[52:55], v[28:31], v[90:93], v[52:55]\n" \
  "ds_read_b64_tr_b16 v[78:79], v10 offset:2944\nds_read_b64_tr_b16 v[76:77], v10 offset:3072\n" \
  "v_mfma_f32_16x16x32_f16 v[56:59], v[28:31], v[88:91], v[56:59]\ns_waitcnt lgkmcnt(6)\n" \
  "v_mfma_f32_16x16x32_f16 v[48:51], v[44:47], v[64:67], v[48:51]\nv_mfma_f32_16x16x32_f16 v[52:55], v[44:47], v[68:71], v[52:55]\n" \
  "v_mfma_f32_16x16x32_f16 v[56:59], v[44:47], v[72:75], v[56:59]\ns_setprio 0\ns_bitcmp1_b32 s89, 2\ns_cbranch_scc1 18f\n" \
  "ds_read_b128 v[64:67], v14 offset:2976\nds_read_b128 v[68:71], v14 offset:2992\ns_bitcmp1_b32 s89, 0\ns_waitcnt lgkmcnt(0)\n" \
  "v_alignbit_b32 v108, v65, v64, 16\nv_alignbit_b32 v109, v66, v65, 16\nv_alignbit_b32 v110, v67, v66, 16\nv_alignbit_b32 v111, v68, v67, 16\n" \
  "v_alignbit_b32 v112, v69, v68, 16\nv_alignbit_b32 v113, v70, v69, 16\nv_alignbit_b32 v114, v71, v70, 16\nv_pk_add_f16 v116, v109, v110\n" \
  "v_pk_add_f16 v117, v110, v111\nv_pk_add_f16 v118, v111, v112\nv_pk_add_f16 v119, v112, v113\nv_pk_add_f16 v116, v66, v116\n" \
  "v_pk_add_f16 v117, v67, v117\nv_pk_add_f16 v118, v68, v118\nv_pk_add_f16 v119, v69, v119\nds_write_b128 v14, v[116:119] offset:7120\n" \
  "v_pk_add_f16 v120, v65, v67\nv_pk_add_f16 v121, v66, v68\nv_pk_add_f16 v122, v67, v69\nv_pk_add_f16 v123, v68, v70\nv_pk_add_f16 v120, v116, v120\n" \
  "v_pk_add_f16 v121, v117, v121\nv_pk_add_f16 v122, v118, v122\nv_pk_add_f16 v123, v119, v123\nds_write_b128 v14, v[120:123] offset:10320\n" \
  "s_cbranch_scc1 19f\nv_pk_add_f16 v116, v108, v111\nv_pk_add_f16 v117, v109, v112\nv_pk_add_f16 v118, v110, v113\nv_pk_add_f16 v119, v111, v114\n" \
  "v_pk_add_f16 v116, v120, v116\nv_pk_add_f16 v117, v121, v117\nv_pk_add_f16 v118, v122, v118\nv_pk_add_f16 v119, v123, v119\n" \
  "ds_write_b128 v14, v[116:119] offset:13520\nv_pk_add_f16 v120, v64, v68\nv_pk_add_f16 v121, v65, v69\nv_pk_add_f16 v122, v66, v70\n" \
  "v_pk_add_f16 v123, v67, v71\nv_pk_add_f16 v120, v116, v120\nv_pk_add_f16 v121, v117, v121\nv_pk_add_f16 v122, v118, v122\n" \
  "v_pk_add_f16 v123, v119, v123\nds_write_b128 v14, v[120:123] offset:15712\n19:\ns_mov_b64 exec, 1\nds_inc_u32 v15, v14\ns_mov_b64 exec, -1\n18:\n" \
  "ds_read_b64_tr_b16 v[64:65], v10 offset:1408\nds_read_b64_tr_b16 v[66:67], v10 offset:384\nds_read_b64_tr_b16 v[68:69], v10 offset:1536\n" \
  "ds_read_b64_tr_b16 v[70:71], v10 offset:512\nds_read_b64_tr_b16 v[72:73], v10 offset:1664\nds_read_b64_tr_b16 v[74:75], v10 offset:640\n" \
  "v_add_f32_dpp v114, v51, v54 wave_ror:1 row_mask:0x1 bank_mask:0x1\nv_add_f32_dpp v118, v55, v58 wave_ror:1 row_mask:0x1 bank_mask:0x1\n" \
  "v_add_f32_dpp v110, v51, v50 wave_shr:1 row_mask:0xf bank_mask:0xf bound_ctrl:1\n" \
  "v_add_f32_dpp v114, v55, v54 wave_shr:1 row_mask:0xf bank_mask:0xf\nv_add_f32_dpp v118, v59, v58 wave_shr:1 row_mask:0xf bank_mask:0xf\n" \
  "v_add_f32_dpp v113, v110, v53 wave_ror:1 row_mask:0x1 bank_mask:0x1\nv_add_f32_dpp v117, v114, v57 wave_ror:1 row_mask:0x1 bank_mask:0x1\n" \
  "v_add_f32_dpp v109, v110, v49 wave_shr:1 row_mask:0xf bank_mask:0xf bound_ctrl:1\n" \
  "v_add_f32_dpp v113, v114, v53 wave_shr:1 row_mask:0xf bank_mask:0xf\nv_add_f32_dpp v117, v118, v57 wave_shr:1 row_mask:0xf bank_mask:0xf\n" \
  "v_add_f32_dpp v112, v109, v52 wave_ror:1 row_mask:0x1 bank_mask:0x1\nv_add_f32_dpp v116, v113, v56 wave_ror:1 row_mask:0x1 bank_mask:0x1\n" \
  "v_add_f32_dpp v108, v109, v48 wave_shr:1 row_mask:0xf bank_mask:0xf bound_ctrl:1\n" \
  "v_add_f32_dpp v112, v113, v52 wave_shr:1 row_mask:0xf bank_mask:0xf\nv_add_f32_dpp v116, v117, v56 wave_shr:1 row_mask:0xf bank_mask:0xf\n" \
  "ds_write2st64_b32 v12, v108, v112 offset0:0 offset1:1\ns_waitcnt lgkmcnt(1)\nv_mfma_f32_16x16x32_f16 v[60:63], v[40:43], v[98:101], 0\n" \
  "s_setprio 1\nv_mfma_f32_16x16x32_f16 v[48:51], v[40:43], v[96:99], 0\nv_mfma_f32_16x16x32_f16 v[52:55], v[40:43], v[94:97], 0\n" \
  "v_mfma_f32_16x16x32_f16 v[60:63], v[36:39], v[94:97], v[60:63]\nv_mfma_f32_16x16x32_f16 v[48:51], v[36:39], v[92:95], v[48:51]\n" \
  "v_mfma_f32_16x16x32_f16 v[52:55], v[36:39], v[90:93], v[52:55]\nv_mfma_f32_16x16x32_f16 v[60:63], v[32:35], v[90:93], v[60:63]\n" \
  "v_mfma_f32_16x16x32_f16 v[48:51], v[32:35], v[88:91], v[48:51]\nv_mfma_f32_16x16x32_f16 v[52:55], v[32:35], v[86:89], v[52:55]\n" \
  "v_mfma_f32_16x16x32_f16 v[60:63], v[28:31], v[86:89], v[60:63]\nv_mfma_f32_16x16x32_f16 v[48:51], v[28:31], v[84:87], v[48:51]\n" \
  "v_mfma_f32_16x16x32_f16 v[52:55], v[28:31], v[82:85], v[52:55]\nv_mfma_f32_16x16x32_f16 v[60:63], v[44:47], v[64:67], v[60:63]\n" \
  "v_mfma_f32_16x16x32_f16 v[48:51], v[44:47], v[68:71], v[48:51]\nv_mfma_f32_16x16x32_f16 v[52:55], v[44:47], v[72:75], v[52:55]\ns_setprio 0\n" \
  "ds_read_b64_tr_b16 v[64:65], v10 offset:1792\nds_read_b64_tr_b16 v[66:67], v10 offset:768\nds_read_b64_tr_b16 v[68:69], v10 offset:1920\n" \
  "ds_read_b64_tr_b16 v[70:71], v10 offset:896\nds_read_b64_tr_b16 v[72:73], v10 offset:2048\nds_read_b64_tr_b16 v[74:75], v10 offset:1024\n" \
  "v_add_f32_dpp v122, v59, v62 wave_ror:1 row_mask:0x1 bank_mask:0x1\nv_add_f32_dpp v110, v63, v50 wave_ror:1 row_mask:0x1 bank_mask:0x1\n" \
  "v_add_f32_dpp v114, v51, v54 wave_ror:1 row_mask:0x1 bank_mask:0x1\nv_add_f32_dpp v122, v63, v62 wave_shr:1 row_mask:0xf bank_mask:0xf\n" \
  "v_add_f32_dpp v110, v51, v50 wave_shr:1 row_mask:0xf bank_mask:0xf\nv_add_f32_dpp v114, v55, v54 wave_shr:1 row_mask:0xf bank_mask:0xf\n" \
  "v_add_f32_dpp v121, v118, v61 wave_ror:1 row_mask:0x1 bank_mask:0x1\nv_add_f32_dpp v109, v122, v49 wave_ror:1 row_mask:0x1 bank_mask:0x1\n" \
  "v_add_f32_dpp v113, v110, v53 wave_ror:1 row_mask:0x1 bank_mask:0x1\nv_add_f32_dpp v121, v122, v61 wave_shr:1 row_mask:0xf bank_mask:0xf\n" \
  "v_add_f32_dpp v109, v110, v49 wave_shr:1 row_mask:0xf bank_mask:0xf\nv_add_f32_dpp v113, v114, v53 wave_shr:1 row_mask:0xf bank_mask:0xf\n" \
  "v_add_f32_dpp v120, v117, v60 wave_ror:1 row_mask:0x1 bank_mask:0x1\nv_add_f32_dpp v108, v121, v48 wave_ror:1 row_mask:0x1 bank_mask:0x1\n" \
  "v_add_f32_dpp v112, v109, v52 wave_ror:1 row_mask:0x1 bank_mask:0x1\nv_add_f32_dpp v120, v121, v60 wave_shr:1 row_mask:0xf bank_mask:0xf\n" \
  "v_add_f32_dpp v108, v109, v48 wave_shr:1 row_mask:0xf bank_mask:0xf\nv_add_f32_dpp v112, v113, v52 wave_shr:1 row_mask:0xf bank_mask:0xf\n" \
  "ds_write2st64_b32 v12, v116, v120 offset0:2 offset1:3\nds_write2st64_b32 v12, v108, v112 offset0:4 offset1:5\ns_waitcnt lgkmcnt(2)\n" \
  "v_mfma_f32_16x16x32_f16 v[56:59], v[40:43], v[92:95], 0\ns_setprio 1\nv_mfma_f32_16x16x32_f16 v[60:63], v[40:43], v[90:93], 0\n" \
  "v_mfma_f32_16x16x32_f16 v[48:51], v[40:43], v[88:91], 0\nv_mfma_f32_16x16x32_f16 v[56:59], v[36:39], v[88:91], v[56:59]\n" \
  "v_mfma_f32_16x16x32_f16 v[60:63], v[36:39], v[86:89], v[60:63]\nv_mfma_f32_16x16x32_f16 v[48:51], v[36:39], v[84:87], v[48:51]\n" \
  "v_mfma_f32_16x16x32_f16 v[56:59], v[32:35], v[84:87], v[56:59]\nv_mfma_f32_16x16x32_f16 v[60:63], v[32:35], v[82:85], v[60:63]\n" \
  "v_mfma_f32_16x16x32_f16 v[48:51], v[32:35], v[80:83], v[48:51]\nv_mfma_f32_16x16x32_f16 v[56:59], v[28:31], v[80:83], v[56:59]\n" \
  "v_mfma_f32_16x16x32_f16 v[60:63], v[28:31], v[78:81], v[60:63]\nv_mfma_f32_16x16x32_f16 v[48:51], v[28:31], v[76:79], v[48:51]\n" \
  "v_mfma_f32_16x16x32_f16 v[56:59], v[44:47], v[64:67], v[56:59]\nv_mfma_f32_16x16x32_f16 v[60:63], v[44:47], v[68:71], v[60:63]\n" \
  "v_mfma_f32_16x16x32_f16 v[48:51], v[44:47], v[72:75], v[48:51]\ns_setprio 0\ns_nop 4\n" \
  "v_add_f32_dpp v118, v55, v58 wave_ror:1 row_mask:0x1 bank_mask:0x1\nv_add_f32_dpp v122, v59, v62 wave_ror:1 row_mask:0x1 bank_mask:0x1\n" \
  "v_add_f32_dpp v110, v63, v50 wave_ror:1 row_mask:0x1 bank_mask:0x1\nv_add_f32_dpp v118, v59, v58 wave_shr:1 row_mask:0xf bank_mask:0xf\n" \
  "v_add_f32_dpp v122, v63, v62 wave_shr:1 row_mask:0xf bank_mask:0xf\nv_add_f32_dpp v110, v51, v50 wave_shr:1 row_mask:0xf bank_mask:0xf\n" \
  "v_add_f32_dpp v117, v114, v57 wave_ror:1 row_mask:0x1 bank_mask:0x1\nv_add_f32_dpp v121, v118, v61 wave_ror:1 row_mask:0x1 bank_mask:0x1\n" \
  "v_add_f32_dpp v109, v122, v49 wave_ror:1 row_mask:0x1 bank_mask:0x1\nv_add_f32_dpp v117, v118, v57 wave_shr:1 row_mask:0xf bank_mask:0xf\n" \
  "v_add_f32_dpp v121, v122, v61 wave_shr:1 row_mask:0xf bank_mask:0xf\nv_add_f32_dpp v109, v110, v49 wave_shr:1 row_mask:0xf bank_mask:0xf\n" \
  "v_add_f32_dpp v116, v113, v56 wave_ror:1 row_mask:0x1 bank_mask:0x1\nv_add_f32_dpp v120, v117, v60 wave_ror:1 row_mask:0x1 bank_mask:0x1\n" \
  "v_add_f32_dpp v108, v121, v48 wave_ror:1 row_mask:0x1 bank_mask:0x1\nv_add_f32_dpp v116, v117, v56 wave_shr:1 row_mask:0xf bank_mask:0xf\n" \
  "v_add_f32_dpp v120, v121, v60 wave_shr:1 row_mask:0xf bank_mask:0xf\nv_add_f32_dpp v108, v109, v48 wave_shr:1 row_mask:0xf bank_mask:0xf\n" \
  "ds_write2st64_b32 v12, v116, v120 offset0:6 offset1:7\nds_write_b32 v12, v108 offset:2048\nv_mov_b32 v111, v51\ns_mov_b64 exec, s[92:93]\n" \
  "ds_write_b128 v13, v[108:111]\ns_mov_b64 exec, -1\ns_setpc_b64 s[90:91]\n10:\nds_read_b64 v[28:29], v11 offset:0\n" \
  "ds_read_b64 v[30:31], v11 offset:32\nds_read_b64_tr_b16 v[104:105], v10 offset:1920\nds_read_b64_tr_b16 v[102:103], v10 offset:2048\n" \
  "ds_read_b64_tr_b16 v[100:101], v10 offset:2176\nds_read_b64_tr_b16 v[98:99], v10 offset:2304\nds_read_b64_tr_b16 v[96:97], v10 offset:2432\n" \
  "ds_read_b64_tr_b16 v[94:95], v10 offset:2560\nds_read_b64_tr_b16 v[92:93], v10 offset:2688\nds_read_b64 v[32:33], v11 offset:64\n" \
  "s_waitcnt lgkmcnt(6)\nv_mfma_f32_16x16x32_f16 v[48:51], v[28:31], v[102:105], 0\ns_setprio 1\nds_read_b64 v[34:35], v11 offset:96\n" \
  "ds_read_b64 v[36:37], v11 offset:128\ns_waitcnt lgkmcnt(7)\nv_mfma_f32_16x16x32_f16 v[52:55], v[28:31], v[100:103], 0\n" \
  "ds_read_b64 v[38:39], v11 offset:160\nds_read_b64_tr_b16 v[90:91], v10 offset:2816\ns_waitcnt lgkmcnt(8)\n" \
  "v_mfma_f32_16x16x32_f16 v[56:59], v[28:31], v[98:101], 0\nds_read_b64_tr_b16 v[88:89], v10 offset:2944\n" \
  "ds_read_b64_tr_b16 v[86:87], v10 offset:3072\ns_waitcnt lgkmcnt(9)\nv_mfma_f32_16x16x32_f16 v[60:63], v[28:31], v[96:99], 0\n" \
  "ds_read_b64 v[40:41], v11 offset:192\nds_read_b64 v[42:43], v11 offset:224\ns_waitcnt lgkmcnt(10)\n" \
  "v_mfma_f32_16x16x32_f16 v[64:67], v[28:31], v[94:97], 0\nds_read_b64 v[44:45], v11 offset:256\nds_read_b64 v[46:47], v11 offset:328\n" \
  "s_waitcnt lgkmcnt(11)\nv_mfma_f32_16x16x32_f16 v[68:71], v[28:31], v[92:95], 0\nds_read_b64_tr_b16 v[84:85], v10 offset:3200\n" \
  "ds_read_b64_tr_b16 v[82:83], v10 offset:3328\ns_waitcnt lgkmcnt(11)\nv_mfma_f32_16x16x32_f16 v[56:59], v[32:35], v[102:105], v[56:59]\n" \
  "ds_read_b64_tr_b16 v[80:81], v10 offset:3456\nds_read_b64_tr_b16 v[78:79], v10 offset:3584\n" \
  "v_mfma_f32_16x16x32_f16 v[60:63], v[32:35], v[100:103], v[60:63]\nds_read_b64_tr_b16 v[76:77], v10 offset:3712\n" \
  "v_mfma_f32_16x16x32_f16 v[64:67], v[32:35], v[98:101], v[64:67]\nv_mfma_f32_16x16x32_f16 v[68:71], v[32:35], v[96:99], v[68:71]\n" \
  "s_waitcnt lgkmcnt(12)\nv_mfma_f32_16x16x32_f16 v[64:67], v[36:39], v[102:105], v[64:67]\n" \
  "v_mfma_f32_16x16x32_f16 v[68:71], v[36:39], v[100:103], v[68:71]\ns_setprio 0\nv_add_f32_dpp v112, v51, v54 wave_ror:1 row_mask:0x1 bank_mask:0x1\n" \
  "v_add_f32_dpp v116, v55, v58 wave_ror:1 row_mask:0x1 bank_mask:0x1\n" \
  "v_add_f32_dpp v108, v51, v50 wave_shr:1 row_mask:0xf bank_mask:0xf bound_ctrl:1\n" \
  "v_add_f32_dpp v112, v55, v54 wave_shr:1 row_mask:0xf bank_mask:0xf\nv_add_f32_dpp v116, v59, v58 wave_shr:1 row_mask:0xf bank_mask:0xf\n" \
  "v_add_f32_dpp v111, v108, v53 wave_ror:1 row_mask:0x1 bank_mask:0x1\nv_add_f32_dpp v115, v112, v57 wave_ror:1 row_mask:0x1 bank_mask:0x1\n" \
  "v_add_f32_dpp v107, v108, v49 wave_shr:1 row_mask:0xf bank_mask:0xf bound_ctrl:1\n" \
  "v_add_f32_dpp v111, v112, v53 wave_shr:1 row_mask:0xf bank_mask:0xf\nv_add_f32_dpp v115, v116, v57 wave_shr:1 row_mask:0xf bank_mask:0xf\n" \
  "v_add_f32_dpp v110, v107, v52 wave_ror:1 row_mask:0x1 bank_mask:0x1\nv_add_f32_dpp v114, v111, v56 wave_ror:1 row_mask:0x1 bank_mask:0x1\n" \
  "v_add_f32_dpp v106, v107, v48 wave_shr:1 row_mask:0xf bank_mask:0xf bound_ctrl:1\n" \
  "v_add_f32_dpp v110, v111, v52 wave_shr:1 row_mask:0xf bank_mask:0xf\nv_add_f32_dpp v114, v115, v56 wave_shr:1 row_mask:0xf bank_mask:0xf\n" \
  "ds_write2st64_b32 v12, v106, v110 offset0:0 offset1:1\nv_add_f32_dpp v120, v59, v62 wave_ror:1 row_mask:0x1 bank_mask:0x1\n" \
  "v_add_f32_dpp v108, v63, v66 wave_ror:1 row_mask:0x1 bank_mask:0x1\nv_add_f32_dpp v112, v67, v70 wave_ror:1 row_mask:0x1 bank_mask:0x1\n" \
  "v_add_f32_dpp v120, v63, v62 wave_shr:1 row_mask:0xf bank_mask:0xf\nv_add_f32_dpp v108, v67, v66 wave_shr:1 row_mask:0xf bank_mask:0xf\n" \
  "v_add_f32_dpp v112, v71, v70 wave_shr:1 row_mask:0xf bank_mask:0xf\nv_add_f32_dpp v119, v116, v61 wave_ror:1 row_mask:0x1 bank_mask:0x1\n" \
  "v_add_f32_dpp v107, v120, v65 wave_ror:1 row_mask:0x1 bank_mask:0x1\nv_add_f32_dpp v111, v108, v69 wave_ror:1 row_mask:0x1 bank_mask:0x1\n" \
  "v_add_f32_dpp v119, v120, v61 wave_shr:1 row_mask:0xf bank_mask:0xf\nv_add_f32_dpp v107, v108, v65 wave_shr:1 row_mask:0xf bank_mask:0xf\n" \
  "v_add_f32_dpp v111, v112, v69 wave_shr:1 row_mask:0xf bank_mask:0xf\nv_add_f32_dpp v118, v115, v60 wave_ror:1 row_mask:0x1 bank_mask:0x1\n" \
  "v_add_f32_dpp v106, v119, v64 wave_ror:1 row_mask:0x1 bank_mask:0x1\nv_add_f32_dpp v110, v107, v68 wave_ror:1 row_mask:0x1 bank_mask:0x1\n" \
  "v_add_f32_dpp v118, v119, v60 wave_shr:1 row_mask:0xf bank_mask:0xf\nv_add_f32_dpp v106, v107, v64 wave_shr:1 row_mask:0xf bank_mask:0xf\n" \
  "v_add_f32_dpp v110, v111, v68 wave_shr:1 row_mask:0xf bank_mask:0xf\nds_write2st64_b32 v12, v114, v118 offset0:2 offset1:3\n" \
  "ds_write2st64_b32 v12, v106, v110 offset0:4 offset1:5\ns_waitcnt lgkmcnt(3)\nv_mfma_f32_16x16x32_f16 v[72:75], v[28:31], v[90:93], 0\ns_setprio 1\n" \
  "v_mfma_f32_16x16x32_f16 v[48:51], v[28:31], v[88:91], 0\nv_mfma_f32_16x16x32_f16 v[52:55], v[28:31], v[86:89], 0\n" \
  "v_mfma_f32_16x16x32_f16 v[72:75], v[32:35], v[94:97], v[72:75]\nv_mfma_f32_16x16x32_f16 v[48:51], v[32:35], v[92:95], v[48:51]\n" \
  "v_mfma_f32_16x16x32_f16 v[52:55], v[32:35], v[90:93], v[52:55]\nv_mfma_f32_16x16x32_f16 v[72:75], v[36:39], v[98:101], v[72:75]\n" \
  "v_mfma_f32_16x16x32_f16 v[48:51], v[36:39], v[96:99], v[48:51]\nv_mfma_f32_16x16x32_f16 v[52:55], v[36:39], v[94:97], v[52:55]\n" \
  "v_mfma_f32_16x16x32_f16 v[72:75], v[40:43], v[102:105], v[72:75]\nv_mfma_f32_16x16x32_f16 v[48:51], v[40:43], v[100:103], v[48:51]\n" \
  "v_mfma_f32_16x16x32_f16 v[52:55], v[40:43], v[98:101], v[52:55]\nv_mfma_f32_16x16x32_f16 v[52:55], v[44:47], v[102:105], v[52:55]\ns_setprio 0\n" \
  "s_nop 3\nv_add_f32_dpp v116, v71, v74 wave_ror:1 row_mask:0x1 bank_mask:0x1\nv_add_f32_dpp v120, v75, v50 wave_ror:1 row_mask:0x1 bank_mask:0x1\n" \
  "s_nop 0\nv_add_f32_dpp v108, v51, v54 wave_ror:1 row_mask:0x1 bank_mask:0x1\nv_add_f32_dpp v116, v75, v74 wave_shr:1 row_mask:0xf bank_mask:0xf\n" \
  "v_add_f32_dpp v120, v51, v50 wave_shr:1 row_mask:0xf bank_mask:0xf\nv_add_f32_dpp v108, v55, v54 wave_shr:1 row_mask:0xf bank_mask:0xf\n" \
  "v_add_f32_dpp v115, v112, v73 wave_ror:1 row_mask:0x1 bank_mask:0x1\nv_add_f32_dpp v119, v116, v49 wave_ror:1 row_mask:0x1 bank_mask:0x1\n" \
  "v_add_f32_dpp v107, v120, v53 wave_ror:1 row_mask:0x1 bank_mask:0x1\nv_add_f32_dpp v115, v116, v73 wave_shr:1 row_mask:0xf bank_mask:0xf\n" \
  "v_add_f32_dpp v119, v120, v49 wave_shr:1 row_mask:0xf bank_mask:0xf\nv_add_f32_dpp v107, v108, v53 wave_shr:1 row_mask:0xf bank_mask:0xf\n" \
  "v_add_f32_dpp v114, v111, v72 wave_ror:1 row_mask:0x1 bank_mask:0x1\nv_add_f32_dpp v118, v115, v48 wave_ror:1 row_mask:0x1 bank_mask:0x1\n" \
  "v_add_f32_dpp v106, v119, v52 wave_ror:1 row_mask:0x1 bank_mask:0x1\nv_add_f32_dpp v114, v115, v72 wave_shr:1 row_mask:0xf bank_mask:0xf\n" \
  "v_add_f32_dpp v118, v119, v48 wave_shr:1 row_mask:0xf bank_mask:0xf\nv_add_f32_dpp v106, v107, v52 wave_shr:1 row_mask:0xf bank_mask:0xf\n" \
  "ds_write2st64_b32 v12, v114, v118 offset0:6 offset1:7\nv_mfma_f32_16x16x32_f16 v[56:59], v[28:31], v[84:87], 0\ns_setprio 1\n" \
  "v_mfma_f32_16x16x32_f16 v[60:63], v[28:31], v[82:85], 0\nv_mfma_f32_16x16x32_f16 v[64:67], v[28:31], v[80:83], 0\n" \
  "v_mfma_f32_16x16x32_f16 v[56:59], v[32:35], v[88:91], v[56:59]\nv_mfma_f32_16x16x32_f16 v[60:63], v[32:35], v[86:89], v[60:63]\n" \
  "v_mfma_f32_16x16x32_f16 v[64:67], v[32:35], v[84:87], v[64:67]\nv_mfma_f32_16x16x32_f16 v[56:59], v[36:39], v[92:95], v[56:59]\n" \
  "v_mfma_f32_16x16x32_f16 v[60:63], v[36:39], v[90:93], v[60:63]\nv_mfma_f32_16x16x32_f16 v[64:67], v[36:39], v[88:91], v[64:67]\n" \
  "v_mfma_f32_16x16x32_f16 v[56:59], v[40:43], v[96:99], v[56:59]\nv_mfma_f32_16x16x32_f16 v[60:63], v[40:43], v[94:97], v[60:63]\n" \
  "v_mfma_f32_16x16x32_f16 v[64:67], v[40:43], v[92:95], v[64:67]\nv_mfma_f32_16x16x32_f16 v[56:59], v[44:47], v[100:103], v[56:59]\n" \
  "v_mfma_f32_16x16x32_f16 v[60:63], v[44:47], v[98:101], v[60:63]\nv_mfma_f32_16x16x32_f16 v[64:67], v[44:47], v[96:99], v[64:67]\ns_setprio 0\n" \
  "s_nop 4\nv_add_f32_dpp v112, v55, v58 wave_ror:1 row_mask:0x1 bank_mask:0x1\nv_add_f32_dpp v116, v59, v62 wave_ror:1 row_mask:0x1 bank_mask:0x1\n" \
  "v_add_f32_dpp v120, v63, v66 wave_ror:1 row_mask:0x1 bank_mask:0x1\nv_add_f32_dpp v112, v59, v58 wave_shr:1 row_mask:0xf bank_mask:0xf\n" \
  "v_add_f32_dpp v116, v63, v62 wave_shr:1 row_mask:0xf bank_mask:0xf\nv_add_f32_dpp v120, v67, v66 wave_shr:1 row_mask:0xf bank_mask:0xf\n" \
  "v_add_f32_dpp v111, v108, v57 wave_ror:1 row_mask:0x1 bank_mask:0x1\nv_add_f32_dpp v115, v112, v61 wave_ror:1 row_mask:0x1 bank_mask:0x1\n" \
  "v_add_f32_dpp v119, v116, v65 wave_ror:1 row_mask:0x1 bank_mask:0x1\nv_add_f32_dpp v111, v112, v57 wave_shr:1 row_mask:0xf bank_mask:0xf\n" \
  "v_add_f32_dpp v115, v116, v61 wave_shr:1 row_mask:0xf bank_mask:0xf\nv_add_f32_dpp v119, v120, v65 wave_shr:1 row_mask:0xf bank_mask:0xf\n" \
  "v_add_f32_dpp v110, v107, v56 wave_ror:1 row_mask:0x1 bank_mask:0x1\nv_add_f32_dpp v114, v111, v60 wave_ror:1 row_mask:0x1 bank_mask:0x1\n" \
  "v_add_f32_dpp v118, v115, v64 wave_ror:1 row_mask:0x1 bank_mask:0x1\nv_add_f32_dpp v110, v111, v56 wave_shr:1 row_mask:0xf bank_mask:0xf\n" \
  "v_add_f32_dpp v114, v115, v60 wave_shr:1 row_mask:0xf bank_mask:0xf\nv_add_f32_dpp v118, v119, v64 wave_shr:1 row_mask:0xf bank_mask:0xf\n" \
  "ds_write2st64_b32 v12, v106, v110 offset0:8 offset1:9\nds_write2st64_b32 v12, v114, v118 offset0:10 offset1:11\n" \
  "v_mfma_f32_16x16x32_f16 v[68:71], v[28:31], v[78:81], 0\ns_setprio 1\nv_mfma_f32_16x16x32_f16 v[72:75], v[28:31], v[76:79], 0\n" \
  "v_mfma_f32_16x16x32_f16 v[68:71], v[32:35], v[82:85], v[68:71]\nv_mfma_f32_16x16x32_f16 v[72:75], v[32:35], v[80:83], v[72:75]\n" \
  "v_mfma_f32_16x16x32_f16 v[68:71], v[36:39], v[86:89], v[68:71]\nv_mfma_f32_16x16x32_f16 v[72:75], v[36:39], v[84:87], v[72:75]\n" \
  "v_mfma_f32_16x16x32_f16 v[68:71], v[40:43], v[90:93], v[68:71]\nv_mfma_f32_16x16x32_f16 v[72:75], v[40:43], v[88:91], v[72:75]\n" \
  "v_mfma_f32_16x16x32_f16 v[68:71], v[44:47], v[94:97], v[68:71]\nv_mfma_f32_16x16x32_f16 v[72:75], v[44:47], v[92:95], v[72:75]\ns_setprio 0\n" \
  "s_nop 5\nv_add_f32_dpp v108, v67, v70 wave_ror:1 row_mask:0x1 bank_mask:0x1\nv_add_f32_dpp v112, v71, v74 wave_ror:1 row_mask:0x1 bank_mask:0x1\n" \
  "s_nop 0\nv_add_f32_dpp v108, v71, v70 wave_shr:1 row_mask:0xf bank_mask:0xf\nv_add_f32_dpp v112, v75, v74 wave_shr:1 row_mask:0xf bank_mask:0xf\n" \
  "v_add_f32_dpp v107, v120, v69 wave_ror:1 row_mask:0x1 bank_mask:0x1\nv_add_f32_dpp v111, v108, v73 wave_ror:1 row_mask:0x1 bank_mask:0x1\ns_nop 0\n" \
  "v_add_f32_dpp v107, v108, v69 wave_shr:1 row_mask:0xf bank_mask:0xf\nv_add_f32_dpp v111, v112, v73 wave_shr:1 row_mask:0xf bank_mask:0xf\n" \
  "v_add_f32_dpp v106, v119, v68 wave_ror:1 row_mask:0x1 bank_mask:0x1\nv_add_f32_dpp v110, v107, v72 wave_ror:1 row_mask:0x1 bank_mask:0x1\ns_nop 0\n" \
  "v_add_f32_dpp v106, v107, v68 wave_shr:1 row_mask:0xf bank_mask:0xf\nv_add_f32_dpp v110, v111, v72 wave_shr:1 row_mask:0xf bank_mask:0xf\n" \
  "ds_write2st64_b32 v12, v106, v110 offset0:12 offset1:13\nv_mov_b32 v113, v75\ns_mov_b64 exec, s[92:93]\nds_write_b128 v13, v[110:113]\n" \
  "s_mov_b64 exec, -1\ns_setpc_b64 s[90:91]\n11:\nds_read_b128 v[68:71], v13 offset:2976\nds_read_b128 v[72:75], v13 offset:2992\n" \
  "ds_read2_b32 v[52:53], v15 offset0:0 offset1:1\nds_read2_b32 v[54:55], v15 offset0:8 offset1:9\nds_read2_b32 v[56:57], v15 offset0:16 offset1:17\n" \
  "ds_read2_b32 v[58:59], v15 offset0:24 offset1:25\nds_read2_b32 v[60:61], v15 offset0:32 offset1:33\n" \
  "ds_read2_b32 v[62:63], v15 offset0:40 offset1:41\nds_read2_b32 v[64:65], v15 offset0:48 offset1:49\n" \
  "ds_read2_b32 v[66:67], v15 offset0:56 offset1:57\ns_waitcnt lgkmcnt(8)\nv_alignbit_b32 v76, v69, v68, 16\nv_alignbit_b32 v77, v70, v69, 16\n" \
  "v_alignbit_b32 v78, v71, v70, 16\nv_alignbit_b32 v79, v72, v71, 16\nv_alignbit_b32 v80, v73, v72, 16\nv_alignbit_b32 v81, v74, v73, 16\n" \
  "v_alignbit_b32 v82, v75, v74, 16\ns_waitcnt lgkmcnt(0)\nds_read_b64 v[28:29], v11 offset:0\nds_read_b64 v[30:31], v11 offset:32\n" \
  "ds_read_b64 v[32:33], v11 offset:64\nds_read_b64 v[34:35], v11 offset:96\nds_read_b64 v[36:37], v11 offset:128\n" \
  "ds_read_b64 v[38:39], v11 offset:160\nds_read_b64 v[40:41], v11 offset:192\nds_read_b64 v[42:43], v11 offset:224\n" \
  "ds_read_b64 v[44:45], v11 offset:256\nds_read_b64 v[46:47], v11 offset:288\nds_read_b64 v[48:49], v11 offset:320\n" \
  "ds_read_b64 v[50:51], v11 offset:352\nv_pk_add_f16 v84, v77, v78\nv_pk_add_f16 v85, v78, v79\nv_pk_add_f16 v86, v79, v80\n" \
  "v_pk_add_f16 v87, v80, v81\nv_pk_add_f16 v84, v70, v84\nv_pk_add_f16 v85, v71, v85\nv_pk_add_f16 v86, v72, v86\nv_pk_add_f16 v87, v73, v87\n" \
  "ds_write_b128 v13, v[84:87] offset:7120\nv_pk_add_f16 v88, v69, v71\nv_pk_add_f16 v89, v70, v72\nv_pk_add_f16 v90, v71, v73\n" \
  "v_pk_add_f16 v91, v72, v74\nv_pk_add_f16 v88, v84, v88\nv_pk_add_f16 v89, v85, v89\nv_pk_add_f16 v90, v86, v90\nv_pk_add_f16 v91, v87, v91\n" \
  "s_waitcnt lgkmcnt(1)\nds_write_b128 v13, v[88:91] offset:10320\nv_pk_add_f16 v84, v76, v79\nv_pk_add_f16 v85, v77, v80\n" \
  "v_pk_add_f16 v86, v78, v81\nv_pk_add_f16 v87, v79, v82\nv_pk_add_f16 v84, v88, v84\nv_pk_add_f16 v85, v89, v85\nv_pk_add_f16 v86, v90, v86\n" \
  "v_pk_add_f16 v87, v91, v87\nds_write_b128 v13, v[84:87] offset:13520\nv_pk_add_f16 v88, v68, v72\nv_pk_add_f16 v89, v69, v73\n" \
  "v_pk_add_f16 v90, v70, v74\nv_pk_add_f16 v91, v71, v75\nv_pk_add_f16 v88, v84, v88\nv_pk_add_f16 v89, v85, v89\nv_pk_add_f16 v90, v86, v90\n" \
  "v_pk_add_f16 v91, v87, v91\nds_write_b128 v13, v[88:91] offset:15712\nds_read_b64_tr_b16 v[92:93], v14 offset:256\n" \
  "ds_read_b64_tr_b16 v[94:95], v14 offset:0\nds_read_b64_tr_b16 v[96:97], v14 offset:768\nds_read_b64_tr_b16 v[98:99], v14 offset:512\n" \
  "ds_read_b64_tr_b16 v[100:101], v10 offset:128\nds_read_b64_tr_b16 v[102:103], v10 offset:0\ns_waitcnt lgkmcnt(4)\n" \
  "v_mfma_f32_16x16x32_f16 v[76:79], v[52:55], v[92:95], 0\ns_setprio 1\nds_read_b64_tr_b16 v[104:105], v10 offset:384\n" \
  "ds_read_b64_tr_b16 v[106:107], v10 offset:256\nv_mfma_f32_16x16x32_f16 v[80:83], v[56:59], v[92:95], 0\n" \
  "v_mfma_f32_16x16x32_f16 v[84:87], v[60:63], v[92:95], 0\nv_mfma_f32_16x16x32_f16 v[88:91], v[64:67], v[92:95], 0\ns_waitcnt lgkmcnt(4)\n" \
  "v_mfma_f32_16x16x32_f16 v[80:83], v[52:55], v[96:99], v[80:83]\nds_read_b64_tr_b16 v[92:93], v10 offset:640\n" \
  "ds_read_b64_tr_b16 v[94:95], v10 offset:512\nv_mfma_f32_16x16x32_f16 v[84:87], v[56:59], v[96:99], v[84:87]\n" \
  "v_mfma_f32_16x16x32_f16 v[88:91], v[60:63], v[96:99], v[88:91]\ns_waitcnt lgkmcnt(4)\nv_mfma_f32_16x16x32_f16 v[68:71], v[32:35], v[100:103], 0\n" \
  "ds_read_b64_tr_b16 v[96:97], v10 offset:896\nds_read_b64_tr_b16 v[98:99], v10 offset:768\n" \
  "v_mfma_f32_16x16x32_f16 v[72:75], v[40:43], v[100:103], 0\nv_mfma_f32_16x16x32_f16 v[76:79], v[48:51], v[100:103], v[76:79]\ns_waitcnt lgkmcnt(4)\n" \
  "v_mfma_f32_16x16x32_f16 v[68:71], v[28:31], v[104:107], v[68:71]\ns_movk_i32 s88, 0x4000\n14:\nds_read_b32 v108, v13 offset:0\n" \
  "s_sub_u32 s88, s88, 1\ns_cbranch_scc1 15f\ns_waitcnt lgkmcnt(0)\nv_readfirstlane_b32 s87, v108\ns_cmp_lt_u32 s87, 2\ns_cbranch_scc1 14b\n15:\n" \
  "ds_read_b64_tr_b16 v[100:101], v14 offset:1280\nds_read_b64_tr_b16 v[102:103], v14 offset:1024\n" \
  "v_mfma_f32_16x16x32_f16 v[72:75], v[36:39], v[104:107], v[72:75]\nv_mfma_f32_16x16x32_f16 v[76:79], v[44:47], v[104:107], v[76:79]\n" \
  "v_mfma_f32_16x16x32_f16 v[72:75], v[32:35], v[92:95], v[72:75]\nds_read_b64_tr_b16 v[104:105], v14 offset:1792\n" \
  "ds_read_b64_tr_b16 v[106:107], v14 offset:1536\nv_mfma_f32_16x16x32_f16 v[76:79], v[40:43], v[92:95], v[76:79]\n" \
  "v_mfma_f32_16x16x32_f16 v[80:83], v[48:51], v[92:95], v[80:83]\nds_write2st64_b32 v12, v68, v69 offset0:0 offset1:1\n" \
  "ds_write2st64_b32 v12, v70, v71 offset0:2 offset1:3\nv_mfma_f32_16x16x32_f16 v[72:75], v[28:31], v[96:99], v[72:75]\n" \
  "ds_read_b64_tr_b16 v[92:93], v10 offset:1152\nds_read_b64_tr_b16 v[94:95], v10 offset:1024\n" \
  "v_mfma_f32_16x16x32_f16 v[76:79], v[36:39], v[96:99], v[76:79]\nv_mfma_f32_16x16x32_f16 v[80:83], v[44:47], v[96:99], v[80:83]\n" \
  "s_waitcnt lgkmcnt(6)\nv_mfma_f32_16x16x32_f16 v[84:87], v[52:55], v[100:103], v[84:87]\nds_read_b64_tr_b16 v[96:97], v10 offset:1408\n" \
  "ds_read_b64_tr_b16 v[98:99], v10 offset:1280\nv_mfma_f32_16x16x32_f16 v[88:91], v[56:59], v[100:103], v[88:91]\n" \
  "ds_write2st64_b32 v12, v72, v73 offset0:4 offset1:5\nds_write2st64_b32 v12, v74, v75 offset0:6 offset1:7\ns_waitcnt lgkmcnt(8)\n" \
  "v_mfma_f32_16x16x32_f16 v[88:91], v[52:55], v[104:107], v[88:91]\nds_read_b64_tr_b16 v[100:101], v10 offset:1664\n" \
  "ds_read_b64_tr_b16 v[102:103], v10 offset:1536\ns_waitcnt lgkmcnt(6)\nv_mfma_f32_16x16x32_f16 v[76:79], v[32:35], v[92:95], v[76:79]\n" \
  "ds_read_b64_tr_b16 v[104:105], v10 offset:1920\nds_read_b64_tr_b16 v[106:107], v10 offset:1792\n" \
  "v_mfma_f32_16x16x32_f16 v[80:83], v[40:43], v[92:95], v[80:83]\nv_mfma_f32_16x16x32_f16 v[84:87], v[48:51], v[92:95], v[84:87]\n" \
  "s_waitcnt lgkmcnt(6)\nv_mfma_f32_16x16x32_f16 v[76:79], v[28:31], v[96:99], v[76:79]\nds_read_b64_tr_b16 v[92:93], v10 offset:2176\n" \
  "ds_read_b64_tr_b16 v[94:95], v10 offset:2048\nv_mfma_f32_16x16x32_f16 v[80:83], v[36:39], v[96:99], v[80:83]\n" \
  "v_mfma_f32_16x16x32_f16 v[84:87], v[44:47], v[96:99], v[84:87]\ns_waitcnt lgkmcnt(4)\n" \
  "v_mfma_f32_16x16x32_f16 v[80:83], v[32:35], v[100:103], v[80:83]\nds_read_b64_tr_b16 v[96:97], v10 offset:2432\n" \
  "ds_read_b64_tr_b16 v[98:99], v10 offset:2304\nv_mfma_f32_16x16x32_f16 v[84:87], v[40:43], v[100:103], v[84:87]\n" \
  "v_mfma_f32_16x16x32_f16 v[88:91], v[48:51], v[100:103], v[88:91]\nds_write2st64_b32 v12, v76, v77 offset0:8 offset1:9\n" \
  "ds_write2st64_b32 v12, v78, v79 offset0:10 offset1:11\ns_waitcnt lgkmcnt(6)\nv_mfma_f32_16x16x32_f16 v[80:83], v[28:31], v[104:107], v[80:83]\n" \
  "ds_read_b64_tr_b16 v[100:101], v10 offset:2688\nds_read_b64_tr_b16 v[102:103], v10 offset:2560\n" \
  "v_mfma_f32_16x16x32_f16 v[84:87], v[36:39], v[104:107], v[84:87]\nv_mfma_f32_16x16x32_f16 v[88:91], v[44:47], v[104:107], v[88:91]\n" \
  "s_waitcnt lgkmcnt(6)\nv_mfma_f32_16x16x32_f16 v[84:87], v[32:35], v[92:95], v[84:87]\nds_read_b64_tr_b16 v[104:105], v10 offset:2944\n" \
  "ds_read_b64_tr_b16 v[106:107], v10 offset:2816\nv_mfma_f32_16x16x32_f16 v[88:91], v[40:43], v[92:95], v[88:91]\n" \
  "ds_write2st64_b32 v12, v80, v81 offset0:12 offset1:13\nds_write2st64_b32 v12, v82, v83 offset0:14 offset1:15\ns_waitcnt lgkmcnt(8)\n" \
  "v_mfma_f32_16x16x32_f16 v[84:87], v[28:31], v[96:99], v[84:87]\nv_mfma_f32_16x16x32_f16 v[88:91], v[36:39], v[96:99], v[88:91]\n" \
  "s_waitcnt lgkmcnt(4)\nv_mfma_f32_16x16x32_f16 v[88:91], v[32:35], v[100:103], v[88:91]\ns_waitcnt lgkmcnt(2)\n" \
  "v_mfma_f32_16x16x32_f16 v[88:91], v[28:31], v[104:107], v[88:91]\ns_setprio 0\ns_nop 1\nds_write2st64_b32 v12, v84, v85 offset0:16 offset1:17\n" \
  "ds_write2st64_b32 v12, v86, v87 offset0:18 offset1:19\ns_nop 2\nds_write2st64_b32 v12, v88, v89 offset0:20 offset1:21\n" \
  "ds_write2st64_b32 v12, v90, v91 offset0:22 offset1:23\ns_setpc_b64 s[90:91]\n"
#define CONV_CLOB "memory", "scc", "s87", "s88", "s90", "s91", "s92", "s93", "v28", "v29", "v30", "v31", "v32", "v33", "v34", "v35", "v36", "v37", "v38", "v39", "v40", "v41", "v42", "v43", "v44", "v45", "v46", "v47", "v48", "v49", "v50", "v51", "v52", "v53", "v54", "v55", "v56", "v57", "v58", "v59", "v60", "v61", "v62", "v63", "v64", "v65", "v66", "v67", "v68", "v69", "v70", "v71", "v72", "v73", "v74", "v75", "v76", "v77", "v78", "v79", "v80", "v81", "v82", "v83", "v84", "v85", "v86", "v87", "v88", "v89", "v90", "v91", "v92", "v93", "v94", "v95", "v96", "v97", "v98", "v99", "v100", "v101", "v102", "v103", "v104", "v105", "v106", "v107", "v108", "v109", "v110", "v111", "v112", "v113", "v114", "v115", "v116", "v117", "v118", "v119", "v120", "v121", "v122", "v123"
constexpr int C_T = 32, PLP = 448, C_ZS = 2720, C_UL = 2992, C_G3 = 7088, C_G5 = 10288, C_G7 = 13488, C_G9 = 15680,
              C_YD = 17872, C_CY = 26064, C_YF = 26128, C_ES = 32272, CB = 32304, C_NZ = C_UL / 16;
constexpr int RP = 1216;
constexpr int LP_PART = 10240, LA = 10240, LW = 36864, LM = 73728;
constexpr int SMEM = LM + 2048;
struct COps { uint32_t a, b, c, d, e, f; };
__device__ __forceinline__ void conv_ops(uint32_t cb, int role, int lane, COps& e) {
  const uint32_t kq = lane >> 4, q4 = (lane >> 2) & 3, p4 = lane & 3, l4 = lane >> 2;
  const uint32_t g0 = (0x170e00u >> (8 * role)) & 0xffu, fm = (role == 3) ? 0xffffffffu : 0u;
  const uint32_t t1 = 8u * (p4 - l4), t2 = (l4 & 1u) * 16u;
  e.a = cb + (fm ? C_G3 + 128 : C_UL + 96 - 2048) + 128u * g0 + t1 + t2 * (fm & (uint32_t)((C_G5 - C_G3) / 16));
  e.b = cb + (fm ? C_T + 4 * PLP - 120 : C_T - 24) + p4 * (fm ? 32u : (uint32_t)PLP) + 8u * (q4 + kq);
  e.c = cb + (fm ? C_YF : C_YD) + 256u * g0 + 4u * lane;
  const uint32_t l16 = cb + 16u * lane + (3072u - 1024u * role);
  if (role == 3) {
    e.d = l16;
    e.e = cb + C_G7 + 248 + t1 - 8u * l4 + t2 * (uint32_t)((C_G9 - C_G7) / 16);
    e.f = cb + C_T + 5 * PLP - 60 + 4u * (q4 + 4u * p4 + 2u * kq);
  } else {
    e.d = cb + C_CY + 16u * role;
    e.e = l16;
    e.f = cb;
  }
}
__device__ __forceinline__ int pad_at(int r) {
  return C_G3 + ((r >> 1) & 3) * (C_G5 - C_G3) - ((r >= 6) ? 2 * C_G5 - C_G3 + C_G5 - C_G3 - C_G9 : 0) + 16 * (r & 1);
}
static_assert(C_G7 - C_G5 == C_G5 - C_G3, "");
__device__ __forceinline__ void table_at(int& g, bool sp, int cb, int& ds, int& dh) {
  if (sp) asm("v_writelane_b32 %0, %1, 59\n\tv_writelane_b32 %0, %1, 60\n\tv_writelane_b32 %0, %1, 61\n\tv_writelane_b32 %0, %2, 62\n\tv_writelane_b32 %0, %2, 63"
              : "+v"(g) : "s"(127), "s"(159));
  ds = cb + C_T + (g >> 5) * (PLP - 256) + 8 * g;
  dh = ds + 6;
  if (sp) asm("v_writelane_b32 %0, %1, 59\n\tv_writelane_b32 %0, %2, 60\n\tv_writelane_b32 %0, %3, 61\n\tv_writelane_b32 %0, %4, 62\n\tv_writelane_b32 %0, %5, 63"
              : "+v"(dh) : "s"(cb + C_T + 256), "s"(cb + C_T + PLP + 256), "s"(cb + C_T + 2 * PLP + 256), "s"(cb + C_T + 3 * PLP + 288), "s"(cb + C_T + 290));
}
__device__ __forceinline__ void image_put(float* part, int lx, int ly2, const f32x4* acc) {
  asm volatile("" ::: "memory");
  part[lx + 447] = acc[1][0]; part[lx + 767] = acc[2][0];
  asm volatile("" ::: "memory");
  part[lx] = acc[0][0]; part[lx + 128] = acc[0][1]; part[lx + 256] = acc[0][2]; part[lx + 384] = acc[0][3];
  part[lx + 64] = acc[1][0]; part[lx + 192] = acc[1][0]; part[lx + 320] = acc[1][2];
  part[lx + 511] = acc[1][1]; part[lx + 575] = acc[1][3];
  part[ly2 + 639] = acc[2][1]; part[ly2 + 671] = acc[2][3];
}
__device__ __forceinline__ void skip_term(const u32x4 uw, float d32, f32x2* ud) {
#pragma unroll
  for (int e = 0; e < 4; ++e) {
    const uint32_t uu = uw[e];
    float lo, hi;
    asm("v_fma_mix_f32 %0, %2, %3, 0 op_sel_hi:[1,0,0]\n\tv_fma_mix_f32 %1, %2, %3, 0 op_sel:[1,0,0] op_sel_hi:[1,0,0]"
        : "=&v"(lo), "=&v"(hi) : "v"(uu), "v"(d32));
    const uint32_t pk = pack2(lo, hi);
    ud[e] = (f32x2){bflo(pk), bfhi(pk)};
  }
  asm volatile("" : "+v"(ud[0]), "+v"(ud[1]), "+v"(ud[2]), "+v"(ud[3]));
}
__device__ __forceinline__ u32x4 conv_out(const char* smem, uint32_t ry, uint32_t rf, uint32_t rc, float ys, const f32x2* ud) {
  const f32x4 a0 = *(const f32x4*)(smem + ry), a1 = *(const f32x4*)(smem + ry + 16), cy = *(const f32x4*)(smem + rc);
  const f32x4 f0 = *(const f32x4*)(smem + rf), f1 = *(const f32x4*)(smem + rf + 16);
  f32x4 y0 = a0 + f0;
  const f32x4 y1 = a1 + f1;
  y0[0] += cy[1]; y0[1] += cy[2]; y0[2] += cy[3];
  const uint32_t yb[4] = {pack2(y0[0], y0[1]), pack2(y0[2], y0[3]), pack2(y1[0], y1[1]), pack2(y1[2], y1[3])};
  u32x4 ov;
#pragma unroll
  for (int e = 0; e < 4; ++e) {
    const f32x2 r = __builtin_elementwise_fma((f32x2){bflo(yb[e]), bfhi(yb[e])}, (f32x2){ys, ys}, ud[e]);
    ov[e] = min(pack2(r[0], r[1]), 0xfffffffeu);
  }
  return ov;
}
#define CONV_RUN(role, e, k0, k1, k2, k3, k4, k5, ...) do { int role_ = (role); \
    asm volatile("s_call_b64 s[90:91], kf_conv" : "+{s89}"(role_), "+{v10}"(e.a), "+{v11}"(e.b), "+{v12}"(e.c), "+{v13}"(e.d), "+{v14}"(e.e), "+{v15}"(e.f), \
                 "+{v16}"(k0), "+{v17}"(k1), "+{v18}"(k2), "+{v19}"(k3), "+{v20}"(k4), "+{v21}"(k5) __VA_ARGS__ : : CONV_CLOB); } while (0)

template <class F>
__device__ __forceinline__ void ln_two(const uint32_t* v, int lane, float sc, uint32_t* c, f32x2* t, f32x4 z, F&& mid) {
  float s1a = z[0], s2a = z[1], s1b = z[2], s2b = z[3];
#pragma unroll
  for (int k = 0; k < 4; ++k) {
    s1a = dot2(v[k], 0x3f803f80u, s1a); s2a = dot2(v[k], v[k], s2a);
    s1b = dot2(v[4 + k], 0x3f803f80u, s1b); s2b = dot2(v[4 + k], v[4 + k], s2b);
  }
#pragma unroll
  for (int k = 0; k < 9; ++k) t[k] = (f32x2){bflo(v[k]), bfhi(v[k])};
  const float rs = row_sum(fold16(dot2(v[8], 0x3f803f80u, fold32(s1a, s1b)), dot2(v[8], v[8], fold32(s2a, s2b))));
  const f32x2 mean = (f32x2){rdl(rs, 15), rdl(rs, 47)} * (1.f / D_), m2 = mean * mean;
  f32x2 var = (f32x2){rdl(rs, 31), rdl(rs, 63)} * (1.f / D_) - m2;
  asm volatile("" : "+v"(t[0]), "+v"(t[1]), "+v"(t[2]), "+v"(t[3]), "+v"(t[4]), "+v"(t[5]), "+v"(t[6]), "+v"(t[7]), "+v"(t[8]));
  if (__builtin_expect(!(m2[0] <= 16.f * var[0] && m2[1] <= 16.f * var[1]), 0)) {
    float q[3] = {0.f, 0.f, 0.f};
#pragma unroll
    for (int k = 0; k < 9; ++k) {
      const f32x2 a = t[k] - ((k < 4 || (k == 8 && lane < 32)) ? mean[0] : mean[1]);
      q[k >> 2] += a[0] * a[0] + a[1] * a[1];
    }
    float qa = q[0] + ((lane < 32) ? q[2] : 0.f), qb = q[1] + ((lane < 32) ? 0.f : q[2]);
    wave_sum2(qa, qb);
    var = (f32x2){qa, qb} * (1.f / D_);
  }
  mid();
  const uint32_t mp = pack2(mean[0], mean[1]), vp = pack2(var[0], var[1]);
  const f32x2 ve = (f32x2){bflo(vp), bfhi(vp)} + 1e-5f;
  const uint32_t e2 = pack2(ve[0], ve[1]);
  const uint32_t sp = pack2(__builtin_amdgcn_sqrtf(bflo(e2)), __builtin_amdgcn_sqrtf(bfhi(e2)));
  const f32x2 rc = (f32x2){__builtin_amdgcn_rcpf(bflo(sp)), __builtin_amdgcn_rcpf(bfhi(sp))} * sc;
  const float nm[3] = {bflo(mp), bfhi(mp), (lane < 32) ? bflo(mp) : bfhi(mp)}, rr[3] = {rc[0], rc[1], (lane < 32) ? rc[0] : rc[1]};
#pragma unroll
  for (int k = 0; k < 9; ++k) { const f32x2 d = t[k] - nm[k >> 2]; c[k] = pack2(d[0], d[1]); }
#pragma unroll
  for (int k = 0; k < 9; ++k) { const f32x2 d = (f32x2){bflo(c[k]), bfhi(c[k])} * rr[k >> 2]; c[k] = pack2(d[0], d[1]); }
}
__device__ __forceinline__ uint32_t med3u(uint32_t a, uint32_t b, uint32_t c) { return max(min(a, b), min(max(a, b), c)); }
__device__ __forceinline__ uint32_t oct_maxu(uint32_t v) {
  v = max(v, (uint32_t)DPP_I((int)v, 0xb1, 0xf));
  v = max(v, (uint32_t)DPP_I((int)v, 0x4e, 0xf));
  v = max(v, (uint32_t)DPP_I((int)v, 0x141, 0xf));
  return v;
}

#define KARGS const u16* __restrict__ x, const u16* __restrict__ Are, const u16* __restrict__ Aim, const u16* __restrict__ Bre, \
    const u16* __restrict__ Bim, const u16* __restrict__ Cre, const u16* __restrict__ Cim, const u16* __restrict__ logdt, \
    const u16* __restrict__ Dskip, char* __restrict__ ws, const u16* __restrict__ wr, const u16* __restrict__ wg, \
    const u16* __restrict__ wu, const u16* __restrict__ wd, u16* __restrict__ out
#define KPASS x, Are, Aim, Bre, Bim, Cre, Cim, logdt, Dskip, ws, wr, wg, wu, wd, out
template <int ROLE>
__device__ __forceinline__ void role_body(char* smem, KARGS) {
  const int tid = threadIdx.x, lane = tid & 63;
  const int w = __builtin_amdgcn_readfirstlane(tid >> 6), wl = w & 3;
  const int b = blockIdx.x & 255, bx = b & 7, bj = b >> 3;
  constexpr bool isl = ROLE == 1 || ROLE == 2;
  const int tg = 32 * bx + bj;
  const int dP = 512 + 8 * ((bx + XOFF) & 7) + ((bj >> 1) & 7), pq = bj & 1;
  const int d = isl ? dP : 2 * tg + (wl >> 1);
  u32x4 xa, xc;
  uint32_t ep, r0, r1, r2, r3, r4, r5, r6, r7;
  if constexpr (ROLE != 2) {
  } else if constexpr (ROLE == 2) {
    const uint32_t po = (uint32_t)(d * N_ + 64 * pq + 16 * wl + (lane & 15)) * 2u;
    asm volatile("s_cmp_lt_u32 %22, 16\n\ts_cbranch_scc0 1f\n\t"
                 "global_load_ushort %1, %10, %13" XPOL "\n\tglobal_load_ushort %2, %9, %14" XPOL "\n\tglobal_load_ushort %3, %9, %15" XPOL "\n\t"
                 "global_load_ushort %4, %9, %16" XPOL "\n\tglobal_load_ushort %5, %9, %17" XPOL "\n\tglobal_load_ushort %6, %9, %18" XPOL "\n\t"
                 "global_load_ushort %7, %9, %19" XPOL "\n1:\n\tglobal_load_ushort %8, %11, %20" XPOL "\n\tglobal_load_dword %0, %12, %21 sc1"
                 : "=&v"(ep), "=&v"(r0), "=&v"(r1), "=&v"(r2), "=&v"(r3), "=&v"(r4), "=&v"(r5), "=&v"(r6), "=&v"(r7)
                 : "v"(po), "v"(2u * d), "v"(2u * (512 - 24 + 8 * bx + bj)), "v"(O_EP), "s"(logdt), "s"(Are), "s"(Aim),
                   "s"(Bre), "s"(Bim), "s"(Cre), "s"(Cim), "s"(Dskip), "s"(ws), "s"(bj) : "scc");
  }
#define K_WAIT(n) asm volatile("s_waitcnt vmcnt(" #n ")\n\tv_lshlrev_b32 %0, 16, %8\n\tv_lshlrev_b32 %1, 16, %9\n\tv_lshlrev_b32 %2, 16, %10\n\t" \
    "v_lshlrev_b32 %3, 16, %11\n\tv_lshlrev_b32 %4, 16, %12\n\tv_lshlrev_b32 %5, 16, %13\n\tv_lshlrev_b32 %6, 16, %14\n\tv_lshlrev_b32 %7, 16, %15" \
    : "=&v"(kp.ldt), "=&v"(kp.are), "=&v"(kp.aim), "=&v"(kp.bre), "=&v"(kp.bim), "=&v"(kp.cre), "=&v"(kp.cim), "=&v"(dsk) \
    : "v"(r0), "v"(r1), "v"(r2), "v"(r3), "v"(r4), "v"(r5), "v"(r6), "v"(r7))
  TSD;
  TS(0, 0);
  const uint32_t ck0 = (ROLE == 0) ? 0u : (uint32_t)__builtin_amdgcn_s_memtime(), rt0 = (ROLE == 0) ? 0u : (uint32_t)__builtin_amdgcn_s_memrealtime();
  const uint32_t lb0 = (uint32_t)(uintptr_t)(__attribute__((address_space(3))) char*)smem;
  const int i = lane & 15, kq = lane >> 4;
  const u32x4 ones = {0xffffffffu, 0xffffffffu, 0xffffffffu, 0xffffffffu};
  if constexpr (!isl) {
    if (ROLE == 0) __builtin_amdgcn_s_setprio(KPRIO);
    constexpr int wh = ROLE == 3;
    char* sm = smem + (wl >> 1) * CB;
    float* prm = (float*)(sm + C_G3) + (wl & 1) * 1152;
    float* part = (float*)(sm + C_YD) + (2 * (wl & 1) + wh) * CP;
    float lx0 = (float)kbase(i), ly = (float)(8 * i), dsk, ox;
    int pp = (2 * kq + 16 * wh) * PS;
    int o1 = pp + 8 + (i & 1) * (4 + 2 * (i & 2)), o2 = pp + 20 + (i & 1) * (4 + (i & 4));
    int lx = 2 * i + (kq & 1) + 32 * (kq >> 1), ly2 = i + 16 * (kq >> 1) + 64 * (kq & 1);
    asm volatile("" : "+v"(lx0), "+v"(ly), "+v"(o1), "+v"(o2), "+v"(pp), "+v"(lx), "+v"(ly2));
    const int role = (w + wh) & 3;
    COps e;
    u32x4 ux;
    f32x2 ud[4];
    uint32_t vo, rf, rc, ru, ry, T = 0u;
    int gm, ds, dh;
    auto late_terms = [&] {
      const int te = tid >> 1;
      conv_ops(lb0 + (uint32_t)(wh * CB), role, lane, e);
      vo = v_at(2 * tg + (tid & 1), te);
      const uint32_t cb = (tid & 1) * CB;
      ry = cb + C_YD + 32 * te;
      ru = cb + C_UL + 16 * te;
      rc = cb + C_ZS;
      rf = (w < 2) ? rc : ry + (C_YF - 2048 - C_YD);
      if (w == 3 + 2 * wh) rc = (te == 112 + 72 * wh) ? cb + C_CY + 16 * wh : rc;
      gm = 96 * (wl & 1) + 48 * wh + min(lane, 47);
      table_at(gm, wh && (wl & 1), (wl >> 1) * CB, ds, dh);
      gm = (wl >> 1) * CB + C_YD + 16 * gm;
      asm volatile("" : "+v"(ry));
      asm volatile("" : "+v"(vo), "+v"(e.a), "+v"(e.b), "+v"(e.c), "+v"(e.d), "+v"(e.e), "+v"(e.f), "+v"(rf), "+v"(rc), "+v"(ru), "+v"(gm), "+v"(ds), "+v"(dh));
    };
    float sa, sb, ys;
    auto scales = [&] {
      const u32x2 ev = *(const u32x2*)(sm + C_ES), ew = *(const u32x2*)(smem + (tid & 1) * CB + C_ES);
      const int emax = min(max(max((int)ev[0], (int)ev[1]), -110), 115);
      sa = __builtin_amdgcn_ldexpf(1.f, (int)ev[0] - 22 - emax); sb = __builtin_amdgcn_ldexpf(1.f, (int)ev[1] - 22 - emax);
      ys = __int_as_float((127 - 7 + 5 + min(max(max((int)ew[0], (int)ew[1]), -110), 115)) << 23);
      asm volatile("" : "+v"(sa), "+v"(sb), "+v"(ys));
    };
    auto table = [&] {
      const float* pr = (const float*)(smem + gm);
      const f32x4 k0 = (*(const f32x4*)pr + *(const f32x4*)(pr + CP)) * sa + (*(const f32x4*)(pr + 2 * CP) + *(const f32x4*)(pr + 3 * CP)) * sb;
      const u32x2 kh = {packh(k0.xy), packh(k0.zw)};
      *(u32x2*)(smem + ds) = kh;
      *(u16*)(smem + dh) = (u16)(kh[1] >> 16);
    };
    auto steps = [&](auto&& mid) {
      f32x4 acc[3];
#pragma unroll
      for (int t = 0; t < 3; ++t) acc[t] = (f32x4){0.f, 0.f, 0.f, 0.f};
#pragma unroll
      for (int g = 0; g < 2; ++g) {
        u32x4 xh[3], yh;
        kgen_pair2(prm + pp + 8 * g * PS, prm + o1 + 8 * g * PS, prm + o2 + 8 * g * PS, lx0, ly, ox, xh, yh, 0);
        if (g == 1) mid();
        kgen_pair2(prm + pp + (8 * g + 1) * PS, prm + o1 + (8 * g + 1) * PS, prm + o2 + (8 * g + 1) * PS, lx0, ly, ox, xh, yh, 2);
        kgen_mma(xh, yh, acc);
      }
      image_put(part, lx, ly2, acc);
    };
    if constexpr (ROLE == 0) {
      int ps = (lane >> 1) * PS + (lane & 1);
      asm volatile("" : "+v"(ps));
      if (KLATE == 0) late_terms();
      KPar kp;
      int eb;
      asm volatile("s_waitcnt vmcnt(0)\n\tv_lshlrev_b32 %0, 16, v100\n\tv_lshlrev_b32 %1, 16, v101\n\tv_lshlrev_b32 %2, 16, v102\n\t"
                   "v_lshlrev_b32 %3, 16, v103\n\tv_lshlrev_b32 %4, 16, v104\n\tv_lshlrev_b32 %5, 16, v105\n\tv_lshlrev_b32 %6, 16, v106\n\tv_lshlrev_b32 %7, 16, v107"
                   : "=v"(kp.ldt), "=v"(kp.are), "=v"(kp.aim), "=v"(kp.bre), "=v"(kp.bim), "=v"(kp.cre), "=v"(kp.cim), "=v"(dsk)
                   : : "v100", "v101", "v102", "v103", "v104", "v105", "v106", "v107");
      TS(0, 1);
      {
        const f32x4 pb = kgen_base<64>(kp, ox, eb);
        float* pw = prm + ps;
        pw[0] = pb[0]; pw[2] = pb[1]; pw[4] = pb[2]; pw[6] = pb[3];
        const float kc[6] = {256.f, 511.f, 765.f, 1792.f, 512.f, 1020.f};
#pragma unroll
        for (int j = 0; j < 6; ++j) {
          float er, ei;
          cexp2(pb[0], pb[1], kc[j], er, ei);
          pw[8 + 4 * j] = er; pw[10 + 4 * j] = ei;
        }
      }
      ((int*)(sm + C_ES))[wl & 1] = eb;
      ((float*)(sm + C_ES))[2 + (wl & 1)] = ox;
      TS(0, 8);
      if (KLATE == 1) late_terms();
      BAR();
      if (KLATE == 2) late_terms();
      scales();
      dsk *= 32.f;
      steps([] {});
      if (KLATE == 3) late_terms();
      TS(0, 2);
      BAR();
      TS(0, 9);
      table();
    } else {
      const int tl = tid - 256;
      if (tl < C_NZ) {
        *(u32x4*)(smem + 16 * tl) = (u32x4){0u, 0u, 0u, 0u};
        *(u32x4*)(smem + CB + 16 * tl) = (u32x4){0u, 0u, 0u, 0u};
      }
      uint32_t uo = u_at(tl >> 1, 2 * tg + (tid & 1)), go = O_SU + (uint32_t)(256 * bx + 32 * wl + (lane & 31) + 4 * (lane & 32)) * 128u;
      asm volatile("" : "+v"(uo), "+v"(go));
      asm volatile("global_load_ushort %0, %2, %4\n\tglobal_load_dword %1, %3, %5 sc1" : "=&v"(r7), "=&v"(ep) : "v"(4u * tg + 2u * (lane & 1)), "v"(O_EP), "s"(Dskip), "s"(ws));
      auto v_reset = [&] {
        if (w == 7) {
          const uint32_t ro = v_at(2 * tg + (lane & 1), lane >> 1);
#pragma unroll
          for (int k = 0; k < 8; ++k) ST16(ro + (VDENSE ? 2048u : 1024u) * k, ones);
        }
      };
      if (RPOS == 0) v_reset();
      late_terms();
      if (RPOS == 2) v_reset();
      TS(0, 1);
      __builtin_amdgcn_s_setprio(UPRIO);
      BAR();
      if (RPOS == 1) v_reset();
      ox = ((const float*)(sm + C_ES))[2 + (wl & 1)];
      u32x2 pv;
      steps([&] {
        asm volatile("s_waitcnt vmcnt(0)" : "+v"(r7), "+v"(ep));
        T = ((uint32_t)__builtin_amdgcn_readfirstlane(ep) + 1u) * 0x9E3779B1u;
        asm volatile("s_cmp_lg_u32 %5, 7\n\ts_cbranch_scc1 1f\n\ts_mov_b64 exec, 0xff\n\tglobal_store_dwordx4 %2, %3, %4 sc1\n\ts_mov_b64 exec, -1\n"
                     "1:\n\tglobal_load_dwordx2 %0, %1, %4 sc1"
                     : "=&v"(pv) : "v"(go), "v"(O_KEY + 128u * b + 16u * lane), "v"((u32x4){~T, ~T, ~T, ~T}), "s"(ws), "s"(w) : "scc");
      });
      TS(0, 2);
      asm volatile("s_waitcnt vmcnt(0)" : "+v"(pv));
      dsk = bflo(r7);
      if (__any(pv[0] != T)) pv[1] = gate(ws, go, T);
      if (__any((int)((uint32_t)__builtin_amdgcn_s_memrealtime() - pv[1]) < UD)) until(latest(pv[1]) + UD);
      TS(0, 7);
      u32x4 uw;
      asm volatile("global_load_dwordx4 %0, %2, %4 sc1\n\tglobal_load_dwordx4 %1, %3, %4 sc1"
                   : "=&v"(uw), "=&v"(ux) : "v"(uo), "v"(uo + 64 * UP * 32), "s"(ws) : "memory");
      if (w == 7) SIGNAL(O_SV + 128u * tg, 320 * 128, (bj >= 24) ? VD : VD2);
      scales();
      u32x4 ut = {utag(T, 0), utag(T, 1), utag(T, 2), utag(T, 3)};
      int pz = (lane & 8) / 8 * CB + pad_at(lane & 7);
      dsk *= 32.f;
      asm volatile("" : "+v"(ut), "+v"(pz), "+v"(dsk));
      asm volatile("s_waitcnt vmcnt(1)\n\ts_cmp_eq_u32 %2, 7\n\ts_cbranch_scc1 1f\n\ts_waitcnt vmcnt(0)\n1:" : "+v"(uw), "+v"(ux) : "s"(w) : "scc");
      TS(0, 9);
      uw ^= ut; ux ^= ut;
      if (__builtin_expect(__any(((uw[0] | uw[1] | uw[2] | uw[3] | ux[0] | ux[1] | ux[2] | ux[3]) & UMASK) != 0u), 0)) {
#pragma unroll 1
        for (int spin = 0; spin < SPIN_CAP; ++spin) {
          const bool na = ((uw[0] | uw[1] | uw[2] | uw[3]) & UMASK) != 0u, nb = ((ux[0] | ux[1] | ux[2] | ux[3]) & UMASK) != 0u;
          if (!__any(na || nb)) break;
          TSC(19);
          SLEEP(USLEEP);
          if (na) { LD16W(uw, uo); uw ^= ut; }
          if (nb) { LD16W(ux, uo + 64 * UP * 32); ux ^= ut; }
        }
      }
      *(u32x4*)(smem + ru - 2048) = uw;
      *(u32x4*)(smem + ru) = ux;
      if (SKIPB) skip_term(ux, dsk, ud);
      BAR();
      table();
      if (tl < 16) *(u32x4*)(smem + pz) = (u32x4){0u, 0u, 0u, 0u};
    }
    TS(0, 3);
    __builtin_amdgcn_s_setprio(0);
    BAR();
    TS(0, 4);
    if (ROLE == 3) {
      asm volatile("" : "+v"(ux));
      if (!SKIPB) skip_term(ux, dsk, ud);
      CONV_RUN((role == 1 || role == 2) ? role + 4 : role, e, vo, rf, rc, ry, ys, dsk, , "+{v[2:3]}"(ud[0]), "+{v[4:5]}"(ud[1]), "+{v[6:7]}"(ud[2]), "+{v[8:9]}"(ud[3]));
      TS(0, 5);
    } else {
      CONV_RUN(role, e, vo, rf, rc, ru, ys, dsk, , "+{v22}"(ry));
      TS(0, 5);
      skip_term(*(const u32x4*)(smem + ru), dsk, ud);
    }
    BAR();
    TS(0, 10);
    ST16(vo, conv_out(smem, ry, rf, rc, ys, ud));
    TS(0, 6);
    TSE();
  } else {
  uint32_t* cand = (uint32_t*)(smem + LM);
  uint32_t* sel = cand + 128;
  uint32_t* kl = sel + 48;
  uint32_t* cnt = kl + 8;
  const uint32_t ca = lb0 + LM + 4 * (128 + 48 + 8);
  u16* hm = (u16*)(cnt + 8);
  uint32_t T;
  const bool ffn = bj < 18, isx = bj >= 24;
  const int fb = 18 * bx + bj;
  u32x4 bw[18];
#pragma unroll
  for (int s = 0; s < 9; ++s) bw[s] = (u32x4){0u, 0u, 0u, 0u};
  int tid2 = threadIdx.x;
  auto gu_dma = [&](int h) {
    const char* gs = (const char*)((w < 2) ? wg : wu) + (size_t)((288 * h + 144 * (w & 1)) * (F_ * 2) + fb * 64);
    const uint32_t gv = (uint32_t)((lane >> 2) * (F_ * 2) + 16 * ((lane & 3) ^ (lane >> 5) ^ (w & 2)));
#pragma unroll 1
    for (int k = 0; k < 9; ++k) {
      dma16(gs + (size_t)(16 * k) * (F_ * 2), gv, lb0 + LW * h + 9216 * w + 1024 * k);
      asm volatile("s_waitcnt vmcnt(%0)" : : "n"(WDEPTH) : "memory");
    }
  };
  auto gu_read = [&](int h) {
    const uint32_t ra = lb0 + LW * h + (uint32_t)(((lane >> 1) & 1) * 18432 + (8 * kq + ((lane >> 2) & 3)) * 64 + ((w ^ (kq & 1) ^ (lane & 2)) * 16) + 8 * (lane & 1));
#pragma unroll
    for (int s = 0; s < 9; ++s) {
      const u32x2 h0 = trd(ra + 2048 * s), h1 = trd(ra + 2048 * s + 256);
      bw[9 * h + s] = (u32x4){h0[0], h0[1], h1[0], h1[1]};
    }
#pragma unroll
    for (int s = 0; s < 9; ++s) asm volatile("" : "+v"(bw[9 * h + s]));
  };
  uint32_t tv = rt0;
  auto v_gate = [&](uint32_t tu, int t) {
    until(tu + UD + PD);
    tv = gate2(ws, O_SV + (uint32_t)(320 * bx + t) * 128u, 0u, O_SV + (uint32_t)(320 * bx + 256 + (t >> 2)) * 128u, T);
  };
  auto w_line = [&](int n) {
    const int q = min(lane, 62) / 9, r = min(7 * (n >> 2) + q, 287), h = r >= 144;
    return (const char*)((n & 1) ? wu : wg) + (uint32_t)((r + 144 * (h + ((n >> 1) & 1))) * (F_ * 2) + bx * 1152 + (min(lane, 62) - 9 * q) * 128);
  };
#define TOUCH(pa) asm volatile("global_load_dword v126, %0, off" : : "v"(pa) : "memory", "v126")
  uint32_t wv[5], xv[9];
  auto rows_xw = [&] {
    u32x4 wa, xr, xs;
    const uint32_t xo = (uint32_t)((8 * tg + 2 * wl) * (D_ * 2));
    asm volatile("global_load_dwordx4 %0, %5, %9\n\tglobal_load_dword %1, %6, %9 offset:1024\n\t"
                 "global_load_dwordx4 %2, %7, %10\n\tglobal_load_dwordx4 %3, %7, %10 offset:1152\n\tglobal_load_dword %4, %8, %10 offset:1024\n\ts_waitcnt vmcnt(0)"
                 : "=&v"(wa), "=&v"(wv[4]), "=&v"(xr), "=&v"(xs), "=&v"(xv[8])
                 : "v"(16u * lane), "v"(4u * (lane & 31)), "v"(xo + 16u * lane), "v"(xo + 4u * lane + 32u * (lane & 32)), "s"(wr), "s"(x) : "memory");
#pragma unroll
    for (int k = 0; k < 4; ++k) { wv[k] = wa[k]; xv[k] = xr[k]; xv[4 + k] = xs[k]; }
  };
  if constexpr (ROLE == 2) {
    const int tl = tid - 256;
    if (isx) __builtin_amdgcn_s_setprio(2);
    if (isx) ST16(v_at(512 - 24 + 8 * bx + bj, tl), ones);
    BAR();
    char* sm = smem + LW;
    uint32_t go, eo, uo, vo, rf, rc, ru;
    int ds, dh;
    COps e;
    if (isx) {
      const int dX = 512 - 24 + 8 * bx + bj;
      if (tl < C_NZ + 8) *(u32x4*)(sm + ((tl < C_NZ) ? 16 * tl : pad_at(tl - C_NZ))) = (u32x4){0u, 0u, 0u, 0u};
      int qd = min(tl, 191);
      table_at(qd, wl == 3, 0, ds, dh);
      go = O_GQ + (uint32_t)((dX - 512) * (2 * GQP) + 4 * qd) * 4u; eo = O_GQ + (uint32_t)((dX - 512) * (2 * GQP) + (lane & 1) * GQP + 768) * 4u;
      uo = u_at(tl, dX);
      conv_ops(lb0 + LW, (wl + XROT) & 3, lane, e);
      vo = v_at(dX, tl); rf = wl ? C_YF + 32 * (tl - 64) : C_ZS; rc = (tl == 112) ? C_CY : (tl == 184) ? C_CY + 16 : C_ZS; ru = 16 * tl;
    }
    asm volatile("" : "+v"(go), "+v"(eo), "+v"(uo), "+v"(vo), "+v"(rf), "+v"(rc), "+v"(ru), "+v"(ds), "+v"(dh), "+v"(e.a), "+v"(e.b), "+v"(e.c), "+v"(e.d), "+v"(e.e), "+v"(e.f));
    KPar kp;
    float dsk;
    K_WAIT(0);
    asm volatile("" : "+v"(ep) : : "memory");
    T = ((uint32_t)__builtin_amdgcn_readfirstlane(ep) + 1u) * 0x9E3779B1u;
    TS(0, 1);
    rows_xw();
    if (QWAIT) gwait(ca + 12, QWAIT);
    __builtin_amdgcn_s_setprio(QPRIO);
    const u32x4 gt = {T & 0xffu, (T >> 8) & 0xffu, (T >> 16) & 0xffu, T >> 24};
    if (bj < 16) {
      float* part = (float*)(smem + LP_PART);
      int* esc = (int*)(smem + LP_PART + 4 * CP * 4);
      float* prm = part + wl * CP;
      float ox;
      int eb;
      const f32x4 pb = kgen_base<16>(kp, ox, eb);
      float* pt = prm + (i >> 1) * PS + (i & 1);
      float er, ei;
      pt[0] = pb[0]; pt[2] = pb[1]; pt[4] = pb[2]; pt[6] = pb[3];
      cexp2(pb[0], pb[1], kconst(kq), er, ei);
      pt[8 + 4 * kq] = er; pt[10 + 4 * kq] = ei;
      cexp2(pb[0], pb[1], (kq & 1) ? 1020.f : 512.f, er, ei);
      pt[24 + 4 * (kq & 1)] = er; pt[26 + 4 * (kq & 1)] = ei;
      esc[wl] = eb;
      if (w == 7) {
        const uint32_t t_ = ahead(ck0, rt0, GD);
        if (lane < 8) ST16(O_SG + (uint32_t)((dP - 512) * 2 + pq) * 128u + 16u * lane, ((u32x4){T, t_, T, t_}));
      }
      f32x4 acc[3];
      u32x4 xh[3], yh;
#pragma unroll
      for (int t = 0; t < 3; ++t) acc[t] = (f32x4){0.f, 0.f, 0.f, 0.f};
      const float* pp = prm + 2 * kq * PS;
      const int o1 = 8 + (i & 1) * (4 + 2 * (i & 2)), o2 = 20 + (i & 1) * (4 + (i & 4));
      wave_lds_sync();
      kgen_pair2(pp, pp + o1, pp + o2, (float)kbase(i), (float)(8 * i), ox, xh, yh, 0);
      kgen_pair2(pp + PS, pp + PS + o1, pp + PS + o2, (float)kbase(i), (float)(8 * i), ox, xh, yh, 2);
      kgen_mma(xh, yh, acc);
      image_put(prm, 2 * i + (kq & 1) + 32 * (kq >> 1), i + 16 * (kq >> 1) + 64 * (kq & 1), acc);
      TS(0, 2);
      gsync(ca + 8, 4);
      const uint32_t gs = O_GQ + (uint32_t)(((dP - 512) * 2 + pq) * GQP + 4 * tl) * 4u;
      const u32x4 ev = *(const u32x4*)esc;
      const int em = max(max((int)ev[0], (int)ev[1]), max((int)ev[2], (int)ev[3]));
      if (wl < 3) {
        const f32x4 kv = *(const f32x4*)&part[4 * tl] * __builtin_amdgcn_ldexpf(1.f, (int)ev[0] - em) + *(const f32x4*)&part[CP + 4 * tl] * __builtin_amdgcn_ldexpf(1.f, (int)ev[1] - em) +
                         *(const f32x4*)&part[2 * CP + 4 * tl] * __builtin_amdgcn_ldexpf(1.f, (int)ev[2] - em) + *(const f32x4*)&part[3 * CP + 4 * tl] * __builtin_amdgcn_ldexpf(1.f, (int)ev[3] - em);
        ST16(gs, (__builtin_bit_cast(u32x4, kv) & ~0xffu) | gt);
      } else if (lane < 8) ST16(gs, ((uint32_t)em << 8) | gt);
      SLEEP(PW1);
    } else if (!isx) SLEEP(PW2);
    TS(0, 3);
    __builtin_amdgcn_s_setprio(PPRIO);
    if (isx) {
      const u32x4 ut = {utag(T, 0), utag(T, 1), utag(T, 2), utag(T, 3)};
      dsk *= 32.f;
      gsync(ca + 8, 4);
      u32x4 p0, p1, ev, uw;
      SLEEP(XSLEEP);
      TS(0, 2);
      until(gate2(ws, O_SU + (uint32_t)(256 * bx + tl) * 128u, UD, O_SG + (uint32_t)((8 * bx + bj - 24) * 2 + (lane & 1)) * 128u, T));
      TS(0, 13);
      asm volatile("global_load_dwordx4 %0, %4, %7 sc1\n\tglobal_load_dwordx4 %1, %4, %7 offset:3200 sc1\n\tglobal_load_dwordx4 %2, %5, %7 sc1\n\t"
                   "global_load_dwordx4 %3, %6, %7 sc1\n\ts_waitcnt vmcnt(0)"
                   : "=&v"(p0), "=&v"(p1), "=&v"(ev), "=&v"(uw) : "v"(go), "v"(eo), "v"(uo), "s"(ws) : "memory");
#pragma unroll 1
      for (int spin = 0; spin < SPIN_CAP; ++spin) {
        uw ^= ut;
        if (!__any(((uw[0] | uw[1] | uw[2] | uw[3]) & UMASK) != 0u)) break;
        TSC(19);
        SLEEP(USLEEP);
        LD16W(uw, uo);
      }
      *(u32x4*)(sm + C_UL + 16 * tl) = uw;
#pragma unroll 1
      for (int spin = 0; spin < SPIN_CAP; ++spin) {
        p0 ^= gt; p1 ^= gt; ev ^= gt;
        const u32x4 bad = p0 | p1 | ev;
        if (!__any(((bad[0] | bad[1] | bad[2] | bad[3]) & 0xffu) != 0u)) break;
        TSC(18);
        __builtin_amdgcn_s_sleep(3);
        asm volatile("global_load_dwordx4 %0, %3, %5 sc1\n\tglobal_load_dwordx4 %1, %3, %5 offset:3200 sc1\n\tglobal_load_dwordx4 %2, %4, %5 sc1\n\ts_waitcnt vmcnt(0)"
                     : "=&v"(p0), "=&v"(p1), "=&v"(ev) : "v"(go), "v"(eo), "s"(ws) : "memory");
      }
      TS(0, 4);
      const int e0 = __builtin_amdgcn_readlane((int)ev[0], 0) >> 8, e1 = __builtin_amdgcn_readlane((int)ev[0], 1) >> 8;
      const int emax = __builtin_amdgcn_readfirstlane(min(max(max(e0, e1), -110), 115));
      const float s0 = __uint_as_float((uint32_t)min(max(105 + e0 - emax, 0), 254) << 23), s1 = __uint_as_float((uint32_t)min(max(105 + e1 - emax, 0), 254) << 23);
      const f32x4 kv = __builtin_bit_cast(f32x4, p0) * s0 + __builtin_bit_cast(f32x4, p1) * s1;
      const u32x2 kh = {packh(kv.xy), packh(kv.zw)};
      *(u32x2*)(sm + ds) = kh;
      *(u16*)(sm + dh) = (u16)(kh[1] >> 16);
      BAR();
      TS(0, 5);
      CONV_RUN((wl + XROT) & 3, e, vo, rf, rc, ru, tid2, dsk, , "+{v22}"(xv[0]), "+{v23}"(xv[1]), "+{v24}"(xv[2]), "+{v25}"(xv[3]), "+{v26}"(xv[4]),
               "+{v27}"(xv[5]), "+{v2}"(xv[6]), "+{v3}"(xv[7]), "+{v4}"(xv[8]), "+{v5}"(wv[0]), "+{v6}"(wv[1]), "+{v7}"(wv[2]), "+{v8}"(wv[3]), "+{v9}"(wv[4]));
      f32x2 ud[4];
      skip_term(*(const u32x4*)(sm + C_UL + ru), dsk, ud);
      gsync(ca + 8, 8);
      ST16(vo, conv_out(sm, C_YD + 2 * ru, rf, rc, __int_as_float((127 - 7 + 5 + emax) << 23), ud));
      TS(0, 6);
    }
    asm volatile("" : "+v"(tid2));
    if (!isx) BAR();
    const uint32_t tp = (uint32_t)__builtin_amdgcn_readfirstlane(cnt[4]);
    if (!isx && !ffn) {
      SLEEP(RSLEEP);
#pragma unroll
      for (int k = 0; k < 7; ++k) ST16(O_SL + (uint32_t)((6 * bx + bj - 18) * 1792 + 256 * k - 256 + tid2) * 16u, ones);
      DRAIN();
    }
    if (!isx) v_gate(tp, tid2 - 256);
  } else {
    __builtin_amdgcn_s_setprio(3);
    uint32_t uo = u_at(tg, 64 * w + lane);
    uint32_t ta = lb0 + (uint32_t)(((lane >> 2) & 3) * RP + (64 * w + 16 * kq + 4 * (lane & 3)) * 2);
    if (tid < 20) ((uint32_t*)(hm + 320))[tid] = 0u;
    if (tid < 8) cnt[tid] = 0u;
    uint32_t la = (uint32_t)(2 * w * RP + 16 * lane), lt = (uint32_t)((2 * w + (lane >> 5)) * RP + 1024 + 4 * (lane & 31));
    uint32_t sg = O_SU + (uint32_t)(256 * (lane >> 3) + tg) * 128u + 16u * (lane & 7);
    asm volatile("" : "+v"(uo), "+v"(ta), "+v"(la), "+v"(lt), "+v"(sg));
    uint32_t sink = 0;
    f32x4 z4 = {0.f, 0.f, 0.f, 0.f};
    asm volatile("" : "+v"(z4));
    BAR();
    asm volatile("s_waitcnt vmcnt(1)" : "={v[100:103]}"(xa), "={v[104:107]}"(xc), "={v108}"(r0) : : "v109");
    TS(0, 1);
    {
      const uint32_t v[9] = {xa[0], xa[1], xa[2], xa[3], xc[0], xc[1], xc[2], xc[3], r0};
      uint32_t c[9];
      f32x2 tf[9];
      ln_two(v, lane, 0.03125f, c, tf, z4, [&] {
        asm volatile("s_waitcnt vmcnt(0)" : "={v109}"(ep));
        T = ((uint32_t)__builtin_amdgcn_readfirstlane(ep) + 1u) * 0x9E3779B1u;
        if (w == 3) { const uint32_t now = (uint32_t)__builtin_amdgcn_s_memrealtime() + SD; ST16(sg, ((u32x4){T, now, T, now})); }
      });
      const uint32_t tj = ((T >> (3 * w)) & 0x00070007u) | (((T >> (12 + w)) & 0x00010001u) << 14);
      uint32_t tw[3] = {(tj & 0xffffu) * 0x10001u, (tj >> 16) * 0x10001u, 0u};
      tw[2] = (lane < 32) ? tw[0] : tw[1];
      asm volatile("" : "+v"(tw[0]), "+v"(tw[1]), "+v"(tw[2]));
#pragma unroll
      for (int k = 0; k < 9; ++k) c[k] = (packh((f32x2){bflo(c[k]), bfhi(c[k])}) & ~UMASK) | tw[k >> 2];
      *(u32x4*)(smem + la) = (u32x4){c[0], c[1], c[2], c[3]};
      *(u32x4*)(smem + la + RP) = (u32x4){c[4], c[5], c[6], c[7]};
      *(uint32_t*)(smem + lt) = c[8];
    }
    gsync0(ca + 12, 4);
    {
      const u32x2 h0 = trd(ta), h1 = trd(ta + 4 * RP), h2 = trd(ta + 512), h3 = trd(ta + 512 + 4 * RP);
      ST16(uo, ((u32x4){h0[0], h0[1], h1[0], h1[1]}));
      ST16(uo + 64 * 128, ((u32x4){h2[0], h2[1], h3[0], h3[1]}));
    }
    if (w == 0) {
      const u32x2 h0 = trd(ta + 1024), h1 = trd(ta + 1024 + 4 * RP);
      ST16(uo + 128 * 128, ((u32x4){h0[0], h0[1], h1[0], h1[1]}));
    }
    TS(0, 2);
    if (!isx && 3 - w < ((bj < 16) ? TB1 : TB1I)) TOUCH(w_line(24 * (3 - w) + 23 - bj));
    asm volatile("s_waitcnt vmcnt(0)" : "+v"(sink) : : "memory");
    TS(0, 13);
    if (w == 0) {
#pragma unroll
      for (int k = 0; k < 2; ++k) ST16(O_XC + (uint32_t)((511 - 2 * tg) * 144 + 64 * k + lane) * 16u, ones);
      const uint32_t oz = (lane < 16) ? O_XC + (uint32_t)((511 - 2 * tg) * 144 + 128 + lane) * 16u : O_FB + (uint32_t)(8 * b + lane - 16) * 16u;
      if (lane < ((b < 72) ? 24 : 16)) ST16(oz, ones);
    }
    SLEEP(ESLEEP);
    const uint32_t tu = latest(gate(ws, O_SU + (uint32_t)(256 * bx + 16 * i + 4 * w + (bj & 3)) * 128u, T));
    if (w == 0) cnt[4] = tu;
    if (!isx) until(tu + UD + WD);
    BAR();
    TS(0, 3);
    if (isx && w == 1) SIGNAL(O_SV + 128u * (256 - 24 + 8 * bx + bj), 320 * 128, VDX);
    if (ffn) {
      if (DD) until(tu + UD + WD + DD);
      gu_dma(0);
      gu_dma(1);
      asm volatile("s_waitcnt vmcnt(9)" ::: "memory");
    } else {
      if (!isx) {
        const int n = 6 * min(tid, 191) + bj - 18, r = n >> 2;
        const char* wb = (const char*)((n & 1) ? wu : wg) + bx * 1152;
        const int c0 = (3 - (int)((uintptr_t)wb >> 7)) & 7;
        wb += (uint32_t)((r + 144 * ((r >= 144) + ((n >> 1) & 1))) * (F_ * 2) + 128 * c0);
        TOUCH(wb);
        if (c0 == 0) TOUCH(wb + 1024);
      }
      if (w < TW2) TOUCH(w_line(167 - 14 * w - (bj - 18)));
      if (w + 4 < TW2) TOUCH(w_line(167 - 14 * (w + 4) - (bj - 18)));
      if (TD) TOUCH((const char*)wd + (uint32_t)(bx * 5184 + 14 * min(tid, TD - 1) * 370 / TD + bj - 18) * 128u);
      asm volatile("s_waitcnt vmcnt(0)" : "+v"(sink));
    }
    if (isx) v_gate(tu, tid);
  }
  {
  const int tid = tid2, lane = tid & 63, i = lane & 15, kq = lane >> 4;
  __builtin_amdgcn_s_setprio(3);
  uint32_t vo = v_at(tid, tg), vo2 = v_at(512 + 8 * w + (lane & 7), tg);
  asm volatile("" : "+v"(vo), "+v"(vo2));
  if (ROLE == 1 && ffn) {
    gsync(ca + 4, 4);
    gu_read(0);
    DRAIN();
    TS(0, 4);
  }
  until(tv);
  TS(0, 7);
  BAR();
  TS(0, 8);
  auto down_dma = [&](int k0, int k1) {
#pragma unroll 1
      for (int k = k0; k < k1; ++k) {
        const int o = 9216 * wl + 2304 * k;
        const char* ds = (const char*)wd + (size_t)(fb * 36864 + o);
        const uint32_t dl = lb0 + LW - 512 + o + 32 * ((4 * wl + k + 1) >> 1);
        dma16(ds, 16u * lane, dl);
        dma16(ds + 1024, 16u * lane, dl + 1024);
        uint32_t keep;
        asm volatile("s_mov_b32 %0, m0\n\ts_mov_b32 m0, %3\n\ts_mov_b64 exec, 0xffff\n\tglobal_load_lds_dwordx4 %1, %2" DPOL "\n\ts_mov_b64 exec, -1\n\ts_mov_b32 m0, %0"
                     : "=&s"(keep) : "v"(16u * lane), "s"(ds + 2048), "s"(dl + 2048) : "memory");
      }
    };
  u32x4 va, vb = {0u, 0u, 0u, 0u};
  asm volatile("global_load_dwordx4 %0, %2, %4 sc1\n\ts_mov_b64 exec, 0xff\n\tglobal_load_dwordx4 %1, %3, %4 sc1\n\ts_mov_b64 exec, -1"
               : "=&v"(va), "+v"(vb) : "v"(vo), "v"(vo2), "s"(ws) : "memory");
  __builtin_amdgcn_sched_barrier(0);
  const uint32_t tk = ahead(ck0, rt0, KD);
  f32x2 xf[9];
  if (ROLE == 2) {
#pragma unroll
    for (int k = 0; k < 9; ++k) xf[k] = (f32x2){bflo(xv[k]), bfhi(xv[k])};
    asm volatile("" : "+v"(xf[0]), "+v"(xf[1]), "+v"(xf[2]), "+v"(xf[3]), "+v"(xf[4]), "+v"(xf[5]), "+v"(xf[6]), "+v"(xf[7]), "+v"(xf[8]));
  }
  f32x4 za = {0.f, 0.f, 0.f, 0.f}, zb = za;
  uint32_t pa = (uint32_t)(2560 * wl + 32 * lane), pb = (uint32_t)(2560 * wl + 2048 + 8 * (lane & 31)), ps = (lane < 32) ? 0x01000c0cu : 0x03020c0cu;
  uint32_t kid = (uint32_t)(((255 - tg) << 5) | (7 - 2 * wl - (lane >> 5))), px = (uint32_t)(2048 + 32 * w + 4 * lane);
  uint32_t al = O_XC + 64u * (i >> 3) + 16u * kq + 128u * w;
  uint32_t ha = (ROLE == 1) ? (uint32_t)((4 * (kq & 1) + 2 * (kq >> 1) + (i >> 3)) * 80 + 16 * w + 2 * (i & 7)) : 0u;
  uint32_t h0a = (uint32_t)(min(i, 8) * 80 + 16 * kq), h1a = (uint32_t)(min(i ^ 8, 8) * 80 + 16 * kq);
  uint32_t so = O_SL + (uint32_t)fb * SP + ((ROLE == 2) ? 16u * tid - 4096u : 8192u + 16u * lane);
  uint32_t da = lb0 + LW - 512 + (uint32_t)((8 * kq + ((lane >> 2) & 3)) * 1152 + 32 * ((8 * kq + ((lane >> 2) & 3) + 2) >> 2) + 8 * (lane & 3) + ((ROLE == 2) ? 128 * wl : 1024));
  if (ROLE == 2) asm volatile("" : "+v"(za), "+v"(zb), "+v"(pa), "+v"(pb), "+v"(ps), "+v"(kid), "+v"(h0a), "+v"(h1a), "+v"(so));
  else asm volatile("" : "+v"(ha), "+v"(al));
  asm volatile("" : "+v"(px), "+v"(da));
  if (ROLE == 1) gu_read(1);
  asm volatile("s_waitcnt vmcnt(0)" : "+v"(va), "+v"(vb) : : "memory");
  {
    uint32_t* pln = (uint32_t*)smem;
    if (__builtin_expect(__any(max(max4(va), max4(vb)) == 0xffffffffu), 0)) {
#pragma unroll 1
      for (int spin = 0; spin < SPIN_CAP; ++spin) {
        const bool na = max4(va) == 0xffffffffu, nb = max4(vb) == 0xffffffffu;
        if (!__any(na || nb)) break;
        TSC(16);
        SLEEP(PSLEEP);
        if (na) LD16W(va, vo);
        if (nb) LD16W(vb, vo2);
      }
    }
#pragma unroll
    for (int e = 0; e < 4; ++e) pln[640 * e + tid] = va[e];
    if (lane < 8) {
#pragma unroll
      for (int e = 0; e < 4; ++e) *(uint32_t*)(smem + px + 2560 * e) = vb[e];
    }
  }
  BAR();
  TS(0, 9);
  if (ROLE == 1 && ffn) down_dma(0, KN);
  uint32_t hv[9], nv[9], keya = 0u, keyb = 0u;
  if constexpr (ROLE == 2) {
    const u32x4 a0 = *(const u32x4*)(smem + pa), a1 = *(const u32x4*)(smem + pa + 16);
    const u32x2 a2 = *(const u32x2*)(smem + pb);
    const f32x2 h[9] = {xf[0] + (f32x2){bflo(a0[0]), bflo(a0[1])}, xf[1] + (f32x2){bflo(a0[2]), bflo(a0[3])},
                        xf[2] + (f32x2){bflo(a1[0]), bflo(a1[1])}, xf[3] + (f32x2){bflo(a1[2]), bflo(a1[3])},
                        xf[4] + (f32x2){bfhi(a0[0]), bfhi(a0[1])}, xf[5] + (f32x2){bfhi(a0[2]), bfhi(a0[3])},
                        xf[6] + (f32x2){bfhi(a1[0]), bfhi(a1[1])}, xf[7] + (f32x2){bfhi(a1[2]), bfhi(a1[3])},
                        xf[8] + (f32x2){__uint_as_float(__builtin_amdgcn_perm(0u, a2[0], ps)), __uint_as_float(__builtin_amdgcn_perm(0u, a2[1], ps))}};
#pragma unroll
    for (int k = 0; k < 9; ++k) hv[k] = pack2(h[k][0], h[k][1]);
    ln_two(hv, lane, 1.f, nv, xf, za, [] {});
    const float da = dot2(nv[2], wv[2], dot2(nv[0], wv[0], zb[0])) + dot2(nv[3], wv[3], dot2(nv[1], wv[1], zb[1]));
    const float db = dot2(nv[6], wv[2], dot2(nv[4], wv[0], zb[2])) + dot2(nv[7], wv[3], dot2(nv[5], wv[1], zb[3]));
    float dt = row_sum(dot2(nv[8], wv[4], fold32(da, db)));
    asm("s_nop 1\n\tv_add_f32_dpp %0, %0, %0 row_bcast:15 row_mask:0xa bank_mask:0xf\n\ts_nop 0" : "+v"(dt));
    const uint32_t bb = pack2(dt, dt) & 0xffff0000u;
    const uint32_t kk = (bb ^ (((uint32_t)((int)bb >> 31) & 0x7fff0000u) | 0x80000000u)) | kid;
    if ((lane & 31) == 31) kl[2 * wl + (lane >> 5)] = kk;
    keya = (uint32_t)__builtin_amdgcn_readlane(kk, 31); keyb = (uint32_t)__builtin_amdgcn_readlane(kk, 63);
  }
  BAR();
  if (ROLE == 1 && w == 0) {
    const u32x4 ka = *(const u32x4*)kl, kb = *(const u32x4*)(kl + 4);
    uint32_t m1 = max(max(ka[0], ka[1]), ka[2]), m2 = med3u(ka[0], ka[1], ka[2]), m3 = min(min(ka[0], ka[1]), ka[2]);
#pragma unroll
    for (int e = 0; e < 5; ++e) {
      const uint32_t kx = e ? kb[e - 1] : ka[3];
      m3 = med3u(m2, m3, kx); m2 = med3u(m1, m2, kx); m1 = max(m1, kx);
    }
    if (lane < 8) ST16(O_KEY + (uint32_t)(256 * lane + b) * 16u, ((u32x4){m1 | 24u, m2 | 16u, m3 | 8u, T}));
  }
  if (ROLE == 1 && w == 1) {
    const u32x4 kv = *(const u32x4*)&kl[4 * (lane & 1)];
    if (lane < 2) ST16(O_KF + (uint32_t)(2 * tg + lane) * 16u, ((u32x4){__builtin_amdgcn_perm(kv[1], kv[0], 0x07060302u), T, __builtin_amdgcn_perm(kv[3], kv[2], 0x07060302u), T}));
    if (b == 0 && lane == 0) ST4(O_EP, T * 0x0E8B2F51u);
  }
  TS(0, 10);
  auto put_row = [&](const uint32_t* r, int j, const void* base, uint32_t ro) {
    const u32x4 tl = {r[8], (uint32_t)DPP_I((int)r[8], 0x55, 0xf), (uint32_t)DPP_I((int)r[8], 0xaa, 0xf), (uint32_t)DPP_I((int)r[8], 0xff, 0xf)};
    asm volatile("global_store_dwordx4 %0, %1, %2 sc1\n\ts_nop 1" : : "v"(ro + 16u * lane), "v"((u32x4){r[4 * j], r[4 * j + 1], r[4 * j + 2], r[4 * j + 3]}), "s"(base) : "memory");
    if ((lane & 35) == 32 * j) asm volatile("global_store_dwordx4 %0, %1, %2 offset:1024 sc1\n\ts_nop 1" : : "v"(ro + 4u * (lane & 31)), "v"(tl), "s"(base) : "memory");
  };
  int rka = 0, rkb = 0;
  uint32_t k3rd = 0u;
  if constexpr (ROLE == 2) {
    __builtin_amdgcn_s_setprio(0);
    const uint32_t ko = kl[lane & 7];
    rka = FORCEFB + __popcll(__ballot(ko > keya) & 0xffull); rkb = FORCEFB + __popcll(__ballot(ko > keyb) & 0xffull);
#pragma unroll
    for (int k = 0; k < 9; ++k) {
      const f32x2 o = xf[k] + (f32x2){bflo(nv[k]), bfhi(nv[k])};
      hv[k] = pack2(o[0], o[1]);
      nv[k] = min(nv[k], 0xfffffffeu);
    }
    if (rka < 2) put_row(nv, 0, ws, O_XC + (uint32_t)(1023 - 4 * tg - rka) * 1152u);
    if (rkb < 2) put_row(nv, 1, ws, O_XC + (uint32_t)(1023 - 4 * tg - rkb) * 1152u);
    SLEEP(OD);
    if (rka >= 2) put_row(hv, 0, out, (uint32_t)((8 * tg + 2 * wl) * (D_ * 2)));
    if (rkb >= 2) put_row(hv, 1, out, (uint32_t)((8 * tg + 2 * wl + 1) * (D_ * 2)));
    __builtin_amdgcn_s_setprio(3);
  } else {
    u32x4 key;
    const uint32_t kof = O_KEY + (uint32_t)(256 * bx + tid) * 16u;
    if (w > 1) SLEEP(3);
    if (KN < 4 && !ffn) {
      SLEEP(HSLEEP);
      constexpr int NL = 18 * (4 - KN), TL = 72 * NL;
#pragma unroll
      for (int k = 0; k < (TL + 14 * 256 - 1) / (14 * 256); ++k) {
        const int n = min(14 * (256 * k + tid) + bj - 18, TL - 1);
        if (14 * (256 * k + 64 * w) < TL) TOUCH((const char*)wd + (uint32_t)(bx * 5184 + (n / NL) * 72 + 18 * KN + n % NL) * 128u);
      }
    }
    SLEEP(KSLEEP);
    until((uint32_t)__builtin_amdgcn_readfirstlane(tk));
    if (!ffn) SLEEP(NSLEEP);
    LD16W(key, kof);
    if (__builtin_expect(__any(key[3] != T), 0)) {
#pragma unroll 1
      for (int spin = 0; spin < SPIN_CAP; ++spin) {
        const bool need = key[3] != T;
        if (!__any(need)) break;
        TSC(15);
        SLEEP(KBACK);
        if (need) LD16W(key, kof);
      }
    }
    TS(0, 11);
    const uint32_t m1 = oct_maxu(key[0]);
    const bool hit = key[0] == m1;
    const uint32_t k0 = hit ? key[1] : key[0], k1 = hit ? key[2] : key[1];
    const uint32_t m2 = oct_maxu(k0);
    if ((lane & 7) == 0) *(u32x2*)&cand[tid >> 2] = (u32x2){m1, m2};
    k3rd = (k0 == m2) ? k1 : k0;
    if (RPF && !ffn && w == (bj & 3)) {
      const uint32_t r1 = O_XC + ((m1 >> 3) & 0x3ffu) * 1152u + 128u * (lane & 7), r2 = O_XC + ((m2 >> 3) & 0x3ffu) * 1152u + 128u * (lane & 7);
      asm volatile("global_load_dword v126, %0, %3 sc1\n\tglobal_load_dword v126, %1, %3 sc1\n\tglobal_load_dword v126, %2, %3 offset:1024 sc1"
                   : : "v"(r1), "v"(r2), "v"(((lane & 1) ? r1 : r2) - 128u * (lane & 7)), "s"(ws) : "memory", "v126");
    }
  }
  auto pick = [&](const uint32_t* cd, uint32_t* sl) {
    BAR();
    const uint32_t c = cd[lane], own = cd[8 * w + (lane & 7)];
    const u32x4 la = *(const u32x4*)&cd[8 * w], lb = *(const u32x4*)&cd[8 * w + 4];
    uint32_t r = C_;
    asm("v_cmp_gt_u32 s[40:41], %1, %2\n\tv_cmp_gt_u32 s[42:43], %1, %3\n\tv_cmp_gt_u32 s[44:45], %1, %4\n\tv_cmp_gt_u32 s[46:47], %1, %5\n\t"
        "v_cmp_gt_u32 s[48:49], %1, %6\n\tv_cmp_gt_u32 s[50:51], %1, %7\n\tv_cmp_gt_u32 s[52:53], %1, %8\n\tv_cmp_gt_u32 s[54:55], %1, %9\n\t"
        "s_bcnt1_i32_b64 s40, s[40:41]\n\ts_bcnt1_i32_b64 s42, s[42:43]\n\tv_writelane_b32 %0, s40, 0\n\ts_bcnt1_i32_b64 s44, s[44:45]\n\t"
        "v_writelane_b32 %0, s42, 1\n\ts_bcnt1_i32_b64 s46, s[46:47]\n\tv_writelane_b32 %0, s44, 2\n\ts_bcnt1_i32_b64 s48, s[48:49]\n\t"
        "v_writelane_b32 %0, s46, 3\n\ts_bcnt1_i32_b64 s50, s[50:51]\n\tv_writelane_b32 %0, s48, 4\n\ts_bcnt1_i32_b64 s52, s[52:53]\n\t"
        "v_writelane_b32 %0, s50, 5\n\ts_bcnt1_i32_b64 s54, s[54:55]\n\tv_writelane_b32 %0, s52, 6\n\tv_writelane_b32 %0, s54, 7"
        : "+v"(r) : "v"(c), "v"(la[0]), "v"(la[1]), "v"(la[2]), "v"(la[3]), "v"(lb[0]), "v"(lb[1]), "v"(lb[2]), "v"(lb[3])
        : "scc", "s40", "s41", "s42", "s43", "s44", "s45", "s46", "s47", "s48", "s49", "s50", "s51", "s52", "s53", "s54", "s55");
    sl[min(r, (uint32_t)C_)] = own;
    BAR();
  };
  auto rows = [&](uint32_t ao, auto&& meanwhile) {
    u32x4 aa, ab, ac = __builtin_bit_cast(u32x4, zb);
    asm volatile("global_load_dwordx4 %0, %3, %4 sc1\n\tglobal_load_dwordx4 %1, %3, %4 offset:512 sc1\n\t"
                 "s_cmp_lg_u32 %5, 0\n\ts_cbranch_scc1 1f\n\tglobal_load_dwordx4 %2, %3, %4 offset:1024 sc1\n1:"
                 : "=&v"(aa), "=&v"(ab), "+v"(ac) : "v"(ao), "s"(ws), "s"(w) : "memory", "scc");
    __builtin_amdgcn_sched_barrier(0);
    meanwhile();
    asm volatile("s_waitcnt vmcnt(0)" : "+v"(aa), "+v"(ab), "+v"(ac) : : "memory");
    if (__builtin_expect(__any(max(max(max4(aa), max4(ab)), max4(ac)) == 0xffffffffu), 0)) {
#pragma unroll 1
      for (int spin = 0; spin < SPIN_CAP; ++spin) {
        const bool na = max4(aa) == 0xffffffffu, nb = max4(ab) == 0xffffffffu, nc = max4(ac) == 0xffffffffu;
        if (!__any(na || nb || nc)) break;
        TSC(14);
        SLEEP(PSLEEP);
        if (na) LD16W(aa, ao);
        if (nb) LD16W(ab, ao + 512u);
        if (nc) LD16W(ac, ao + 1024u);
      }
    }
    *(u32x4*)(smem + LA + 16 * tid) = aa;
    *(u32x4*)(smem + LA + 4096 + 16 * tid) = ab;
    if (w == 0) *(u32x4*)(smem + LA + 8192 + 16 * tid) = ac;
  };
  auto get_bd = [&](int t0, int t1) {
#pragma unroll
    for (int t = t0; t < t1; ++t) {
      const u32x2 h0 = trd(da + 512 * (t >> 2) + 32 * (t & 3)), h1 = trd(da + 512 * (t >> 2) + 32 * (t & 3) + 4608 + 32);
      bw[t] = (u32x4){h0[0], h0[1], h1[0], h1[1]};
    }
#pragma unroll
    for (int t = t0; t < t1; ++t) asm volatile("" : "+v"(bw[t]));
  };
  pick(cand, sel);
  uint32_t mykey = sel[lane & 7], ro = ((mykey >> 3) & 0x3ffu) * 1152u;
  TS(0, 12);
  auto check = [&] {
    cnt[(k3rd > (uint32_t)__builtin_amdgcn_readlane(mykey, 7)) ? 5 : 6] = 1u;
  };
  if constexpr (ROLE == 1) {
    if (ffn && !FORCEFB) rows(ro + al, check); else { check(); DRAIN(); }
  } else if (ffn) {
    if (KN < 4) { SLEEP(LSLEEP); down_dma(KN, 4); } else get_bd(0, 8);
  }
  BAR();
  TS(0, 5);
  auto put_out = [&](int j, uint32_t k0, int rk) {
    const uint64_t mine = __ballot(((mykey ^ k0) & ~24u) == 0u) & 0xffull;
    if (mine == 0ull) { if (rk < 2) put_row(hv, j, out, (uint32_t)((8 * tg + 2 * wl + j) * (D_ * 2))); }
    else if (rk >= 2) { DRAIN(); put_row(nv, j, ws, O_FB + 1152u * (uint32_t)__builtin_ctzll(mine)); }
  };
  auto rest_out = [&] { put_out(0, keya, rka); put_out(1, keyb, rkb); };
  f32x4 lo, hi;
  auto gate_up = [&] {
    u32x4 av[5];
    const uint32_t aa = lb0 + LA + 16u * lane;
#pragma unroll
    for (int j = 0; j < 5; ++j) asm volatile("ds_read_b128 %0, %1 offset:%2" : "=v"(av[j]) : "v"(aa), "n"(1024 * j) : "memory");
    lo = (f32x4){0.f, 0.f, 0.f, 0.f}; hi = lo;
#pragma unroll
    for (int j = 0; j < 9; ++j) {
      if (j < 5) asm volatile("s_waitcnt lgkmcnt(4)" : "+v"(av[j % 5]));
      else if (j == 5) asm volatile("s_waitcnt lgkmcnt(2)" : "+v"(av[0]), "+v"(av[1]));
      else if (j == 7) asm volatile("s_waitcnt lgkmcnt(0)" : "+v"(av[2]), "+v"(av[3]));
      lo = MMB(av[j % 5], bw[2 * j], lo);
      hi = MMB(av[j % 5], bw[2 * j + 1], hi);
      __builtin_amdgcn_sched_barrier(0);
      if (j < 4) asm volatile("ds_read_b128 %0, %1 offset:%2" : "=v"(av[j]) : "v"(aa), "n"(1024 * (j + 5)) : "memory");
    }
  };
  uint32_t fl;
  asm volatile("ds_read_b32 %0, %1" : "=v"(fl) : "v"(ca + 20) : "memory");
  if (ROLE == 1) gate_up();
  asm volatile("s_waitcnt lgkmcnt(0)" : "+v"(fl));
  const bool slow = FORCESLOW || FORCEFB || __builtin_amdgcn_readfirstlane(fl) != 0;
  if (__builtin_expect(slow, 0)) {
    u32x4 key;
#pragma unroll 1
    for (int spin = 0; spin < SPIN_CAP; ++spin) {
      LD16W(key, O_KF + 16u * tid);
      if (!__any(key[1] != T || key[3] != T)) break;
      SLEEP(KBACK);
    }
    const uint32_t ib = (uint32_t)(((255 - (tid >> 1)) << 5) | (7 - 4 * (tid & 1)));
    uint32_t k0 = (key[0] << 16) | ib, k1 = (key[0] & 0xffff0000u) | (ib - 1), k2 = (key[2] << 16) | (ib - 2), k3 = (key[2] & 0xffff0000u) | (ib - 3);
    CEX(k0, k1) CEX(k2, k3) CEX(k0, k2) CEX(k1, k3) CEX(k1, k2)
#pragma unroll 1
    for (int c = 0; c < C_; ++c) {
      const uint32_t mx = wave_maxu(k0);
      const bool hit = (k0 == mx);
      k0 = hit ? k1 : k0; k1 = hit ? k2 : k1; k2 = hit ? k3 : k2; k3 = hit ? 0u : k3;
      if (lane == 0) cand[64 + 8 * w + c] = mx;
    }
    pick(cand + 64, sel + 16);
    mykey = sel[16 + (lane & 7)];
    const uint32_t okey = sel[16 + (lane >> 3)];
    const uint64_t same = __ballot(((mykey ^ okey) & 0x1fe0u) == 0u && okey > mykey);
    const int sl = FORCEFB + __popcll((same >> (lane & 7)) & 0x0101010101010101ull);
    ro = (sl < 2) ? (((mykey >> 3) & 0x3ffu) + 3 - sl) * 1152u : O_FB - O_XC + 1152u * (lane & 7);
    if (ROLE == 2) rest_out();
    if (ffn) {
      if (ROLE == 1) rows(ro + al, [] {});
      BAR();
      if (ROLE == 1) gate_up();
    }
  }
  if (!ffn) {
    if (ROLE == 2 && !slow) rest_out();
    TSE();
    return;
  }
  uint32_t fx = 0, fv = 0, fn = 0, fo = 0, sa[9];
  float rs, hx, xs;
  u32x4 se;
  if constexpr (ROLE == 1) {
    const f32x4 lh = (lane < 32) ? lo : hi;
    const float fa = fold32(lh[0], lh[2]), fb2 = fold32(lh[1], lh[3]);
    const float g = __int_as_float(__builtin_amdgcn_update_dpp(__float_as_int(fa), __float_as_int(fb2), 0x128, 0xf, 0xc, false));
    const float u = __int_as_float(__builtin_amdgcn_update_dpp(__float_as_int(fb2), __float_as_int(fa), 0x128, 0xf, 0x3, false));
    const uint32_t gu = pack2(g, u);
    const float gp = bflo(gu);
    const float sg = rbf(gp * __builtin_amdgcn_rcpf(1.f + __expf(-gp)));
    *(u16*)((char*)hm + ha) = f2bf(sg * bfhi(gu));
  } else if (KN < 4) {
    DRAIN();
    gsync0(ca, 4);
    get_bd(0, 8);
  }
  BAR();
  TS(0, 6);
  if (ROLE == 2 || w == 2) {
    const u32x4 a0 = *(const u32x4*)((char*)hm + h0a), a1 = *(const u32x4*)((char*)hm + h1a);
    if (ROLE == 1) get_bd(0, 4);
    const f32x4 z = {0.f, 0.f, 0.f, 0.f};
    f32x4 o[4];
#pragma unroll
    for (int t = 0; t < 2 * ROLE; ++t) o[t] = MMB(a1, bw[2 * t + 1], MMB(a0, bw[2 * t], z));
#pragma unroll
    for (int t = 0; t < ROLE; ++t) {
      u32x4 sv;
#pragma unroll
      for (int r = 0; r < 4; ++r) sv[r] = min(pack2(o[2 * t][r], o[2 * t + 1][r]), 0xfffffffeu);
      ST16(so + 4096u * t, sv);
    }
    TS(0, 13);
  }
  if (ROLE == 2) {
    SLEEP(OSLEEP);
    if (!slow) rest_out();
  }
  if (ROLE == 1 && w == 1) {
    const int q = (fb & 15) >> 2, ts = 4 * (q & 1) + ((i & 7) >> 1);
    const int dd = 64 * (fb >> 4) + 16 * (2 * (i & 1) + (q >> 1)) + 4 * (fb & 3) + 2 * (kq & 1) + (kq >> 1);
    const uint32_t fkey = (uint32_t)__builtin_amdgcn_ds_bpermute(4 * ts, (int)mykey), fr = (uint32_t)__builtin_amdgcn_ds_bpermute(4 * ts, (int)ro);
    const int ft = 2047 - (int)(((fkey >> 2) & 0x7f8u) | (fkey & 7u));
    fo = (uint32_t)(ft * D_ + dd) * 2u;
    asm volatile("global_load_ushort %0, %3, %6\n\tglobal_load_ushort %1, %4, %7 sc1\n\tglobal_load_ushort %2, %5, %7 sc1"
                 : "=&v"(fx), "=&v"(fv), "=&v"(fn) : "v"(fo), "v"(v_at(dd, ft >> 3) + 2u * (ft & 7)), "v"(O_XC + fr + 2u * dd), "s"(x), "s"(ws) : "memory");
    const uint32_t o = fkey >> 16;
    const float rs0 = bf2f((u16)((o & 0x8000u) ? (o & 0x7fffu) : (~o & 0xffffu)));
    rs = rbf(__builtin_amdgcn_rcpf(1.f + __expf(-rs0)));
    const uint32_t one = (i < 8) ? (0x3f80u << (16 * (i & 1))) : 0u;
    se = (u32x4){(i >> 1) == 0 ? one : 0u, (i >> 1) == 1 ? one : 0u, (i >> 1) == 2 ? one : 0u, (i >> 1) == 3 ? one : 0u};
#pragma unroll
    for (int m = 0; m < 9; ++m) sa[m] = O_SL + (uint32_t)(16 * m + (lane >> 2)) * SP + (uint32_t)(4 * fb + (lane & 3)) * 16u;
    asm volatile("" : "+v"(rs), "+v"(se), "+v"(sa[0]), "+v"(sa[1]), "+v"(sa[2]), "+v"(sa[3]), "+v"(sa[4]), "+v"(sa[5]), "+v"(sa[6]), "+v"(sa[7]), "+v"(sa[8]));
    SLEEP(SSLEEP);
    TS(0, 1);
    float sum;
#pragma unroll 1
    for (int spin = 0; spin < SPIN_CAP; ++spin) {
      u32x4 sv[9];
#pragma unroll
      for (int m = 0; m < 9; ++m) LD16(sv[m], sa[m]);
      asm volatile("s_waitcnt vmcnt(9)" : "+v"(fx), "+v"(fv), "+v"(fn));
      hx = rbf(bf2f((u16)fx) + bf2f((u16)fv));
      xs = bf2f((u16)fn);
      asm volatile("" : "+v"(hx), "+v"(xs));
      f32x4 dv = {0.f, 0.f, 0.f, 0.f};
      static_for<0, 9>([&](auto mm) {
        constexpr int m = mm.value;
        asm volatile("s_waitcnt vmcnt(%1)" : "+v"(sv[m]) : "n"(8 - m));
        dv = MMB(sv[m], se, dv);
        __builtin_amdgcn_sched_barrier(0);
      });
      sum = fold16(fold32(dv[0], dv[1]), fold32(dv[2], dv[3]));
      if (__builtin_expect(!__any(sum != sum), 1)) break;
      uint32_t mx = 0u;
#pragma unroll
      for (int m = 0; m < 9; ++m) mx = max(mx, max4(sv[m]));
      if (!__any(mx == 0xffffffffu)) break;
      TSC(18);
      SLEEP(PSLEEP);
    }
    const float o1 = rbf(xs + rbf(rbf(sum) * rs));
    if (i < 8) asm volatile("global_store_short %0, %1, %2 sc1" : : "v"(fo), "v"((uint32_t)f2bf(hx + o1)), "s"(out) : "memory");
    TS(0, 2);
  }
  TSE();
  }
  }
}
extern "C" __global__ __launch_bounds__(512, 4) void kf_all(KARGS) {
  __shared__ __attribute__((aligned(16))) char smem[SMEM];
  asm volatile("s_load_dword s96, s[0:1], 0x0\n\ts_load_dword s97, s[0:1], 0x40\n\tv_readfirstlane_b32 s98, v0\n\t"
               "s_cmpk_lt_u32 s98, 0x100\n\ts_cbranch_scc0 2f\n\t"
               "s_load_dwordx16 s[64:79], s[0:1], 0x0\n\ts_load_dwordx4 s[80:83], s[0:1], 0x40\n\t"
               "s_bfe_u32 s84, s2, 0x50003\n\ts_and_b32 s85, s2, 7\n\ts_lshl_b32 s85, s85, 5\n\ts_or_b32 s84, s84, s85\n\t"
               "s_cmpk_lt_u32 s2, 0x100\n\ts_cbranch_scc0 1f\n\t"
               "s_mulk_i32 s84, 0x2400\n\ts_mulk_i32 s98, 36\n\ts_add_u32 s84, s84, s98\n\tv_and_b32 v110, 63, v0\n\tv_and_b32 v111, 32, v0\n\t"
               "v_lshlrev_b32 v111, 5, v111\n\tv_lshl_add_u32 v111, v110, 2, v111\n\tv_lshl_add_u32 v110, v110, 4, s84\n\tv_add_u32 v111, s84, v111\n\t"
               "v_mov_b32 v109, 0\n\ts_waitcnt lgkmcnt(0)\n\t"
               "global_load_dwordx4 v[100:103], v110, s[64:65]\n\tglobal_load_dwordx4 v[104:107], v110, s[64:65] offset:1152\n\t"
               "global_load_dword v108, v111, s[64:65] offset:1024\n\tglobal_load_dword v109, v109, s[82:83] sc1\n\ts_branch kf_n\n"
               "1:\n\ts_lshl_b32 s85, s84, 9\n\ts_lshl_b32 s84, s84, 2\n\tv_lshl_add_u32 v110, v0, 1, s85\n\tv_lshrrev_b32 v100, 7, v0\n\t"
               "v_lshl_add_u32 v100, v100, 1, s84\n\tv_and_b32 v107, 1, v0\n\tv_lshl_add_u32 v107, v107, 1, s84\n\ts_waitcnt lgkmcnt(0)\n\t"
               "global_load_ushort v100, v100, s[78:79]\n\tglobal_load_ushort v101, v110, s[66:67]\n\tglobal_load_ushort v102, v110, s[68:69]\n\t"
               "global_load_ushort v103, v110, s[70:71]\n\tglobal_load_ushort v104, v110, s[72:73]\n\tglobal_load_ushort v105, v110, s[74:75]\n\t"
               "global_load_ushort v106, v110, s[76:77]\n\tglobal_load_ushort v107, v107, s[80:81]\n\ts_branch kf_c\n"
               "2:\n\ts_cmpk_lt_u32 s2, 0x100\n\ts_cbranch_scc1 kf_p\n\t"
               "s_and_b32 s99, s2, 0xf8\n\ts_lshl_b32 s99, s99, 5\n\ts_and_b32 s98, s98, 0xc0\n\ts_or_b32 s98, s98, s99\n\ts_lshl_b32 s98, s98, 1\n\t"
               "s_getpc_b64 s[94:95]\n\ts_add_u32 s94, s94, s98\n\ts_addc_u32 s95, s95, 0\n\t"
               "s_load_dword s96, s[94:95], 0x0\n\ts_load_dword s96, s[94:95], 0x40\n\t"
               "s_load_dword s96, s[94:95], 0x4000\n\ts_load_dword s96, s[94:95], 0x4040\n\t"
               "s_cmp_gt_i32 s98, kf_end-kf_all-0x8300\n\ts_cbranch_scc1 kf_u\n\t"
               "s_load_dword s96, s[94:95], 0x8000\n\ts_load_dword s96, s[94:95], 0x8040\n\ts_branch kf_u"
               : : "s"(__builtin_amdgcn_kernarg_segment_ptr()), "s"(blockIdx.x), "v"(threadIdx.x),
                   "s"((uint32_t)(uintptr_t)(__attribute__((address_space(3))) char*)smem) : "scc", "s94", "s95", "s96", "s97", "s98", "s99", "v127");
}
extern "C" __global__ __launch_bounds__(512, 4) void kf_c(KARGS) {
  __shared__ __attribute__((aligned(16))) char smem[SMEM];
  role_body<0>(smem, KPASS);
}
extern "C" __global__ __launch_bounds__(512, 4) void kf_u(KARGS) {
  __shared__ __attribute__((aligned(16))) char smem[SMEM];
  role_body<3>(smem, KPASS);
}
extern "C" __global__ __launch_bounds__(512, 4) void kf_n(KARGS) {
  __shared__ __attribute__((aligned(16))) char smem[SMEM];
  role_body<1>(smem, KPASS);
}
extern "C" __global__ __launch_bounds__(512, 4) void kf_p(KARGS) {
  __shared__ __attribute__((aligned(16))) char smem[SMEM];
  role_body<2>(smem, KPASS);
}
extern "C" __global__ __launch_bounds__(512, 4) void kf_conv() { asm volatile(CONV_ASM ::: CONV_CLOB); }
extern "C" __global__ void kf_end() {}

extern "C" void kernel_launch(void* const* d_in, const int* in_sizes, int n_in,
                              void* d_out, int out_size, void* d_ws, size_t ws_size,
                              hipStream_t stream) {
  kf_all<<<512, 512, 0, stream>>>((const u16*)d_in[0], (const u16*)d_in[1], (const u16*)d_in[2], (const u16*)d_in[3], (const u16*)d_in[4],
                                  (const u16*)d_in[5], (const u16*)d_in[6], (const u16*)d_in[7], (const u16*)d_in[8], (char*)d_ws + (size_t)WSOFF * 1048576,
                                  (const u16*)d_in[10], (const u16*)d_in[11], (const u16*)d_in[12], (const u16*)d_in[13], (u16*)d_out);
}
```

```cpp
#include <hip/hip_runtime.h>
#include <hip/hip_bf16.h>
#include <stdint.h>

typedef __attribute__((ext_vector_type(8))) short bf16x8;
typedef __attribute__((ext_vector_type(4))) float f32x4;
typedef uint16_t u16;
typedef __attribute__((ext_vector_type(4))) uint32_t u32x4;
typedef __attribute__((ext_vector_type(2))) uint32_t u32x2;
#define TSD uint32_t pr0_ = 0u, pr1_ = 0u
__device__ unsigned long long g_prb[4096];
#define TS(k, i) do { if ((i) == 0) pr0_ = (uint32_t)__builtin_amdgcn_s_memrealtime() | 1u; \
    if (ROLE == 1 && (i) == 4 && (-1 < 0 || w == -1)) pr1_ = (uint32_t)__builtin_amdgcn_s_memrealtime() | 1u; } while (0)
#define TSC(i)
#define TSE() do { if (__builtin_amdgcn_mbcnt_hi(-1, __builtin_amdgcn_mbcnt_lo(-1, 0)) == 0) \
    __hip_atomic_store(&g_prb[blockIdx.x * 8 + w], ((unsigned long long)pr1_ << 32) | pr0_, __ATOMIC_RELAXED, __HIP_MEMORY_SCOPE_AGENT); } while (0)
#define TSP
#define TSA

constexpr int L_ = 2048, D_ = 576, N_ = 128, F_ = 4608, C_ = 8;
constexpr int NF = 144;
constexpr uint32_t O_EP = 0;
constexpr uint32_t O_KF = 4096;
constexpr uint32_t O_KEY = O_KF + 8192;
constexpr uint32_t O_SV = O_KEY + 32768;
constexpr uint32_t O_SU = O_SV + 8 * 320 * 128;
constexpr uint32_t O_SG = O_SU + 262144;
constexpr uint32_t O_FB = O_SG + 16384;
constexpr uint32_t O_XC = O_FB + 8 * 1152;
constexpr uint32_t SP = 9216 + 128;
constexpr uint32_t O_SL = O_XC + 512 * 1152;
constexpr uint32_t SLU = 336;
constexpr uint32_t O_U = O_SL + 256 * SLU * 16;
constexpr int UP = 584;
constexpr uint32_t VP = 8192 + 128, VPP = 4096 + 128;
constexpr uint32_t O_V = O_U + 256 * UP * 16, O_VP = O_V + 256 * VP;
constexpr int GQP = 800;
constexpr uint32_t O_GQ = O_VP + 64 * VPP;
static_assert(O_GQ - O_V == 256 * 586 * 16 && 112 * 768 * 16 >= NF * SP && 112 * 1320 >= L_ * D_ / 8 && 256 * SLU * 16 >= NF * SP && O_SL % 128 == 0 && O_U % 128 == 0 && O_GQ % 128 == 0, "");
__host__ __device__ __forceinline__ uint32_t u_at(int tg, int d) { return O_U + 16u * (uint32_t)((tg >> 1) * (UP * 2) + (d >> 2) * 8 + (tg & 1) * 4 + (d & 3)); }
__host__ __device__ __forceinline__ uint32_t v_at(int d, int tg) {
  return (d < 512) ? O_V + (uint32_t)(d >> 1) * VP + 16u * (uint32_t)(2 * tg + (d & 1)) : O_VP + (uint32_t)(d - 512) * VPP + 16u * (uint32_t)tg;
}
#define HAVE_VAT
__device__ __forceinline__ float bf2f(u16 v) { return __uint_as_float((uint32_t)v << 16); }
__device__ __forceinline__ float bflo(uint32_t v) { return __uint_as_float(v << 16); }
__device__ __forceinline__ float bfhi(uint32_t v) { return __uint_as_float(v & 0xffff0000u); }
__device__ __forceinline__ u16 f2bf(float f) { return __builtin_bit_cast(u16, (__bf16)f); }
__device__ __forceinline__ float rbf(float f) { return bf2f(f2bf(f)); }
typedef __attribute__((ext_vector_type(2))) float f32x2;
typedef __attribute__((ext_vector_type(2))) __bf16 bf16x2v;
__device__ __forceinline__ uint32_t pack2(float a, float b) {
  return __builtin_bit_cast(uint32_t, __builtin_convertvector((f32x2){a, b}, bf16x2v));
}

#define DPP_I(v, ctrl, rmask) __builtin_amdgcn_update_dpp(0, (v), (ctrl), (rmask), 0xf, true)
__device__ __forceinline__ float row_sum(float v) {
  v += __int_as_float(DPP_I(__float_as_int(v), 0x111, 0xf));
  v += __int_as_float(DPP_I(__float_as_int(v), 0x112, 0xf));
  v += __int_as_float(DPP_I(__float_as_int(v), 0x114, 0xf));
  v += __int_as_float(DPP_I(__float_as_int(v), 0x118, 0xf));
  return v;
}
__device__ __forceinline__ float rdl(float v, int l) { return __int_as_float(__builtin_amdgcn_readlane(__float_as_int(v), l)); }
__device__ __forceinline__ float wave_sum(float v) {
  v = row_sum(v);
  asm("s_nop 1\n\tv_add_f32_dpp %0, %0, %0 row_bcast:15 row_mask:0xa bank_mask:0xf\n\t"
      "s_nop 1\n\tv_add_f32_dpp %0, %0, %0 row_bcast:31 row_mask:0xc bank_mask:0xf\n\ts_nop 0" : "+v"(v));
  return rdl(v, 63);
}
__device__ __forceinline__ float fold32(float a, float b) {
  const auto r = __builtin_amdgcn_permlane32_swap(__float_as_uint(a), __float_as_uint(b), false, false);
  return __uint_as_float(r[0]) + __uint_as_float(r[1]);
}
__device__ __forceinline__ float fold16(float a, float b) {
  const auto r = __builtin_amdgcn_permlane16_swap(__float_as_uint(a), __float_as_uint(b), false, false);
  return __uint_as_float(r[0]) + __uint_as_float(r[1]);
}
__device__ __forceinline__ void wave_sum2(float& a, float& b) {
  float v = row_sum(fold32(a, b));
  asm("s_nop 1\n\tv_add_f32_dpp %0, %0, %0 row_bcast:15 row_mask:0xa bank_mask:0xf\n\ts_nop 0" : "+v"(v));
  a = rdl(v, 31); b = rdl(v, 63);
}
__device__ __forceinline__ void wave_sum4(float& a, float& b, float& c, float& d) {
  const float v = row_sum(fold16(fold32(a, b), fold32(c, d)));
  a = rdl(v, 15); c = rdl(v, 31); b = rdl(v, 47); d = rdl(v, 63);
}
__device__ __forceinline__ float dot2(uint32_t a, uint32_t b, float c) {
  return __builtin_amdgcn_fdot2_f32_bf16(__builtin_bit_cast(bf16x2v, a), __builtin_bit_cast(bf16x2v, b), c, false);
}
__device__ __forceinline__ uint32_t row_maxu(uint32_t v) {
  v = max(v, (uint32_t)DPP_I((int)v, 0xb1, 0xf));
  v = max(v, (uint32_t)DPP_I((int)v, 0x4e, 0xf));
  v = max(v, (uint32_t)DPP_I((int)v, 0x141, 0xf));
  v = max(v, (uint32_t)DPP_I((int)v, 0x140, 0xf));
  return v;
}
__device__ __forceinline__ uint32_t wave_maxu(uint32_t v) {
  v = max(v, (uint32_t)DPP_I((int)v, 0x111, 0xf));
  v = max(v, (uint32_t)DPP_I((int)v, 0x112, 0xf));
  v = max(v, (uint32_t)DPP_I((int)v, 0x114, 0xf));
  v = max(v, (uint32_t)DPP_I((int)v, 0x118, 0xf));
  v = max(v, (uint32_t)DPP_I((int)v, 0x142, 0xa));
  v = max(v, (uint32_t)DPP_I((int)v, 0x143, 0xc));
  return (uint32_t)__builtin_amdgcn_readlane((int)v, 63);
}
__device__ __forceinline__ void wave_lds_sync() {
  __builtin_amdgcn_s_waitcnt(0xc07f);
  __builtin_amdgcn_wave_barrier();
}

__device__ __forceinline__ int kdel1(int i) { return (i & 1) ? ((i & 2) ? 765 : 511) : 256; }
__device__ __forceinline__ int kdel2(int i) { return (i & 1) ? ((i & 4) ? 1020 : 512) : 1792; }
__device__ __forceinline__ int kbase(int e) {
  const int i = e & 15;
  int l = (i & 7) + 128 * (i >> 3);
  if (e >= 16) l += kdel1(i);
  if (e >= 32) l += kdel2(i);
  return l;
}

__device__ __forceinline__ void cexp2(float ar2, float air, float l, float& er, float& ei) {
  float m = __builtin_amdgcn_exp2f(ar2 * l);
  float t = __builtin_amdgcn_fractf(air * l);
  er = m * __builtin_amdgcn_cosf(t);
  ei = m * __builtin_amdgcn_sinf(t);
}

constexpr int PS = 36;
typedef __attribute__((ext_vector_type(8))) _Float16 f16x8;
typedef __attribute__((ext_vector_type(2))) _Float16 f16x2;
__device__ __forceinline__ uint32_t packh(f32x2 v) { return __builtin_bit_cast(uint32_t, __builtin_convertvector(v, f16x2)); }
struct KPar { float ldt, are, aim, bre, bim, cre, cim; };
template <int NS> __device__ __forceinline__ uint32_t grp_maxu(uint32_t v) {
  v = max(v, (uint32_t)DPP_I((int)v, 0x111, 0xf));
  v = max(v, (uint32_t)DPP_I((int)v, 0x112, 0xf));
  v = max(v, (uint32_t)DPP_I((int)v, 0x114, 0xf));
  if (NS > 8) v = max(v, (uint32_t)DPP_I((int)v, 0x118, 0xf));
  if (NS > 16) v = max(v, (uint32_t)DPP_I((int)v, 0x142, 0xa));
  if (NS > 32) v = max(v, (uint32_t)DPP_I((int)v, 0x143, 0xc));
  return (uint32_t)__builtin_amdgcn_readlane((int)v, NS - 1);
}
template <int NS> __device__ __forceinline__ f32x4 kgen_base(const KPar& kp, float& ox, int& eb) {
  const float dt = rbf(__expf(kp.ldt));
  const float ar = dt * kp.are, ai = dt * kp.aim;
  const float dbr = dt * kp.bre, dbi = dt * kp.bim;
  const float cr = kp.cre, ci = kp.cim;
  const float ar2 = ar * 1.4426950408889634f, air = ai * 0.15915494309189535f;
  const float pr = cr * dbr - ci * dbi, pi = cr * dbi + ci * dbr;
  int ey = (int)(grp_maxu<NS>(__float_as_uint(fmaxf(fabsf(pr), fabsf(pi)))) >> 23) - 126;
  int gx = 0;
  if (__any(ar2 > 0.f)) {
    const float g = fminf(__uint_as_float(grp_maxu<NS>(__float_as_uint(fmaxf(ar2, 0.f)))), 1.f);
    gx = min((int)ceilf(g * 2304.f), 300);
    ey += (int)ceilf(g * 120.f);
  }
  ox = (float)(15 - gx);
  eb = ey + gx;
  return (f32x4){ar2, air - __builtin_rintf(air), __builtin_amdgcn_ldexpf(pr, 14 - ey), __builtin_amdgcn_ldexpf(pi, 14 - ey)};
}
__device__ __forceinline__ float kconst(int j) {
  const uint32_t wd = (j < 3) ? (256u | (511u << 11) | (765u << 22)) : (1792u | (512u << 11) | (1020u << 22));
  return (float)((wd >> (11 * ((j < 3) ? j : (j - 3) & 3))) & 0x7ffu);
}
__device__ __forceinline__ void cexp2p(f32x2 a, f32x2 t, f32x2& er, f32x2& ei) {
  const f32x2 m = {__builtin_amdgcn_exp2f(a[0]), __builtin_amdgcn_exp2f(a[1])};
  er = m * (f32x2){__builtin_amdgcn_cosf(t[0]), __builtin_amdgcn_cosf(t[1])};
  ei = m * (f32x2){__builtin_amdgcn_sinf(t[0]), __builtin_amdgcn_sinf(t[1])};
}
__device__ __forceinline__ void kgen_pair2(const float* ps, const float* p1, const float* p2, float lx0, float ly, float ox,
                                           u32x4* xh, u32x4& yh, int q) {
  const f32x4 pa = *(const f32x4*)ps, pc = *(const f32x4*)(ps + 4);
  const f32x4 w1 = *(const f32x4*)p1, w2 = *(const f32x4*)p2;
  f32x2 er, ei, xr, xi;
  cexp2p(pa.xy * ly, pa.zw * ly, er, ei);
  cexp2p(pa.xy * lx0 + ox, pa.zw * lx0, xr, xi);
  const f32x2 yr = pc.xy * er - pc.zw * ei, yi = -(pc.xy * ei) - pc.zw * er;
  const f32x2 x1r = xr * w1.xy - xi * w1.zw, x1i = xr * w1.zw + xi * w1.xy;
  const f32x2 x2r = x1r * w2.xy - x1i * w2.zw, x2i = x1r * w2.zw + x1i * w2.xy;
  yh[q] = packh(yr); yh[q + 1] = packh(yi);
  xh[0][q] = packh(xr); xh[0][q + 1] = packh(xi);
  xh[1][q] = packh(x1r); xh[1][q + 1] = packh(x1i);
  xh[2][q] = packh(x2r); xh[2][q + 1] = packh(x2i);
}
__device__ __forceinline__ void kgen_mma(const u32x4* xh, const u32x4& yh, f32x4* acc) {
#pragma unroll
  for (int t = 0; t < 3; ++t)
    acc[t] = __builtin_amdgcn_mfma_f32_16x16x32_f16(__builtin_bit_cast(f16x8, xh[t]), __builtin_bit_cast(f16x8, yh), acc[t], 0, 0, 0);
}
__device__ __forceinline__ f32x4 ldexp4(f32x4 v, int e) {
  return (f32x4){__builtin_amdgcn_ldexpf(v[0], e), __builtin_amdgcn_ldexpf(v[1], e), __builtin_amdgcn_ldexpf(v[2], e), __builtin_amdgcn_ldexpf(v[3], e)};
}

template <int I> struct IC { static constexpr int value = I; };
template <int B, int E, class F> __device__ __forceinline__ void static_for(F&& f) {
  if constexpr (B < E) { f(IC<B>{}); static_for<B + 1, E>(f); }
}
typedef __attribute__((ext_vector_type(4))) short s16x4;
__device__ __forceinline__ u32x2 trd(uint32_t a) {
  return __builtin_bit_cast(u32x2, __builtin_amdgcn_ds_read_tr16_b64_v4i16((__attribute__((address_space(3))) s16x4*)(uintptr_t)a));
}
#define DPP_F(v, ctrl) __int_as_float(DPP_I(__float_as_int(v), (ctrl), 0xf))
__device__ __forceinline__ float s_add(float a, float b) { float r; asm("v_add_f32 %0, %1, %2" : "=v"(r) : "v"(a), "v"(b)); return r; }
__device__ __forceinline__ float s_fma(float a, float b, float c) { float r; asm("v_fma_f32 %0, %1, %2, %3" : "=v"(r) : "v"(a), "v"(b), "v"(c)); return r; }
#define BAR() asm volatile("s_waitcnt lgkmcnt(0)\n\ts_barrier" ::: "memory")
#define ST16(off, v) asm volatile("global_store_dwordx4 %0, %1, %2 sc1\n\ts_nop 1" : : "v"(off), "v"(v), "s"(ws) : "memory")
#define ST4(off, v) asm volatile("global_store_dword %0, %1, %2 sc1" : : "v"(off), "v"(v), "s"(ws) : "memory")
#define LD16(r, off) asm volatile("global_load_dwordx4 %0, %1, %2 sc1" : "=v"(r) : "v"(off), "s"(ws) : "memory")
#define LD16W(r, off) asm volatile("global_load_dwordx4 %0, %1, %2 sc1\n\ts_waitcnt vmcnt(0)" : "=&v"(r) : "v"(off), "s"(ws) : "memory")
#define DRAIN() asm volatile("s_waitcnt vmcnt(0)" ::: "memory")
#define CEX(a, b) { const uint32_t hi_ = max(a, b), lo_ = min(a, b); a = hi_; b = lo_; }
#define MMB(a_, b_, c_) __builtin_amdgcn_mfma_f32_16x16x32_bf16(__builtin_bit_cast(bf16x8, a_), __builtin_bit_cast(bf16x8, b_), c_, 0, 0, 0)
constexpr int SPIN_CAP = 1 << 18;
constexpr uint32_t UMASK = 0x40074007u;
__device__ __forceinline__ uint32_t utag(uint32_t T, int j) {
  return ((T >> (3 * j)) & 0x00070007u) | (((j <= 2) ? (T << (2 - j)) : (T >> (j - 2))) & 0x40004000u);
}
__device__ __forceinline__ uint32_t max4(const u32x4& a) { return max(max(a[0], a[1]), max(a[2], a[3])); }
__device__ __forceinline__ uint32_t min4(const u32x4& a) { return min(min(a[0], a[1]), min(a[2], a[3])); }
__device__ __forceinline__ void until(uint32_t t) {
#pragma unroll 1
  for (int spin = 0; spin < 200 && (int)((uint32_t)__builtin_amdgcn_s_memrealtime() - t) < 0; ++spin) __builtin_amdgcn_s_sleep(1);
}
__device__ __forceinline__ uint32_t latest(uint32_t t) {
  const uint32_t t0 = (uint32_t)__builtin_amdgcn_readfirstlane(t) - 0x40000000u;
  return wave_maxu(t - t0) + t0;
}
__device__ __forceinline__ uint32_t gate(const char* ws, uint32_t po, uint32_t T) {
  u32x2 pv;
#pragma unroll 1
  for (int spin = 0; spin < SPIN_CAP; ++spin) {
    asm volatile("global_load_dwordx2 %0, %1, %2 sc1\n\ts_waitcnt vmcnt(0)" : "=&v"(pv) : "v"(po), "s"(ws) : "memory");
    if (!__any(pv[0] != T)) break;
    __builtin_amdgcn_s_sleep(2);
  }
  return pv[1];
}
__device__ __forceinline__ uint32_t gate2(const char* ws, uint32_t oa, uint32_t da, uint32_t ob, uint32_t T) {
  u32x2 pa, pb;
#pragma unroll 1
  for (int spin = 0; spin < SPIN_CAP; ++spin) {
    asm volatile("global_load_dwordx2 %0, %2, %4 sc1\n\tglobal_load_dwordx2 %1, %3, %4 sc1\n\ts_waitcnt vmcnt(0)" : "=&v"(pa), "=&v"(pb) : "v"(oa), "v"(ob), "s"(ws) : "memory");
    if (!__any(pa[0] != T || pb[0] != T)) break;
    __builtin_amdgcn_s_sleep(4);
  }
  pa[1] += da;
  return latest(((int)(pa[1] - pb[1]) > 0) ? pa[1] : pb[1]);
}
#define SIGNAL(off, pitch, dt) do { const uint32_t t_ = (uint32_t)__builtin_amdgcn_s_memrealtime() + (dt); \
    ST16((off) + (uint32_t)(lane >> 3) * (pitch) + 16u * (lane & 7), ((u32x4){T, t_, T, t_})); } while (0)
__device__ __forceinline__ void gsync(uint32_t a, uint32_t target) {
  uint32_t t, c, n = SPIN_CAP;
  uint64_t ex;
  asm volatile("s_waitcnt lgkmcnt(0)\n\ts_mov_b64 %3, exec\n\ts_mov_b64 exec, 1\n\tv_mov_b32 %0, 1\n\tds_add_u32 %4, %0\n\ts_mov_b64 exec, %3\n"
               "1:\n\ts_sleep 1\n\tds_read_b32 %0, %4\n\ts_sub_u32 %2, %2, 1\n\ts_cbranch_scc1 2f\n\ts_waitcnt lgkmcnt(0)\n\tv_readfirstlane_b32 %1, %0\n\t"
               "s_cmp_lt_u32 %1, %5\n\ts_cbranch_scc1 1b\n2:\n\ts_waitcnt lgkmcnt(0)"
               : "=&v"(t), "=&s"(c), "+s"(n), "=&s"(ex) : "v"(a), "n"(target) : "memory", "scc");
}
__device__ __forceinline__ void gsync0(uint32_t a, uint32_t target) {
  uint32_t t, c, n = SPIN_CAP;
  asm volatile("s_waitcnt lgkmcnt(0)\n\ts_mov_b64 exec, 1\n\tv_mov_b32 %0, 1\n\tds_add_u32 %3, %0\n\ts_mov_b64 exec, -1\n"
               "1:\n\tds_read_b32 %0, %3\n\ts_sub_u32 %2, %2, 1\n\ts_cbranch_scc1 2f\n\ts_waitcnt lgkmcnt(0)\n\tv_readfirstlane_b32 %1, %0\n\t"
               "s_cmp_lt_u32 %1, %4\n\ts_cbranch_scc1 1b\n2:\n\ts_waitcnt lgkmcnt(0)"
               : "=&v"(t), "=&s"(c), "+s"(n) : "v"(a), "n"(target) : "memory", "scc");
}
__device__ __forceinline__ void gwait(uint32_t a, uint32_t target) {
  uint32_t t, c, n = SPIN_CAP;
  asm volatile("1:\n\ts_sleep 2\n\tds_read_b32 %0, %3\n\ts_sub_u32 %2, %2, 1\n\ts_cbranch_scc1 2f\n\ts_waitcnt lgkmcnt(0)\n\tv_readfirstlane_b32 %1, %0\n\t"
               "s_cmp_lt_u32 %1, %4\n\ts_cbranch_scc1 1b\n2:\n\ts_waitcnt lgkmcnt(0)"
               : "=&v"(t), "=&s"(c), "+s"(n) : "v"(a), "n"(target) : "memory", "scc");
}
__device__ __forceinline__ void dma16(const void* base, uint32_t voff, uint32_t lds) {
  uint32_t keep;
  asm volatile("s_mov_b32 %0, m0\n\ts_mov_b32 m0, %3\n\ts_nop 0\n\tglobal_load_lds_dwordx4 %1, %2\n\ts_mov_b32 m0, %0" : "=&s"(keep) : "v"(voff), "s"(base), "s"(lds) : "memory");
}
__device__ __forceinline__ void nap(int n) {
  for (; n > 15; n -= 15) __builtin_amdgcn_s_sleep(15);
  switch (n) {
    case 1: __builtin_amdgcn_s_sleep(1); break; case 2: __builtin_amdgcn_s_sleep(2); break; case 3: __builtin_amdgcn_s_sleep(3); break;
    case 4: __builtin_amdgcn_s_sleep(4); break; case 5: __builtin_amdgcn_s_sleep(5); break; case 6: __builtin_amdgcn_s_sleep(6); break;
    case 7: __builtin_amdgcn_s_sleep(7); break; case 8: __builtin_amdgcn_s_sleep(8); break; case 9: __builtin_amdgcn_s_sleep(9); break;
    case 10: __builtin_amdgcn_s_sleep(10); break; case 11: __builtin_amdgcn_s_sleep(11); break; case 12: __builtin_amdgcn_s_sleep(12); break;
    case 13: __builtin_amdgcn_s_sleep(13); break; case 14: __builtin_amdgcn_s_sleep(14); break; case 15: __builtin_amdgcn_s_sleep(15); break;
    default: break;
  }
}
#define SLEEP(n) do { if ((n) > 0) __builtin_amdgcn_s_sleep(n); } while (0)
#define USLEEP 3
#define UPOS 1
#define QSLEEP 0
#define CSLEEP 4
#define WSLEEP 4
#define TSLEEP 0
#define VSLEEP 45
#define KSLEEP 0
#define SSLEEP 16
#define PSLEEP 2
#define PPRIO 2
#define APRIO 1
#define NTOUCH 4
#define SWZ 1
#define PREOUT 0
#define LFIRST 1
#define CBR "0"
#define WD 130
#define CD 3
#define OSLEEP 12
#define QPRIO 3
#define KPRIO 1
#define UDRAIN 0
#define GSLEEP 0
#define SIGPOS 2
#define LPRIO 3
#define TW 0
#define TPOS 0
#define R1EARLY 1
#define DPOS 1
#define NSLEEP 8
#define ASLEEP 40
#define FPRIO 1
#define PRBW 1
#define PRBPOS 1
#define TW2 0
#define DSLEEP 8
#define VD 300
#define XSLEEP 20
#define RPOS 1
#define KPRE 1
#define UD 45
#define XD 70
#define TRIG 1
#define CPF 0
#define RSLEEP 0
#define UPRIO 2
#define VD2 260
#define TWP 256
#define WDEPTH 5
#define PD 260
#define VDX 170
#define EARLY 0
#define ESLEEP 0
#define WDEPTH1 5
#define WD2 85
#define SD 45
#define KD 195
#define UFIRST 0
#define TX 1
#define QWAIT 0
#define OD 5
#define KBACK 6
#define DDEPTH 7
#define C3 1
#define XOFF 1
#define GD 120
#define XROT 2
#define WSOFF 0
#define FORCESLOW 0
#define FORCEFB 0
constexpr int CP = 832;
#define CONV_ASM \
  "s_mov_b32 s92, 0\ns_brev_b32 s93, 1\ns_cmp_eq_u32 s89, 0\ns_cbranch_scc1 10f\ns_cmp_eq_u32 s89, 3\ns_cbranch_scc1 11f\n" \
  "ds_read_b64 v[40:41], v11 offset:192\nds_read_b64 v[42:43], v11 offset:224\nds_read_b64_tr_b16 v[106:107], v10 offset:1152\n" \
  "ds_read_b64_tr_b16 v[104:105], v10 offset:1280\nds_read_b64_tr_b16 v[102:103], v10 offset:1408\nds_read_b64_tr_b16 v[100:101], v10 offset:1536\n" \
  "ds_read_b64 v[36:37], v11 offset:128\nds_read_b64 v[38:39], v11 offset:160\nds_read_b64_tr_b16 v[98:99], v10 offset:1664\n" \
  "ds_read_b64_tr_b16 v[96:97], v10 offset:1792\ns_bitcmp0_b32 s89, 2\ns_cbranch_scc1 16f\nds_read_b128 v[48:51], v14 offset:2976\n" \
  "ds_read_b128 v[52:55], v14 offset:2992\ns_bitcmp1_b32 s89, 0\ns_waitcnt lgkmcnt(0)\nv_alignbit_b32 v56, v49, v48, 16\n" \
  "v_alignbit_b32 v57, v50, v49, 16\nv_alignbit_b32 v58, v51, v50, 16\nv_alignbit_b32 v59, v52, v51, 16\nv_alignbit_b32 v60, v53, v52, 16\n" \
  "v_alignbit_b32 v61, v54, v53, 16\nv_alignbit_b32 v62, v55, v54, 16\nv_pk_add_f16 v108, v57, v58\nv_pk_add_f16 v109, v58, v59\n" \
  "v_pk_add_f16 v110, v59, v60\nv_pk_add_f16 v111, v60, v61\nv_pk_add_f16 v108, v50, v108\nv_pk_add_f16 v109, v51, v109\n" \
  "v_pk_add_f16 v110, v52, v110\nv_pk_add_f16 v111, v53, v111\nds_write_b128 v14, v[108:111] offset:7120\nv_pk_add_f16 v112, v49, v51\n" \
  "v_pk_add_f16 v113, v50, v52\nv_pk_add_f16 v114, v51, v53\nv_pk_add_f16 v115, v52, v54\nv_pk_add_f16 v112, v108, v112\n" \
  "v_pk_add_f16 v113, v109, v113\nv_pk_add_f16 v114, v110, v114\nv_pk_add_f16 v115, v111, v115\nds_write_b128 v14, v[112:115] offset:10320\n" \
  "s_cbranch_scc1 17f\nv_pk_add_f16 v108, v56, v59\nv_pk_add_f16 v109, v57, v60\nv_pk_add_f16 v110, v58, v61\nv_pk_add_f16 v111, v59, v62\n" \
  "v_pk_add_f16 v108, v112, v108\nv_pk_add_f16 v109, v113, v109\nv_pk_add_f16 v110, v114, v110\nv_pk_add_f16 v111, v115, v111\n" \
  "ds_write_b128 v14, v[108:111] offset:13520\nv_pk_add_f16 v112, v48, v52\nv_pk_add_f16 v113, v49, v53\nv_pk_add_f16 v114, v50, v54\n" \
  "v_pk_add_f16 v115, v51, v55\nv_pk_add_f16 v112, v108, v112\nv_pk_add_f16 v113, v109, v113\nv_pk_add_f16 v114, v110, v114\n" \
  "v_pk_add_f16 v115, v111, v115\nds_write_b128 v14, v[112:115] offset:15712\n17:\ns_mov_b64 exec, 1\nds_inc_u32 v15, v14\ns_mov_b64 exec, -1\n16:\n" \
  "s_waitcnt lgkmcnt(4)\nv_mfma_f32_16x16x32_f16 v[48:51], v[40:43], v[104:107], 0\ns_setprio 1\nds_read_b64 v[32:33], v11 offset:64\n" \
  "ds_read_b64 v[34:35], v11 offset:96\nv_mfma_f32_16x16x32_f16 v[52:55], v[40:43], v[102:105], 0\nds_read_b64_tr_b16 v[94:95], v10 offset:1920\n" \
  "ds_read_b64_tr_b16 v[92:93], v10 offset:2048\nv_mfma_f32_16x16x32_f16 v[56:59], v[40:43], v[100:103], 0\nds_read_b64 v[28:29], v11 offset:0\n" \
  "ds_read_b64 v[30:31], v11 offset:32\ns_waitcnt lgkmcnt(6)\nv_mfma_f32_16x16x32_f16 v[48:51], v[36:39], v[100:103], v[48:51]\n" \
  "ds_read_b64_tr_b16 v[90:91], v10 offset:2176\nds_read_b64_tr_b16 v[88:89], v10 offset:2304\n" \
  "v_mfma_f32_16x16x32_f16 v[52:55], v[36:39], v[98:101], v[52:55]\nds_read_b64 v[44:45], v11 offset:256\nds_read_b64 v[46:47], v11 offset:288\n" \
  "v_mfma_f32_16x16x32_f16 v[56:59], v[36:39], v[96:99], v[56:59]\nds_read_b64_tr_b16 v[64:65], v10 offset:1024\n" \
  "ds_read_b64_tr_b16 v[66:67], v10 offset:0\ns_waitcnt lgkmcnt(8)\nv_mfma_f32_16x16x32_f16 v[48:51], v[32:35], v[96:99], v[48:51]\n" \
  "ds_read_b64_tr_b16 v[68:69], v10 offset:1152\nds_read_b64_tr_b16 v[70:71], v10 offset:128\n" \
  "v_mfma_f32_16x16x32_f16 v[52:55], v[32:35], v[94:97], v[52:55]\nds_read_b64_tr_b16 v[72:73], v10 offset:1280\n" \
  "ds_read_b64_tr_b16 v[74:75], v10 offset:256\nv_mfma_f32_16x16x32_f16 v[56:59], v[32:35], v[92:95], v[56:59]\n" \
  "ds_read_b64_tr_b16 v[86:87], v10 offset:2432\nds_read_b64_tr_b16 v[84:85], v10 offset:2560\ns_waitcnt lgkmcnt(10)\n" \
  "v_mfma_f32_16x16x32_f16 v[48:51], v[28:31], v[92:95], v[48:51]\nds_read_b64_tr_b16 v[82:83], v10 offset:2688\n" \
  "ds_read_b64_tr_b16 v[80:81], v10 offset:2816\nv_mfma_f32_16x16x32_f16 v[52:55], v[28:31], v[90:93], v[52:55]\n" \
  "ds_read_b64_tr_b16 v[78:79], v10 offset:2944\nds_read_b64_tr_b16 v[76:77], v10 offset:3072\n" \
  "v_mfma_f32_16x16x32_f16 v[56:59], v[28:31], v[88:91], v[56:59]\ns_waitcnt lgkmcnt(6)\n" \
  "v_mfma_f32_16x16x32_f16 v[48:51], v[44:47], v[64:67], v[48:51]\nv_mfma_f32_16x16x32_f16 v[52:55], v[44:47], v[68:71], v[52:55]\n" \
  "v_mfma_f32_16x16x32_f16 v[56:59], v[44:47], v[72:75], v[56:59]\ns_setprio 0\ns_bitcmp1_b32 s89, 2\ns_cbranch_scc1 18f\n" \
  "ds_read_b128 v[64:67], v14 offset:2976\nds_read_b128 v[68:71], v14 offset:2992\ns_bitcmp1_b32 s89, 0\ns_waitcnt lgkmcnt(0)\n" \
  "v_alignbit_b32 v108, v65, v64, 16\nv_alignbit_b32 v109, v66, v65, 16\nv_alignbit_b32 v110, v67, v66, 16\nv_alignbit_b32 v111, v68, v67, 16\n" \
  "v_alignbit_b32 v112, v69, v68, 16\nv_alignbit_b32 v113, v70, v69, 16\nv_alignbit_b32 v114, v71, v70, 16\nv_pk_add_f16 v116, v109, v110\n" \
  "v_pk_add_f16 v117, v110, v111\nv_pk_add_f16 v118, v111, v112\nv_pk_add_f16 v119, v112, v113\nv_pk_add_f16 v116, v66, v116\n" \
  "v_pk_add_f16 v117, v67, v117\nv_pk_add_f16 v118, v68, v118\nv_pk_add_f16 v119, v69, v119\nds_write_b128 v14, v[116:119] offset:7120\n" \
  "v_pk_add_f16 v120, v65, v67\nv_pk_add_f16 v121, v66, v68\nv_pk_add_f16 v122, v67, v69\nv_pk_add_f16 v123, v68, v70\nv_pk_add_f16 v120, v116, v120\n" \
  "v_pk_add_f16 v121, v117, v121\nv_pk_add_f16 v122, v118, v122\nv_pk_add_f16 v123, v119, v123\nds_write_b128 v14, v[120:123] offset:10320\n" \
  "s_cbranch_scc1 19f\nv_pk_add_f16 v116, v108, v111\nv_pk_add_f16 v117, v109, v112\nv_pk_add_f16 v118, v110, v113\nv_pk_add_f16 v119, v111, v114\n" \
  "v_pk_add_f16 v116, v120, v116\nv_pk_add_f16 v117, v121, v117\nv_pk_add_f16 v118, v122, v118\nv_pk_add_f16 v119, v123, v119\n" \
  "ds_write_b128 v14, v[116:119] offset:13520\nv_pk_add_f16 v120, v64, v68\nv_pk_add_f16 v121, v65, v69\nv_pk_add_f16 v122, v66, v70\n" \
  "v_pk_add_f16 v123, v67, v71\nv_pk_add_f16 v120, v116, v120\nv_pk_add_f16 v121, v117, v121\nv_pk_add_f16 v122, v118, v122\n" \
  "v_pk_add_f16 v123, v119, v123\nds_write_b128 v14, v[120:123] offset:15712\n19:\ns_mov_b64 exec, 1\nds_inc_u32 v15, v14\ns_mov_b64 exec, -1\n18:\n" \
  "ds_read_b64_tr_b16 v[64:65], v10 offset:1408\nds_read_b64_tr_b16 v[66:67], v10 offset:384\nds_read_b64_tr_b16 v[68:69], v10 offset:1536\n" \
  "ds_read_b64_tr_b16 v[70:71], v10 offset:512\nds_read_b64_tr_b16 v[72:73], v10 offset:1664\nds_read_b64_tr_b16 v[74:75], v10 offset:640\n" \
  "v_add_f32_dpp v114, v51, v54 wave_ror:1 row_mask:0x1 bank_mask:0x1\nv_add_f32_dpp v118, v55, v58 wave_ror:1 row_mask:0x1 bank_mask:0x1\n" \
  "v_add_f32_dpp v110, v51, v50 wave_shr:1 row_mask:0xf bank_mask:0xf bound_ctrl:1\n" \
  "v_add_f32_dpp v114, v55, v54 wave_shr:1 row_mask:0xf bank_mask:0xf\nv_add_f32_dpp v118, v59, v58 wave_shr:1 row_mask:0xf bank_mask:0xf\n" \
  "v_add_f32_dpp v113, v110, v53 wave_ror:1 row_mask:0x1 bank_mask:0x1\nv_add_f32_dpp v117, v114, v57 wave_ror:1 row_mask:0x1 bank_mask:0x1\n" \
  "v_add_f32_dpp v109, v110, v49 wave_shr:1 row_mask:0xf bank_mask:0xf bound_ctrl:1\n" \
  "v_add_f32_dpp v113, v114, v53 wave_shr:1 row_mask:0xf bank_mask:0xf\nv_add_f32_dpp v117, v118, v57 wave_shr:1 row_mask:0xf bank_mask:0xf\n" \
  "v_add_f32_dpp v112, v109, v52 wave_ror:1 row_mask:0x1 bank_mask:0x1\nv_add_f32_dpp v116, v113, v56 wave_ror:1 row_mask:0x1 bank_mask:0x1\n" \
  "v_add_f32_dpp v108, v109, v48 wave_shr:1 row_mask:0xf bank_mask:0xf bound_ctrl:1\n" \
  "v_add_f32_dpp v112, v113, v52 wave_shr:1 row_mask:0xf bank_mask:0xf\nv_add_f32_dpp v116, v117, v56 wave_shr:1 row_mask:0xf bank_mask:0xf\n" \
  "ds_write2st64_b32 v12, v108, v112 offset0:0 offset1:1\ns_waitcnt lgkmcnt(1)\nv_mfma_f32_16x16x32_f16 v[60:63], v[40:43], v[98:101], 0\n" \
  "s_setprio 1\nv_mfma_f32_16x16x32_f16 v[48:51], v[40:43], v[96:99], 0\nv_mfma_f32_16x16x32_f16 v[52:55], v[40:43], v[94:97], 0\n" \
  "v_mfma_f32_16x16x32_f16 v[60:63], v[36:39], v[94:97], v[60:63]\nv_mfma_f32_16x16x32_f16 v[48:51], v[36:39], v[92:95], v[48:51]\n" \
  "v_mfma_f32_16x16x32_f16 v[52:55], v[36:39], v[90:93], v[52:55]\nv_mfma_f32_16x16x32_f16 v[60:63], v[32:35], v[90:93], v[60:63]\n" \
  "v_mfma_f32_16x16x32_f16 v[48:51], v[32:35], v[88:91], v[48:51]\nv_mfma_f32_16x16x32_f16 v[52:55], v[32:35], v[86:89], v[52:55]\n" \
  "v_mfma_f32_16x16x32_f16 v[60:63], v[28:31], v[86:89], v[60:63]\nv_mfma_f32_16x16x32_f16 v[48:51], v[28:31], v[84:87], v[48:51]\n" \
  "v_mfma_f32_16x16x32_f16 v[52:55], v[28:31], v[82:85], v[52:55]\nv_mfma_f32_16x16x32_f16 v[60:63], v[44:47], v[64:67], v[60:63]\n" \
  "v_mfma_f32_16x16x32_f16 v[48:51], v[44:47], v[68:71], v[48:51]\nv_mfma_f32_16x16x32_f16 v[52:55], v[44:47], v[72:75], v[52:55]\ns_setprio 0\n" \
  "ds_read_b64_tr_b16 v[64:65], v10 offset:1792\nds_read_b64_tr_b16 v[66:67], v10 offset:768\nds_read_b64_tr_b16 v[68:69], v10 offset:1920\n" \
  "ds_read_b64_tr_b16 v[70:71], v10 offset:896\nds_read_b64_tr_b16 v[72:73], v10 offset:2048\nds_read_b64_tr_b16 v[74:75], v10 offset:1024\n" \
  "v_add_f32_dpp v122, v59, v62 wave_ror:1 row_mask:0x1 bank_mask:0x1\nv_add_f32_dpp v110, v63, v50 wave_ror:1 row_mask:0x1 bank_mask:0x1\n" \
  "v_add_f32_dpp v114, v51, v54 wave_ror:1 row_mask:0x1 bank_mask:0x1\nv_add_f32_dpp v122, v63, v62 wave_shr:1 row_mask:0xf bank_mask:0xf\n" \
  "v_add_f32_dpp v110, v51, v50 wave_shr:1 row_mask:0xf bank_mask:0xf\nv_add_f32_dpp v114, v55, v54 wave_shr:1 row_mask:0xf bank_mask:0xf\n" \
  "v_add_f32_dpp v121, v118, v61 wave_ror:1 row_mask:0x1 bank_mask:0x1\nv_add_f32_dpp v109, v122, v49 wave_ror:1 row_mask:0x1 bank_mask:0x1\n" \
  "v_add_f32_dpp v113, v110, v53 wave_ror:1 row_mask:0x1 bank_mask:0x1\nv_add_f32_dpp v121, v122, v61 wave_shr:1 row_mask:0xf bank_mask:0xf\n" \
  "v_add_f32_dpp v109, v110, v49 wave_shr:1 row_mask:0xf bank_mask:0xf\nv_add_f32_dpp v113, v114, v53 wave_shr:1 row_mask:0xf bank_mask:0xf\n" \
  "v_add_f32_dpp v120, v117, v60 wave_ror:1 row_mask:0x1 bank_mask:0x1\nv_add_f32_dpp v108, v121, v48 wave_ror:1 row_mask:0x1 bank_mask:0x1\n" \
  "v_add_f32_dpp v112, v109, v52 wave_ror:1 row_mask:0x1 bank_mask:0x1\nv_add_f32_dpp v120, v121, v60 wave_shr:1 row_mask:0xf bank_mask:0xf\n" \
  "v_add_f32_dpp v108, v109, v48 wave_shr:1 row_mask:0xf bank_mask:0xf\nv_add_f32_dpp v112, v113, v52 wave_shr:1 row_mask:0xf bank_mask:0xf\n" \
  "ds_write2st64_b32 v12, v116, v120 offset0:2 offset1:3\nds_write2st64_b32 v12, v108, v112 offset0:4 offset1:5\ns_waitcnt lgkmcnt(2)\n" \
  "v_mfma_f32_16x16x32_f16 v[56:59], v[40:43], v[92:95], 0\ns_setprio 1\nv_mfma_f32_16x16x32_f16 v[60:63], v[40:43], v[90:93], 0\n" \
  "v_mfma_f32_16x16x32_f16 v[48:51], v[40:43], v[88:91], 0\nv_mfma_f32_16x16x32_f16 v[56:59], v[36:39], v[88:91], v[56:59]\n" \
  "v_mfma_f32_16x16x32_f16 v[60:63], v[36:39], v[86:89], v[60:63]\nv_mfma_f32_16x16x32_f16 v[48:51], v[36:39], v[84:87], v[48:51]\n" \
  "v_mfma_f32_16x16x32_f16 v[56:59], v[32:35], v[84:87], v[56:59]\nv_mfma_f32_16x16x32_f16 v[60:63], v[32:35], v[82:85], v[60:63]\n" \
  "v_mfma_f32_16x16x32_f16 v[48:51], v[32:35], v[80:83], v[48:51]\nv_mfma_f32_16x16x32_f16 v[56:59], v[28:31], v[80:83], v[56:59]\n" \
  "v_mfma_f32_16x16x32_f16 v[60:63], v[28:31], v[78:81], v[60:63]\nv_mfma_f32_16x16x32_f16 v[48:51], v[28:31], v[76:79], v[48:51]\n" \
  "v_mfma_f32_16x16x32_f16 v[56:59], v[44:47], v[64:67], v[56:59]\nv_mfma_f32_16x16x32_f16 v[60:63], v[44:47], v[68:71], v[60:63]\n" \
  "v_mfma_f32_16x16x32_f16 v[48:51], v[44:47], v[72:75], v[48:51]\ns_setprio 0\ns_nop 4\n" \
  "v_add_f32_dpp v118, v55, v58 wave_ror:1 row_mask:0x1 bank_mask:0x1\nv_add_f32_dpp v122, v59, v62 wave_ror:1 row_mask:0x1 bank_mask:0x1\n" \
  "v_add_f32_dpp v110, v63, v50 wave_ror:1 row_mask:0x1 bank_mask:0x1\nv_add_f32_dpp v118, v59, v58 wave_shr:1 row_mask:0xf bank_mask:0xf\n" \
  "v_add_f32_dpp v122, v63, v62 wave_shr:1 row_mask:0xf bank_mask:0xf\nv_add_f32_dpp v110, v51, v50 wave_shr:1 row_mask:0xf bank_mask:0xf\n" \
  "v_add_f32_dpp v117, v114, v57 wave_ror:1 row_mask:0x1 bank_mask:0x1\nv_add_f32_dpp v121, v118, v61 wave_ror:1 row_mask:0x1 bank_mask:0x1\n" \
  "v_add_f32_dpp v109, v122, v49 wave_ror:1 row_mask:0x1 bank_mask:0x1\nv_add_f32_dpp v117, v118, v57 wave_shr:1 row_mask:0xf bank_mask:0xf\n" \
  "v_add_f32_dpp v121, v122, v61 wave_shr:1 row_mask:0xf bank_mask:0xf\nv_add_f32_dpp v109, v110, v49 wave_shr:1 row_mask:0xf bank_mask:0xf\n" \
  "v_add_f32_dpp v116, v113, v56 wave_ror:1 row_mask:0x1 bank_mask:0x1\nv_add_f32_dpp v120, v117, v60 wave_ror:1 row_mask:0x1 bank_mask:0x1\n" \
  "v_add_f32_dpp v108, v121, v48 wave_ror:1 row_mask:0x1 bank_mask:0x1\nv_add_f32_dpp v116, v117, v56 wave_shr:1 row_mask:0xf bank_mask:0xf\n" \
  "v_add_f32_dpp v120, v121, v60 wave_shr:1 row_mask:0xf bank_mask:0xf\nv_add_f32_dpp v108, v109, v48 wave_shr:1 row_mask:0xf bank_mask:0xf\n" \
  "ds_write2st64_b32 v12, v116, v120 offset0:6 offset1:7\nds_write_b32 v12, v108 offset:2048\nv_mov_b32 v111, v51\ns_mov_b64 exec, s[92:93]\n" \
  "ds_write_b128 v13, v[108:111]\ns_mov_b64 exec, -1\ns_setpc_b64 s[90:91]\n10:\nds_read_b64 v[28:29], v11 offset:0\n" \
  "ds_read_b64 v[30:31], v11 offset:32\nds_read_b64_tr_b16 v[104:105], v10 offset:1920\nds_read_b64_tr_b16 v[102:103], v10 offset:2048\n" \
  "ds_read_b64_tr_b16 v[100:101], v10 offset:2176\nds_read_b64_tr_b16 v[98:99], v10 offset:2304\nds_read_b64_tr_b16 v[96:97], v10 offset:2432\n" \
  "ds_read_b64_tr_b16 v[94:95], v10 offset:2560\nds_read_b64_tr_b16 v[92:93], v10 offset:2688\nds_read_b64 v[32:33], v11 offset:64\n" \
  "s_waitcnt lgkmcnt(6)\nv_mfma_f32_16x16x32_f16 v[48:51], v[28:31], v[102:105], 0\ns_setprio 1\nds_read_b64 v[34:35], v11 offset:96\n" \
  "ds_read_b64 v[36:37], v11 offset:128\ns_waitcnt lgkmcnt(7)\nv_mfma_f32_16x16x32_f16 v[52:55], v[28:31], v[100:103], 0\n" \
  "ds_read_b64 v[38:39], v11 offset:160\nds_read_b64_tr_b16 v[90:91], v10 offset:2816\ns_waitcnt lgkmcnt(8)\n" \
  "v_mfma_f32_16x16x32_f16 v[56:59], v[28:31], v[98:101], 0\nds_read_b64_tr_b16 v[88:89], v10 offset:2944\n" \
  "ds_read_b64_tr_b16 v[86:87], v10 offset:3072\ns_waitcnt lgkmcnt(9)\nv_mfma_f32_16x16x32_f16 v[60:63], v[28:31], v[96:99], 0\n" \
  "ds_read_b64 v[40:41], v11 offset:192\nds_read_b64 v[42:43], v11 offset:224\ns_waitcnt lgkmcnt(10)\n" \
  "v_mfma_f32_16x16x32_f16 v[64:67], v[28:31], v[94:97], 0\nds_read_b64 v[44:45], v11 offset:256\nds_read_b64 v[46:47], v11 offset:328\n" \
  "s_waitcnt lgkmcnt(11)\nv_mfma_f32_16x16x32_f16 v[68:71], v[28:31], v[92:95], 0\nds_read_b64_tr_b16 v[84:85], v10 offset:3200\n" \
  "ds_read_b64_tr_b16 v[82:83], v10 offset:3328\ns_waitcnt lgkmcnt(11)\nv_mfma_f32_16x16x32_f16 v[56:59], v[32:35], v[102:105], v[56:59]\n" \
  "ds_read_b64_tr_b16 v[80:81], v10 offset:3456\nds_read_b64_tr_b16 v[78:79], v10 offset:3584\n" \
  "v_mfma_f32_16x16x32_f16 v[60:63], v[32:35], v[100:103], v[60:63]\nds_read_b64_tr_b16 v[76:77], v10 offset:3712\n" \
  "v_mfma_f32_16x16x32_f16 v[64:67], v[32:35], v[98:101], v[64:67]\nv_mfma_f32_16x16x32_f16 v[68:71], v[32:35], v[96:99], v[68:71]\n" \
  "s_waitcnt lgkmcnt(12)\nv_mfma_f32_16x16x32_f16 v[64:67], v[36:39], v[102:105], v[64:67]\n" \
  "v_mfma_f32_16x16x32_f16 v[68:71], v[36:39], v[100:103], v[68:71]\ns_setprio 0\nv_add_f32_dpp v112, v51, v54 wave_ror:1 row_mask:0x1 bank_mask:0x1\n" \
  "v_add_f32_dpp v116, v55, v58 wave_ror:1 row_mask:0x1 bank_mask:0x1\n" \
  "v_add_f32_dpp v108, v51, v50 wave_shr:1 row_mask:0xf bank_mask:0xf bound_ctrl:1\n" \
  "v_add_f32_dpp v112, v55, v54 wave_shr:1 row_mask:0xf bank_mask:0xf\nv_add_f32_dpp v116, v59, v58 wave_shr:1 row_mask:0xf bank_mask:0xf\n" \
  "v_add_f32_dpp v111, v108, v53 wave_ror:1 row_mask:0x1 bank_mask:0x1\nv_add_f32_dpp v115, v112, v57 wave_ror:1 row_mask:0x1 bank_mask:0x1\n" \
  "v_add_f32_dpp v107, v108, v49 wave_shr:1 row_mask:0xf bank_mask:0xf bound_ctrl:1\n" \
  "v_add_f32_dpp v111, v112, v53 wave_shr:1 row_mask:0xf bank_mask:0xf\nv_add_f32_dpp v115, v116, v57 wave_shr:1 row_mask:0xf bank_mask:0xf\n" \
  "v_add_f32_dpp v110, v107, v52 wave_ror:1 row_mask:0x1 bank_mask:0x1\nv_add_f32_dpp v114, v111, v56 wave_ror:1 row_mask:0x1 bank_mask:0x1\n" \
  "v_add_f32_dpp v106, v107, v48 wave_shr:1 row_mask:0xf bank_mask:0xf bound_ctrl:1\n" \
  "v_add_f32_dpp v110, v111, v52 wave_shr:1 row_mask:0xf bank_mask:0xf\nv_add_f32_dpp v114, v115, v56 wave_shr:1 row_mask:0xf bank_mask:0xf\n" \
  "ds_write2st64_b32 v12, v106, v110 offset0:0 offset1:1\nv_add_f32_dpp v120, v59, v62 wave_ror:1 row_mask:0x1 bank_mask:0x1\n" \
  "v_add_f32_dpp v108, v63, v66 wave_ror:1 row_mask:0x1 bank_mask:0x1\nv_add_f32_dpp v112, v67, v70 wave_ror:1 row_mask:0x1 bank_mask:0x1\n" \
  "v_add_f32_dpp v120, v63, v62 wave_shr:1 row_mask:0xf bank_mask:0xf\nv_add_f32_dpp v108, v67, v66 wave_shr:1 row_mask:0xf bank_mask:0xf\n" \
  "v_add_f32_dpp v112, v71, v70 wave_shr:1 row_mask:0xf bank_mask:0xf\nv_add_f32_dpp v119, v116, v61 wave_ror:1 row_mask:0x1 bank_mask:0x1\n" \
  "v_add_f32_dpp v107, v120, v65 wave_ror:1 row_mask:0x1 bank_mask:0x1\nv_add_f32_dpp v111, v108, v69 wave_ror:1 row_mask:0x1 bank_mask:0x1\n" \
  "v_add_f32_dpp v119, v120, v61 wave_shr:1 row_mask:0xf bank_mask:0xf\nv_add_f32_dpp v107, v108, v65 wave_shr:1 row_mask:0xf bank_mask:0xf\n" \
  "v_add_f32_dpp v111, v112, v69 wave_shr:1 row_mask:0xf bank_mask:0xf\nv_add_f32_dpp v118, v115, v60 wave_ror:1 row_mask:0x1 bank_mask:0x1\n" \
  "v_add_f32_dpp v106, v119, v64 wave_ror:1 row_mask:0x1 bank_mask:0x1\nv_add_f32_dpp v110, v107, v68 wave_ror:1 row_mask:0x1 bank_mask:0x1\n" \
  "v_add_f32_dpp v118, v119, v60 wave_shr:1 row_mask:0xf bank_mask:0xf\nv_add_f32_dpp v106, v107, v64 wave_shr:1 row_mask:0xf bank_mask:0xf\n" \
  "v_add_f32_dpp v110, v111, v68 wave_shr:1 row_mask:0xf bank_mask:0xf\nds_write2st64_b32 v12, v114, v118 offset0:2 offset1:3\n" \
  "ds_write2st64_b32 v12, v106, v110 offset0:4 offset1:5\ns_waitcnt lgkmcnt(3)\nv_mfma_f32_16x16x32_f16 v[72:75], v[28:31], v[90:93], 0\ns_setprio 1\n" \
  "v_mfma_f32_16x16x32_f16 v[48:51], v[28:31], v[88:91], 0\nv_mfma_f32_16x16x32_f16 v[52:55], v[28:31], v[86:89], 0\n" \
  "v_mfma_f32_16x16x32_f16 v[72:75], v[32:35], v[94:97], v[72:75]\nv_mfma_f32_16x16x32_f16 v[48:51], v[32:35], v[92:95], v[48:51]\n" \
  "v_mfma_f32_16x16x32_f16 v[52:55], v[32:35], v[90:93], v[52:55]\nv_mfma_f32_16x16x32_f16 v[72:75], v[36:39], v[98:101], v[72:75]\n" \
  "v_mfma_f32_16x16x32_f16 v[48:51], v[36:39], v[96:99], v[48:51]\nv_mfma_f32_16x16x32_f16 v[52:55], v[36:39], v[94:97], v[52:55]\n" \
  "v_mfma_f32_16x16x32_f16 v[72:75], v[40:43], v[102:105], v[72:75]\nv_mfma_f32_16x16x32_f16 v[48:51], v[40:43], v[100:103], v[48:51]\n" \
  "v_mfma_f32_16x16x32_f16 v[52:55], v[40:43], v[98:101], v[52:55]\nv_mfma_f32_16x16x32_f16 v[52:55], v[44:47], v[102:105], v[52:55]\ns_setprio 0\n" \
  "s_nop 3\nv_add_f32_dpp v116, v71, v74 wave_ror:1 row_mask:0x1 bank_mask:0x1\nv_add_f32_dpp v120, v75, v50 wave_ror:1 row_mask:0x1 bank_mask:0x1\n" \
  "s_nop 0\nv_add_f32_dpp v108, v51, v54 wave_ror:1 row_mask:0x1 bank_mask:0x1\nv_add_f32_dpp v116, v75, v74 wave_shr:1 row_mask:0xf bank_mask:0xf\n" \
  "v_add_f32_dpp v120, v51, v50 wave_shr:1 row_mask:0xf bank_mask:0xf\nv_add_f32_dpp v108, v55, v54 wave_shr:1 row_mask:0xf bank_mask:0xf\n" \
  "v_add_f32_dpp v115, v112, v73 wave_ror:1 row_mask:0x1 bank_mask:0x1\nv_add_f32_dpp v119, v116, v49 wave_ror:1 row_mask:0x1 bank_mask:0x1\n" \
  "v_add_f32_dpp v107, v120, v53 wave_ror:1 row_mask:0x1 bank_mask:0x1\nv_add_f32_dpp v115, v116, v73 wave_shr:1 row_mask:0xf bank_mask:0xf\n" \
  "v_add_f32_dpp v119, v120, v49 wave_shr:1 row_mask:0xf bank_mask:0xf\nv_add_f32_dpp v107, v108, v53 wave_shr:1 row_mask:0xf bank_mask:0xf\n" \
  "v_add_f32_dpp v114, v111, v72 wave_ror:1 row_mask:0x1 bank_mask:0x1\nv_add_f32_dpp v118, v115, v48 wave_ror:1 row_mask:0x1 bank_mask:0x1\n" \
  "v_add_f32_dpp v106, v119, v52 wave_ror:1 row_mask:0x1 bank_mask:0x1\nv_add_f32_dpp v114, v115, v72 wave_shr:1 row_mask:0xf bank_mask:0xf\n" \
  "v_add_f32_dpp v118, v119, v48 wave_shr:1 row_mask:0xf bank_mask:0xf\nv_add_f32_dpp v106, v107, v52 wave_shr:1 row_mask:0xf bank_mask:0xf\n" \
  "ds_write2st64_b32 v12, v114, v118 offset0:6 offset1:7\nv_mfma_f32_16x16x32_f16 v[56:59], v[28:31], v[84:87], 0\ns_setprio 1\n" \
  "v_mfma_f32_16x16x32_f16 v[60:63], v[28:31], v[82:85], 0\nv_mfma_f32_16x16x32_f16 v[64:67], v[28:31], v[80:83], 0\n" \
  "v_mfma_f32_16x16x32_f16 v[56:59], v[32:35], v[88:91], v[56:59]\nv_mfma_f32_16x16x32_f16 v[60:63], v[32:35], v[86:89], v[60:63]\n" \
  "v_mfma_f32_16x16x32_f16 v[64:67], v[32:35], v[84:87], v[64:67]\nv_mfma_f32_16x16x32_f16 v[56:59], v[36:39], v[92:95], v[56:59]\n" \
  "v_mfma_f32_16x16x32_f16 v[60:63], v[36:39], v[90:93], v[60:63]\nv_mfma_f32_16x16x32_f16 v[64:67], v[36:39], v[88:91], v[64:67]\n" \
  "v_mfma_f32_16x16x32_f16 v[56:59], v[40:43], v[96:99], v[56:59]\nv_mfma_f32_16x16x32_f16 v[60:63], v[40:43], v[94:97], v[60:63]\n" \
  "v_mfma_f32_16x16x32_f16 v[64:67], v[40:43], v[92:95], v[64:67]\nv_mfma_f32_16x16x32_f16 v[56:59], v[44:47], v[100:103], v[56:59]\n" \
  "v_mfma_f32_16x16x32_f16 v[60:63], v[44:47], v[98:101], v[60:63]\nv_mfma_f32_16x16x32_f16 v[64:67], v[44:47], v[96:99], v[64:67]\ns_setprio 0\n" \
  "s_nop 4\nv_add_f32_dpp v112, v55, v58 wave_ror:1 row_mask:0x1 bank_mask:0x1\nv_add_f32_dpp v116, v59, v62 wave_ror:1 row_mask:0x1 bank_mask:0x1\n" \
  "v_add_f32_dpp v120, v63, v66 wave_ror:1 row_mask:0x1 bank_mask:0x1\nv_add_f32_dpp v112, v59, v58 wave_shr:1 row_mask:0xf bank_mask:0xf\n" \
  "v_add_f32_dpp v116, v63, v62 wave_shr:1 row_mask:0xf bank_mask:0xf\nv_add_f32_dpp v120, v67, v66 wave_shr:1 row_mask:0xf bank_mask:0xf\n" \
  "v_add_f32_dpp v111, v108, v57 wave_ror:1 row_mask:0x1 bank_mask:0x1\nv_add_f32_dpp v115, v112, v61 wave_ror:1 row_mask:0x1 bank_mask:0x1\n" \
  "v_add_f32_dpp v119, v116, v65 wave_ror:1 row_mask:0x1 bank_mask:0x1\nv_add_f32_dpp v111, v112, v57 wave_shr:1 row_mask:0xf bank_mask:0xf\n" \
  "v_add_f32_dpp v115, v116, v61 wave_shr:1 row_mask:0xf bank_mask:0xf\nv_add_f32_dpp v119, v120, v65 wave_shr:1 row_mask:0xf bank_mask:0xf\n" \
  "v_add_f32_dpp v110, v107, v56 wave_ror:1 row_mask:0x1 bank_mask:0x1\nv_add_f32_dpp v114, v111, v60 wave_ror:1 row_mask:0x1 bank_mask:0x1\n" \
  "v_add_f32_dpp v118, v115, v64 wave_ror:1 row_mask:0x1 bank_mask:0x1\nv_add_f32_dpp v110, v111, v56 wave_shr:1 row_mask:0xf bank_mask:0xf\n" \
  "v_add_f32_dpp v114, v115, v60 wave_shr:1 row_mask:0xf bank_mask:0xf\nv_add_f32_dpp v118, v119, v64 wave_shr:1 row_mask:0xf bank_mask:0xf\n" \
  "ds_write2st64_b32 v12, v106, v110 offset0:8 offset1:9\nds_write2st64_b32 v12, v114, v118 offset0:10 offset1:11\n" \
  "v_mfma_f32_16x16x32_f16 v[68:71], v[28:31], v[78:81], 0\ns_setprio 1\nv_mfma_f32_16x16x32_f16 v[72:75], v[28:31], v[76:79], 0\n" \
  "v_mfma_f32_16x16x32_f16 v[68:71], v[32:35], v[82:85], v[68:71]\nv_mfma_f32_16x16x32_f16 v[72:75], v[32:35], v[80:83], v[72:75]\n" \
  "v_mfma_f32_16x16x32_f16 v[68:71], v[36:39], v[86:89], v[68:71]\nv_mfma_f32_16x16x32_f16 v[72:75], v[36:39], v[84:87], v[72:75]\n" \
  "v_mfma_f32_16x16x32_f16 v[68:71], v[40:43], v[90:93], v[68:71]\nv_mfma_f32_16x16x32_f16 v[72:75], v[40:43], v[88:91], v[72:75]\n" \
  "v_mfma_f32_16x16x32_f16 v[68:71], v[44:47], v[94:97], v[68:71]\nv_mfma_f32_16x16x32_f16 v[72:75], v[44:47], v[92:95], v[72:75]\ns_setprio 0\n" \
  "s_nop 5\nv_add_f32_dpp v108, v67, v70 wave_ror:1 row_mask:0x1 bank_mask:0x1\nv_add_f32_dpp v112, v71, v74 wave_ror:1 row_mask:0x1 bank_mask:0x1\n" \
  "s_nop 0\nv_add_f32_dpp v108, v71, v70 wave_shr:1 row_mask:0xf bank_mask:0xf\nv_add_f32_dpp v112, v75, v74 wave_shr:1 row_mask:0xf bank_mask:0xf\n" \
  "v_add_f32_dpp v107, v120, v69 wave_ror:1 row_mask:0x1 bank_mask:0x1\nv_add_f32_dpp v111, v108, v73 wave_ror:1 row_mask:0x1 bank_mask:0x1\ns_nop 0\n" \
  "v_add_f32_dpp v107, v108, v69 wave_shr:1 row_mask:0xf bank_mask:0xf\nv_add_f32_dpp v111, v112, v73 wave_shr:1 row_mask:0xf bank_mask:0xf\n" \
  "v_add_f32_dpp v106, v119, v68 wave_ror:1 row_mask:0x1 bank_mask:0x1\nv_add_f32_dpp v110, v107, v72 wave_ror:1 row_mask:0x1 bank_mask:0x1\ns_nop 0\n" \
  "v_add_f32_dpp v106, v107, v68 wave_shr:1 row_mask:0xf bank_mask:0xf\nv_add_f32_dpp v110, v111, v72 wave_shr:1 row_mask:0xf bank_mask:0xf\n" \
  "ds_write2st64_b32 v12, v106, v110 offset0:12 offset1:13\nv_mov_b32 v113, v75\ns_mov_b64 exec, s[92:93]\nds_write_b128 v13, v[110:113]\n" \
  "s_mov_b64 exec, -1\ns_setpc_b64 s[90:91]\n11:\nds_read_b128 v[68:71], v13 offset:2976\nds_read_b128 v[72:75], v13 offset:2992\n" \
  "ds_read2_b32 v[52:53], v15 offset0:0 offset1:1\nds_read2_b32 v[54:55], v15 offset0:8 offset1:9\nds_read2_b32 v[56:57], v15 offset0:16 offset1:17\n" \
  "ds_read2_b32 v[58:59], v15 offset0:24 offset1:25\nds_read2_b32 v[60:61], v15 offset0:32 offset1:33\n" \
  "ds_read2_b32 v[62:63], v15 offset0:40 offset1:41\nds_read2_b32 v[64:65], v15 offset0:48 offset1:49\n" \
  "ds_read2_b32 v[66:67], v15 offset0:56 offset1:57\ns_waitcnt lgkmcnt(8)\nv_alignbit_b32 v76, v69, v68, 16\nv_alignbit_b32 v77, v70, v69, 16\n" \
  "v_alignbit_b32 v78, v71, v70, 16\nv_alignbit_b32 v79, v72, v71, 16\nv_alignbit_b32 v80, v73, v72, 16\nv_alignbit_b32 v81, v74, v73, 16\n" \
  "v_alignbit_b32 v82, v75, v74, 16\ns_waitcnt lgkmcnt(0)\nds_read_b64 v[28:29], v11 offset:0\nds_read_b64 v[30:31], v11 offset:32\n" \
  "ds_read_b64 v[32:33], v11 offset:64\nds_read_b64 v[34:35], v11 offset:96\nds_read_b64 v[36:37], v11 offset:128\n" \
  "ds_read_b64 v[38:39], v11 offset:160\nds_read_b64 v[40:41], v11 offset:192\nds_read_b64 v[42:43], v11 offset:224\n" \
  "ds_read_b64 v[44:45], v11 offset:256\nds_read_b64 v[46:47], v11 offset:288\nds_read_b64 v[48:49], v11 offset:320\n" \
  "ds_read_b64 v[50:51], v11 offset:352\nv_pk_add_f16 v84, v77, v78\nv_pk_add_f16 v85, v78, v79\nv_pk_add_f16 v86, v79, v80\n" \
  "v_pk_add_f16 v87, v80, v81\nv_pk_add_f16 v84, v70, v84\nv_pk_add_f16 v85, v71, v85\nv_pk_add_f16 v86, v72, v86\nv_pk_add_f16 v87, v73, v87\n" \
  "ds_write_b128 v13, v[84:87] offset:7120\nv_pk_add_f16 v88, v69, v71\nv_pk_add_f16 v89, v70, v72\nv_pk_add_f16 v90, v71, v73\n" \
  "v_pk_add_f16 v91, v72, v74\nv_pk_add_f16 v88, v84, v88\nv_pk_add_f16 v89, v85, v89\nv_pk_add_f16 v90, v86, v90\nv_pk_add_f16 v91, v87, v91\n" \
  "s_waitcnt lgkmcnt(1)\nds_write_b128 v13, v[88:91] offset:10320\nv_pk_add_f16 v84, v76, v79\nv_pk_add_f16 v85, v77, v80\n" \
  "v_pk_add_f16 v86, v78, v81\nv_pk_add_f16 v87, v79, v82\nv_pk_add_f16 v84, v88, v84\nv_pk_add_f16 v85, v89, v85\nv_pk_add_f16 v86, v90, v86\n" \
  "v_pk_add_f16 v87, v91, v87\nds_write_b128 v13, v[84:87] offset:13520\nv_pk_add_f16 v88, v68, v72\nv_pk_add_f16 v89, v69, v73\n" \
  "v_pk_add_f16 v90, v70, v74\nv_pk_add_f16 v91, v71, v75\nv_pk_add_f16 v88, v84, v88\nv_pk_add_f16 v89, v85, v89\nv_pk_add_f16 v90, v86, v90\n" \
  "v_pk_add_f16 v91, v87, v91\nds_write_b128 v13, v[88:91] offset:15712\nds_read_b64_tr_b16 v[92:93], v14 offset:256\n" \
  "ds_read_b64_tr_b16 v[94:95], v14 offset:0\nds_read_b64_tr_b16 v[96:97], v14 offset:768\nds_read_b64_tr_b16 v[98:99], v14 offset:512\n" \
  "ds_read_b64_tr_b16 v[100:101], v10 offset:128\nds_read_b64_tr_b16 v[102:103], v10 offset:0\ns_waitcnt lgkmcnt(4)\n" \
  "v_mfma_f32_16x16x32_f16 v[76:79], v[52:55], v[92:95], 0\ns_setprio 1\nds_read_b64_tr_b16 v[104:105], v10 offset:384\n" \
  "ds_read_b64_tr_b16 v[106:107], v10 offset:256\nv_mfma_f32_16x16x32_f16 v[80:83], v[56:59], v[92:95], 0\n" \
  "v_mfma_f32_16x16x32_f16 v[84:87], v[60:63], v[92:95], 0\nv_mfma_f32_16x16x32_f16 v[88:91], v[64:67], v[92:95], 0\ns_waitcnt lgkmcnt(4)\n" \
  "v_mfma_f32_16x16x32_f16 v[80:83], v[52:55], v[96:99], v[80:83]\nds_read_b64_tr_b16 v[92:93], v10 offset:640\n" \
  "ds_read_b64_tr_b16 v[94:95], v10 offset:512\nv_mfma_f32_16x16x32_f16 v[84:87], v[56:59], v[96:99], v[84:87]\n" \
  "v_mfma_f32_16x16x32_f16 v[88:91], v[60:63], v[96:99], v[88:91]\ns_waitcnt lgkmcnt(4)\nv_mfma_f32_16x16x32_f16 v[68:71], v[32:35], v[100:103], 0\n" \
  "ds_read_b64_tr_b16 v[96:97], v10 offset:896\nds_read_b64_tr_b16 v[98:99], v10 offset:768\n" \
  "v_mfma_f32_16x16x32_f16 v[72:75], v[40:43], v[100:103], 0\nv_mfma_f32_16x16x32_f16 v[76:79], v[48:51], v[100:103], v[76:79]\ns_waitcnt lgkmcnt(4)\n" \
  "v_mfma_f32_16x16x32_f16 v[68:71], v[28:31], v[104:107], v[68:71]\ns_movk_i32 s88, 0x4000\n14:\nds_read_b32 v108, v13 offset:872\n" \
  "s_sub_u32 s88, s88, 1\ns_cbranch_scc1 15f\ns_waitcnt lgkmcnt(0)\nv_readfirstlane_b32 s87, v108\ns_cmp_lt_u32 s87, 2\ns_cbranch_scc1 14b\n15:\n" \
  "ds_read_b64_tr_b16 v[100:101], v14 offset:1280\nds_read_b64_tr_b16 v[102:103], v14 offset:1024\n" \
  "v_mfma_f32_16x16x32_f16 v[72:75], v[36:39], v[104:107], v[72:75]\nv_mfma_f32_16x16x32_f16 v[76:79], v[44:47], v[104:107], v[76:79]\n" \
  "v_mfma_f32_16x16x32_f16 v[72:75], v[32:35], v[92:95], v[72:75]\nds_read_b64_tr_b16 v[104:105], v14 offset:1792\n" \
  "ds_read_b64_tr_b16 v[106:107], v14 offset:1536\nv_mfma_f32_16x16x32_f16 v[76:79], v[40:43], v[92:95], v[76:79]\n" \
  "v_mfma_f32_16x16x32_f16 v[80:83], v[48:51], v[92:95], v[80:83]\nds_write2st64_b32 v12, v68, v69 offset0:0 offset1:1\n" \
  "ds_write2st64_b32 v12, v70, v71 offset0:2 offset1:3\nv_mfma_f32_16x16x32_f16 v[72:75], v[28:31], v[96:99], v[72:75]\n" \
  "ds_read_b64_tr_b16 v[92:93], v10 offset:1152\nds_read_b64_tr_b16 v[94:95], v10 offset:1024\n" \
  "v_mfma_f32_16x16x32_f16 v[76:79], v[36:39], v[96:99], v[76:79]\nv_mfma_f32_16x16x32_f16 v[80:83], v[44:47], v[96:99], v[80:83]\n" \
  "s_waitcnt lgkmcnt(6)\nv_mfma_f32_16x16x32_f16 v[84:87], v[52:55], v[100:103], v[84:87]\nds_read_b64_tr_b16 v[96:97], v10 offset:1408\n" \
  "ds_read_b64_tr_b16 v[98:99], v10 offset:1280\nv_mfma_f32_16x16x32_f16 v[88:91], v[56:59], v[100:103], v[88:91]\n" \
  "ds_write2st64_b32 v12, v72, v73 offset0:4 offset1:5\nds_write2st64_b32 v12, v74, v75 offset0:6 offset1:7\ns_waitcnt lgkmcnt(8)\n" \
  "v_mfma_f32_16x16x32_f16 v[88:91], v[52:55], v[104:107], v[88:91]\nds_read_b64_tr_b16 v[100:101], v10 offset:1664\n" \
  "ds_read_b64_tr_b16 v[102:103], v10 offset:1536\ns_waitcnt lgkmcnt(6)\nv_mfma_f32_16x16x32_f16 v[76:79], v[32:35], v[92:95], v[76:79]\n" \
  "ds_read_b64_tr_b16 v[104:105], v10 offset:1920\nds_read_b64_tr_b16 v[106:107], v10 offset:1792\n" \
  "v_mfma_f32_16x16x32_f16 v[80:83], v[40:43], v[92:95], v[80:83]\nv_mfma_f32_16x16x32_f16 v[84:87], v[48:51], v[92:95], v[84:87]\n" \
  "s_waitcnt lgkmcnt(6)\nv_mfma_f32_16x16x32_f16 v[76:79], v[28:31], v[96:99], v[76:79]\nds_read_b64_tr_b16 v[92:93], v10 offset:2176\n" \
  "ds_read_b64_tr_b16 v[94:95], v10 offset:2048\nv_mfma_f32_16x16x32_f16 v[80:83], v[36:39], v[96:99], v[80:83]\n" \
  "v_mfma_f32_16x16x32_f16 v[84:87], v[44:47], v[96:99], v[84:87]\ns_waitcnt lgkmcnt(4)\n" \
  "v_mfma_f32_16x16x32_f16 v[80:83], v[32:35], v[100:103], v[80:83]\nds_read_b64_tr_b16 v[96:97], v10 offset:2432\n" \
  "ds_read_b64_tr_b16 v[98:99], v10 offset:2304\nv_mfma_f32_16x16x32_f16 v[84:87], v[40:43], v[100:103], v[84:87]\n" \
  "v_mfma_f32_16x16x32_f16 v[88:91], v[48:51], v[100:103], v[88:91]\nds_write2st64_b32 v12, v76, v77 offset0:8 offset1:9\n" \
  "ds_write2st64_b32 v12, v78, v79 offset0:10 offset1:11\ns_waitcnt lgkmcnt(6)\nv_mfma_f32_16x16x32_f16 v[80:83], v[28:31], v[104:107], v[80:83]\n" \
  "ds_read_b64_tr_b16 v[100:101], v10 offset:2688\nds_read_b64_tr_b16 v[102:103], v10 offset:2560\n" \
  "v_mfma_f32_16x16x32_f16 v[84:87], v[36:39], v[104:107], v[84:87]\nv_mfma_f32_16x16x32_f16 v[88:91], v[44:47], v[104:107], v[88:91]\n" \
  "s_waitcnt lgkmcnt(6)\nv_mfma_f32_16x16x32_f16 v[84:87], v[32:35], v[92:95], v[84:87]\nds_read_b64_tr_b16 v[104:105], v10 offset:2944\n" \
  "ds_read_b64_tr_b16 v[106:107], v10 offset:2816\nv_mfma_f32_16x16x32_f16 v[88:91], v[40:43], v[92:95], v[88:91]\n" \
  "ds_write2st64_b32 v12, v80, v81 offset0:12 offset1:13\nds_write2st64_b32 v12, v82, v83 offset0:14 offset1:15\ns_waitcnt lgkmcnt(8)\n" \
  "v_mfma_f32_16x16x32_f16 v[84:87], v[28:31], v[96:99], v[84:87]\nv_mfma_f32_16x16x32_f16 v[88:91], v[36:39], v[96:99], v[88:91]\n" \
  "s_waitcnt lgkmcnt(4)\nv_mfma_f32_16x16x32_f16 v[88:91], v[32:35], v[100:103], v[88:91]\ns_waitcnt lgkmcnt(2)\n" \
  "v_mfma_f32_16x16x32_f16 v[88:91], v[28:31], v[104:107], v[88:91]\ns_setprio 0\ns_nop 1\nds_write2st64_b32 v12, v84, v85 offset0:16 offset1:17\n" \
  "ds_write2st64_b32 v12, v86, v87 offset0:18 offset1:19\ns_nop 2\nds_write2st64_b32 v12, v88, v89 offset0:20 offset1:21\n" \
  "ds_write2st64_b32 v12, v90, v91 offset0:22 offset1:23\ns_setpc_b64 s[90:91]\n"
#define CONV_CLOB "memory", "scc", "s87", "s88", "s90", "s91", "s92", "s93", "v28", "v29", "v30", "v31", "v32", "v33", "v34", "v35", "v36", "v37", "v38", "v39", "v40", "v41", "v42", "v43", "v44", "v45", "v46", "v47", "v48", "v49", "v50", "v51", "v52", "v53", "v54", "v55", "v56", "v57", "v58", "v59", "v60", "v61", "v62", "v63", "v64", "v65", "v66", "v67", "v68", "v69", "v70", "v71", "v72", "v73", "v74", "v75", "v76", "v77", "v78", "v79", "v80", "v81", "v82", "v83", "v84", "v85", "v86", "v87", "v88", "v89", "v90", "v91", "v92", "v93", "v94", "v95", "v96", "v97", "v98", "v99", "v100", "v101", "v102", "v103", "v104", "v105", "v106", "v107", "v108", "v109", "v110", "v111", "v112", "v113", "v114", "v115", "v116", "v117", "v118", "v119", "v120", "v121", "v122", "v123"
constexpr int C_F2 = 0, C_F1 = 352, C_PL = 880, PLP = 448, C_ZS = 2672, C_UL = 2992, C_G3 = 7088, C_G5 = 10288, C_G7 = 13488, C_G9 = 15680,
              C_YD = 17872, C_CY = 26064, C_YF = 26128, C_ES = 32272, CB = 32304, C_NZ = C_UL / 16;
constexpr int RP = 1216;
constexpr int LP_PART = 10240, LA = 10240, LW = 36864, LM = 73728;
constexpr int SMEM = LM + 2048;
struct COps { uint32_t a, b, c, d, e, f; };
__device__ __forceinline__ void conv_ops(uint32_t cb, int role, int lane, COps& e) {
  const int kq = lane >> 4, q4 = (lane >> 2) & 3, p4 = lane & 3, g0 = role ? 5 + 9 * role : 0;
  const bool far = role == 3;
  e.a = cb + (uint32_t)((far ? C_G3 + 128 : C_UL + 128 * g0 + 96 - 2048) + (q4 & 1) * (far ? C_G5 - C_G3 : 0) - 8 * (lane >> 2) + 8 * p4);
  e.b = cb + (uint32_t)((far ? C_F1 : C_PL) + 8 + p4 * (far ? 32 : PLP) + 8 * (q4 + kq));
  e.c = cb + (uint32_t)((far ? C_YF : C_YD + 256 * g0) + 4 * lane);
  e.d = cb + (uint32_t)((far ? 0 : C_CY + 16 * role) + lane * (far ? 16 : 0));
  e.e = cb + (uint32_t)(far ? C_G7 + 248 + (q4 & 1) * (C_G9 - C_G7) - 16 * (lane >> 2) + 8 * p4 : 16 * lane + 1024 * (3 - role));
  e.f = cb + (uint32_t)(far ? C_F2 + 4 + 4 * q4 + 16 * p4 + 8 * kq : C_F1 + 520);
}
__device__ __forceinline__ int pad_at(int r) {
  return C_G3 + ((r >> 1) & 3) * (C_G5 - C_G3) - ((r >= 6) ? 2 * C_G5 - C_G3 + C_G5 - C_G3 - C_G9 : 0) + 16 * (r & 1);
}
static_assert(C_G7 - C_G5 == C_G5 - C_G3, "");
template <int N> __device__ __forceinline__ void table_at(int g, int e, int& ds, int& dh) {
  const int hi = g * N >> 7;
  ds = ((hi < 4) ? C_PL + 32 + hi * PLP : C_F1 + 128 + (hi - 4) * (C_F2 + 64 - C_F1 - 128)) + 2 * ((g * N) & 127);
  dh = ((unsigned)e < 5u) ? C_PL + 288 + (e & 3) * PLP + ((e >= 3) ? 26 + 2 * e : 0) : ds + 2 * N - 2;
}
__device__ __forceinline__ void image_put(float* part, int lx, int ly2, const f32x4* acc) {
  asm volatile("" ::: "memory");
  part[lx + 447] = acc[1][0]; part[lx + 767] = acc[2][0];
  asm volatile("" ::: "memory");
  part[lx] = acc[0][0]; part[lx + 128] = acc[0][1]; part[lx + 256] = acc[0][2]; part[lx + 384] = acc[0][3];
  part[lx + 64] = acc[1][0]; part[lx + 192] = acc[1][0]; part[lx + 320] = acc[1][2];
  part[lx + 511] = acc[1][1]; part[lx + 575] = acc[1][3];
  part[ly2 + 639] = acc[2][1]; part[ly2 + 671] = acc[2][3];
}
__device__ __forceinline__ void skip_term(const u32x4 uw, float d32, f32x2* ud) {
#pragma unroll
  for (int e = 0; e < 4; ++e) {
    const uint32_t uu = uw[e];
    float lo, hi;
    asm("v_fma_mix_f32 %0, %2, %3, 0 op_sel_hi:[1,0,0]\n\tv_fma_mix_f32 %1, %2, %3, 0 op_sel:[1,0,0] op_sel_hi:[1,0,0]"
        : "=&v"(lo), "=&v"(hi) : "v"(uu), "v"(d32));
    const uint32_t pk = pack2(lo, hi);
    ud[e] = (f32x2){bflo(pk), bfhi(pk)};
  }
  asm volatile("" : "+v"(ud[0]), "+v"(ud[1]), "+v"(ud[2]), "+v"(ud[3]));
}
__device__ __forceinline__ u32x4 conv_out(const char* smem, uint32_t ry, uint32_t rf, uint32_t rc, float ys, const f32x2* ud) {
  const f32x4 a0 = *(const f32x4*)(smem + ry), a1 = *(const f32x4*)(smem + ry + 16), cy = *(const f32x4*)(smem + rc);
  const f32x4 f0 = *(const f32x4*)(smem + rf), f1 = *(const f32x4*)(smem + rf + 16);
  f32x4 y0 = a0 + f0;
  const f32x4 y1 = a1 + f1;
  y0[0] += cy[1]; y0[1] += cy[2]; y0[2] += cy[3];
  const uint32_t yb[4] = {pack2(y0[0], y0[1]), pack2(y0[2], y0[3]), pack2(y1[0], y1[1]), pack2(y1[2], y1[3])};
  u32x4 ov;
#pragma unroll
  for (int e = 0; e < 4; ++e) {
    const f32x2 r = __builtin_elementwise_fma((f32x2){bflo(yb[e]), bfhi(yb[e])}, (f32x2){ys, ys}, ud[e]);
    ov[e] = min(pack2(r[0], r[1]), 0xfffffffeu);
  }
  return ov;
}
#define CONV_RUN(role, e, k0, k1, k2, k3, k4, k5, ...) do { int role_ = (role); \
    asm volatile("s_call_b64 s[90:91], kf_conv" : "+{s89}"(role_), "+{v10}"(e.a), "+{v11}"(e.b), "+{v12}"(e.c), "+{v13}"(e.d), "+{v14}"(e.e), "+{v15}"(e.f), \
                 "+{v16}"(k0), "+{v17}"(k1), "+{v18}"(k2), "+{v19}"(k3), "+{v20}"(k4), "+{v21}"(k5) __VA_ARGS__ : : CONV_CLOB); } while (0)

template <class F>
__device__ __forceinline__ void ln_two(const uint32_t* v, int lane, float sc, uint32_t* c, F&& mid) {
  float s1a = 0.f, s2a = 0.f, s1b = 0.f, s2b = 0.f;
#pragma unroll
  for (int k = 0; k < 4; ++k) {
    s1a = dot2(v[k], 0x3f803f80u, s1a); s2a = dot2(v[k], v[k], s2a);
    s1b = dot2(v[4 + k], 0x3f803f80u, s1b); s2b = dot2(v[4 + k], v[4 + k], s2b);
  }
  f32x2 t[9];
#pragma unroll
  for (int k = 0; k < 9; ++k) t[k] = (f32x2){bflo(v[k]), bfhi(v[k])};
  const float rs = row_sum(fold16(dot2(v[8], 0x3f803f80u, fold32(s1a, s1b)), dot2(v[8], v[8], fold32(s2a, s2b))));
  const f32x2 mean = (f32x2){rdl(rs, 15), rdl(rs, 47)} * (1.f / D_), m2 = mean * mean;
  f32x2 var = (f32x2){rdl(rs, 31), rdl(rs, 63)} * (1.f / D_) - m2;
  asm volatile("" : "+v"(t[0]), "+v"(t[1]), "+v"(t[2]), "+v"(t[3]), "+v"(t[4]), "+v"(t[5]), "+v"(t[6]), "+v"(t[7]), "+v"(t[8]));
  if (__builtin_expect(!(m2[0] <= 16.f * var[0] && m2[1] <= 16.f * var[1]), 0)) {
    float q[3] = {0.f, 0.f, 0.f};
#pragma unroll
    for (int k = 0; k < 9; ++k) {
      const f32x2 a = t[k] - ((k < 4 || (k == 8 && lane < 32)) ? mean[0] : mean[1]);
      q[k >> 2] += a[0] * a[0] + a[1] * a[1];
    }
    float qa = q[0] + ((lane < 32) ? q[2] : 0.f), qb = q[1] + ((lane < 32) ? 0.f : q[2]);
    wave_sum2(qa, qb);
    var = (f32x2){qa, qb} * (1.f / D_);
  }
  mid();
  const uint32_t mp = pack2(mean[0], mean[1]), vp = pack2(var[0], var[1]);
  const f32x2 ve = (f32x2){bflo(vp), bfhi(vp)} + 1e-5f;
  const uint32_t e2 = pack2(ve[0], ve[1]);
  const uint32_t sp = pack2(__builtin_amdgcn_sqrtf(bflo(e2)), __builtin_amdgcn_sqrtf(bfhi(e2)));
  const f32x2 rc = (f32x2){__builtin_amdgcn_rcpf(bflo(sp)), __builtin_amdgcn_rcpf(bfhi(sp))} * sc;
  const float nm[3] = {bflo(mp), bfhi(mp), (lane < 32) ? bflo(mp) : bfhi(mp)}, rr[3] = {rc[0], rc[1], (lane < 32) ? rc[0] : rc[1]};
#pragma unroll
  for (int k = 0; k < 9; ++k) { t[k] -= nm[k >> 2]; c[k] = pack2(t[k][0], t[k][1]); }
#pragma unroll
  for (int k = 0; k < 9; ++k) t[k] = (f32x2){bflo(c[k]), bfhi(c[k])} * rr[k >> 2];
#pragma unroll
  for (int k = 0; k < 9; ++k) c[k] = pack2(t[k][0], t[k][1]);
}
__device__ __forceinline__ uint32_t med3u(uint32_t a, uint32_t b, uint32_t c) { return max(min(a, b), min(max(a, b), c)); }
__device__ __forceinline__ uint32_t oct_maxu(uint32_t v) {
  v = max(v, (uint32_t)DPP_I((int)v, 0xb1, 0xf));
  v = max(v, (uint32_t)DPP_I((int)v, 0x4e, 0xf));
  v = max(v, (uint32_t)DPP_I((int)v, 0x141, 0xf));
  return v;
}

#define KARGS const u16* __restrict__ x, const u16* __restrict__ Are, const u16* __restrict__ Aim, const u16* __restrict__ Bre, \
    const u16* __restrict__ Bim, const u16* __restrict__ Cre, const u16* __restrict__ Cim, const u16* __restrict__ logdt, \
    const u16* __restrict__ Dskip, char* __restrict__ ws, const u16* __restrict__ wr, const u16* __restrict__ wg, \
    const u16* __restrict__ wu, const u16* __restrict__ wd, u16* __restrict__ out
#define KPASS x, Are, Aim, Bre, Bim, Cre, Cim, logdt, Dskip, ws, wr, wg, wu, wd, out
template <int ROLE>
__device__ __forceinline__ void role_body(char* smem, KARGS) {
  const int tid = threadIdx.x, lane = tid & 63;
  const int w = __builtin_amdgcn_readfirstlane(tid >> 6), wl = w & 3;
  const int b = blockIdx.x & 255, bx = b & 7, bj = b >> 3;
  constexpr bool isl = ROLE == 1 || ROLE == 2;
  const int tg = 32 * bx + bj;
  const int dP = 512 + 8 * ((bx + XOFF) & 7) + ((bj >> 1) & 7), pq = bj & 1;
  const int d = isl ? dP : 2 * tg + (wl >> 1);
  u32x4 xa, xc;
  uint32_t ep, r0, r1, r2, r3, r4, r5, r6, r7;
  if constexpr (ROLE == 1) {
    const uint32_t xo = (uint32_t)((8 * tg + 2 * w) * (D_ * 2));
    asm volatile("global_load_dwordx4 %0, %4, %7\n\tglobal_load_dwordx4 %1, %4, %7 offset:1152\n\t"
                 "global_load_dword %3, %5, %7 offset:1024\n\tglobal_load_dword %2, %6, %8 sc1"
                 : "=&v"(xa), "=&v"(xc), "=&v"(ep), "=&v"(r0)
                 : "v"(xo + 16 * lane), "v"(xo + 4 * lane + 32 * (lane & 32)), "v"(O_EP), "s"(x), "s"(ws));
  } else if constexpr (ROLE == 0) {
    const uint32_t po = (uint32_t)(d * N_ + 64 * (wl & 1) + lane) * 2u;
    asm volatile("global_load_ushort %0, %9, %11\n\tglobal_load_ushort %1, %8, %12\n\tglobal_load_ushort %2, %8, %13\n\t"
                 "global_load_ushort %3, %8, %14\n\tglobal_load_ushort %4, %8, %15\n\tglobal_load_ushort %5, %8, %16\n\t"
                 "global_load_ushort %6, %8, %17\n\tglobal_load_ushort %7, %10, %18"
                 : "=&v"(r0), "=&v"(r1), "=&v"(r2), "=&v"(r3), "=&v"(r4), "=&v"(r5), "=&v"(r6), "=&v"(r7)
                 : "v"(po), "v"(2u * d), "v"(4u * tg + 2u * (lane & 1)), "s"(logdt), "s"(Are), "s"(Aim), "s"(Bre), "s"(Bim), "s"(Cre), "s"(Cim), "s"(Dskip));
  } else if constexpr (ROLE == 2) {
    const uint32_t po = (uint32_t)(d * N_ + 64 * pq + 16 * wl + (lane & 15)) * 2u;
    asm volatile("s_cmp_lt_u32 %22, 16\n\ts_cbranch_scc0 1f\n\t"
                 "global_load_ushort %1, %10, %13\n\tglobal_load_ushort %2, %9, %14\n\tglobal_load_ushort %3, %9, %15\n\t"
                 "global_load_ushort %4, %9, %16\n\tglobal_load_ushort %5, %9, %17\n\tglobal_load_ushort %6, %9, %18\n\t"
                 "global_load_ushort %7, %9, %19\n1:\n\tglobal_load_ushort %8, %11, %20\n\tglobal_load_dword %0, %12, %21 sc1"
                 : "=&v"(ep), "=&v"(r0), "=&v"(r1), "=&v"(r2), "=&v"(r3), "=&v"(r4), "=&v"(r5), "=&v"(r6), "=&v"(r7)
                 : "v"(po), "v"(2u * d), "v"(2u * (512 - 24 + 8 * bx + bj)), "v"(O_EP), "s"(logdt), "s"(Are), "s"(Aim),
                   "s"(Bre), "s"(Bim), "s"(Cre), "s"(Cim), "s"(Dskip), "s"(ws), "s"(bj) : "scc");
  }
#define K_WAIT(n) asm volatile("s_waitcnt vmcnt(" #n ")\n\tv_lshlrev_b32 %0, 16, %8\n\tv_lshlrev_b32 %1, 16, %9\n\tv_lshlrev_b32 %2, 16, %10\n\t" \
    "v_lshlrev_b32 %3, 16, %11\n\tv_lshlrev_b32 %4, 16, %12\n\tv_lshlrev_b32 %5, 16, %13\n\tv_lshlrev_b32 %6, 16, %14\n\tv_lshlrev_b32 %7, 16, %15" \
    : "=&v"(kp.ldt), "=&v"(kp.are), "=&v"(kp.aim), "=&v"(kp.bre), "=&v"(kp.bim), "=&v"(kp.cre), "=&v"(kp.cim), "=&v"(dsk) \
    : "v"(r0), "v"(r1), "v"(r2), "v"(r3), "v"(r4), "v"(r5), "v"(r6), "v"(r7))
  TSD;
  TS(0, 0);
  const uint32_t lb0 = (uint32_t)(uintptr_t)(__attribute__((address_space(3))) char*)smem;
  const int i = lane & 15, kq = lane >> 4;
  const u32x4 ones = {0xffffffffu, 0xffffffffu, 0xffffffffu, 0xffffffffu};
  if constexpr (!isl) {
    if (ROLE == 0) __builtin_amdgcn_s_setprio(KPRIO);
    constexpr int wh = ROLE == 3;
    char* sm = smem + (wl >> 1) * CB;
    float* prm = (float*)(sm + C_UL) + (wl & 1) * 1152;
    float* part = (float*)(sm + C_YD) + (2 * (wl & 1) + wh) * CP;
    float lx0 = (float)kbase(i), ly = (float)(8 * i), dsk, ox;
    int pp = (2 * kq + 16 * wh) * PS;
    int o1 = pp + 8 + (i & 1) * (4 + 2 * (i & 2)), o2 = pp + 20 + (i & 1) * (4 + (i & 4));
    int lx = 2 * i + (kq & 1) + 32 * (kq >> 1), ly2 = i + 16 * (kq >> 1) + 64 * (kq & 1);
    asm volatile("" : "+v"(lx0), "+v"(ly), "+v"(o1), "+v"(o2), "+v"(pp), "+v"(lx), "+v"(ly2));
    const int role = (w + wh) & 3;
    COps e;
    u32x4 ux;
    uint32_t vo, rf, rc, ru, T = 0u;
    int gm, ds, dh;
    auto late_terms = [&] {
      const int te = tid >> 1;
      conv_ops(lb0 + (uint32_t)(wh * CB), role, lane, e);
      vo = O_V + (uint32_t)tg * VP + 16u * tid;
      rf = (uint32_t)((tid & 1) * CB + ((te >= 64) ? C_YF + 32 * (te - 64) : C_ZS));
      rc = (uint32_t)((tid & 1) * CB + ((te == 112) ? C_CY : (te == 184) ? C_CY + 16 : C_ZS));
      ru = (uint32_t)((tid & 1) * CB + C_UL + 16 * te);
      gm = (lane < 48) ? 96 * (wl & 1) + 48 * wh + lane : ((unsigned)(lane - 51) < 2u) ? 159 : 127;
      table_at<4>(gm, lane - 48, ds, dh);
      gm = C_YD + 16 * gm;
      asm volatile("" : "+v"(vo), "+v"(e.a), "+v"(e.b), "+v"(e.c), "+v"(e.d), "+v"(e.e), "+v"(e.f), "+v"(rf), "+v"(rc), "+v"(ru), "+v"(gm), "+v"(ds), "+v"(dh));
    };
    float sa, sb, ys;
    auto scales = [&] {
      const u32x2 ev = *(const u32x2*)(sm + C_ES), ew = *(const u32x2*)(smem + (tid & 1) * CB + C_ES);
      const int emax = min(max(max((int)ev[0], (int)ev[1]), -110), 115);
      sa = __builtin_amdgcn_ldexpf(1.f, (int)ev[0] - 22 - emax); sb = __builtin_amdgcn_ldexpf(1.f, (int)ev[1] - 22 - emax);
      ys = __int_as_float((127 - 7 + 5 + min(max(max((int)ew[0], (int)ew[1]), -110), 115)) << 23);
      asm volatile("" : "+v"(sa), "+v"(sb), "+v"(ys));
    };
    auto table = [&] {
      const float* pr = (const float*)(sm + gm);
      const f32x4 k0 = (*(const f32x4*)pr + *(const f32x4*)(pr + CP)) * sa + (*(const f32x4*)(pr + 2 * CP) + *(const f32x4*)(pr + 3 * CP)) * sb;
      const u32x2 kh = {packh(k0.xy), packh(k0.zw)};
      *(u32x2*)(sm + ds) = kh;
      *(u16*)(sm + dh) = (u16)(kh[1] >> 16);
    };
    auto steps = [&](auto&& mid) {
      f32x4 acc[3];
#pragma unroll
      for (int t = 0; t < 3; ++t) acc[t] = (f32x4){0.f, 0.f, 0.f, 0.f};
#pragma unroll
      for (int g = 0; g < 2; ++g) {
        u32x4 xh[3], yh;
        kgen_pair2(prm + pp + 8 * g * PS, prm + o1 + 8 * g * PS, prm + o2 + 8 * g * PS, lx0, ly, ox, xh, yh, 0);
        if (g == 1) mid();
        kgen_pair2(prm + pp + (8 * g + 1) * PS, prm + o1 + (8 * g + 1) * PS, prm + o2 + (8 * g + 1) * PS, lx0, ly, ox, xh, yh, 2);
        kgen_mma(xh, yh, acc);
      }
      image_put(part, lx, ly2, acc);
    };
    if constexpr (ROLE == 0) {
      int ps = (lane >> 1) * PS + (lane & 1);
      asm volatile("" : "+v"(ps));
      late_terms();
      KPar kp;
      int eb;
      K_WAIT(0);
      TS(0, 1);
      {
        const f32x4 pb = kgen_base<64>(kp, ox, eb);
        float* pw = prm + ps;
        pw[0] = pb[0]; pw[2] = pb[1]; pw[4] = pb[2]; pw[6] = pb[3];
        const float kc[6] = {256.f, 511.f, 765.f, 1792.f, 512.f, 1020.f};
#pragma unroll
        for (int j = 0; j < 6; ++j) {
          float er, ei;
          cexp2(pb[0], pb[1], kc[j], er, ei);
          pw[8 + 4 * j] = er; pw[10 + 4 * j] = ei;
        }
      }
      ((int*)(sm + C_ES))[wl & 1] = eb;
      ((float*)(sm + C_ES))[2 + (wl & 1)] = ox;
      TS(0, 8);
      BAR();
      scales();
      steps([] {});
      TS(0, 2);
      BAR();
      TS(0, 9);
      table();
    } else {
      const int tl = tid - 256;
      if (tl < C_NZ) {
        *(u32x4*)(smem + 16 * tl) = (u32x4){0u, 0u, 0u, 0u};
        *(u32x4*)(smem + CB + 16 * tl) = (u32x4){0u, 0u, 0u, 0u};
      }
      late_terms();
      uint32_t uo = u_at(tl >> 1, 2 * tg + (tid & 1)), go = O_SU + (uint32_t)(256 * bx + 32 * wl + (lane & 31) + 4 * (lane & 32)) * 128u;
      asm volatile("" : "+v"(uo), "+v"(go));
      asm volatile("global_load_ushort %0, %2, %4\n\tglobal_load_dword %1, %3, %5 sc1" : "=&v"(r7), "=&v"(ep) : "v"(4u * tg + 2u * (lane & 1)), "v"(O_EP), "s"(Dskip), "s"(ws));
      if (w == 7) {
#pragma unroll
        for (int k = 0; k < 8; ++k)
          asm volatile("global_store_dwordx4 %0, %1, %2 offset:%3 sc1\n\ts_nop 1" : : "v"(vo - 3072u), "v"(ones), "s"(ws), "n"(1024 * k - 4096) : "memory");
      }
      TS(0, 1);
      __builtin_amdgcn_s_setprio(UPRIO);
      BAR();
      ox = ((const float*)(sm + C_ES))[2 + (wl & 1)];
      u32x2 pv;
      steps([&] {
        asm volatile("s_waitcnt vmcnt(0)" : "+v"(r7), "+v"(ep));
        T = ((uint32_t)__builtin_amdgcn_readfirstlane(ep) + 1u) * 0x9E3779B1u;
        asm volatile("s_cmp_lg_u32 %5, 7\n\ts_cbranch_scc1 1f\n\ts_mov_b64 exec, 0xff\n\tglobal_store_dwordx4 %2, %3, %4 sc1\n\ts_mov_b64 exec, -1\n"
                     "1:\n\tglobal_load_dwordx2 %0, %1, %4 sc1"
                     : "=&v"(pv) : "v"(go), "v"(O_KEY + 128u * b + 16u * lane), "v"((u32x4){~T, ~T, ~T, ~T}), "s"(ws), "s"(w) : "scc");
      });
      TS(0, 2);
      asm volatile("s_waitcnt vmcnt(0)" : "+v"(pv));
      dsk = bflo(r7);
      if (__any(pv[0] != T)) pv[1] = gate(ws, go, T);
      if (__any((int)((uint32_t)__builtin_amdgcn_s_memrealtime() - pv[1]) < UD)) until(latest(pv[1]) + UD);
      TS(0, 7);
      u32x4 uw;
      asm volatile("global_load_dwordx4 %0, %2, %4 sc1\n\tglobal_load_dwordx4 %1, %3, %4 sc1"
                   : "=&v"(uw), "=&v"(ux) : "v"(uo), "v"(uo + 64 * UP * 32), "s"(ws) : "memory");
      if (w == 7) SIGNAL(O_SV + 128u * tg, 320 * 128, (bj >= 24) ? VD : VD2);
      scales();
      u32x4 ut = {utag(T, 0), utag(T, 1), utag(T, 2), utag(T, 3)};
      int pz = (lane & 8) / 8 * CB + pad_at(lane & 7);
      asm volatile("" : "+v"(ut), "+v"(pz));
      BAR();
      table();
      asm volatile("s_waitcnt vmcnt(1)\n\ts_cmp_eq_u32 %2, 7\n\ts_cbranch_scc1 1f\n\ts_waitcnt vmcnt(0)\n1:" : "+v"(uw), "+v"(ux) : "s"(w) : "scc");
      TS(0, 9);
      uw ^= ut; ux ^= ut;
      if (__builtin_expect(__any(((uw[0] | uw[1] | uw[2] | uw[3] | ux[0] | ux[1] | ux[2] | ux[3]) & UMASK) != 0u), 0)) {
#pragma unroll 1
        for (int spin = 0; spin < SPIN_CAP; ++spin) {
          const bool na = ((uw[0] | uw[1] | uw[2] | uw[3]) & UMASK) != 0u, nb = ((ux[0] | ux[1] | ux[2] | ux[3]) & UMASK) != 0u;
          if (!__any(na || nb)) break;
          TSC(19);
          SLEEP(USLEEP);
          if (na) { LD16W(uw, uo); uw ^= ut; }
          if (nb) { LD16W(ux, uo + 64 * UP * 32); ux ^= ut; }
        }
      }
      *(u32x4*)(smem + ru - 2048) = uw;
      *(u32x4*)(smem + ru) = ux;
      if (tl < 16) *(u32x4*)(smem + pz) = (u32x4){0u, 0u, 0u, 0u};
    }
    TS(0, 3);
    __builtin_amdgcn_s_setprio(0);
    BAR();
    TS(0, 4);
    dsk *= 32.f;
    f32x2 ud[4];
    uint32_t ry = 2u * ru - (uint32_t)((vo & 16u) ? CB : 0) + (uint32_t)(C_YD - 2 * C_UL);
    if (ROLE == 3) {
      skip_term(ux, dsk, ud);
      CONV_RUN((role == 1 || role == 2) ? role + 4 : role, e, vo, rf, rc, ry, ys, dsk, , "+{v[2:3]}"(ud[0]), "+{v[4:5]}"(ud[1]), "+{v[6:7]}"(ud[2]), "+{v[8:9]}"(ud[3]));
      TS(0, 5);
    } else {
      CONV_RUN(role, e, vo, rf, rc, ru, ys, dsk, , "+{v22}"(ry));
      TS(0, 5);
      skip_term(*(const u32x4*)(smem + ru), dsk, ud);
    }
    BAR();
    TS(0, 10);
    ST16(vo, conv_out(smem, ry, rf, rc, ys, ud));
    TS(0, 6);
    TSE();
  } else {
  uint32_t* cand = (uint32_t*)(smem + LM);
  uint32_t* sel = cand + 128;
  uint32_t* kl = sel + 48;
  uint32_t* cnt = kl + 8;
  const uint32_t ca = lb0 + LM + 4 * (128 + 48 + 8);
  u16* hm = (u16*)(cnt + 8);
  uint32_t T;
  const bool ffn = bj < 18, isx = bj >= 24;
  const int fb = 18 * bx + bj;
  u32x4 bw[18];
  int tid2 = threadIdx.x;
  auto gu_dma = [&](int h) {
    const char* gs = (const char*)((w < 2) ? wg : wu) + (size_t)((288 * h + 144 * (w & 1)) * (F_ * 2) + fb * 64);
    const uint32_t gv = (uint32_t)((lane >> 2) * (F_ * 2) + 16 * ((lane & 3) ^ (lane >> 5) ^ (w & 2)));
#pragma unroll 1
    for (int k = 0; k < 9; ++k) {
      dma16(gs + (size_t)(16 * k) * (F_ * 2), gv, lb0 + LW * (1 - h) + 9216 * w + 1024 * k);
      asm volatile("s_waitcnt vmcnt(%0)" : : "n"(WDEPTH) : "memory");
    }
  };
  auto gu_read = [&](int h) {
    const uint32_t ra = lb0 + LW * (1 - h) + (uint32_t)(((lane >> 1) & 1) * 18432 + (8 * kq + ((lane >> 2) & 3)) * 64 + ((w ^ (kq & 1) ^ (lane & 2)) * 16) + 8 * (lane & 1));
#pragma unroll
    for (int s = 0; s < 9; ++s) {
      const u32x2 h0 = trd(ra + 2048 * s), h1 = trd(ra + 2048 * s + 256);
      bw[9 * h + s] = (u32x4){h0[0], h0[1], h1[0], h1[1]};
    }
#pragma unroll
    for (int s = 0; s < 9; ++s) asm volatile("" : "+v"(bw[9 * h + s]));
  };
  auto v_gate = [&](uint32_t tu, int t) {
    until(tu + UD + PD);
    until(gate2(ws, O_SV + (uint32_t)(320 * bx + t) * 128u, 0u, O_SV + (uint32_t)(320 * bx + 256 + (t >> 2)) * 128u, T));
  };
  uint32_t wv[5], xv[9];
  auto rows_xw = [&] {
    u32x4 wa, xr, xs;
    const uint32_t xo = (uint32_t)((8 * tg + 2 * wl) * (D_ * 2));
    asm volatile("global_load_dwordx4 %0, %5, %9\n\tglobal_load_dword %1, %6, %9 offset:1024\n\t"
                 "global_load_dwordx4 %2, %7, %10\n\tglobal_load_dwordx4 %3, %7, %10 offset:1152\n\tglobal_load_dword %4, %8, %10 offset:1024\n\ts_waitcnt vmcnt(0)"
                 : "=&v"(wa), "=&v"(wv[4]), "=&v"(xr), "=&v"(xs), "=&v"(xv[8])
                 : "v"(16u * lane), "v"(4u * (lane & 31)), "v"(xo + 16u * lane), "v"(xo + 4u * lane + 32u * (lane & 32)), "s"(wr), "s"(x) : "memory");
#pragma unroll
    for (int k = 0; k < 4; ++k) { wv[k] = wa[k]; xv[k] = xr[k]; xv[4 + k] = xs[k]; }
  };
  if constexpr (ROLE == 2) {
    const int tl = tid - 256;
    if (isx) __builtin_amdgcn_s_setprio(2);
    if (isx) ST16(v_at(512 - 24 + 8 * bx + bj, tl), ones);
    BAR();
    char* sm = smem + LW;
    uint32_t go, eo, uo, vo, rf, rc, ru;
    int ds, dh;
    COps e;
    if (isx) {
      const int dX = 512 - 24 + 8 * bx + bj;
      if (tl < C_NZ + 8) *(u32x4*)(sm + ((tl < C_NZ) ? 16 * tl : pad_at(tl - C_NZ))) = (u32x4){0u, 0u, 0u, 0u};
      const int qd = (tl < 192) ? tl : ((unsigned)(tl - 195) < 2u) ? 159 : 127;
      go = O_GQ + (uint32_t)((dX - 512) * (2 * GQP) + 4 * qd) * 4u; eo = O_GQ + (uint32_t)((dX - 512) * (2 * GQP) + (lane & 1) * GQP + 768) * 4u;
      uo = u_at(tl, dX);
      table_at<4>(qd, tl - 192, ds, dh);
      conv_ops(lb0 + LW, (wl + XROT) & 3, lane, e);
      vo = v_at(dX, tl); rf = (tl >= 64) ? C_YF + 32 * (tl - 64) : C_ZS; rc = (tl == 112) ? C_CY : (tl == 184) ? C_CY + 16 : C_ZS; ru = 16 * tl;
    }
    asm volatile("" : "+v"(go), "+v"(eo), "+v"(uo), "+v"(vo), "+v"(rf), "+v"(rc), "+v"(ru), "+v"(ds), "+v"(dh), "+v"(e.a), "+v"(e.b), "+v"(e.c), "+v"(e.d), "+v"(e.e), "+v"(e.f));
    KPar kp;
    float dsk;
    K_WAIT(0);
    asm volatile("" : "+v"(ep) : : "memory");
    T = ((uint32_t)__builtin_amdgcn_readfirstlane(ep) + 1u) * 0x9E3779B1u;
    TS(0, 1);
    if (QWAIT) gwait(ca + 12, QWAIT);
    __builtin_amdgcn_s_setprio(QPRIO);
    const u32x4 gt = {T & 0xffu, (T >> 8) & 0xffu, (T >> 16) & 0xffu, T >> 24};
    if (bj < 16) {
      float* part = (float*)(smem + LP_PART);
      int* esc = (int*)(smem + LP_PART + 4 * CP * 4);
      float* prm = part + wl * CP;
      float ox;
      int eb;
      const f32x4 pb = kgen_base<16>(kp, ox, eb);
      float* pt = prm + (i >> 1) * PS + (i & 1);
      float er, ei;
      pt[0] = pb[0]; pt[2] = pb[1]; pt[4] = pb[2]; pt[6] = pb[3];
      cexp2(pb[0], pb[1], kconst(kq), er, ei);
      pt[8 + 4 * kq] = er; pt[10 + 4 * kq] = ei;
      cexp2(pb[0], pb[1], (kq & 1) ? 1020.f : 512.f, er, ei);
      pt[24 + 4 * (kq & 1)] = er; pt[26 + 4 * (kq & 1)] = ei;
      esc[wl] = eb;
      if (w == 7) {
        const uint32_t t_ = (uint32_t)__builtin_amdgcn_s_memrealtime() + GD;
        if (lane < 8) ST16(O_SG + (uint32_t)((dP - 512) * 2 + pq) * 128u + 16u * lane, ((u32x4){T, t_, T, t_}));
      }
      f32x4 acc[3];
      u32x4 xh[3], yh;
#pragma unroll
      for (int t = 0; t < 3; ++t) acc[t] = (f32x4){0.f, 0.f, 0.f, 0.f};
      const float* pp = prm + 2 * kq * PS;
      const int o1 = 8 + (i & 1) * (4 + 2 * (i & 2)), o2 = 20 + (i & 1) * (4 + (i & 4));
      wave_lds_sync();
      kgen_pair2(pp, pp + o1, pp + o2, (float)kbase(i), (float)(8 * i), ox, xh, yh, 0);
      kgen_pair2(pp + PS, pp + PS + o1, pp + PS + o2, (float)kbase(i), (float)(8 * i), ox, xh, yh, 2);
      kgen_mma(xh, yh, acc);
      image_put(prm, 2 * i + (kq & 1) + 32 * (kq >> 1), i + 16 * (kq >> 1) + 64 * (kq & 1), acc);
      TS(0, 2);
      gsync(ca + 8, 4);
      const uint32_t gs = O_GQ + (uint32_t)(((dP - 512) * 2 + pq) * GQP + 4 * tl) * 4u;
      const u32x4 ev = *(const u32x4*)esc;
      const int em = max(max((int)ev[0], (int)ev[1]), max((int)ev[2], (int)ev[3]));
      if (wl < 3) {
        const f32x4 kv = *(const f32x4*)&part[4 * tl] * __builtin_amdgcn_ldexpf(1.f, (int)ev[0] - em) + *(const f32x4*)&part[CP + 4 * tl] * __builtin_amdgcn_ldexpf(1.f, (int)ev[1] - em) +
                         *(const f32x4*)&part[2 * CP + 4 * tl] * __builtin_amdgcn_ldexpf(1.f, (int)ev[2] - em) + *(const f32x4*)&part[3 * CP + 4 * tl] * __builtin_amdgcn_ldexpf(1.f, (int)ev[3] - em);
        ST16(gs, (__builtin_bit_cast(u32x4, kv) & ~0xffu) | gt);
      } else if (lane < 8) ST16(gs, ((uint32_t)em << 8) | gt);
    }
    TS(0, 3);
    __builtin_amdgcn_s_setprio(PPRIO);
    rows_xw();
    if (isx) {
      const u32x4 ut = {utag(T, 0), utag(T, 1), utag(T, 2), utag(T, 3)};
      dsk *= 32.f;
      gsync(ca + 8, 4);
      u32x4 p0, p1, ev, uw;
      gwait(ca + 12, 4);
      SLEEP(XSLEEP);
      TS(0, 2);
      until(gate2(ws, O_SU + (uint32_t)(256 * bx + tl) * 128u, UD, O_SG + (uint32_t)((8 * bx + bj - 24) * 2 + (lane & 1)) * 128u, T));
      TS(0, 13);
      asm volatile("global_load_dwordx4 %0, %4, %7 sc1\n\tglobal_load_dwordx4 %1, %4, %7 offset:3200 sc1\n\tglobal_load_dwordx4 %2, %5, %7 sc1\n\t"
                   "global_load_dwordx4 %3, %6, %7 sc1\n\ts_waitcnt vmcnt(0)"
                   : "=&v"(p0), "=&v"(p1), "=&v"(ev), "=&v"(uw) : "v"(go), "v"(eo), "v"(uo), "s"(ws) : "memory");
#pragma unroll 1
      for (int spin = 0; spin < SPIN_CAP; ++spin) {
        uw ^= ut;
        if (!__any(((uw[0] | uw[1] | uw[2] | uw[3]) & UMASK) != 0u)) break;
        TSC(19);
        SLEEP(USLEEP);
        LD16W(uw, uo);
      }
      *(u32x4*)(sm + C_UL + 16 * tl) = uw;
#pragma unroll 1
      for (int spin = 0; spin < SPIN_CAP; ++spin) {
        p0 ^= gt; p1 ^= gt; ev ^= gt;
        const u32x4 bad = p0 | p1 | ev;
        if (!__any(((bad[0] | bad[1] | bad[2] | bad[3]) & 0xffu) != 0u)) break;
        TSC(18);
        __builtin_amdgcn_s_sleep(3);
        asm volatile("global_load_dwordx4 %0, %3, %5 sc1\n\tglobal_load_dwordx4 %1, %3, %5 offset:3200 sc1\n\tglobal_load_dwordx4 %2, %4, %5 sc1\n\ts_waitcnt vmcnt(0)"
                     : "=&v"(p0), "=&v"(p1), "=&v"(ev) : "v"(go), "v"(eo), "s"(ws) : "memory");
      }
      TS(0, 4);
      const int e0 = __builtin_amdgcn_readlane((int)ev[0], 0) >> 8, e1 = __builtin_amdgcn_readlane((int)ev[0], 1) >> 8;
      const int emax = __builtin_amdgcn_readfirstlane(min(max(max(e0, e1), -110), 115));
      const float s0 = __uint_as_float((uint32_t)min(max(105 + e0 - emax, 0), 254) << 23), s1 = __uint_as_float((uint32_t)min(max(105 + e1 - emax, 0), 254) << 23);
      const f32x4 kv = __builtin_bit_cast(f32x4, p0) * s0 + __builtin_bit_cast(f32x4, p1) * s1;
      const u32x2 kh = {packh(kv.xy), packh(kv.zw)};
      *(u32x2*)(sm + ds) = kh;
      *(u16*)(sm + dh) = (u16)(kh[1] >> 16);
      BAR();
      TS(0, 5);
      CONV_RUN((wl + XROT) & 3, e, vo, rf, rc, ru, tid2, dsk, , "+{v22}"(xv[0]), "+{v23}"(xv[1]), "+{v24}"(xv[2]), "+{v25}"(xv[3]), "+{v26}"(xv[4]),
               "+{v27}"(xv[5]), "+{v2}"(xv[6]), "+{v3}"(xv[7]), "+{v4}"(xv[8]), "+{v5}"(wv[0]), "+{v6}"(wv[1]), "+{v7}"(wv[2]), "+{v8}"(wv[3]), "+{v9}"(wv[4]));
      f32x2 ud[4];
      skip_term(*(const u32x4*)(sm + C_UL + ru), dsk, ud);
      gsync(ca + 8, 8);
      ST16(vo, conv_out(sm, C_YD + 2 * ru, rf, rc, __int_as_float((127 - 7 + 5 + emax) << 23), ud));
      TS(0, 6);
    }
    asm volatile("" : "+v"(tid2));
    if (!isx) BAR();
    const uint32_t tp = (uint32_t)__builtin_amdgcn_s_memrealtime() - UD - WD;
    if (!isx && !ffn) {
      SLEEP(RSLEEP);
#pragma unroll
      for (int k = 0; k < 7; ++k) ST16(O_SL + (uint32_t)((6 * bx + bj - 18) * 1792 + 256 * k - 256 + tid2) * 16u, ones);
      DRAIN();
    }
    if (!isx) v_gate(tp, tid2 - 256);
  } else {
    __builtin_amdgcn_s_setprio(3);
    uint32_t uo = u_at(tg, 64 * w + lane);
    uint32_t ta = lb0 + (uint32_t)(((lane >> 2) & 3) * RP + (64 * w + 16 * kq + 4 * (lane & 3)) * 2);
    const int tn = bj * TW + min(tid, TW ? TW - 1 : 0);
    const char* wb = (const char*)wd + (uint32_t)(bx * 5184 + tn) * 128u;
    if (tid < 20) ((uint32_t*)(hm + 320))[tid] = 0u;
    if (tid < 4) cnt[tid] = 0u;
    uint32_t la = (uint32_t)(2 * w * RP + 16 * lane), lt = (uint32_t)((2 * w + (lane >> 5)) * RP + 1024 + 4 * (lane & 31));
    uint32_t sg = O_SU + (uint32_t)(256 * (lane >> 3) + tg) * 128u + 16u * (lane & 7);
    asm volatile("" : "+v"(uo), "+v"(ta), "+v"(wb), "+v"(la), "+v"(lt), "+v"(sg));
    uint32_t sink = 0;
    BAR();
    asm volatile("s_waitcnt vmcnt(1)" : "+v"(xa), "+v"(xc), "+v"(r0));
    if (TW && TPOS == 0) asm volatile("global_load_dword %0, %1, off" : "+v"(sink) : "v"(wb));
    TS(0, 1);
    {
      const uint32_t v[9] = {xa[0], xa[1], xa[2], xa[3], xc[0], xc[1], xc[2], xc[3], r0};
      uint32_t c[9];
      ln_two(v, lane, 0.03125f, c, [&] {
        asm volatile("s_waitcnt vmcnt(%1)" : "+v"(ep) : "n"((TW && TPOS == 0) ? 1 : 0));
        T = ((uint32_t)__builtin_amdgcn_readfirstlane(ep) + 1u) * 0x9E3779B1u;
        if (w == 3) { const uint32_t now = (uint32_t)__builtin_amdgcn_s_memrealtime() + SD; ST16(sg, ((u32x4){T, now, T, now})); }
      });
      const uint32_t tj = ((T >> (3 * w)) & 0x00070007u) | (((T >> (12 + w)) & 0x00010001u) << 14);
      uint32_t tw[3] = {(tj & 0xffffu) * 0x10001u, (tj >> 16) * 0x10001u, 0u};
      tw[2] = (lane < 32) ? tw[0] : tw[1];
      asm volatile("" : "+v"(tw[0]), "+v"(tw[1]), "+v"(tw[2]));
#pragma unroll
      for (int k = 0; k < 9; ++k) c[k] = (packh((f32x2){bflo(c[k]), bfhi(c[k])}) & ~UMASK) | tw[k >> 2];
      *(u32x4*)(smem + la) = (u32x4){c[0], c[1], c[2], c[3]};
      *(u32x4*)(smem + la + RP) = (u32x4){c[4], c[5], c[6], c[7]};
      *(uint32_t*)(smem + lt) = c[8];
    }
    gsync0(ca + 12, 4);
    {
      const u32x2 h0 = trd(ta), h1 = trd(ta + 4 * RP), h2 = trd(ta + 512), h3 = trd(ta + 512 + 4 * RP);
      ST16(uo, ((u32x4){h0[0], h0[1], h1[0], h1[1]}));
      ST16(uo + 64 * 128, ((u32x4){h2[0], h2[1], h3[0], h3[1]}));
    }
    if (w == 0) {
      const u32x2 h0 = trd(ta + 1024), h1 = trd(ta + 1024 + 4 * RP);
      ST16(uo + 128 * 128, ((u32x4){h0[0], h0[1], h1[0], h1[1]}));
    }
    if (TW && TPOS == 1) asm volatile("global_load_dword %0, %1, off" : "+v"(sink) : "v"(wb));
    TS(0, 2);
    asm volatile("s_waitcnt vmcnt(0)" : "+v"(sink) : : "memory");
    TS(0, 13);
    if (w == 0) {
#pragma unroll
      for (int k = 0; k < 2; ++k) ST16(O_XC + (uint32_t)(tg * 144 + 64 * k + lane) * 16u, ones);
      const uint32_t oz = (lane < 16) ? O_XC + (uint32_t)(tg * 144 + 128 + lane) * 16u : O_FB + (uint32_t)(8 * b + lane - 16) * 16u;
      if (lane < ((b < 72) ? 24 : 16)) ST16(oz, ones);
    }
    if (C3 && (w == 1 || w == 2)) {
      const char* wm = (const char*)((lane & 32) ? wu : wg) + bx * 1152;
      const int c0 = (3 - (int)((uintptr_t)wm >> 7)) & 7;
      wm += (uint32_t)((18 * bj + min(lane & 31, 17)) * (F_ * 2) + 128 * ((w == 2 && c0 == 0) ? 8 : c0));
      asm volatile("global_load_dword %0, %1, off\n\ts_waitcnt vmcnt(0)" : "+v"(sink) : "v"(wm) : "memory");
    }
    SLEEP(ESLEEP);
    const uint32_t tu = latest(gate(ws, O_SU + (uint32_t)(256 * bx + 16 * i + 4 * w + (bj & 3)) * 128u, T));
    if (!isx) until(tu + UD + WD);
    BAR();
    TS(0, 3);
    if (ffn) {
      gu_dma(0);
      gu_dma(1);
      DRAIN();
      gsync(ca + 4, 4);
      TS(0, 4);
      gu_read(0);
      gu_read(1);
    } else {
      if (isx && w == 1) SIGNAL(O_SV + 128u * (256 - 24 + 8 * bx + bj), 320 * 128, VDX);
      if (TW2) {
        uint32_t sink = 0;
#pragma unroll
        for (int k = 0; k < 2; ++k) {
          const int tn = 10367 - 14 * min(256 * k + tid, TW2 - 1) - (bj - 18), tr = tn / 9, th = tr / 576, tq = tr % 576;
          asm volatile("global_load_dword %0, %1, off" : "+v"(sink)
                       : "v"((const char*)((tq & 1) ? wu : wg) + (uint32_t)((288 * th + 144 * ((tq >> 1) & 1) + (tq >> 2)) * (F_ * 2) + bx * 1152 + (tn % 9) * 128)) : "memory");
        }
        asm volatile("s_waitcnt vmcnt(0)" : "+v"(sink));
      }
#pragma unroll
      for (int s = 0; s < 18; ++s) asm volatile("" : "=v"(bw[s]));
    }
    DRAIN();
    if (isx) v_gate(tu, tid);
  }
  {
  const int tid = tid2, lane = tid & 63, i = lane & 15, kq = lane >> 4;
  __builtin_amdgcn_s_setprio(3);
  f32x2 xf[9];
  if (ROLE == 2) {
#pragma unroll
    for (int k = 0; k < 9; ++k) xf[k] = (f32x2){bflo(xv[k]), bfhi(xv[k])};
    asm volatile("" : "+v"(xf[0]), "+v"(xf[1]), "+v"(xf[2]), "+v"(xf[3]), "+v"(xf[4]), "+v"(xf[5]), "+v"(xf[6]), "+v"(xf[7]), "+v"(xf[8]));
  }
  uint32_t vo = v_at(tid, tg), vo2 = v_at(512 + 8 * w + (lane & 7), tg);
  asm volatile("" : "+v"(vo), "+v"(vo2));
  TS(0, 7);
  BAR();
  TS(0, 8);
  const uint32_t tk = (uint32_t)__builtin_amdgcn_s_memrealtime() + KD;
  {
    uint32_t* pln = (uint32_t*)smem;
    u32x4 va, vb = {0u, 0u, 0u, 0u};
    asm volatile("global_load_dwordx4 %0, %2, %4 sc1\n\ts_mov_b64 exec, 0xff\n\tglobal_load_dwordx4 %1, %3, %4 sc1\n\ts_mov_b64 exec, -1\n\ts_waitcnt vmcnt(0)"
                 : "=&v"(va), "+v"(vb) : "v"(vo), "v"(vo2), "s"(ws) : "memory");
    if (__builtin_expect(__any(max(max4(va), max4(vb)) == 0xffffffffu), 0)) {
#pragma unroll 1
      for (int spin = 0; spin < SPIN_CAP; ++spin) {
        const bool na = max4(va) == 0xffffffffu, nb = max4(vb) == 0xffffffffu;
        if (!__any(na || nb)) break;
        TSC(16);
        SLEEP(PSLEEP);
        if (na) LD16W(va, vo);
        if (nb) LD16W(vb, vo2);
      }
    }
#pragma unroll
    for (int e = 0; e < 4; ++e) pln[640 * e + tid] = va[e];
    if (lane < 8) {
#pragma unroll
      for (int e = 0; e < 4; ++e) pln[640 * e + 512 + 8 * w + lane] = vb[e];
    }
  }
  BAR();
  TS(0, 9);
  if (ROLE == 1 && ffn && w) {
#pragma unroll 1
    for (int k = 0; k < 12; ++k) {
      dma16((const char*)wd + (size_t)(fb * 36864 + 12288 * (w - 1) + 1024 * k), 16u * lane, lb0 + LW + 12288 * (w - 1) + 1024 * k);
      asm volatile("s_waitcnt vmcnt(%0)" : : "n"(DDEPTH) : "memory");
    }
  }
  uint32_t hv[9], nv[9], keya = 0u, keyb = 0u;
  if constexpr (ROLE == 2) {
    const uint32_t* pr = (const uint32_t*)smem + 640 * wl;
    const u32x4 a0 = *(const u32x4*)(pr + 8 * lane), a1 = *(const u32x4*)(pr + 8 * lane + 4);
    const u32x2 a2 = *(const u32x2*)(pr + 512 + 2 * (lane & 31));
    const uint32_t ps = (lane < 32) ? 0x01000c0cu : 0x03020c0cu;
    const f32x2 h[9] = {xf[0] + (f32x2){bflo(a0[0]), bflo(a0[1])}, xf[1] + (f32x2){bflo(a0[2]), bflo(a0[3])},
                        xf[2] + (f32x2){bflo(a1[0]), bflo(a1[1])}, xf[3] + (f32x2){bflo(a1[2]), bflo(a1[3])},
                        xf[4] + (f32x2){bfhi(a0[0]), bfhi(a0[1])}, xf[5] + (f32x2){bfhi(a0[2]), bfhi(a0[3])},
                        xf[6] + (f32x2){bfhi(a1[0]), bfhi(a1[1])}, xf[7] + (f32x2){bfhi(a1[2]), bfhi(a1[3])},
                        xf[8] + (f32x2){__uint_as_float(__builtin_amdgcn_perm(0u, a2[0], ps)), __uint_as_float(__builtin_amdgcn_perm(0u, a2[1], ps))}};
#pragma unroll
    for (int k = 0; k < 9; ++k) hv[k] = pack2(h[k][0], h[k][1]);
    ln_two(hv, lane, 1.f, nv, [] {});
    const float da = dot2(nv[2], wv[2], dot2(nv[0], wv[0], 0.f)) + dot2(nv[3], wv[3], dot2(nv[1], wv[1], 0.f));
    const float db = dot2(nv[6], wv[2], dot2(nv[4], wv[0], 0.f)) + dot2(nv[7], wv[3], dot2(nv[5], wv[1], 0.f));
    float dt = row_sum(dot2(nv[8], wv[4], fold32(da, db)));
    asm("s_nop 1\n\tv_add_f32_dpp %0, %0, %0 row_bcast:15 row_mask:0xa bank_mask:0xf\n\ts_nop 0" : "+v"(dt));
    const uint32_t bb = pack2(dt, dt) & 0xffff0000u;
    const uint32_t kk = (bb ^ (((uint32_t)((int)bb >> 31) & 0x7fff0000u) | 0x80000000u)) | (uint32_t)(0xffff - (8 * tg + 2 * wl + (lane >> 5)));
    if ((lane & 31) == 31) kl[2 * wl + (lane >> 5)] = kk;
    keya = (uint32_t)__builtin_amdgcn_readlane(kk, 31); keyb = (uint32_t)__builtin_amdgcn_readlane(kk, 63);
  }
  BAR();
  if (ROLE == 1 && w == 0) {
    const u32x4 ka = *(const u32x4*)kl, kb = *(const u32x4*)(kl + 4);
    uint32_t m1 = max(max(ka[0], ka[1]), ka[2]), m2 = med3u(ka[0], ka[1], ka[2]), m3 = min(min(ka[0], ka[1]), ka[2]);
#pragma unroll
    for (int e = 0; e < 5; ++e) {
      const uint32_t kx = e ? kb[e - 1] : ka[3];
      m3 = med3u(m2, m3, kx); m2 = med3u(m1, m2, kx); m1 = max(m1, kx);
    }
    if (lane < 8) ST16(O_KEY + (uint32_t)(256 * lane + b) * 16u, ((u32x4){m1, m2, m3, T}));
  }
  if (ROLE == 1 && w == 1) {
    const u32x4 kv = *(const u32x4*)&kl[4 * (lane & 1)];
    if (lane < 2) ST16(O_KF + (uint32_t)(2 * tg + lane) * 16u, ((u32x4){__builtin_amdgcn_perm(kv[1], kv[0], 0x07060302u), T, __builtin_amdgcn_perm(kv[3], kv[2], 0x07060302u), T}));
    if (b == 0 && lane == 0) ST4(O_EP, T * 0x0E8B2F51u);
  }
  TS(0, 10);
  auto put_row = [&](const uint32_t* r, int j, const void* base, uint32_t ro) {
    const u32x4 tl = {r[8], (uint32_t)DPP_I((int)r[8], 0x55, 0xf), (uint32_t)DPP_I((int)r[8], 0xaa, 0xf), (uint32_t)DPP_I((int)r[8], 0xff, 0xf)};
    asm volatile("global_store_dwordx4 %0, %1, %2 sc1\n\ts_nop 1" : : "v"(ro + 16u * lane), "v"((u32x4){r[4 * j], r[4 * j + 1], r[4 * j + 2], r[4 * j + 3]}), "s"(base) : "memory");
    if ((lane & 35) == 32 * j) asm volatile("global_store_dwordx4 %0, %1, %2 offset:1024 sc1\n\ts_nop 1" : : "v"(ro + 4u * (lane & 31)), "v"(tl), "s"(base) : "memory");
  };
  int rka = 0, rkb = 0;
  uint32_t k3rd = 0u;
  if constexpr (ROLE == 2) {
    __builtin_amdgcn_s_setprio(0);
    const uint32_t ko = kl[lane & 7];
    rka = FORCEFB + __popcll(__ballot(ko > keya) & 0xffull); rkb = FORCEFB + __popcll(__ballot(ko > keyb) & 0xffull);
#pragma unroll
    for (int k = 0; k < 9; ++k) {
      hv[k] = pack2(bflo(hv[k]) + bflo(nv[k]), bfhi(hv[k]) + bfhi(nv[k]));
      nv[k] = min(nv[k], 0xfffffffeu);
    }
    if (rka < 2) put_row(nv, 0, ws, O_XC + (uint32_t)(2 * tg + rka) * 1152u);
    if (rkb < 2) put_row(nv, 1, ws, O_XC + (uint32_t)(2 * tg + rkb) * 1152u);
    until(tk + OD);
    if (rka >= 2) put_row(hv, 0, out, (uint32_t)((8 * tg + 2 * wl) * (D_ * 2)));
    if (rkb >= 2) put_row(hv, 1, out, (uint32_t)((8 * tg + 2 * wl + 1) * (D_ * 2)));
    __builtin_amdgcn_s_setprio(3);
  } else {
    u32x4 key;
    const uint32_t kof = O_KEY + (uint32_t)(256 * bx + tid) * 16u;
    until(tk);
    if (!ffn) SLEEP(NSLEEP);
    LD16W(key, kof);
    if (__builtin_expect(__any(key[3] != T), 0)) {
#pragma unroll 1
      for (int spin = 0; spin < SPIN_CAP; ++spin) {
        const bool need = key[3] != T;
        if (!__any(need)) break;
        TSC(15);
        SLEEP(KBACK);
        if (need) LD16W(key, kof);
      }
    }
    TS(0, 11);
    const uint32_t m1 = oct_maxu(key[0]);
    const bool hit = key[0] == m1;
    const uint32_t k0 = hit ? key[1] : key[0], k1 = hit ? key[2] : key[1];
    const uint32_t m2 = oct_maxu(k0);
    if ((lane & 7) == 0) *(u32x2*)&cand[tid >> 2] = (u32x2){m1, m2};
    k3rd = (k0 == m2) ? k1 : k0;
  }
  auto pick = [&](const uint32_t* cd, uint32_t* sl, bool first) {
    BAR();
    const uint32_t c = cd[lane], own = cd[8 * w + (lane & 7)];
    const u32x4 la = *(const u32x4*)&cd[8 * w], lb = *(const u32x4*)&cd[8 * w + 4];
    if (ROLE == 1 && first) {
      const uint32_t m3 = oct_maxu(k3rd);
      if ((lane & 7) == 0) sl[8 + (tid >> 3)] = m3;
    }
    uint32_t r = C_;
#pragma unroll
    for (int e = 0; e < 8; ++e) {
      const uint32_t cn = (uint32_t)__popcll(__ballot(c > ((e < 4) ? la[e & 3] : lb[e & 3])));
      asm("v_writelane_b32 %0, %1, %2" : "+v"(r) : "s"(cn), "n"(e));
    }
    if (r < C_) sl[r] = own;
    BAR();
  };
  pick(cand, sel, true);
  uint32_t mykey = sel[lane & 7], okey = sel[lane >> 3];
  if (FORCESLOW || __any(sel[8 + (lane & 31)] > (uint32_t)__builtin_amdgcn_readlane(mykey, 7))) {
    u32x4 key;
#pragma unroll 1
    for (int spin = 0; spin < SPIN_CAP; ++spin) {
      LD16W(key, O_KF + 16u * tid);
      if (!__any(key[1] != T || key[3] != T)) break;
      SLEEP(KBACK);
    }
    const uint32_t ib = 0xffffu - 4u * tid;
    uint32_t k0 = (key[0] << 16) | ib, k1 = (key[0] & 0xffff0000u) | (ib - 1), k2 = (key[2] << 16) | (ib - 2), k3 = (key[2] & 0xffff0000u) | (ib - 3);
    CEX(k0, k1) CEX(k2, k3) CEX(k0, k2) CEX(k1, k3) CEX(k1, k2)
#pragma unroll 1
    for (int c = 0; c < C_; ++c) {
      const uint32_t mx = wave_maxu(k0);
      const bool hit = (k0 == mx);
      k0 = hit ? k1 : k0; k1 = hit ? k2 : k1; k2 = hit ? k3 : k2; k3 = hit ? 0u : k3;
      if (lane == 0) cand[64 + 8 * w + c] = mx;
    }
    pick(cand + 64, sel + 40, false);
    mykey = sel[40 + (lane & 7)]; okey = sel[40 + (lane >> 3)];
  }
  uint32_t ro = O_XC + ((~mykey & (L_ - 1)) >> 3) * 2304u;
  bool late = false;
  {
    const uint64_t same = __ballot(((mykey ^ okey) & 0x7f8u) == 0u && okey > mykey);
    if (FORCEFB || __builtin_expect(same != 0ull, 0)) {
      const int sl = FORCEFB + __popcll((same >> (lane & 7)) & 0x0101010101010101ull);
      ro = (sl < 2) ? ro + 1152u * sl : O_FB + 1152u * (lane & 7);
      late = __any(sl >= 2);
    }
  }
  TS(0, 12);
  auto put_out = [&](int j, uint32_t k0, int rk) {
    const uint64_t mine = __ballot(mykey == k0) & 0xffull;
    if (mine == 0ull) { if (rk < 2) put_row(hv, j, out, (uint32_t)((8 * tg + 2 * wl + j) * (D_ * 2))); }
    else if (rk >= 2) { DRAIN(); put_row(nv, j, ws, O_FB + 1152u * (uint32_t)__builtin_ctzll(mine)); }
  };
  auto rest_out = [&] { put_out(0, keya, rka); put_out(1, keyb, rkb); };
  if (ROLE == 2 && (!ffn || FORCEFB || late)) rest_out();
  if (!ffn) { TSE(); return; }
  {
    const uint32_t ao = ro + 64u * (i >> 3) + 16u * kq + 128u * w;
    u32x4 aa, ab = {0u, 0u, 0u, 0u};
    if (ROLE == 2) {
      LD16(aa, ao);
    {
      const uint32_t da = lb0 + LW + (uint32_t)((8 * kq + ((lane >> 2) & 3)) * 1152 + 8 * (lane & 3) + 128 * wl);
#pragma unroll
      for (int t = 0; t < 12; ++t) {
        const u32x2 h0 = trd(da + 512 * (t >> 2) + 32 * (t & 3)), h1 = trd(da + 512 * (t >> 2) + 32 * (t & 3) + 4608);
        bw[t] = (u32x4){h0[0], h0[1], h1[0], h1[1]};
      }
#pragma unroll
      for (int t = 0; t < 12; ++t) asm volatile("" : "+v"(bw[t]));
    }
      asm volatile("s_waitcnt vmcnt(0)" : "+v"(aa) : : "memory");
    } else if (w == 0) asm volatile("global_load_dwordx4 %0, %2, %3 sc1\n\tglobal_load_dwordx4 %1, %2, %3 offset:1024 sc1\n\ts_waitcnt vmcnt(0)"
                             : "=&v"(aa), "=&v"(ab) : "v"(ao), "s"(ws) : "memory");
    else LD16W(aa, ao);
    if (__builtin_expect(__any(max(max4(aa), max4(ab)) == 0xffffffffu), 0)) {
#pragma unroll 1
      for (int spin = 0; spin < SPIN_CAP; ++spin) {
        const bool na = max4(aa) == 0xffffffffu, nb = max4(ab) == 0xffffffffu;
        if (!__any(na || nb)) break;
        TSC(14);
        SLEEP(PSLEEP);
        if (na) LD16W(aa, ao);
        if (nb) LD16W(ab, ao + 1024u);
      }
    }
    *(u32x4*)(smem + LA + 16 * tid) = aa;
    if (ROLE == 1 && w == 0) *(u32x4*)(smem + LA + 8192 + 16 * tid) = ab;
  }
  BAR();
  TS(0, 5);
  uint32_t fx = 0, fv = 0, fn = 0, fo = 0, sa[9];
  float rs, hx, xs;
  u32x4 se;
  if constexpr (ROLE == 1) {
    f32x4 lo = {0.f, 0.f, 0.f, 0.f}, hi = {0.f, 0.f, 0.f, 0.f};
#pragma unroll
    for (int j = 0; j < 9; ++j) {
      const u32x4 av = *(const u32x4*)(smem + LA + 1024 * j + 16 * lane);
      lo = MMB(av, bw[2 * j], lo);
      hi = MMB(av, bw[2 * j + 1], hi);
    }
    const auto l0 = __builtin_amdgcn_permlane32_swap(__float_as_uint(lo[0]), __float_as_uint(lo[2]), false, false);
    const auto l1 = __builtin_amdgcn_permlane32_swap(__float_as_uint(lo[1]), __float_as_uint(lo[3]), false, false);
    const auto h0 = __builtin_amdgcn_permlane32_swap(__float_as_uint(hi[0]), __float_as_uint(hi[2]), false, false);
    const auto h1 = __builtin_amdgcn_permlane32_swap(__float_as_uint(hi[1]), __float_as_uint(hi[3]), false, false);
    const float fa = __uint_as_float(l0[0]) + __uint_as_float(h0[1]), fb2 = __uint_as_float(l1[0]) + __uint_as_float(h1[1]);
    const float g = __int_as_float(__builtin_amdgcn_update_dpp(__float_as_int(fa), __float_as_int(fb2), 0x128, 0xf, 0xc, false));
    const float u = __int_as_float(__builtin_amdgcn_update_dpp(__float_as_int(fb2), __float_as_int(fa), 0x128, 0xf, 0x3, false));
    const uint32_t gu = pack2(g, u);
    const float gp = bflo(gu);
    const float sg = rbf(gp * __builtin_amdgcn_rcpf(1.f + __expf(-gp)));
    hm[(4 * (kq & 1) + 2 * (kq >> 1) + (i >> 3)) * 40 + 8 * w + (i & 7)] = f2bf(sg * bfhi(gu));
  }
  BAR();
  TS(0, 6);
  if constexpr (ROLE == 2) {
    const u32x4 a0 = *(const u32x4*)&hm[min(i, 8) * 40 + 8 * kq], a1 = *(const u32x4*)&hm[min(i ^ 8, 8) * 40 + 8 * kq];
    const uint32_t so = O_SL - 4096u + (uint32_t)fb * SP + 16u * tid;
    const f32x4 z = {0.f, 0.f, 0.f, 0.f};
#pragma unroll
    for (int t = 0; t < 3; ++t) {
      if (t < 2 || w == 4) {
        const f32x4 o0 = MMB(a1, bw[4 * t + 1], MMB(a0, bw[4 * t], z)), o1 = MMB(a1, bw[4 * t + 3], MMB(a0, bw[4 * t + 2], z));
        u32x4 sv;
#pragma unroll
        for (int r = 0; r < 4; ++r) sv[r] = min(pack2(o0[r], o1[r]), 0xfffffffeu);
        ST16(so + 4096u * t, sv);
      }
    }
    TS(0, 13);
    SLEEP(OSLEEP);
    if (!(FORCEFB || late)) rest_out();
  }
  if (ROLE == 1 && w == 1) {
    const int q = (fb & 15) >> 2, ts = 4 * (q & 1) + ((i & 7) >> 1);
    const int dd = 64 * (fb >> 4) + 16 * (2 * (i & 1) + (q >> 1)) + 4 * (fb & 3) + 2 * (kq & 1) + (kq >> 1);
    const uint32_t fkey = (uint32_t)__builtin_amdgcn_ds_bpermute(4 * ts, (int)mykey), fr = (uint32_t)__builtin_amdgcn_ds_bpermute(4 * ts, (int)ro);
    const int ft = (int)(~fkey & (L_ - 1));
    fo = (uint32_t)(ft * D_ + dd) * 2u;
    asm volatile("global_load_ushort %0, %3, %6\n\tglobal_load_ushort %1, %4, %7 sc1\n\tglobal_load_ushort %2, %5, %7 sc1"
                 : "=&v"(fx), "=&v"(fv), "=&v"(fn) : "v"(fo), "v"(v_at(dd, ft >> 3) + 2u * (ft & 7)), "v"(fr + 2u * dd), "s"(x), "s"(ws) : "memory");
    const uint32_t o = fkey >> 16;
    const float rs0 = bf2f((u16)((o & 0x8000u) ? (o & 0x7fffu) : (~o & 0xffffu)));
    rs = rbf(__builtin_amdgcn_rcpf(1.f + __expf(-rs0)));
    const uint32_t one = (i < 8) ? (0x3f80u << (16 * (i & 1))) : 0u;
    se = (u32x4){(i >> 1) == 0 ? one : 0u, (i >> 1) == 1 ? one : 0u, (i >> 1) == 2 ? one : 0u, (i >> 1) == 3 ? one : 0u};
#pragma unroll
    for (int m = 0; m < 9; ++m) sa[m] = O_SL + (uint32_t)(16 * m + (lane >> 2)) * SP + (uint32_t)(4 * fb + (lane & 3)) * 16u;
    asm volatile("" : "+v"(rs), "+v"(se), "+v"(sa[0]), "+v"(sa[1]), "+v"(sa[2]), "+v"(sa[3]), "+v"(sa[4]), "+v"(sa[5]), "+v"(sa[6]), "+v"(sa[7]), "+v"(sa[8]));
    SLEEP(SSLEEP);
    TS(0, 1);
    float sum;
#pragma unroll 1
    for (int spin = 0; spin < SPIN_CAP; ++spin) {
      u32x4 sv[9];
#pragma unroll
      for (int m = 0; m < 9; ++m) LD16(sv[m], sa[m]);
      asm volatile("s_waitcnt vmcnt(9)" : "+v"(fx), "+v"(fv), "+v"(fn));
      hx = rbf(bf2f((u16)fx) + bf2f((u16)fv));
      xs = bf2f((u16)fn);
      asm volatile("" : "+v"(hx), "+v"(xs));
      f32x4 dv = {0.f, 0.f, 0.f, 0.f};
      static_for<0, 9>([&](auto mm) {
        constexpr int m = mm.value;
        asm volatile("s_waitcnt vmcnt(%1)" : "+v"(sv[m]) : "n"(8 - m));
        dv = MMB(sv[m], se, dv);
        __builtin_amdgcn_sched_barrier(0);
      });
      sum = fold16(fold32(dv[0], dv[1]), fold32(dv[2], dv[3]));
      if (__builtin_expect(!__any(sum != sum), 1)) break;
      uint32_t mx = 0u;
#pragma unroll
      for (int m = 0; m < 9; ++m) mx = max(mx, max4(sv[m]));
      if (!__any(mx == 0xffffffffu)) break;
      TSC(18);
      SLEEP(PSLEEP);
    }
    const float o1 = rbf(xs + rbf(rbf(sum) * rs));
    if (i < 8) asm volatile("global_store_short %0, %1, %2 sc1" : : "v"(fo), "v"((uint32_t)f2bf(hx + o1)), "s"(out) : "memory");
    TS(0, 2);
  }
  TSE();
  }
  }
}
extern "C" __global__ __launch_bounds__(512, 4) void kf_all(KARGS) {
  __shared__ __attribute__((aligned(16))) char smem[SMEM];
  asm volatile("s_load_dword s96, s[0:1], 0x0\n\ts_load_dword s97, s[0:1], 0x40\n\tv_readfirstlane_b32 s98, v0\n\t"
               "s_cmpk_lt_u32 s2, 0x100\n\ts_cbranch_scc" CBR " 1f\n\t"
               "s_cmpk_lt_u32 s98, 0x100\n\ts_cbranch_scc1 kf_n\n\ts_branch kf_p\n"
               "1:\n\ts_cmpk_lt_u32 s98, 0x100\n\ts_cbranch_scc1 kf_c\n\t"
               "s_and_b32 s99, s2, 0xf8\n\ts_lshl_b32 s99, s99, 5\n\ts_and_b32 s98, s98, 0xc0\n\ts_or_b32 s98, s98, s99\n\ts_lshl_b32 s98, s98, 1\n\t"
               "s_getpc_b64 s[94:95]\n\ts_add_u32 s94, s94, s98\n\ts_addc_u32 s95, s95, 0\n\t"
               "s_load_dword s96, s[94:95], 0x0\n\ts_load_dword s96, s[94:95], 0x40\n\t"
               "s_load_dword s96, s[94:95], 0x4000\n\ts_load_dword s96, s[94:95], 0x4040\n\t"
               "s_cmp_gt_i32 s98, kf_end-kf_all-0x8100\n\ts_cbranch_scc1 kf_u\n\t"
               "s_load_dword s96, s[94:95], 0x8000\n\ts_load_dword s96, s[94:95], 0x8040\n\t"
               "s_branch kf_u"
               : : "s"(__builtin_amdgcn_kernarg_segment_ptr()), "s"(blockIdx.x), "v"(threadIdx.x),
                   "s"((uint32_t)(uintptr_t)(__attribute__((address_space(3))) char*)smem) : "scc", "s94", "s95", "s96", "s97", "s98", "s99", "v127");
}
extern "C" __global__ __launch_bounds__(512, 4) void kf_c(KARGS) {
  __shared__ __attribute__((aligned(16))) char smem[SMEM];
  role_body<0>(smem, KPASS);
}
extern "C" __global__ __launch_bounds__(512, 4) void kf_u(KARGS) {
  __shared__ __attribute__((aligned(16))) char smem[SMEM];
  role_body<3>(smem, KPASS);
}
extern "C" __global__ __launch_bounds__(512, 4) void kf_n(KARGS) {
  __shared__ __attribute__((aligned(16))) char smem[SMEM];
  role_body<1>(smem, KPASS);
}
extern "C" __global__ __launch_bounds__(512, 4) void kf_p(KARGS) {
  __shared__ __attribute__((aligned(16))) char smem[SMEM];
  role_body<2>(smem, KPASS);
}
extern "C" __global__ __launch_bounds__(512, 4) void kf_conv() { asm volatile(CONV_ASM ::: CONV_CLOB); }
extern "C" __global__ void kf_end() {}

extern "C" __global__ __launch_bounds__(1024) void k_probe(int* dummy) {
  __shared__ int sm[2];
  const uint32_t now = (uint32_t)__builtin_amdgcn_s_memrealtime();
  if (threadIdx.x == 0) { sm[0] = 0; sm[1] = 1 ? 0x7fffffff : 0; }
  __syncthreads();
  for (int k = threadIdx.x; k < 4096; k += 1024) {
    const uint32_t a = (uint32_t)g_prb[k], b = (uint32_t)(g_prb[k] >> 32);
    if (a) atomicMax(&sm[0], (int)(now - a));
    if (b) { if (1) atomicMin(&sm[1], (int)(now - b)); else atomicMax(&sm[1], (int)(now - b)); }
  }
  __syncthreads();
  const int q = min(max(sm[0] - sm[1], 0), 3000) * 10;
  if (threadIdx.x == 0) while ((int)((uint32_t)__builtin_amdgcn_s_memrealtime() - now) < q) __builtin_amdgcn_s_sleep(2);
}
extern "C" void kernel_launch(void* const* d_in, const int* in_sizes, int n_in,
                              void* d_out, int out_size, void* d_ws, size_t ws_size,
                              hipStream_t stream) {
  kf_all<<<512, 512, 0, stream>>>((const u16*)d_in[0], (const u16*)d_in[1], (const u16*)d_in[2], (const u16*)d_in[3], (const u16*)d_in[4],
                                  (const u16*)d_in[5], (const u16*)d_in[6], (const u16*)d_in[7], (const u16*)d_in[8], (char*)d_ws + (size_t)WSOFF * 1048576,
                                  (const u16*)d_in[10], (const u16*)d_in[11], (const u16*)d_in[12], (const u16*)d_in[13], (u16*)d_out);
  k_probe<<<1, 1024, 0, stream>>>((int*)d_ws);
}
```

```cpp
#include <hip/hip_runtime.h>
#include <hip/hip_bf16.h>
#include <stdint.h>$UNK

typedef __attribute__((ext_vector_type(8))) short bf16x8;
typedef __attribute__((ext_vector_type(4))) float f32x4;
typedef uint16_t u16;
typedef __attribute__((ext_vector_type(4))) uint32_t u32x4;
typedef __attribute__((ext_vector_type(2))) uint32_t u32x2;
#define TSD
#define TS(k, i)
#define TSC(i)
#define TSE()
#define TSP
#define TSA

constexpr int L_ = 2048, D_ = 576, N_ = 128, F_ = 4608, C_ = 8;
constexpr int NF = 144;
constexpr uint32_t O_EP = 0;
constexpr uint32_t O_KF = 4096;
constexpr uint32_t O_KEY = O_KF + 8192;
constexpr uint32_t O_SC = O_KEY + 32768;
constexpr uint32_t O_DN = O_SC + 9216;
constexpr uint32_t O_SU = O_DN + 20480;
constexpr uint32_t O_FB = O_SU + 262144;
constexpr uint32_t O_XC = O_FB + 8 * 1152;
constexpr uint32_t SP = 9216 + 128;
constexpr uint32_t O_SL = O_XC + 1024 * 1152;
constexpr uint32_t SLU = 336;
constexpr uint32_t O_U = O_SL + 256 * SLU * 16;
constexpr int UP = 584;
constexpr uint32_t VP = 8192 + 128, VPP = 4096 + 128;
constexpr uint32_t O_V = O_U + 256 * UP * 16, O_VP = O_V + 256 * VP;
static_assert(256 * SLU * 16 >= NF * SP && O_SL % 128 == 0 && O_U % 128 == 0 && O_V % 128 == 0, "");
__host__ __device__ __forceinline__ uint32_t u_at(int tg, int d) { return O_U + 16u * (uint32_t)((tg >> 1) * (UP * 2) + (d >> 2) * 8 + (tg & 1) * 4 + (d & 3)); }
__host__ __device__ __forceinline__ uint32_t v_at(int d, int tg) {
  return (d < 512) ? O_V + (uint32_t)(d >> 1) * VP + 16u * (uint32_t)(2 * tg + (d & 1)) : O_VP + (uint32_t)(d - 512) * VPP + 16u * (uint32_t)tg;
}
#define HAVE_VAT
#define HAVE_VTAG
__device__ __forceinline__ float bf2f(u16 v) { return __uint_as_float((uint32_t)v << 16); }
__device__ __forceinline__ float bflo(uint32_t v) { return __uint_as_float(v << 16); }
__device__ __forceinline__ float bfhi(uint32_t v) { return __uint_as_float(v & 0xffff0000u); }
__device__ __forceinline__ u16 f2bf(float f) { return __builtin_bit_cast(u16, (__bf16)f); }
__device__ __forceinline__ float rbf(float f) { return bf2f(f2bf(f)); }
typedef __attribute__((ext_vector_type(2))) float f32x2;
typedef __attribute__((ext_vector_type(2))) __bf16 bf16x2v;
__device__ __forceinline__ uint32_t pack2(float a, float b) {
  return __builtin_bit_cast(uint32_t, __builtin_convertvector((f32x2){a, b}, bf16x2v));
}

#define DPP_I(v, ctrl, rmask) __builtin_amdgcn_update_dpp(0, (v), (ctrl), (rmask), 0xf, true)
__device__ __forceinline__ float row_sum(float v) {
  v += __int_as_float(DPP_I(__float_as_int(v), 0x111, 0xf));
  v += __int_as_float(DPP_I(__float_as_int(v), 0x112, 0xf));
  v += __int_as_float(DPP_I(__float_as_int(v), 0x114, 0xf));
  v += __int_as_float(DPP_I(__float_as_int(v), 0x118, 0xf));
  return v;
}
__device__ __forceinline__ float rdl(float v, int l) { return __int_as_float(__builtin_amdgcn_readlane(__float_as_int(v), l)); }
__device__ __forceinline__ float wave_sum(float v) {
  v = row_sum(v);
  asm("s_nop 1\n\tv_add_f32_dpp %0, %0, %0 row_bcast:15 row_mask:0xa bank_mask:0xf\n\t"
      "s_nop 1\n\tv_add_f32_dpp %0, %0, %0 row_bcast:31 row_mask:0xc bank_mask:0xf\n\ts_nop 0" : "+v"(v));
  return rdl(v, 63);
}
__device__ __forceinline__ float fold32(float a, float b) {
  const auto r = __builtin_amdgcn_permlane32_swap(__float_as_uint(a), __float_as_uint(b), false, false);
  return __uint_as_float(r[0]) + __uint_as_float(r[1]);
}
__device__ __forceinline__ float fold16(float a, float b) {
  const auto r = __builtin_amdgcn_permlane16_swap(__float_as_uint(a), __float_as_uint(b), false, false);
  return __uint_as_float(r[0]) + __uint_as_float(r[1]);
}
__device__ __forceinline__ void wave_sum2(float& a, float& b) {
  float v = row_sum(fold32(a, b));
  asm("s_nop 1\n\tv_add_f32_dpp %0, %0, %0 row_bcast:15 row_mask:0xa bank_mask:0xf\n\ts_nop 0" : "+v"(v));
  a = rdl(v, 31); b = rdl(v, 63);
}
__device__ __forceinline__ void wave_sum4(float& a, float& b, float& c, float& d) {
  const float v = row_sum(fold16(fold32(a, b), fold32(c, d)));
  a = rdl(v, 15); c = rdl(v, 31); b = rdl(v, 47); d = rdl(v, 63);
}
__device__ __forceinline__ float dot2(uint32_t a, uint32_t b, float c) {
  return __builtin_amdgcn_fdot2_f32_bf16(__builtin_bit_cast(bf16x2v, a), __builtin_bit_cast(bf16x2v, b), c, false);
}
__device__ __forceinline__ uint32_t row_maxu(uint32_t v) {
  v = max(v, (uint32_t)DPP_I((int)v, 0xb1, 0xf));
  v = max(v, (uint32_t)DPP_I((int)v, 0x4e, 0xf));
  v = max(v, (uint32_t)DPP_I((int)v, 0x141, 0xf));
  v = max(v, (uint32_t)DPP_I((int)v, 0x140, 0xf));
  return v;
}
__device__ __forceinline__ uint32_t wave_maxu(uint32_t v) {
  v = max(v, (uint32_t)DPP_I((int)v, 0x111, 0xf));
  v = max(v, (uint32_t)DPP_I((int)v, 0x112, 0xf));
  v = max(v, (uint32_t)DPP_I((int)v, 0x114, 0xf));
  v = max(v, (uint32_t)DPP_I((int)v, 0x118, 0xf));
  v = max(v, (uint32_t)DPP_I((int)v, 0x142, 0xa));
  v = max(v, (uint32_t)DPP_I((int)v, 0x143, 0xc));
  return (uint32_t)__builtin_amdgcn_readlane((int)v, 63);
}
__device__ __forceinline__ void wave_lds_sync() {
  __builtin_amdgcn_s_waitcnt(0xc07f);
  __builtin_amdgcn_wave_barrier();
}

typedef __attribute__((ext_vector_type(8))) _Float16 f16x8;
typedef __attribute__((ext_vector_type(2))) _Float16 f16x2;
__device__ __forceinline__ uint32_t packh(f32x2 v) { return __builtin_bit_cast(uint32_t, __builtin_convertvector(v, f16x2)); }
__device__ __forceinline__ uint32_t packh(float a, float b) { return packh((f32x2){a, b}); }
constexpr int IP = 224, I_X = 128;
__device__ __forceinline__ f32x2 cmul(f32x2 z, f32x2 w) {
  f32x2 r;
  asm("v_pk_mul_f32 %0, %1, %2 op_sel_hi:[0,1]\n\tv_pk_fma_f32 %0, %1, %2, %0 op_sel:[1,1,0] op_sel_hi:[1,0,1] neg_lo:[0,1,0]" : "=&v"(r) : "v"(z), "v"(w));
  return r;
}
__device__ __forceinline__ f32x2 cexp2(float ar2, float air, float l) {
  const float t = air * l;
  return (f32x2){__builtin_amdgcn_cosf(t), __builtin_amdgcn_sinf(t)} * __builtin_amdgcn_exp2f(ar2 * l);
}
__device__ __forceinline__ float state_a(float ldt, float are, float aim, float& ar2, float& air, float& g) {
  const float dt = rbf(__expf(ldt));
  ar2 = dt * are * 1.4426950408889634f;
  air = dt * aim * 0.15915494309189535f;
  air -= __builtin_rintf(air);
  g = 0.f;
  if (__builtin_expect(__any(ar2 > 0.f), 0)) g = fminf(__uint_as_float(wave_maxu(__float_as_uint(fmaxf(ar2, 0.f)))), 1.f);
  return dt;
}
template <int N> __device__ __forceinline__ void put_im(char* p, int im, const f32x2* z) {
  *(u32x4*)p = (u32x4){packh(z[0][0], z[1][0]), packh(z[2][0], z[3][0]), packh(z[4][0], z[5][0]), packh(z[6][0], z[7][0])};
  *(u32x4*)(p + im) = (u32x4){packh(z[0][1], z[1][1]), packh(z[2][1], z[3][1]), packh(z[4][1], z[5][1]), packh(z[6][1], z[7][1])};
}
#define Y_ASM \
  "v_mov_b64 v[40:41], %0\nv_pk_mul_f32 v[42:43], v[40:41], %1 op_sel_hi:[0,1]\nv_pk_mul_f32 v[44:45], %0, %2 op_sel_hi:[0,1]\n" \
  "v_pk_fma_f32 v[42:43], v[40:41], %1, v[42:43] op_sel:[1,1,0] op_sel_hi:[1,0,1] neg_lo:[0,1,0]\n" \
  "v_pk_fma_f32 v[44:45], %0, %2, v[44:45] op_sel:[1,1,0] op_sel_hi:[1,0,1] neg_lo:[0,1,0]\nv_cvt_pk_f16_f32 v56, v40, v42\n" \
  "v_cvt_pk_f16_f32 v60, v41, v43\nv_pk_mul_f32 v[40:41], v[42:43], %1 op_sel_hi:[0,1]\nv_pk_mul_f32 v[48:49], v[44:45], %2 op_sel_hi:[0,1]\n" \
  "v_pk_fma_f32 v[40:41], v[42:43], %1, v[40:41] op_sel:[1,1,0] op_sel_hi:[1,0,1] neg_lo:[0,1,0]\n" \
  "v_pk_fma_f32 v[48:49], v[44:45], %2, v[48:49] op_sel:[1,1,0] op_sel_hi:[1,0,1] neg_lo:[0,1,0]\n" \
  "v_pk_mul_f32 v[42:43], v[40:41], %1 op_sel_hi:[0,1]\nv_pk_mul_f32 v[52:53], v[48:49], %2 op_sel_hi:[0,1]\n" \
  "v_pk_fma_f32 v[42:43], v[40:41], %1, v[42:43] op_sel:[1,1,0] op_sel_hi:[1,0,1] neg_lo:[0,1,0]\n" \
  "v_pk_fma_f32 v[52:53], v[48:49], %2, v[52:53] op_sel:[1,1,0] op_sel_hi:[1,0,1] neg_lo:[0,1,0]\nv_cvt_pk_f16_f32 v57, v40, v42\n" \
  "v_cvt_pk_f16_f32 v61, v41, v43\nv_pk_mul_f32 v[40:41], v[42:43], %1 op_sel_hi:[0,1]\n" \
  "v_pk_fma_f32 v[40:41], v[42:43], %1, v[40:41] op_sel:[1,1,0] op_sel_hi:[1,0,1] neg_lo:[0,1,0]\n" \
  "v_pk_mul_f32 v[42:43], v[40:41], %1 op_sel_hi:[0,1]\nv_pk_mul_f32 v[46:47], v[44:45], %1 op_sel_hi:[0,1]\n" \
  "v_pk_fma_f32 v[42:43], v[40:41], %1, v[42:43] op_sel:[1,1,0] op_sel_hi:[1,0,1] neg_lo:[0,1,0]\n" \
  "v_pk_fma_f32 v[46:47], v[44:45], %1, v[46:47] op_sel:[1,1,0] op_sel_hi:[1,0,1] neg_lo:[0,1,0]\nv_cvt_pk_f16_f32 v58, v40, v42\n" \
  "v_cvt_pk_f16_f32 v62, v41, v43\nv_cvt_pk_f16_f32 v64, v44, v46\nv_cvt_pk_f16_f32 v68, v45, v47\n" \
  "v_pk_mul_f32 v[40:41], v[42:43], %1 op_sel_hi:[0,1]\nv_pk_mul_f32 v[44:45], v[46:47], %1 op_sel_hi:[0,1]\n" \
  "v_pk_fma_f32 v[40:41], v[42:43], %1, v[40:41] op_sel:[1,1,0] op_sel_hi:[1,0,1] neg_lo:[0,1,0]\n" \
  "v_pk_fma_f32 v[44:45], v[46:47], %1, v[44:45] op_sel:[1,1,0] op_sel_hi:[1,0,1] neg_lo:[0,1,0]\n" \
  "v_pk_mul_f32 v[42:43], v[40:41], %1 op_sel_hi:[0,1]\nv_pk_mul_f32 v[46:47], v[44:45], %1 op_sel_hi:[0,1]\n" \
  "v_pk_fma_f32 v[42:43], v[40:41], %1, v[42:43] op_sel:[1,1,0] op_sel_hi:[1,0,1] neg_lo:[0,1,0]\n" \
  "v_pk_fma_f32 v[46:47], v[44:45], %1, v[46:47] op_sel:[1,1,0] op_sel_hi:[1,0,1] neg_lo:[0,1,0]\nv_cvt_pk_f16_f32 v59, v40, v42\n" \
  "v_cvt_pk_f16_f32 v63, v41, v43\nv_cvt_pk_f16_f32 v65, v44, v46\nv_cvt_pk_f16_f32 v69, v45, v47\n" \
  "v_pk_mul_f32 v[44:45], v[46:47], %1 op_sel_hi:[0,1]\nds_write_b128 %3, v[56:59] offset:0\n" \
  "v_pk_fma_f32 v[44:45], v[46:47], %1, v[44:45] op_sel:[1,1,0] op_sel_hi:[1,0,1] neg_lo:[0,1,0]\nds_write_b128 %3, v[60:63] offset:64\n" \
  "v_pk_mul_f32 v[46:47], v[44:45], %1 op_sel_hi:[0,1]\nv_pk_mul_f32 v[50:51], v[48:49], %1 op_sel_hi:[0,1]\n" \
  "v_pk_fma_f32 v[46:47], v[44:45], %1, v[46:47] op_sel:[1,1,0] op_sel_hi:[1,0,1] neg_lo:[0,1,0]\n" \
  "v_pk_fma_f32 v[50:51], v[48:49], %1, v[50:51] op_sel:[1,1,0] op_sel_hi:[1,0,1] neg_lo:[0,1,0]\nv_cvt_pk_f16_f32 v66, v44, v46\n" \
  "v_cvt_pk_f16_f32 v70, v45, v47\nv_cvt_pk_f16_f32 v56, v48, v50\nv_cvt_pk_f16_f32 v60, v49, v51\n" \
  "v_pk_mul_f32 v[44:45], v[46:47], %1 op_sel_hi:[0,1]\nv_pk_mul_f32 v[48:49], v[50:51], %1 op_sel_hi:[0,1]\n" \
  "v_pk_fma_f32 v[44:45], v[46:47], %1, v[44:45] op_sel:[1,1,0] op_sel_hi:[1,0,1] neg_lo:[0,1,0]\n" \
  "v_pk_fma_f32 v[48:49], v[50:51], %1, v[48:49] op_sel:[1,1,0] op_sel_hi:[1,0,1] neg_lo:[0,1,0]\n" \
  "v_pk_mul_f32 v[46:47], v[44:45], %1 op_sel_hi:[0,1]\nv_pk_mul_f32 v[50:51], v[48:49], %1 op_sel_hi:[0,1]\n" \
  "v_pk_fma_f32 v[46:47], v[44:45], %1, v[46:47] op_sel:[1,1,0] op_sel_hi:[1,0,1] neg_lo:[0,1,0]\n" \
  "v_pk_fma_f32 v[50:51], v[48:49], %1, v[50:51] op_sel:[1,1,0] op_sel_hi:[1,0,1] neg_lo:[0,1,0]\nv_cvt_pk_f16_f32 v67, v44, v46\n" \
  "v_cvt_pk_f16_f32 v71, v45, v47\nv_cvt_pk_f16_f32 v57, v48, v50\nv_cvt_pk_f16_f32 v61, v49, v51\n" \
  "v_pk_mul_f32 v[48:49], v[50:51], %1 op_sel_hi:[0,1]\nds_write_b128 %3, v[64:67] offset:16\n" \
  "v_pk_fma_f32 v[48:49], v[50:51], %1, v[48:49] op_sel:[1,1,0] op_sel_hi:[1,0,1] neg_lo:[0,1,0]\nds_write_b128 %3, v[68:71] offset:80\n" \
  "v_pk_mul_f32 v[50:51], v[48:49], %1 op_sel_hi:[0,1]\nv_pk_mul_f32 v[54:55], v[52:53], %1 op_sel_hi:[0,1]\n" \
  "v_pk_fma_f32 v[50:51], v[48:49], %1, v[50:51] op_sel:[1,1,0] op_sel_hi:[1,0,1] neg_lo:[0,1,0]\n" \
  "v_pk_fma_f32 v[54:55], v[52:53], %1, v[54:55] op_sel:[1,1,0] op_sel_hi:[1,0,1] neg_lo:[0,1,0]\nv_cvt_pk_f16_f32 v58, v48, v50\n" \
  "v_cvt_pk_f16_f32 v62, v49, v51\nv_cvt_pk_f16_f32 v64, v52, v54\nv_cvt_pk_f16_f32 v68, v53, v55\n" \
  "v_pk_mul_f32 v[48:49], v[50:51], %1 op_sel_hi:[0,1]\nv_pk_mul_f32 v[52:53], v[54:55], %1 op_sel_hi:[0,1]\n" \
  "v_pk_fma_f32 v[48:49], v[50:51], %1, v[48:49] op_sel:[1,1,0] op_sel_hi:[1,0,1] neg_lo:[0,1,0]\n" \
  "v_pk_fma_f32 v[52:53], v[54:55], %1, v[52:53] op_sel:[1,1,0] op_sel_hi:[1,0,1] neg_lo:[0,1,0]\n" \
  "v_pk_mul_f32 v[50:51], v[48:49], %1 op_sel_hi:[0,1]\nv_pk_mul_f32 v[54:55], v[52:53], %1 op_sel_hi:[0,1]\n" \
  "v_pk_fma_f32 v[50:51], v[48:49], %1, v[50:51] op_sel:[1,1,0] op_sel_hi:[1,0,1] neg_lo:[0,1,0]\n" \
  "v_pk_fma_f32 v[54:55], v[52:53], %1, v[54:55] op_sel:[1,1,0] op_sel_hi:[1,0,1] neg_lo:[0,1,0]\nv_cvt_pk_f16_f32 v59, v48, v50\n" \
  "v_cvt_pk_f16_f32 v63, v49, v51\nv_cvt_pk_f16_f32 v65, v52, v54\nv_cvt_pk_f16_f32 v69, v53, v55\n" \
  "v_pk_mul_f32 v[52:53], v[54:55], %1 op_sel_hi:[0,1]\nds_write_b128 %3, v[56:59] offset:32\n" \
  "v_pk_fma_f32 v[52:53], v[54:55], %1, v[52:53] op_sel:[1,1,0] op_sel_hi:[1,0,1] neg_lo:[0,1,0]\nds_write_b128 %3, v[60:63] offset:96\n" \
  "v_pk_mul_f32 v[54:55], v[52:53], %1 op_sel_hi:[0,1]\n" \
  "v_pk_fma_f32 v[54:55], v[52:53], %1, v[54:55] op_sel:[1,1,0] op_sel_hi:[1,0,1] neg_lo:[0,1,0]\nv_cvt_pk_f16_f32 v66, v52, v54\n" \
  "v_cvt_pk_f16_f32 v70, v53, v55\nv_pk_mul_f32 v[52:53], v[54:55], %1 op_sel_hi:[0,1]\n" \
  "v_pk_fma_f32 v[52:53], v[54:55], %1, v[52:53] op_sel:[1,1,0] op_sel_hi:[1,0,1] neg_lo:[0,1,0]\n" \
  "v_pk_mul_f32 v[54:55], v[52:53], %1 op_sel_hi:[0,1]\n" \
  "v_pk_fma_f32 v[54:55], v[52:53], %1, v[54:55] op_sel:[1,1,0] op_sel_hi:[1,0,1] neg_lo:[0,1,0]\nv_cvt_pk_f16_f32 v67, v52, v54\n" \
  "v_cvt_pk_f16_f32 v71, v53, v55\nds_write_b128 %3, v[64:67] offset:48\nds_write_b128 %3, v[68:71] offset:112\n"
#define Y_CLOB "memory", "v40", "v41", "v42", "v43", "v44", "v45", "v46", "v47", "v48", "v49", "v50", "v51", "v52", "v53", "v54", "v55", "v56", "v57", "v58", "v59", "v60", "v61", "v62", "v63", "v64", "v65", "v66", "v67", "v68", "v69", "v70", "v71"
#define T_ASM \
  "ds_read_b128 %4, %8\nds_read_b64_tr_b16 v[80:81], %7 offset:0\nds_read_b64_tr_b16 v[82:83], %7 offset:64\n" \
  "ds_read_b64_tr_b16 v[84:85], %5 offset:0\nds_read_b64_tr_b16 v[86:87], %5 offset:48\nds_read_b64_tr_b16 v[88:89], %6 offset:0\n" \
  "ds_read_b64_tr_b16 v[90:91], %6 offset:48\nds_read_b64_tr_b16 v[92:93], %7 offset:3584\nds_read_b64_tr_b16 v[94:95], %7 offset:3648\n" \
  "ds_read_b64_tr_b16 v[96:97], %5 offset:3584\nds_read_b64_tr_b16 v[98:99], %5 offset:3632\nds_read_b64_tr_b16 v[100:101], %6 offset:3584\n" \
  "ds_read_b64_tr_b16 v[102:103], %6 offset:3632\nds_read_b64_tr_b16 v[104:105], %7 offset:7168\nds_read_b64_tr_b16 v[106:107], %7 offset:7232\n" \
  "s_waitcnt lgkmcnt(8)\nv_mfma_f32_16x16x32_f16 %0, v[84:87], v[80:83], 0\nds_read_b64_tr_b16 v[108:109], %5 offset:7168\n" \
  "ds_read_b64_tr_b16 v[110:111], %5 offset:7216\nds_read_b64_tr_b16 v[112:113], %6 offset:7168\nv_mfma_f32_16x16x32_f16 %2, v[88:91], v[80:83], 0\n" \
  "ds_read_b64_tr_b16 v[114:115], %6 offset:7216\nds_read_b64_tr_b16 v[116:117], %7 offset:10752\nds_read_b64_tr_b16 v[118:119], %7 offset:10816\n" \
  "s_waitcnt lgkmcnt(8)\nv_mfma_f32_16x16x32_f16 %0, v[96:99], v[92:95], %0\nds_read_b64_tr_b16 v[120:121], %5 offset:10752\n" \
  "ds_read_b64_tr_b16 v[122:123], %5 offset:10800\nds_read_b64_tr_b16 v[124:125], %6 offset:10752\n" \
  "v_mfma_f32_16x16x32_f16 %2, v[100:103], v[92:95], %2\nds_read_b64_tr_b16 v[126:127], %6 offset:10800\n" \
  "ds_read_b64_tr_b16 v[80:81], %7 offset:14336\nds_read_b64_tr_b16 v[82:83], %7 offset:14400\ns_waitcnt lgkmcnt(8)\n" \
  "v_mfma_f32_16x16x32_f16 %0, v[108:111], v[104:107], %0\nds_read_b64_tr_b16 v[84:85], %5 offset:14336\n" \
  "ds_read_b64_tr_b16 v[86:87], %5 offset:14384\nds_read_b64_tr_b16 v[88:89], %6 offset:14336\n" \
  "v_mfma_f32_16x16x32_f16 %2, v[112:115], v[104:107], %2\nds_read_b64_tr_b16 v[90:91], %6 offset:14384\n" \
  "ds_read_b64_tr_b16 v[92:93], %7 offset:17920\nds_read_b64_tr_b16 v[94:95], %7 offset:17984\ns_waitcnt lgkmcnt(8)\n" \
  "v_mfma_f32_16x16x32_f16 %0, v[120:123], v[116:119], %0\nds_read_b64_tr_b16 v[96:97], %5 offset:17920\n" \
  "ds_read_b64_tr_b16 v[98:99], %5 offset:17968\nds_read_b64_tr_b16 v[100:101], %6 offset:17920\n" \
  "v_mfma_f32_16x16x32_f16 %2, v[124:127], v[116:119], %2\nds_read_b64_tr_b16 v[102:103], %6 offset:17968\n" \
  "ds_read_b64_tr_b16 v[104:105], %7 offset:21504\nds_read_b64_tr_b16 v[106:107], %7 offset:21568\ns_waitcnt lgkmcnt(8)\n" \
  "v_mfma_f32_16x16x32_f16 %1, v[84:87], v[80:83], 0\nds_read_b64_tr_b16 v[108:109], %5 offset:21504\nds_read_b64_tr_b16 v[110:111], %5 offset:21552\n" \
  "ds_read_b64_tr_b16 v[112:113], %6 offset:21504\nv_mfma_f32_16x16x32_f16 %3, v[88:91], v[80:83], 0\nds_read_b64_tr_b16 v[114:115], %6 offset:21552\n" \
  "ds_read_b64_tr_b16 v[116:117], %7 offset:25088\nds_read_b64_tr_b16 v[118:119], %7 offset:25152\ns_waitcnt lgkmcnt(8)\n" \
  "v_mfma_f32_16x16x32_f16 %1, v[96:99], v[92:95], %1\nds_read_b64_tr_b16 v[120:121], %5 offset:25088\n" \
  "ds_read_b64_tr_b16 v[122:123], %5 offset:25136\nds_read_b64_tr_b16 v[124:125], %6 offset:25088\n" \
  "v_mfma_f32_16x16x32_f16 %3, v[100:103], v[92:95], %3\nds_read_b64_tr_b16 v[126:127], %6 offset:25136\ns_waitcnt lgkmcnt(6)\n" \
  "v_mfma_f32_16x16x32_f16 %1, v[108:111], v[104:107], %1\nv_mfma_f32_16x16x32_f16 %3, v[112:115], v[104:107], %3\ns_waitcnt lgkmcnt(0)\n" \
  "v_mfma_f32_16x16x32_f16 %1, v[120:123], v[116:119], %1\nv_mfma_f32_16x16x32_f16 %3, v[124:127], v[116:119], %3\n"
#define T_CLOB "memory", "v80", "v81", "v82", "v83", "v84", "v85", "v86", "v87", "v88", "v89", "v90", "v91", "v92", "v93", "v94", "v95", "v96", "v97", "v98", "v99", "v100", "v101", "v102", "v103", "v104", "v105", "v106", "v107", "v108", "v109", "v110", "v111", "v112", "v113", "v114", "v115", "v116", "v117", "v118", "v119", "v120", "v121", "v122", "v123", "v124", "v125", "v126", "v127"
template <class F> __device__ __forceinline__ int gen_y(float ldt, float are, float aim, float bre, float bim, float cre, float cim, uint32_t im, F&& mid TSP) {
  float ar2, air, g;
  const float dt = state_a(ldt, are, aim, ar2, air, g);
  const float dbr = dt * bre, dbi = dt * bim, pr = cre * dbr - cim * dbi, pi = cre * dbi + cim * dbr;
  const f32x2 w8 = cexp2(ar2, -air, 8.f), w64 = cexp2(ar2, -air, 64.f);
  const int e = (int)(wave_maxu(__float_as_uint(fmaxf(fabsf(pr), fabsf(pi)))) >> 23) - 126 + (int)ceilf(g * 250.f);
  const f32x2 y0 = {__builtin_amdgcn_ldexpf(pr, 14 - e), __builtin_amdgcn_ldexpf(-pi, 14 - e)};
  mid();
  TS(0, 7);
  asm volatile(Y_ASM : : "v"(y0), "v"(w8), "v"(w64), "v"(im) : Y_CLOB);
  return e;
}
__device__ __forceinline__ int gen_x(float ldt, float are, float aim, char* im) {
  float ar2, air, g;
  state_a(ldt, are, aim, ar2, air, g);
  const int e = min((int)ceilf(g * 1810.f), 300);
  const float s = __builtin_amdgcn_ldexpf(1.f, 15 - e);
  const f32x2 w1 = cexp2(ar2, air, 1.f), w256 = cexp2(ar2, air, 256.f), w2 = cmul(w1, w1);
  f32x2 a[8], b[8], c[8];
  a[0] = (f32x2){s, 0.f}; a[1] = w1 * s; a[2] = w2 * s;
  b[0] = b[1] = w256 * s;
  b[2] = cmul(b[0], w2); a[3] = cmul(a[1], w2); a[4] = cmul(a[2], w2);
  b[3] = b[4] = b[5] = cmul(b[2], w2); a[5] = cmul(a[3], w2); a[6] = cmul(a[4], w2);
  b[6] = cmul(b[3], w2); a[7] = cmul(a[5], w2); c[0] = cmul(b[3], w256);
  b[7] = cmul(b[6], w2); c[2] = cmul(c[0], w256);
  put_im<0>(im + I_X, 48, a);
  c[1] = cmul(b[7], w256); c[3] = cmul(c[1], w256);
  put_im<0>(im + I_X + 16, 48, b);
#pragma unroll
  for (int k = 4; k < 8; ++k) c[k] = cmul(c[k - 1], w256);
  put_im<0>(im + I_X + 32, 48, c);
  return e;
}

template <int I> struct IC { static constexpr int value = I; };
template <int B, int E, class F> __device__ __forceinline__ void static_for(F&& f) {
  if constexpr (B < E) { f(IC<B>{}); static_for<B + 1, E>(f); }
}
typedef __attribute__((ext_vector_type(4))) short s16x4;
__device__ __forceinline__ u32x2 trd(uint32_t a) {
  return __builtin_bit_cast(u32x2, __builtin_amdgcn_ds_read_tr16_b64_v4i16((__attribute__((address_space(3))) s16x4*)(uintptr_t)a));
}
#define DPP_F(v, ctrl) __int_as_float(DPP_I(__float_as_int(v), (ctrl), 0xf))
__device__ __forceinline__ float s_add(float a, float b) { float r; asm("v_add_f32 %0, %1, %2" : "=v"(r) : "v"(a), "v"(b)); return r; }
__device__ __forceinline__ float s_fma(float a, float b, float c) { float r; asm("v_fma_f32 %0, %1, %2, %3" : "=v"(r) : "v"(a), "v"(b), "v"(c)); return r; }
#define BAR() asm volatile("s_waitcnt lgkmcnt(0)\n\ts_barrier" ::: "memory")
#define ST16(off, v) asm volatile("global_store_dwordx4 %0, %1, %2 sc1\n\ts_nop 1" : : "v"(off), "v"(v), "s"(ws) : "memory")
#define ST4(off, v) asm volatile("global_store_dword %0, %1, %2 sc1" : : "v"(off), "v"(v), "s"(ws) : "memory")
#define LD16(r, off) asm volatile("global_load_dwordx4 %0, %1, %2 sc1" : "=v"(r) : "v"(off), "s"(ws) : "memory")
#define LD16W(r, off) asm volatile("global_load_dwordx4 %0, %1, %2 sc1\n\ts_waitcnt vmcnt(0)" : "=&v"(r) : "v"(off), "s"(ws) : "memory")
#define DRAIN() asm volatile("s_waitcnt vmcnt(0)" ::: "memory")
#define CEX(a, b) { const uint32_t hi_ = max(a, b), lo_ = min(a, b); a = hi_; b = lo_; }
#define MMB(a_, b_, c_) __builtin_amdgcn_mfma_f32_16x16x32_bf16(__builtin_bit_cast(bf16x8, a_), __builtin_bit_cast(bf16x8, b_), c_, 0, 0, 0)
#define NTM 1
#define POLS(b) ((NTM & (b)) ? " nt" : "")
#define DPOL " nt"
#define DPOL2 ""
#define DPOL4 ""
#define XPOL ""
#define TPOL ""
constexpr int SPIN_CAP = 1 << 18;
constexpr uint32_t UMASK = 0x40074007u;
__device__ __forceinline__ uint32_t utag(uint32_t T, int j) {
  return ((T >> (3 * j)) & 0x00070007u) | (((j <= 2) ? (T << (2 - j)) : (T >> (j - 2))) & 0x40004000u);
}
__device__ __forceinline__ uint32_t max4(const u32x4& a) { return max(max(a[0], a[1]), max(a[2], a[3])); }
__device__ __forceinline__ uint32_t min4(const u32x4& a) { return min(min(a[0], a[1]), min(a[2], a[3])); }
__device__ __forceinline__ void until(uint32_t t) {
#pragma unroll 1
  for (int spin = 0; spin < 600 && (int)((uint32_t)__builtin_amdgcn_s_memrealtime() - t) < 0; ++spin) __builtin_amdgcn_s_sleep(1);
}
__device__ __forceinline__ uint32_t latest(uint32_t t) {
  const uint32_t t0 = (uint32_t)__builtin_amdgcn_readfirstlane(t) - 0x40000000u;
  return wave_maxu(t - t0) + t0;
}
__device__ __forceinline__ uint32_t gate(const char* ws, uint32_t po, uint32_t T) {
  u32x2 pv;
#pragma unroll 1
  for (int spin = 0; spin < SPIN_CAP; ++spin) {
    asm volatile("global_load_dwordx2 %0, %1, %2 sc1\n\ts_waitcnt vmcnt(0)" : "=&v"(pv) : "v"(po), "s"(ws) : "memory");
    if (!__any(pv[0] != T)) break;
    __builtin_amdgcn_s_sleep(2);
  }
  return pv[1];
}
__device__ __forceinline__ uint32_t gate2(const char* ws, uint32_t oa, uint32_t da, uint32_t ob, uint32_t T) {
  u32x2 pa, pb;
#pragma unroll 1
  for (int spin = 0; spin < SPIN_CAP; ++spin) {
    asm volatile("global_load_dwordx2 %0, %2, %4 sc1\n\tglobal_load_dwordx2 %1, %3, %4 sc1\n\ts_waitcnt vmcnt(0)" : "=&v"(pa), "=&v"(pb) : "v"(oa), "v"(ob), "s"(ws) : "memory");
    if (!__any(pa[0] != T || pb[0] != T)) break;
    __builtin_amdgcn_s_sleep(4);
  }
  pa[1] += da;
  return latest(((int)(pa[1] - pb[1]) > 0) ? pa[1] : pb[1]);
}
__device__ __forceinline__ uint32_t ahead(uint32_t c0, uint32_t t0, int dt) {
  const uint32_t c = (uint32_t)__builtin_amdgcn_s_memtime(), t = (uint32_t)__builtin_amdgcn_s_memrealtime();
  return t + (uint32_t)((float)((t - t0) * (uint32_t)(20 * dt)) * __builtin_amdgcn_rcpf((float)(c - c0)));
}
#define SIGNAL(off, pitch, dt) do { const uint32_t t_ = ahead(ck0, rt0, dt); \
    ST16((off) + (uint32_t)(lane >> 3) * (pitch) + 16u * (lane & 7), ((u32x4){T, t_, T, t_})); } while (0)
__device__ __forceinline__ void gsync(uint32_t a, uint32_t target) {
  uint32_t t, c, n = SPIN_CAP;
  uint64_t ex;
  asm volatile("s_waitcnt lgkmcnt(0)\n\ts_mov_b64 %3, exec\n\ts_mov_b64 exec, 1\n\tv_mov_b32 %0, 1\n\tds_add_u32 %4, %0\n\ts_mov_b64 exec, %3\n"
               "1:\n\ts_sleep 1\n\tds_read_b32 %0, %4\n\ts_sub_u32 %2, %2, 1\n\ts_cbranch_scc1 2f\n\ts_waitcnt lgkmcnt(0)\n\tv_readfirstlane_b32 %1, %0\n\t"
               "s_cmp_lt_u32 %1, %5\n\ts_cbranch_scc1 1b\n2:\n\ts_waitcnt lgkmcnt(0)"
               : "=&v"(t), "=&s"(c), "+s"(n), "=&s"(ex) : "v"(a), "n"(target) : "memory", "scc");
}
__device__ __forceinline__ void gsync0(uint32_t a, uint32_t target) {
  uint32_t t, c, n = SPIN_CAP;
  asm volatile("s_waitcnt lgkmcnt(0)\n\ts_mov_b64 exec, 1\n\tv_mov_b32 %0, 1\n\tds_add_u32 %3, %0\n\ts_mov_b64 exec, -1\n"
               "1:\n\tds_read_b32 %0, %3\n\ts_sub_u32 %2, %2, 1\n\ts_cbranch_scc1 2f\n\ts_waitcnt lgkmcnt(0)\n\tv_readfirstlane_b32 %1, %0\n\t"
               "s_cmp_lt_u32 %1, %4\n\ts_cbranch_scc1 1b\n2:\n\ts_waitcnt lgkmcnt(0)"
               : "=&v"(t), "=&s"(c), "+s"(n) : "v"(a), "n"(target) : "memory", "scc");
}
__device__ __forceinline__ void gwait(uint32_t a, uint32_t target) {
  uint32_t t, c, n = SPIN_CAP;
  asm volatile("1:\n\ts_sleep 2\n\tds_read_b32 %0, %3\n\ts_sub_u32 %2, %2, 1\n\ts_cbranch_scc1 2f\n\ts_waitcnt lgkmcnt(0)\n\tv_readfirstlane_b32 %1, %0\n\t"
               "s_cmp_lt_u32 %1, %4\n\ts_cbranch_scc1 1b\n2:\n\ts_waitcnt lgkmcnt(0)"
               : "=&v"(t), "=&s"(c), "+s"(n) : "v"(a), "n"(target) : "memory", "scc");
}
__device__ __forceinline__ void dma16(const void* base, uint32_t voff, uint32_t lds) {
  uint32_t keep;
  asm volatile("s_mov_b32 %0, m0\n\ts_mov_b32 m0, %3\n\ts_nop 0\n\tglobal_load_lds_dwordx4 %1, %2" DPOL "\n\ts_mov_b32 m0, %0" : "=&s"(keep) : "v"(voff), "s"(base), "s"(lds) : "memory");
}
__device__ __forceinline__ void dma16d(const void* base, uint32_t voff, uint32_t lds) {
  uint32_t keep;
  asm volatile("s_mov_b32 %0, m0\n\ts_mov_b32 m0, %3\n\ts_nop 0\n\tglobal_load_lds_dwordx4 %1, %2" DPOL2 "\n\ts_mov_b32 m0, %0" : "=&s"(keep) : "v"(voff), "s"(base), "s"(lds) : "memory");
}
__device__ __forceinline__ void nap(int n) {
  for (; n > 15; n -= 15) __builtin_amdgcn_s_sleep(15);
  switch (n) {
    case 1: __builtin_amdgcn_s_sleep(1); break; case 2: __builtin_amdgcn_s_sleep(2); break; case 3: __builtin_amdgcn_s_sleep(3); break;
    case 4: __builtin_amdgcn_s_sleep(4); break; case 5: __builtin_amdgcn_s_sleep(5); break; case 6: __builtin_amdgcn_s_sleep(6); break;
    case 7: __builtin_amdgcn_s_sleep(7); break; case 8: __builtin_amdgcn_s_sleep(8); break; case 9: __builtin_amdgcn_s_sleep(9); break;
    case 10: __builtin_amdgcn_s_sleep(10); break; case 11: __builtin_amdgcn_s_sleep(11); break; case 12: __builtin_amdgcn_s_sleep(12); break;
    case 13: __builtin_amdgcn_s_sleep(13); break; case 14: __builtin_amdgcn_s_sleep(14); break; case 15: __builtin_amdgcn_s_sleep(15); break;
    default: break;
  }
}
#define SLEEP(n) do { if ((n) > 0) __builtin_amdgcn_s_sleep(n); } while (0)
#define USLEEP 3
#define UPOS 1
#define QSLEEP 0
#define CSLEEP 4
#define WSLEEP 4
#define TSLEEP 0
#define VSLEEP 45
#define KSLEEP 2
#define SSLEEP 11
#define PSLEEP 2
#define PPRIO 0
#define APRIO 1
#define NTOUCH 4
#define SWZ 1
#define PREOUT 0
#define LFIRST 1
#define CBR "0"
#define WD 40
#define CD 3
#define OSLEEP 12
#define QPRIO 1
#define KPRIO 2
#define UDRAIN 0
#define GSLEEP 0
#define SIGPOS 0
#define LPRIO 3
#define TW 0
#define TPOS 0
#define R1EARLY 1
#define DPOS 0
#define NSLEEP 0
#define ASLEEP 40
#define FPRIO 1
#define PRBW 1
#define PRBPOS 1
#define TW2 6
#define DSLEEP 8
#define VD 400
#define XSLEEP 20
#define RPOS 1
#define KPRE 1
#define UD 40
#define XD 0
#define TRIG 1
#define CPF 0
#define RSLEEP 0
#define UPRIO 2
#define VD2 360
#define TWP 256
#define WDEPTH 8
#define PD 170
#define VDX 390
#define EARLY 0
#define ESLEEP 0
#define WDEPTH1 5
#define WD2 85
#define SD 70
#define KD 200
#define UFIRST 0
#define TX 1
#define QWAIT 4
#define OD 0
#define KBACK 6
#define DDEPTH 6
#define C3 1
#define XOFF 1
#define GD 105
#define XROT 2
#define WSOFF 0
#define T0 0
#define TF 192
#define DD 0
#define TDEPTH 1
#define TB1 0
#define TB1I 0
#define TB1X 2
#define B1SLEEP 0
#define DFIRST 0
#define T0SLEEP 0
#define BSLEEP 6
#define KN 3
#define TD 0
#define KE 0
#define SKIPB 0
#define NFIRST 0
#define PW1 16
#define PW2 38
#define KLATE 0
#define RPF 1
#define HSLEEP 6
#define LSLEEP 2
#define TSTOP 150
#define HSTOP 20
#define HL 0
#define VEARLY 0ull
#define FPR 3
#define SPR 3
#define DPRIO 0
#define T1 0
#define T1SLEEP 0
#define QPRIO2 3
#define HW 2
#define HN 3
#define RD 60
#define OPOS 0
#define OD2 8
#define NA 15
#define HA 2
#define HAX 0
#define RD2 60
#define ORD1 ((unsigned __int128)0x687210543u << 36 | 0x321087654ull)
#define RSL 10
#define FORCESLOW 0
#define FORCEFB 0
#define CONV_ASM \
  "s_cmp_eq_u32 s89, 1\ns_cbranch_scc1 1f\ns_cmp_gt_u32 s89, 1\ns_cbranch_scc1 9f\nds_read_b64 v[124:125], v3 offset:576\n" \
  "ds_read_b64 v[126:127], v3 offset:584\nds_read_b64 v[120:121], v3 offset:64\nds_read_b64 v[122:123], v3 offset:72\n" \
  "ds_read_b64 v[116:117], v3 offset:640\nds_read_b64 v[118:119], v3 offset:648\nds_read_b64 v[112:113], v3 offset:128\n" \
  "ds_read_b64 v[114:115], v3 offset:136\nds_read_b64 v[108:109], v3 offset:704\nds_read_b64 v[110:111], v3 offset:712\n" \
  "ds_read_b64 v[104:105], v3 offset:192\nds_read_b64 v[106:107], v3 offset:200\nds_read_b64 v[100:101], v3 offset:768\n" \
  "ds_read_b64 v[102:103], v3 offset:776\nds_read_b64 v[96:97], v3 offset:256\nds_read_b64 v[98:99], v3 offset:264\n" \
  "ds_read_b64 v[92:93], v3 offset:832\nds_read_b64 v[94:95], v3 offset:840\nds_read_b64 v[88:89], v3 offset:320\n" \
  "ds_read_b64 v[90:91], v3 offset:328\nds_read_b64 v[84:85], v3 offset:896\nds_read_b64 v[86:87], v3 offset:904\n" \
  "ds_read_b64 v[80:81], v3 offset:384\nds_read_b64 v[82:83], v3 offset:392\nds_read_b64 v[76:77], v3 offset:960\n" \
  "ds_read_b64 v[78:79], v3 offset:968\nds_read_b64 v[72:73], v3 offset:448\nds_read_b64 v[74:75], v3 offset:456\n" \
  "ds_read_b64 v[68:69], v3 offset:1024\nds_read_b64 v[70:71], v3 offset:1032\nds_read_b64 v[64:65], v3 offset:512\n" \
  "ds_read_b64 v[66:67], v3 offset:520\nds_read_b64 v[60:61], v3 offset:0\nds_read_b64 v[62:63], v3 offset:8\nds_read_b64 v[56:57], v3 offset:1088\n" \
  "ds_read_b64 v[58:59], v3 offset:1096\ns_waitcnt lgkmcnt(0)\ns_barrier\nds_read_b128 v[36:39], v2 offset:0\nds_read_b128 v[40:43], v2 offset:64\n" \
  "ds_read_b128 v[44:47], v2 offset:128\nds_read_b128 v[48:51], v2 offset:192\ns_waitcnt lgkmcnt(3)\n" \
  "v_mfma_f32_16x16x32_f16 v[12:15], v[124:127], v[36:39], 0\ns_setprio 1\nds_read_b128 v[52:55], v2 offset:256\n" \
  "v_mfma_f32_16x16x32_f16 v[16:19], v[120:123], v[36:39], 0\ns_waitcnt lgkmcnt(3)\nv_mfma_f32_16x16x32_f16 v[12:15], v[116:119], v[40:43], v[12:15]\n" \
  "ds_read_b128 v[36:39], v2 offset:320\nv_mfma_f32_16x16x32_f16 v[16:19], v[112:115], v[40:43], v[16:19]\ns_waitcnt lgkmcnt(3)\n" \
  "v_mfma_f32_16x16x32_f16 v[12:15], v[108:111], v[44:47], v[12:15]\nds_read_b128 v[40:43], v2 offset:384\n" \
  "v_mfma_f32_16x16x32_f16 v[16:19], v[104:107], v[44:47], v[16:19]\ns_waitcnt lgkmcnt(3)\n" \
  "v_mfma_f32_16x16x32_f16 v[12:15], v[100:103], v[48:51], v[12:15]\nds_read_b128 v[44:47], v2 offset:448\n" \
  "v_mfma_f32_16x16x32_f16 v[16:19], v[96:99], v[48:51], v[16:19]\ns_waitcnt lgkmcnt(3)\n" \
  "v_mfma_f32_16x16x32_f16 v[12:15], v[92:95], v[52:55], v[12:15]\nv_mfma_f32_16x16x32_f16 v[16:19], v[88:91], v[52:55], v[16:19]\n" \
  "s_waitcnt lgkmcnt(2)\nv_mfma_f32_16x16x32_f16 v[12:15], v[84:87], v[36:39], v[12:15]\nds_read_b128 v[48:51], v2 offset:512\n" \
  "v_mfma_f32_16x16x32_f16 v[16:19], v[80:83], v[36:39], v[16:19]\ns_waitcnt lgkmcnt(2)\n" \
  "v_mfma_f32_16x16x32_f16 v[12:15], v[76:79], v[40:43], v[12:15]\nds_read_b128 v[52:55], v2 offset:576\n" \
  "v_mfma_f32_16x16x32_f16 v[16:19], v[72:75], v[40:43], v[16:19]\ns_waitcnt lgkmcnt(2)\n" \
  "v_mfma_f32_16x16x32_f16 v[12:15], v[68:71], v[44:47], v[12:15]\nds_read_b128 v[36:39], v2 offset:640\n" \
  "v_mfma_f32_16x16x32_f16 v[16:19], v[64:67], v[44:47], v[16:19]\nv_mfma_f32_16x16x32_f16 v[20:23], v[60:63], v[44:47], 0\n" \
  "ds_read_b128 v[40:43], v2 offset:704\nv_mfma_f32_16x16x32_f16 v[28:31], v[56:59], v[44:47], 0\ns_waitcnt lgkmcnt(3)\n" \
  "v_mfma_f32_16x16x32_f16 v[16:19], v[124:127], v[48:51], v[16:19]\nds_read_b128 v[56:59], v2 offset:768\nv_cvt_pkrtz_f16_f32 v32, v12, v13\n" \
  "v_mfma_f32_16x16x32_f16 v[20:23], v[120:123], v[48:51], v[20:23]\nv_cvt_pkrtz_f16_f32 v33, v14, v15\ns_waitcnt lgkmcnt(3)\n" \
  "v_mfma_f32_16x16x32_f16 v[16:19], v[116:119], v[52:55], v[16:19]\nds_read_b128 v[44:47], v2 offset:832\n" \
  "v_pk_add_u16 v32, v32, 4 op_sel_hi:[1,0] clamp\nv_mfma_f32_16x16x32_f16 v[20:23], v[112:115], v[52:55], v[20:23]\n" \
  "v_pk_add_u16 v33, v33, 4 op_sel_hi:[1,0] clamp\ns_waitcnt lgkmcnt(3)\nv_mfma_f32_16x16x32_f16 v[16:19], v[108:111], v[36:39], v[16:19]\n" \
  "ds_read_b128 v[48:51], v2 offset:896\nds_write_b64 v4, v[32:33] offset:0\nv_mfma_f32_16x16x32_f16 v[20:23], v[104:107], v[36:39], v[20:23]\n" \
  "s_waitcnt lgkmcnt(3)\nv_mfma_f32_16x16x32_f16 v[16:19], v[100:103], v[40:43], v[16:19]\nds_read_b128 v[52:55], v2 offset:960\n" \
  "v_mfma_f32_16x16x32_f16 v[20:23], v[96:99], v[40:43], v[20:23]\nv_mfma_f32_16x16x32_f16 v[16:19], v[92:95], v[56:59], v[16:19]\n" \
  "v_mfma_f32_16x16x32_f16 v[20:23], v[88:91], v[56:59], v[20:23]\nds_read_b128 v[36:39], v2 offset:1024\ns_waitcnt lgkmcnt(3)\n" \
  "v_mfma_f32_16x16x32_f16 v[16:19], v[84:87], v[44:47], v[16:19]\nv_mfma_f32_16x16x32_f16 v[20:23], v[80:83], v[44:47], v[20:23]\n" \
  "ds_read_b128 v[40:43], v2 offset:1088\nds_read_b64 v[56:57], v6 offset:256\nds_read_b64 v[58:59], v6 offset:264\n" \
  "v_mfma_f32_16x16x32_f16 v[16:19], v[76:79], v[48:51], v[16:19]\nv_mfma_f32_16x16x32_f16 v[20:23], v[72:75], v[48:51], v[20:23]\n" \
  "ds_read_b128 v[44:47], v2 offset:1152\ns_waitcnt lgkmcnt(4)\nv_mfma_f32_16x16x32_f16 v[16:19], v[68:71], v[52:55], v[16:19]\n" \
  "v_mfma_f32_16x16x32_f16 v[20:23], v[64:67], v[52:55], v[20:23]\nds_read_b128 v[48:51], v2 offset:1216\n" \
  "v_mfma_f32_16x16x32_f16 v[24:27], v[60:63], v[52:55], 0\nv_mfma_f32_16x16x32_f16 v[20:23], v[124:127], v[36:39], v[20:23]\n" \
  "ds_read_b128 v[52:55], v2 offset:1280\nv_mfma_f32_16x16x32_f16 v[24:27], v[120:123], v[36:39], v[24:27]\ns_waitcnt lgkmcnt(5)\n" \
  "v_mfma_f32_16x16x32_f16 v[20:23], v[116:119], v[40:43], v[20:23]\nds_read_b128 v[36:39], v2 offset:1344\nv_cvt_pkrtz_f16_f32 v32, v16, v17\n" \
  "v_mfma_f32_16x16x32_f16 v[24:27], v[112:115], v[40:43], v[24:27]\nv_cvt_pkrtz_f16_f32 v33, v18, v19\ns_waitcnt lgkmcnt(3)\n" \
  "v_mfma_f32_16x16x32_f16 v[20:23], v[108:111], v[44:47], v[20:23]\nds_read_b128 v[40:43], v2 offset:1408\n" \
  "v_pk_add_u16 v32, v32, 4 op_sel_hi:[1,0] clamp\nds_read_b32 v35, v9 offset:32\nv_mfma_f32_16x16x32_f16 v[24:27], v[104:107], v[44:47], v[24:27]\n" \
  "v_pk_add_u16 v33, v33, 4 op_sel_hi:[1,0] clamp\ns_waitcnt lgkmcnt(3)\nv_mfma_f32_16x16x32_f16 v[20:23], v[100:103], v[48:51], v[20:23]\n" \
  "ds_read_b128 v[44:47], v2 offset:1472\nds_write_b64 v4, v[32:33] offset:1024\nv_mfma_f32_16x16x32_f16 v[24:27], v[96:99], v[48:51], v[24:27]\n" \
  "v_mfma_f32_16x16x32_f16 v[20:23], v[92:95], v[52:55], v[20:23]\nv_mfma_f32_16x16x32_f16 v[24:27], v[88:91], v[52:55], v[24:27]\n" \
  "ds_read_b128 v[48:51], v2 offset:1536\ns_waitcnt lgkmcnt(3)\nv_readfirstlane_b32 s85, v35\ns_cmp_lt_u32 s85, 4\ns_cbranch_scc1 21f\n22:\n" \
  "v_mfma_f32_16x16x32_f16 v[20:23], v[84:87], v[36:39], v[20:23]\nv_mfma_f32_16x16x32_f16 v[24:27], v[80:83], v[36:39], v[24:27]\n" \
  "ds_read_b128 v[52:55], v2 offset:1600\nv_mfma_f32_16x16x32_f16 v[20:23], v[76:79], v[40:43], v[20:23]\n" \
  "v_mfma_f32_16x16x32_f16 v[24:27], v[72:75], v[40:43], v[24:27]\nds_read_b128 v[36:39], v2 offset:1664\ns_waitcnt lgkmcnt(4)\n" \
  "v_mfma_f32_16x16x32_f16 v[20:23], v[68:71], v[44:47], v[20:23]\nv_mfma_f32_16x16x32_f16 v[24:27], v[64:67], v[44:47], v[24:27]\n" \
  "ds_read_b128 v[40:43], v2 offset:1728\nv_mfma_f32_16x16x32_f16 v[28:31], v[60:63], v[44:47], v[28:31]\nds_read_b64_tr_b16 v[60:61], v5 offset:0\n" \
  "ds_read_b64_tr_b16 v[62:63], v5 offset:32\ns_waitcnt lgkmcnt(4)\nv_mfma_f32_16x16x32_f16 v[24:27], v[124:127], v[48:51], v[24:27]\n" \
  "v_mfma_f32_16x16x32_f16 v[28:31], v[120:123], v[48:51], v[28:31]\nds_read_b64_tr_b16 v[44:45], v5 offset:256\n" \
  "ds_read_b64_tr_b16 v[46:47], v5 offset:288\nv_mfma_f32_16x16x32_f16 v[24:27], v[116:119], v[52:55], v[24:27]\n" \
  "ds_read_b64 v[120:121], v6 offset:320\nds_read_b64 v[122:123], v6 offset:328\nv_mfma_f32_16x16x32_f16 v[28:31], v[112:115], v[52:55], v[28:31]\n" \
  "ds_read_b128 v[48:51], v2 offset:1792\ns_waitcnt lgkmcnt(7)\nv_mfma_f32_16x16x32_f16 v[24:27], v[108:111], v[36:39], v[24:27]\n" \
  "ds_read_b128 v[112:115], v2 offset:1856\nv_mfma_f32_16x16x32_f16 v[28:31], v[104:107], v[36:39], v[28:31]\nds_read_b64 v[52:53], v6 offset:128\n" \
  "ds_read_b64 v[54:55], v6 offset:136\nv_mfma_f32_16x16x32_f16 v[24:27], v[100:103], v[40:43], v[24:27]\nds_read_b128 v[104:107], v2 offset:1920\n" \
  "v_mfma_f32_16x16x32_f16 v[28:31], v[96:99], v[40:43], v[28:31]\nds_read_b64 v[36:37], v6 offset:192\nds_read_b64 v[38:39], v6 offset:200\n" \
  "s_waitcnt lgkmcnt(9)\nv_mfma_f32_16x16x32_f16 v[20:23], v[60:63], v[56:59], v[20:23]\nds_read_b128 v[96:99], v2 offset:1984\ns_waitcnt lgkmcnt(7)\n" \
  "v_mfma_f32_16x16x32_f16 v[20:23], v[44:47], v[120:123], v[20:23]\nv_mfma_f32_16x16x32_f16 v[24:27], v[92:95], v[48:51], v[24:27]\n" \
  "ds_read_b128 v[40:43], v2 offset:2048\nv_mfma_f32_16x16x32_f16 v[28:31], v[88:91], v[48:51], v[28:31]\nds_read_b64_tr_b16 v[88:89], v5 offset:512\n" \
  "ds_read_b64_tr_b16 v[90:91], v5 offset:544\ns_waitcnt lgkmcnt(6)\nv_mfma_f32_16x16x32_f16 v[24:27], v[84:87], v[112:115], v[24:27]\n" \
  "v_mfma_f32_16x16x32_f16 v[28:31], v[80:83], v[112:115], v[28:31]\nv_cvt_pkrtz_f16_f32 v32, v20, v21\nv_cvt_pkrtz_f16_f32 v33, v22, v23\n" \
  "v_mfma_f32_16x16x32_f16 v[24:27], v[76:79], v[104:107], v[24:27]\nds_read_b64_tr_b16 v[48:49], v5 offset:768\n" \
  "ds_read_b64_tr_b16 v[50:51], v5 offset:800\nv_mfma_f32_16x16x32_f16 v[28:31], v[72:75], v[104:107], v[28:31]\n" \
  "ds_read_b128 v[80:83], v2 offset:2112\ns_waitcnt lgkmcnt(5)\nv_mfma_f32_16x16x32_f16 v[24:27], v[68:71], v[96:99], v[24:27]\n" \
  "ds_read_b128 v[112:115], v2 offset:2176\nv_pk_add_u16 v32, v32, 4 op_sel_hi:[1,0] clamp\n" \
  "v_mfma_f32_16x16x32_f16 v[28:31], v[64:67], v[96:99], v[28:31]\nds_read_b128 v[72:75], v2 offset:2240\n" \
  "v_pk_add_u16 v33, v33, 4 op_sel_hi:[1,0] clamp\nv_mfma_f32_16x16x32_f16 v[28:31], v[124:127], v[40:43], v[28:31]\n" \
  "ds_read_b128 v[104:107], v2 offset:2304\nds_write_b64 v4, v[32:33] offset:2048\nv_mfma_f32_16x16x32_f16 v[24:27], v[60:63], v[52:55], v[24:27]\n" \
  "ds_read_b128 v[64:67], v2 offset:2368\nds_read_b64 v[96:97], v6 offset:0\nds_read_b64 v[98:99], v6 offset:8\n" \
  "v_mfma_f32_16x16x32_f16 v[24:27], v[44:47], v[36:39], v[24:27]\nds_read_b128 v[124:127], v2 offset:2432\ns_waitcnt lgkmcnt(9)\n" \
  "v_mfma_f32_16x16x32_f16 v[24:27], v[88:91], v[56:59], v[24:27]\nds_read_b128 v[40:43], v2 offset:2496\n" \
  "v_mfma_f32_16x16x32_f16 v[24:27], v[48:51], v[120:123], v[24:27]\ns_waitcnt lgkmcnt(8)\n" \
  "v_mfma_f32_16x16x32_f16 v[28:31], v[116:119], v[80:83], v[28:31]\nds_read_b64 v[116:117], v6 offset:64\nds_read_b64 v[118:119], v6 offset:72\n" \
  "v_mfma_f32_16x16x32_f16 v[28:31], v[108:111], v[112:115], v[28:31]\nds_read_b64_tr_b16 v[80:81], v5 offset:1024\n" \
  "ds_read_b64_tr_b16 v[82:83], v5 offset:1056\ns_waitcnt lgkmcnt(10)\nv_mfma_f32_16x16x32_f16 v[28:31], v[100:103], v[72:75], v[28:31]\n" \
  "v_cvt_pkrtz_f16_f32 v32, v24, v25\nv_cvt_pkrtz_f16_f32 v33, v26, v27\nv_mfma_f32_16x16x32_f16 v[28:31], v[92:95], v[104:107], v[28:31]\n" \
  "v_pk_add_u16 v32, v32, 4 op_sel_hi:[1,0] clamp\ns_waitcnt lgkmcnt(5)\nv_mfma_f32_16x16x32_f16 v[28:31], v[84:87], v[64:67], v[28:31]\n" \
  "ds_read_b64_tr_b16 v[108:109], v5 offset:1280\nds_read_b64_tr_b16 v[110:111], v5 offset:1312\n" \
  "v_mfma_f32_16x16x32_f16 v[28:31], v[76:79], v[124:127], v[28:31]\nds_read_b64_tr_b16 v[112:113], v7 offset:0\n" \
  "ds_read_b64_tr_b16 v[114:115], v7 offset:64\ns_waitcnt lgkmcnt(6)\nv_mfma_f32_16x16x32_f16 v[28:31], v[68:71], v[40:43], v[28:31]\n" \
  "ds_read2_b32 v[100:101], v8 offset0:48 offset1:49\nds_read2_b32 v[102:103], v8 offset0:50 offset1:51\n" \
  "v_mfma_f32_16x16x32_f16 v[28:31], v[60:63], v[96:99], v[28:31]\nv_pk_add_u16 v33, v33, 4 op_sel_hi:[1,0] clamp\n" \
  "ds_write_b64 v4, v[32:33] offset:3072\nv_mfma_f32_16x16x32_f16 v[28:31], v[44:47], v[116:119], v[28:31]\n" \
  "v_mfma_f32_16x16x32_f16 v[28:31], v[88:91], v[52:55], v[28:31]\nv_mfma_f32_16x16x32_f16 v[28:31], v[48:51], v[36:39], v[28:31]\n" \
  "s_waitcnt lgkmcnt(5)\nv_mfma_f32_16x16x32_f16 v[28:31], v[80:83], v[56:59], v[28:31]\n" \
  "v_mfma_f32_16x16x32_f16 v[28:31], v[108:111], v[120:123], v[28:31]\ns_waitcnt lgkmcnt(1)\n" \
  "v_mfma_f32_16x16x32_f16 v[28:31], v[112:115], v[100:103], v[28:31]\ns_nop 7\nv_cvt_pkrtz_f16_f32 v32, v28, v29\nv_cvt_pkrtz_f16_f32 v33, v30, v31\n" \
  "v_pk_add_u16 v32, v32, 4 op_sel_hi:[1,0] clamp\nv_pk_add_u16 v33, v33, 4 op_sel_hi:[1,0] clamp\nds_write_b64 v4, v[32:33] offset:4096\n" \
  "s_setprio 0\ns_setpc_b64 s[90:91]\n21:\ns_movk_i32 s84, 0x4000\n23:\nds_read_b32 v35, v9 offset:32\ns_sub_u32 s84, s84, 1\ns_cbranch_scc1 22b\n" \
  "s_waitcnt lgkmcnt(0)\nv_readfirstlane_b32 s85, v35\ns_cmp_lt_u32 s85, 4\ns_cbranch_scc1 23b\ns_branch 22b\n1:\n" \
  "ds_read_b64 v[124:125], v3 offset:1088\nds_read_b64 v[126:127], v3 offset:1096\nds_read_b64 v[120:121], v3 offset:0\n" \
  "ds_read_b64 v[122:123], v3 offset:8\nds_read_b64 v[116:117], v3 offset:64\nds_read_b64 v[118:119], v3 offset:72\n" \
  "ds_read_b64 v[112:113], v3 offset:128\nds_read_b64 v[114:115], v3 offset:136\nds_read_b64 v[108:109], v3 offset:192\n" \
  "ds_read_b64 v[110:111], v3 offset:200\nds_read_b64 v[104:105], v3 offset:256\nds_read_b64 v[106:107], v3 offset:264\n" \
  "ds_read_b64 v[100:101], v3 offset:320\nds_read_b64 v[102:103], v3 offset:328\nds_read_b64 v[96:97], v3 offset:384\n" \
  "ds_read_b64 v[98:99], v3 offset:392\nds_read_b64 v[92:93], v3 offset:448\nds_read_b64 v[94:95], v3 offset:456\n" \
  "ds_read_b64 v[88:89], v3 offset:512\nds_read_b64 v[90:91], v3 offset:520\nds_read_b64 v[84:85], v3 offset:576\n" \
  "ds_read_b64 v[86:87], v3 offset:584\nds_read_b64 v[80:81], v3 offset:640\nds_read_b64 v[82:83], v3 offset:648\n" \
  "ds_read_b64 v[76:77], v3 offset:704\nds_read_b64 v[78:79], v3 offset:712\nds_read_b64 v[72:73], v3 offset:768\n" \
  "ds_read_b64 v[74:75], v3 offset:776\nds_read_b64 v[68:69], v3 offset:832\nds_read_b64 v[70:71], v3 offset:840\n" \
  "ds_read_b64 v[64:65], v3 offset:896\nds_read_b64 v[66:67], v3 offset:904\nds_read_b64 v[60:61], v3 offset:960\n" \
  "ds_read_b64 v[62:63], v3 offset:968\nds_read_b64 v[56:57], v3 offset:1024\nds_read_b64 v[58:59], v3 offset:1032\n" \
  "ds_read_b64 v[52:53], v6 offset:0\nds_read_b64 v[54:55], v6 offset:8\nds_read_b64 v[48:49], v6 offset:64\nds_read_b64 v[50:51], v6 offset:72\n" \
  "s_waitcnt lgkmcnt(0)\ns_barrier\nds_read_b128 v[28:31], v2 offset:960\nds_read_b128 v[32:35], v2 offset:1472\n" \
  "ds_read_b128 v[36:39], v2 offset:1984\nds_read_b128 v[40:43], v2 offset:2048\nds_read_b128 v[44:47], v2 offset:2112\ns_waitcnt lgkmcnt(3)\n" \
  "v_mfma_f32_16x16x32_f16 v[12:15], v[124:127], v[28:31], 0\ns_setprio 1\nds_read_b128 v[28:31], v2 offset:2176\n" \
  "v_mfma_f32_16x16x32_f16 v[16:19], v[124:127], v[32:35], 0\nds_read_b128 v[32:35], v2 offset:2240\ns_waitcnt lgkmcnt(3)\n" \
  "v_mfma_f32_16x16x32_f16 v[12:15], v[120:123], v[36:39], v[12:15]\nv_mfma_f32_16x16x32_f16 v[20:23], v[124:127], v[36:39], 0\n" \
  "ds_read_b128 v[124:127], v2 offset:2304\nds_read_b128 v[36:39], v2 offset:2368\nv_mfma_f32_16x16x32_f16 v[12:15], v[116:119], v[40:43], v[12:15]\n" \
  "ds_read_b128 v[40:43], v2 offset:2432\ns_waitcnt lgkmcnt(4)\nv_mfma_f32_16x16x32_f16 v[12:15], v[112:115], v[44:47], v[12:15]\n" \
  "ds_read_b128 v[44:47], v2 offset:2496\nv_mfma_f32_16x16x32_f16 v[12:15], v[108:111], v[28:31], v[12:15]\nds_read_b128 v[28:31], v2 offset:2560\n" \
  "s_waitcnt lgkmcnt(4)\nv_mfma_f32_16x16x32_f16 v[12:15], v[104:107], v[32:35], v[12:15]\nds_read_b128 v[32:35], v2 offset:2624\n" \
  "v_mfma_f32_16x16x32_f16 v[12:15], v[100:103], v[124:127], v[12:15]\ns_waitcnt lgkmcnt(3)\n" \
  "v_mfma_f32_16x16x32_f16 v[12:15], v[96:99], v[36:39], v[12:15]\nds_read_b128 v[124:127], v2 offset:2688\n" \
  "v_mfma_f32_16x16x32_f16 v[12:15], v[92:95], v[40:43], v[12:15]\ns_waitcnt lgkmcnt(3)\n" \
  "v_mfma_f32_16x16x32_f16 v[12:15], v[88:91], v[44:47], v[12:15]\nds_read_b128 v[36:39], v2 offset:2752\n" \
  "v_mfma_f32_16x16x32_f16 v[16:19], v[120:123], v[44:47], v[16:19]\ns_waitcnt lgkmcnt(3)\n" \
  "v_mfma_f32_16x16x32_f16 v[12:15], v[84:87], v[28:31], v[12:15]\nds_read_b128 v[40:43], v2 offset:2816\n" \
  "v_mfma_f32_16x16x32_f16 v[16:19], v[116:119], v[28:31], v[16:19]\ns_waitcnt lgkmcnt(3)\n" \
  "v_mfma_f32_16x16x32_f16 v[12:15], v[80:83], v[32:35], v[12:15]\nds_read_b128 v[44:47], v2 offset:2880\n" \
  "v_mfma_f32_16x16x32_f16 v[16:19], v[112:115], v[32:35], v[16:19]\ns_waitcnt lgkmcnt(3)\n" \
  "v_mfma_f32_16x16x32_f16 v[12:15], v[76:79], v[124:127], v[12:15]\nds_read_b128 v[28:31], v2 offset:2944\n" \
  "v_mfma_f32_16x16x32_f16 v[16:19], v[108:111], v[124:127], v[16:19]\ns_waitcnt lgkmcnt(3)\n" \
  "v_mfma_f32_16x16x32_f16 v[12:15], v[72:75], v[36:39], v[12:15]\nds_read_b128 v[32:35], v2 offset:3008\n" \
  "v_mfma_f32_16x16x32_f16 v[16:19], v[104:107], v[36:39], v[16:19]\ns_waitcnt lgkmcnt(3)\n" \
  "v_mfma_f32_16x16x32_f16 v[12:15], v[68:71], v[40:43], v[12:15]\nv_mfma_f32_16x16x32_f16 v[16:19], v[100:103], v[40:43], v[16:19]\n" \
  "ds_read_b128 v[124:127], v2 offset:3072\ns_waitcnt lgkmcnt(3)\nv_mfma_f32_16x16x32_f16 v[12:15], v[64:67], v[44:47], v[12:15]\n" \
  "v_mfma_f32_16x16x32_f16 v[16:19], v[96:99], v[44:47], v[16:19]\nds_read_b128 v[36:39], v2 offset:3136\ns_waitcnt lgkmcnt(3)\n" \
  "v_mfma_f32_16x16x32_f16 v[12:15], v[60:63], v[28:31], v[12:15]\nds_read_b64 v[40:41], v6 offset:128\nds_read_b64 v[42:43], v6 offset:136\n" \
  "v_mfma_f32_16x16x32_f16 v[16:19], v[92:95], v[28:31], v[16:19]\nds_read_b128 v[44:47], v2 offset:3200\ns_waitcnt lgkmcnt(4)\n" \
  "v_mfma_f32_16x16x32_f16 v[12:15], v[56:59], v[32:35], v[12:15]\nv_mfma_f32_16x16x32_f16 v[16:19], v[88:91], v[32:35], v[16:19]\n" \
  "ds_read_b128 v[28:31], v2 offset:3264\nv_mfma_f32_16x16x32_f16 v[20:23], v[120:123], v[32:35], v[20:23]\nds_read_b64 v[120:121], v6 offset:192\n" \
  "ds_read_b64 v[122:123], v6 offset:200\nv_mfma_f32_16x16x32_f16 v[16:19], v[84:87], v[124:127], v[16:19]\nds_read_b128 v[32:35], v2 offset:3328\n" \
  "v_mfma_f32_16x16x32_f16 v[20:23], v[116:119], v[124:127], v[20:23]\nds_read_b64 v[116:117], v6 offset:256\nds_read_b64 v[118:119], v6 offset:264\n" \
  "s_waitcnt lgkmcnt(6)\nv_mfma_f32_16x16x32_f16 v[16:19], v[80:83], v[36:39], v[16:19]\nds_read_b128 v[124:127], v2 offset:3392\n" \
  "v_mfma_f32_16x16x32_f16 v[20:23], v[112:115], v[36:39], v[20:23]\nds_read_b64 v[112:113], v6 offset:320\nds_read_b64 v[114:115], v6 offset:328\n" \
  "v_mfma_f32_16x16x32_f16 v[16:19], v[76:79], v[44:47], v[16:19]\nds_read_b128 v[36:39], v2 offset:3456\n" \
  "v_mfma_f32_16x16x32_f16 v[20:23], v[108:111], v[44:47], v[20:23]\ns_waitcnt lgkmcnt(6)\n" \
  "v_mfma_f32_16x16x32_f16 v[16:19], v[72:75], v[28:31], v[16:19]\nds_read_b128 v[108:111], v2 offset:3520\n" \
  "v_mfma_f32_16x16x32_f16 v[20:23], v[104:107], v[28:31], v[20:23]\nv_mfma_f32_16x16x32_f16 v[16:19], v[68:71], v[32:35], v[16:19]\n" \
  "ds_read_b128 v[44:47], v2 offset:3584\nv_mfma_f32_16x16x32_f16 v[20:23], v[100:103], v[32:35], v[20:23]\nds_read_b128 v[104:107], v2 offset:3648\n" \
  "ds_read_b32 v27, v9 offset:32\ns_waitcnt lgkmcnt(4)\nv_mfma_f32_16x16x32_f16 v[16:19], v[64:67], v[124:127], v[16:19]\n" \
  "ds_read_b128 v[28:31], v2 offset:3712\nv_mfma_f32_16x16x32_f16 v[20:23], v[96:99], v[124:127], v[20:23]\nds_read_b128 v[100:103], v2 offset:3776\n" \
  "ds_read2_b32 v[32:33], v8 offset0:32 offset1:33\nds_read2_b32 v[34:35], v8 offset0:34 offset1:35\n" \
  "v_mfma_f32_16x16x32_f16 v[16:19], v[60:63], v[36:39], v[16:19]\nds_read_b128 v[96:99], v2 offset:3840\n" \
  "v_mfma_f32_16x16x32_f16 v[20:23], v[92:95], v[36:39], v[20:23]\nds_read_b128 v[124:127], v2 offset:3904\ns_waitcnt lgkmcnt(8)\n" \
  "v_mfma_f32_16x16x32_f16 v[16:19], v[56:59], v[108:111], v[16:19]\nds_read_b128 v[92:95], v2 offset:3968\ns_waitcnt lgkmcnt(7)\n" \
  "v_readfirstlane_b32 s85, v27\ns_cmp_lt_u32 s85, 4\ns_cbranch_scc1 31f\n32:\nv_mfma_f32_16x16x32_f16 v[20:23], v[88:91], v[108:111], v[20:23]\n" \
  "ds_read_b128 v[36:39], v2 offset:4032\nds_read_b64_tr_b16 v[88:89], v5 offset:512\nds_read_b64_tr_b16 v[90:91], v5 offset:544\n" \
  "v_mfma_f32_16x16x32_f16 v[20:23], v[84:87], v[44:47], v[20:23]\nds_read_b64_tr_b16 v[108:109], v5 offset:768\n" \
  "ds_read_b64_tr_b16 v[110:111], v5 offset:800\nv_mfma_f32_16x16x32_f16 v[20:23], v[80:83], v[104:107], v[20:23]\n" \
  "ds_read_b64_tr_b16 v[84:85], v5 offset:1024\nds_read_b64_tr_b16 v[86:87], v5 offset:1056\ns_waitcnt lgkmcnt(12)\n" \
  "v_mfma_f32_16x16x32_f16 v[20:23], v[76:79], v[28:31], v[20:23]\nds_read_b64_tr_b16 v[44:45], v5 offset:1280\n" \
  "ds_read_b64_tr_b16 v[46:47], v5 offset:1312\nv_mfma_f32_16x16x32_f16 v[20:23], v[72:75], v[100:103], v[20:23]\ns_waitcnt lgkmcnt(10)\n" \
  "v_mfma_f32_16x16x32_f16 v[20:23], v[68:71], v[96:99], v[20:23]\nds_read_b64_tr_b16 v[80:81], v5 offset:1536\n" \
  "ds_read_b64_tr_b16 v[82:83], v5 offset:1568\nv_mfma_f32_16x16x32_f16 v[20:23], v[64:67], v[124:127], v[20:23]\n" \
  "ds_read_b64_tr_b16 v[104:105], v5 offset:1792\nds_read_b64_tr_b16 v[106:107], v5 offset:1824\ns_waitcnt lgkmcnt(12)\n" \
  "v_mfma_f32_16x16x32_f16 v[20:23], v[60:63], v[92:95], v[20:23]\nds_read_b64_tr_b16 v[76:77], v5 offset:2048\n" \
  "ds_read_b64_tr_b16 v[78:79], v5 offset:2080\nv_mfma_f32_16x16x32_f16 v[20:23], v[56:59], v[36:39], v[20:23]\ns_waitcnt lgkmcnt(10)\n" \
  "v_mfma_f32_16x16x32_f16 v[12:15], v[88:91], v[52:55], v[12:15]\nds_read_b64_tr_b16 v[28:29], v5 offset:2304\n" \
  "ds_read_b64_tr_b16 v[30:31], v5 offset:2336\nv_mfma_f32_16x16x32_f16 v[12:15], v[108:111], v[48:51], v[12:15]\n" \
  "ds_read_b64_tr_b16 v[72:73], v5 offset:2560\nds_read_b64_tr_b16 v[74:75], v5 offset:2592\ns_waitcnt lgkmcnt(10)\n" \
  "v_mfma_f32_16x16x32_f16 v[12:15], v[84:87], v[40:43], v[12:15]\nds_read_b64_tr_b16 v[100:101], v5 offset:2816\n" \
  "ds_read_b64_tr_b16 v[102:103], v5 offset:2848\nv_mfma_f32_16x16x32_f16 v[16:19], v[84:87], v[52:55], v[16:19]\n" \
  "ds_read_b64_tr_b16 v[68:69], v7 offset:0\nds_read_b64_tr_b16 v[70:71], v7 offset:64\n" \
  "v_mfma_f32_16x16x32_f16 v[12:15], v[44:47], v[120:123], v[12:15]\nv_mfma_f32_16x16x32_f16 v[16:19], v[44:47], v[48:51], v[16:19]\n" \
  "s_waitcnt lgkmcnt(10)\nv_mfma_f32_16x16x32_f16 v[12:15], v[80:83], v[116:119], v[12:15]\nds_read2_b32 v[96:97], v8 offset0:16 offset1:17\n" \
  "ds_read2_b32 v[98:99], v8 offset0:18 offset1:19\nv_mfma_f32_16x16x32_f16 v[16:19], v[80:83], v[40:43], v[16:19]\n" \
  "ds_read2_b32 v[64:65], v8 offset0:0 offset1:1\nds_read2_b32 v[66:67], v8 offset0:2 offset1:3\n" \
  "v_mfma_f32_16x16x32_f16 v[20:23], v[80:83], v[52:55], v[20:23]\nv_mfma_f32_16x16x32_f16 v[12:15], v[104:107], v[112:115], v[12:15]\n" \
  "v_mfma_f32_16x16x32_f16 v[16:19], v[104:107], v[120:123], v[16:19]\nv_mfma_f32_16x16x32_f16 v[20:23], v[104:107], v[48:51], v[20:23]\n" \
  "s_waitcnt lgkmcnt(10)\nv_mfma_f32_16x16x32_f16 v[16:19], v[76:79], v[116:119], v[16:19]\nds_read_b64_tr_b16 v[124:125], v7 offset:512\n" \
  "ds_read_b64_tr_b16 v[126:127], v7 offset:576\nv_mfma_f32_16x16x32_f16 v[20:23], v[76:79], v[40:43], v[20:23]\n" \
  "ds_read2_b32 v[60:61], v8 offset0:48 offset1:49\nds_read2_b32 v[62:63], v8 offset0:50 offset1:51\n" \
  "v_mfma_f32_16x16x32_f16 v[16:19], v[28:31], v[112:115], v[16:19]\nv_mfma_f32_16x16x32_f16 v[20:23], v[28:31], v[120:123], v[20:23]\n" \
  "s_waitcnt lgkmcnt(10)\nv_mfma_f32_16x16x32_f16 v[20:23], v[72:75], v[116:119], v[20:23]\nds_read_b64_tr_b16 v[92:93], v7 offset:1024\n" \
  "ds_read_b64_tr_b16 v[94:95], v7 offset:1088\nv_mfma_f32_16x16x32_f16 v[20:23], v[100:103], v[112:115], v[20:23]\n" \
  "ds_read_b64_tr_b16 v[56:57], v7 offset:1536\nds_read_b64_tr_b16 v[58:59], v7 offset:1600\ns_waitcnt lgkmcnt(10)\n" \
  "v_mfma_f32_16x16x32_f16 v[12:15], v[68:71], v[32:35], v[12:15]\nv_mfma_f32_16x16x32_f16 v[16:19], v[68:71], v[96:99], v[16:19]\n" \
  "s_waitcnt lgkmcnt(6)\nv_mfma_f32_16x16x32_f16 v[20:23], v[68:71], v[64:67], v[20:23]\ns_waitcnt lgkmcnt(2)\n" \
  "v_mfma_f32_16x16x32_f16 v[12:15], v[124:127], v[60:63], v[12:15]\nv_mfma_f32_16x16x32_f16 v[16:19], v[124:127], v[32:35], v[16:19]\n" \
  "v_mfma_f32_16x16x32_f16 v[20:23], v[124:127], v[96:99], v[20:23]\nv_mfma_f32_16x16x32_f16 v[16:19], v[92:95], v[60:63], v[16:19]\n" \
  "v_mfma_f32_16x16x32_f16 v[20:23], v[92:95], v[32:35], v[20:23]\ns_waitcnt lgkmcnt(0)\n" \
  "v_mfma_f32_16x16x32_f16 v[20:23], v[56:59], v[60:63], v[20:23]\ns_nop 1\nv_cvt_pkrtz_f16_f32 v24, v12, v13\nv_cvt_pkrtz_f16_f32 v25, v14, v15\n" \
  "v_pk_add_u16 v24, v24, 4 op_sel_hi:[1,0] clamp\nv_pk_add_u16 v25, v25, 4 op_sel_hi:[1,0] clamp\nds_write_b64 v4, v[24:25] offset:5120\n" \
  "v_cvt_pkrtz_f16_f32 v24, v16, v17\nv_cvt_pkrtz_f16_f32 v25, v18, v19\nv_pk_add_u16 v24, v24, 4 op_sel_hi:[1,0] clamp\n" \
  "v_pk_add_u16 v25, v25, 4 op_sel_hi:[1,0] clamp\nds_write_b64 v4, v[24:25] offset:6144\nv_cvt_pkrtz_f16_f32 v24, v20, v21\n" \
  "v_cvt_pkrtz_f16_f32 v25, v22, v23\nv_pk_add_u16 v24, v24, 4 op_sel_hi:[1,0] clamp\nv_pk_add_u16 v25, v25, 4 op_sel_hi:[1,0] clamp\n" \
  "ds_write_b64 v4, v[24:25] offset:7168\ns_setprio 0\ns_setpc_b64 s[90:91]\n31:\ns_movk_i32 s84, 0x4000\n33:\nds_read_b32 v27, v9 offset:32\n" \
  "s_sub_u32 s84, s84, 1\ns_cbranch_scc1 32b\ns_waitcnt lgkmcnt(0)\nv_readfirstlane_b32 s85, v27\ns_cmp_lt_u32 s85, 4\ns_cbranch_scc1 33b\n" \
  "s_branch 32b\n9:\ns_cmp_eq_u32 s89, 3\ns_cbranch_scc1 3f\ns_cmp_eq_u32 s89, 4\ns_cbranch_scc1 4f\ns_cmp_eq_u32 s89, 5\ns_cbranch_scc1 5f\n" \
  "ds_read_b64 v[124:125], v3 offset:576\nds_read_b64 v[126:127], v3 offset:584\nds_read_b64 v[120:121], v3 offset:64\n" \
  "ds_read_b64 v[122:123], v3 offset:72\nds_read_b64 v[116:117], v3 offset:640\nds_read_b64 v[118:119], v3 offset:648\n" \
  "ds_read_b64 v[112:113], v3 offset:128\nds_read_b64 v[114:115], v3 offset:136\nds_read_b64 v[108:109], v3 offset:704\n" \
  "ds_read_b64 v[110:111], v3 offset:712\nds_read_b64 v[104:105], v3 offset:192\nds_read_b64 v[106:107], v3 offset:200\n" \
  "ds_read_b64 v[100:101], v3 offset:768\nds_read_b64 v[102:103], v3 offset:776\nds_read_b64 v[96:97], v3 offset:256\n" \
  "ds_read_b64 v[98:99], v3 offset:264\nds_read_b64 v[92:93], v3 offset:832\nds_read_b64 v[94:95], v3 offset:840\n" \
  "ds_read_b64 v[88:89], v3 offset:320\nds_read_b64 v[90:91], v3 offset:328\nds_read_b64 v[84:85], v3 offset:896\n" \
  "ds_read_b64 v[86:87], v3 offset:904\nds_read_b64 v[80:81], v3 offset:384\nds_read_b64 v[82:83], v3 offset:392\n" \
  "ds_read_b64 v[76:77], v3 offset:960\nds_read_b64 v[78:79], v3 offset:968\nds_read_b64 v[72:73], v3 offset:448\n" \
  "ds_read_b64 v[74:75], v3 offset:456\nds_read_b64 v[68:69], v3 offset:1024\nds_read_b64 v[70:71], v3 offset:1032\n" \
  "ds_read_b64 v[64:65], v3 offset:512\nds_read_b64 v[66:67], v3 offset:520\ns_waitcnt lgkmcnt(0)\ns_barrier\nds_read_b128 v[24:27], v2 offset:0\n" \
  "ds_read_b128 v[28:31], v2 offset:64\nds_read_b128 v[32:35], v2 offset:128\nds_read_b128 v[36:39], v2 offset:192\ns_waitcnt lgkmcnt(3)\n" \
  "v_mfma_f32_16x16x32_f16 v[12:15], v[124:127], v[24:27], 0\ns_setprio 1\nds_read_b128 v[40:43], v2 offset:256\n" \
  "v_mfma_f32_16x16x32_f16 v[16:19], v[120:123], v[24:27], 0\ns_waitcnt lgkmcnt(3)\nv_mfma_f32_16x16x32_f16 v[12:15], v[116:119], v[28:31], v[12:15]\n" \
  "ds_read_b128 v[44:47], v2 offset:320\nv_mfma_f32_16x16x32_f16 v[16:19], v[112:115], v[28:31], v[16:19]\ns_waitcnt lgkmcnt(3)\n" \
  "v_mfma_f32_16x16x32_f16 v[12:15], v[108:111], v[32:35], v[12:15]\nds_read_b128 v[48:51], v2 offset:384\n" \
  "v_mfma_f32_16x16x32_f16 v[16:19], v[104:107], v[32:35], v[16:19]\ns_waitcnt lgkmcnt(3)\n" \
  "v_mfma_f32_16x16x32_f16 v[12:15], v[100:103], v[36:39], v[12:15]\nds_read_b128 v[52:55], v2 offset:448\n" \
  "v_mfma_f32_16x16x32_f16 v[16:19], v[96:99], v[36:39], v[16:19]\ns_waitcnt lgkmcnt(3)\n" \
  "v_mfma_f32_16x16x32_f16 v[12:15], v[92:95], v[40:43], v[12:15]\nds_read_b128 v[56:59], v2 offset:512\n" \
  "v_mfma_f32_16x16x32_f16 v[16:19], v[88:91], v[40:43], v[16:19]\nds_read_b128 v[60:63], v2 offset:576\ns_waitcnt lgkmcnt(3)\n" \
  "v_mfma_f32_16x16x32_f16 v[12:15], v[84:87], v[44:47], v[12:15]\nds_read_b128 v[120:123], v2 offset:640\n" \
  "v_mfma_f32_16x16x32_f16 v[16:19], v[80:83], v[44:47], v[16:19]\nds_read_b128 v[24:27], v2 offset:704\n" \
  "v_mfma_f32_16x16x32_f16 v[12:15], v[76:79], v[48:51], v[12:15]\nds_read_b128 v[112:115], v2 offset:768\n" \
  "v_mfma_f32_16x16x32_f16 v[16:19], v[72:75], v[48:51], v[16:19]\nds_read_b128 v[28:31], v2 offset:832\ns_waitcnt lgkmcnt(5)\n" \
  "v_mfma_f32_16x16x32_f16 v[12:15], v[68:71], v[52:55], v[12:15]\nds_read_b128 v[104:107], v2 offset:896\n" \
  "v_mfma_f32_16x16x32_f16 v[16:19], v[64:67], v[52:55], v[16:19]\nds_read_b128 v[32:35], v2 offset:960\n" \
  "v_mfma_f32_16x16x32_f16 v[16:19], v[124:127], v[56:59], v[16:19]\ns_waitcnt lgkmcnt(5)\n" \
  "v_mfma_f32_16x16x32_f16 v[16:19], v[116:119], v[60:63], v[16:19]\nv_mfma_f32_16x16x32_f16 v[16:19], v[108:111], v[120:123], v[16:19]\n" \
  "s_waitcnt lgkmcnt(3)\nv_mfma_f32_16x16x32_f16 v[16:19], v[100:103], v[24:27], v[16:19]\nv_cvt_pkrtz_f16_f32 v20, v12, v13\n" \
  "v_cvt_pkrtz_f16_f32 v21, v14, v15\nv_mfma_f32_16x16x32_f16 v[16:19], v[92:95], v[112:115], v[16:19]\n" \
  "v_pk_add_u16 v20, v20, 4 op_sel_hi:[1,0] clamp\ns_waitcnt lgkmcnt(2)\nv_mfma_f32_16x16x32_f16 v[16:19], v[84:87], v[28:31], v[16:19]\n" \
  "v_pk_add_u16 v21, v21, 4 op_sel_hi:[1,0] clamp\ns_waitcnt lgkmcnt(1)\nv_mfma_f32_16x16x32_f16 v[16:19], v[76:79], v[104:107], v[16:19]\n" \
  "ds_write_b64 v4, v[20:21] offset:0\ns_waitcnt lgkmcnt(1)\nv_mfma_f32_16x16x32_f16 v[16:19], v[68:71], v[32:35], v[16:19]\ns_nop 7\n" \
  "v_cvt_pkrtz_f16_f32 v20, v16, v17\nv_cvt_pkrtz_f16_f32 v21, v18, v19\nv_pk_add_u16 v20, v20, 4 op_sel_hi:[1,0] clamp\n" \
  "v_pk_add_u16 v21, v21, 4 op_sel_hi:[1,0] clamp\nds_write_b64 v4, v[20:21] offset:1024\ns_setprio 0\ns_setpc_b64 s[90:91]\n3:\n" \
  "ds_read_b64 v[124:125], v3 offset:0\nds_read_b64 v[126:127], v3 offset:8\nds_read_b64 v[120:121], v3 offset:64\n" \
  "ds_read_b64 v[122:123], v3 offset:72\nds_read_b64 v[116:117], v3 offset:128\nds_read_b64 v[118:119], v3 offset:136\n" \
  "ds_read_b64 v[112:113], v3 offset:192\nds_read_b64 v[114:115], v3 offset:200\nds_read_b64 v[108:109], v3 offset:256\n" \
  "ds_read_b64 v[110:111], v3 offset:264\nds_read_b64 v[104:105], v3 offset:320\nds_read_b64 v[106:107], v3 offset:328\n" \
  "ds_read_b64 v[100:101], v3 offset:384\nds_read_b64 v[102:103], v3 offset:392\nds_read_b64 v[96:97], v3 offset:448\n" \
  "ds_read_b64 v[98:99], v3 offset:456\nds_read_b64 v[92:93], v3 offset:512\nds_read_b64 v[94:95], v3 offset:520\n" \
  "ds_read_b64 v[88:89], v3 offset:576\nds_read_b64 v[90:91], v3 offset:584\nds_read_b64 v[84:85], v3 offset:640\n" \
  "ds_read_b64 v[86:87], v3 offset:648\nds_read_b64 v[80:81], v3 offset:704\nds_read_b64 v[82:83], v3 offset:712\n" \
  "ds_read_b64 v[76:77], v3 offset:768\nds_read_b64 v[78:79], v3 offset:776\nds_read_b64 v[72:73], v3 offset:832\n" \
  "ds_read_b64 v[74:75], v3 offset:840\nds_read_b64 v[68:69], v3 offset:896\nds_read_b64 v[70:71], v3 offset:904\n" \
  "ds_read_b64 v[64:65], v3 offset:960\nds_read_b64 v[66:67], v3 offset:968\nds_read_b64 v[60:61], v3 offset:1024\n" \
  "ds_read_b64 v[62:63], v3 offset:1032\nds_read_b64 v[56:57], v6 offset:256\nds_read_b64 v[58:59], v6 offset:264\n" \
  "ds_read_b64 v[52:53], v6 offset:128\nds_read_b64 v[54:55], v6 offset:136\nds_read_b64 v[48:49], v6 offset:320\n" \
  "ds_read_b64 v[50:51], v6 offset:328\nds_read_b64 v[44:45], v6 offset:192\nds_read_b64 v[46:47], v6 offset:200\ns_waitcnt lgkmcnt(0)\ns_barrier\n" \
  "ds_read_b128 v[24:27], v2 offset:448\nds_read_b128 v[28:31], v2 offset:512\nds_read_b128 v[32:35], v2 offset:576\n" \
  "ds_read_b128 v[36:39], v2 offset:640\nds_read_b128 v[40:43], v2 offset:704\ns_waitcnt lgkmcnt(3)\n" \
  "v_mfma_f32_16x16x32_f16 v[12:15], v[124:127], v[24:27], 0\ns_setprio 1\nds_read_b128 v[24:27], v2 offset:768\n" \
  "v_mfma_f32_16x16x32_f16 v[12:15], v[120:123], v[28:31], v[12:15]\nds_read_b128 v[28:31], v2 offset:832\ns_waitcnt lgkmcnt(3)\n" \
  "v_mfma_f32_16x16x32_f16 v[12:15], v[116:119], v[32:35], v[12:15]\nds_read_b128 v[32:35], v2 offset:896\n" \
  "v_mfma_f32_16x16x32_f16 v[12:15], v[112:115], v[36:39], v[12:15]\nds_read_b128 v[36:39], v2 offset:960\ns_waitcnt lgkmcnt(3)\n" \
  "v_mfma_f32_16x16x32_f16 v[12:15], v[108:111], v[40:43], v[12:15]\nds_read_b128 v[40:43], v2 offset:1024\n" \
  "v_mfma_f32_16x16x32_f16 v[12:15], v[104:107], v[24:27], v[12:15]\nds_read_b128 v[24:27], v2 offset:1088\ns_waitcnt lgkmcnt(3)\n" \
  "v_mfma_f32_16x16x32_f16 v[12:15], v[100:103], v[28:31], v[12:15]\nds_read_b128 v[28:31], v2 offset:1152\n" \
  "v_mfma_f32_16x16x32_f16 v[12:15], v[96:99], v[32:35], v[12:15]\ns_waitcnt lgkmcnt(3)\n" \
  "v_mfma_f32_16x16x32_f16 v[12:15], v[92:95], v[36:39], v[12:15]\nds_read_b128 v[32:35], v2 offset:1216\n" \
  "v_mfma_f32_16x16x32_f16 v[16:19], v[124:127], v[36:39], 0\ns_waitcnt lgkmcnt(3)\nv_mfma_f32_16x16x32_f16 v[12:15], v[88:91], v[40:43], v[12:15]\n" \
  "ds_read_b128 v[124:127], v2 offset:1280\nv_mfma_f32_16x16x32_f16 v[16:19], v[120:123], v[40:43], v[16:19]\ns_waitcnt lgkmcnt(3)\n" \
  "v_mfma_f32_16x16x32_f16 v[12:15], v[84:87], v[24:27], v[12:15]\nds_read_b128 v[36:39], v2 offset:1344\n" \
  "v_mfma_f32_16x16x32_f16 v[16:19], v[116:119], v[24:27], v[16:19]\ns_waitcnt lgkmcnt(3)\n" \
  "v_mfma_f32_16x16x32_f16 v[12:15], v[80:83], v[28:31], v[12:15]\nds_read_b128 v[120:123], v2 offset:1408\n" \
  "v_mfma_f32_16x16x32_f16 v[16:19], v[112:115], v[28:31], v[16:19]\ns_waitcnt lgkmcnt(3)\n" \
  "v_mfma_f32_16x16x32_f16 v[12:15], v[76:79], v[32:35], v[12:15]\nds_read_b128 v[40:43], v2 offset:1472\n" \
  "v_mfma_f32_16x16x32_f16 v[16:19], v[108:111], v[32:35], v[16:19]\ns_waitcnt lgkmcnt(3)\n" \
  "v_mfma_f32_16x16x32_f16 v[12:15], v[72:75], v[124:127], v[12:15]\nds_read_b128 v[116:119], v2 offset:1536\n" \
  "v_mfma_f32_16x16x32_f16 v[16:19], v[104:107], v[124:127], v[16:19]\nds_read_b128 v[24:27], v2 offset:1600\ns_waitcnt lgkmcnt(3)\n" \
  "v_mfma_f32_16x16x32_f16 v[12:15], v[68:71], v[36:39], v[12:15]\nds_read_b128 v[112:115], v2 offset:1664\nds_read_b32 v23, v9 offset:32\n" \
  "v_mfma_f32_16x16x32_f16 v[16:19], v[100:103], v[36:39], v[16:19]\nds_read_b128 v[28:31], v2 offset:1728\n" \
  "v_mfma_f32_16x16x32_f16 v[12:15], v[64:67], v[120:123], v[12:15]\nds_read_b128 v[108:111], v2 offset:1792\n" \
  "v_mfma_f32_16x16x32_f16 v[16:19], v[96:99], v[120:123], v[16:19]\nds_read_b128 v[32:35], v2 offset:1856\ns_waitcnt lgkmcnt(6)\n" \
  "v_mfma_f32_16x16x32_f16 v[12:15], v[60:63], v[40:43], v[12:15]\nds_read_b128 v[104:107], v2 offset:1920\n" \
  "v_mfma_f32_16x16x32_f16 v[16:19], v[92:95], v[40:43], v[16:19]\nds_read_b128 v[124:127], v2 offset:1984\ns_waitcnt lgkmcnt(5)\n" \
  "v_readfirstlane_b32 s85, v23\ns_cmp_lt_u32 s85, 3\ns_cbranch_scc1 51f\n52:\nv_mfma_f32_16x16x32_f16 v[16:19], v[88:91], v[116:119], v[16:19]\n" \
  "ds_read_b64_tr_b16 v[100:101], v5 offset:0\nds_read_b64_tr_b16 v[102:103], v5 offset:32\n" \
  "v_mfma_f32_16x16x32_f16 v[16:19], v[84:87], v[24:27], v[16:19]\nds_read_b64_tr_b16 v[36:37], v5 offset:256\n" \
  "ds_read_b64_tr_b16 v[38:39], v5 offset:288\nv_mfma_f32_16x16x32_f16 v[16:19], v[80:83], v[112:115], v[16:19]\n" \
  "ds_read_b64_tr_b16 v[96:97], v5 offset:512\nds_read_b64_tr_b16 v[98:99], v5 offset:544\ns_waitcnt lgkmcnt(9)\n" \
  "v_mfma_f32_16x16x32_f16 v[16:19], v[76:79], v[28:31], v[16:19]\nds_read_b64_tr_b16 v[120:121], v5 offset:768\n" \
  "ds_read_b64_tr_b16 v[122:123], v5 offset:800\nv_mfma_f32_16x16x32_f16 v[16:19], v[72:75], v[108:111], v[16:19]\ns_waitcnt lgkmcnt(9)\n" \
  "v_mfma_f32_16x16x32_f16 v[16:19], v[68:71], v[32:35], v[16:19]\nv_mfma_f32_16x16x32_f16 v[16:19], v[64:67], v[104:107], v[16:19]\n" \
  "s_waitcnt lgkmcnt(6)\nv_mfma_f32_16x16x32_f16 v[16:19], v[60:63], v[124:127], v[16:19]\n" \
  "v_mfma_f32_16x16x32_f16 v[12:15], v[100:103], v[56:59], v[12:15]\nv_mfma_f32_16x16x32_f16 v[16:19], v[100:103], v[52:55], v[16:19]\n" \
  "s_waitcnt lgkmcnt(2)\nv_mfma_f32_16x16x32_f16 v[12:15], v[36:39], v[48:51], v[12:15]\n" \
  "v_mfma_f32_16x16x32_f16 v[16:19], v[36:39], v[44:47], v[16:19]\nv_mfma_f32_16x16x32_f16 v[16:19], v[96:99], v[56:59], v[16:19]\n" \
  "s_waitcnt lgkmcnt(0)\nv_mfma_f32_16x16x32_f16 v[16:19], v[120:123], v[48:51], v[16:19]\ns_nop 3\nv_cvt_pkrtz_f16_f32 v20, v12, v13\n" \
  "v_cvt_pkrtz_f16_f32 v21, v14, v15\nv_pk_add_u16 v20, v20, 4 op_sel_hi:[1,0] clamp\nv_pk_add_u16 v21, v21, 4 op_sel_hi:[1,0] clamp\n" \
  "ds_write_b64 v4, v[20:21] offset:2048\nv_cvt_pkrtz_f16_f32 v20, v16, v17\nv_cvt_pkrtz_f16_f32 v21, v18, v19\n" \
  "v_pk_add_u16 v20, v20, 4 op_sel_hi:[1,0] clamp\nv_pk_add_u16 v21, v21, 4 op_sel_hi:[1,0] clamp\nds_write_b64 v4, v[20:21] offset:3072\n" \
  "s_setprio 0\ns_setpc_b64 s[90:91]\n51:\ns_movk_i32 s84, 0x4000\n53:\nds_read_b32 v23, v9 offset:32\ns_sub_u32 s84, s84, 1\ns_cbranch_scc1 52b\n" \
  "s_waitcnt lgkmcnt(0)\nv_readfirstlane_b32 s85, v23\ns_cmp_lt_u32 s85, 3\ns_cbranch_scc1 53b\ns_branch 52b\n4:\n" \
  "ds_read_b64 v[124:125], v3 offset:1088\nds_read_b64 v[126:127], v3 offset:1096\nds_read_b64 v[120:121], v3 offset:0\n" \
  "ds_read_b64 v[122:123], v3 offset:8\nds_read_b64 v[116:117], v3 offset:64\nds_read_b64 v[118:119], v3 offset:72\n" \
  "ds_read_b64 v[112:113], v3 offset:128\nds_read_b64 v[114:115], v3 offset:136\nds_read_b64 v[108:109], v3 offset:192\n" \
  "ds_read_b64 v[110:111], v3 offset:200\nds_read_b64 v[104:105], v3 offset:256\nds_read_b64 v[106:107], v3 offset:264\n" \
  "ds_read_b64 v[100:101], v3 offset:320\nds_read_b64 v[102:103], v3 offset:328\nds_read_b64 v[96:97], v3 offset:384\n" \
  "ds_read_b64 v[98:99], v3 offset:392\nds_read_b64 v[92:93], v3 offset:448\nds_read_b64 v[94:95], v3 offset:456\n" \
  "ds_read_b64 v[88:89], v3 offset:512\nds_read_b64 v[90:91], v3 offset:520\nds_read_b64 v[84:85], v3 offset:576\n" \
  "ds_read_b64 v[86:87], v3 offset:584\nds_read_b64 v[80:81], v3 offset:640\nds_read_b64 v[82:83], v3 offset:648\n" \
  "ds_read_b64 v[76:77], v3 offset:704\nds_read_b64 v[78:79], v3 offset:712\nds_read_b64 v[72:73], v3 offset:768\n" \
  "ds_read_b64 v[74:75], v3 offset:776\nds_read_b64 v[68:69], v3 offset:832\nds_read_b64 v[70:71], v3 offset:840\n" \
  "ds_read_b64 v[64:65], v3 offset:896\nds_read_b64 v[66:67], v3 offset:904\nds_read_b64 v[60:61], v3 offset:960\n" \
  "ds_read_b64 v[62:63], v3 offset:968\nds_read_b64 v[56:57], v3 offset:1024\nds_read_b64 v[58:59], v3 offset:1032\n" \
  "ds_read_b64 v[52:53], v6 offset:0\nds_read_b64 v[54:55], v6 offset:8\nds_read_b64 v[48:49], v6 offset:64\nds_read_b64 v[50:51], v6 offset:72\n" \
  "ds_read_b64 v[44:45], v6 offset:128\nds_read_b64 v[46:47], v6 offset:136\nds_read_b64 v[40:41], v6 offset:192\n" \
  "ds_read_b64 v[42:43], v6 offset:200\ns_waitcnt lgkmcnt(0)\ns_barrier\nds_read_b128 v[24:27], v2 offset:448\nds_read_b128 v[28:31], v2 offset:960\n" \
  "ds_read_b128 v[32:35], v2 offset:1472\nds_read_b128 v[36:39], v2 offset:1536\ns_waitcnt lgkmcnt(3)\n" \
  "v_mfma_f32_16x16x32_f16 v[12:15], v[124:127], v[24:27], 0\ns_setprio 1\nds_read_b128 v[24:27], v2 offset:1600\ns_waitcnt lgkmcnt(3)\n" \
  "v_mfma_f32_16x16x32_f16 v[16:19], v[124:127], v[28:31], 0\nds_read_b128 v[124:127], v2 offset:1664\ns_waitcnt lgkmcnt(3)\n" \
  "v_mfma_f32_16x16x32_f16 v[12:15], v[120:123], v[32:35], v[12:15]\nds_read_b128 v[28:31], v2 offset:1728\ns_waitcnt lgkmcnt(3)\n" \
  "v_mfma_f32_16x16x32_f16 v[12:15], v[116:119], v[36:39], v[12:15]\nds_read_b128 v[32:35], v2 offset:1792\ns_waitcnt lgkmcnt(3)\n" \
  "v_mfma_f32_16x16x32_f16 v[12:15], v[112:115], v[24:27], v[12:15]\nds_read_b128 v[36:39], v2 offset:1856\ns_waitcnt lgkmcnt(3)\n" \
  "v_mfma_f32_16x16x32_f16 v[12:15], v[108:111], v[124:127], v[12:15]\nds_read_b128 v[24:27], v2 offset:1920\ns_waitcnt lgkmcnt(3)\n" \
  "v_mfma_f32_16x16x32_f16 v[12:15], v[104:107], v[28:31], v[12:15]\nds_read_b128 v[124:127], v2 offset:1984\ns_waitcnt lgkmcnt(3)\n" \
  "v_mfma_f32_16x16x32_f16 v[12:15], v[100:103], v[32:35], v[12:15]\nds_read_b128 v[28:31], v2 offset:2048\ns_waitcnt lgkmcnt(3)\n" \
  "v_mfma_f32_16x16x32_f16 v[12:15], v[96:99], v[36:39], v[12:15]\nds_read_b128 v[32:35], v2 offset:2112\ns_waitcnt lgkmcnt(3)\n" \
  "v_mfma_f32_16x16x32_f16 v[12:15], v[92:95], v[24:27], v[12:15]\nds_read_b128 v[36:39], v2 offset:2176\ns_waitcnt lgkmcnt(3)\n" \
  "v_mfma_f32_16x16x32_f16 v[12:15], v[88:91], v[124:127], v[12:15]\nds_read_b128 v[24:27], v2 offset:2240\n" \
  "v_mfma_f32_16x16x32_f16 v[16:19], v[120:123], v[124:127], v[16:19]\nds_read_b64 v[120:121], v6 offset:256\nds_read_b64 v[122:123], v6 offset:264\n" \
  "s_waitcnt lgkmcnt(4)\nv_mfma_f32_16x16x32_f16 v[12:15], v[84:87], v[28:31], v[12:15]\nds_read_b128 v[124:127], v2 offset:2304\n" \
  "v_mfma_f32_16x16x32_f16 v[16:19], v[116:119], v[28:31], v[16:19]\nds_read_b64 v[116:117], v6 offset:320\nds_read_b64 v[118:119], v6 offset:328\n" \
  "v_mfma_f32_16x16x32_f16 v[12:15], v[80:83], v[32:35], v[12:15]\nds_read_b128 v[28:31], v2 offset:2368\n" \
  "v_mfma_f32_16x16x32_f16 v[16:19], v[112:115], v[32:35], v[16:19]\ns_waitcnt lgkmcnt(6)\n" \
  "v_mfma_f32_16x16x32_f16 v[12:15], v[76:79], v[36:39], v[12:15]\nds_read_b128 v[112:115], v2 offset:2432\n" \
  "v_mfma_f32_16x16x32_f16 v[16:19], v[108:111], v[36:39], v[16:19]\nds_read2_b32 v[32:33], v8 offset0:48 offset1:49\n" \
  "ds_read2_b32 v[34:35], v8 offset0:50 offset1:51\nv_mfma_f32_16x16x32_f16 v[12:15], v[72:75], v[24:27], v[12:15]\n" \
  "ds_read_b128 v[108:111], v2 offset:2496\nv_mfma_f32_16x16x32_f16 v[16:19], v[104:107], v[24:27], v[16:19]\n" \
  "ds_read2_b32 v[36:37], v8 offset0:32 offset1:33\nds_read2_b32 v[38:39], v8 offset0:34 offset1:35\ns_waitcnt lgkmcnt(6)\n" \
  "v_mfma_f32_16x16x32_f16 v[12:15], v[68:71], v[124:127], v[12:15]\nds_read_b128 v[104:107], v2 offset:2560\n" \
  "v_mfma_f32_16x16x32_f16 v[16:19], v[100:103], v[124:127], v[16:19]\nds_read_b128 v[24:27], v2 offset:2624\n" \
  "v_mfma_f32_16x16x32_f16 v[12:15], v[64:67], v[28:31], v[12:15]\nds_read_b128 v[100:103], v2 offset:2688\nds_read_b32 v23, v9 offset:32\n" \
  "v_mfma_f32_16x16x32_f16 v[16:19], v[96:99], v[28:31], v[16:19]\nds_read_b128 v[124:127], v2 offset:2752\ns_waitcnt lgkmcnt(7)\n" \
  "v_mfma_f32_16x16x32_f16 v[12:15], v[60:63], v[112:115], v[12:15]\nds_read_b128 v[96:99], v2 offset:2816\n" \
  "v_mfma_f32_16x16x32_f16 v[16:19], v[92:95], v[112:115], v[16:19]\nds_read_b128 v[28:31], v2 offset:2880\n" \
  "v_mfma_f32_16x16x32_f16 v[12:15], v[56:59], v[108:111], v[12:15]\nds_read_b128 v[92:95], v2 offset:2944\n" \
  "v_mfma_f32_16x16x32_f16 v[16:19], v[88:91], v[108:111], v[16:19]\nds_read_b128 v[112:115], v2 offset:3008\ns_waitcnt lgkmcnt(5)\n" \
  "v_readfirstlane_b32 s85, v23\ns_cmp_lt_u32 s85, 3\ns_cbranch_scc1 61f\n62:\nv_mfma_f32_16x16x32_f16 v[16:19], v[84:87], v[104:107], v[16:19]\n" \
  "ds_read_b64_tr_b16 v[88:89], v5 offset:0\nds_read_b64_tr_b16 v[90:91], v5 offset:32\n" \
  "v_mfma_f32_16x16x32_f16 v[16:19], v[80:83], v[24:27], v[16:19]\nds_read_b64_tr_b16 v[108:109], v5 offset:256\n" \
  "ds_read_b64_tr_b16 v[110:111], v5 offset:288\nv_mfma_f32_16x16x32_f16 v[16:19], v[76:79], v[100:103], v[16:19]\n" \
  "ds_read_b64_tr_b16 v[84:85], v5 offset:512\nds_read_b64_tr_b16 v[86:87], v5 offset:544\ns_waitcnt lgkmcnt(9)\n" \
  "v_mfma_f32_16x16x32_f16 v[16:19], v[72:75], v[124:127], v[16:19]\nds_read_b64_tr_b16 v[104:105], v5 offset:768\n" \
  "ds_read_b64_tr_b16 v[106:107], v5 offset:800\nv_mfma_f32_16x16x32_f16 v[16:19], v[68:71], v[96:99], v[16:19]\n" \
  "ds_read_b64_tr_b16 v[80:81], v5 offset:1024\nds_read_b64_tr_b16 v[82:83], v5 offset:1056\ns_waitcnt lgkmcnt(11)\n" \
  "v_mfma_f32_16x16x32_f16 v[16:19], v[64:67], v[28:31], v[16:19]\nds_read_b64_tr_b16 v[24:25], v5 offset:1280\n" \
  "ds_read_b64_tr_b16 v[26:27], v5 offset:1312\nv_mfma_f32_16x16x32_f16 v[16:19], v[60:63], v[92:95], v[16:19]\n" \
  "ds_read_b64_tr_b16 v[76:77], v5 offset:1536\nds_read_b64_tr_b16 v[78:79], v5 offset:1568\ns_waitcnt lgkmcnt(12)\n" \
  "v_mfma_f32_16x16x32_f16 v[16:19], v[56:59], v[112:115], v[16:19]\nds_read_b64_tr_b16 v[100:101], v5 offset:1792\n" \
  "ds_read_b64_tr_b16 v[102:103], v5 offset:1824\nv_mfma_f32_16x16x32_f16 v[12:15], v[88:91], v[52:55], v[12:15]\ns_waitcnt lgkmcnt(10)\n" \
  "v_mfma_f32_16x16x32_f16 v[12:15], v[108:111], v[48:51], v[12:15]\nds_read_b64_tr_b16 v[72:73], v7 offset:0\n" \
  "ds_read_b64_tr_b16 v[74:75], v7 offset:64\nv_mfma_f32_16x16x32_f16 v[12:15], v[84:87], v[44:47], v[12:15]\n" \
  "ds_read_b64_tr_b16 v[124:125], v7 offset:512\nds_read_b64_tr_b16 v[126:127], v7 offset:576\n" \
  "v_mfma_f32_16x16x32_f16 v[16:19], v[84:87], v[52:55], v[16:19]\ns_waitcnt lgkmcnt(10)\n" \
  "v_mfma_f32_16x16x32_f16 v[12:15], v[104:107], v[40:43], v[12:15]\nv_mfma_f32_16x16x32_f16 v[16:19], v[104:107], v[48:51], v[16:19]\n" \
  "v_mfma_f32_16x16x32_f16 v[12:15], v[80:83], v[120:123], v[12:15]\nv_mfma_f32_16x16x32_f16 v[16:19], v[80:83], v[44:47], v[16:19]\n" \
  "s_waitcnt lgkmcnt(6)\nv_mfma_f32_16x16x32_f16 v[12:15], v[24:27], v[116:119], v[12:15]\n" \
  "v_mfma_f32_16x16x32_f16 v[16:19], v[24:27], v[40:43], v[16:19]\nv_mfma_f32_16x16x32_f16 v[16:19], v[76:79], v[120:123], v[16:19]\n" \
  "s_waitcnt lgkmcnt(2)\nv_mfma_f32_16x16x32_f16 v[16:19], v[100:103], v[116:119], v[16:19]\n" \
  "v_mfma_f32_16x16x32_f16 v[12:15], v[72:75], v[32:35], v[12:15]\nv_mfma_f32_16x16x32_f16 v[16:19], v[72:75], v[36:39], v[16:19]\n" \
  "s_waitcnt lgkmcnt(0)\nv_mfma_f32_16x16x32_f16 v[16:19], v[124:127], v[32:35], v[16:19]\ns_nop 4\nv_cvt_pkrtz_f16_f32 v20, v12, v13\n" \
  "v_cvt_pkrtz_f16_f32 v21, v14, v15\nv_pk_add_u16 v20, v20, 4 op_sel_hi:[1,0] clamp\nv_pk_add_u16 v21, v21, 4 op_sel_hi:[1,0] clamp\n" \
  "ds_write_b64 v4, v[20:21] offset:4096\nv_cvt_pkrtz_f16_f32 v20, v16, v17\nv_cvt_pkrtz_f16_f32 v21, v18, v19\n" \
  "v_pk_add_u16 v20, v20, 4 op_sel_hi:[1,0] clamp\nv_pk_add_u16 v21, v21, 4 op_sel_hi:[1,0] clamp\nds_write_b64 v4, v[20:21] offset:5120\n" \
  "s_setprio 0\ns_setpc_b64 s[90:91]\n61:\ns_movk_i32 s84, 0x4000\n63:\nds_read_b32 v23, v9 offset:32\ns_sub_u32 s84, s84, 1\ns_cbranch_scc1 62b\n" \
  "s_waitcnt lgkmcnt(0)\nv_readfirstlane_b32 s85, v23\ns_cmp_lt_u32 s85, 3\ns_cbranch_scc1 63b\ns_branch 62b\n5:\n" \
  "ds_read_b64 v[124:125], v3 offset:1088\nds_read_b64 v[126:127], v3 offset:1096\nds_read_b64 v[120:121], v3 offset:0\n" \
  "ds_read_b64 v[122:123], v3 offset:8\nds_read_b64 v[116:117], v3 offset:64\nds_read_b64 v[118:119], v3 offset:72\n" \
  "ds_read_b64 v[112:113], v3 offset:128\nds_read_b64 v[114:115], v3 offset:136\nds_read_b64 v[108:109], v3 offset:192\n" \
  "ds_read_b64 v[110:111], v3 offset:200\nds_read_b64 v[104:105], v3 offset:256\nds_read_b64 v[106:107], v3 offset:264\n" \
  "ds_read_b64 v[100:101], v3 offset:320\nds_read_b64 v[102:103], v3 offset:328\nds_read_b64 v[96:97], v3 offset:384\n" \
  "ds_read_b64 v[98:99], v3 offset:392\nds_read_b64 v[92:93], v3 offset:448\nds_read_b64 v[94:95], v3 offset:456\n" \
  "ds_read_b64 v[88:89], v3 offset:512\nds_read_b64 v[90:91], v3 offset:520\nds_read_b64 v[84:85], v3 offset:576\n" \
  "ds_read_b64 v[86:87], v3 offset:584\nds_read_b64 v[80:81], v3 offset:640\nds_read_b64 v[82:83], v3 offset:648\n" \
  "ds_read_b64 v[76:77], v3 offset:704\nds_read_b64 v[78:79], v3 offset:712\nds_read_b64 v[72:73], v3 offset:768\n" \
  "ds_read_b64 v[74:75], v3 offset:776\nds_read_b64 v[68:69], v3 offset:832\nds_read_b64 v[70:71], v3 offset:840\n" \
  "ds_read_b64 v[64:65], v3 offset:896\nds_read_b64 v[66:67], v3 offset:904\nds_read_b64 v[60:61], v3 offset:960\n" \
  "ds_read_b64 v[62:63], v3 offset:968\nds_read_b64 v[56:57], v3 offset:1024\nds_read_b64 v[58:59], v3 offset:1032\n" \
  "ds_read_b64 v[52:53], v6 offset:0\nds_read_b64 v[54:55], v6 offset:8\nds_read_b64 v[48:49], v6 offset:64\nds_read_b64 v[50:51], v6 offset:72\n" \
  "ds_read_b64 v[44:45], v6 offset:128\nds_read_b64 v[46:47], v6 offset:136\nds_read_b64 v[40:41], v6 offset:192\n" \
  "ds_read_b64 v[42:43], v6 offset:200\ns_waitcnt lgkmcnt(0)\ns_barrier\nds_read_b128 v[24:27], v2 offset:1472\n" \
  "ds_read_b128 v[28:31], v2 offset:1984\nds_read_b128 v[32:35], v2 offset:2496\nds_read_b128 v[36:39], v2 offset:2560\ns_waitcnt lgkmcnt(3)\n" \
  "v_mfma_f32_16x16x32_f16 v[12:15], v[124:127], v[24:27], 0\ns_setprio 1\nds_read_b128 v[24:27], v2 offset:2624\ns_waitcnt lgkmcnt(3)\n" \
  "v_mfma_f32_16x16x32_f16 v[16:19], v[124:127], v[28:31], 0\nds_read_b128 v[124:127], v2 offset:2688\ns_waitcnt lgkmcnt(3)\n" \
  "v_mfma_f32_16x16x32_f16 v[12:15], v[120:123], v[32:35], v[12:15]\nds_read_b128 v[28:31], v2 offset:2752\ns_waitcnt lgkmcnt(3)\n" \
  "v_mfma_f32_16x16x32_f16 v[12:15], v[116:119], v[36:39], v[12:15]\nds_read_b128 v[32:35], v2 offset:2816\ns_waitcnt lgkmcnt(3)\n" \
  "v_mfma_f32_16x16x32_f16 v[12:15], v[112:115], v[24:27], v[12:15]\nds_read_b128 v[36:39], v2 offset:2880\ns_waitcnt lgkmcnt(3)\n" \
  "v_mfma_f32_16x16x32_f16 v[12:15], v[108:111], v[124:127], v[12:15]\nds_read_b128 v[24:27], v2 offset:2944\ns_waitcnt lgkmcnt(3)\n" \
  "v_mfma_f32_16x16x32_f16 v[12:15], v[104:107], v[28:31], v[12:15]\nds_read_b128 v[124:127], v2 offset:3008\ns_waitcnt lgkmcnt(3)\n" \
  "v_mfma_f32_16x16x32_f16 v[12:15], v[100:103], v[32:35], v[12:15]\nds_read_b128 v[28:31], v2 offset:3072\ns_waitcnt lgkmcnt(3)\n" \
  "v_mfma_f32_16x16x32_f16 v[12:15], v[96:99], v[36:39], v[12:15]\nds_read_b128 v[32:35], v2 offset:3136\ns_waitcnt lgkmcnt(3)\n" \
  "v_mfma_f32_16x16x32_f16 v[12:15], v[92:95], v[24:27], v[12:15]\nds_read_b128 v[36:39], v2 offset:3200\ns_waitcnt lgkmcnt(3)\n" \
  "v_mfma_f32_16x16x32_f16 v[12:15], v[88:91], v[124:127], v[12:15]\nds_read_b128 v[24:27], v2 offset:3264\n" \
  "v_mfma_f32_16x16x32_f16 v[16:19], v[120:123], v[124:127], v[16:19]\nds_read_b64 v[120:121], v6 offset:256\nds_read_b64 v[122:123], v6 offset:264\n" \
  "s_waitcnt lgkmcnt(4)\nv_mfma_f32_16x16x32_f16 v[12:15], v[84:87], v[28:31], v[12:15]\nds_read_b128 v[124:127], v2 offset:3328\n" \
  "v_mfma_f32_16x16x32_f16 v[16:19], v[116:119], v[28:31], v[16:19]\nds_read_b64 v[116:117], v6 offset:320\nds_read_b64 v[118:119], v6 offset:328\n" \
  "v_mfma_f32_16x16x32_f16 v[12:15], v[80:83], v[32:35], v[12:15]\nds_read_b128 v[28:31], v2 offset:3392\n" \
  "v_mfma_f32_16x16x32_f16 v[16:19], v[112:115], v[32:35], v[16:19]\ns_waitcnt lgkmcnt(6)\n" \
  "v_mfma_f32_16x16x32_f16 v[12:15], v[76:79], v[36:39], v[12:15]\nds_read_b128 v[112:115], v2 offset:3456\n" \
  "v_mfma_f32_16x16x32_f16 v[16:19], v[108:111], v[36:39], v[16:19]\nds_read2_b32 v[32:33], v8 offset0:16 offset1:17\n" \
  "ds_read2_b32 v[34:35], v8 offset0:18 offset1:19\nv_mfma_f32_16x16x32_f16 v[12:15], v[72:75], v[24:27], v[12:15]\n" \
  "ds_read_b128 v[108:111], v2 offset:3520\nv_mfma_f32_16x16x32_f16 v[16:19], v[104:107], v[24:27], v[16:19]\n" \
  "ds_read2_b32 v[36:37], v8 offset0:0 offset1:1\nds_read2_b32 v[38:39], v8 offset0:2 offset1:3\ns_waitcnt lgkmcnt(6)\n" \
  "v_mfma_f32_16x16x32_f16 v[12:15], v[68:71], v[124:127], v[12:15]\nds_read_b128 v[104:107], v2 offset:3584\n" \
  "v_mfma_f32_16x16x32_f16 v[16:19], v[100:103], v[124:127], v[16:19]\nds_read_b128 v[24:27], v2 offset:3648\n" \
  "ds_read2_b32 v[100:101], v8 offset0:32 offset1:33\nds_read2_b32 v[102:103], v8 offset0:34 offset1:35\n" \
  "v_mfma_f32_16x16x32_f16 v[12:15], v[64:67], v[28:31], v[12:15]\nds_read_b128 v[124:127], v2 offset:3712\nds_read_b32 v23, v9 offset:32\n" \
  "v_mfma_f32_16x16x32_f16 v[16:19], v[96:99], v[28:31], v[16:19]\nds_read_b128 v[96:99], v2 offset:3776\ns_waitcnt lgkmcnt(9)\n" \
  "v_mfma_f32_16x16x32_f16 v[12:15], v[60:63], v[112:115], v[12:15]\nds_read_b128 v[28:31], v2 offset:3840\n" \
  "v_mfma_f32_16x16x32_f16 v[16:19], v[92:95], v[112:115], v[16:19]\nds_read_b128 v[92:95], v2 offset:3904\n" \
  "v_mfma_f32_16x16x32_f16 v[12:15], v[56:59], v[108:111], v[12:15]\nds_read_b128 v[112:115], v2 offset:3968\n" \
  "v_mfma_f32_16x16x32_f16 v[16:19], v[88:91], v[108:111], v[16:19]\nds_read_b128 v[88:91], v2 offset:4032\ns_waitcnt lgkmcnt(5)\n" \
  "v_readfirstlane_b32 s85, v23\ns_cmp_lt_u32 s85, 3\ns_cbranch_scc1 71f\n72:\nv_mfma_f32_16x16x32_f16 v[16:19], v[84:87], v[104:107], v[16:19]\n" \
  "ds_read_b64_tr_b16 v[108:109], v5 offset:1024\nds_read_b64_tr_b16 v[110:111], v5 offset:1056\n" \
  "v_mfma_f32_16x16x32_f16 v[16:19], v[80:83], v[24:27], v[16:19]\nds_read_b64_tr_b16 v[84:85], v5 offset:1280\n" \
  "ds_read_b64_tr_b16 v[86:87], v5 offset:1312\nv_mfma_f32_16x16x32_f16 v[16:19], v[76:79], v[124:127], v[16:19]\n" \
  "ds_read_b64_tr_b16 v[104:105], v5 offset:1536\nds_read_b64_tr_b16 v[106:107], v5 offset:1568\ns_waitcnt lgkmcnt(9)\n" \
  "v_mfma_f32_16x16x32_f16 v[16:19], v[72:75], v[96:99], v[16:19]\nds_read_b64_tr_b16 v[80:81], v5 offset:1792\n" \
  "ds_read_b64_tr_b16 v[82:83], v5 offset:1824\nv_mfma_f32_16x16x32_f16 v[16:19], v[68:71], v[28:31], v[16:19]\n" \
  "ds_read_b64_tr_b16 v[24:25], v5 offset:2048\nds_read_b64_tr_b16 v[26:27], v5 offset:2080\ns_waitcnt lgkmcnt(11)\n" \
  "v_mfma_f32_16x16x32_f16 v[16:19], v[64:67], v[92:95], v[16:19]\nds_read_b64_tr_b16 v[76:77], v5 offset:2304\n" \
  "ds_read_b64_tr_b16 v[78:79], v5 offset:2336\nv_mfma_f32_16x16x32_f16 v[16:19], v[60:63], v[112:115], v[16:19]\n" \
  "ds_read_b64_tr_b16 v[124:125], v5 offset:2560\nds_read_b64_tr_b16 v[126:127], v5 offset:2592\ns_waitcnt lgkmcnt(12)\n" \
  "v_mfma_f32_16x16x32_f16 v[16:19], v[56:59], v[88:91], v[16:19]\nds_read_b64_tr_b16 v[72:73], v5 offset:2816\n" \
  "ds_read_b64_tr_b16 v[74:75], v5 offset:2848\nv_mfma_f32_16x16x32_f16 v[12:15], v[108:111], v[52:55], v[12:15]\ns_waitcnt lgkmcnt(10)\n" \
  "v_mfma_f32_16x16x32_f16 v[12:15], v[84:87], v[48:51], v[12:15]\nds_read_b64_tr_b16 v[96:97], v7 offset:0\n" \
  "ds_read_b64_tr_b16 v[98:99], v7 offset:64\nv_mfma_f32_16x16x32_f16 v[12:15], v[104:107], v[44:47], v[12:15]\n" \
  "ds_read_b64_tr_b16 v[68:69], v7 offset:512\nds_read_b64_tr_b16 v[70:71], v7 offset:576\n" \
  "v_mfma_f32_16x16x32_f16 v[16:19], v[104:107], v[52:55], v[16:19]\ns_waitcnt lgkmcnt(10)\n" \
  "v_mfma_f32_16x16x32_f16 v[12:15], v[80:83], v[40:43], v[12:15]\nds_read_b64_tr_b16 v[28:29], v7 offset:1024\n" \
  "ds_read_b64_tr_b16 v[30:31], v7 offset:1088\nv_mfma_f32_16x16x32_f16 v[16:19], v[80:83], v[48:51], v[16:19]\n" \
  "ds_read2_b32 v[64:65], v8 offset0:48 offset1:49\nds_read2_b32 v[66:67], v8 offset0:50 offset1:51\n" \
  "v_mfma_f32_16x16x32_f16 v[12:15], v[24:27], v[120:123], v[12:15]\nv_mfma_f32_16x16x32_f16 v[16:19], v[24:27], v[44:47], v[16:19]\n" \
  "s_waitcnt lgkmcnt(10)\nv_mfma_f32_16x16x32_f16 v[12:15], v[76:79], v[116:119], v[12:15]\nds_read_b64_tr_b16 v[92:93], v7 offset:1536\n" \
  "ds_read_b64_tr_b16 v[94:95], v7 offset:1600\nv_mfma_f32_16x16x32_f16 v[16:19], v[76:79], v[40:43], v[16:19]\n" \
  "v_mfma_f32_16x16x32_f16 v[16:19], v[124:127], v[120:123], v[16:19]\ns_waitcnt lgkmcnt(8)\n" \
  "v_mfma_f32_16x16x32_f16 v[16:19], v[72:75], v[116:119], v[16:19]\nv_mfma_f32_16x16x32_f16 v[12:15], v[96:99], v[32:35], v[12:15]\n" \
  "v_mfma_f32_16x16x32_f16 v[16:19], v[96:99], v[36:39], v[16:19]\ns_waitcnt lgkmcnt(4)\n" \
  "v_mfma_f32_16x16x32_f16 v[12:15], v[68:71], v[100:103], v[12:15]\nv_mfma_f32_16x16x32_f16 v[16:19], v[68:71], v[32:35], v[16:19]\n" \
  "s_waitcnt lgkmcnt(2)\nv_mfma_f32_16x16x32_f16 v[12:15], v[28:31], v[64:67], v[12:15]\n" \
  "v_mfma_f32_16x16x32_f16 v[16:19], v[28:31], v[100:103], v[16:19]\ns_waitcnt lgkmcnt(0)\n" \
  "v_mfma_f32_16x16x32_f16 v[16:19], v[92:95], v[64:67], v[16:19]\ns_nop 4\nv_cvt_pkrtz_f16_f32 v20, v12, v13\nv_cvt_pkrtz_f16_f32 v21, v14, v15\n" \
  "v_pk_add_u16 v20, v20, 4 op_sel_hi:[1,0] clamp\nv_pk_add_u16 v21, v21, 4 op_sel_hi:[1,0] clamp\nds_write_b64 v4, v[20:21] offset:6144\n" \
  "v_cvt_pkrtz_f16_f32 v20, v16, v17\nv_cvt_pkrtz_f16_f32 v21, v18, v19\nv_pk_add_u16 v20, v20, 4 op_sel_hi:[1,0] clamp\n" \
  "v_pk_add_u16 v21, v21, 4 op_sel_hi:[1,0] clamp\nds_write_b64 v4, v[20:21] offset:7168\ns_setprio 0\ns_setpc_b64 s[90:91]\n71:\n" \
  "s_movk_i32 s84, 0x4000\n73:\nds_read_b32 v23, v9 offset:32\ns_sub_u32 s84, s84, 1\ns_cbranch_scc1 72b\ns_waitcnt lgkmcnt(0)\n" \
  "v_readfirstlane_b32 s85, v23\ns_cmp_lt_u32 s85, 3\ns_cbranch_scc1 73b\ns_branch 72b\n"
#define CONV_CLOB "memory", "scc", "s84", "s85", "s90", "s91", "v12", "v13", "v14", "v15", "v16", "v17", "v18", "v19", "v20", "v21", "v22", "v23", "v24", "v25", "v26", "v27", "v28", "v29", "v30", "v31", "v32", "v33", "v34", "v35", "v36", "v37", "v38", "v39", "v40", "v41", "v42", "v43", "v44", "v45", "v46", "v47", "v48", "v49", "v50", "v51", "v52", "v53", "v54", "v55", "v56", "v57", "v58", "v59", "v60", "v61", "v62", "v63", "v64", "v65", "v66", "v67", "v68", "v69", "v70", "v71", "v72", "v73", "v74", "v75", "v76", "v77", "v78", "v79", "v80", "v81", "v82", "v83", "v84", "v85", "v86", "v87", "v88", "v89", "v90", "v91", "v92", "v93", "v94", "v95", "v96", "v97", "v98", "v99", "v100", "v101", "v102", "v103", "v104", "v105", "v106", "v107", "v108", "v109", "v110", "v111", "v112", "v113", "v114", "v115", "v116", "v117", "v118", "v119", "v120", "v121", "v122", "v123", "v124", "v125", "v126", "v127"
constexpr int TDP = 1216, T_3 = 4 * TDP, T_4 = T_3 + 512, TB = T_4 + 320;
constexpr int C_U = 480, UB = C_U + 4096 + 96, C_G5 = 0, C_G3 = 3200, C_G9 = 6400, C_G7 = C_G9 + 2096, GB = C_G7 + 2080 + 112;
constexpr int O_FL = 2 * TB, O_UL = O_FL + 64, O_IM = O_UL + 2 * UB, O_ST = O_IM + 2 * GB;
constexpr int X_TB = 10240, X_UL = X_TB + TB, X_IM = X_UL + UB, X_ST = X_IM + GB;
constexpr int RP = 1216;
constexpr int LW = 36864 + 512, LA = LW, LM = LW + 36864;
constexpr int L_SC = LM + 2048, SMEM = O_IM + 256 * IP;
static_assert(L_SC + 1152 <= SMEM && O_ST + 8192 <= SMEM && X_IM + 128 * IP <= LM && X_ST + 8192 <= LM && UB % 128 == 64 && GB % 128 == 64 && SMEM <= 79872, "");
struct TOps { uint32_t xa, xb, ya, pd, pf, pg, pe; };
__device__ __forceinline__ void table_ops(uint32_t im, uint32_t tb, int ct, int lane, TOps& t) {
  const int j = lane & 15, kq = lane >> 4, q4 = (lane >> 2) & 3, p4 = lane & 3, jj = j + 16 * ct;
  const uint32_t sa = im + (uint32_t)(IP * (lane >> 2));
  t.xa = sa + (uint32_t)(I_X + 8 * p4); t.xb = sa + (uint32_t)(I_X + 32 + 8 * (p4 & 1)); t.ya = sa + (uint32_t)(32 * ct + 8 * p4);
  t.pd = tb + (uint32_t)(2 * (541 - (8 * jj + 4 * (kq & 1) + 256 * (kq >> 1))));
  t.pf = tb + (uint32_t)((kq & 1) ? T_4 + 2 * (63 - jj) : T_3 + 2 * (127 - 2 * jj));
  t.pg = t.pf - ((kq & 1) ? 64 : 2);
  t.pe = tb + (uint32_t)(p4 * (TDP + 2) + ((lane < 12) ? 2 * (32 - q4) : 2 * (572 + (q4 & 1))));
  asm volatile("" : "+v"(t.xa), "+v"(t.xb), "+v"(t.ya), "+v"(t.pd), "+v"(t.pf), "+v"(t.pg), "+v"(t.pe));
}
typedef __attribute__((address_space(3))) char* lptr;
#define LP(T, a) ((__attribute__((address_space(3))) T*)(uintptr_t)(a))
__device__ __forceinline__ uint32_t table_tile(const TOps& t, int ct, int lane, uint32_t evp, float dsk TSP) {
  u32x4 ev;
  f32x4 ad[2], af[2];
  asm volatile(T_ASM : "=&v"(ad[0]), "=&v"(ad[1]), "=&v"(af[0]), "=&v"(af[1]), "=&v"(ev) : "v"(t.xa), "v"(t.xb), "v"(t.ya), "v"(evp) : T_CLOB);
  TS(0, 4);
  const int e0 = (int)(ev[0] + ev[2]), e1 = (int)(ev[1] + ev[3]);
  const int em = min(max(max(max(e0, e1), (int)((__float_as_uint(dsk) >> 23) & 0xffu) - 133), -110), 115);
  const int ts = ((uint32_t)(em + 12) <= 8u) ? 1 : -3 - em;
  float s0 = __builtin_amdgcn_ldexpf(1.f, e0 - 29 + ts), s1 = __builtin_amdgcn_ldexpf(1.f, e1 - 29 + ts);
  float d0 = (ct == 0 && lane == 0) ? __builtin_amdgcn_ldexpf(dsk, ts) : 0.f;
  asm volatile("s_nop 1" : "+v"(ad[0]), "+v"(ad[1]), "+v"(af[0]), "+v"(af[1]), "+v"(s0), "+v"(s1), "+v"(d0));
  const f32x4 k = ad[0] * s0 + (f32x4){d0, 0.f, 0.f, 0.f} + ad[1] * s1, f = af[0] * s0 + af[1] * s1;
  const uint32_t p32 = packh(k[3], k[2]), p10 = packh(k[1], k[0]), p21 = packh(k[2], k[1]), p01 = packh(f[0], f[1]), p23 = packh(f[2], f[3]);
  *LP(u32x2, t.pd + 3 * TDP + 6) = (u32x2){p32, p10};
  *LP(uint32_t, t.pd + TDP + 2) = p32; *LP(uint32_t, t.pd + TDP + 6) = p10;
  *LP(uint32_t, t.pd + 2) = p21; *LP(uint32_t, t.pd + 2 * TDP + 6) = p21;
  *LP(u16, t.pd) = (u16)p32; *LP(u16, t.pd + 2 * TDP + 4) = (u16)p32;
  *LP(u16, t.pd + 6) = (u16)(p10 >> 16); *LP(u16, t.pd + 2 * TDP + 10) = (u16)(p10 >> 16);
  if (ct) {
    const uint32_t va = (uint32_t)__builtin_amdgcn_readlane((int)p32, 63), vb = (uint32_t)__builtin_amdgcn_readlane((int)p23, 15) >> 16;
    *LP(u16, t.pe) = (u16)((lane < 12) ? va : vb);
  }
  if (lane < 32) {
    *LP(u16, t.pf + 128) = (u16)p01; *LP(u16, t.pg + 128) = (u16)(p01 >> 16);
    *LP(u16, t.pf) = (u16)p23; *LP(u16, t.pg) = (u16)(p23 >> 16);
  }
  return (uint32_t)(127 + 5 - ts) << 23;
}
__device__ __forceinline__ uint32_t hadd(uint32_t a, uint32_t b) { return __builtin_bit_cast(uint32_t, __builtin_bit_cast(f16x2, a) + __builtin_bit_cast(f16x2, b)); }
template <bool FULL> __device__ __forceinline__ void sums(char* sm, const u32x4 p, const u32x4 c) {
  const uint32_t D[8] = {p[0], p[1], p[2], p[3], c[0], c[1], c[2], c[3]};
  uint32_t O[7];
#pragma unroll
  for (int j = 0; j < 7; ++j) O[j] = __builtin_amdgcn_alignbit(D[j + 1], D[j], 16);
  u32x4 a, b;
#pragma unroll
  for (int j = 0; j < 4; ++j) a[j] = hadd(D[j + 2], hadd(O[j + 1], O[j + 2]));
  *(u32x4*)(sm + C_G3 + 32) = a;
#pragma unroll
  for (int j = 0; j < 4; ++j) b[j] = hadd(a[j], hadd(D[j + 1], D[j + 3]));
  *(u32x4*)(sm + C_G5 + 32) = b;
  if (FULL) {
#pragma unroll
    for (int j = 0; j < 4; ++j) a[j] = hadd(b[j], hadd(O[j], O[j + 3]));
    *(u32x4*)(sm + C_G7 + 32) = a;
#pragma unroll
    for (int j = 0; j < 4; ++j) b[j] = hadd(a[j], hadd(D[j], D[j + 4]));
    *(u32x4*)(sm + C_G9 + 32) = b;
  }
}
__device__ __forceinline__ void count_up(uint32_t a) {
  uint32_t t;
  asm volatile("s_mov_b64 exec, 1\n\tv_mov_b32 %0, 1\n\tds_add_u32 %1, %0\n\ts_mov_b64 exec, -1" : "=&v"(t) : "v"(a) : "memory");
}
__device__ __forceinline__ int pad_at(int r) { return 16 * (r & 1) + 3200 * (r >> 1) - ((r >= 6) ? 3200 - 2096 : 0); }
struct COps { uint32_t b, a, o, g3, k3, g4, k4, f; };
__device__ __forceinline__ void conv_ops(uint32_t tb, uint32_t ub, uint32_t cb, int lane, COps& e) {
  const uint32_t j = lane & 15, kq = lane >> 4, q4 = (lane >> 2) & 3, p4 = lane & 3;
  e.b = ub + 32u * j + 16u * kq;
  e.a = tb + p4 * TDP + 32u - 8u * q4 + 16u * kq;
  e.g3 = cb + C_G5 + 8 + 64u * kq + 8u * (q4 + p4) + (q4 & 1u) * 3200u;
  e.g4 = cb + C_G9 + 8 + 128u * kq + 16u * q4 + 8u * p4 + (q4 & 1u) * 2096u;
  e.k3 = tb + T_3 + 120 - 8u * j + 16u * kq;
  e.k4 = tb + T_4 + 60 - 4u * j + 16u * kq;
  asm volatile("" : "+v"(e.b), "+v"(e.a), "+v"(e.g3), "+v"(e.g4), "+v"(e.k3), "+v"(e.k4));
}
#define CONV_RUN(prog, e, ...) do { int prog_ = (prog); \
    asm volatile("s_call_b64 s[90:91], kf_conv" : "+{s89}"(prog_), "+{v2}"(e.b), "+{v3}"(e.a), "+{v4}"(e.o), "+{v5}"(e.g3), "+{v6}"(e.k3), "+{v7}"(e.g4), \
                 "+{v8}"(e.k4), "+{v9}"(e.f) __VA_ARGS__ : : CONV_CLOB); } while (0)

template <class F>
__device__ __forceinline__ void ln_two(const uint32_t* v, int lane, float sc, uint32_t* c, f32x2* t, f32x4 z, F&& mid) {
  float s1a = z[0], s2a = z[1], s1b = z[2], s2b = z[3];
#pragma unroll
  for (int k = 0; k < 4; ++k) {
    s1a = dot2(v[k], 0x3f803f80u, s1a); s2a = dot2(v[k], v[k], s2a);
    s1b = dot2(v[4 + k], 0x3f803f80u, s1b); s2b = dot2(v[4 + k], v[4 + k], s2b);
  }
#pragma unroll
  for (int k = 0; k < 9; ++k) t[k] = (f32x2){bflo(v[k]), bfhi(v[k])};
  const float rs = row_sum(fold16(dot2(v[8], 0x3f803f80u, fold32(s1a, s1b)), dot2(v[8], v[8], fold32(s2a, s2b))));
  const f32x2 mean = (f32x2){rdl(rs, 15), rdl(rs, 47)} * (1.f / D_), m2 = mean * mean;
  f32x2 var = (f32x2){rdl(rs, 31), rdl(rs, 63)} * (1.f / D_) - m2;
  asm volatile("" : "+v"(t[0]), "+v"(t[1]), "+v"(t[2]), "+v"(t[3]), "+v"(t[4]), "+v"(t[5]), "+v"(t[6]), "+v"(t[7]), "+v"(t[8]));
  if (__builtin_expect(!(m2[0] <= 16.f * var[0] && m2[1] <= 16.f * var[1]), 0)) {
    float q[3] = {0.f, 0.f, 0.f};
#pragma unroll
    for (int k = 0; k < 9; ++k) {
      const f32x2 a = t[k] - ((k < 4 || (k == 8 && lane < 32)) ? mean[0] : mean[1]);
      q[k >> 2] += a[0] * a[0] + a[1] * a[1];
    }
    float qa = q[0] + ((lane < 32) ? q[2] : 0.f), qb = q[1] + ((lane < 32) ? 0.f : q[2]);
    wave_sum2(qa, qb);
    var = (f32x2){qa, qb} * (1.f / D_);
  }
  mid();
  const uint32_t mp = pack2(mean[0], mean[1]), vp = pack2(var[0], var[1]);
  const f32x2 ve = (f32x2){bflo(vp), bfhi(vp)} + 1e-5f;
  const uint32_t e2 = pack2(ve[0], ve[1]);
  const uint32_t sp = pack2(__builtin_amdgcn_sqrtf(bflo(e2)), __builtin_amdgcn_sqrtf(bfhi(e2)));
  const f32x2 rc = (f32x2){__builtin_amdgcn_rcpf(bflo(sp)), __builtin_amdgcn_rcpf(bfhi(sp))} * sc;
  const float nm[3] = {bflo(mp), bfhi(mp), (lane < 32) ? bflo(mp) : bfhi(mp)}, rr[3] = {rc[0], rc[1], (lane < 32) ? rc[0] : rc[1]};
#pragma unroll
  for (int k = 0; k < 9; ++k) { const f32x2 d = t[k] - nm[k >> 2]; c[k] = pack2(d[0], d[1]); }
#pragma unroll
  for (int k = 0; k < 9; ++k) { const f32x2 d = (f32x2){bflo(c[k]), bfhi(c[k])} * rr[k >> 2]; c[k] = pack2(d[0], d[1]); }
}
__device__ __forceinline__ uint32_t med3u(uint32_t a, uint32_t b, uint32_t c) { return max(min(a, b), min(max(a, b), c)); }
__device__ __forceinline__ uint32_t oct_maxu(uint32_t v) {
  v = max(v, (uint32_t)DPP_I((int)v, 0xb1, 0xf));
  v = max(v, (uint32_t)DPP_I((int)v, 0x4e, 0xf));
  v = max(v, (uint32_t)DPP_I((int)v, 0x141, 0xf));
  return v;
}

#define KARGS const u16* __restrict__ x, const u16* __restrict__ Are, const u16* __restrict__ Aim, const u16* __restrict__ Bre, \
    const u16* __restrict__ Bim, const u16* __restrict__ Cre, const u16* __restrict__ Cim, const u16* __restrict__ logdt, \
    const u16* __restrict__ Dskip, char* __restrict__ ws, const u16* __restrict__ wr, const u16* __restrict__ wg, \
    const u16* __restrict__ wu, const u16* __restrict__ wd, u16* __restrict__ out
#define KPASS x, Are, Aim, Bre, Bim, Cre, Cim, logdt, Dskip, ws, wr, wg, wu, wd, out
template <int ROLE>
__device__ __forceinline__ void role_body(char* smem, KARGS) {
  const int tid = threadIdx.x, lane = tid & 63;
  const int w = __builtin_amdgcn_readfirstlane(tid >> 6), wl = w & 3;
  const int b = blockIdx.x & 255, bx = b & 7, bj = b >> 3;
  constexpr bool isl = ROLE == 1 || ROLE == 2;
  const int tg = isl ? 32 * (bj >> 2) + 4 * bx + (bj & 3) : 32 * bx + bj;
  u32x4 xa, xc;
  uint32_t ep, r0;
#define P_WAIT(n, ...) asm volatile("s_waitcnt vmcnt(" #n ")\n\tv_lshlrev_b32 %0, 16, v100\n\tv_lshlrev_b32 %1, 16, v101\n\tv_lshlrev_b32 %2, 16, v102" \
    : "=v"(ldt), "=v"(are), "=v"(aim) : : "v100", "v101", "v102" __VA_ARGS__)
#define P_MORE() asm volatile("v_lshlrev_b32 %0, 16, v103\n\tv_lshlrev_b32 %1, 16, v104\n\tv_lshlrev_b32 %2, 16, v105\n\tv_lshlrev_b32 %3, 16, v106" \
    : "=v"(bre), "=v"(bim), "=v"(cre), "=v"(cim) : : "v103", "v104", "v105", "v106")
  TSD;
  TS(0, 0);
  const uint32_t ck0 = (ROLE == 0) ? 0u : (uint32_t)__builtin_amdgcn_s_memtime(), rt0 = (ROLE == 0) ? 0u : (uint32_t)__builtin_amdgcn_s_memrealtime();
  const uint32_t lb0 = (uint32_t)(uintptr_t)(__attribute__((address_space(3))) char*)smem;
  const int i = lane & 15, kq = lane >> 4;
  const u32x4 ones = {0xffffffffu, 0xffffffffu, 0xffffffffu, 0xffffffffu}, zero4 = {0u, 0u, 0u, 0u};
  auto u_again = [&](u32x4& uw, const u32x4& ut, uint32_t uo) {
#pragma unroll 1
    for (int spin = 0; spin < SPIN_CAP; ++spin) {
      const bool na = ((uw[0] | uw[1] | uw[2] | uw[3]) & UMASK) != 0u;
      if (__builtin_expect(!__any(na), 1)) break;
      TSC(19);
      SLEEP(USLEEP);
      if (na) { LD16W(uw, uo); uw ^= ut; }
    }
  };
  constexpr uint32_t S16 = 0x41800000u, LMASK = 0x00070007u;
  auto v_put = [&](u32x4 v, const u32x4& ut, bool other, uint32_t vo) {
    const bool second = __any(other || ((v[0] | v[1] | v[2] | v[3]) & 0x40004000u) != 0u);
    if (__builtin_expect(second, 0)) v = (v & ~LMASK) | (~ut & LMASK); else v = (v & ~LMASK) | ut;
    ST16(vo, v);
    return second;
  };
  auto v_end = [&](uint32_t T, int n) { DRAIN(); if (lane == 0) ST4(O_DN + 16u * n, T); };
  if constexpr (!isl) {
    const int ch = wl >> 1, tl = tid & 255;
    int* evp = (int*)(smem + O_FL + 16 * ch);
    float ldt, are, aim, dsk;
    if constexpr (ROLE == 0) {
      __builtin_amdgcn_s_setprio(KPRIO);
      COps e;
      conv_ops(lb0 + (uint32_t)(ch * TB), lb0 + (uint32_t)(O_UL + ch * UB), lb0 + (uint32_t)(O_IM + ch * GB), lane, e);
      e.o = lb0 + O_ST + (uint32_t)(16 * ((4 * i + 2 * (kq >> 1) + ch) ^ (i >> 1)) + 8 * (kq & 1));
      e.f = lb0 + O_FL;
      TOps t;
      table_ops(lb0 + (uint32_t)(O_IM + 128 * IP * ch), lb0 + (uint32_t)(TB * ch), wl & 1, lane, t);
      uint32_t pz = lb0 + O_IM + (uint32_t)(((lane >> 3) & 1) * GB + pad_at(lane & 7)), im = lb0 + (uint32_t)(O_IM + IP * tl);
      asm volatile("" : "+v"(e.o), "+v"(e.f), "+v"(pz), "+v"(im));
      float bre, bim, cre, cim;
      P_WAIT(0, , "v103", "v104", "v105", "v106", "v107", "v109");
      asm volatile("" : "={v109}"(ep));
      const uint32_t T = ((uint32_t)__builtin_amdgcn_readfirstlane(ep) + 1u) * 0x9E3779B1u;
      if (w == 3 && lane < 8) ST16(O_KEY + 128u * b + 16u * lane, ones);
      P_MORE();
      asm volatile("v_lshlrev_b32 %0, 16, v107" : "=v"(dsk) : : "v107");
      TS(0, 1);
      evp[wl & 1] = gen_y(ldt, are, aim, bre, bim, cre, cim, im, [] {} TSA);
      TS(0, 8);
      BAR();
      TS(0, 2);
      const uint32_t cs = table_tile(t, wl & 1, lane, lb0 + O_FL + 16 * ch, dsk TSA);
      *(uint32_t*)(smem + O_FL + 40 + 4 * ch) = cs;
      DRAIN();
      if ((wl & 1) && lane == 0) asm volatile("global_store_dwordx2 %0, %1, %2 sc1" : : "v"(O_SC + 16u * (2 * tg + ch)), "v"((u32x2){T, cs}), "s"(ws) : "memory");
      TS(0, 9);
      gsync0(lb0 + O_FL + 36, 4);
      TS(0, 3);
      *(u32x4*)(smem + O_ST + 16 * tl) = ones;
      *(u32x4*)(smem + O_ST + 4096 + 16 * tl) = ones;
      if (w == 0) *LP(u32x4, pz) = zero4;
      CONV_RUN(wl & 1, e);
      TS(0, 5);
    } else {
      __builtin_amdgcn_s_setprio(UPRIO);
#pragma unroll
      for (int k = 0; k < 3; ++k) *(u32x4*)(smem + ((16 * (tl + 256 * k) < O_FL) ? 16 * (tl + 256 * k) : O_FL + 32)) = zero4;
      if (w == 7) *(u32x4*)(smem + O_UL + (lane >> 5) * UB + 16 * min(lane & 31, 29)) = zero4;
      uint32_t uo = u_at(tl >> 1, 2 * tg + (tid & 1)), go = O_SU + (uint32_t)(256 * bx + 32 * wl + (lane & 31) + 4 * (lane & 32)) * 128u;
      uint32_t ul = (uint32_t)(O_UL + (tid & 1) * UB + C_U - 16 + 16 * (tl >> 1)), ug = (uint32_t)(O_IM + (tid & 1) * GB + 16 * (tl >> 1)), so = lb0 + O_ST + 16u * (uint32_t)(lane ^ (lane >> 3));
      uint32_t vo = O_V + (uint32_t)tg * VP + 16u * lane;
      asm volatile("" : "+v"(uo), "+v"(go), "+v"(ul), "+v"(ug), "+v"(so), "+v"(vo));
      P_WAIT(1, , "v109");
      TS(0, 1);
      evp[2 + (wl & 1)] = gen_x(ldt, are, aim, smem + O_IM + IP * tl);
      asm volatile("s_waitcnt vmcnt(0)" : "={v109}"(ep));
      const uint32_t T = ((uint32_t)__builtin_amdgcn_readfirstlane(ep) + 1u) * 0x9E3779B1u;
      TS(0, 8);
      BAR();
      TS(0, 2);
      u32x4 ut = {utag(T, 0), utag(T, 1), utag(T, 2), utag(T, 3)};
      asm volatile("" : "+v"(ut));
      SLEEP(GSLEEP);
      u32x2 pv;
      pv[1] = gate(ws, go, T);
      TS(0, 3);
      until(latest(pv[1]) + UD);
      TS(0, 7);
      u32x4 uw, ux;
      asm volatile("global_load_dwordx4 %0, %2, %4 sc1\n\tglobal_load_dwordx4 %1, %3, %4 sc1"
                   : "=&v"(uw), "=&v"(ux) : "v"(uo), "v"(uo + 64 * UP * 32), "s"(ws) : "memory");
      __builtin_amdgcn_s_setprio(FPR);
      asm volatile("s_waitcnt vmcnt(1)" : "+v"(uw));
      uw ^= ut;
      *(u32x4*)(smem + ul + 16) = uw;
      asm volatile("s_waitcnt vmcnt(0)" : "+v"(ux));
      TS(0, 11);
      ux ^= ut;
      *(u32x4*)(smem + ul + 2048 + 16) = ux;
      if (__builtin_expect(__any(((uw[0] | uw[1] | uw[2] | uw[3] | ux[0] | ux[1] | ux[2] | ux[3]) & UMASK) != 0u), 0)) {
        u_again(uw, ut, uo);
        u_again(ux, ut, uo + 64 * UP * 32);
        *(u32x4*)(smem + ul + 16) = uw;
        *(u32x4*)(smem + ul + 2048 + 16) = ux;
      }
      BAR();
      TS(0, 4);
      const u32x4 pw = *(const u32x4*)(smem + ul);
      if (wl < 2) {
        const u32x4 px = *(const u32x4*)(smem + ul + 2048);
        sums<true>(smem + ug, pw, uw);
        sums<false>(smem + ug + 2048, px, ux);
      } else sums<true>(smem + ug, pw, uw);
      count_up(lb0 + O_FL + 32);
      __builtin_amdgcn_s_setprio(SPR);
      TS(0, 5);
      const bool other = *(const uint32_t*)(smem + O_FL + 40 + 4 * (lane & 1)) != S16;
      bool second = false;
#pragma unroll
      for (int k = 0; k < 2; ++k) {
        const uint32_t o = ((k ? 0x3247u : 0x1065u) >> (4 * wl) & 7u) * 1024u;
        u32x4 v;
#pragma unroll 1
        for (int spin = 0; spin < SPIN_CAP; ++spin) {
          asm volatile("ds_read_b128 %0, %1\n\ts_waitcnt lgkmcnt(0)" : "=&v"(v) : "v"(so + o) : "memory");
          if (!__any(max4(v) == 0xffffffffu)) break;
          __builtin_amdgcn_s_sleep(1);
        }
        TS(0, 12 + k);
        second |= v_put(v, ut, other, vo + o);
        TS(0, 10 - 4 * k);
      }
      if (__builtin_expect(second, 0)) v_end(T, 4 * tg + wl);
    }
    TSE();
  } else {
  uint32_t* cand = (uint32_t*)(smem + LM);
  uint32_t* sel = cand + 128;
  uint32_t* kl = sel + 48;
  uint32_t* cnt = kl + 8;
  const uint32_t ca = lb0 + LM + 4 * (128 + 48 + 8);
  u16* hm = (u16*)(cnt + 12);
  uint32_t T;
  const bool ffn = bj < 18, isx = bj >= 24;
  const int fb = 18 * bx + bj;
  u32x4 bw[18];
#pragma unroll
  for (int s = 0; s < 9; ++s) bw[s] = (u32x4){0u, 0u, 0u, 0u};
  int tid2 = threadIdx.x;
  auto gu_dma = [&](int q0, int q1, auto depth) {
    const char* gs = (const char*)((w < 2) ? wg : wu) + (size_t)((144 * (w & 1)) * (F_ * 2) + fb * 64);
    const uint32_t gv = (uint32_t)((lane >> 2) * (F_ * 2) + 16 * ((lane & 3) ^ (lane >> 5) ^ (w & 2)));
#pragma unroll 1
    for (int q = q0; q < q1; ++q) {
      const int k = (int)((((bj & 1) ? ORD1 : ((unsigned __int128)0x876543210u << 36 | 0x876543210ull)) >> (4 * (q - (q >= 9 ? 9 : 0)) + (q >= 9 ? 36 : 0))) & 15u), h = q >= 9;
      dma16(gs + (size_t)(288 * h + 16 * k) * (F_ * 2), gv, lb0 + LW * h + 9216 * w + 1024 * k);
      asm volatile("s_waitcnt vmcnt(%0)" : : "n"(depth.value) : "memory");
    }
  };
  auto down_dma = [&] {
#pragma unroll 1
    for (int k = 0; k < 4; ++k) {
      const int o = 9216 * wl + 2304 * k;
      const char* ds = (const char*)wd + (size_t)(fb * 36864 + o);
      const uint32_t dl = lb0 + o + 32 * ((4 * wl + k + 1) >> 1);
      dma16d(ds, 16u * lane, dl);
      dma16d(ds + 1024, 16u * lane, dl + 1024);
      uint32_t keep;
      asm volatile("s_mov_b32 %0, m0\n\ts_mov_b32 m0, %3\n\ts_mov_b64 exec, 0xffff\n\tglobal_load_lds_dwordx4 %1, %2" DPOL2 "\n\ts_mov_b64 exec, -1\n\ts_mov_b32 m0, %0\n\ts_waitcnt vmcnt(%4)"
                   : "=&s"(keep) : "v"(16u * lane), "s"(ds + 2048), "s"(dl + 2048), "n"(DDEPTH) : "memory");
    }
  };
  auto gu_read = [&](int h) {
    const uint32_t ra = lb0 + LW * h + (uint32_t)(((lane >> 1) & 1) * 18432 + (8 * kq + ((lane >> 2) & 3)) * 64 + ((w ^ (kq & 1) ^ (lane & 2)) * 16) + 8 * (lane & 1));
#pragma unroll
    for (int s = 0; s < 9; ++s) {
      const u32x2 h0 = trd(ra + 2048 * s), h1 = trd(ra + 2048 * s + 256);
      bw[9 * h + s] = (u32x4){h0[0], h0[1], h1[0], h1[1]};
    }
#pragma unroll
    for (int s = 0; s < 9; ++s) asm volatile("" : "+v"(bw[9 * h + s]));
  };
  uint32_t tv = rt0, tk = 0u;
  const uint32_t vearly = (uint32_t)((VEARLY >> (8 * (bj >> 2))) & 0xffull);
  auto w_line = [&](int n) {
    const int q = min(lane, 62) / 9, r = min(7 * (n >> 2) + q, 287), h = r >= 144;
    return (const char*)((n & 1) ? wu : wg) + (uint32_t)((r + 144 * (h + ((n >> 1) & 1))) * (F_ * 2) + bx * 1152 + (min(lane, 62) - 9 * q) * 128);
  };
#define TOUCH(pa) asm volatile("global_load_dword v126, %0, off" DPOL4 : : "v"(pa) : "memory", "v126")
  uint32_t wv[5], xv[9];
  auto rows_xw = [&](int lane) {
    u32x4 wa, xr, xs;
    const uint32_t xo = (uint32_t)((8 * tg + 2 * wl) * (D_ * 2));
    asm volatile("global_load_dwordx4 %0, %5, %9\n\tglobal_load_dword %1, %6, %9 offset:1024\n\t"
                 "global_load_dwordx4 %2, %7, %10\n\tglobal_load_dwordx4 %3, %7, %10 offset:1152\n\tglobal_load_dword %4, %8, %10 offset:1024\n\ts_waitcnt vmcnt(0)"
                 : "=&v"(wa), "=&v"(wv[4]), "=&v"(xr), "=&v"(xs), "=&v"(xv[8])
                 : "v"(16u * lane), "v"(4u * (lane & 31)), "v"(xo + 16u * lane), "v"(xo + 4u * lane + 32u * (lane & 32)), "s"(wr), "s"(x) : "memory");
#pragma unroll
    for (int k = 0; k < 4; ++k) { wv[k] = wa[k]; xv[k] = xr[k]; xv[4 + k] = xs[k]; }
  };
  const int dX = 512 - 24 + 8 * bx + bj;
  if constexpr (ROLE == 2) {
    const int tl = tid - 256;
    if (!isx && T0) {
      SLEEP(T0SLEEP);
      if (wl < T0) TOUCH(w_line(24 * wl + bj));
      if (T1) { asm volatile("s_waitcnt vmcnt(0)" ::: "memory"); SLEEP(T1SLEEP); if (wl < T1) TOUCH(w_line(24 * (T0 + wl) + bj)); }
    }
    if (isx) {
      *(u32x4*)(smem + X_TB + 16 * tl) = zero4;
      *(u32x4*)(smem + X_TB + 16 * min(256 + tl, (TB + C_U) / 16 - 1)) = zero4;
      const int pr = (wl + XROT) & 3;
      COps e;
      conv_ops(lb0 + X_TB, lb0 + X_UL, lb0 + X_IM, lane, e);
      TOps t;
      table_ops(lb0 + X_IM, lb0 + X_TB, wl & 1, lane, t);
      e.o = lb0 + X_ST + (uint32_t)(16 * ((2 * i + (kq >> 1)) ^ ((i >> 2) & 1)) + 8 * (kq & 1));
      e.f = ca + 28 - 32;
      asm volatile("" : "+v"(e.o), "+v"(e.f));
      float ldt, are, aim, bre, bim, cre, cim, dsk;
      P_WAIT(0, , "v103", "v104", "v105", "v106", "v107", "v109");
      asm volatile("" : "={v109}"(ep));
      T = ((uint32_t)__builtin_amdgcn_readfirstlane(ep) + 1u) * 0x9E3779B1u;
      P_MORE();
      asm volatile("v_lshlrev_b32 %0, 16, v107" : "=v"(dsk) : : "v107");
      TS(0, 1);
      __builtin_amdgcn_s_setprio(QPRIO);
      int* evp = (int*)(sel + 44);
      char* im = smem + X_IM + IP * (tl & 127);
      evp[wl] = (wl < 2) ? gen_y(ldt, are, aim, bre, bim, cre, cim, lb0 + X_IM + IP * tl, [] {} TSA) : gen_x(ldt, are, aim, im);
      TS(0, 2);
      __builtin_amdgcn_s_setprio(QPRIO2);
      gsync(ca + 8, 4);
      if (wl < 2) evp[-4 + wl] = (int)table_tile(t, wl & 1, lane, lb0 + LM + 4 * (128 + 44), dsk TSA);
      gsync0(ca + 8, 8);
      const uint32_t cs = (uint32_t)__builtin_amdgcn_readfirstlane(evp[-4]);
      if (wl == 2 && lane == 0) asm volatile("global_store_dwordx2 %0, %1, %2 sc1" : : "v"(O_SC + 16u * dX), "v"((u32x2){T, cs}), "s"(ws) : "memory");
      if (wl == 3) *(u32x4*)(smem + X_IM + pad_at(lane & 7)) = zero4;
      TS(0, 3);
      CONV_RUN(2 + pr, e, , "+{v11}"(tid2));
      {
        const int lane = tid2 & 63;
        const u32x4 v = *(const u32x4*)(smem + X_ST + 2048 * pr + 1024 * (lane >> 5) + 16 * ((lane & 31) ^ ((lane >> 3) & 1)));
        if (__builtin_expect(v_put(v, (u32x4){utag(T, 0), utag(T, 1), utag(T, 2), utag(T, 3)}, cs != S16, v_at(dX, 64 * pr + lane)), 0)) {
          DRAIN();
          if (lane == 0) ST4(O_DN + 16u * (4 * (dX - 256) + pr), T);
        }
      }
      TS(0, 6);
    }
    __builtin_amdgcn_s_setprio(PPRIO);
    asm volatile("" : "+v"(tid2));
    rows_xw(tid2 & 63);
    if (!isx && !ffn) {
      SLEEP(RSLEEP);
#pragma unroll
      for (int k = 0; k < 7; ++k) ST16(O_SL + (uint32_t)((6 * bx + bj - 18) * 1792 + 256 * k - 256 + tid2) * 16u, ones);
      DRAIN();
    }
    if (!isx) BAR();
    const uint32_t tp = (uint32_t)__builtin_amdgcn_readfirstlane(cnt[4]);
    if (!isx) T = (uint32_t)__builtin_amdgcn_readfirstlane(sel[43]);
    tv = tp + UD + VD2 - vearly;
  } else {
    __builtin_amdgcn_s_setprio(3);
    if (tid < 12) cnt[tid] = 0u;
    BAR();
    uint32_t uo = u_at(tg, 64 * w + lane);
    uint32_t ta = lb0 + (uint32_t)(((lane >> 2) & 3) * RP + (64 * w + 16 * kq + 4 * (lane & 3)) * 2);
    if (tid < 20) ((uint32_t*)(hm + 320))[tid] = 0u;
    if (tid >= 184) *(u32x4*)(smem + L_SC - 16 * 184 + 16 * tid) = (u32x4){0x41804180u, 0x41804180u, 0x41804180u, 0x41804180u};
    uint32_t la = (uint32_t)(2 * w * RP + 16 * lane), lt = (uint32_t)((2 * w + (lane >> 5)) * RP + 1024 + 4 * (lane & 31));
    uint32_t sg = O_SU + (uint32_t)(256 * (lane >> 3) + tg) * 128u + 16u * (lane & 7);
    asm volatile("" : "+v"(uo), "+v"(ta), "+v"(la), "+v"(lt), "+v"(sg));
    uint32_t sink = 0;
    f32x4 z4 = {0.f, 0.f, 0.f, 0.f};
    asm volatile("" : "+v"(z4));
    asm volatile("s_waitcnt vmcnt(1)" : "={v[100:103]}"(xa), "={v[104:107]}"(xc), "={v108}"(r0) : : "v109");
    TS(0, 1);
    {
      const uint32_t v[9] = {xa[0], xa[1], xa[2], xa[3], xc[0], xc[1], xc[2], xc[3], r0};
      uint32_t c[9];
      f32x2 tf[9];
      auto sig = [&] {
        asm volatile("s_waitcnt vmcnt(0)" : "={v109}"(ep));
        T = ((uint32_t)__builtin_amdgcn_readfirstlane(ep) + 1u) * 0x9E3779B1u;
        if (w == 3) { const uint32_t now = (uint32_t)__builtin_amdgcn_s_memrealtime() + SD; ST16(sg, ((u32x4){T, now, T, now})); }
      };
      if (SIGPOS == 0) sig();
      ln_two(v, lane, 0.03125f, c, tf, z4, [&] { if (SIGPOS) sig(); });
      const uint32_t tj = ((T >> (3 * w)) & 0x00070007u) | (((T >> (12 + w)) & 0x00010001u) << 14);
      uint32_t tw[3] = {(tj & 0xffffu) * 0x10001u, (tj >> 16) * 0x10001u, 0u};
      tw[2] = (lane < 32) ? tw[0] : tw[1];
      asm volatile("" : "+v"(tw[0]), "+v"(tw[1]), "+v"(tw[2]));
#pragma unroll
      for (int k = 0; k < 9; ++k) c[k] = (packh((f32x2){bflo(c[k]), bfhi(c[k])}) & ~UMASK) | tw[k >> 2];
      *(u32x4*)(smem + la) = (u32x4){c[0], c[1], c[2], c[3]};
      *(u32x4*)(smem + la + RP) = (u32x4){c[4], c[5], c[6], c[7]};
      *(uint32_t*)(smem + lt) = c[8];
    }
    gsync0(ca + 12, 4);
    {
      const u32x2 h0 = trd(ta), h1 = trd(ta + 4 * RP), h2 = trd(ta + 512), h3 = trd(ta + 512 + 4 * RP);
      ST16(uo, ((u32x4){h0[0], h0[1], h1[0], h1[1]}));
      ST16(uo + 64 * 128, ((u32x4){h2[0], h2[1], h3[0], h3[1]}));
    }
    if (w == 0) {
      const u32x2 h0 = trd(ta + 1024), h1 = trd(ta + 1024 + 4 * RP);
      ST16(uo + 128 * 128, ((u32x4){h0[0], h0[1], h1[0], h1[1]}));
    }
    TS(0, 2);
    if (!isx && 3 - w < TB1) TOUCH(w_line(24 * (3 - w) + 23 - bj));
    asm volatile("s_waitcnt vmcnt(0)" : "+v"(sink) : : "memory");
    TS(0, 13);
    if (w == 0) {
#pragma unroll
      for (int k = 0; k < 2; ++k) ST16(O_XC + (uint32_t)((511 - 2 * tg) * 144 + 64 * k + lane) * 16u, ones);
      const uint32_t oz = (lane < 16) ? O_XC + (uint32_t)((511 - 2 * tg) * 144 + 128 + lane) * 16u : O_FB + (uint32_t)(8 * b + lane - 16) * 16u;
      if (lane < ((b < 72) ? 24 : 16)) ST16(oz, ones);
    }
    SLEEP(ESLEEP);
    uint32_t tu = latest(gate(ws, O_SU + (uint32_t)(256 * bx + 64 * w + 4 * i + (bj & 3)) * 128u, T));
    asm volatile("s_mov_b64 exec, 1\n\tds_max_u32 %0, %1\n\ts_mov_b64 exec, -1" : : "v"(ca + 16), "v"(tu) : "memory");
    if (w == 0) sel[43] = T;
    if (isx) {
      const uint32_t xo = u_at(tid, dX);
      char* sm = smem + X_UL + C_U - 16 + 16 * tid;
      const u32x4 ut = {utag(T, 0), utag(T, 1), utag(T, 2), utag(T, 3)};
      until(tu + UD + XD);
      u32x4 uw;
      asm volatile("global_load_dwordx4 %0, %1, %2 sc1" : "=&v"(uw) : "v"(xo), "s"(ws) : "memory");
      asm volatile("s_waitcnt vmcnt(0)" : "+v"(uw));
      uw ^= ut;
      u_again(uw, ut, xo);
      *(u32x4*)(sm + 16) = uw;
      BAR();
      if (w < 3) {
        const u32x4 pw = *(const u32x4*)sm;
        if (w < 2) sums<true>(smem + X_IM + 16 * tid, pw, uw); else sums<false>(smem + X_IM + 16 * tid, pw, uw);
        count_up(ca + 28);
      }
    } else {
      until(tu + UD + WD);
      BAR();
    }
    tu = (uint32_t)__builtin_amdgcn_readfirstlane(cnt[4]);
    TS(0, 3);
    __builtin_amdgcn_s_setprio(DPRIO);
    auto behind_v = [&](int dt) {
      until(tu + UD + VD2 + dt);
    };
    if (ffn) {
      gu_dma(0, NA, IC<WDEPTH>{});
      if (NA >= 9) {
        asm volatile("s_waitcnt vmcnt(%0)" : : "n"(NA - 9) : "memory");
        gsync(ca + 4, 4);
        gu_read(0);
      }
      DRAIN();
      TS(0, 4);
      behind_v(RD);
      TS(0, 8);
      gu_dma(NA, 15, IC<DDEPTH>{});
      if (NA < 9) {
        asm volatile("s_waitcnt vmcnt(6)" ::: "memory");
        gsync(ca + 4, 4);
        gu_read(0);
      }
      gsync(ca + 4, 8);
      down_dma();
      gwait(ca, 8);
      gu_dma(15, 18, IC<DDEPTH>{});
      DRAIN();
      TS(0, 7);
      gsync0(ca + 4, 12);
    } else {
      if (!isx && C3) {
        const int n = 6 * min(tid, 191) + bj - 18, r = n >> 2;
        const char* wb = (const char*)((n & 1) ? wu : wg) + bx * 1152;
        const int c0 = (3 - (int)((uintptr_t)wb >> 7)) & 7;
        wb += (uint32_t)((r + 144 * ((r >= 144) + ((n >> 1) & 1))) * (F_ * 2) + 128 * c0);
        TOUCH(wb);
        if (c0 == 0) TOUCH(wb + 1024);
      }
      auto d_line = [&](int k) {
        const int n = min((k * 14 + bj - 18) * 64 + lane, 5183), q = n / 1296, r = n - 1296 * q;
        return (const char*)wd + (uint32_t)(bx * 5184 + (r / 18) * 72 + 18 * q + r % 18) * 128u;
      };
      if (w < (isx ? HAX : HA)) TOUCH(d_line(w));
      DRAIN();
      TS(0, 4);
      behind_v(RD2);
      if (w < HW) {
#pragma unroll 1
        for (int k = (isx ? HAX : HA) + w; k < 6; k += HW) { TOUCH(d_line(k)); DRAIN(); }
      }
      TS(0, 7);
      gwait(ca, 8);
    }
    tk = tu + UD + VD2 + KD;
  }
  {
  const int tid = tid2, lane = tid & 63, i = lane & 15, kq = lane >> 4;
  __builtin_amdgcn_s_setprio(3);
  constexpr int PL = LW + 9216 - 2560;
  f32x2 xf[9];
  f32x4 za = {0.f, 0.f, 0.f, 0.f}, zb = za;
  uint32_t pa = (uint32_t)(PL + 9216 * wl + 32 * lane), pb = (uint32_t)(PL + 9216 * wl + 2048 + 8 * (lane & 31)), ps = (lane < 32) ? 0u : 16u;
  uint32_t kid = (uint32_t)(((255 - tg) << 5) | (7 - 2 * wl - (lane >> 5)));
  uint32_t al = O_XC + 64u * (i >> 3) + 16u * kq + 128u * w;
  uint32_t ha = (ROLE == 1) ? (uint32_t)((4 * (kq & 1) + 2 * (kq >> 1) + (i >> 3)) * 80 + 16 * w + 2 * (i & 7)) : 0u;
  uint32_t h0a = (uint32_t)(min(i, 8) * 80 + 16 * kq), h1a = (uint32_t)(min(i ^ 8, 8) * 80 + 16 * kq);
  uint32_t so = O_SL + (uint32_t)fb * SP + ((ROLE == 2) ? 16u * tid - 4096u : 8192u + 16u * lane);
  uint32_t da = lb0 + (uint32_t)((8 * kq + ((lane >> 2) & 3)) * 1152 + 32 * ((8 * kq + ((lane >> 2) & 3) + 2) >> 2) + 8 * (lane & 3) + ((ROLE == 2) ? 128 * wl : 1024));
  if constexpr (ROLE == 2) {
    const int tl = tid - 256;
    uint32_t vo = v_at(tl, tg), vo2 = v_at(512 + 16 * wl + i, tg), px = (uint32_t)(PL + 4 * tl), py = (uint32_t)(PL + 2048 + 64 * wl + 4 * i);
#pragma unroll
    for (int k = 0; k < 9; ++k) xf[k] = (f32x2){bflo(xv[k]), bfhi(xv[k])};
    asm volatile("" : "+v"(xf[0]), "+v"(xf[1]), "+v"(xf[2]), "+v"(xf[3]), "+v"(xf[4]), "+v"(xf[5]), "+v"(xf[6]), "+v"(xf[7]), "+v"(xf[8]));
    asm volatile("" : "+v"(za), "+v"(zb), "+v"(pa), "+v"(pb), "+v"(ps), "+v"(kid), "+v"(h0a), "+v"(h1a), "+v"(so), "+v"(da), "+v"(vo), "+v"(vo2), "+v"(px), "+v"(py));
    u32x4 ut = {utag(T, 0), utag(T, 1), utag(T, 2), utag(T, 3)};
    asm volatile("" : "+v"(ut));
    TS(0, 7);
    until(tv);
    TS(0, 8);
    u32x4 va, vc, vb = ut;
    asm volatile("global_load_dwordx4 %0, %3, %6 sc1\n\tglobal_load_dwordx4 %1, %4, %6 sc1\n\ts_mov_b64 exec, 0xffff\n\tglobal_load_dwordx4 %2, %5, %6 sc1\n\ts_mov_b64 exec, -1\n\t"
                 "s_waitcnt vmcnt(2)" : "=&v"(va), "=&v"(vc), "+v"(vb) : "v"(vo), "v"(vo + 128 * VP), "v"(vo2), "s"(ws) : "memory");
    auto turn = [&] {
#pragma unroll
      for (int e = 0; e < 4; ++e) *(uint32_t*)(smem + px + 1024 + 9216 * e) = vc[e];
      if (lane < 16) {
#pragma unroll
        for (int e = 0; e < 4; ++e) *(uint32_t*)(smem + py + 9216 * e) = vb[e];
      }
    };
    va ^= ut;
#pragma unroll
    for (int e = 0; e < 4; ++e) *(uint32_t*)(smem + px + 9216 * e) = va[e];
    asm volatile("s_waitcnt vmcnt(0)" : "+v"(vc), "+v"(vb) : : "memory");
    vc ^= ut; vb ^= ut;
    turn();
    const u32x4 vs = va | vb | vc;
    if (__builtin_expect(__any(((vs[0] | vs[1] | vs[2] | vs[3]) & UMASK) != 0u), 0)) {
      auto again = [&](u32x4& v, uint32_t o, int d) {
        const int J = tg >> 5;
        const uint32_t dn = O_DN + 16u * (uint32_t)((d < 512) ? 4 * (d >> 1) + ((0x01013232 >> (4 * J)) & 3) : 4 * (d - 256) + (J >> 1));
        bool pend = ((v[0] | v[1] | v[2] | v[3]) & UMASK) != 0u;
#pragma unroll 1
        for (int spin = 0; spin < SPIN_CAP && __any(pend); ++spin) {
          TSC(16);
          SLEEP(PSLEEP);
          if (pend) {
            uint32_t fin = ~T;
            if ((v[0] & v[1] & v[2] & v[3] & LMASK) == LMASK)
              asm volatile("global_load_dword %0, %1, %2 sc1\n\ts_waitcnt vmcnt(0)" : "=&v"(fin) : "v"(dn), "s"(ws) : "memory");
            LD16W(v, o);
            v ^= ut;
            pend = ((v[0] | v[1] | v[2] | v[3]) & UMASK) != 0u;
            if (pend && fin == T) {
              u32x2 sc;
              for (int k = 0; k < SPIN_CAP; ++k) {
                asm volatile("global_load_dwordx2 %0, %1, %2 sc1\n\ts_waitcnt vmcnt(0)" : "=&v"(sc) : "v"(O_SC + 16u * d), "s"(ws) : "memory");
                if (sc[0] == T) break;
              }
              *(u16*)(smem + L_SC + 2 * d) = (u16)(sc[1] >> 16);
              cnt[10] = 1u;
              v = (v ^ ut) & ~LMASK;
              pend = false;
            }
          }
        }
      };
      again(va, vo, tl); again(vc, vo + 128 * VP, tl + 256); again(vb, vo2, 512 + 16 * wl + i);
#pragma unroll
      for (int e = 0; e < 4; ++e) *(uint32_t*)(smem + px + 9216 * e) = va[e];
      turn();
    }
    gsync0(ca, 4);
    TS(0, 9);
  } else asm volatile("" : "+v"(ha), "+v"(al), "+v"(da));
  uint32_t hv[9], nv[9], keya = 0u, keyb = 0u;
  if constexpr (ROLE == 2) {
    const u32x4 a0 = *(const u32x4*)(smem + pa), a1 = *(const u32x4*)(smem + pa + 16);
    const u32x2 a2 = *(const u32x2*)(smem + pb);
    float sv[10];
#pragma unroll
    for (int k = 0; k < 10; ++k) sv[k] = 16.f;
    if (__builtin_expect(__builtin_amdgcn_readfirstlane(cnt[10]) != 0, 0)) {
      const u32x4 q = *(const u32x4*)(smem + L_SC + 16 * lane);
      const uint32_t r = *(const uint32_t*)(smem + L_SC + 1024 + 4 * (lane & 31));
#pragma unroll
      for (int k = 0; k < 4; ++k) { sv[2 * k] = bflo(q[k]); sv[2 * k + 1] = bfhi(q[k]); }
      sv[8] = bflo(r); sv[9] = bfhi(r);
    }
    const uint32_t av[10] = {a0[0], a0[1], a0[2], a0[3], a1[0], a1[1], a1[2], a1[3], a2[0] >> ps, a2[1] >> ps};
    f32x2 h[9];
#pragma unroll
    for (int k = 0; k < 10; ++k) {
      asm("v_fma_mix_f32 %0, %1, %2, %3 op_sel_hi:[1,0,0]" : "=v"(h[(k < 8) ? k >> 1 : 8][k & 1]) : "v"(av[k]), "v"(sv[k]), "v"(xf[(k < 8) ? k >> 1 : 8][k & 1]));
      if (k < 8) asm("v_fma_mix_f32 %0, %1, %2, %3 op_sel:[1,0,0] op_sel_hi:[1,0,0]" : "=v"(h[4 + (k >> 1)][k & 1]) : "v"(av[k]), "v"(sv[k]), "v"(xf[4 + (k >> 1)][k & 1]));
    }
#pragma unroll
    for (int k = 0; k < 9; ++k) hv[k] = pack2(h[k][0], h[k][1]);
    ln_two(hv, lane, 1.f, nv, xf, za, [] {});
    const float da = dot2(nv[2], wv[2], dot2(nv[0], wv[0], zb[0])) + dot2(nv[3], wv[3], dot2(nv[1], wv[1], zb[1]));
    const float db = dot2(nv[6], wv[2], dot2(nv[4], wv[0], zb[2])) + dot2(nv[7], wv[3], dot2(nv[5], wv[1], zb[3]));
    float dt = row_sum(dot2(nv[8], wv[4], fold32(da, db)));
    asm("s_nop 1\n\tv_add_f32_dpp %0, %0, %0 row_bcast:15 row_mask:0xa bank_mask:0xf\n\ts_nop 0" : "+v"(dt));
    const uint32_t bb = pack2(dt, dt) & 0xffff0000u;
    const uint32_t kk = (bb ^ (((uint32_t)((int)bb >> 31) & 0x7fff0000u) | 0x80000000u)) | kid;
    if ((lane & 31) == 31) kl[2 * wl + (lane >> 5)] = kk;
    keya = (uint32_t)__builtin_amdgcn_readlane(kk, 31); keyb = (uint32_t)__builtin_amdgcn_readlane(kk, 63);
    gsync0(ca, 8);
  }
  if (ROLE == 2 && w == 4) {
    const u32x4 ka = *(const u32x4*)kl, kb = *(const u32x4*)(kl + 4);
    uint32_t m1 = max(max(ka[0], ka[1]), ka[2]), m2 = med3u(ka[0], ka[1], ka[2]), m3 = min(min(ka[0], ka[1]), ka[2]);
#pragma unroll
    for (int e = 0; e < 5; ++e) {
      const uint32_t kx = e ? kb[e - 1] : ka[3];
      m3 = med3u(m2, m3, kx); m2 = med3u(m1, m2, kx); m1 = max(m1, kx);
    }
    if (lane < 8) ST16(O_KEY + (uint32_t)(256 * lane + 8 * (tg & 31) + (tg >> 5)) * 16u, ((u32x4){m1 | 24u, m2 | 16u, m3 | 8u, T & ~1u}));
  }
  if (ROLE == 2 && w == 5) {
    const u32x4 kv = *(const u32x4*)&kl[4 * (lane & 1)];
    if (lane < 2) ST16(O_KF + (uint32_t)(2 * tg + lane) * 16u, ((u32x4){__builtin_amdgcn_perm(kv[1], kv[0], 0x07060302u), T, __builtin_amdgcn_perm(kv[3], kv[2], 0x07060302u), T}));
    if (b == 0 && lane == 0) ST4(O_EP, T * 0x0E8B2F51u);
  }
  if (ROLE == 2) TS(0, 10);
  auto put_row = [&](const uint32_t* r, int j, const void* base, uint32_t ro) {
    const u32x4 tl = {r[8], (uint32_t)DPP_I((int)r[8], 0x55, 0xf), (uint32_t)DPP_I((int)r[8], 0xaa, 0xf), (uint32_t)DPP_I((int)r[8], 0xff, 0xf)};
    asm volatile("global_store_dwordx4 %0, %1, %2 sc1\n\ts_nop 1" : : "v"(ro + 16u * lane), "v"((u32x4){r[4 * j], r[4 * j + 1], r[4 * j + 2], r[4 * j + 3]}), "s"(base) : "memory");
    if ((lane & 35) == 32 * j) asm volatile("global_store_dwordx4 %0, %1, %2 offset:1024 sc1\n\ts_nop 1" : : "v"(ro + 4u * (lane & 31)), "v"(tl), "s"(base) : "memory");
  };
  int rka = 0, rkb = 0;
  uint32_t k3rd = 0u;
  if constexpr (ROLE == 2) {
    __builtin_amdgcn_s_setprio(0);
    const uint32_t ko = kl[lane & 7];
    rka = FORCEFB + __popcll(__ballot(ko > keya) & 0xffull); rkb = FORCEFB + __popcll(__ballot(ko > keyb) & 0xffull);
#pragma unroll
    for (int k = 0; k < 9; ++k) {
      const f32x2 o = xf[k] + (f32x2){bflo(nv[k]), bfhi(nv[k])};
      hv[k] = pack2(o[0], o[1]);
      nv[k] = min(nv[k], 0xfffffffeu);
    }
    if (rka < 2) put_row(nv, 0, ws, O_XC + (uint32_t)(1023 - 4 * tg - rka) * 1152u);
    if (rkb < 2) put_row(nv, 1, ws, O_XC + (uint32_t)(1023 - 4 * tg - rkb) * 1152u);
    __builtin_amdgcn_s_setprio(3);
  } else {
    u32x4 key;
    const uint32_t kof = O_KEY + (uint32_t)(256 * bx + tid) * 16u;
    gu_read(1);
    TS(0, 10);
    until(tk);
    if (!ffn) SLEEP(NSLEEP);
    LD16W(key, kof);
    if (__builtin_expect(__any(key[3] != (T & ~1u)), 0)) {
#pragma unroll 1
      for (int spin = 0; spin < SPIN_CAP; ++spin) {
        const bool need = key[3] != (T & ~1u);
        if (!__any(need)) break;
        TSC(15);
        SLEEP(KBACK);
        if (need) LD16W(key, kof);
      }
    }
    TS(0, 11);
    const uint32_t m1 = oct_maxu(key[0]);
    const bool hit = key[0] == m1;
    const uint32_t k0 = hit ? key[1] : key[0], k1 = hit ? key[2] : key[1];
    const uint32_t m2 = oct_maxu(k0);
    if ((lane & 7) == 0) *(u32x2*)&cand[tid >> 2] = (u32x2){m1, m2};
    k3rd = (k0 == m2) ? k1 : k0;
    if (RPF && !ffn && w == (bj & 3)) {
      const uint32_t r1 = O_XC + ((m1 >> 3) & 0x3ffu) * 1152u + 128u * (lane & 7), r2 = O_XC + ((m2 >> 3) & 0x3ffu) * 1152u + 128u * (lane & 7);
      asm volatile("global_load_dword v126, %0, %3 sc1\n\tglobal_load_dword v126, %1, %3 sc1\n\tglobal_load_dword v126, %2, %3 offset:1024 sc1"
                   : : "v"(r1), "v"(r2), "v"(((lane & 1) ? r1 : r2) - 128u * (lane & 7)), "s"(ws) : "memory", "v126");
    }
  }
  auto pick = [&](const uint32_t* cd, uint32_t* sl) {
    BAR();
    const uint32_t c = cd[lane], own = cd[8 * w + (lane & 7)];
    const u32x4 la = *(const u32x4*)&cd[8 * w], lb = *(const u32x4*)&cd[8 * w + 4];
    uint32_t r = C_;
    asm("v_cmp_gt_u32 s[40:41], %1, %2\n\tv_cmp_gt_u32 s[42:43], %1, %3\n\tv_cmp_gt_u32 s[44:45], %1, %4\n\tv_cmp_gt_u32 s[46:47], %1, %5\n\t"
        "v_cmp_gt_u32 s[48:49], %1, %6\n\tv_cmp_gt_u32 s[50:51], %1, %7\n\tv_cmp_gt_u32 s[52:53], %1, %8\n\tv_cmp_gt_u32 s[54:55], %1, %9\n\t"
        "s_bcnt1_i32_b64 s40, s[40:41]\n\ts_bcnt1_i32_b64 s42, s[42:43]\n\tv_writelane_b32 %0, s40, 0\n\ts_bcnt1_i32_b64 s44, s[44:45]\n\t"
        "v_writelane_b32 %0, s42, 1\n\ts_bcnt1_i32_b64 s46, s[46:47]\n\tv_writelane_b32 %0, s44, 2\n\ts_bcnt1_i32_b64 s48, s[48:49]\n\t"
        "v_writelane_b32 %0, s46, 3\n\ts_bcnt1_i32_b64 s50, s[50:51]\n\tv_writelane_b32 %0, s48, 4\n\ts_bcnt1_i32_b64 s52, s[52:53]\n\t"
        "v_writelane_b32 %0, s50, 5\n\ts_bcnt1_i32_b64 s54, s[54:55]\n\tv_writelane_b32 %0, s52, 6\n\tv_writelane_b32 %0, s54, 7"
        : "+v"(r) : "v"(c), "v"(la[0]), "v"(la[1]), "v"(la[2]), "v"(la[3]), "v"(lb[0]), "v"(lb[1]), "v"(lb[2]), "v"(lb[3])
        : "scc", "s40", "s41", "s42", "s43", "s44", "s45", "s46", "s47", "s48", "s49", "s50", "s51", "s52", "s53", "s54", "s55");
    sl[min(r, (uint32_t)C_)] = own;
    BAR();
  };
  auto rows = [&](uint32_t ao, auto&& meanwhile) {
    u32x4 aa, ab, ac = __builtin_bit_cast(u32x4, zb);
    asm volatile("global_load_dwordx4 %0, %3, %4 sc1\n\tglobal_load_dwordx4 %1, %3, %4 offset:512 sc1\n\t"
                 "s_cmp_lg_u32 %5, 0\n\ts_cbranch_scc1 1f\n\tglobal_load_dwordx4 %2, %3, %4 offset:1024 sc1\n1:"
                 : "=&v"(aa), "=&v"(ab), "+v"(ac) : "v"(ao), "s"(ws), "s"(w) : "memory", "scc");
    __builtin_amdgcn_sched_barrier(0);
    meanwhile();
    asm volatile("s_waitcnt vmcnt(0)" : "+v"(aa), "+v"(ab), "+v"(ac) : : "memory");
    if (__builtin_expect(__any(max(max(max4(aa), max4(ab)), max4(ac)) == 0xffffffffu), 0)) {
#pragma unroll 1
      for (int spin = 0; spin < SPIN_CAP; ++spin) {
        const bool na = max4(aa) == 0xffffffffu, nb = max4(ab) == 0xffffffffu, nc = max4(ac) == 0xffffffffu;
        if (!__any(na || nb || nc)) break;
        TSC(14);
        SLEEP(PSLEEP);
        if (na) LD16W(aa, ao);
        if (nb) LD16W(ab, ao + 512u);
        if (nc) LD16W(ac, ao + 1024u);
      }
    }
    *(u32x4*)(smem + LA + 16 * tid) = aa;
    *(u32x4*)(smem + LA + 4096 + 16 * tid) = ab;
    if (w == 0) *(u32x4*)(smem + LA + 8192 + 16 * tid) = ac;
  };
  auto get_bd = [&](int t0, int t1) {
#pragma unroll
    for (int t = t0; t < t1; ++t) {
      const u32x2 h0 = trd(da + 512 * (t >> 2) + 32 * (t & 3)), h1 = trd(da + 512 * (t >> 2) + 32 * (t & 3) + 4608 + 32);
      bw[t] = (u32x4){h0[0], h0[1], h1[0], h1[1]};
    }
#pragma unroll
    for (int t = t0; t < t1; ++t) asm volatile("" : "+v"(bw[t]));
  };
  pick(cand, sel);
  uint32_t mykey = sel[lane & 7], ro = ((mykey >> 3) & 0x3ffu) * 1152u;
  TS(0, 12);
  auto check = [&] {
    cnt[(k3rd > (uint32_t)__builtin_amdgcn_readlane(mykey, 7)) ? 5 : 6] = 1u;
  };
  if constexpr (ROLE == 1) {
    if (ffn && !FORCEFB) rows(ro + al, check); else { check(); DRAIN(); }
  } else if (ffn) get_bd(0, 8);
  BAR();
  TS(0, 5);
  auto put_out = [&](int j, uint32_t k0, int rk) {
    const uint64_t mine = __ballot(((mykey ^ k0) & ~24u) == 0u) & 0xffull;
    if (mine == 0ull) put_row(hv, j, out, (uint32_t)((8 * tg + 2 * wl + j) * (D_ * 2)));
    else if (rk >= 2) put_row(nv, j, ws, O_FB + 1152u * (uint32_t)__builtin_ctzll(mine));
  };
  auto rest_out = [&] { put_out(0, keya, rka); put_out(1, keyb, rkb); };
  f32x4 lo, hi;
  auto gate_up = [&] {
    u32x4 av[5];
    const uint32_t aa = lb0 + LA + 16u * lane;
#pragma unroll
    for (int j = 0; j < 5; ++j) asm volatile("ds_read_b128 %0, %1 offset:%2" : "=v"(av[j]) : "v"(aa), "n"(1024 * j) : "memory");
    lo = (f32x4){0.f, 0.f, 0.f, 0.f}; hi = lo;
#pragma unroll
    for (int j = 0; j < 9; ++j) {
      if (j < 5) asm volatile("s_waitcnt lgkmcnt(4)" : "+v"(av[j % 5]));
      else if (j == 5) asm volatile("s_waitcnt lgkmcnt(2)" : "+v"(av[0]), "+v"(av[1]));
      else if (j == 7) asm volatile("s_waitcnt lgkmcnt(0)" : "+v"(av[2]), "+v"(av[3]));
      lo = MMB(av[j % 5], bw[2 * j], lo);
      hi = MMB(av[j % 5], bw[2 * j + 1], hi);
      __builtin_amdgcn_sched_barrier(0);
      if (j < 4) asm volatile("ds_read_b128 %0, %1 offset:%2" : "=v"(av[j]) : "v"(aa), "n"(1024 * (j + 5)) : "memory");
    }
  };
  uint32_t fl;
  asm volatile("ds_read_b32 %0, %1" : "=v"(fl) : "v"(ca + 20) : "memory");
  if (ROLE == 1) gate_up();
  asm volatile("s_waitcnt lgkmcnt(0)" : "+v"(fl));
  const bool slow = FORCESLOW || FORCEFB || __builtin_amdgcn_readfirstlane(fl) != 0;
  if (__builtin_expect(slow, 0)) {
    u32x4 key;
#pragma unroll 1
    for (int spin = 0; spin < SPIN_CAP; ++spin) {
      LD16W(key, O_KF + 16u * tid);
      if (!__any(key[1] != T || key[3] != T)) break;
      SLEEP(KBACK);
    }
    const uint32_t ib = (uint32_t)(((255 - (tid >> 1)) << 5) | (7 - 4 * (tid & 1)));
    uint32_t k0 = (key[0] << 16) | ib, k1 = (key[0] & 0xffff0000u) | (ib - 1), k2 = (key[2] << 16) | (ib - 2), k3 = (key[2] & 0xffff0000u) | (ib - 3);
    CEX(k0, k1) CEX(k2, k3) CEX(k0, k2) CEX(k1, k3) CEX(k1, k2)
#pragma unroll 1
    for (int c = 0; c < C_; ++c) {
      const uint32_t mx = wave_maxu(k0);
      const bool hit = (k0 == mx);
      k0 = hit ? k1 : k0; k1 = hit ? k2 : k1; k2 = hit ? k3 : k2; k3 = hit ? 0u : k3;
      if (lane == 0) cand[64 + 8 * w + c] = mx;
    }
    pick(cand + 64, sel + 16);
    mykey = sel[16 + (lane & 7)];
    const uint32_t okey = sel[16 + (lane >> 3)];
    const uint64_t same = __ballot(((mykey ^ okey) & 0x1fe0u) == 0u && okey > mykey);
    const int sl = FORCEFB + __popcll((same >> (lane & 7)) & 0x0101010101010101ull);
    ro = (sl < 2) ? (((mykey >> 3) & 0x3ffu) + 3 - sl) * 1152u : O_FB - O_XC + 1152u * (lane & 7);
    if (ROLE == 2) rest_out();
    if (ffn) {
      if (ROLE == 1) rows(ro + al, [] {});
      BAR();
      if (ROLE == 1) gate_up();
    }
  }
  if (OPOS == 0 && ROLE == 2 && ffn && !slow) { SLEEP(OD); rest_out(); }
  if (!ffn) {
    if (ROLE == 2 && !slow) { SLEEP(OD2); rest_out(); }
    TSE();
    return;
  }
  uint32_t fx = 0, fv = 0, fn = 0, fo = 0, sa[9];
  float rs, hx, xs;
  u32x4 se;
  if constexpr (ROLE == 1) {
    const f32x4 lh = (lane < 32) ? lo : hi;
    const float fa = fold32(lh[0], lh[2]), fb2 = fold32(lh[1], lh[3]);
    const float g = __int_as_float(__builtin_amdgcn_update_dpp(__float_as_int(fa), __float_as_int(fb2), 0x128, 0xf, 0xc, false));
    const float u = __int_as_float(__builtin_amdgcn_update_dpp(__float_as_int(fb2), __float_as_int(fa), 0x128, 0xf, 0x3, false));
    const uint32_t gu = pack2(g, u);
    const float gp = bflo(gu);
    const float sg = rbf(gp * __builtin_amdgcn_rcpf(1.f + __expf(-gp)));
    *(u16*)((char*)hm + ha) = f2bf(sg * bfhi(gu));
  }
  BAR();
  TS(0, 6);
  if (ROLE == 2 || w == 2) {
    const u32x4 a0 = *(const u32x4*)((char*)hm + h0a), a1 = *(const u32x4*)((char*)hm + h1a);
    if (ROLE == 1) get_bd(0, 4);
    const f32x4 z = {0.f, 0.f, 0.f, 0.f};
    f32x4 o[4];
#pragma unroll
    for (int t = 0; t < 2 * ROLE; ++t) o[t] = MMB(a1, bw[2 * t + 1], MMB(a0, bw[2 * t], z));
#pragma unroll
    for (int t = 0; t < ROLE; ++t) {
      u32x4 sv;
#pragma unroll
      for (int r = 0; r < 4; ++r) sv[r] = min(pack2(o[2 * t][r], o[2 * t + 1][r]), 0xfffffffeu);
      ST16(so + 4096u * t, sv);
    }
    TS(0, 13);
  }
  if (OPOS == 1 && ROLE == 2) {
    SLEEP(OSLEEP);
    if (!slow) rest_out();
  }
  if (ROLE == 1 && w == 1) {
    const int q = (fb & 15) >> 2, ts = 4 * (q & 1) + ((i & 7) >> 1);
    const int dd = 64 * (fb >> 4) + 16 * (2 * (i & 1) + (q >> 1)) + 4 * (fb & 3) + 2 * (kq & 1) + (kq >> 1);
    const uint32_t fkey = (uint32_t)__builtin_amdgcn_ds_bpermute(4 * ts, (int)mykey), fr = (uint32_t)__builtin_amdgcn_ds_bpermute(4 * ts, (int)ro);
    const int ft = 2047 - (int)(((fkey >> 2) & 0x7f8u) | (fkey & 7u));
    fo = (uint32_t)(ft * D_ + dd) * 2u;
    u32x2 fs;
    asm volatile("global_load_ushort %0, %4, %8\n\tglobal_load_ushort %1, %5, %9 sc1\n\tglobal_load_ushort %2, %6, %9 sc1\n\tglobal_load_dwordx2 %3, %7, %9 sc1"
                 : "=&v"(fx), "=&v"(fv), "=&v"(fn), "=&v"(fs) : "v"(fo), "v"(v_at(dd, ft >> 3) + 2u * (ft & 7)), "v"(O_XC + fr + 2u * dd), "v"(O_SC + 16u * dd), "s"(x), "s"(ws) : "memory");
    uint32_t t16 = (((T >> (3 * ((ft >> 1) & 3))) & LMASK) | (((T >> (12 + ((ft >> 1) & 3))) & 0x00010001u) << 14)) >> (16 * (ft & 1)) & 0xffffu;
    const uint32_t o = fkey >> 16;
    const float rs0 = bf2f((u16)((o & 0x8000u) ? (o & 0x7fffu) : (~o & 0xffffu)));
    rs = rbf(__builtin_amdgcn_rcpf(1.f + __expf(-rs0)));
    const uint32_t one = (i < 8) ? (0x3f80u << (16 * (i & 1))) : 0u;
    se = (u32x4){(i >> 1) == 0 ? one : 0u, (i >> 1) == 1 ? one : 0u, (i >> 1) == 2 ? one : 0u, (i >> 1) == 3 ? one : 0u};
#pragma unroll
    for (int m = 0; m < 9; ++m) sa[m] = O_SL + (uint32_t)(16 * m + (lane >> 2)) * SP + (uint32_t)(4 * fb + (lane & 3)) * 16u;
    asm volatile("" : "+v"(t16), "+v"(rs), "+v"(se), "+v"(sa[0]), "+v"(sa[1]), "+v"(sa[2]), "+v"(sa[3]), "+v"(sa[4]), "+v"(sa[5]), "+v"(sa[6]), "+v"(sa[7]), "+v"(sa[8]));
    SLEEP(SSLEEP);
    TS(0, 1);
    float sum;
#pragma unroll 1
    for (int spin = 0; spin < SPIN_CAP; ++spin) {
      u32x4 sv[9];
#pragma unroll
      for (int m = 0; m < 9; ++m) LD16(sv[m], sa[m]);
      asm volatile("s_waitcnt vmcnt(9)" : "+v"(fx), "+v"(fv), "+v"(fn), "+v"(fs));
      if (__builtin_expect(__any(fs[0] != T), 0)) {
        for (int k = 0; k < SPIN_CAP && fs[0] != T; ++k)
          asm volatile("global_load_dwordx2 %0, %1, %2 sc1\n\ts_waitcnt vmcnt(0)" : "=&v"(fs) : "v"(O_SC + 16u * dd), "s"(ws) : "memory");
      }
      const uint32_t f1 = fv ^ t16, f2 = (f1 & 7u) ? fv & ~7u : f1;
      hx = rbf(bf2f((u16)fx) + (float)__builtin_bit_cast(_Float16, (u16)f2) * __uint_as_float(fs[1]));
      xs = bf2f((u16)fn);
      asm volatile("" : "+v"(hx), "+v"(xs));
      f32x4 dv = {0.f, 0.f, 0.f, 0.f};
      static_for<0, 9>([&](auto mm) {
        constexpr int m = mm.value;
        asm volatile("s_waitcnt vmcnt(%1)" : "+v"(sv[m]) : "n"(8 - m));
        dv = MMB(sv[m], se, dv);
        __builtin_amdgcn_sched_barrier(0);
      });
      sum = fold16(fold32(dv[0], dv[1]), fold32(dv[2], dv[3]));
      if (__builtin_expect(!__any(sum != sum), 1)) break;
      uint32_t mx = 0u;
#pragma unroll
      for (int m = 0; m < 9; ++m) mx = max(mx, max4(sv[m]));
      if (!__any(mx == 0xffffffffu)) break;
      TSC(18);
      SLEEP(PSLEEP);
    }
    const float o1 = rbf(xs + rbf(rbf(sum) * rs));
    if (i < 8) asm volatile("global_store_short %0, %1, %2 sc1" : : "v"(fo), "v"((uint32_t)f2bf(hx + o1)), "s"(out) : "memory");
    TS(0, 2);
  }
  TSE();
  }
  }
}
extern "C" __global__ __launch_bounds__(512, 4) void kf_all(KARGS) {
  __shared__ __attribute__((aligned(16))) char smem[SMEM];
  asm volatile("v_readfirstlane_b32 s98, v0\n\ts_load_dwordx16 s[64:79], s[0:1], 0x0\n\ts_load_dwordx4 s[80:83], s[0:1], 0x40\n\t"
               "s_cmpk_lt_u32 s2, 0x100\n\ts_cbranch_scc0 1f\n\ts_cmpk_lt_u32 s98, 0x100\n\ts_cbranch_scc0 2f\n\t"
               "s_and_b32 s84, s2, 0xe0\n\ts_bfe_u32 s85, s2, 0x20003\n\ts_or_b32 s84, s84, s85\n\ts_and_b32 s85, s2, 7\n\ts_lshl2_add_u32 s84, s85, s84\n\t"
               "s_mulk_i32 s84, 0x2400\n\ts_mulk_i32 s98, 36\n\ts_add_u32 s84, s84, s98\n\tv_and_b32 v110, 63, v0\n\tv_and_b32 v111, 32, v0\n\t"
               "v_lshlrev_b32 v111, 5, v111\n\tv_lshl_add_u32 v111, v110, 2, v111\n\tv_lshl_add_u32 v110, v110, 4, s84\n\tv_add_u32 v111, s84, v111\n\t"
               "v_mov_b32 v109, 0\n\ts_waitcnt lgkmcnt(0)\n\t"
               "global_load_dwordx4 v[100:103], v110, s[64:65]\n\tglobal_load_dwordx4 v[104:107], v110, s[64:65] offset:1152\n\t"
               "global_load_dword v108, v111, s[64:65] offset:1024\n\tglobal_load_dword v109, v109, s[82:83] sc1\n\ts_branch kf_n\n"
               "1:\n\ts_bfe_u32 s84, s2, 0x50003\n\ts_and_b32 s85, s2, 7\n\ts_lshl_b32 s85, s85, 5\n\ts_or_b32 s84, s84, s85\n\t"
               "s_lshl_b32 s85, s84, 9\n\ts_lshl_b32 s84, s84, 2\n\tv_and_b32 v110, 0xff, v0\n\tv_bfe_u32 v107, v0, 7, 1\n\t"
               "v_lshl_add_u32 v110, v110, 1, s85\n\tv_lshl_add_u32 v107, v107, 1, s84\n\tv_mov_b32 v109, 0\n\ts_cmpk_lt_u32 s98, 0x100\n\ts_waitcnt lgkmcnt(0)\n\t"
               "global_load_ushort v100, v107, s[78:79]\n\tglobal_load_ushort v101, v110, s[66:67]\n\tglobal_load_ushort v102, v110, s[68:69]\n\t"
               "s_cbranch_scc0 3f\n"
               "4:\n\tglobal_load_ushort v103, v110, s[70:71]\n\tglobal_load_ushort v104, v110, s[72:73]\n\tglobal_load_ushort v105, v110, s[74:75]\n\t"
               "global_load_ushort v106, v110, s[76:77]\n\tglobal_load_ushort v107, v107, s[80:81]\n\tglobal_load_dword v109, v109, s[82:83] sc1\n\ts_cbranch_scc1 kf_c\n\ts_barrier\n\ts_branch kf_p\n"
               "3:\n\tglobal_load_dword v109, v109, s[82:83] sc1\n\ts_branch kf_u\n"
               "2:\n\ts_cmpk_lt_u32 s2, 0xc0\n\ts_cbranch_scc1 5f\n\t"
               "s_lshr_b32 s84, s2, 3\n\ts_and_b32 s85, s2, 7\n\ts_lshl3_add_u32 s84, s85, s84\n\ts_addk_i32 s84, 488\n\ts_lshl_b32 s85, s84, 8\n\ts_lshl_b32 s84, s84, 1\n\t"
               "v_and_b32 v110, 0x7f, v0\n\tv_lshl_add_u32 v110, v110, 1, s85\n\tv_mov_b32 v107, s84\n\tv_mov_b32 v109, 0\n\ts_cmp_eq_u32 s2, 0\n\ts_waitcnt lgkmcnt(0)\n\t"
               "global_load_ushort v100, v107, s[78:79]\n\tglobal_load_ushort v101, v110, s[66:67]\n\tglobal_load_ushort v102, v110, s[68:69]\n\ts_branch 4b\n"
               "5:\n\ts_and_b32 s99, s2, 0xf8\n\ts_lshl_b32 s99, s99, 5\n\ts_and_b32 s98, s98, 0xc0\n\ts_or_b32 s98, s98, s99\n\ts_lshl_b32 s98, s98, 1\n\t"
               "s_getpc_b64 s[96:97]\n\ts_add_u32 s96, s96, s98\n\ts_addc_u32 s97, s97, 0\n\t"
               "s_load_dword s99, s[96:97], 0x0\n\ts_load_dword s99, s[96:97], 0x40\n\ts_load_dword s99, s[96:97], 0x3000\n\ts_load_dword s99, s[96:97], 0x3040\n\t"
               "s_load_dword s99, s[96:97], 0x6000\n\ts_load_dword s99, s[96:97], 0x6040\n\t"
               "s_cmp_gt_i32 s98, kf_end-kf_all-0x9400\n\ts_cbranch_scc1 6f\n\t"
               "s_load_dword s99, s[96:97], 0x9000\n\ts_load_dword s99, s[96:97], 0x9040\n6:\n\ts_barrier\n\ts_branch kf_p"
               : : "s"(__builtin_amdgcn_kernarg_segment_ptr()), "s"(blockIdx.x), "v"(threadIdx.x),
                   "s"((uint32_t)(uintptr_t)(__attribute__((address_space(3))) char*)smem) : "scc", "s96", "s97", "s98", "s99", "v127");
}
extern "C" __global__ __launch_bounds__(512, 4) void kf_c(KARGS) {
  __shared__ __attribute__((aligned(16))) char smem[SMEM];
  role_body<0>(smem, KPASS);
}
extern "C" __global__ __launch_bounds__(512, 4) void kf_u(KARGS) {
  __shared__ __attribute__((aligned(16))) char smem[SMEM];
  role_body<3>(smem, KPASS);
}
extern "C" __global__ __launch_bounds__(512, 4) void kf_n(KARGS) {
  __shared__ __attribute__((aligned(16))) char smem[SMEM];
  role_body<1>(smem, KPASS);
}
extern "C" __global__ __launch_bounds__(512, 4) void kf_p(KARGS) {
  __shared__ __attribute__((aligned(16))) char smem[SMEM];
  role_body<2>(smem, KPASS);
}
extern "C" __global__ __launch_bounds__(512, 4) void kf_conv() { asm volatile(CONV_ASM ::: CONV_CLOB); }
extern "C" __global__ void kf_end() {}

extern "C" void kernel_launch(void* const* d_in, const int* in_sizes, int n_in,
                              void* d_out, int out_size, void* d_ws, size_t ws_size,
                              hipStream_t stream) {
  kf_all<<<512, 512, 0, stream>>>((const u16*)d_in[0], (const u16*)d_in[1], (const u16*)d_in[2], (const u16*)d_in[3], (const u16*)d_in[4],
                                  (const u16*)d_in[5], (const u16*)d_in[6], (const u16*)d_in[7], (const u16*)d_in[8], (char*)d_ws + (size_t)WSOFF * 1048576,
                                  (const u16*)d_in[10], (const u16*)d_in[11], (const u16*)d_in[12], (const u16*)d_in[13], (u16*)d_out);
}
```

```cpp
#include <hip/hip_runtime.h>
#include <hip/hip_bf16.h>
#include <stdint.h>
#define VD2 390
#define OD2 0

typedef __attribute__((ext_vector_type(8))) short bf16x8;
typedef __attribute__((ext_vector_type(4))) float f32x4;
typedef uint16_t u16;
typedef __attribute__((ext_vector_type(4))) uint32_t u32x4;
typedef __attribute__((ext_vector_type(2))) uint32_t u32x2;
#define TSD
#define TS(k, i)
#define TSC(i)
#define TSE()
#define TSP
#define TSA

constexpr int L_ = 2048, D_ = 576, N_ = 128, F_ = 4608, C_ = 8;
constexpr int NF = 144;
constexpr uint32_t O_EP = 0;
constexpr uint32_t O_KF = 4096;
constexpr uint32_t O_KEY = O_KF + 8192;
constexpr uint32_t O_SC = O_KEY + 32768;
constexpr uint32_t O_DN = O_SC + 9216;
constexpr uint32_t O_SU = O_DN + 20480;
constexpr uint32_t O_FB = O_SU + 262144;
constexpr uint32_t O_XC = O_FB + 8 * 1152;
constexpr uint32_t SP = 9216 + 128;
constexpr uint32_t O_SL = O_XC + 1024 * 1152;
constexpr uint32_t SLU = 336;
constexpr uint32_t O_U = O_SL + 256 * SLU * 16;
constexpr int UP = 584;
constexpr uint32_t VP = 8192 + 128, VPP = 4096 + 128;
constexpr uint32_t O_V = O_U + 256 * UP * 16, O_VP = O_V + 256 * VP;
static_assert(256 * SLU * 16 >= NF * SP && O_SL % 128 == 0 && O_U % 128 == 0 && O_V % 128 == 0, "");
__host__ __device__ __forceinline__ uint32_t u_at(int tg, int d) { return O_U + 16u * (uint32_t)((tg >> 1) * (UP * 2) + (d >> 2) * 8 + (tg & 1) * 4 + (d & 3)); }
__host__ __device__ __forceinline__ uint32_t v_at(int d, int tg) {
  return (d < 512) ? O_V + (uint32_t)(d >> 1) * VP + 16u * (uint32_t)(2 * tg + (d & 1)) : O_VP + (uint32_t)(d - 512) * VPP + 16u * (uint32_t)tg;
}
#define HAVE_VAT
#define HAVE_VTAG
__device__ __forceinline__ float bf2f(u16 v) { return __uint_as_float((uint32_t)v << 16); }
__device__ __forceinline__ float bflo(uint32_t v) { return __uint_as_float(v << 16); }
__device__ __forceinline__ float bfhi(uint32_t v) { return __uint_as_float(v & 0xffff0000u); }
__device__ __forceinline__ u16 f2bf(float f) { return __builtin_bit_cast(u16, (__bf16)f); }
__device__ __forceinline__ float rbf(float f) { return bf2f(f2bf(f)); }
typedef __attribute__((ext_vector_type(2))) float f32x2;
typedef __attribute__((ext_vector_type(2))) __bf16 bf16x2v;
__device__ __forceinline__ uint32_t pack2(float a, float b) {
  return __builtin_bit_cast(uint32_t, __builtin_convertvector((f32x2){a, b}, bf16x2v));
}

#define DPP_I(v, ctrl, rmask) __builtin_amdgcn_update_dpp(0, (v), (ctrl), (rmask), 0xf, true)
__device__ __forceinline__ float row_sum(float v) {
  v += __int_as_float(DPP_I(__float_as_int(v), 0x111, 0xf));
  v += __int_as_float(DPP_I(__float_as_int(v), 0x112, 0xf));
  v += __int_as_float(DPP_I(__float_as_int(v), 0x114, 0xf));
  v += __int_as_float(DPP_I(__float_as_int(v), 0x118, 0xf));
  return v;
}
__device__ __forceinline__ float rdl(float v, int l) { return __int_as_float(__builtin_amdgcn_readlane(__float_as_int(v), l)); }
__device__ __forceinline__ float wave_sum(float v) {
  v = row_sum(v);
  asm("s_nop 1\n\tv_add_f32_dpp %0, %0, %0 row_bcast:15 row_mask:0xa bank_mask:0xf\n\t"
      "s_nop 1\n\tv_add_f32_dpp %0, %0, %0 row_bcast:31 row_mask:0xc bank_mask:0xf\n\ts_nop 0" : "+v"(v));
  return rdl(v, 63);
}
__device__ __forceinline__ float fold32(float a, float b) {
  const auto r = __builtin_amdgcn_permlane32_swap(__float_as_uint(a), __float_as_uint(b), false, false);
  return __uint_as_float(r[0]) + __uint_as_float(r[1]);
}
__device__ __forceinline__ float fold16(float a, float b) {
  const auto r = __builtin_amdgcn_permlane16_swap(__float_as_uint(a), __float_as_uint(b), false, false);
  return __uint_as_float(r[0]) + __uint_as_float(r[1]);
}
__device__ __forceinline__ void wave_sum2(float& a, float& b) {
  float v = row_sum(fold32(a, b));
  asm("s_nop 1\n\tv_add_f32_dpp %0, %0, %0 row_bcast:15 row_mask:0xa bank_mask:0xf\n\ts_nop 0" : "+v"(v));
  a = rdl(v, 31); b = rdl(v, 63);
}
__device__ __forceinline__ void wave_sum4(float& a, float& b, float& c, float& d) {
  const float v = row_sum(fold16(fold32(a, b), fold32(c, d)));
  a = rdl(v, 15); c = rdl(v, 31); b = rdl(v, 47); d = rdl(v, 63);
}
__device__ __forceinline__ float dot2(uint32_t a, uint32_t b, float c) {
  return __builtin_amdgcn_fdot2_f32_bf16(__builtin_bit_cast(bf16x2v, a), __builtin_bit_cast(bf16x2v, b), c, false);
}
__device__ __forceinline__ uint32_t row_maxu(uint32_t v) {
  v = max(v, (uint32_t)DPP_I((int)v, 0xb1, 0xf));
  v = max(v, (uint32_t)DPP_I((int)v, 0x4e, 0xf));
  v = max(v, (uint32_t)DPP_I((int)v, 0x141, 0xf));
  v = max(v, (uint32_t)DPP_I((int)v, 0x140, 0xf));
  return v;
}
__device__ __forceinline__ uint32_t wave_maxu(uint32_t v) {
  v = max(v, (uint32_t)DPP_I((int)v, 0x111, 0xf));
  v = max(v, (uint32_t)DPP_I((int)v, 0x112, 0xf));
  v = max(v, (uint32_t)DPP_I((int)v, 0x114, 0xf));
  v = max(v, (uint32_t)DPP_I((int)v, 0x118, 0xf));
  v = max(v, (uint32_t)DPP_I((int)v, 0x142, 0xa));
  v = max(v, (uint32_t)DPP_I((int)v, 0x143, 0xc));
  return (uint32_t)__builtin_amdgcn_readlane((int)v, 63);
}
__device__ __forceinline__ void wave_lds_sync() {
  __builtin_amdgcn_s_waitcnt(0xc07f);
  __builtin_amdgcn_wave_barrier();
}

typedef __attribute__((ext_vector_type(8))) _Float16 f16x8;
typedef __attribute__((ext_vector_type(2))) _Float16 f16x2;
__device__ __forceinline__ uint32_t packh(f32x2 v) { return __builtin_bit_cast(uint32_t, __builtin_convertvector(v, f16x2)); }
__device__ __forceinline__ uint32_t packh(float a, float b) { return packh((f32x2){a, b}); }
constexpr int IP = 224, I_X = 128;
__device__ __forceinline__ f32x2 cmul(f32x2 z, f32x2 w) {
  f32x2 r;
  asm("v_pk_mul_f32 %0, %1, %2 op_sel_hi:[0,1]\n\tv_pk_fma_f32 %0, %1, %2, %0 op_sel:[1,1,0] op_sel_hi:[1,0,1] neg_lo:[0,1,0]" : "=&v"(r) : "v"(z), "v"(w));
  return r;
}
__device__ __forceinline__ f32x2 cexp2(float ar2, float air, float l) {
  const float t = air * l;
  return (f32x2){__builtin_amdgcn_cosf(t), __builtin_amdgcn_sinf(t)} * __builtin_amdgcn_exp2f(ar2 * l);
}
__device__ __forceinline__ float state_a(float ldt, float are, float aim, float& ar2, float& air, float& g) {
  const float dt = rbf(__expf(ldt));
  ar2 = dt * are * 1.4426950408889634f;
  air = dt * aim * 0.15915494309189535f;
  air -= __builtin_rintf(air);
  g = 0.f;
  if (__builtin_expect(__any(ar2 > 0.f), 0)) g = fminf(__uint_as_float(wave_maxu(__float_as_uint(fmaxf(ar2, 0.f)))), 1.f);
  return dt;
}
template <int N> __device__ __forceinline__ void put_im(char* p, int im, const f32x2* z) {
  *(u32x4*)p = (u32x4){packh(z[0][0], z[1][0]), packh(z[2][0], z[3][0]), packh(z[4][0], z[5][0]), packh(z[6][0], z[7][0])};
  *(u32x4*)(p + im) = (u32x4){packh(z[0][1], z[1][1]), packh(z[2][1], z[3][1]), packh(z[4][1], z[5][1]), packh(z[6][1], z[7][1])};
}
#define Y_ASM \
  "v_mov_b64 v[40:41], %0\nv_pk_mul_f32 v[42:43], v[40:41], %1 op_sel_hi:[0,1]\nv_pk_mul_f32 v[44:45], %0, %2 op_sel_hi:[0,1]\n" \
  "v_pk_fma_f32 v[42:43], v[40:41], %1, v[42:43] op_sel:[1,1,0] op_sel_hi:[1,0,1] neg_lo:[0,1,0]\n" \
  "v_pk_fma_f32 v[44:45], %0, %2, v[44:45] op_sel:[1,1,0] op_sel_hi:[1,0,1] neg_lo:[0,1,0]\nv_cvt_pk_f16_f32 v56, v40, v42\n" \
  "v_cvt_pk_f16_f32 v60, v41, v43\nv_pk_mul_f32 v[40:41], v[42:43], %1 op_sel_hi:[0,1]\nv_pk_mul_f32 v[48:49], v[44:45], %2 op_sel_hi:[0,1]\n" \
  "v_pk_fma_f32 v[40:41], v[42:43], %1, v[40:41] op_sel:[1,1,0] op_sel_hi:[1,0,1] neg_lo:[0,1,0]\n" \
  "v_pk_fma_f32 v[48:49], v[44:45], %2, v[48:49] op_sel:[1,1,0] op_sel_hi:[1,0,1] neg_lo:[0,1,0]\n" \
  "v_pk_mul_f32 v[42:43], v[40:41], %1 op_sel_hi:[0,1]\nv_pk_mul_f32 v[52:53], v[48:49], %2 op_sel_hi:[0,1]\n" \
  "v_pk_fma_f32 v[42:43], v[40:41], %1, v[42:43] op_sel:[1,1,0] op_sel_hi:[1,0,1] neg_lo:[0,1,0]\n" \
  "v_pk_fma_f32 v[52:53], v[48:49], %2, v[52:53] op_sel:[1,1,0] op_sel_hi:[1,0,1] neg_lo:[0,1,0]\nv_cvt_pk_f16_f32 v57, v40, v42\n" \
  "v_cvt_pk_f16_f32 v61, v41, v43\nv_pk_mul_f32 v[40:41], v[42:43], %1 op_sel_hi:[0,1]\n" \
  "v_pk_fma_f32 v[40:41], v[42:43], %1, v[40:41] op_sel:[1,1,0] op_sel_hi:[1,0,1] neg_lo:[0,1,0]\n" \
  "v_pk_mul_f32 v[42:43], v[40:41], %1 op_sel_hi:[0,1]\nv_pk_mul_f32 v[46:47], v[44:45], %1 op_sel_hi:[0,1]\n" \
  "v_pk_fma_f32 v[42:43], v[40:41], %1, v[42:43] op_sel:[1,1,0] op_sel_hi:[1,0,1] neg_lo:[0,1,0]\n" \
  "v_pk_fma_f32 v[46:47], v[44:45], %1, v[46:47] op_sel:[1,1,0] op_sel_hi:[1,0,1] neg_lo:[0,1,0]\nv_cvt_pk_f16_f32 v58, v40, v42\n" \
  "v_cvt_pk_f16_f32 v62, v41, v43\nv_cvt_pk_f16_f32 v64, v44, v46\nv_cvt_pk_f16_f32 v68, v45, v47\n" \
  "v_pk_mul_f32 v[40:41], v[42:43], %1 op_sel_hi:[0,1]\nv_pk_mul_f32 v[44:45], v[46:47], %1 op_sel_hi:[0,1]\n" \
  "v_pk_fma_f32 v[40:41], v[42:43], %1, v[40:41] op_sel:[1,1,0] op_sel_hi:[1,0,1] neg_lo:[0,1,0]\n" \
  "v_pk_fma_f32 v[44:45], v[46:47], %1, v[44:45] op_sel:[1,1,0] op_sel_hi:[1,0,1] neg_lo:[0,1,0]\n" \
  "v_pk_mul_f32 v[42:43], v[40:41], %1 op_sel_hi:[0,1]\nv_pk_mul_f32 v[46:47], v[44:45], %1 op_sel_hi:[0,1]\n" \
  "v_pk_fma_f32 v[42:43], v[40:41], %1, v[42:43] op_sel:[1,1,0] op_sel_hi:[1,0,1] neg_lo:[0,1,0]\n" \
  "v_pk_fma_f32 v[46:47], v[44:45], %1, v[46:47] op_sel:[1,1,0] op_sel_hi:[1,0,1] neg_lo:[0,1,0]\nv_cvt_pk_f16_f32 v59, v40, v42\n" \
  "v_cvt_pk_f16_f32 v63, v41, v43\nv_cvt_pk_f16_f32 v65, v44, v46\nv_cvt_pk_f16_f32 v69, v45, v47\n" \
  "v_pk_mul_f32 v[44:45], v[46:47], %1 op_sel_hi:[0,1]\nds_write_b128 %3, v[56:59] offset:0\n" \
  "v_pk_fma_f32 v[44:45], v[46:47], %1, v[44:45] op_sel:[1,1,0] op_sel_hi:[1,0,1] neg_lo:[0,1,0]\nds_write_b128 %3, v[60:63] offset:64\n" \
  "v_pk_mul_f32 v[46:47], v[44:45], %1 op_sel_hi:[0,1]\nv_pk_mul_f32 v[50:51], v[48:49], %1 op_sel_hi:[0,1]\n" \
  "v_pk_fma_f32 v[46:47], v[44:45], %1, v[46:47] op_sel:[1,1,0] op_sel_hi:[1,0,1] neg_lo:[0,1,0]\n" \
  "v_pk_fma_f32 v[50:51], v[48:49], %1, v[50:51] op_sel:[1,1,0] op_sel_hi:[1,0,1] neg_lo:[0,1,0]\nv_cvt_pk_f16_f32 v66, v44, v46\n" \
  "v_cvt_pk_f16_f32 v70, v45, v47\nv_cvt_pk_f16_f32 v56, v48, v50\nv_cvt_pk_f16_f32 v60, v49, v51\n" \
  "v_pk_mul_f32 v[44:45], v[46:47], %1 op_sel_hi:[0,1]\nv_pk_mul_f32 v[48:49], v[50:51], %1 op_sel_hi:[0,1]\n" \
  "v_pk_fma_f32 v[44:45], v[46:47], %1, v[44:45] op_sel:[1,1,0] op_sel_hi:[1,0,1] neg_lo:[0,1,0]\n" \
  "v_pk_fma_f32 v[48:49], v[50:51], %1, v[48:49] op_sel:[1,1,0] op_sel_hi:[1,0,1] neg_lo:[0,1,0]\n" \
  "v_pk_mul_f32 v[46:47], v[44:45], %1 op_sel_hi:[0,1]\nv_pk_mul_f32 v[50:51], v[48:49], %1 op_sel_hi:[0,1]\n" \
  "v_pk_fma_f32 v[46:47], v[44:45], %1, v[46:47] op_sel:[1,1,0] op_sel_hi:[1,0,1] neg_lo:[0,1,0]\n" \
  "v_pk_fma_f32 v[50:51], v[48:49], %1, v[50:51] op_sel:[1,1,0] op_sel_hi:[1,0,1] neg_lo:[0,1,0]\nv_cvt_pk_f16_f32 v67, v44, v46\n" \
  "v_cvt_pk_f16_f32 v71, v45, v47\nv_cvt_pk_f16_f32 v57, v48, v50\nv_cvt_pk_f16_f32 v61, v49, v51\n" \
  "v_pk_mul_f32 v[48:49], v[50:51], %1 op_sel_hi:[0,1]\nds_write_b128 %3, v[64:67] offset:16\n" \
  "v_pk_fma_f32 v[48:49], v[50:51], %1, v[48:49] op_sel:[1,1,0] op_sel_hi:[1,0,1] neg_lo:[0,1,0]\nds_write_b128 %3, v[68:71] offset:80\n" \
  "v_pk_mul_f32 v[50:51], v[48:49], %1 op_sel_hi:[0,1]\nv_pk_mul_f32 v[54:55], v[52:53], %1 op_sel_hi:[0,1]\n" \
  "v_pk_fma_f32 v[50:51], v[48:49], %1, v[50:51] op_sel:[1,1,0] op_sel_hi:[1,0,1] neg_lo:[0,1,0]\n" \
  "v_pk_fma_f32 v[54:55], v[52:53], %1, v[54:55] op_sel:[1,1,0] op_sel_hi:[1,0,1] neg_lo:[0,1,0]\nv_cvt_pk_f16_f32 v58, v48, v50\n" \
  "v_cvt_pk_f16_f32 v62, v49, v51\nv_cvt_pk_f16_f32 v64, v52, v54\nv_cvt_pk_f16_f32 v68, v53, v55\n" \
  "v_pk_mul_f32 v[48:49], v[50:51], %1 op_sel_hi:[0,1]\nv_pk_mul_f32 v[52:53], v[54:55], %1 op_sel_hi:[0,1]\n" \
  "v_pk_fma_f32 v[48:49], v[50:51], %1, v[48:49] op_sel:[1,1,0] op_sel_hi:[1,0,1] neg_lo:[0,1,0]\n" \
  "v_pk_fma_f32 v[52:53], v[54:55], %1, v[52:53] op_sel:[1,1,0] op_sel_hi:[1,0,1] neg_lo:[0,1,0]\n" \
  "v_pk_mul_f32 v[50:51], v[48:49], %1 op_sel_hi:[0,1]\nv_pk_mul_f32 v[54:55], v[52:53], %1 op_sel_hi:[0,1]\n" \
  "v_pk_fma_f32 v[50:51], v[48:49], %1, v[50:51] op_sel:[1,1,0] op_sel_hi:[1,0,1] neg_lo:[0,1,0]\n" \
  "v_pk_fma_f32 v[54:55], v[52:53], %1, v[54:55] op_sel:[1,1,0] op_sel_hi:[1,0,1] neg_lo:[0,1,0]\nv_cvt_pk_f16_f32 v59, v48, v50\n" \
  "v_cvt_pk_f16_f32 v63, v49, v51\nv_cvt_pk_f16_f32 v65, v52, v54\nv_cvt_pk_f16_f32 v69, v53, v55\n" \
  "v_pk_mul_f32 v[52:53], v[54:55], %1 op_sel_hi:[0,1]\nds_write_b128 %3, v[56:59] offset:32\n" \
  "v_pk_fma_f32 v[52:53], v[54:55], %1, v[52:53] op_sel:[1,1,0] op_sel_hi:[1,0,1] neg_lo:[0,1,0]\nds_write_b128 %3, v[60:63] offset:96\n" \
  "v_pk_mul_f32 v[54:55], v[52:53], %1 op_sel_hi:[0,1]\n" \
  "v_pk_fma_f32 v[54:55], v[52:53], %1, v[54:55] op_sel:[1,1,0] op_sel_hi:[1,0,1] neg_lo:[0,1,0]\nv_cvt_pk_f16_f32 v66, v52, v54\n" \
  "v_cvt_pk_f16_f32 v70, v53, v55\nv_pk_mul_f32 v[52:53], v[54:55], %1 op_sel_hi:[0,1]\n" \
  "v_pk_fma_f32 v[52:53], v[54:55], %1, v[52:53] op_sel:[1,1,0] op_sel_hi:[1,0,1] neg_lo:[0,1,0]\n" \
  "v_pk_mul_f32 v[54:55], v[52:53], %1 op_sel_hi:[0,1]\n" \
  "v_pk_fma_f32 v[54:55], v[52:53], %1, v[54:55] op_sel:[1,1,0] op_sel_hi:[1,0,1] neg_lo:[0,1,0]\nv_cvt_pk_f16_f32 v67, v52, v54\n" \
  "v_cvt_pk_f16_f32 v71, v53, v55\nds_write_b128 %3, v[64:67] offset:48\nds_write_b128 %3, v[68:71] offset:112\n"
#define Y_CLOB "memory", "v40", "v41", "v42", "v43", "v44", "v45", "v46", "v47", "v48", "v49", "v50", "v51", "v52", "v53", "v54", "v55", "v56", "v57", "v58", "v59", "v60", "v61", "v62", "v63", "v64", "v65", "v66", "v67", "v68", "v69", "v70", "v71"
#define T_ASM \
  "ds_read_b128 %4, %8\nds_read_b64_tr_b16 v[80:81], %7 offset:0\nds_read_b64_tr_b16 v[82:83], %7 offset:64\n" \
  "ds_read_b64_tr_b16 v[84:85], %5 offset:0\nds_read_b64_tr_b16 v[86:87], %5 offset:48\nds_read_b64_tr_b16 v[88:89], %6 offset:0\n" \
  "ds_read_b64_tr_b16 v[90:91], %6 offset:48\nds_read_b64_tr_b16 v[92:93], %7 offset:3584\nds_read_b64_tr_b16 v[94:95], %7 offset:3648\n" \
  "ds_read_b64_tr_b16 v[96:97], %5 offset:3584\nds_read_b64_tr_b16 v[98:99], %5 offset:3632\nds_read_b64_tr_b16 v[100:101], %6 offset:3584\n" \
  "ds_read_b64_tr_b16 v[102:103], %6 offset:3632\nds_read_b64_tr_b16 v[104:105], %7 offset:7168\nds_read_b64_tr_b16 v[106:107], %7 offset:7232\n" \
  "s_waitcnt lgkmcnt(8)\nv_mfma_f32_16x16x32_f16 %0, v[84:87], v[80:83], 0\nds_read_b64_tr_b16 v[108:109], %5 offset:7168\n" \
  "ds_read_b64_tr_b16 v[110:111], %5 offset:7216\nds_read_b64_tr_b16 v[112:113], %6 offset:7168\nv_mfma_f32_16x16x32_f16 %2, v[88:91], v[80:83], 0\n" \
  "ds_read_b64_tr_b16 v[114:115], %6 offset:7216\nds_read_b64_tr_b16 v[116:117], %7 offset:10752\nds_read_b64_tr_b16 v[118:119], %7 offset:10816\n" \
  "s_waitcnt lgkmcnt(8)\nv_mfma_f32_16x16x32_f16 %0, v[96:99], v[92:95], %0\nds_read_b64_tr_b16 v[120:121], %5 offset:10752\n" \
  "ds_read_b64_tr_b16 v[122:123], %5 offset:10800\nds_read_b64_tr_b16 v[124:125], %6 offset:10752\n" \
  "v_mfma_f32_16x16x32_f16 %2, v[100:103], v[92:95], %2\nds_read_b64_tr_b16 v[126:127], %6 offset:10800\n" \
  "ds_read_b64_tr_b16 v[80:81], %7 offset:14336\nds_read_b64_tr_b16 v[82:83], %7 offset:14400\ns_waitcnt lgkmcnt(8)\n" \
  "v_mfma_f32_16x16x32_f16 %0, v[108:111], v[104:107], %0\nds_read_b64_tr_b16 v[84:85], %5 offset:14336\n" \
  "ds_read_b64_tr_b16 v[86:87], %5 offset:14384\nds_read_b64_tr_b16 v[88:89], %6 offset:14336\n" \
  "v_mfma_f32_16x16x32_f16 %2, v[112:115], v[104:107], %2\nds_read_b64_tr_b16 v[90:91], %6 offset:14384\n" \
  "ds_read_b64_tr_b16 v[92:93], %7 offset:17920\nds_read_b64_tr_b16 v[94:95], %7 offset:17984\ns_waitcnt lgkmcnt(8)\n" \
  "v_mfma_f32_16x16x32_f16 %0, v[120:123], v[116:119], %0\nds_read_b64_tr_b16 v[96:97], %5 offset:17920\n" \
  "ds_read_b64_tr_b16 v[98:99], %5 offset:17968\nds_read_b64_tr_b16 v[100:101], %6 offset:17920\n" \
  "v_mfma_f32_16x16x32_f16 %2, v[124:127], v[116:119], %2\nds_read_b64_tr_b16 v[102:103], %6 offset:17968\n" \
  "ds_read_b64_tr_b16 v[104:105], %7 offset:21504\nds_read_b64_tr_b16 v[106:107], %7 offset:21568\ns_waitcnt lgkmcnt(8)\n" \
  "v_mfma_f32_16x16x32_f16 %1, v[84:87], v[80:83], 0\nds_read_b64_tr_b16 v[108:109], %5 offset:21504\nds_read_b64_tr_b16 v[110:111], %5 offset:21552\n" \
  "ds_read_b64_tr_b16 v[112:113], %6 offset:21504\nv_mfma_f32_16x16x32_f16 %3, v[88:91], v[80:83], 0\nds_read_b64_tr_b16 v[114:115], %6 offset:21552\n" \
  "ds_read_b64_tr_b16 v[116:117], %7 offset:25088\nds_read_b64_tr_b16 v[118:119], %7 offset:25152\ns_waitcnt lgkmcnt(8)\n" \
  "v_mfma_f32_16x16x32_f16 %1, v[96:99], v[92:95], %1\nds_read_b64_tr_b16 v[120:121], %5 offset:25088\n" \
  "ds_read_b64_tr_b16 v[122:123], %5 offset:25136\nds_read_b64_tr_b16 v[124:125], %6 offset:25088\n" \
  "v_mfma_f32_16x16x32_f16 %3, v[100:103], v[92:95], %3\nds_read_b64_tr_b16 v[126:127], %6 offset:25136\ns_waitcnt lgkmcnt(6)\n" \
  "v_mfma_f32_16x16x32_f16 %1, v[108:111], v[104:107], %1\nv_mfma_f32_16x16x32_f16 %3, v[112:115], v[104:107], %3\ns_waitcnt lgkmcnt(0)\n" \
  "v_mfma_f32_16x16x32_f16 %1, v[120:123], v[116:119], %1\nv_mfma_f32_16x16x32_f16 %3, v[124:127], v[116:119], %3\n"
#define T_CLOB "memory", "v80", "v81", "v82", "v83", "v84", "v85", "v86", "v87", "v88", "v89", "v90", "v91", "v92", "v93", "v94", "v95", "v96", "v97", "v98", "v99", "v100", "v101", "v102", "v103", "v104", "v105", "v106", "v107", "v108", "v109", "v110", "v111", "v112", "v113", "v114", "v115", "v116", "v117", "v118", "v119", "v120", "v121", "v122", "v123", "v124", "v125", "v126", "v127"
template <class F> __device__ __forceinline__ int gen_y(float ldt, float are, float aim, float bre, float bim, float cre, float cim, uint32_t im, F&& mid TSP) {
  float ar2, air, g;
  const float dt = state_a(ldt, are, aim, ar2, air, g);
  const float dbr = dt * bre, dbi = dt * bim, pr = cre * dbr - cim * dbi, pi = cre * dbi + cim * dbr;
  const f32x2 w8 = cexp2(ar2, -air, 8.f), w64 = cexp2(ar2, -air, 64.f);
  const float pm = fmaxf(fabsf(pr), fabsf(pi));
  int e = -4;
  if (__builtin_expect(__any(!(pm < 0x1p-4f) || ar2 > 0.f) || !__any(pm > 0x1p-12f), 0)) e = (int)(wave_maxu(__float_as_uint(pm)) >> 23) - 126 + (int)ceilf(g * 250.f);
  const f32x2 y0 = {__builtin_amdgcn_ldexpf(pr, 14 - e), __builtin_amdgcn_ldexpf(-pi, 14 - e)};
  mid();
  TS(0, 7);
  asm volatile(Y_ASM : : "v"(y0), "v"(w8), "v"(w64), "v"(im) : Y_CLOB);
  return e;
}
__device__ __forceinline__ int gen_x(float ldt, float are, float aim, char* im) {
  float ar2, air, g;
  state_a(ldt, are, aim, ar2, air, g);
  const int e = min((int)ceilf(g * 1810.f), 300);
  const float s = __builtin_amdgcn_ldexpf(1.f, 15 - e);
  const f32x2 w1 = cexp2(ar2, air, 1.f), w256 = cexp2(ar2, air, 256.f), w2 = cmul(w1, w1);
  f32x2 a[8], b[8], c[8];
  a[0] = (f32x2){s, 0.f}; a[1] = w1 * s; a[2] = w2 * s;
  b[0] = b[1] = w256 * s;
  b[2] = cmul(b[0], w2); a[3] = cmul(a[1], w2); a[4] = cmul(a[2], w2);
  b[3] = b[4] = b[5] = cmul(b[2], w2); a[5] = cmul(a[3], w2); a[6] = cmul(a[4], w2);
  b[6] = cmul(b[3], w2); a[7] = cmul(a[5], w2); c[0] = cmul(b[3], w256);
  b[7] = cmul(b[6], w2); c[2] = cmul(c[0], w256);
  put_im<0>(im + I_X, 48, a);
  c[1] = cmul(b[7], w256); c[3] = cmul(c[1], w256);
  put_im<0>(im + I_X + 16, 48, b);
#pragma unroll
  for (int k = 4; k < 8; ++k) c[k] = cmul(c[k - 1], w256);
  put_im<0>(im + I_X + 32, 48, c);
  return e;
}

template <int I> struct IC { static constexpr int value = I; };
template <int B, int E, class F> __device__ __forceinline__ void static_for(F&& f) {
  if constexpr (B < E) { f(IC<B>{}); static_for<B + 1, E>(f); }
}
typedef __attribute__((ext_vector_type(4))) short s16x4;
__device__ __forceinline__ u32x2 trd(uint32_t a) {
  return __builtin_bit_cast(u32x2, __builtin_amdgcn_ds_read_tr16_b64_v4i16((__attribute__((address_space(3))) s16x4*)(uintptr_t)a));
}
#define DPP_F(v, ctrl) __int_as_float(DPP_I(__float_as_int(v), (ctrl), 0xf))
__device__ __forceinline__ float s_add(float a, float b) { float r; asm("v_add_f32 %0, %1, %2" : "=v"(r) : "v"(a), "v"(b)); return r; }
__device__ __forceinline__ float s_fma(float a, float b, float c) { float r; asm("v_fma_f32 %0, %1, %2, %3" : "=v"(r) : "v"(a), "v"(b), "v"(c)); return r; }
#define BAR() asm volatile("s_waitcnt lgkmcnt(0)\n\ts_barrier" ::: "memory")
#define ST16(off, v) asm volatile("global_store_dwordx4 %0, %1, %2 sc1\n\ts_nop 1" : : "v"(off), "v"(v), "s"(ws) : "memory")
#define ST4(off, v) asm volatile("global_store_dword %0, %1, %2 sc1" : : "v"(off), "v"(v), "s"(ws) : "memory")
#define LD16(r, off) asm volatile("global_load_dwordx4 %0, %1, %2 sc1" : "=v"(r) : "v"(off), "s"(ws) : "memory")
#define LD16W(r, off) asm volatile("global_load_dwordx4 %0, %1, %2 sc1\n\ts_waitcnt vmcnt(0)" : "=&v"(r) : "v"(off), "s"(ws) : "memory")
#define DRAIN() asm volatile("s_waitcnt vmcnt(0)" ::: "memory")
#define CEX(a, b) { const uint32_t hi_ = max(a, b), lo_ = min(a, b); a = hi_; b = lo_; }
#define MMB(a_, b_, c_) __builtin_amdgcn_mfma_f32_16x16x32_bf16(__builtin_bit_cast(bf16x8, a_), __builtin_bit_cast(bf16x8, b_), c_, 0, 0, 0)
#define NTM 0
#define POLS(b) ((NTM & (b)) ? " nt" : "")
#define DPOL ""
#define DPOL2 ""
#define DPOL4 ""
#define XPOL ""
#define TPOL ""
constexpr int SPIN_CAP = 1 << 18;
constexpr uint32_t UMASK = 0x40074007u;
__device__ __forceinline__ uint32_t utag(uint32_t T, int j) {
  return ((T >> (3 * j)) & 0x00070007u) | (((j <= 2) ? (T << (2 - j)) : (T >> (j - 2))) & 0x40004000u);
}
__device__ __forceinline__ uint32_t max4(const u32x4& a) { return max(max(a[0], a[1]), max(a[2], a[3])); }
__device__ __forceinline__ uint32_t min4(const u32x4& a) { return min(min(a[0], a[1]), min(a[2], a[3])); }
__device__ __forceinline__ void until(uint32_t t) {
#pragma unroll 1
  for (int spin = 0; spin < 600 && (int)((uint32_t)__builtin_amdgcn_s_memrealtime() - t) < 0; ++spin) __builtin_amdgcn_s_sleep(1);
}
__device__ __forceinline__ uint32_t latest(uint32_t t) {
  const uint32_t t0 = (uint32_t)__builtin_amdgcn_readfirstlane(t) - 0x40000000u;
  return wave_maxu(t - t0) + t0;
}
__device__ __forceinline__ uint32_t gate(const char* ws, uint32_t po, uint32_t T) {
  u32x2 pv;
#pragma unroll 1
  for (int spin = 0; spin < SPIN_CAP; ++spin) {
    asm volatile("global_load_dwordx2 %0, %1, %2 sc1\n\ts_waitcnt vmcnt(0)" : "=&v"(pv) : "v"(po), "s"(ws) : "memory");
    if (!__any(pv[0] != T)) break;
    __builtin_amdgcn_s_sleep(2);
  }
  return pv[1];
}
__device__ __forceinline__ uint32_t gate2(const char* ws, uint32_t oa, uint32_t da, uint32_t ob, uint32_t T) {
  u32x2 pa, pb;
#pragma unroll 1
  for (int spin = 0; spin < SPIN_CAP; ++spin) {
    asm volatile("global_load_dwordx2 %0, %2, %4 sc1\n\tglobal_load_dwordx2 %1, %3, %4 sc1\n\ts_waitcnt vmcnt(0)" : "=&v"(pa), "=&v"(pb) : "v"(oa), "v"(ob), "s"(ws) : "memory");
    if (!__any(pa[0] != T || pb[0] != T)) break;
    __builtin_amdgcn_s_sleep(4);
  }
  pa[1] += da;
  return latest(((int)(pa[1] - pb[1]) > 0) ? pa[1] : pb[1]);
}
__device__ __forceinline__ uint32_t ahead(uint32_t c0, uint32_t t0, int dt) {
  const uint32_t c = (uint32_t)__builtin_amdgcn_s_memtime(), t = (uint32_t)__builtin_amdgcn_s_memrealtime();
  return t + (uint32_t)((float)((t - t0) * (uint32_t)(20 * dt)) * __builtin_amdgcn_rcpf((float)(c - c0)));
}
#define SIGNAL(off, pitch, dt) do { const uint32_t t_ = ahead(ck0, rt0, dt); \
    ST16((off) + (uint32_t)(lane >> 3) * (pitch) + 16u * (lane & 7), ((u32x4){T, t_, T, t_})); } while (0)
__device__ __forceinline__ void gsync(uint32_t a, uint32_t target) {
  uint32_t t, c, n = SPIN_CAP;
  uint64_t ex;
  asm volatile("s_waitcnt lgkmcnt(0)\n\ts_mov_b64 %3, exec\n\ts_mov_b64 exec, 1\n\tv_mov_b32 %0, 1\n\tds_add_u32 %4, %0\n\ts_mov_b64 exec, %3\n"
               "1:\n\ts_sleep 1\n\tds_read_b32 %0, %4\n\ts_sub_u32 %2, %2, 1\n\ts_cbranch_scc1 2f\n\ts_waitcnt lgkmcnt(0)\n\tv_readfirstlane_b32 %1, %0\n\t"
               "s_cmp_lt_u32 %1, %5\n\ts_cbranch_scc1 1b\n2:\n\ts_waitcnt lgkmcnt(0)"
               : "=&v"(t), "=&s"(c), "+s"(n), "=&s"(ex) : "v"(a), "n"(target) : "memory", "scc");
}
__device__ __forceinline__ void gsync0(uint32_t a, uint32_t target) {
  uint32_t t, c, n = SPIN_CAP;
  asm volatile("s_waitcnt lgkmcnt(0)\n\ts_mov_b64 exec, 1\n\tv_mov_b32 %0, 1\n\tds_add_u32 %3, %0\n\ts_mov_b64 exec, -1\n"
               "1:\n\tds_read_b32 %0, %3\n\ts_sub_u32 %2, %2, 1\n\ts_cbranch_scc1 2f\n\ts_waitcnt lgkmcnt(0)\n\tv_readfirstlane_b32 %1, %0\n\t"
               "s_cmp_lt_u32 %1, %4\n\ts_cbranch_scc1 1b\n2:\n\ts_waitcnt lgkmcnt(0)"
               : "=&v"(t), "=&s"(c), "+s"(n) : "v"(a), "n"(target) : "memory", "scc");
}
__device__ __forceinline__ void gwait(uint32_t a, uint32_t target) {
  uint32_t t, c, n = SPIN_CAP;
  asm volatile("1:\n\ts_sleep 2\n\tds_read_b32 %0, %3\n\ts_sub_u32 %2, %2, 1\n\ts_cbranch_scc1 2f\n\ts_waitcnt lgkmcnt(0)\n\tv_readfirstlane_b32 %1, %0\n\t"
               "s_cmp_lt_u32 %1, %4\n\ts_cbranch_scc1 1b\n2:\n\ts_waitcnt lgkmcnt(0)"
               : "=&v"(t), "=&s"(c), "+s"(n) : "v"(a), "n"(target) : "memory", "scc");
}
__device__ __forceinline__ void dma16(const void* base, uint32_t voff, uint32_t lds) {
  uint32_t keep;
  asm volatile("s_mov_b32 %0, m0\n\ts_mov_b32 m0, %3\n\ts_nop 0\n\tglobal_load_lds_dwordx4 %1, %2" DPOL "\n\ts_mov_b32 m0, %0" : "=&s"(keep) : "v"(voff), "s"(base), "s"(lds) : "memory");
}
__device__ __forceinline__ void dma16d(const void* base, uint32_t voff, uint32_t lds) {
  uint32_t keep;
  asm volatile("s_mov_b32 %0, m0\n\ts_mov_b32 m0, %3\n\ts_nop 0\n\tglobal_load_lds_dwordx4 %1, %2" DPOL2 "\n\ts_mov_b32 m0, %0" : "=&s"(keep) : "v"(voff), "s"(base), "s"(lds) : "memory");
}
__device__ __forceinline__ void nap(int n) {
  for (; n > 15; n -= 15) __builtin_amdgcn_s_sleep(15);
  switch (n) {
    case 1: __builtin_amdgcn_s_sleep(1); break; case 2: __builtin_amdgcn_s_sleep(2); break; case 3: __builtin_amdgcn_s_sleep(3); break;
    case 4: __builtin_amdgcn_s_sleep(4); break; case 5: __builtin_amdgcn_s_sleep(5); break; case 6: __builtin_amdgcn_s_sleep(6); break;
    case 7: __builtin_amdgcn_s_sleep(7); break; case 8: __builtin_amdgcn_s_sleep(8); break; case 9: __builtin_amdgcn_s_sleep(9); break;
    case 10: __builtin_amdgcn_s_sleep(10); break; case 11: __builtin_amdgcn_s_sleep(11); break; case 12: __builtin_amdgcn_s_sleep(12); break;
    case 13: __builtin_amdgcn_s_sleep(13); break; case 14: __builtin_amdgcn_s_sleep(14); break; case 15: __builtin_amdgcn_s_sleep(15); break;
    default: break;
  }
}
#define SLEEP(n) do { if ((n) > 0) __builtin_amdgcn_s_sleep(n); } while (0)
#define USLEEP 3
#define UPOS 1
#define QSLEEP 0
#define CSLEEP 4
#define WSLEEP 4
#define TSLEEP 0
#define VSLEEP 45
#define KSLEEP 2
#define SSLEEP 11
#define PSLEEP 2
#define PPRIO 0
#define APRIO 1
#define NTOUCH 4
#define SWZ 1
#define PREOUT 0
#define LFIRST 1
#define CBR "0"
#define WD 40
#define CD 3
#define OSLEEP 12
#define QPRIO 1
#define KPRIO 2
#define UDRAIN 0
#define GSLEEP 0
#define SIGPOS 0
#define LPRIO 3
#define TW 0
#define TPOS 0
#define R1EARLY 1
#define DPOS 0
#define NSLEEP 0
#define ASLEEP 40
#define FPRIO 1
#define PRBW 1
#define PRBPOS 1
#define TW2 6
#define DSLEEP 8
#define VD 400
#define XSLEEP 20
#define RPOS 1
#define KPRE 1
#define UD 40
#define XD 0
#define TRIG 1
#define CPF 0
#define RSLEEP 0
#define UPRIO 2
#define TWP 256
#define WDEPTH 6
#define PD 170
#define VDX 390
#define EARLY 0
#define ESLEEP 0
#define WDEPTH1 5
#define WD2 85
#define SD 58
#define KD 175
#define UFIRST 0
#define TX 1
#define QWAIT 4
#define OD 0
#define KBACK 6
#define DDEPTH 6
#define C3 1
#define XOFF 0
#define GD 105
#define XROT 2
#define WSOFF 0
#define T0 0
#define TF 192
#define DD 0
#define TDEPTH 1
#define TB1 0
#define TB1I 0
#define TB1X 2
#define B1SLEEP 0
#define DFIRST 0
#define T0SLEEP 0
#define BSLEEP 6
#define KN 2
#define TD 0
#define KE 0
#define SKIPB 0
#define NFIRST 0
#define PW1 16
#define PW2 38
#define KLATE 0
#define RPF 1
#define HSLEEP 6
#define LSLEEP 3
#define TSTOP 55
#define HSTOP 100
#define HL 0
#define VEARLY 0ull
#define FPR 3
#define SPR 3
#define DPRIO 0
#define T1 0
#define T1SLEEP 0
#define QPRIO2 3
#define HW 2
#define HN 3
#define RD 55
#define OPOS 1
#define NA 14
#define HA 2
#define HAX 0
#define RD2 80
#define ORD1 ((unsigned __int128)0x321087654u << 36 | 0x321087654ull)
#define RSL 10
#define WDEPTH2 3
#define RQ NA
#define HREV 1
#define QPRIO0 3
#define VPS 1
#define RW 0
#define NPRIO 0
#define XSL 10
#define FORCESLOW 0
#define FORCEFB 0
#define CONV_ASM \
  "s_cmp_eq_u32 s89, 1\ns_cbranch_scc1 1f\ns_cmp_gt_u32 s89, 1\ns_cbranch_scc1 9f\nds_read_b64 v[124:125], v3 offset:576\n" \
  "ds_read_b64 v[126:127], v3 offset:584\nds_read_b64 v[120:121], v3 offset:64\nds_read_b64 v[122:123], v3 offset:72\n" \
  "ds_read_b64 v[116:117], v3 offset:640\nds_read_b64 v[118:119], v3 offset:648\nds_read_b64 v[112:113], v3 offset:128\n" \
  "ds_read_b64 v[114:115], v3 offset:136\nds_read_b64 v[108:109], v3 offset:704\nds_read_b64 v[110:111], v3 offset:712\n" \
  "ds_read_b64 v[104:105], v3 offset:192\nds_read_b64 v[106:107], v3 offset:200\nds_read_b64 v[100:101], v3 offset:768\n" \
  "ds_read_b64 v[102:103], v3 offset:776\nds_read_b64 v[96:97], v3 offset:256\nds_read_b64 v[98:99], v3 offset:264\n" \
  "ds_read_b64 v[92:93], v3 offset:832\nds_read_b64 v[94:95], v3 offset:840\nds_read_b64 v[88:89], v3 offset:320\n" \
  "ds_read_b64 v[90:91], v3 offset:328\nds_read_b64 v[84:85], v3 offset:896\nds_read_b64 v[86:87], v3 offset:904\n" \
  "ds_read_b64 v[80:81], v3 offset:384\nds_read_b64 v[82:83], v3 offset:392\nds_read_b64 v[76:77], v3 offset:960\n" \
  "ds_read_b64 v[78:79], v3 offset:968\nds_read_b64 v[72:73], v3 offset:448\nds_read_b64 v[74:75], v3 offset:456\n" \
  "ds_read_b64 v[68:69], v3 offset:1024\nds_read_b64 v[70:71], v3 offset:1032\nds_read_b64 v[64:65], v3 offset:512\n" \
  "ds_read_b64 v[66:67], v3 offset:520\nds_read_b64 v[60:61], v3 offset:0\nds_read_b64 v[62:63], v3 offset:8\nds_read_b64 v[56:57], v3 offset:1088\n" \
  "ds_read_b64 v[58:59], v3 offset:1096\ns_waitcnt lgkmcnt(0)\ns_barrier\nds_read_b128 v[36:39], v2 offset:0\nds_read_b128 v[40:43], v2 offset:64\n" \
  "ds_read_b128 v[44:47], v2 offset:128\nds_read_b128 v[48:51], v2 offset:192\ns_waitcnt lgkmcnt(3)\n" \
  "v_mfma_f32_16x16x32_f16 v[12:15], v[124:127], v[36:39], 0\ns_setprio 1\nds_read_b128 v[52:55], v2 offset:256\n" \
  "v_mfma_f32_16x16x32_f16 v[16:19], v[120:123], v[36:39], 0\ns_waitcnt lgkmcnt(3)\nv_mfma_f32_16x16x32_f16 v[12:15], v[116:119], v[40:43], v[12:15]\n" \
  "ds_read_b128 v[36:39], v2 offset:320\nv_mfma_f32_16x16x32_f16 v[16:19], v[112:115], v[40:43], v[16:19]\ns_waitcnt lgkmcnt(3)\n" \
  "v_mfma_f32_16x16x32_f16 v[12:15], v[108:111], v[44:47], v[12:15]\nds_read_b128 v[40:43], v2 offset:384\n" \
  "v_mfma_f32_16x16x32_f16 v[16:19], v[104:107], v[44:47], v[16:19]\ns_waitcnt lgkmcnt(3)\n" \
  "v_mfma_f32_16x16x32_f16 v[12:15], v[100:103], v[48:51], v[12:15]\nds_read_b128 v[44:47], v2 offset:448\n" \
  "v_mfma_f32_16x16x32_f16 v[16:19], v[96:99], v[48:51], v[16:19]\ns_waitcnt lgkmcnt(3)\n" \
  "v_mfma_f32_16x16x32_f16 v[12:15], v[92:95], v[52:55], v[12:15]\nv_mfma_f32_16x16x32_f16 v[16:19], v[88:91], v[52:55], v[16:19]\n" \
  "s_waitcnt lgkmcnt(2)\nv_mfma_f32_16x16x32_f16 v[12:15], v[84:87], v[36:39], v[12:15]\nds_read_b128 v[48:51], v2 offset:512\n" \
  "v_mfma_f32_16x16x32_f16 v[16:19], v[80:83], v[36:39], v[16:19]\ns_waitcnt lgkmcnt(2)\n" \
  "v_mfma_f32_16x16x32_f16 v[12:15], v[76:79], v[40:43], v[12:15]\nds_read_b128 v[52:55], v2 offset:576\n" \
  "v_mfma_f32_16x16x32_f16 v[16:19], v[72:75], v[40:43], v[16:19]\ns_waitcnt lgkmcnt(2)\n" \
  "v_mfma_f32_16x16x32_f16 v[12:15], v[68:71], v[44:47], v[12:15]\nds_read_b128 v[36:39], v2 offset:640\n" \
  "v_mfma_f32_16x16x32_f16 v[16:19], v[64:67], v[44:47], v[16:19]\nv_mfma_f32_16x16x32_f16 v[20:23], v[60:63], v[44:47], 0\n" \
  "ds_read_b128 v[40:43], v2 offset:704\nv_mfma_f32_16x16x32_f16 v[28:31], v[56:59], v[44:47], 0\ns_waitcnt lgkmcnt(3)\n" \
  "v_mfma_f32_16x16x32_f16 v[16:19], v[124:127], v[48:51], v[16:19]\nds_read_b128 v[56:59], v2 offset:768\nv_cvt_pkrtz_f16_f32 v32, v12, v13\n" \
  "v_mfma_f32_16x16x32_f16 v[20:23], v[120:123], v[48:51], v[20:23]\nv_cvt_pkrtz_f16_f32 v33, v14, v15\ns_waitcnt lgkmcnt(3)\n" \
  "v_mfma_f32_16x16x32_f16 v[16:19], v[116:119], v[52:55], v[16:19]\nds_read_b128 v[44:47], v2 offset:832\n" \
  "v_pk_add_u16 v32, v32, 4 op_sel_hi:[1,0] clamp\nv_mfma_f32_16x16x32_f16 v[20:23], v[112:115], v[52:55], v[20:23]\n" \
  "v_pk_add_u16 v33, v33, 4 op_sel_hi:[1,0] clamp\ns_waitcnt lgkmcnt(3)\nv_mfma_f32_16x16x32_f16 v[16:19], v[108:111], v[36:39], v[16:19]\n" \
  "ds_read_b128 v[48:51], v2 offset:896\nds_write_b64 v4, v[32:33] offset:0\nv_mfma_f32_16x16x32_f16 v[20:23], v[104:107], v[36:39], v[20:23]\n" \
  "s_waitcnt lgkmcnt(3)\nv_mfma_f32_16x16x32_f16 v[16:19], v[100:103], v[40:43], v[16:19]\nds_read_b128 v[52:55], v2 offset:960\n" \
  "v_mfma_f32_16x16x32_f16 v[20:23], v[96:99], v[40:43], v[20:23]\nv_mfma_f32_16x16x32_f16 v[16:19], v[92:95], v[56:59], v[16:19]\n" \
  "v_mfma_f32_16x16x32_f16 v[20:23], v[88:91], v[56:59], v[20:23]\nds_read_b128 v[36:39], v2 offset:1024\ns_waitcnt lgkmcnt(3)\n" \
  "v_mfma_f32_16x16x32_f16 v[16:19], v[84:87], v[44:47], v[16:19]\nv_mfma_f32_16x16x32_f16 v[20:23], v[80:83], v[44:47], v[20:23]\n" \
  "ds_read_b128 v[40:43], v2 offset:1088\nds_read_b64 v[56:57], v6 offset:256\nds_read_b64 v[58:59], v6 offset:264\n" \
  "v_mfma_f32_16x16x32_f16 v[16:19], v[76:79], v[48:51], v[16:19]\nv_mfma_f32_16x16x32_f16 v[20:23], v[72:75], v[48:51], v[20:23]\n" \
  "ds_read_b128 v[44:47], v2 offset:1152\ns_waitcnt lgkmcnt(4)\nv_mfma_f32_16x16x32_f16 v[16:19], v[68:71], v[52:55], v[16:19]\n" \
  "v_mfma_f32_16x16x32_f16 v[20:23], v[64:67], v[52:55], v[20:23]\nds_read_b128 v[48:51], v2 offset:1216\n" \
  "v_mfma_f32_16x16x32_f16 v[24:27], v[60:63], v[52:55], 0\nv_mfma_f32_16x16x32_f16 v[20:23], v[124:127], v[36:39], v[20:23]\n" \
  "ds_read_b128 v[52:55], v2 offset:1280\nv_mfma_f32_16x16x32_f16 v[24:27], v[120:123], v[36:39], v[24:27]\ns_waitcnt lgkmcnt(5)\n" \
  "v_mfma_f32_16x16x32_f16 v[20:23], v[116:119], v[40:43], v[20:23]\nds_read_b128 v[36:39], v2 offset:1344\nv_cvt_pkrtz_f16_f32 v32, v16, v17\n" \
  "v_mfma_f32_16x16x32_f16 v[24:27], v[112:115], v[40:43], v[24:27]\nv_cvt_pkrtz_f16_f32 v33, v18, v19\ns_waitcnt lgkmcnt(3)\n" \
  "v_mfma_f32_16x16x32_f16 v[20:23], v[108:111], v[44:47], v[20:23]\nds_read_b128 v[40:43], v2 offset:1408\n" \
  "v_pk_add_u16 v32, v32, 4 op_sel_hi:[1,0] clamp\nds_read_b32 v35, v9 offset:32\nv_mfma_f32_16x16x32_f16 v[24:27], v[104:107], v[44:47], v[24:27]\n" \
  "v_pk_add_u16 v33, v33, 4 op_sel_hi:[1,0] clamp\ns_waitcnt lgkmcnt(3)\nv_mfma_f32_16x16x32_f16 v[20:23], v[100:103], v[48:51], v[20:23]\n" \
  "ds_read_b128 v[44:47], v2 offset:1472\nds_write_b64 v4, v[32:33] offset:1024\nv_mfma_f32_16x16x32_f16 v[24:27], v[96:99], v[48:51], v[24:27]\n" \
  "v_mfma_f32_16x16x32_f16 v[20:23], v[92:95], v[52:55], v[20:23]\nv_mfma_f32_16x16x32_f16 v[24:27], v[88:91], v[52:55], v[24:27]\n" \
  "ds_read_b128 v[48:51], v2 offset:1536\ns_waitcnt lgkmcnt(3)\nv_readfirstlane_b32 s85, v35\ns_cmp_lt_u32 s85, 4\ns_cbranch_scc1 21f\n22:\n" \
  "v_mfma_f32_16x16x32_f16 v[20:23], v[84:87], v[36:39], v[20:23]\nv_mfma_f32_16x16x32_f16 v[24:27], v[80:83], v[36:39], v[24:27]\n" \
  "ds_read_b128 v[52:55], v2 offset:1600\nv_mfma_f32_16x16x32_f16 v[20:23], v[76:79], v[40:43], v[20:23]\n" \
  "v_mfma_f32_16x16x32_f16 v[24:27], v[72:75], v[40:43], v[24:27]\nds_read_b128 v[36:39], v2 offset:1664\ns_waitcnt lgkmcnt(4)\n" \
  "v_mfma_f32_16x16x32_f16 v[20:23], v[68:71], v[44:47], v[20:23]\nv_mfma_f32_16x16x32_f16 v[24:27], v[64:67], v[44:47], v[24:27]\n" \
  "ds_read_b128 v[40:43], v2 offset:1728\nv_mfma_f32_16x16x32_f16 v[28:31], v[60:63], v[44:47], v[28:31]\nds_read_b64_tr_b16 v[60:61], v5 offset:0\n" \
  "ds_read_b64_tr_b16 v[62:63], v5 offset:32\ns_waitcnt lgkmcnt(4)\nv_mfma_f32_16x16x32_f16 v[24:27], v[124:127], v[48:51], v[24:27]\n" \
  "v_mfma_f32_16x16x32_f16 v[28:31], v[120:123], v[48:51], v[28:31]\nds_read_b64_tr_b16 v[44:45], v5 offset:256\n" \
  "ds_read_b64_tr_b16 v[46:47], v5 offset:288\nv_mfma_f32_16x16x32_f16 v[24:27], v[116:119], v[52:55], v[24:27]\n" \
  "ds_read_b64 v[120:121], v6 offset:320\nds_read_b64 v[122:123], v6 offset:328\nv_mfma_f32_16x16x32_f16 v[28:31], v[112:115], v[52:55], v[28:31]\n" \
  "ds_read_b128 v[48:51], v2 offset:1792\ns_waitcnt lgkmcnt(7)\nv_mfma_f32_16x16x32_f16 v[24:27], v[108:111], v[36:39], v[24:27]\n" \
  "ds_read_b128 v[112:115], v2 offset:1856\nv_mfma_f32_16x16x32_f16 v[28:31], v[104:107], v[36:39], v[28:31]\nds_read_b64 v[52:53], v6 offset:128\n" \
  "ds_read_b64 v[54:55], v6 offset:136\nv_mfma_f32_16x16x32_f16 v[24:27], v[100:103], v[40:43], v[24:27]\nds_read_b128 v[104:107], v2 offset:1920\n" \
  "v_mfma_f32_16x16x32_f16 v[28:31], v[96:99], v[40:43], v[28:31]\nds_read_b64 v[36:37], v6 offset:192\nds_read_b64 v[38:39], v6 offset:200\n" \
  "s_waitcnt lgkmcnt(9)\nv_mfma_f32_16x16x32_f16 v[20:23], v[60:63], v[56:59], v[20:23]\nds_read_b128 v[96:99], v2 offset:1984\ns_waitcnt lgkmcnt(7)\n" \
  "v_mfma_f32_16x16x32_f16 v[20:23], v[44:47], v[120:123], v[20:23]\nv_mfma_f32_16x16x32_f16 v[24:27], v[92:95], v[48:51], v[24:27]\n" \
  "ds_read_b128 v[40:43], v2 offset:2048\nv_mfma_f32_16x16x32_f16 v[28:31], v[88:91], v[48:51], v[28:31]\nds_read_b64_tr_b16 v[88:89], v5 offset:512\n" \
  "ds_read_b64_tr_b16 v[90:91], v5 offset:544\ns_waitcnt lgkmcnt(6)\nv_mfma_f32_16x16x32_f16 v[24:27], v[84:87], v[112:115], v[24:27]\n" \
  "v_mfma_f32_16x16x32_f16 v[28:31], v[80:83], v[112:115], v[28:31]\nv_cvt_pkrtz_f16_f32 v32, v20, v21\nv_cvt_pkrtz_f16_f32 v33, v22, v23\n" \
  "v_mfma_f32_16x16x32_f16 v[24:27], v[76:79], v[104:107], v[24:27]\nds_read_b64_tr_b16 v[48:49], v5 offset:768\n" \
  "ds_read_b64_tr_b16 v[50:51], v5 offset:800\nv_mfma_f32_16x16x32_f16 v[28:31], v[72:75], v[104:107], v[28:31]\n" \
  "ds_read_b128 v[80:83], v2 offset:2112\ns_waitcnt lgkmcnt(5)\nv_mfma_f32_16x16x32_f16 v[24:27], v[68:71], v[96:99], v[24:27]\n" \
  "ds_read_b128 v[112:115], v2 offset:2176\nv_pk_add_u16 v32, v32, 4 op_sel_hi:[1,0] clamp\n" \
  "v_mfma_f32_16x16x32_f16 v[28:31], v[64:67], v[96:99], v[28:31]\nds_read_b128 v[72:75], v2 offset:2240\n" \
  "v_pk_add_u16 v33, v33, 4 op_sel_hi:[1,0] clamp\nv_mfma_f32_16x16x32_f16 v[28:31], v[124:127], v[40:43], v[28:31]\n" \
  "ds_read_b128 v[104:107], v2 offset:2304\nds_write_b64 v4, v[32:33] offset:2048\nv_mfma_f32_16x16x32_f16 v[24:27], v[60:63], v[52:55], v[24:27]\n" \
  "ds_read_b128 v[64:67], v2 offset:2368\nds_read_b64 v[96:97], v6 offset:0\nds_read_b64 v[98:99], v6 offset:8\n" \
  "v_mfma_f32_16x16x32_f16 v[24:27], v[44:47], v[36:39], v[24:27]\nds_read_b128 v[124:127], v2 offset:2432\ns_waitcnt lgkmcnt(9)\n" \
  "v_mfma_f32_16x16x32_f16 v[24:27], v[88:91], v[56:59], v[24:27]\nds_read_b128 v[40:43], v2 offset:2496\n" \
  "v_mfma_f32_16x16x32_f16 v[24:27], v[48:51], v[120:123], v[24:27]\ns_waitcnt lgkmcnt(8)\n" \
  "v_mfma_f32_16x16x32_f16 v[28:31], v[116:119], v[80:83], v[28:31]\nds_read_b64 v[116:117], v6 offset:64\nds_read_b64 v[118:119], v6 offset:72\n" \
  "v_mfma_f32_16x16x32_f16 v[28:31], v[108:111], v[112:115], v[28:31]\nds_read_b64_tr_b16 v[80:81], v5 offset:1024\n" \
  "ds_read_b64_tr_b16 v[82:83], v5 offset:1056\ns_waitcnt lgkmcnt(10)\nv_mfma_f32_16x16x32_f16 v[28:31], v[100:103], v[72:75], v[28:31]\n" \
  "v_cvt_pkrtz_f16_f32 v32, v24, v25\nv_cvt_pkrtz_f16_f32 v33, v26, v27\nv_mfma_f32_16x16x32_f16 v[28:31], v[92:95], v[104:107], v[28:31]\n" \
  "v_pk_add_u16 v32, v32, 4 op_sel_hi:[1,0] clamp\ns_waitcnt lgkmcnt(5)\nv_mfma_f32_16x16x32_f16 v[28:31], v[84:87], v[64:67], v[28:31]\n" \
  "ds_read_b64_tr_b16 v[108:109], v5 offset:1280\nds_read_b64_tr_b16 v[110:111], v5 offset:1312\n" \
  "v_mfma_f32_16x16x32_f16 v[28:31], v[76:79], v[124:127], v[28:31]\nds_read_b64_tr_b16 v[112:113], v7 offset:0\n" \
  "ds_read_b64_tr_b16 v[114:115], v7 offset:64\ns_waitcnt lgkmcnt(6)\nv_mfma_f32_16x16x32_f16 v[28:31], v[68:71], v[40:43], v[28:31]\n" \
  "ds_read2_b32 v[100:101], v8 offset0:48 offset1:49\nds_read2_b32 v[102:103], v8 offset0:50 offset1:51\n" \
  "v_mfma_f32_16x16x32_f16 v[28:31], v[60:63], v[96:99], v[28:31]\nv_pk_add_u16 v33, v33, 4 op_sel_hi:[1,0] clamp\n" \
  "ds_write_b64 v4, v[32:33] offset:3072\nv_mfma_f32_16x16x32_f16 v[28:31], v[44:47], v[116:119], v[28:31]\n" \
  "v_mfma_f32_16x16x32_f16 v[28:31], v[88:91], v[52:55], v[28:31]\nv_mfma_f32_16x16x32_f16 v[28:31], v[48:51], v[36:39], v[28:31]\n" \
  "s_waitcnt lgkmcnt(5)\nv_mfma_f32_16x16x32_f16 v[28:31], v[80:83], v[56:59], v[28:31]\n" \
  "v_mfma_f32_16x16x32_f16 v[28:31], v[108:111], v[120:123], v[28:31]\ns_waitcnt lgkmcnt(1)\n" \
  "v_mfma_f32_16x16x32_f16 v[28:31], v[112:115], v[100:103], v[28:31]\ns_nop 7\nv_cvt_pkrtz_f16_f32 v32, v28, v29\nv_cvt_pkrtz_f16_f32 v33, v30, v31\n" \
  "v_pk_add_u16 v32, v32, 4 op_sel_hi:[1,0] clamp\nv_pk_add_u16 v33, v33, 4 op_sel_hi:[1,0] clamp\nds_write_b64 v4, v[32:33] offset:4096\n" \
  "s_setprio 0\ns_setpc_b64 s[90:91]\n21:\ns_movk_i32 s84, 0x4000\n23:\nds_read_b32 v35, v9 offset:32\ns_sub_u32 s84, s84, 1\ns_cbranch_scc1 22b\n" \
  "s_waitcnt lgkmcnt(0)\nv_readfirstlane_b32 s85, v35\ns_cmp_lt_u32 s85, 4\ns_cbranch_scc1 23b\ns_branch 22b\n1:\n" \
  "ds_read_b64 v[124:125], v3 offset:1088\nds_read_b64 v[126:127], v3 offset:1096\nds_read_b64 v[120:121], v3 offset:0\n" \
  "ds_read_b64 v[122:123], v3 offset:8\nds_read_b64 v[116:117], v3 offset:64\nds_read_b64 v[118:119], v3 offset:72\n" \
  "ds_read_b64 v[112:113], v3 offset:128\nds_read_b64 v[114:115], v3 offset:136\nds_read_b64 v[108:109], v3 offset:192\n" \
  "ds_read_b64 v[110:111], v3 offset:200\nds_read_b64 v[104:105], v3 offset:256\nds_read_b64 v[106:107], v3 offset:264\n" \
  "ds_read_b64 v[100:101], v3 offset:320\nds_read_b64 v[102:103], v3 offset:328\nds_read_b64 v[96:97], v3 offset:384\n" \
  "ds_read_b64 v[98:99], v3 offset:392\nds_read_b64 v[92:93], v3 offset:448\nds_read_b64 v[94:95], v3 offset:456\n" \
  "ds_read_b64 v[88:89], v3 offset:512\nds_read_b64 v[90:91], v3 offset:520\nds_read_b64 v[84:85], v3 offset:576\n" \
  "ds_read_b64 v[86:87], v3 offset:584\nds_read_b64 v[80:81], v3 offset:640\nds_read_b64 v[82:83], v3 offset:648\n" \
  "ds_read_b64 v[76:77], v3 offset:704\nds_read_b64 v[78:79], v3 offset:712\nds_read_b64 v[72:73], v3 offset:768\n" \
  "ds_read_b64 v[74:75], v3 offset:776\nds_read_b64 v[68:69], v3 offset:832\nds_read_b64 v[70:71], v3 offset:840\n" \
  "ds_read_b64 v[64:65], v3 offset:896\nds_read_b64 v[66:67], v3 offset:904\nds_read_b64 v[60:61], v3 offset:960\n" \
  "ds_read_b64 v[62:63], v3 offset:968\nds_read_b64 v[56:57], v3 offset:1024\nds_read_b64 v[58:59], v3 offset:1032\n" \
  "ds_read_b64 v[52:53], v6 offset:0\nds_read_b64 v[54:55], v6 offset:8\nds_read_b64 v[48:49], v6 offset:64\nds_read_b64 v[50:51], v6 offset:72\n" \
  "s_waitcnt lgkmcnt(0)\ns_barrier\nds_read_b128 v[28:31], v2 offset:960\nds_read_b128 v[32:35], v2 offset:1472\n" \
  "ds_read_b128 v[36:39], v2 offset:1984\nds_read_b128 v[40:43], v2 offset:2048\nds_read_b128 v[44:47], v2 offset:2112\ns_waitcnt lgkmcnt(3)\n" \
  "v_mfma_f32_16x16x32_f16 v[12:15], v[124:127], v[28:31], 0\ns_setprio 1\nds_read_b128 v[28:31], v2 offset:2176\n" \
  "v_mfma_f32_16x16x32_f16 v[16:19], v[124:127], v[32:35], 0\nds_read_b128 v[32:35], v2 offset:2240\ns_waitcnt lgkmcnt(3)\n" \
  "v_mfma_f32_16x16x32_f16 v[12:15], v[120:123], v[36:39], v[12:15]\nv_mfma_f32_16x16x32_f16 v[20:23], v[124:127], v[36:39], 0\n" \
  "ds_read_b128 v[124:127], v2 offset:2304\nds_read_b128 v[36:39], v2 offset:2368\nv_mfma_f32_16x16x32_f16 v[12:15], v[116:119], v[40:43], v[12:15]\n" \
  "ds_read_b128 v[40:43], v2 offset:2432\ns_waitcnt lgkmcnt(4)\nv_mfma_f32_16x16x32_f16 v[12:15], v[112:115], v[44:47], v[12:15]\n" \
  "ds_read_b128 v[44:47], v2 offset:2496\nv_mfma_f32_16x16x32_f16 v[12:15], v[108:111], v[28:31], v[12:15]\nds_read_b128 v[28:31], v2 offset:2560\n" \
  "s_waitcnt lgkmcnt(4)\nv_mfma_f32_16x16x32_f16 v[12:15], v[104:107], v[32:35], v[12:15]\nds_read_b128 v[32:35], v2 offset:2624\n" \
  "v_mfma_f32_16x16x32_f16 v[12:15], v[100:103], v[124:127], v[12:15]\ns_waitcnt lgkmcnt(3)\n" \
  "v_mfma_f32_16x16x32_f16 v[12:15], v[96:99], v[36:39], v[12:15]\nds_read_b128 v[124:127], v2 offset:2688\n" \
  "v_mfma_f32_16x16x32_f16 v[12:15], v[92:95], v[40:43], v[12:15]\ns_waitcnt lgkmcnt(3)\n" \
  "v_mfma_f32_16x16x32_f16 v[12:15], v[88:91], v[44:47], v[12:15]\nds_read_b128 v[36:39], v2 offset:2752\n" \
  "v_mfma_f32_16x16x32_f16 v[16:19], v[120:123], v[44:47], v[16:19]\ns_waitcnt lgkmcnt(3)\n" \
  "v_mfma_f32_16x16x32_f16 v[12:15], v[84:87], v[28:31], v[12:15]\nds_read_b128 v[40:43], v2 offset:2816\n" \
  "v_mfma_f32_16x16x32_f16 v[16:19], v[116:119], v[28:31], v[16:19]\ns_waitcnt lgkmcnt(3)\n" \
  "v_mfma_f32_16x16x32_f16 v[12:15], v[80:83], v[32:35], v[12:15]\nds_read_b128 v[44:47], v2 offset:2880\n" \
  "v_mfma_f32_16x16x32_f16 v[16:19], v[112:115], v[32:35], v[16:19]\ns_waitcnt lgkmcnt(3)\n" \
  "v_mfma_f32_16x16x32_f16 v[12:15], v[76:79], v[124:127], v[12:15]\nds_read_b128 v[28:31], v2 offset:2944\n" \
  "v_mfma_f32_16x16x32_f16 v[16:19], v[108:111], v[124:127], v[16:19]\ns_waitcnt lgkmcnt(3)\n" \
  "v_mfma_f32_16x16x32_f16 v[12:15], v[72:75], v[36:39], v[12:15]\nds_read_b128 v[32:35], v2 offset:3008\n" \
  "v_mfma_f32_16x16x32_f16 v[16:19], v[104:107], v[36:39], v[16:19]\ns_waitcnt lgkmcnt(3)\n" \
  "v_mfma_f32_16x16x32_f16 v[12:15], v[68:71], v[40:43], v[12:15]\nv_mfma_f32_16x16x32_f16 v[16:19], v[100:103], v[40:43], v[16:19]\n" \
  "ds_read_b128 v[124:127], v2 offset:3072\ns_waitcnt lgkmcnt(3)\nv_mfma_f32_16x16x32_f16 v[12:15], v[64:67], v[44:47], v[12:15]\n" \
  "v_mfma_f32_16x16x32_f16 v[16:19], v[96:99], v[44:47], v[16:19]\nds_read_b128 v[36:39], v2 offset:3136\ns_waitcnt lgkmcnt(3)\n" \
  "v_mfma_f32_16x16x32_f16 v[12:15], v[60:63], v[28:31], v[12:15]\nds_read_b64 v[40:41], v6 offset:128\nds_read_b64 v[42:43], v6 offset:136\n" \
  "v_mfma_f32_16x16x32_f16 v[16:19], v[92:95], v[28:31], v[16:19]\nds_read_b128 v[44:47], v2 offset:3200\ns_waitcnt lgkmcnt(4)\n" \
  "v_mfma_f32_16x16x32_f16 v[12:15], v[56:59], v[32:35], v[12:15]\nv_mfma_f32_16x16x32_f16 v[16:19], v[88:91], v[32:35], v[16:19]\n" \
  "ds_read_b128 v[28:31], v2 offset:3264\nv_mfma_f32_16x16x32_f16 v[20:23], v[120:123], v[32:35], v[20:23]\nds_read_b64 v[120:121], v6 offset:192\n" \
  "ds_read_b64 v[122:123], v6 offset:200\nv_mfma_f32_16x16x32_f16 v[16:19], v[84:87], v[124:127], v[16:19]\nds_read_b128 v[32:35], v2 offset:3328\n" \
  "v_mfma_f32_16x16x32_f16 v[20:23], v[116:119], v[124:127], v[20:23]\nds_read_b64 v[116:117], v6 offset:256\nds_read_b64 v[118:119], v6 offset:264\n" \
  "s_waitcnt lgkmcnt(6)\nv_mfma_f32_16x16x32_f16 v[16:19], v[80:83], v[36:39], v[16:19]\nds_read_b128 v[124:127], v2 offset:3392\n" \
  "v_mfma_f32_16x16x32_f16 v[20:23], v[112:115], v[36:39], v[20:23]\nds_read_b64 v[112:113], v6 offset:320\nds_read_b64 v[114:115], v6 offset:328\n" \
  "v_mfma_f32_16x16x32_f16 v[16:19], v[76:79], v[44:47], v[16:19]\nds_read_b128 v[36:39], v2 offset:3456\n" \
  "v_mfma_f32_16x16x32_f16 v[20:23], v[108:111], v[44:47], v[20:23]\ns_waitcnt lgkmcnt(6)\n" \
  "v_mfma_f32_16x16x32_f16 v[16:19], v[72:75], v[28:31], v[16:19]\nds_read_b128 v[108:111], v2 offset:3520\n" \
  "v_mfma_f32_16x16x32_f16 v[20:23], v[104:107], v[28:31], v[20:23]\nv_mfma_f32_16x16x32_f16 v[16:19], v[68:71], v[32:35], v[16:19]\n" \
  "ds_read_b128 v[44:47], v2 offset:3584\nv_mfma_f32_16x16x32_f16 v[20:23], v[100:103], v[32:35], v[20:23]\nds_read_b128 v[104:107], v2 offset:3648\n" \
  "ds_read_b32 v27, v9 offset:32\ns_waitcnt lgkmcnt(4)\nv_mfma_f32_16x16x32_f16 v[16:19], v[64:67], v[124:127], v[16:19]\n" \
  "ds_read_b128 v[28:31], v2 offset:3712\nv_mfma_f32_16x16x32_f16 v[20:23], v[96:99], v[124:127], v[20:23]\nds_read_b128 v[100:103], v2 offset:3776\n" \
  "ds_read2_b32 v[32:33], v8 offset0:32 offset1:33\nds_read2_b32 v[34:35], v8 offset0:34 offset1:35\n" \
  "v_mfma_f32_16x16x32_f16 v[16:19], v[60:63], v[36:39], v[16:19]\nds_read_b128 v[96:99], v2 offset:3840\n" \
  "v_mfma_f32_16x16x32_f16 v[20:23], v[92:95], v[36:39], v[20:23]\nds_read_b128 v[124:127], v2 offset:3904\ns_waitcnt lgkmcnt(8)\n" \
  "v_mfma_f32_16x16x32_f16 v[16:19], v[56:59], v[108:111], v[16:19]\nds_read_b128 v[92:95], v2 offset:3968\ns_waitcnt lgkmcnt(7)\n" \
  "v_readfirstlane_b32 s85, v27\ns_cmp_lt_u32 s85, 4\ns_cbranch_scc1 31f\n32:\nv_mfma_f32_16x16x32_f16 v[20:23], v[88:91], v[108:111], v[20:23]\n" \
  "ds_read_b128 v[36:39], v2 offset:4032\nds_read_b64_tr_b16 v[88:89], v5 offset:512\nds_read_b64_tr_b16 v[90:91], v5 offset:544\n" \
  "v_mfma_f32_16x16x32_f16 v[20:23], v[84:87], v[44:47], v[20:23]\nds_read_b64_tr_b16 v[108:109], v5 offset:768\n" \
  "ds_read_b64_tr_b16 v[110:111], v5 offset:800\nv_mfma_f32_16x16x32_f16 v[20:23], v[80:83], v[104:107], v[20:23]\n" \
  "ds_read_b64_tr_b16 v[84:85], v5 offset:1024\nds_read_b64_tr_b16 v[86:87], v5 offset:1056\ns_waitcnt lgkmcnt(12)\n" \
  "v_mfma_f32_16x16x32_f16 v[20:23], v[76:79], v[28:31], v[20:23]\nds_read_b64_tr_b16 v[44:45], v5 offset:1280\n" \
  "ds_read_b64_tr_b16 v[46:47], v5 offset:1312\nv_mfma_f32_16x16x32_f16 v[20:23], v[72:75], v[100:103], v[20:23]\ns_waitcnt lgkmcnt(10)\n" \
  "v_mfma_f32_16x16x32_f16 v[20:23], v[68:71], v[96:99], v[20:23]\nds_read_b64_tr_b16 v[80:81], v5 offset:1536\n" \
  "ds_read_b64_tr_b16 v[82:83], v5 offset:1568\nv_mfma_f32_16x16x32_f16 v[20:23], v[64:67], v[124:127], v[20:23]\n" \
  "ds_read_b64_tr_b16 v[104:105], v5 offset:1792\nds_read_b64_tr_b16 v[106:107], v5 offset:1824\ns_waitcnt lgkmcnt(12)\n" \
  "v_mfma_f32_16x16x32_f16 v[20:23], v[60:63], v[92:95], v[20:23]\nds_read_b64_tr_b16 v[76:77], v5 offset:2048\n" \
  "ds_read_b64_tr_b16 v[78:79], v5 offset:2080\nv_mfma_f32_16x16x32_f16 v[20:23], v[56:59], v[36:39], v[20:23]\ns_waitcnt lgkmcnt(10)\n" \
  "v_mfma_f32_16x16x32_f16 v[12:15], v[88:91], v[52:55], v[12:15]\nds_read_b64_tr_b16 v[28:29], v5 offset:2304\n" \
  "ds_read_b64_tr_b16 v[30:31], v5 offset:2336\nv_mfma_f32_16x16x32_f16 v[12:15], v[108:111], v[48:51], v[12:15]\n" \
  "ds_read_b64_tr_b16 v[72:73], v5 offset:2560\nds_read_b64_tr_b16 v[74:75], v5 offset:2592\ns_waitcnt lgkmcnt(10)\n" \
  "v_mfma_f32_16x16x32_f16 v[12:15], v[84:87], v[40:43], v[12:15]\nds_read_b64_tr_b16 v[100:101], v5 offset:2816\n" \
  "ds_read_b64_tr_b16 v[102:103], v5 offset:2848\nv_mfma_f32_16x16x32_f16 v[16:19], v[84:87], v[52:55], v[16:19]\n" \
  "ds_read_b64_tr_b16 v[68:69], v7 offset:0\nds_read_b64_tr_b16 v[70:71], v7 offset:64\n" \
  "v_mfma_f32_16x16x32_f16 v[12:15], v[44:47], v[120:123], v[12:15]\nv_mfma_f32_16x16x32_f16 v[16:19], v[44:47], v[48:51], v[16:19]\n" \
  "s_waitcnt lgkmcnt(10)\nv_mfma_f32_16x16x32_f16 v[12:15], v[80:83], v[116:119], v[12:15]\nds_read2_b32 v[96:97], v8 offset0:16 offset1:17\n" \
  "ds_read2_b32 v[98:99], v8 offset0:18 offset1:19\nv_mfma_f32_16x16x32_f16 v[16:19], v[80:83], v[40:43], v[16:19]\n" \
  "ds_read2_b32 v[64:65], v8 offset0:0 offset1:1\nds_read2_b32 v[66:67], v8 offset0:2 offset1:3\n" \
  "v_mfma_f32_16x16x32_f16 v[20:23], v[80:83], v[52:55], v[20:23]\nv_mfma_f32_16x16x32_f16 v[12:15], v[104:107], v[112:115], v[12:15]\n" \
  "v_mfma_f32_16x16x32_f16 v[16:19], v[104:107], v[120:123], v[16:19]\nv_mfma_f32_16x16x32_f16 v[20:23], v[104:107], v[48:51], v[20:23]\n" \
  "s_waitcnt lgkmcnt(10)\nv_mfma_f32_16x16x32_f16 v[16:19], v[76:79], v[116:119], v[16:19]\nds_read_b64_tr_b16 v[124:125], v7 offset:512\n" \
  "ds_read_b64_tr_b16 v[126:127], v7 offset:576\nv_mfma_f32_16x16x32_f16 v[20:23], v[76:79], v[40:43], v[20:23]\n" \
  "ds_read2_b32 v[60:61], v8 offset0:48 offset1:49\nds_read2_b32 v[62:63], v8 offset0:50 offset1:51\n" \
  "v_mfma_f32_16x16x32_f16 v[16:19], v[28:31], v[112:115], v[16:19]\nv_mfma_f32_16x16x32_f16 v[20:23], v[28:31], v[120:123], v[20:23]\n" \
  "s_waitcnt lgkmcnt(10)\nv_mfma_f32_16x16x32_f16 v[20:23], v[72:75], v[116:119], v[20:23]\nds_read_b64_tr_b16 v[92:93], v7 offset:1024\n" \
  "ds_read_b64_tr_b16 v[94:95], v7 offset:1088\nv_mfma_f32_16x16x32_f16 v[20:23], v[100:103], v[112:115], v[20:23]\n" \
  "ds_read_b64_tr_b16 v[56:57], v7 offset:1536\nds_read_b64_tr_b16 v[58:59], v7 offset:1600\ns_waitcnt lgkmcnt(10)\n" \
  "v_mfma_f32_16x16x32_f16 v[12:15], v[68:71], v[32:35], v[12:15]\nv_mfma_f32_16x16x32_f16 v[16:19], v[68:71], v[96:99], v[16:19]\n" \
  "s_waitcnt lgkmcnt(6)\nv_mfma_f32_16x16x32_f16 v[20:23], v[68:71], v[64:67], v[20:23]\ns_waitcnt lgkmcnt(2)\n" \
  "v_mfma_f32_16x16x32_f16 v[12:15], v[124:127], v[60:63], v[12:15]\nv_mfma_f32_16x16x32_f16 v[16:19], v[124:127], v[32:35], v[16:19]\n" \
  "v_mfma_f32_16x16x32_f16 v[20:23], v[124:127], v[96:99], v[20:23]\nv_mfma_f32_16x16x32_f16 v[16:19], v[92:95], v[60:63], v[16:19]\n" \
  "v_mfma_f32_16x16x32_f16 v[20:23], v[92:95], v[32:35], v[20:23]\ns_waitcnt lgkmcnt(0)\n" \
  "v_mfma_f32_16x16x32_f16 v[20:23], v[56:59], v[60:63], v[20:23]\ns_nop 1\nv_cvt_pkrtz_f16_f32 v24, v12, v13\nv_cvt_pkrtz_f16_f32 v25, v14, v15\n" \
  "v_pk_add_u16 v24, v24, 4 op_sel_hi:[1,0] clamp\nv_pk_add_u16 v25, v25, 4 op_sel_hi:[1,0] clamp\nds_write_b64 v4, v[24:25] offset:5120\n" \
  "v_cvt_pkrtz_f16_f32 v24, v16, v17\nv_cvt_pkrtz_f16_f32 v25, v18, v19\nv_pk_add_u16 v24, v24, 4 op_sel_hi:[1,0] clamp\n" \
  "v_pk_add_u16 v25, v25, 4 op_sel_hi:[1,0] clamp\nds_write_b64 v4, v[24:25] offset:6144\nv_cvt_pkrtz_f16_f32 v24, v20, v21\n" \
  "v_cvt_pkrtz_f16_f32 v25, v22, v23\nv_pk_add_u16 v24, v24, 4 op_sel_hi:[1,0] clamp\nv_pk_add_u16 v25, v25, 4 op_sel_hi:[1,0] clamp\n" \
  "ds_write_b64 v4, v[24:25] offset:7168\ns_setprio 0\ns_setpc_b64 s[90:91]\n31:\ns_movk_i32 s84, 0x4000\n33:\nds_read_b32 v27, v9 offset:32\n" \
  "s_sub_u32 s84, s84, 1\ns_cbranch_scc1 32b\ns_waitcnt lgkmcnt(0)\nv_readfirstlane_b32 s85, v27\ns_cmp_lt_u32 s85, 4\ns_cbranch_scc1 33b\n" \
  "s_branch 32b\n9:\ns_cmp_eq_u32 s89, 3\ns_cbranch_scc1 3f\ns_cmp_eq_u32 s89, 4\ns_cbranch_scc1 4f\ns_cmp_eq_u32 s89, 5\ns_cbranch_scc1 5f\n" \
  "ds_read_b64 v[124:125], v3 offset:576\nds_read_b64 v[126:127], v3 offset:584\nds_read_b64 v[120:121], v3 offset:64\n" \
  "ds_read_b64 v[122:123], v3 offset:72\nds_read_b64 v[116:117], v3 offset:640\nds_read_b64 v[118:119], v3 offset:648\n" \
  "ds_read_b64 v[112:113], v3 offset:128\nds_read_b64 v[114:115], v3 offset:136\nds_read_b64 v[108:109], v3 offset:704\n" \
  "ds_read_b64 v[110:111], v3 offset:712\nds_read_b64 v[104:105], v3 offset:192\nds_read_b64 v[106:107], v3 offset:200\n" \
  "ds_read_b64 v[100:101], v3 offset:768\nds_read_b64 v[102:103], v3 offset:776\nds_read_b64 v[96:97], v3 offset:256\n" \
  "ds_read_b64 v[98:99], v3 offset:264\nds_read_b64 v[92:93], v3 offset:832\nds_read_b64 v[94:95], v3 offset:840\n" \
  "ds_read_b64 v[88:89], v3 offset:320\nds_read_b64 v[90:91], v3 offset:328\nds_read_b64 v[84:85], v3 offset:896\n" \
  "ds_read_b64 v[86:87], v3 offset:904\nds_read_b64 v[80:81], v3 offset:384\nds_read_b64 v[82:83], v3 offset:392\n" \
  "ds_read_b64 v[76:77], v3 offset:960\nds_read_b64 v[78:79], v3 offset:968\nds_read_b64 v[72:73], v3 offset:448\n" \
  "ds_read_b64 v[74:75], v3 offset:456\nds_read_b64 v[68:69], v3 offset:1024\nds_read_b64 v[70:71], v3 offset:1032\n" \
  "ds_read_b64 v[64:65], v3 offset:512\nds_read_b64 v[66:67], v3 offset:520\ns_waitcnt lgkmcnt(0)\ns_barrier\nds_read_b128 v[24:27], v2 offset:0\n" \
  "ds_read_b128 v[28:31], v2 offset:64\nds_read_b128 v[32:35], v2 offset:128\nds_read_b128 v[36:39], v2 offset:192\ns_waitcnt lgkmcnt(3)\n" \
  "v_mfma_f32_16x16x32_f16 v[12:15], v[124:127], v[24:27], 0\ns_setprio 1\nds_read_b128 v[40:43], v2 offset:256\n" \
  "v_mfma_f32_16x16x32_f16 v[16:19], v[120:123], v[24:27], 0\ns_waitcnt lgkmcnt(3)\nv_mfma_f32_16x16x32_f16 v[12:15], v[116:119], v[28:31], v[12:15]\n" \
  "ds_read_b128 v[44:47], v2 offset:320\nv_mfma_f32_16x16x32_f16 v[16:19], v[112:115], v[28:31], v[16:19]\ns_waitcnt lgkmcnt(3)\n" \
  "v_mfma_f32_16x16x32_f16 v[12:15], v[108:111], v[32:35], v[12:15]\nds_read_b128 v[48:51], v2 offset:384\n" \
  "v_mfma_f32_16x16x32_f16 v[16:19], v[104:107], v[32:35], v[16:19]\ns_waitcnt lgkmcnt(3)\n" \
  "v_mfma_f32_16x16x32_f16 v[12:15], v[100:103], v[36:39], v[12:15]\nds_read_b128 v[52:55], v2 offset:448\n" \
  "v_mfma_f32_16x16x32_f16 v[16:19], v[96:99], v[36:39], v[16:19]\ns_waitcnt lgkmcnt(3)\n" \
  "v_mfma_f32_16x16x32_f16 v[12:15], v[92:95], v[40:43], v[12:15]\nds_read_b128 v[56:59], v2 offset:512\n" \
  "v_mfma_f32_16x16x32_f16 v[16:19], v[88:91], v[40:43], v[16:19]\nds_read_b128 v[60:63], v2 offset:576\ns_waitcnt lgkmcnt(3)\n" \
  "v_mfma_f32_16x16x32_f16 v[12:15], v[84:87], v[44:47], v[12:15]\nds_read_b128 v[120:123], v2 offset:640\n" \
  "v_mfma_f32_16x16x32_f16 v[16:19], v[80:83], v[44:47], v[16:19]\nds_read_b128 v[24:27], v2 offset:704\n" \
  "v_mfma_f32_16x16x32_f16 v[12:15], v[76:79], v[48:51], v[12:15]\nds_read_b128 v[112:115], v2 offset:768\n" \
  "v_mfma_f32_16x16x32_f16 v[16:19], v[72:75], v[48:51], v[16:19]\nds_read_b128 v[28:31], v2 offset:832\ns_waitcnt lgkmcnt(5)\n" \
  "v_mfma_f32_16x16x32_f16 v[12:15], v[68:71], v[52:55], v[12:15]\nds_read_b128 v[104:107], v2 offset:896\n" \
  "v_mfma_f32_16x16x32_f16 v[16:19], v[64:67], v[52:55], v[16:19]\nds_read_b128 v[32:35], v2 offset:960\n" \
  "v_mfma_f32_16x16x32_f16 v[16:19], v[124:127], v[56:59], v[16:19]\ns_waitcnt lgkmcnt(5)\n" \
  "v_mfma_f32_16x16x32_f16 v[16:19], v[116:119], v[60:63], v[16:19]\nv_mfma_f32_16x16x32_f16 v[16:19], v[108:111], v[120:123], v[16:19]\n" \
  "s_waitcnt lgkmcnt(3)\nv_mfma_f32_16x16x32_f16 v[16:19], v[100:103], v[24:27], v[16:19]\nv_cvt_pkrtz_f16_f32 v20, v12, v13\n" \
  "v_cvt_pkrtz_f16_f32 v21, v14, v15\nv_mfma_f32_16x16x32_f16 v[16:19], v[92:95], v[112:115], v[16:19]\n" \
  "v_pk_add_u16 v20, v20, 4 op_sel_hi:[1,0] clamp\ns_waitcnt lgkmcnt(2)\nv_mfma_f32_16x16x32_f16 v[16:19], v[84:87], v[28:31], v[16:19]\n" \
  "v_pk_add_u16 v21, v21, 4 op_sel_hi:[1,0] clamp\ns_waitcnt lgkmcnt(1)\nv_mfma_f32_16x16x32_f16 v[16:19], v[76:79], v[104:107], v[16:19]\n" \
  "ds_write_b64 v4, v[20:21] offset:0\ns_waitcnt lgkmcnt(1)\nv_mfma_f32_16x16x32_f16 v[16:19], v[68:71], v[32:35], v[16:19]\ns_nop 7\n" \
  "v_cvt_pkrtz_f16_f32 v20, v16, v17\nv_cvt_pkrtz_f16_f32 v21, v18, v19\nv_pk_add_u16 v20, v20, 4 op_sel_hi:[1,0] clamp\n" \
  "v_pk_add_u16 v21, v21, 4 op_sel_hi:[1,0] clamp\nds_write_b64 v4, v[20:21] offset:1024\ns_setprio 0\ns_setpc_b64 s[90:91]\n3:\n" \
  "ds_read_b64 v[124:125], v3 offset:0\nds_read_b64 v[126:127], v3 offset:8\nds_read_b64 v[120:121], v3 offset:64\n" \
  "ds_read_b64 v[122:123], v3 offset:72\nds_read_b64 v[116:117], v3 offset:128\nds_read_b64 v[118:119], v3 offset:136\n" \
  "ds_read_b64 v[112:113], v3 offset:192\nds_read_b64 v[114:115], v3 offset:200\nds_read_b64 v[108:109], v3 offset:256\n" \
  "ds_read_b64 v[110:111], v3 offset:264\nds_read_b64 v[104:105], v3 offset:320\nds_read_b64 v[106:107], v3 offset:328\n" \
  "ds_read_b64 v[100:101], v3 offset:384\nds_read_b64 v[102:103], v3 offset:392\nds_read_b64 v[96:97], v3 offset:448\n" \
  "ds_read_b64 v[98:99], v3 offset:456\nds_read_b64 v[92:93], v3 offset:512\nds_read_b64 v[94:95], v3 offset:520\n" \
  "ds_read_b64 v[88:89], v3 offset:576\nds_read_b64 v[90:91], v3 offset:584\nds_read_b64 v[84:85], v3 offset:640\n" \
  "ds_read_b64 v[86:87], v3 offset:648\nds_read_b64 v[80:81], v3 offset:704\nds_read_b64 v[82:83], v3 offset:712\n" \
  "ds_read_b64 v[76:77], v3 offset:768\nds_read_b64 v[78:79], v3 offset:776\nds_read_b64 v[72:73], v3 offset:832\n" \
  "ds_read_b64 v[74:75], v3 offset:840\nds_read_b64 v[68:69], v3 offset:896\nds_read_b64 v[70:71], v3 offset:904\n" \
  "ds_read_b64 v[64:65], v3 offset:960\nds_read_b64 v[66:67], v3 offset:968\nds_read_b64 v[60:61], v3 offset:1024\n" \
  "ds_read_b64 v[62:63], v3 offset:1032\nds_read_b64 v[56:57], v6 offset:256\nds_read_b64 v[58:59], v6 offset:264\n" \
  "ds_read_b64 v[52:53], v6 offset:128\nds_read_b64 v[54:55], v6 offset:136\nds_read_b64 v[48:49], v6 offset:320\n" \
  "ds_read_b64 v[50:51], v6 offset:328\nds_read_b64 v[44:45], v6 offset:192\nds_read_b64 v[46:47], v6 offset:200\ns_waitcnt lgkmcnt(0)\ns_barrier\n" \
  "ds_read_b128 v[24:27], v2 offset:448\nds_read_b128 v[28:31], v2 offset:512\nds_read_b128 v[32:35], v2 offset:576\n" \
  "ds_read_b128 v[36:39], v2 offset:640\nds_read_b128 v[40:43], v2 offset:704\ns_waitcnt lgkmcnt(3)\n" \
  "v_mfma_f32_16x16x32_f16 v[12:15], v[124:127], v[24:27], 0\ns_setprio 1\nds_read_b128 v[24:27], v2 offset:768\n" \
  "v_mfma_f32_16x16x32_f16 v[12:15], v[120:123], v[28:31], v[12:15]\nds_read_b128 v[28:31], v2 offset:832\ns_waitcnt lgkmcnt(3)\n" \
  "v_mfma_f32_16x16x32_f16 v[12:15], v[116:119], v[32:35], v[12:15]\nds_read_b128 v[32:35], v2 offset:896\n" \
  "v_mfma_f32_16x16x32_f16 v[12:15], v[112:115], v[36:39], v[12:15]\nds_read_b128 v[36:39], v2 offset:960\ns_waitcnt lgkmcnt(3)\n" \
  "v_mfma_f32_16x16x32_f16 v[12:15], v[108:111], v[40:43], v[12:15]\nds_read_b128 v[40:43], v2 offset:1024\n" \
  "v_mfma_f32_16x16x32_f16 v[12:15], v[104:107], v[24:27], v[12:15]\nds_read_b128 v[24:27], v2 offset:1088\ns_waitcnt lgkmcnt(3)\n" \
  "v_mfma_f32_16x16x32_f16 v[12:15], v[100:103], v[28:31], v[12:15]\nds_read_b128 v[28:31], v2 offset:1152\n" \
  "v_mfma_f32_16x16x32_f16 v[12:15], v[96:99], v[32:35], v[12:15]\ns_waitcnt lgkmcnt(3)\n" \
  "v_mfma_f32_16x16x32_f16 v[12:15], v[92:95], v[36:39], v[12:15]\nds_read_b128 v[32:35], v2 offset:1216\n" \
  "v_mfma_f32_16x16x32_f16 v[16:19], v[124:127], v[36:39], 0\ns_waitcnt lgkmcnt(3)\nv_mfma_f32_16x16x32_f16 v[12:15], v[88:91], v[40:43], v[12:15]\n" \
  "ds_read_b128 v[124:127], v2 offset:1280\nv_mfma_f32_16x16x32_f16 v[16:19], v[120:123], v[40:43], v[16:19]\ns_waitcnt lgkmcnt(3)\n" \
  "v_mfma_f32_16x16x32_f16 v[12:15], v[84:87], v[24:27], v[12:15]\nds_read_b128 v[36:39], v2 offset:1344\n" \
  "v_mfma_f32_16x16x32_f16 v[16:19], v[116:119], v[24:27], v[16:19]\ns_waitcnt lgkmcnt(3)\n" \
  "v_mfma_f32_16x16x32_f16 v[12:15], v[80:83], v[28:31], v[12:15]\nds_read_b128 v[120:123], v2 offset:1408\n" \
  "v_mfma_f32_16x16x32_f16 v[16:19], v[112:115], v[28:31], v[16:19]\ns_waitcnt lgkmcnt(3)\n" \
  "v_mfma_f32_16x16x32_f16 v[12:15], v[76:79], v[32:35], v[12:15]\nds_read_b128 v[40:43], v2 offset:1472\n" \
  "v_mfma_f32_16x16x32_f16 v[16:19], v[108:111], v[32:35], v[16:19]\ns_waitcnt lgkmcnt(3)\n" \
  "v_mfma_f32_16x16x32_f16 v[12:15], v[72:75], v[124:127], v[12:15]\nds_read_b128 v[116:119], v2 offset:1536\n" \
  "v_mfma_f32_16x16x32_f16 v[16:19], v[104:107], v[124:127], v[16:19]\nds_read_b128 v[24:27], v2 offset:1600\ns_waitcnt lgkmcnt(3)\n" \
  "v_mfma_f32_16x16x32_f16 v[12:15], v[68:71], v[36:39], v[12:15]\nds_read_b128 v[112:115], v2 offset:1664\nds_read_b32 v23, v9 offset:32\n" \
  "v_mfma_f32_16x16x32_f16 v[16:19], v[100:103], v[36:39], v[16:19]\nds_read_b128 v[28:31], v2 offset:1728\n" \
  "v_mfma_f32_16x16x32_f16 v[12:15], v[64:67], v[120:123], v[12:15]\nds_read_b128 v[108:111], v2 offset:1792\n" \
  "v_mfma_f32_16x16x32_f16 v[16:19], v[96:99], v[120:123], v[16:19]\nds_read_b128 v[32:35], v2 offset:1856\ns_waitcnt lgkmcnt(6)\n" \
  "v_mfma_f32_16x16x32_f16 v[12:15], v[60:63], v[40:43], v[12:15]\nds_read_b128 v[104:107], v2 offset:1920\n" \
  "v_mfma_f32_16x16x32_f16 v[16:19], v[92:95], v[40:43], v[16:19]\nds_read_b128 v[124:127], v2 offset:1984\ns_waitcnt lgkmcnt(5)\n" \
  "v_readfirstlane_b32 s85, v23\ns_cmp_lt_u32 s85, 3\ns_cbranch_scc1 51f\n52:\nv_mfma_f32_16x16x32_f16 v[16:19], v[88:91], v[116:119], v[16:19]\n" \
  "ds_read_b64_tr_b16 v[100:101], v5 offset:0\nds_read_b64_tr_b16 v[102:103], v5 offset:32\n" \
  "v_mfma_f32_16x16x32_f16 v[16:19], v[84:87], v[24:27], v[16:19]\nds_read_b64_tr_b16 v[36:37], v5 offset:256\n" \
  "ds_read_b64_tr_b16 v[38:39], v5 offset:288\nv_mfma_f32_16x16x32_f16 v[16:19], v[80:83], v[112:115], v[16:19]\n" \
  "ds_read_b64_tr_b16 v[96:97], v5 offset:512\nds_read_b64_tr_b16 v[98:99], v5 offset:544\ns_waitcnt lgkmcnt(9)\n" \
  "v_mfma_f32_16x16x32_f16 v[16:19], v[76:79], v[28:31], v[16:19]\nds_read_b64_tr_b16 v[120:121], v5 offset:768\n" \
  "ds_read_b64_tr_b16 v[122:123], v5 offset:800\nv_mfma_f32_16x16x32_f16 v[16:19], v[72:75], v[108:111], v[16:19]\ns_waitcnt lgkmcnt(9)\n" \
  "v_mfma_f32_16x16x32_f16 v[16:19], v[68:71], v[32:35], v[16:19]\nv_mfma_f32_16x16x32_f16 v[16:19], v[64:67], v[104:107], v[16:19]\n" \
  "s_waitcnt lgkmcnt(6)\nv_mfma_f32_16x16x32_f16 v[16:19], v[60:63], v[124:127], v[16:19]\n" \
  "v_mfma_f32_16x16x32_f16 v[12:15], v[100:103], v[56:59], v[12:15]\nv_mfma_f32_16x16x32_f16 v[16:19], v[100:103], v[52:55], v[16:19]\n" \
  "s_waitcnt lgkmcnt(2)\nv_mfma_f32_16x16x32_f16 v[12:15], v[36:39], v[48:51], v[12:15]\n" \
  "v_mfma_f32_16x16x32_f16 v[16:19], v[36:39], v[44:47], v[16:19]\nv_mfma_f32_16x16x32_f16 v[16:19], v[96:99], v[56:59], v[16:19]\n" \
  "s_waitcnt lgkmcnt(0)\nv_mfma_f32_16x16x32_f16 v[16:19], v[120:123], v[48:51], v[16:19]\ns_nop 3\nv_cvt_pkrtz_f16_f32 v20, v12, v13\n" \
  "v_cvt_pkrtz_f16_f32 v21, v14, v15\nv_pk_add_u16 v20, v20, 4 op_sel_hi:[1,0] clamp\nv_pk_add_u16 v21, v21, 4 op_sel_hi:[1,0] clamp\n" \
  "ds_write_b64 v4, v[20:21] offset:2048\nv_cvt_pkrtz_f16_f32 v20, v16, v17\nv_cvt_pkrtz_f16_f32 v21, v18, v19\n" \
  "v_pk_add_u16 v20, v20, 4 op_sel_hi:[1,0] clamp\nv_pk_add_u16 v21, v21, 4 op_sel_hi:[1,0] clamp\nds_write_b64 v4, v[20:21] offset:3072\n" \
  "s_setprio 0\ns_setpc_b64 s[90:91]\n51:\ns_movk_i32 s84, 0x4000\n53:\nds_read_b32 v23, v9 offset:32\ns_sub_u32 s84, s84, 1\ns_cbranch_scc1 52b\n" \
  "s_waitcnt lgkmcnt(0)\nv_readfirstlane_b32 s85, v23\ns_cmp_lt_u32 s85, 3\ns_cbranch_scc1 53b\ns_branch 52b\n4:\n" \
  "ds_read_b64 v[124:125], v3 offset:1088\nds_read_b64 v[126:127], v3 offset:1096\nds_read_b64 v[120:121], v3 offset:0\n" \
  "ds_read_b64 v[122:123], v3 offset:8\nds_read_b64 v[116:117], v3 offset:64\nds_read_b64 v[118:119], v3 offset:72\n" \
  "ds_read_b64 v[112:113], v3 offset:128\nds_read_b64 v[114:115], v3 offset:136\nds_read_b64 v[108:109], v3 offset:192\n" \
  "ds_read_b64 v[110:111], v3 offset:200\nds_read_b64 v[104:105], v3 offset:256\nds_read_b64 v[106:107], v3 offset:264\n" \
  "ds_read_b64 v[100:101], v3 offset:320\nds_read_b64 v[102:103], v3 offset:328\nds_read_b64 v[96:97], v3 offset:384\n" \
  "ds_read_b64 v[98:99], v3 offset:392\nds_read_b64 v[92:93], v3 offset:448\nds_read_b64 v[94:95], v3 offset:456\n" \
  "ds_read_b64 v[88:89], v3 offset:512\nds_read_b64 v[90:91], v3 offset:520\nds_read_b64 v[84:85], v3 offset:576\n" \
  "ds_read_b64 v[86:87], v3 offset:584\nds_read_b64 v[80:81], v3 offset:640\nds_read_b64 v[82:83], v3 offset:648\n" \
  "ds_read_b64 v[76:77], v3 offset:704\nds_read_b64 v[78:79], v3 offset:712\nds_read_b64 v[72:73], v3 offset:768\n" \
  "ds_read_b64 v[74:75], v3 offset:776\nds_read_b64 v[68:69], v3 offset:832\nds_read_b64 v[70:71], v3 offset:840\n" \
  "ds_read_b64 v[64:65], v3 offset:896\nds_read_b64 v[66:67], v3 offset:904\nds_read_b64 v[60:61], v3 offset:960\n" \
  "ds_read_b64 v[62:63], v3 offset:968\nds_read_b64 v[56:57], v3 offset:1024\nds_read_b64 v[58:59], v3 offset:1032\n" \
  "ds_read_b64 v[52:53], v6 offset:0\nds_read_b64 v[54:55], v6 offset:8\nds_read_b64 v[48:49], v6 offset:64\nds_read_b64 v[50:51], v6 offset:72\n" \
  "ds_read_b64 v[44:45], v6 offset:128\nds_read_b64 v[46:47], v6 offset:136\nds_read_b64 v[40:41], v6 offset:192\n" \
  "ds_read_b64 v[42:43], v6 offset:200\ns_waitcnt lgkmcnt(0)\ns_barrier\nds_read_b128 v[24:27], v2 offset:448\nds_read_b128 v[28:31], v2 offset:960\n" \
  "ds_read_b128 v[32:35], v2 offset:1472\nds_read_b128 v[36:39], v2 offset:1536\ns_waitcnt lgkmcnt(3)\n" \
  "v_mfma_f32_16x16x32_f16 v[12:15], v[124:127], v[24:27], 0\ns_setprio 1\nds_read_b128 v[24:27], v2 offset:1600\ns_waitcnt lgkmcnt(3)\n" \
  "v_mfma_f32_16x16x32_f16 v[16:19], v[124:127], v[28:31], 0\nds_read_b128 v[124:127], v2 offset:1664\ns_waitcnt lgkmcnt(3)\n" \
  "v_mfma_f32_16x16x32_f16 v[12:15], v[120:123], v[32:35], v[12:15]\nds_read_b128 v[28:31], v2 offset:1728\ns_waitcnt lgkmcnt(3)\n" \
  "v_mfma_f32_16x16x32_f16 v[12:15], v[116:119], v[36:39], v[12:15]\nds_read_b128 v[32:35], v2 offset:1792\ns_waitcnt lgkmcnt(3)\n" \
  "v_mfma_f32_16x16x32_f16 v[12:15], v[112:115], v[24:27], v[12:15]\nds_read_b128 v[36:39], v2 offset:1856\ns_waitcnt lgkmcnt(3)\n" \
  "v_mfma_f32_16x16x32_f16 v[12:15], v[108:111], v[124:127], v[12:15]\nds_read_b128 v[24:27], v2 offset:1920\ns_waitcnt lgkmcnt(3)\n" \
  "v_mfma_f32_16x16x32_f16 v[12:15], v[104:107], v[28:31], v[12:15]\nds_read_b128 v[124:127], v2 offset:1984\ns_waitcnt lgkmcnt(3)\n" \
  "v_mfma_f32_16x16x32_f16 v[12:15], v[100:103], v[32:35], v[12:15]\nds_read_b128 v[28:31], v2 offset:2048\ns_waitcnt lgkmcnt(3)\n" \
  "v_mfma_f32_16x16x32_f16 v[12:15], v[96:99], v[36:39], v[12:15]\nds_read_b128 v[32:35], v2 offset:2112\ns_waitcnt lgkmcnt(3)\n" \
  "v_mfma_f32_16x16x32_f16 v[12:15], v[92:95], v[24:27], v[12:15]\nds_read_b128 v[36:39], v2 offset:2176\ns_waitcnt lgkmcnt(3)\n" \
  "v_mfma_f32_16x16x32_f16 v[12:15], v[88:91], v[124:127], v[12:15]\nds_read_b128 v[24:27], v2 offset:2240\n" \
  "v_mfma_f32_16x16x32_f16 v[16:19], v[120:123], v[124:127], v[16:19]\nds_read_b64 v[120:121], v6 offset:256\nds_read_b64 v[122:123], v6 offset:264\n" \
  "s_waitcnt lgkmcnt(4)\nv_mfma_f32_16x16x32_f16 v[12:15], v[84:87], v[28:31], v[12:15]\nds_read_b128 v[124:127], v2 offset:2304\n" \
  "v_mfma_f32_16x16x32_f16 v[16:19], v[116:119], v[28:31], v[16:19]\nds_read_b64 v[116:117], v6 offset:320\nds_read_b64 v[118:119], v6 offset:328\n" \
  "v_mfma_f32_16x16x32_f16 v[12:15], v[80:83], v[32:35], v[12:15]\nds_read_b128 v[28:31], v2 offset:2368\n" \
  "v_mfma_f32_16x16x32_f16 v[16:19], v[112:115], v[32:35], v[16:19]\ns_waitcnt lgkmcnt(6)\n" \
  "v_mfma_f32_16x16x32_f16 v[12:15], v[76:79], v[36:39], v[12:15]\nds_read_b128 v[112:115], v2 offset:2432\n" \
  "v_mfma_f32_16x16x32_f16 v[16:19], v[108:111], v[36:39], v[16:19]\nds_read2_b32 v[32:33], v8 offset0:48 offset1:49\n" \
  "ds_read2_b32 v[34:35], v8 offset0:50 offset1:51\nv_mfma_f32_16x16x32_f16 v[12:15], v[72:75], v[24:27], v[12:15]\n" \
  "ds_read_b128 v[108:111], v2 offset:2496\nv_mfma_f32_16x16x32_f16 v[16:19], v[104:107], v[24:27], v[16:19]\n" \
  "ds_read2_b32 v[36:37], v8 offset0:32 offset1:33\nds_read2_b32 v[38:39], v8 offset0:34 offset1:35\ns_waitcnt lgkmcnt(6)\n" \
  "v_mfma_f32_16x16x32_f16 v[12:15], v[68:71], v[124:127], v[12:15]\nds_read_b128 v[104:107], v2 offset:2560\n" \
  "v_mfma_f32_16x16x32_f16 v[16:19], v[100:103], v[124:127], v[16:19]\nds_read_b128 v[24:27], v2 offset:2624\n" \
  "v_mfma_f32_16x16x32_f16 v[12:15], v[64:67], v[28:31], v[12:15]\nds_read_b128 v[100:103], v2 offset:2688\nds_read_b32 v23, v9 offset:32\n" \
  "v_mfma_f32_16x16x32_f16 v[16:19], v[96:99], v[28:31], v[16:19]\nds_read_b128 v[124:127], v2 offset:2752\ns_waitcnt lgkmcnt(7)\n" \
  "v_mfma_f32_16x16x32_f16 v[12:15], v[60:63], v[112:115], v[12:15]\nds_read_b128 v[96:99], v2 offset:2816\n" \
  "v_mfma_f32_16x16x32_f16 v[16:19], v[92:95], v[112:115], v[16:19]\nds_read_b128 v[28:31], v2 offset:2880\n" \
  "v_mfma_f32_16x16x32_f16 v[12:15], v[56:59], v[108:111], v[12:15]\nds_read_b128 v[92:95], v2 offset:2944\n" \
  "v_mfma_f32_16x16x32_f16 v[16:19], v[88:91], v[108:111], v[16:19]\nds_read_b128 v[112:115], v2 offset:3008\ns_waitcnt lgkmcnt(5)\n" \
  "v_readfirstlane_b32 s85, v23\ns_cmp_lt_u32 s85, 3\ns_cbranch_scc1 61f\n62:\nv_mfma_f32_16x16x32_f16 v[16:19], v[84:87], v[104:107], v[16:19]\n" \
  "ds_read_b64_tr_b16 v[88:89], v5 offset:0\nds_read_b64_tr_b16 v[90:91], v5 offset:32\n" \
  "v_mfma_f32_16x16x32_f16 v[16:19], v[80:83], v[24:27], v[16:19]\nds_read_b64_tr_b16 v[108:109], v5 offset:256\n" \
  "ds_read_b64_tr_b16 v[110:111], v5 offset:288\nv_mfma_f32_16x16x32_f16 v[16:19], v[76:79], v[100:103], v[16:19]\n" \
  "ds_read_b64_tr_b16 v[84:85], v5 offset:512\nds_read_b64_tr_b16 v[86:87], v5 offset:544\ns_waitcnt lgkmcnt(9)\n" \
  "v_mfma_f32_16x16x32_f16 v[16:19], v[72:75], v[124:127], v[16:19]\nds_read_b64_tr_b16 v[104:105], v5 offset:768\n" \
  "ds_read_b64_tr_b16 v[106:107], v5 offset:800\nv_mfma_f32_16x16x32_f16 v[16:19], v[68:71], v[96:99], v[16:19]\n" \
  "ds_read_b64_tr_b16 v[80:81], v5 offset:1024\nds_read_b64_tr_b16 v[82:83], v5 offset:1056\ns_waitcnt lgkmcnt(11)\n" \
  "v_mfma_f32_16x16x32_f16 v[16:19], v[64:67], v[28:31], v[16:19]\nds_read_b64_tr_b16 v[24:25], v5 offset:1280\n" \
  "ds_read_b64_tr_b16 v[26:27], v5 offset:1312\nv_mfma_f32_16x16x32_f16 v[16:19], v[60:63], v[92:95], v[16:19]\n" \
  "ds_read_b64_tr_b16 v[76:77], v5 offset:1536\nds_read_b64_tr_b16 v[78:79], v5 offset:1568\ns_waitcnt lgkmcnt(12)\n" \
  "v_mfma_f32_16x16x32_f16 v[16:19], v[56:59], v[112:115], v[16:19]\nds_read_b64_tr_b16 v[100:101], v5 offset:1792\n" \
  "ds_read_b64_tr_b16 v[102:103], v5 offset:1824\nv_mfma_f32_16x16x32_f16 v[12:15], v[88:91], v[52:55], v[12:15]\ns_waitcnt lgkmcnt(10)\n" \
  "v_mfma_f32_16x16x32_f16 v[12:15], v[108:111], v[48:51], v[12:15]\nds_read_b64_tr_b16 v[72:73], v7 offset:0\n" \
  "ds_read_b64_tr_b16 v[74:75], v7 offset:64\nv_mfma_f32_16x16x32_f16 v[12:15], v[84:87], v[44:47], v[12:15]\n" \
  "ds_read_b64_tr_b16 v[124:125], v7 offset:512\nds_read_b64_tr_b16 v[126:127], v7 offset:576\n" \
  "v_mfma_f32_16x16x32_f16 v[16:19], v[84:87], v[52:55], v[16:19]\ns_waitcnt lgkmcnt(10)\n" \
  "v_mfma_f32_16x16x32_f16 v[12:15], v[104:107], v[40:43], v[12:15]\nv_mfma_f32_16x16x32_f16 v[16:19], v[104:107], v[48:51], v[16:19]\n" \
  "v_mfma_f32_16x16x32_f16 v[12:15], v[80:83], v[120:123], v[12:15]\nv_mfma_f32_16x16x32_f16 v[16:19], v[80:83], v[44:47], v[16:19]\n" \
  "s_waitcnt lgkmcnt(6)\nv_mfma_f32_16x16x32_f16 v[12:15], v[24:27], v[116:119], v[12:15]\n" \
  "v_mfma_f32_16x16x32_f16 v[16:19], v[24:27], v[40:43], v[16:19]\nv_mfma_f32_16x16x32_f16 v[16:19], v[76:79], v[120:123], v[16:19]\n" \
  "s_waitcnt lgkmcnt(2)\nv_mfma_f32_16x16x32_f16 v[16:19], v[100:103], v[116:119], v[16:19]\n" \
  "v_mfma_f32_16x16x32_f16 v[12:15], v[72:75], v[32:35], v[12:15]\nv_mfma_f32_16x16x32_f16 v[16:19], v[72:75], v[36:39], v[16:19]\n" \
  "s_waitcnt lgkmcnt(0)\nv_mfma_f32_16x16x32_f16 v[16:19], v[124:127], v[32:35], v[16:19]\ns_nop 4\nv_cvt_pkrtz_f16_f32 v20, v12, v13\n" \
  "v_cvt_pkrtz_f16_f32 v21, v14, v15\nv_pk_add_u16 v20, v20, 4 op_sel_hi:[1,0] clamp\nv_pk_add_u16 v21, v21, 4 op_sel_hi:[1,0] clamp\n" \
  "ds_write_b64 v4, v[20:21] offset:4096\nv_cvt_pkrtz_f16_f32 v20, v16, v17\nv_cvt_pkrtz_f16_f32 v21, v18, v19\n" \
  "v_pk_add_u16 v20, v20, 4 op_sel_hi:[1,0] clamp\nv_pk_add_u16 v21, v21, 4 op_sel_hi:[1,0] clamp\nds_write_b64 v4, v[20:21] offset:5120\n" \
  "s_setprio 0\ns_setpc_b64 s[90:91]\n61:\ns_movk_i32 s84, 0x4000\n63:\nds_read_b32 v23, v9 offset:32\ns_sub_u32 s84, s84, 1\ns_cbranch_scc1 62b\n" \
  "s_waitcnt lgkmcnt(0)\nv_readfirstlane_b32 s85, v23\ns_cmp_lt_u32 s85, 3\ns_cbranch_scc1 63b\ns_branch 62b\n5:\n" \
  "ds_read_b64 v[124:125], v3 offset:1088\nds_read_b64 v[126:127], v3 offset:1096\nds_read_b64 v[120:121], v3 offset:0\n" \
  "ds_read_b64 v[122:123], v3 offset:8\nds_read_b64 v[116:117], v3 offset:64\nds_read_b64 v[118:119], v3 offset:72\n" \
  "ds_read_b64 v[112:113], v3 offset:128\nds_read_b64 v[114:115], v3 offset:136\nds_read_b64 v[108:109], v3 offset:192\n" \
  "ds_read_b64 v[110:111], v3 offset:200\nds_read_b64 v[104:105], v3 offset:256\nds_read_b64 v[106:107], v3 offset:264\n" \
  "ds_read_b64 v[100:101], v3 offset:320\nds_read_b64 v[102:103], v3 offset:328\nds_read_b64 v[96:97], v3 offset:384\n" \
  "ds_read_b64 v[98:99], v3 offset:392\nds_read_b64 v[92:93], v3 offset:448\nds_read_b64 v[94:95], v3 offset:456\n" \
  "ds_read_b64 v[88:89], v3 offset:512\nds_read_b64 v[90:91], v3 offset:520\nds_read_b64 v[84:85], v3 offset:576\n" \
  "ds_read_b64 v[86:87], v3 offset:584\nds_read_b64 v[80:81], v3 offset:640\nds_read_b64 v[82:83], v3 offset:648\n" \
  "ds_read_b64 v[76:77], v3 offset:704\nds_read_b64 v[78:79], v3 offset:712\nds_read_b64 v[72:73], v3 offset:768\n" \
  "ds_read_b64 v[74:75], v3 offset:776\nds_read_b64 v[68:69], v3 offset:832\nds_read_b64 v[70:71], v3 offset:840\n" \
  "ds_read_b64 v[64:65], v3 offset:896\nds_read_b64 v[66:67], v3 offset:904\nds_read_b64 v[60:61], v3 offset:960\n" \
  "ds_read_b64 v[62:63], v3 offset:968\nds_read_b64 v[56:57], v3 offset:1024\nds_read_b64 v[58:59], v3 offset:1032\n" \
  "ds_read_b64 v[52:53], v6 offset:0\nds_read_b64 v[54:55], v6 offset:8\nds_read_b64 v[48:49], v6 offset:64\nds_read_b64 v[50:51], v6 offset:72\n" \
  "ds_read_b64 v[44:45], v6 offset:128\nds_read_b64 v[46:47], v6 offset:136\nds_read_b64 v[40:41], v6 offset:192\n" \
  "ds_read_b64 v[42:43], v6 offset:200\ns_waitcnt lgkmcnt(0)\ns_barrier\nds_read_b128 v[24:27], v2 offset:1472\n" \
  "ds_read_b128 v[28:31], v2 offset:1984\nds_read_b128 v[32:35], v2 offset:2496\nds_read_b128 v[36:39], v2 offset:2560\ns_waitcnt lgkmcnt(3)\n" \
  "v_mfma_f32_16x16x32_f16 v[12:15], v[124:127], v[24:27], 0\ns_setprio 1\nds_read_b128 v[24:27], v2 offset:2624\ns_waitcnt lgkmcnt(3)\n" \
  "v_mfma_f32_16x16x32_f16 v[16:19], v[124:127], v[28:31], 0\nds_read_b128 v[124:127], v2 offset:2688\ns_waitcnt lgkmcnt(3)\n" \
  "v_mfma_f32_16x16x32_f16 v[12:15], v[120:123], v[32:35], v[12:15]\nds_read_b128 v[28:31], v2 offset:2752\ns_waitcnt lgkmcnt(3)\n" \
  "v_mfma_f32_16x16x32_f16 v[12:15], v[116:119], v[36:39], v[12:15]\nds_read_b128 v[32:35], v2 offset:2816\ns_waitcnt lgkmcnt(3)\n" \
  "v_mfma_f32_16x16x32_f16 v[12:15], v[112:115], v[24:27], v[12:15]\nds_read_b128 v[36:39], v2 offset:2880\ns_waitcnt lgkmcnt(3)\n" \
  "v_mfma_f32_16x16x32_f16 v[12:15], v[108:111], v[124:127], v[12:15]\nds_read_b128 v[24:27], v2 offset:2944\ns_waitcnt lgkmcnt(3)\n" \
  "v_mfma_f32_16x16x32_f16 v[12:15], v[104:107], v[28:31], v[12:15]\nds_read_b128 v[124:127], v2 offset:3008\ns_waitcnt lgkmcnt(3)\n" \
  "v_mfma_f32_16x16x32_f16 v[12:15], v[100:103], v[32:35], v[12:15]\nds_read_b128 v[28:31], v2 offset:3072\ns_waitcnt lgkmcnt(3)\n" \
  "v_mfma_f32_16x16x32_f16 v[12:15], v[96:99], v[36:39], v[12:15]\nds_read_b128 v[32:35], v2 offset:3136\ns_waitcnt lgkmcnt(3)\n" \
  "v_mfma_f32_16x16x32_f16 v[12:15], v[92:95], v[24:27], v[12:15]\nds_read_b128 v[36:39], v2 offset:3200\ns_waitcnt lgkmcnt(3)\n" \
  "v_mfma_f32_16x16x32_f16 v[12:15], v[88:91], v[124:127], v[12:15]\nds_read_b128 v[24:27], v2 offset:3264\n" \
  "v_mfma_f32_16x16x32_f16 v[16:19], v[120:123], v[124:127], v[16:19]\nds_read_b64 v[120:121], v6 offset:256\nds_read_b64 v[122:123], v6 offset:264\n" \
  "s_waitcnt lgkmcnt(4)\nv_mfma_f32_16x16x32_f16 v[12:15], v[84:87], v[28:31], v[12:15]\nds_read_b128 v[124:127], v2 offset:3328\n" \
  "v_mfma_f32_16x16x32_f16 v[16:19], v[116:119], v[28:31], v[16:19]\nds_read_b64 v[116:117], v6 offset:320\nds_read_b64 v[118:119], v6 offset:328\n" \
  "v_mfma_f32_16x16x32_f16 v[12:15], v[80:83], v[32:35], v[12:15]\nds_read_b128 v[28:31], v2 offset:3392\n" \
  "v_mfma_f32_16x16x32_f16 v[16:19], v[112:115], v[32:35], v[16:19]\ns_waitcnt lgkmcnt(6)\n" \
  "v_mfma_f32_16x16x32_f16 v[12:15], v[76:79], v[36:39], v[12:15]\nds_read_b128 v[112:115], v2 offset:3456\n" \
  "v_mfma_f32_16x16x32_f16 v[16:19], v[108:111], v[36:39], v[16:19]\nds_read2_b32 v[32:33], v8 offset0:16 offset1:17\n" \
  "ds_read2_b32 v[34:35], v8 offset0:18 offset1:19\nv_mfma_f32_16x16x32_f16 v[12:15], v[72:75], v[24:27], v[12:15]\n" \
  "ds_read_b128 v[108:111], v2 offset:3520\nv_mfma_f32_16x16x32_f16 v[16:19], v[104:107], v[24:27], v[16:19]\n" \
  "ds_read2_b32 v[36:37], v8 offset0:0 offset1:1\nds_read2_b32 v[38:39], v8 offset0:2 offset1:3\ns_waitcnt lgkmcnt(6)\n" \
  "v_mfma_f32_16x16x32_f16 v[12:15], v[68:71], v[124:127], v[12:15]\nds_read_b128 v[104:107], v2 offset:3584\n" \
  "v_mfma_f32_16x16x32_f16 v[16:19], v[100:103], v[124:127], v[16:19]\nds_read_b128 v[24:27], v2 offset:3648\n" \
  "ds_read2_b32 v[100:101], v8 offset0:32 offset1:33\nds_read2_b32 v[102:103], v8 offset0:34 offset1:35\n" \
  "v_mfma_f32_16x16x32_f16 v[12:15], v[64:67], v[28:31], v[12:15]\nds_read_b128 v[124:127], v2 offset:3712\nds_read_b32 v23, v9 offset:32\n" \
  "v_mfma_f32_16x16x32_f16 v[16:19], v[96:99], v[28:31], v[16:19]\nds_read_b128 v[96:99], v2 offset:3776\ns_waitcnt lgkmcnt(9)\n" \
  "v_mfma_f32_16x16x32_f16 v[12:15], v[60:63], v[112:115], v[12:15]\nds_read_b128 v[28:31], v2 offset:3840\n" \
  "v_mfma_f32_16x16x32_f16 v[16:19], v[92:95], v[112:115], v[16:19]\nds_read_b128 v[92:95], v2 offset:3904\n" \
  "v_mfma_f32_16x16x32_f16 v[12:15], v[56:59], v[108:111], v[12:15]\nds_read_b128 v[112:115], v2 offset:3968\n" \
  "v_mfma_f32_16x16x32_f16 v[16:19], v[88:91], v[108:111], v[16:19]\nds_read_b128 v[88:91], v2 offset:4032\ns_waitcnt lgkmcnt(5)\n" \
  "v_readfirstlane_b32 s85, v23\ns_cmp_lt_u32 s85, 3\ns_cbranch_scc1 71f\n72:\nv_mfma_f32_16x16x32_f16 v[16:19], v[84:87], v[104:107], v[16:19]\n" \
  "ds_read_b64_tr_b16 v[108:109], v5 offset:1024\nds_read_b64_tr_b16 v[110:111], v5 offset:1056\n" \
  "v_mfma_f32_16x16x32_f16 v[16:19], v[80:83], v[24:27], v[16:19]\nds_read_b64_tr_b16 v[84:85], v5 offset:1280\n" \
  "ds_read_b64_tr_b16 v[86:87], v5 offset:1312\nv_mfma_f32_16x16x32_f16 v[16:19], v[76:79], v[124:127], v[16:19]\n" \
  "ds_read_b64_tr_b16 v[104:105], v5 offset:1536\nds_read_b64_tr_b16 v[106:107], v5 offset:1568\ns_waitcnt lgkmcnt(9)\n" \
  "v_mfma_f32_16x16x32_f16 v[16:19], v[72:75], v[96:99], v[16:19]\nds_read_b64_tr_b16 v[80:81], v5 offset:1792\n" \
  "ds_read_b64_tr_b16 v[82:83], v5 offset:1824\nv_mfma_f32_16x16x32_f16 v[16:19], v[68:71], v[28:31], v[16:19]\n" \
  "ds_read_b64_tr_b16 v[24:25], v5 offset:2048\nds_read_b64_tr_b16 v[26:27], v5 offset:2080\ns_waitcnt lgkmcnt(11)\n" \
  "v_mfma_f32_16x16x32_f16 v[16:19], v[64:67], v[92:95], v[16:19]\nds_read_b64_tr_b16 v[76:77], v5 offset:2304\n" \
  "ds_read_b64_tr_b16 v[78:79], v5 offset:2336\nv_mfma_f32_16x16x32_f16 v[16:19], v[60:63], v[112:115], v[16:19]\n" \
  "ds_read_b64_tr_b16 v[124:125], v5 offset:2560\nds_read_b64_tr_b16 v[126:127], v5 offset:2592\ns_waitcnt lgkmcnt(12)\n" \
  "v_mfma_f32_16x16x32_f16 v[16:19], v[56:59], v[88:91], v[16:19]\nds_read_b64_tr_b16 v[72:73], v5 offset:2816\n" \
  "ds_read_b64_tr_b16 v[74:75], v5 offset:2848\nv_mfma_f32_16x16x32_f16 v[12:15], v[108:111], v[52:55], v[12:15]\ns_waitcnt lgkmcnt(10)\n" \
  "v_mfma_f32_16x16x32_f16 v[12:15], v[84:87], v[48:51], v[12:15]\nds_read_b64_tr_b16 v[96:97], v7 offset:0\n" \
  "ds_read_b64_tr_b16 v[98:99], v7 offset:64\nv_mfma_f32_16x16x32_f16 v[12:15], v[104:107], v[44:47], v[12:15]\n" \
  "ds_read_b64_tr_b16 v[68:69], v7 offset:512\nds_read_b64_tr_b16 v[70:71], v7 offset:576\n" \
  "v_mfma_f32_16x16x32_f16 v[16:19], v[104:107], v[52:55], v[16:19]\ns_waitcnt lgkmcnt(10)\n" \
  "v_mfma_f32_16x16x32_f16 v[12:15], v[80:83], v[40:43], v[12:15]\nds_read_b64_tr_b16 v[28:29], v7 offset:1024\n" \
  "ds_read_b64_tr_b16 v[30:31], v7 offset:1088\nv_mfma_f32_16x16x32_f16 v[16:19], v[80:83], v[48:51], v[16:19]\n" \
  "ds_read2_b32 v[64:65], v8 offset0:48 offset1:49\nds_read2_b32 v[66:67], v8 offset0:50 offset1:51\n" \
  "v_mfma_f32_16x16x32_f16 v[12:15], v[24:27], v[120:123], v[12:15]\nv_mfma_f32_16x16x32_f16 v[16:19], v[24:27], v[44:47], v[16:19]\n" \
  "s_waitcnt lgkmcnt(10)\nv_mfma_f32_16x16x32_f16 v[12:15], v[76:79], v[116:119], v[12:15]\nds_read_b64_tr_b16 v[92:93], v7 offset:1536\n" \
  "ds_read_b64_tr_b16 v[94:95], v7 offset:1600\nv_mfma_f32_16x16x32_f16 v[16:19], v[76:79], v[40:43], v[16:19]\n" \
  "v_mfma_f32_16x16x32_f16 v[16:19], v[124:127], v[120:123], v[16:19]\ns_waitcnt lgkmcnt(8)\n" \
  "v_mfma_f32_16x16x32_f16 v[16:19], v[72:75], v[116:119], v[16:19]\nv_mfma_f32_16x16x32_f16 v[12:15], v[96:99], v[32:35], v[12:15]\n" \
  "v_mfma_f32_16x16x32_f16 v[16:19], v[96:99], v[36:39], v[16:19]\ns_waitcnt lgkmcnt(4)\n" \
  "v_mfma_f32_16x16x32_f16 v[12:15], v[68:71], v[100:103], v[12:15]\nv_mfma_f32_16x16x32_f16 v[16:19], v[68:71], v[32:35], v[16:19]\n" \
  "s_waitcnt lgkmcnt(2)\nv_mfma_f32_16x16x32_f16 v[12:15], v[28:31], v[64:67], v[12:15]\n" \
  "v_mfma_f32_16x16x32_f16 v[16:19], v[28:31], v[100:103], v[16:19]\ns_waitcnt lgkmcnt(0)\n" \
  "v_mfma_f32_16x16x32_f16 v[16:19], v[92:95], v[64:67], v[16:19]\ns_nop 4\nv_cvt_pkrtz_f16_f32 v20, v12, v13\nv_cvt_pkrtz_f16_f32 v21, v14, v15\n" \
  "v_pk_add_u16 v20, v20, 4 op_sel_hi:[1,0] clamp\nv_pk_add_u16 v21, v21, 4 op_sel_hi:[1,0] clamp\nds_write_b64 v4, v[20:21] offset:6144\n" \
  "v_cvt_pkrtz_f16_f32 v20, v16, v17\nv_cvt_pkrtz_f16_f32 v21, v18, v19\nv_pk_add_u16 v20, v20, 4 op_sel_hi:[1,0] clamp\n" \
  "v_pk_add_u16 v21, v21, 4 op_sel_hi:[1,0] clamp\nds_write_b64 v4, v[20:21] offset:7168\ns_setprio 0\ns_setpc_b64 s[90:91]\n71:\n" \
  "s_movk_i32 s84, 0x4000\n73:\nds_read_b32 v23, v9 offset:32\ns_sub_u32 s84, s84, 1\ns_cbranch_scc1 72b\ns_waitcnt lgkmcnt(0)\n" \
  "v_readfirstlane_b32 s85, v23\ns_cmp_lt_u32 s85, 3\ns_cbranch_scc1 73b\ns_branch 72b\n"
#define CONV_CLOB "memory", "scc", "s84", "s85", "s90", "s91", "v12", "v13", "v14", "v15", "v16", "v17", "v18", "v19", "v20", "v21", "v22", "v23", "v24", "v25", "v26", "v27", "v28", "v29", "v30", "v31", "v32", "v33", "v34", "v35", "v36", "v37", "v38", "v39", "v40", "v41", "v42", "v43", "v44", "v45", "v46", "v47", "v48", "v49", "v50", "v51", "v52", "v53", "v54", "v55", "v56", "v57", "v58", "v59", "v60", "v61", "v62", "v63", "v64", "v65", "v66", "v67", "v68", "v69", "v70", "v71", "v72", "v73", "v74", "v75", "v76", "v77", "v78", "v79", "v80", "v81", "v82", "v83", "v84", "v85", "v86", "v87", "v88", "v89", "v90", "v91", "v92", "v93", "v94", "v95", "v96", "v97", "v98", "v99", "v100", "v101", "v102", "v103", "v104", "v105", "v106", "v107", "v108", "v109", "v110", "v111", "v112", "v113", "v114", "v115", "v116", "v117", "v118", "v119", "v120", "v121", "v122", "v123", "v124", "v125", "v126", "v127"
constexpr int TDP = 1216, T_3 = 4 * TDP, T_4 = T_3 + 512, TB = T_4 + 320;
constexpr int C_U = 480, UB = C_U + 4096 + 96, C_G5 = 0, C_G3 = 3200, C_G9 = 6400, C_G7 = C_G9 + 2096, GB = C_G7 + 2080 + 112;
constexpr int O_FL = 2 * TB, O_UL = O_FL + 64, O_IM = O_UL + 2 * UB, O_ST = O_IM + 2 * GB;
constexpr int X_TB = 10240, X_UL = X_TB + TB, X_IM = X_UL + UB, X_ST = X_IM + GB;
constexpr int X_WR = X_IM + 128 * IP;
#define X_RW(w) (LW + 9216 * (2 + ((w) >> 1)) + 2560 * ((w) & 1))
constexpr int RP = 1216;
constexpr int LW = 36864 + 512, LA = LW, LM = LW + 36864;
constexpr int L_SC = LM + 2048, SMEM = O_IM + 256 * IP;
static_assert(L_SC + 1152 <= SMEM && O_ST + 8192 <= SMEM && X_IM + 128 * IP <= LM && X_ST + 8192 <= LM && UB % 128 == 64 && GB % 128 == 64 && SMEM <= 79872, "");
struct TOps { uint32_t xa, xb, ya, pd, pf, pg, pe; };
__device__ __forceinline__ void table_ops(uint32_t im, uint32_t tb, int ct, int lane, TOps& t) {
  const int j = lane & 15, kq = lane >> 4, q4 = (lane >> 2) & 3, p4 = lane & 3, jj = j + 16 * ct;
  const uint32_t sa = im + (uint32_t)(IP * (lane >> 2));
  t.xa = sa + (uint32_t)(I_X + 8 * p4); t.xb = sa + (uint32_t)(I_X + 32 + 8 * (p4 & 1)); t.ya = sa + (uint32_t)(32 * ct + 8 * p4);
  t.pd = tb + (uint32_t)(2 * (541 - (8 * jj + 4 * (kq & 1) + 256 * (kq >> 1))));
  t.pf = tb + (uint32_t)((kq & 1) ? T_4 + 2 * (63 - jj) : T_3 + 2 * (127 - 2 * jj));
  t.pg = t.pf - ((kq & 1) ? 64 : 2);
  t.pe = tb + (uint32_t)(p4 * (TDP + 2) + ((lane < 12) ? 2 * (32 - q4) : 2 * (572 + (q4 & 1))));
  asm volatile("" : "+v"(t.xa), "+v"(t.xb), "+v"(t.ya), "+v"(t.pd), "+v"(t.pf), "+v"(t.pg), "+v"(t.pe));
}
typedef __attribute__((address_space(3))) char* lptr;
#define LP(T, a) ((__attribute__((address_space(3))) T*)(uintptr_t)(a))
__device__ __forceinline__ uint32_t table_tile(const TOps& t, int ct, int lane, uint32_t evp, float dsk TSP) {
  u32x4 ev;
  f32x4 ad[2], af[2];
  asm volatile(T_ASM : "=&v"(ad[0]), "=&v"(ad[1]), "=&v"(af[0]), "=&v"(af[1]), "=&v"(ev) : "v"(t.xa), "v"(t.xb), "v"(t.ya), "v"(evp) : T_CLOB);
  TS(0, 4);
  const int e0 = (int)(ev[0] + ev[2]), e1 = (int)(ev[1] + ev[3]);
  const int em = min(max(max(max(e0, e1), (int)((__float_as_uint(dsk) >> 23) & 0xffu) - 133), -110), 115);
  const int ts = ((uint32_t)(em + 12) <= 8u) ? 1 : -3 - em;
  float s0 = __builtin_amdgcn_ldexpf(1.f, e0 - 29 + ts), s1 = __builtin_amdgcn_ldexpf(1.f, e1 - 29 + ts);
  float d0 = (ct == 0 && lane == 0) ? __builtin_amdgcn_ldexpf(dsk, ts) : 0.f;
  asm volatile("s_nop 1" : "+v"(ad[0]), "+v"(ad[1]), "+v"(af[0]), "+v"(af[1]), "+v"(s0), "+v"(s1), "+v"(d0));
  const f32x4 k = ad[0] * s0 + (f32x4){d0, 0.f, 0.f, 0.f} + ad[1] * s1, f = af[0] * s0 + af[1] * s1;
  auto pk = [](float a, float b) { uint32_t r; asm("v_cvt_pk_f16_f32 %0, %1, %2" : "=v"(r) : "v"(a), "v"(b)); return r; };
  asm volatile("s_nop 0" : : "v"(k), "v"(f));
  const uint32_t p32 = pk(k[3], k[2]), p10 = pk(k[1], k[0]), p21 = pk(k[2], k[1]), p01 = pk(f[0], f[1]), p23 = pk(f[2], f[3]);
  *LP(u32x2, t.pd + 3 * TDP + 6) = (u32x2){p32, p10};
  *LP(uint32_t, t.pd + TDP + 2) = p32; *LP(uint32_t, t.pd + TDP + 6) = p10;
  *LP(uint32_t, t.pd + 2) = p21; *LP(uint32_t, t.pd + 2 * TDP + 6) = p21;
  *LP(u16, t.pd) = (u16)p32; *LP(u16, t.pd + 2 * TDP + 4) = (u16)p32;
  asm volatile("ds_write_b16_d16_hi %0, %1 offset:6\n\tds_write_b16_d16_hi %0, %1 offset:%2" : : "v"(t.pd), "v"(p10), "n"(2 * TDP + 10) : "memory");
  if (ct) {
    const uint32_t va = (uint32_t)__builtin_amdgcn_readlane((int)p32, 63), vb = (uint32_t)__builtin_amdgcn_readlane((int)p23, 15) >> 16;
    *LP(u16, t.pe) = (u16)((lane < 12) ? va : vb);
  }
  if (lane < 32) {
    *LP(u16, t.pf + 128) = (u16)p01; *LP(u16, t.pg + 128) = (u16)(p01 >> 16);
    *LP(u16, t.pf) = (u16)p23; *LP(u16, t.pg) = (u16)(p23 >> 16);
  }
  return (uint32_t)(127 + 5 - ts) << 23;
}
__device__ __forceinline__ uint32_t hadd(uint32_t a, uint32_t b) { return __builtin_bit_cast(uint32_t, __builtin_bit_cast(f16x2, a) + __builtin_bit_cast(f16x2, b)); }
template <bool FULL> __device__ __forceinline__ void sums(char* sm, const u32x4 p, const u32x4 c) {
  const uint32_t D[8] = {p[0], p[1], p[2], p[3], c[0], c[1], c[2], c[3]};
  uint32_t O[7];
#pragma unroll
  for (int j = 0; j < 7; ++j) O[j] = __builtin_amdgcn_alignbit(D[j + 1], D[j], 16);
  u32x4 a, b;
#pragma unroll
  for (int j = 0; j < 4; ++j) a[j] = hadd(D[j + 2], hadd(O[j + 1], O[j + 2]));
  *(u32x4*)(sm + C_G3 + 32) = a;
#pragma unroll
  for (int j = 0; j < 4; ++j) b[j] = hadd(a[j], hadd(D[j + 1], D[j + 3]));
  *(u32x4*)(sm + C_G5 + 32) = b;
  if (FULL) {
#pragma unroll
    for (int j = 0; j < 4; ++j) a[j] = hadd(b[j], hadd(O[j], O[j + 3]));
    *(u32x4*)(sm + C_G7 + 32) = a;
#pragma unroll
    for (int j = 0; j < 4; ++j) b[j] = hadd(a[j], hadd(D[j], D[j + 4]));
    *(u32x4*)(sm + C_G9 + 32) = b;
  }
}
__device__ __forceinline__ void count_up(uint32_t a) {
  uint32_t t;
  asm volatile("s_mov_b64 exec, 1\n\tv_mov_b32 %0, 1\n\tds_add_u32 %1, %0\n\ts_mov_b64 exec, -1" : "=&v"(t) : "v"(a) : "memory");
}
__device__ __forceinline__ int pad_at(int r) { return 16 * (r & 1) + 3200 * (r >> 1) - ((r >= 6) ? 3200 - 2096 : 0); }
struct COps { uint32_t b, a, o, g3, k3, g4, k4, f; };
__device__ __forceinline__ void conv_ops(uint32_t tb, uint32_t ub, uint32_t cb, int lane, COps& e) {
  const uint32_t j = lane & 15, kq = lane >> 4, q4 = (lane >> 2) & 3, p4 = lane & 3;
  e.b = ub + 32u * j + 16u * kq;
  e.a = tb + p4 * TDP + 32u - 8u * q4 + 16u * kq;
  e.g3 = cb + C_G5 + 8 + 64u * kq + 8u * (q4 + p4) + (q4 & 1u) * 3200u;
  e.g4 = cb + C_G9 + 8 + 128u * kq + 16u * q4 + 8u * p4 + (q4 & 1u) * 2096u;
  e.k3 = tb + T_3 + 120 - 8u * j + 16u * kq;
  e.k4 = tb + T_4 + 60 - 4u * j + 16u * kq;
  asm volatile("" : "+v"(e.b), "+v"(e.a), "+v"(e.g3), "+v"(e.g4), "+v"(e.k3), "+v"(e.k4));
}
#define CONV_RUN(prog, e, ...) do { int prog_ = (prog); \
    asm volatile("s_call_b64 s[90:91], kf_conv" : "+{s89}"(prog_), "+{v2}"(e.b), "+{v3}"(e.a), "+{v4}"(e.o), "+{v5}"(e.g3), "+{v6}"(e.k3), "+{v7}"(e.g4), \
                 "+{v8}"(e.k4), "+{v9}"(e.f) __VA_ARGS__ : : CONV_CLOB); } while (0)

template <bool E>
__device__ __forceinline__ void ln_stats(const uint32_t* v, int lane, float sc, f32x2* t, f32x4 z, f32x2& rc, f32x2& nc, float& e) {
  float s1a = z[0], s2a = z[1], s1b = z[2], s2b = z[3];
#pragma unroll
  for (int k = 0; k < 4; ++k) {
    s1a = dot2(v[k], 0x3f803f80u, s1a); s2a = dot2(v[k], v[k], s2a);
    s1b = dot2(v[4 + k], 0x3f803f80u, s1b); s2b = dot2(v[4 + k], v[4 + k], s2b);
  }
#pragma unroll
  for (int k = 0; k < 9; ++k) t[k] = (f32x2){bflo(v[k]), bfhi(v[k])};
  const float rs = row_sum(fold16(dot2(v[8], 0x3f803f80u, fold32(s1a, s1b)), dot2(v[8], v[8], fold32(s2a, s2b))));
  if (E) { e = row_sum(e); e += __int_as_float(__builtin_amdgcn_update_dpp(0, __float_as_int(e), 0x142, 0xa, 0xf, false)); }
  const f32x2 mean = (f32x2){rdl(rs, 15), rdl(rs, 47)} * (1.f / D_), m2 = mean * mean;
  f32x2 var = (f32x2){rdl(rs, 31), rdl(rs, 63)} * (1.f / D_) - m2;
  asm volatile("" : "+v"(t[0]), "+v"(t[1]), "+v"(t[2]), "+v"(t[3]), "+v"(t[4]), "+v"(t[5]), "+v"(t[6]), "+v"(t[7]), "+v"(t[8]));
  if (__builtin_expect(!(m2[0] <= 16.f * var[0] && m2[1] <= 16.f * var[1]), 0)) {
    float q[3] = {0.f, 0.f, 0.f};
#pragma unroll
    for (int k = 0; k < 9; ++k) {
      const f32x2 a = t[k] - ((k < 4 || (k == 8 && lane < 32)) ? mean[0] : mean[1]);
      q[k >> 2] += a[0] * a[0] + a[1] * a[1];
    }
    float qa = q[0] + ((lane < 32) ? q[2] : 0.f), qb = q[1] + ((lane < 32) ? 0.f : q[2]);
    wave_sum2(qa, qb);
    var = (f32x2){qa, qb} * (1.f / D_);
  }
  const uint32_t mp = pack2(mean[0], mean[1]), vp = pack2(var[0], var[1]);
  const f32x2 ve = (f32x2){bflo(vp), bfhi(vp)} + 1e-5f;
  const uint32_t e2 = pack2(ve[0], ve[1]);
  const uint32_t sp = pack2(__builtin_amdgcn_sqrtf(bflo(e2)), __builtin_amdgcn_sqrtf(bfhi(e2)));
  rc = (f32x2){__builtin_amdgcn_rcpf(bflo(sp)), __builtin_amdgcn_rcpf(bfhi(sp))} * sc;
  nc = -(f32x2){bflo(mp), bfhi(mp)} * rc;
}
template <bool H>
__device__ __forceinline__ void ln_apply(const f32x2* t, int lane, f32x2 rc, f32x2 nc, uint32_t* c) {
  const float nm[3] = {nc[0], nc[1], (lane < 32) ? nc[0] : nc[1]}, rr[3] = {rc[0], rc[1], (lane < 32) ? rc[0] : rc[1]};
  f32x2 dd[9];
#pragma unroll
  for (int k = 0; k < 9; ++k) dd[k] = __builtin_elementwise_fma(t[k], (f32x2){rr[k >> 2], rr[k >> 2]}, (f32x2){nm[k >> 2], nm[k >> 2]});
  asm volatile("" : "+v"(dd[0]), "+v"(dd[1]), "+v"(dd[2]), "+v"(dd[3]), "+v"(dd[4]), "+v"(dd[5]), "+v"(dd[6]), "+v"(dd[7]), "+v"(dd[8]));
#pragma unroll
  for (int k = 0; k < 9; ++k) {
    const f32x2 d = dd[k];
    typedef __attribute__((ext_vector_type(2))) uint16_t u16x2;
    if (H) c[k] = __builtin_bit_cast(uint32_t, __builtin_elementwise_add_sat(__builtin_bit_cast(u16x2, __builtin_amdgcn_cvt_pkrtz(d[0], d[1])), (u16x2){4, 4}));
    else c[k] = pack2(d[0], d[1]);
  }
}
__device__ __forceinline__ uint32_t med3u(uint32_t a, uint32_t b, uint32_t c) { return max(min(a, b), min(max(a, b), c)); }
__device__ __forceinline__ uint32_t oct_maxu(uint32_t v) {
  v = max(v, (uint32_t)DPP_I((int)v, 0xb1, 0xf));
  v = max(v, (uint32_t)DPP_I((int)v, 0x4e, 0xf));
  v = max(v, (uint32_t)DPP_I((int)v, 0x141, 0xf));
  return v;
}

#define KARGS const u16* __restrict__ x, const u16* __restrict__ Are, const u16* __restrict__ Aim, const u16* __restrict__ Bre, \
    const u16* __restrict__ Bim, const u16* __restrict__ Cre, const u16* __restrict__ Cim, const u16* __restrict__ logdt, \
    const u16* __restrict__ Dskip, char* __restrict__ ws, const u16* __restrict__ wr, const u16* __restrict__ wg, \
    const u16* __restrict__ wu, const u16* __restrict__ wd, u16* __restrict__ out
#define KPASS x, Are, Aim, Bre, Bim, Cre, Cim, logdt, Dskip, ws, wr, wg, wu, wd, out
template <int ROLE, int FFN = -1>
__device__ __forceinline__ void role_body(char* smem, KARGS) {
  const int tid = threadIdx.x, lane = tid & 63;
  const int w = __builtin_amdgcn_readfirstlane(tid >> 6), wl = w & 3;
  const int b = blockIdx.x & 255, bx = b & 7, bj = b >> 3;
  constexpr bool isl = ROLE == 1 || ROLE == 2;
  const int tg = isl ? 32 * (bj >> 2) + 4 * bx + (bj & 3) : 32 * bx + bj;
  u32x4 xa, xc;
  uint32_t ep, r0;
#define P_WAIT(n, ...) asm volatile("s_waitcnt vmcnt(" #n ")\n\tv_lshlrev_b32 %0, 16, v100\n\tv_lshlrev_b32 %1, 16, v101\n\tv_lshlrev_b32 %2, 16, v102" \
    : "=v"(ldt), "=v"(are), "=v"(aim) : : "v100", "v101", "v102" __VA_ARGS__)
#define P_MORE() asm volatile("v_lshlrev_b32 %0, 16, v103\n\tv_lshlrev_b32 %1, 16, v104\n\tv_lshlrev_b32 %2, 16, v105\n\tv_lshlrev_b32 %3, 16, v106" \
    : "=v"(bre), "=v"(bim), "=v"(cre), "=v"(cim) : : "v103", "v104", "v105", "v106")
  TSD;
  TS(0, 0);
  const uint32_t lb0 = (uint32_t)(uintptr_t)(__attribute__((address_space(3))) char*)smem;
  const int i = lane & 15, kq = lane >> 4;
  const u32x4 ones = {0xffffffffu, 0xffffffffu, 0xffffffffu, 0xffffffffu}, zero4 = {0u, 0u, 0u, 0u};
  auto u_again = [&](u32x4& uw, const u32x4& ut, uint32_t uo) {
#pragma unroll 1
    for (int spin = 0; spin < SPIN_CAP; ++spin) {
      const bool na = ((uw[0] | uw[1] | uw[2] | uw[3]) & UMASK) != 0u;
      if (__builtin_expect(!__any(na), 1)) break;
      TSC(19);
      SLEEP(USLEEP);
      if (na) { LD16W(uw, uo); uw ^= ut; }
    }
  };
  constexpr uint32_t S16 = 0x41800000u, LMASK = 0x00070007u;
  auto v_put = [&](u32x4 v, const u32x4& ut, bool other, uint32_t vo) {
    const bool second = __any(other || ((v[0] | v[1] | v[2] | v[3]) & 0x40004000u) != 0u);
    if (__builtin_expect(second, 0)) v = (v & ~LMASK) | (~ut & LMASK); else v = (v & ~LMASK) | ut;
    ST16(vo, v);
    return second;
  };
  auto v_end = [&](uint32_t T, int n) { DRAIN(); if (lane == 0) ST4(O_DN + 16u * n, T); };
  if constexpr (!isl) {
    const int ch = wl >> 1, tl = tid & 255;
    int* evp = (int*)(smem + O_FL + 16 * ch);
    float ldt, are, aim, dsk;
    if constexpr (ROLE == 0) {
      __builtin_amdgcn_s_setprio(KPRIO);
      COps e;
      conv_ops(lb0 + (uint32_t)(ch * TB), lb0 + (uint32_t)(O_UL + ch * UB), lb0 + (uint32_t)(O_IM + ch * GB), lane, e);
      e.o = lb0 + O_ST + (uint32_t)(16 * ((4 * i + 2 * (kq >> 1) + ch) ^ (i >> 1)) + 8 * (kq & 1));
      e.f = lb0 + O_FL;
      TOps t;
      table_ops(lb0 + (uint32_t)(O_IM + 128 * IP * ch), lb0 + (uint32_t)(TB * ch), wl & 1, lane, t);
      uint32_t pz = lb0 + O_IM + (uint32_t)(((lane >> 3) & 1) * GB + pad_at(lane & 7)), im = lb0 + (uint32_t)(O_IM + IP * tl);
      asm volatile("" : "+v"(e.o), "+v"(e.f), "+v"(pz), "+v"(im));
      float bre, bim, cre, cim;
      P_WAIT(0, , "v103", "v104", "v105", "v106", "v107", "v109");
      asm volatile("" : "={v109}"(ep));
      const uint32_t T = ((uint32_t)__builtin_amdgcn_readfirstlane(ep) + 1u) * 0x9E3779B1u;
      if (w == 3 && lane < 8) ST16(O_KEY + 128u * b + 16u * lane, ones);
      P_MORE();
      asm volatile("v_lshlrev_b32 %0, 16, v107" : "=v"(dsk) : : "v107");
      TS(0, 1);
      evp[wl & 1] = gen_y(ldt, are, aim, bre, bim, cre, cim, im, [] {} TSA);
      TS(0, 8);
      BAR();
      TS(0, 2);
      const uint32_t cs = table_tile(t, wl & 1, lane, lb0 + O_FL + 16 * ch, dsk TSA);
      *(uint32_t*)(smem + O_FL + 40 + 4 * ch) = cs;
      DRAIN();
      if ((wl & 1) && lane == 0) asm volatile("global_store_dwordx2 %0, %1, %2 sc1" : : "v"(O_SC + 16u * (2 * tg + ch)), "v"((u32x2){T, cs}), "s"(ws) : "memory");
      TS(0, 9);
      gsync0(lb0 + O_FL + 36, 4);
      TS(0, 3);
      *(u32x4*)(smem + O_ST + 16 * tl) = ones;
      *(u32x4*)(smem + O_ST + 4096 + 16 * tl) = ones;
      if (w == 0) *LP(u32x4, pz) = zero4;
      CONV_RUN(wl & 1, e);
      TS(0, 5);
    } else {
      __builtin_amdgcn_s_setprio(UPRIO);
#pragma unroll
      for (int k = 0; k < 3; ++k) *(u32x4*)(smem + ((16 * (tl + 256 * k) < O_FL) ? 16 * (tl + 256 * k) : O_FL + 32)) = zero4;
      if (w == 7) *(u32x4*)(smem + O_UL + (lane >> 5) * UB + 16 * min(lane & 31, 29)) = zero4;
      uint32_t uo = u_at(tl >> 1, 2 * tg + (tid & 1)), go = O_SU + (uint32_t)(256 * bx + 32 * wl + (lane & 31) + 4 * (lane & 32)) * 128u;
      uint32_t ul = (uint32_t)(O_UL + (tid & 1) * UB + C_U - 16 + 16 * (tl >> 1)), ug = (uint32_t)(O_IM + (tid & 1) * GB + 16 * (tl >> 1)), so = lb0 + O_ST + 16u * (uint32_t)(lane ^ (lane >> 3));
      uint32_t vo = O_V + (uint32_t)tg * VP + 16u * lane;
      asm volatile("" : "+v"(uo), "+v"(go), "+v"(ul), "+v"(ug), "+v"(so), "+v"(vo));
      P_WAIT(1, , "v109");
      TS(0, 1);
      evp[2 + (wl & 1)] = gen_x(ldt, are, aim, smem + O_IM + IP * tl);
      asm volatile("s_waitcnt vmcnt(0)" : "={v109}"(ep));
      const uint32_t T = ((uint32_t)__builtin_amdgcn_readfirstlane(ep) + 1u) * 0x9E3779B1u;
      TS(0, 8);
      BAR();
      TS(0, 2);
      u32x4 ut = {utag(T, 0), utag(T, 1), utag(T, 2), utag(T, 3)};
      asm volatile("" : "+v"(ut));
      SLEEP(GSLEEP);
      u32x2 pv;
      pv[1] = gate(ws, go, T);
      TS(0, 3);
      until(latest(pv[1]) + UD);
      TS(0, 7);
      u32x4 uw, ux;
      asm volatile("global_load_dwordx4 %0, %2, %4 sc1\n\tglobal_load_dwordx4 %1, %3, %4 sc1"
                   : "=&v"(uw), "=&v"(ux) : "v"(uo), "v"(uo + 64 * UP * 32), "s"(ws) : "memory");
      __builtin_amdgcn_s_setprio(FPR);
      asm volatile("s_waitcnt vmcnt(1)" : "+v"(uw));
      uw ^= ut;
      *(u32x4*)(smem + ul + 16) = uw;
      asm volatile("s_waitcnt vmcnt(0)" : "+v"(ux));
      TS(0, 11);
      ux ^= ut;
      *(u32x4*)(smem + ul + 2048 + 16) = ux;
      if (__builtin_expect(__any(((uw[0] | uw[1] | uw[2] | uw[3] | ux[0] | ux[1] | ux[2] | ux[3]) & UMASK) != 0u), 0)) {
        u_again(uw, ut, uo);
        u_again(ux, ut, uo + 64 * UP * 32);
        *(u32x4*)(smem + ul + 16) = uw;
        *(u32x4*)(smem + ul + 2048 + 16) = ux;
      }
      BAR();
      TS(0, 4);
      const u32x4 pw = *(const u32x4*)(smem + ul);
      if (wl < 2) {
        const u32x4 px = *(const u32x4*)(smem + ul + 2048);
        sums<true>(smem + ug, pw, uw);
        sums<false>(smem + ug + 2048, px, ux);
      } else sums<true>(smem + ug, pw, uw);
      count_up(lb0 + O_FL + 32);
      __builtin_amdgcn_s_setprio(SPR);
      TS(0, 5);
      const bool other = *(const uint32_t*)(smem + O_FL + 40 + 4 * (lane & 1)) != S16;
      bool second = false;
#pragma unroll
      for (int k = 0; k < 2; ++k) {
        const uint32_t o = ((k ? 0x3247u : 0x1065u) >> (4 * wl) & 7u) * 1024u;
        u32x4 v;
#pragma unroll 1
        for (int spin = 0; spin < SPIN_CAP; ++spin) {
          asm volatile("ds_read_b128 %0, %1\n\ts_waitcnt lgkmcnt(0)" : "=&v"(v) : "v"(so + o) : "memory");
          if (!__any(max4(v) == 0xffffffffu)) break;
          __builtin_amdgcn_s_sleep(VPS);
        }
        TS(0, 12 + k);
        second |= v_put(v, ut, other, vo + o);
        TS(0, 10 - 4 * k);
      }
      if (__builtin_expect(second, 0)) v_end(T, 4 * tg + wl);
    }
    TSE();
  } else {
  uint32_t* cand = (uint32_t*)(smem + LM);
  uint32_t* sel = cand + 128;
  uint32_t* kl = sel + 48;
  uint32_t* cnt = sel + 64;
  const uint32_t ca = lb0 + LM + 4 * (128 + 64);
  u16* hm = (u16*)(cnt + 12);
  uint32_t T;
  const bool ffn = (FFN < 0) ? bj < 18 : FFN, isx = FFN != 1 && bj >= 24;
  const int fb = 18 * bx + bj;
  constexpr bool ln2 = ROLE == 2;
  const int g0 = tg, g2 = tg;
  u32x4 bw[18];
#pragma unroll
  for (int s = 0; s < 9; ++s) bw[s] = (u32x4){0u, 0u, 0u, 0u};
  int tid2 = threadIdx.x;
  uint32_t ts1 = 0u;
  auto rest = [&] {
    if (ts1 && (int)((uint32_t)__builtin_amdgcn_s_memrealtime() - ts1) > 0) { TS(0, 8); until(ts1 + TSTOP + RD); if (RW) gwait(ca, 4); ts1 = 0u; }
  };
  auto gu_dma = [&](int q0, int q1, auto depth) {
    const char* gs = (const char*)((w < 2) ? wg : wu) + (size_t)((144 * (w & 1)) * (F_ * 2) + fb * 64);
    const uint32_t gv = (uint32_t)((lane >> 2) * (F_ * 2) + 16 * ((lane & 3) ^ (lane >> 5) ^ (w & 2)));
#pragma unroll 1
    for (int q = q0; q < q1; ++q) {
      const int k = (int)((((bj & 1) ? ORD1 : ((unsigned __int128)0x876543210u << 36 | 0x876543210ull)) >> (4 * (q - (q >= 9 ? 9 : 0)) + (q >= 9 ? 36 : 0))) & 15u), h = q >= 9;
      if (q >= RQ) rest();
      dma16(gs + (size_t)(288 * h + 16 * k) * (F_ * 2), gv, lb0 + LW * h + 9216 * w + 1024 * k);
      asm volatile("s_waitcnt vmcnt(%0)" : : "n"(depth.value) : "memory");
    }
  };
  auto down_dma = [&](int k0, int k1, int lane) {
#pragma unroll 1
    for (int k = k0; k < k1; ++k) {
      const int o = 9216 * wl + 2304 * k;
      const char* ds = (const char*)wd + (size_t)(fb * 36864 + o);
      const uint32_t dl = lb0 + o + 32 * ((4 * wl + k + 1) >> 1);
      if (ROLE == 1) rest();
      dma16d(ds, 16u * lane, dl);
      dma16d(ds + 1024, 16u * lane, dl + 1024);
      uint32_t keep;
      asm volatile("s_mov_b32 %0, m0\n\ts_mov_b32 m0, %3\n\ts_mov_b64 exec, 0xffff\n\tglobal_load_lds_dwordx4 %1, %2" DPOL2 "\n\ts_mov_b64 exec, -1\n\ts_mov_b32 m0, %0\n\ts_waitcnt vmcnt(%4)"
                   : "=&s"(keep) : "v"(16u * lane), "s"(ds + 2048), "s"(dl + 2048), "n"(ROLE == 1 ? DDEPTH : 40) : "memory");
    }
  };
  auto gu_read = [&](int h) {
    const uint32_t ra = lb0 + LW * h + (uint32_t)(((lane >> 1) & 1) * 18432 + (8 * kq + ((lane >> 2) & 3)) * 64 + ((w ^ (kq & 1) ^ (lane & 2)) * 16) + 8 * (lane & 1));
#pragma unroll
    for (int s = 0; s < 9; ++s) {
      const u32x2 h0 = trd(ra + 2048 * s), h1 = trd(ra + 2048 * s + 256);
      bw[9 * h + s] = (u32x4){h0[0], h0[1], h1[0], h1[1]};
    }
#pragma unroll
    for (int s = 0; s < 9; ++s) asm volatile("" : "+v"(bw[9 * h + s]));
  };
  uint32_t tv = 0u, tk = 0u;
  const uint32_t vearly = (uint32_t)((VEARLY >> (8 * (g0 >> 5))) & 0xffull);
  auto w_line = [&](int n) {
    const int q = min(lane, 62) / 9, r = min(7 * (n >> 2) + q, 287), h = r >= 144;
    return (const char*)((n & 1) ? wu : wg) + (uint32_t)((r + 144 * (h + ((n >> 1) & 1))) * (F_ * 2) + bx * 1152 + (min(lane, 62) - 9 * q) * 128);
  };
#define TOUCH(pa) asm volatile("global_load_dword v126, %0, off" DPOL4 : : "v"(pa) : "memory", "v126")
  uint32_t wv[5], xv[9];
  auto rows_xw = [&](int lane) {
    u32x4 wa, xr, xs;
    const uint32_t xo = (uint32_t)((8 * g2 + 2 * wl) * (D_ * 2));
    asm volatile("global_load_dwordx4 %0, %5, %9\n\tglobal_load_dword %1, %6, %9 offset:1024\n\t"
                 "global_load_dwordx4 %2, %7, %10\n\tglobal_load_dwordx4 %3, %7, %10 offset:1152\n\tglobal_load_dword %4, %8, %10 offset:1024\n\ts_waitcnt vmcnt(0)"
                 : "=&v"(wa), "=&v"(wv[4]), "=&v"(xr), "=&v"(xs), "=&v"(xv[8])
                 : "v"(16u * lane), "v"(4u * (lane & 31)), "v"(xo + 16u * lane), "v"(xo + 4u * lane + 32u * (lane & 32)), "s"(wr), "s"(x) : "memory");
#pragma unroll
    for (int k = 0; k < 4; ++k) { wv[k] = wa[k]; xv[k] = xr[k]; xv[4 + k] = xs[k]; }
  };
  auto ln1 = [&](int g, int o, uint32_t c0) {
    uint32_t uo = u_at(g, 64 * wl + lane);
    uint32_t ta = lb0 + (uint32_t)(o + ((lane >> 2) & 3) * RP + (64 * wl + 16 * kq + 4 * (lane & 3)) * 2);
    uint32_t la = (uint32_t)(o + 2 * wl * RP + 16 * lane), lt = (uint32_t)(o + (2 * wl + (lane >> 5)) * RP + 1024 + 4 * (lane & 31));
    uint32_t sg = O_SU + (uint32_t)(256 * (lane >> 3) + g) * 128u + 16u * (lane & 7);
    asm volatile("" : "+v"(uo), "+v"(ta), "+v"(la), "+v"(lt), "+v"(sg));
    f32x4 z4 = {0.f, 0.f, 0.f, 0.f};
    asm volatile("" : "+v"(z4));
    asm volatile("s_waitcnt vmcnt(0)" : "={v[100:103]}"(xa), "={v[104:107]}"(xc), "={v108}"(r0), "={v109}"(ep));
    TS(0, 1);
    const uint32_t v[9] = {xa[0], xa[1], xa[2], xa[3], xc[0], xc[1], xc[2], xc[3], r0};
    uint32_t c[9];
    f32x2 tf[9];
    T = ((uint32_t)__builtin_amdgcn_readfirstlane(ep) + 1u) * 0x9E3779B1u;
    if (wl == 3) { const uint32_t now = (uint32_t)__builtin_amdgcn_s_memrealtime() + SD; ST16(sg, ((u32x4){T, now, T, now})); }
    const uint32_t tj = ((T >> (3 * wl)) & 0x00070007u) | (((T >> (12 + wl)) & 0x00010001u) << 14);
    uint32_t tw[3] = {(tj & 0xffffu) * 0x10001u, (tj >> 16) * 0x10001u, 0u};
    tw[2] = (lane < 32) ? tw[0] : tw[1];
    asm volatile("" : "+v"(tw[0]), "+v"(tw[1]), "+v"(tw[2]));
    f32x2 rc, nc;
    float e_;
    ln_stats<false>(v, lane, 0.03125f, tf, z4, rc, nc, e_);
    ln_apply<true>(tf, lane, rc, nc, c);
#pragma unroll
    for (int k = 0; k < 9; ++k) c[k] = (c[k] & ~UMASK) | tw[k >> 2];
    *(u32x4*)(smem + la) = (u32x4){c[0], c[1], c[2], c[3]};
    *(u32x4*)(smem + la + RP) = (u32x4){c[4], c[5], c[6], c[7]};
    *(uint32_t*)(smem + lt) = c[8];
    u32x4 ha, hb;
    {
      uint32_t t, cc, n = SPIN_CAP;
      asm volatile("s_mov_b64 exec, 1\n\tv_mov_b32 %2, 1\n\tds_add_u32 %5, %2\n\ts_mov_b64 exec, -1\n"
                   "1:\n\tds_read_b32 %2, %5\n\tds_read_b64_tr_b16 v[40:41], %6\n\tds_read_b64_tr_b16 v[42:43], %6 offset:%7\n\tds_read_b64_tr_b16 v[44:45], %6 offset:512\n\t"
                   "ds_read_b64_tr_b16 v[46:47], %6 offset:%8\n\ts_sub_u32 %4, %4, 1\n\ts_cbranch_scc1 2f\n\ts_waitcnt lgkmcnt(4)\n\tv_readfirstlane_b32 %3, %2\n\t"
                   "s_cmp_lt_u32 %3, 4\n\ts_cbranch_scc1 1b\n2:\n\ts_waitcnt lgkmcnt(0)"
                   : "=&{v[40:43]}"(ha), "=&{v[44:47]}"(hb), "=&v"(t), "=&s"(cc), "+s"(n) : "v"(c0), "v"(ta), "n"(4 * RP), "n"(512 + 4 * RP) : "memory", "scc");
    }
    ST16(uo, ha);
    ST16(uo + 64 * 128, hb);
    if (wl == 0) {
      const u32x2 h4 = trd(ta + 1024), h5 = trd(ta + 1024 + 4 * RP);
      ST16(uo + 128 * 128, ((u32x4){h4[0], h4[1], h5[0], h5[1]}));
    }
  };
  const int dX = 512 - 24 + 8 * bx + bj;
  if constexpr (ROLE == 2) {
    const int tl = tid - 256;
    if (XOFF && (uint32_t)(bj - 4) < 8u) {
      __builtin_amdgcn_s_setprio(3);
      ln1(tg + 160, 10240, ca + 44);
      TS(0, 2);
    }
    if (!isx && T0) {
      SLEEP(T0SLEEP);
      if (wl < T0) TOUCH(w_line(24 * wl + bj));
      if (T1) { asm volatile("s_waitcnt vmcnt(0)" ::: "memory"); SLEEP(T1SLEEP); if (wl < T1) TOUCH(w_line(24 * (T0 + wl) + bj)); }
    }
    if (isx) {
      __builtin_amdgcn_s_setprio(QPRIO0);
      *(u32x4*)(smem + X_TB + 16 * tl) = zero4;
      *(u32x4*)(smem + X_TB + 16 * min(256 + tl, (TB + C_U) / 16 - 1)) = zero4;
      const int pr = (wl + XROT) & 3;
      COps e;
      conv_ops(lb0 + X_TB, lb0 + X_UL, lb0 + X_IM, lane, e);
      TOps t;
      table_ops(lb0 + X_IM, lb0 + X_TB, wl & 1, lane, t);
      e.o = lb0 + X_ST + (uint32_t)(16 * ((2 * i + (kq >> 1)) ^ ((i >> 2) & 1)) + 8 * (kq & 1));
      e.f = ca + 28 - 32;
      asm volatile("" : "+v"(e.o), "+v"(e.f));
      float ldt, are, aim, bre, bim, cre, cim, dsk;
      P_WAIT(0, , "v103", "v104", "v105", "v106", "v107", "v109");
      asm volatile("" : "={v109}"(ep));
      T = ((uint32_t)__builtin_amdgcn_readfirstlane(ep) + 1u) * 0x9E3779B1u;
      P_MORE();
      asm volatile("v_lshlrev_b32 %0, 16, v107" : "=v"(dsk) : : "v107");
      TS(0, 1);
      __builtin_amdgcn_s_setprio(QPRIO);
      int* evp = (int*)(sel + 44);
      char* im = smem + X_IM + IP * (tl & 127);
      evp[wl] = (wl < 2) ? gen_y(ldt, are, aim, bre, bim, cre, cim, lb0 + X_IM + IP * tl, [] {} TSA) : gen_x(ldt, are, aim, im);
      TS(0, 2);
      __builtin_amdgcn_s_setprio(QPRIO2);
      gsync(ca + 8, 4);
      if (wl < 2) evp[-4 + wl] = (int)table_tile(t, wl & 1, lane, lb0 + LM + 4 * (128 + 44), dsk TSA);
      if (wl < 2) gsync0(ca + 8, 8); else { count_up(ca + 8); SLEEP(XSL); gwait(ca + 8, 8); }
      const uint32_t cs = (uint32_t)__builtin_amdgcn_readfirstlane(evp[-4]);
      if (wl == 2 && lane == 0) asm volatile("global_store_dwordx2 %0, %1, %2 sc1" : : "v"(O_SC + 16u * dX), "v"((u32x2){T, cs}), "s"(ws) : "memory");
      if (wl == 3) *(u32x4*)(smem + X_IM + pad_at(lane & 7)) = zero4;
      TS(0, 3);
      CONV_RUN(2 + pr, e, , "+{v11}"(tid2));
      {
        const int lane = tid2 & 63;
        const u32x4 v = *(const u32x4*)(smem + X_ST + 2048 * pr + 1024 * (lane >> 5) + 16 * ((lane & 31) ^ ((lane >> 3) & 1)));
        if (__builtin_expect(v_put(v, (u32x4){utag(T, 0), utag(T, 1), utag(T, 2), utag(T, 3)}, cs != S16, v_at(dX, 64 * pr + lane)), 0)) {
          DRAIN();
          if (lane == 0) ST4(O_DN + 16u * (4 * (dX - 256) + pr), T);
        }
      }
      TS(0, 6);
    }
    __builtin_amdgcn_s_setprio(PPRIO);
    asm volatile("" : "+v"(tid2));
    if (isx) {
      const int lane = tid2 & 63;
      const u32x4 ra = *(const u32x4*)(smem + X_RW(wl) + 16 * lane), rb = *(const u32x4*)(smem + X_RW(wl) + 1024 + 16 * lane), rw = *(const u32x4*)(smem + X_WR + 16 * lane);
      const u32x2 rc = *(const u32x2*)(smem + X_RW(wl) + 2048 + 8 * lane);
#pragma unroll
      for (int k = 0; k < 4; ++k) { xv[k] = ra[k]; xv[4 + k] = rb[k]; wv[k] = rw[k]; }
      xv[8] = rc[0]; wv[4] = rc[1];
    } else rows_xw(tid2 & 63);
    if (!isx && !ffn) {
      SLEEP(RSLEEP);
#pragma unroll
      for (int k = 0; k < 7; ++k) ST16(O_SL + (uint32_t)((6 * bx + bj - 18) * 1792 + 256 * k - 256 + tid2) * 16u, ones);
      DRAIN();
    }
    if (!isx) BAR();
    const uint32_t tp = (uint32_t)__builtin_amdgcn_readfirstlane(cnt[4]);
    if (!isx) T = (uint32_t)__builtin_amdgcn_readfirstlane(sel[43]);
    tv = tp + UD + VD2 - vearly;
  } else {
    __builtin_amdgcn_s_setprio(3);
    if (tid < 12) cnt[tid] = 0u;
    BAR();
    if (tid < 20) ((uint32_t*)(hm + 320))[tid] = 0u;
    if (tid >= 184) *(u32x4*)(smem + L_SC - 16 * 184 + 16 * tid) = (u32x4){0x41804180u, 0x41804180u, 0x41804180u, 0x41804180u};
    uint32_t sink = 0;
    if (XOFF && isx) {
      asm volatile("s_waitcnt vmcnt(0)" : "={v109}"(ep));
      T = ((uint32_t)__builtin_amdgcn_readfirstlane(ep) + 1u) * 0x9E3779B1u;
    } else ln1(tg, 0, ca + 12);
    TS(0, 2);
    __builtin_amdgcn_s_setprio(NPRIO);
    if (!isx && 3 - w < TB1) TOUCH(w_line(24 * (3 - w) + 23 - bj));
    asm volatile("s_waitcnt vmcnt(0)" : "+v"(sink) : : "memory");
    TS(0, 13);
    if (w == 0) {
      const uint32_t xr = O_XC + (uint32_t)((511 - 2 * (g0 + w)) * 144 + lane) * 16u;
      ST16(xr, ones);
      ST16(xr + 1024u, ones);
      if (lane < 16) ST16(xr + 2048u, ones);
    }
    if (w == 2 && b < 72 && lane < 8) ST16(O_FB + (uint32_t)(8 * b + lane) * 16u, ones);
    if (isx) {
      rows_xw(lane);
      char* sp = smem + X_RW(w) + 16 * lane;
      *(u32x4*)sp = (u32x4){xv[0], xv[1], xv[2], xv[3]};
      *(u32x4*)(sp + 1024) = (u32x4){xv[4], xv[5], xv[6], xv[7]};
      *(u32x2*)(smem + X_RW(w) + 2048 + 8 * lane) = (u32x2){xv[8], wv[4]};
      if (w == 0) *(u32x4*)(smem + X_WR + 16 * lane) = (u32x4){wv[0], wv[1], wv[2], wv[3]};
    }
    SLEEP(ESLEEP);
    uint32_t tu = latest(gate(ws, O_SU + (uint32_t)(256 * bx + 64 * w + 4 * i + (bj & 3)) * 128u, T));
    asm volatile("s_mov_b64 exec, 1\n\tds_max_u32 %0, %1\n\ts_mov_b64 exec, -1" : : "v"(ca + 16), "v"(tu) : "memory");
    if (w == 0) sel[43] = T;
    if (isx) {
      const uint32_t xo = u_at(tid, dX);
      char* sm = smem + X_UL + C_U - 16 + 16 * tid;
      const u32x4 ut = {utag(T, 0), utag(T, 1), utag(T, 2), utag(T, 3)};
      __builtin_amdgcn_s_setprio(3);
      until(tu + UD + XD);
      u32x4 uw;
      asm volatile("global_load_dwordx4 %0, %1, %2 sc1" : "=&v"(uw) : "v"(xo), "s"(ws) : "memory");
      asm volatile("s_waitcnt vmcnt(0)" : "+v"(uw));
      uw ^= ut;
      u_again(uw, ut, xo);
      *(u32x4*)(sm + 16) = uw;
      BAR();
      if (w < 3) {
        const u32x4 pw = *(const u32x4*)sm;
        if (w < 2) sums<true>(smem + X_IM + 16 * tid, pw, uw); else sums<false>(smem + X_IM + 16 * tid, pw, uw);
        count_up(ca + 28);
      }
    } else {
      until(tu + UD + WD);
      BAR();
    }
    tu = (uint32_t)__builtin_amdgcn_readfirstlane(cnt[4]);
    TS(0, 3);
    __builtin_amdgcn_s_setprio(DPRIO);
    if (ffn) {
      ts1 = (tu + UD + VD2 - TSTOP) | 1u;
      gu_dma(0, NA, IC<WDEPTH>{});
      asm volatile("s_waitcnt vmcnt(%0)" : : "n"(NA - 9) : "memory");
      gsync0(ca + 4, 4);
      gu_read(0);
      asm volatile("s_waitcnt lgkmcnt(0)" ::: "memory");
      count_up(ca + 36);
      TS(0, 4);
      gu_dma(NA, 18, IC<WDEPTH2>{});
      gwait(ca + 36, 4);
      down_dma(0, KN, lane);
      asm volatile("s_waitcnt vmcnt(%0)" : : "n"(3 * KN) : "memory");
      gsync0(ca + 4, 8);
      gu_read(1);
      DRAIN();
      TS(0, 7);
    } else {
      if (!isx && C3) {
        const int n = 6 * min(tid, 191) + bj - 18, r = n >> 2;
        const char* wb = (const char*)((n & 1) ? wu : wg) + bx * 1152;
        const int c0 = (3 - (int)((uintptr_t)wb >> 7)) & 7;
        wb += (uint32_t)((r + 144 * ((r >= 144) + ((n >> 1) & 1))) * (F_ * 2) + 128 * c0);
        TOUCH(wb);
        if (c0 == 0) TOUCH(wb + 1024);
      }
      auto d_line = [&](int k) {
        const int n = min((k * 14 + bj - 18) * 64 + lane, 5183), q = n / 1296, r = n - 1296 * q;
        return (const char*)wd + (uint32_t)(bx * 5184 + (r / 18) * 72 + 18 * (HREV ? 3 - q : q) + r % 18) * 128u;
      };
      int k = w;
      if (w < HW) {
#pragma unroll 1
        for (; k < 6 && (int)((uint32_t)__builtin_amdgcn_s_memrealtime() - (tu + UD + VD2 - HSTOP)) < 0; k += HW) { TOUCH(d_line(k)); DRAIN(); }
      }
      TS(0, 4);
      until(tu + UD + VD2 + RD2);
      if (RW) gwait(ca, 4);
      if (w < HW) {
#pragma unroll 1
        for (; k < 6; k += HW) { TOUCH(d_line(k)); DRAIN(); }
      }
      TS(0, 7);
    }
    tv = tu + UD + VD2 - vearly;
    tk = tu + UD + VD2 + KD;
  }
  {
  const int tid = tid2, lane = tid & 63, i = lane & 15, kq = lane >> 4;
  __builtin_amdgcn_s_setprio(3);
  const int PL = ffn ? 6400 : LW + 6656;
  f32x2 xf[9];
  f32x4 za = {0.f, 0.f, 0.f, 0.f}, zb = za;
  uint32_t al = O_XC + 64u * (i >> 3) + 16u * kq + 128u * w;
  uint32_t ha = (ROLE == 1) ? (uint32_t)((4 * (kq & 1) + 2 * (kq >> 1) + (i >> 3)) * 80 + 16 * w + 2 * (i & 7)) : 0u;
  uint32_t h0a = (uint32_t)(min(i, 8) * 80 + 16 * kq), h1a = (uint32_t)(min(i ^ 8, 8) * 80 + 16 * kq);
  uint32_t so = O_SL + (uint32_t)fb * SP + ((ROLE == 2) ? 16u * tid - 4096u : 8192u + 16u * lane);
  uint32_t da = lb0 + (uint32_t)((8 * kq + ((lane >> 2) & 3)) * 1152 + 32 * ((8 * kq + ((lane >> 2) & 3) + 2) >> 2) + 8 * (lane & 3) + ((ROLE == 2) ? 128 * wl : 1024));
  if (ROLE == 2) asm volatile("" : "+v"(h0a), "+v"(h1a), "+v"(so), "+v"(da)); else asm volatile("" : "+v"(ha), "+v"(al), "+v"(da));
  uint32_t hv[9] = {}, nv[9] = {}, keya = 0u, keyb = 0u;
  int rka = 0, rkb = 0;
  auto put_row = [&](const uint32_t* r, int j, const void* base, uint32_t ro) {
    const u32x4 tl = {r[8], (uint32_t)DPP_I((int)r[8], 0x55, 0xf), (uint32_t)DPP_I((int)r[8], 0xaa, 0xf), (uint32_t)DPP_I((int)r[8], 0xff, 0xf)};
    asm volatile("global_store_dwordx4 %0, %1, %2 sc1\n\ts_nop 1" : : "v"(ro + 16u * lane), "v"((u32x4){r[4 * j], r[4 * j + 1], r[4 * j + 2], r[4 * j + 3]}), "s"(base) : "memory");
    if ((lane & 35) == 32 * j) asm volatile("global_store_dwordx4 %0, %1, %2 offset:1024 sc1\n\ts_nop 1" : : "v"(ro + 4u * (lane & 31)), "v"(tl), "s"(base) : "memory");
  };
  if (ln2) {
    const int po = PL - 2560 * (ROLE == 1);
    uint32_t pa = (uint32_t)(po + 9216 * wl + 32 * lane), pb = (uint32_t)(po + 9216 * wl + 2048 + 8 * (lane & 31)), ps = (lane < 32) ? 0u : 16u;
    uint32_t kid = (uint32_t)(((255 - g2) << 5) | (7 - 2 * wl - (lane >> 5)));
    const int d1 = tid - 256, ga = 0, d2 = 512 + 16 * wl + i, gb = 0;
    uint32_t vo = v_at(d1, g0 + ga), vo2 = v_at(d2, g0 + gb), px = (uint32_t)(PL - 2560 * ga + 4 * d1), py = (uint32_t)(PL - 2560 * gb + 4 * d2);
#pragma unroll
    for (int k = 0; k < 9; ++k) xf[k] = (f32x2){bflo(xv[k]), bfhi(xv[k])};
    asm volatile("" : "+v"(xf[0]), "+v"(xf[1]), "+v"(xf[2]), "+v"(xf[3]), "+v"(xf[4]), "+v"(xf[5]), "+v"(xf[6]), "+v"(xf[7]), "+v"(xf[8]));
    asm volatile("" : "+v"(za), "+v"(zb), "+v"(pa), "+v"(pb), "+v"(ps), "+v"(kid), "+v"(vo), "+v"(vo2), "+v"(px), "+v"(py));
    u32x4 ut = {utag(T, 0), utag(T, 1), utag(T, 2), utag(T, 3)};
    float sv[10], ws_ = dot2(wv[4], 0x3f803f80u, 0.f) * 0.5f;
#pragma unroll
    for (int k = 0; k < 4; ++k) ws_ = dot2(wv[k], 0x3f803f80u, ws_);
    ws_ = wave_sum(ws_);
#pragma unroll
    for (int k = 0; k < 10; ++k) sv[k] = 16.f;
    uint32_t ka_ = lb0 + LM + 4u * (128 + 48 + 2 * wl + (lane >> 5)), kb_ = lb0 + LM + 4u * (128 + 48);
    uint32_t ko_ = O_KEY + (uint32_t)(256 * (lane & 7) + 8 * (g2 & 31) + (g2 >> 5)) * 16u;
    asm volatile("" : "+v"(ut), "+v"(ws_), "+v"(sv[0]), "+v"(sv[1]), "+v"(sv[2]), "+v"(sv[3]), "+v"(sv[4]), "+v"(sv[5]), "+v"(sv[6]), "+v"(sv[7]), "+v"(sv[8]), "+v"(sv[9]),
                 "+v"(ka_), "+v"(kb_), "+v"(ko_));
    if (ffn) gwait(ca + 36, 4);
    TS(0, 7);
    until(tv);
    TS(0, 8);
    u32x4 va, vc, vb = ut;
    asm volatile("global_load_dwordx4 %0, %3, %6 sc1\n\tglobal_load_dwordx4 %1, %4, %6 sc1\n\ts_mov_b64 exec, 0xffff\n\tglobal_load_dwordx4 %2, %5, %6 sc1\n\ts_mov_b64 exec, -1\n\t"
                 "s_waitcnt vmcnt(2)" : "=&v"(va), "=&v"(vc), "+v"(vb) : "v"(vo), "v"(vo + 128 * VP), "v"(vo2), "s"(ws) : "memory");
    auto turn = [&] {
#pragma unroll
      for (int e = 0; e < 4; ++e) *(uint32_t*)(smem + px + 1024 + 9216 * e) = vc[e];
      if (lane < 16) {
#pragma unroll
        for (int e = 0; e < 4; ++e) *(uint32_t*)(smem + py + 9216 * e) = vb[e];
      }
    };
    va ^= ut;
#pragma unroll
    for (int e = 0; e < 4; ++e) *(uint32_t*)(smem + px + 9216 * e) = va[e];
    asm volatile("s_waitcnt vmcnt(0)" : "+v"(vc), "+v"(vb) : : "memory");
    vc ^= ut; vb ^= ut;
    turn();
    const u32x4 vs = va | vb | vc;
    if (__builtin_expect(__any(((vs[0] | vs[1] | vs[2] | vs[3]) & UMASK) != 0u), 0)) {
#pragma unroll 1
      for (int spin = 0; spin < 4; ++spin) {
        const bool na = ((va[0] | va[1] | va[2] | va[3]) & UMASK) != 0u, nb = ((vb[0] | vb[1] | vb[2] | vb[3]) & UMASK) != 0u, nc = ((vc[0] | vc[1] | vc[2] | vc[3]) & UMASK) != 0u;
        if (!__any(na || nb || nc)) break;
        TSC(16);
        if (na) LD16(va, vo);
        if (nb) LD16(vb, vo2);
        if (nc) LD16(vc, vo + 128 * VP);
        asm volatile("s_waitcnt vmcnt(0)" : "+v"(va), "+v"(vb), "+v"(vc) : : "memory");
        if (na) va ^= ut;
        if (nb) vb ^= ut;
        if (nc) vc ^= ut;
      }
      auto again = [&](u32x4& v, uint32_t o, int d) {
        const int J = g0 >> 5;
        const uint32_t dn = O_DN + 16u * (uint32_t)((d < 512) ? 4 * (d >> 1) + ((0x01013232 >> (4 * J)) & 3) : 4 * (d - 256) + (J >> 1));
        bool pend = ((v[0] | v[1] | v[2] | v[3]) & UMASK) != 0u;
#pragma unroll 1
        for (int spin = 0; spin < SPIN_CAP && __any(pend); ++spin) {
          TSC(16);
          SLEEP(PSLEEP);
          if (pend) {
            uint32_t fin = ~T;
            if ((v[0] & v[1] & v[2] & v[3] & LMASK) == LMASK)
              asm volatile("global_load_dword %0, %1, %2 sc1\n\ts_waitcnt vmcnt(0)" : "=&v"(fin) : "v"(dn), "s"(ws) : "memory");
            LD16W(v, o);
            v ^= ut;
            pend = ((v[0] | v[1] | v[2] | v[3]) & UMASK) != 0u;
            if (pend && fin == T) {
              u32x2 sc;
              for (int k = 0; k < SPIN_CAP; ++k) {
                asm volatile("global_load_dwordx2 %0, %1, %2 sc1\n\ts_waitcnt vmcnt(0)" : "=&v"(sc) : "v"(O_SC + 16u * d), "s"(ws) : "memory");
                if (sc[0] == T) break;
              }
              *(u16*)(smem + L_SC + 2 * d) = (u16)(sc[1] >> 16);
              cnt[10] = 1u;
              v = (v ^ ut) & ~LMASK;
              pend = false;
            }
          }
        }
      };
      again(va, vo, d1); again(vc, vo + 128 * VP, d1 + 256); again(vb, vo2, d2);
#pragma unroll
      for (int e = 0; e < 4; ++e) *(uint32_t*)(smem + px + 9216 * e) = va[e];
      turn();
    }
    u32x4 a0, a1;
    u32x2 a2, cf;
    {
      uint32_t cc, n = SPIN_CAP;
      asm volatile("s_mov_b64 exec, 1\n\tv_mov_b32 v90, 1\n\tds_add_u32 %6, v90\n\ts_mov_b64 exec, -1\n"
                   "1:\n\tds_read2_b32 %3, %6 offset1:10\n\tds_read_b128 %0, %7\n\tds_read_b128 %1, %7 offset:16\n\tds_read_b64 %2, %8\n\t"
                   "s_sub_u32 %5, %5, 1\n\ts_cbranch_scc1 2f\n\ts_waitcnt lgkmcnt(3)\n\tv_readfirstlane_b32 %4, v90\n\t"
                   "s_cmp_lt_u32 %4, 4\n\ts_cbranch_scc1 1b\n2:\n\ts_waitcnt lgkmcnt(0)"
                   : "=&v"(a0), "=&v"(a1), "=&v"(a2), "=&{v[90:91]}"(cf), "=&s"(cc), "+s"(n) : "v"(ca), "v"(lb0 + pa), "v"(lb0 + pb) : "memory", "scc");
    }
    TS(0, 9);
    if (__builtin_expect(__builtin_amdgcn_readfirstlane(cf[1]) != 0, 0)) {
      const u32x4 q = *(const u32x4*)(smem + L_SC + 16 * lane);
      const uint32_t r = *(const uint32_t*)(smem + L_SC + 1024 + 4 * (lane & 31));
#pragma unroll
      for (int k = 0; k < 4; ++k) { sv[2 * k] = bflo(q[k]); sv[2 * k + 1] = bfhi(q[k]); }
      sv[8] = bflo(r); sv[9] = bfhi(r);
    }
    const uint32_t av[10] = {a0[0], a0[1], a0[2], a0[3], a1[0], a1[1], a1[2], a1[3], a2[0] >> ps, a2[1] >> ps};
    f32x2 h[9];
#pragma unroll
    for (int k = 0; k < 10; ++k) {
      asm("v_fma_mix_f32 %0, %1, %2, %3 op_sel_hi:[1,0,0]" : "=v"(h[(k < 8) ? k >> 1 : 8][k & 1]) : "v"(av[k]), "v"(sv[k]), "v"(xf[(k < 8) ? k >> 1 : 8][k & 1]));
      if (k < 8) asm("v_fma_mix_f32 %0, %1, %2, %3 op_sel:[1,0,0] op_sel_hi:[1,0,0]" : "=v"(h[4 + (k >> 1)][k & 1]) : "v"(av[k]), "v"(sv[k]), "v"(xf[4 + (k >> 1)][k & 1]));
    }
#pragma unroll
    for (int k = 0; k < 9; ++k) hv[k] = pack2(h[k][0], h[k][1]);
    const float da = dot2(hv[2], wv[2], dot2(hv[0], wv[0], zb[0])) + dot2(hv[3], wv[3], dot2(hv[1], wv[1], zb[1]));
    const float db = dot2(hv[6], wv[2], dot2(hv[4], wv[0], zb[2])) + dot2(hv[7], wv[3], dot2(hv[5], wv[1], zb[3]));
    float dt = dot2(hv[8], wv[4], fold32(da, db));
    f32x2 rc, nc;
    ln_stats<true>(hv, lane, 1.f, xf, za, rc, nc, dt);
    dt = (lane < 32) ? dt * rc[0] + ws_ * nc[0] : dt * rc[1] + ws_ * nc[1];
    const uint32_t bb = pack2(dt, dt) & 0xffff0000u;
    const uint32_t kk = (bb ^ (((uint32_t)((int)bb >> 31) & 0x7fff0000u) | 0x80000000u)) | kid;
    u32x4 ka, kb;
    uint32_t old;
    asm volatile("s_mov_b64 exec, %5\n\tds_write_b32 %3, %4\n\ts_mov_b64 exec, 1\n\tv_mov_b32 %2, 1\n\tds_add_rtn_u32 %2, %7, %2\n\ts_mov_b64 exec, -1\n\t"
                 "ds_read_b128 %0, %6\n\tds_read_b128 %1, %6 offset:16\n\ts_waitcnt lgkmcnt(0)"
                 : "=&v"(ka), "=&v"(kb), "=&v"(old) : "v"(ka_), "v"(kk), "s"(0x8000000080000000ull), "v"(kb_), "v"(ca) : "memory");
    if (__builtin_amdgcn_readfirstlane(old) == 7) {
      uint32_t m1 = max(max(ka[0], ka[1]), ka[2]), m2 = med3u(ka[0], ka[1], ka[2]), m3 = min(min(ka[0], ka[1]), ka[2]);
#pragma unroll
      for (int e = 0; e < 5; ++e) {
        const uint32_t kx = e ? kb[e - 1] : ka[3];
        m3 = med3u(m2, m3, kx); m2 = med3u(m1, m2, kx); m1 = max(m1, kx);
      }
      if (lane < 8) ST16(ko_, ((u32x4){m1 | 24u, m2 | 16u, m3 | 8u, T & ~1u}));
    }
    TS(0, 10);
    keya = (uint32_t)__builtin_amdgcn_readlane(kk, 31); keyb = (uint32_t)__builtin_amdgcn_readlane(kk, 63);
    ln_apply<false>(xf, lane, rc, nc, nv);
    gwait(ca, 8);
    if (wl == 1) {
      const u32x4 kv = *(const u32x4*)&kl[4 * (lane & 1)];
      if (lane < 2) ST16(O_KF + (uint32_t)(2 * g2 + lane) * 16u, ((u32x4){__builtin_amdgcn_perm(kv[1], kv[0], 0x07060302u), T, __builtin_amdgcn_perm(kv[3], kv[2], 0x07060302u), T}));
      if (ROLE == 2 && b == 0 && lane == 0) ST4(O_EP, T * 0x0E8B2F51u);
    }
    const uint32_t ko = kl[lane & 7];
    rka = FORCEFB + __popcll(__ballot(ko > keya) & 0xffull); rkb = FORCEFB + __popcll(__ballot(ko > keyb) & 0xffull);
#pragma unroll
    for (int k = 0; k < 9; ++k) {
      const f32x2 o = xf[k] + (f32x2){bflo(nv[k]), bfhi(nv[k])};
      hv[k] = pack2(o[0], o[1]);
      nv[k] = min(nv[k], 0xfffffffeu);
    }
    if (rka < 2) put_row(nv, 0, ws, O_XC + (uint32_t)(1023 - 4 * g2 - rka) * 1152u);
    if (rkb < 2) put_row(nv, 1, ws, O_XC + (uint32_t)(1023 - 4 * g2 - rkb) * 1152u);
  }
  uint32_t k3rd = 0u;
  if constexpr (ROLE == 1) {
    u32x4 key;
    const uint32_t kof = O_KEY + (uint32_t)(256 * bx + tid) * 16u;
    until(tk);
    if (!ffn) SLEEP(NSLEEP);
    LD16W(key, kof);
    if (__builtin_expect(__any(key[3] != (T & ~1u)), 0)) {
#pragma unroll 1
      for (int spin = 0; spin < SPIN_CAP; ++spin) {
        const bool need = key[3] != (T & ~1u);
        if (!__any(need)) break;
        TSC(15);
        SLEEP(KBACK);
        if (need) LD16W(key, kof);
      }
    }
    TS(0, 11);
    const uint32_t m1 = oct_maxu(key[0]);
    const bool hit = key[0] == m1;
    const uint32_t k0 = hit ? key[1] : key[0], k1 = hit ? key[2] : key[1];
    const uint32_t m2 = oct_maxu(k0);
    if ((lane & 7) == 0) *(u32x2*)&cand[tid >> 2] = (u32x2){m1, m2};
    k3rd = (k0 == m2) ? k1 : k0;
    if (RPF && !ffn && w == (bj & 3)) {
      const uint32_t r1 = O_XC + ((m1 >> 3) & 0x3ffu) * 1152u + 128u * (lane & 7), r2 = O_XC + ((m2 >> 3) & 0x3ffu) * 1152u + 128u * (lane & 7);
      asm volatile("global_load_dword v126, %0, %3 sc1\n\tglobal_load_dword v126, %1, %3 sc1\n\tglobal_load_dword v126, %2, %3 offset:1024 sc1"
                   : : "v"(r1), "v"(r2), "v"(((lane & 1) ? r1 : r2) - 128u * (lane & 7)), "s"(ws) : "memory", "v126");
    }
  }
  auto pick = [&](const uint32_t* cd, uint32_t* sl) {
    BAR();
    const uint32_t c = cd[lane], own = cd[8 * w + (lane & 7)];
    const u32x4 la = *(const u32x4*)&cd[8 * w], lb = *(const u32x4*)&cd[8 * w + 4];
    uint32_t r = C_;
    asm("v_cmp_gt_u32 s[40:41], %1, %2\n\tv_cmp_gt_u32 s[42:43], %1, %3\n\tv_cmp_gt_u32 s[44:45], %1, %4\n\tv_cmp_gt_u32 s[46:47], %1, %5\n\t"
        "v_cmp_gt_u32 s[48:49], %1, %6\n\tv_cmp_gt_u32 s[50:51], %1, %7\n\tv_cmp_gt_u32 s[52:53], %1, %8\n\tv_cmp_gt_u32 s[54:55], %1, %9\n\t"
        "s_bcnt1_i32_b64 s40, s[40:41]\n\ts_bcnt1_i32_b64 s42, s[42:43]\n\tv_writelane_b32 %0, s40, 0\n\ts_bcnt1_i32_b64 s44, s[44:45]\n\t"
        "v_writelane_b32 %0, s42, 1\n\ts_bcnt1_i32_b64 s46, s[46:47]\n\tv_writelane_b32 %0, s44, 2\n\ts_bcnt1_i32_b64 s48, s[48:49]\n\t"
        "v_writelane_b32 %0, s46, 3\n\ts_bcnt1_i32_b64 s50, s[50:51]\n\tv_writelane_b32 %0, s48, 4\n\ts_bcnt1_i32_b64 s52, s[52:53]\n\t"
        "v_writelane_b32 %0, s50, 5\n\ts_bcnt1_i32_b64 s54, s[54:55]\n\tv_writelane_b32 %0, s52, 6\n\tv_writelane_b32 %0, s54, 7"
        : "+v"(r) : "v"(c), "v"(la[0]), "v"(la[1]), "v"(la[2]), "v"(la[3]), "v"(lb[0]), "v"(lb[1]), "v"(lb[2]), "v"(lb[3])
        : "scc", "s40", "s41", "s42", "s43", "s44", "s45", "s46", "s47", "s48", "s49", "s50", "s51", "s52", "s53", "s54", "s55");
    sl[min(r, (uint32_t)C_)] = own;
    BAR();
  };
  auto rows = [&](uint32_t ao, auto&& meanwhile) {
    u32x4 aa, ab, ac = __builtin_bit_cast(u32x4, zb);
    asm volatile("global_load_dwordx4 %0, %3, %4 sc1\n\tglobal_load_dwordx4 %1, %3, %4 offset:512 sc1\n\t"
                 "s_cmp_lg_u32 %5, 0\n\ts_cbranch_scc1 1f\n\tglobal_load_dwordx4 %2, %3, %4 offset:1024 sc1\n1:"
                 : "=&v"(aa), "=&v"(ab), "+v"(ac) : "v"(ao), "s"(ws), "s"(w) : "memory", "scc");
    __builtin_amdgcn_sched_barrier(0);
    meanwhile();
    asm volatile("s_waitcnt vmcnt(0)" : "+v"(aa), "+v"(ab), "+v"(ac) : : "memory");
    if (__builtin_expect(__any(max(max(max4(aa), max4(ab)), max4(ac)) == 0xffffffffu), 0)) {
#pragma unroll 1
      for (int spin = 0; spin < SPIN_CAP; ++spin) {
        const bool na = max4(aa) == 0xffffffffu, nb = max4(ab) == 0xffffffffu, nc = max4(ac) == 0xffffffffu;
        if (!__any(na || nb || nc)) break;
        TSC(14);
        SLEEP(PSLEEP);
        if (na) LD16W(aa, ao);
        if (nb) LD16W(ab, ao + 512u);
        if (nc) LD16W(ac, ao + 1024u);
      }
    }
    *(u32x4*)(smem + LA + 16 * tid) = aa;
    *(u32x4*)(smem + LA + 4096 + 16 * tid) = ab;
    if (w == 0) *(u32x4*)(smem + LA + 8192 + 16 * tid) = ac;
  };
  auto get_bd = [&](int t0, int t1) {
#pragma unroll
    for (int t = t0; t < t1; ++t) {
      const u32x2 h0 = trd(da + 512 * (t >> 2) + 32 * (t & 3)), h1 = trd(da + 512 * (t >> 2) + 32 * (t & 3) + 4608 + 32);
      bw[t] = (u32x4){h0[0], h0[1], h1[0], h1[1]};
    }
#pragma unroll
    for (int t = t0; t < t1; ++t) asm volatile("" : "+v"(bw[t]));
  };
  pick(cand, sel);
  uint32_t mykey = sel[lane & 7], ro = ((mykey >> 3) & 0x3ffu) * 1152u;
  TS(0, 12);
  auto check = [&] {
    cnt[(k3rd > (uint32_t)__builtin_amdgcn_readlane(mykey, 7)) ? 5 : 6] = 1u;
  };
  if constexpr (ROLE == 1) {
    if (ffn && !FORCEFB) rows(ro + al, check); else { check(); DRAIN(); }
  } else if (ffn) {
    if (KN < 4) { SLEEP(LSLEEP); down_dma(KN, 4, lane); } else get_bd(0, 8);
  }
  BAR();
  TS(0, 5);
  auto put_out = [&](int j, uint32_t k0, int rk) {
    const uint64_t mine = __ballot(((mykey ^ k0) & ~24u) == 0u) & 0xffull;
    if (mine == 0ull) put_row(hv, j, out, (uint32_t)((8 * g2 + 2 * wl + j) * (D_ * 2)));
    else if (rk >= 2) put_row(nv, j, ws, O_FB + 1152u * (uint32_t)__builtin_ctzll(mine));
  };
  auto rest_out = [&] { put_out(0, keya, rka); put_out(1, keyb, rkb); };
  f32x4 lo, hi;
  auto gate_up = [&] {
    u32x4 av[5];
    const uint32_t aa = lb0 + LA + 16u * lane;
#pragma unroll
    for (int j = 0; j < 5; ++j) asm volatile("ds_read_b128 %0, %1 offset:%2" : "=v"(av[j]) : "v"(aa), "n"(1024 * j) : "memory");
    lo = (f32x4){0.f, 0.f, 0.f, 0.f}; hi = lo;
#pragma unroll
    for (int j = 0; j < 9; ++j) {
      if (j < 5) asm volatile("s_waitcnt lgkmcnt(4)" : "+v"(av[j % 5]));
      else if (j == 5) asm volatile("s_waitcnt lgkmcnt(2)" : "+v"(av[0]), "+v"(av[1]));
      else if (j == 7) asm volatile("s_waitcnt lgkmcnt(0)" : "+v"(av[2]), "+v"(av[3]));
      lo = MMB(av[j % 5], bw[2 * j], lo);
      hi = MMB(av[j % 5], bw[2 * j + 1], hi);
      __builtin_amdgcn_sched_barrier(0);
      if (j < 4) asm volatile("ds_read_b128 %0, %1 offset:%2" : "=v"(av[j]) : "v"(aa), "n"(1024 * (j + 5)) : "memory");
    }
  };
  uint32_t fl;
  asm volatile("ds_read_b32 %0, %1" : "=v"(fl) : "v"(ca + 20) : "memory");
  if (ROLE == 1) gate_up();
  asm volatile("s_waitcnt lgkmcnt(0)" : "+v"(fl));
  const bool slow = FORCESLOW || FORCEFB || __builtin_amdgcn_readfirstlane(fl) != 0;
  if (__builtin_expect(slow, 0)) {
    TSC(17);
    u32x4 key;
#pragma unroll 1
    for (int spin = 0; spin < SPIN_CAP; ++spin) {
      LD16W(key, O_KF + 16u * tid);
      if (!__any(key[1] != T || key[3] != T)) break;
      SLEEP(KBACK);
    }
    const uint32_t ib = (uint32_t)(((255 - (tid >> 1)) << 5) | (7 - 4 * (tid & 1)));
    uint32_t k0 = (key[0] << 16) | ib, k1 = (key[0] & 0xffff0000u) | (ib - 1), k2 = (key[2] << 16) | (ib - 2), k3 = (key[2] & 0xffff0000u) | (ib - 3);
    CEX(k0, k1) CEX(k2, k3) CEX(k0, k2) CEX(k1, k3) CEX(k1, k2)
#pragma unroll 1
    for (int c = 0; c < C_; ++c) {
      const uint32_t mx = wave_maxu(k0);
      const bool hit = (k0 == mx);
      k0 = hit ? k1 : k0; k1 = hit ? k2 : k1; k2 = hit ? k3 : k2; k3 = hit ? 0u : k3;
      if (lane == 0) cand[64 + 8 * w + c] = mx;
    }
    pick(cand + 64, sel + 16);
    mykey = sel[16 + (lane & 7)];
    const uint32_t okey = sel[16 + (lane >> 3)];
    const uint64_t same = __ballot(((mykey ^ okey) & 0x1fe0u) == 0u && okey > mykey);
    const int sl = FORCEFB + __popcll((same >> (lane & 7)) & 0x0101010101010101ull);
    ro = (sl < 2) ? (((mykey >> 3) & 0x3ffu) + 3 - sl) * 1152u : O_FB - O_XC + 1152u * (lane & 7);
    if (ln2) rest_out();
    if (ffn) {
      if (ROLE == 1) rows(ro + al, [] {});
      BAR();
      if (ROLE == 1) gate_up();
    }
  }
  if (OPOS == 0 && ROLE == 2 && ffn && !slow) { SLEEP(OD); rest_out(); }
  if (!ffn) {
    if (ln2 && !slow) { SLEEP(OD2); rest_out(); }
    TSE();
    return;
  }
  uint32_t fx = 0, fv = 0, fn = 0, fo = 0, sa[9];
  float rs, hx, xs;
  u32x4 se;
  if constexpr (ROLE == 1) {
    const f32x4 lh = (lane < 32) ? lo : hi;
    const float fa = fold32(lh[0], lh[2]), fb2 = fold32(lh[1], lh[3]);
    const float g = __int_as_float(__builtin_amdgcn_update_dpp(__float_as_int(fa), __float_as_int(fb2), 0x128, 0xf, 0xc, false));
    const float u = __int_as_float(__builtin_amdgcn_update_dpp(__float_as_int(fb2), __float_as_int(fa), 0x128, 0xf, 0x3, false));
    const uint32_t gu = pack2(g, u);
    const float gp = bflo(gu);
    const float sg = rbf(gp * __builtin_amdgcn_rcpf(1.f + __expf(-gp)));
    *(u16*)((char*)hm + ha) = f2bf(sg * bfhi(gu));
  } else if (KN < 4) {
    DRAIN();
    gsync0(ca, 12);
    get_bd(0, 8);
  }
  BAR();
  TS(0, 6);
  if (ROLE == 2 || w == 2) {
    const u32x4 a0 = *(const u32x4*)((char*)hm + h0a), a1 = *(const u32x4*)((char*)hm + h1a);
    if (ROLE == 1) get_bd(0, 4);
    const f32x4 z = {0.f, 0.f, 0.f, 0.f};
    f32x4 o[4];
#pragma unroll
    for (int t = 0; t < 2 * ROLE; ++t) o[t] = MMB(a1, bw[2 * t + 1], MMB(a0, bw[2 * t], z));
#pragma unroll
    for (int t = 0; t < ROLE; ++t) {
      u32x4 sv;
#pragma unroll
      for (int r = 0; r < 4; ++r) sv[r] = min(pack2(o[2 * t][r], o[2 * t + 1][r]), 0xfffffffeu);
      ST16(so + 4096u * t, sv);
    }
    TS(0, 13);
  }
  if (OPOS == 1 && ROLE == 2) {
    SLEEP(OSLEEP);
    if (!slow) rest_out();
  }
  if (ROLE == 1 && w == 1) {
    const int q = (fb & 15) >> 2, ts = 4 * (q & 1) + ((i & 7) >> 1);
    const int dd = 64 * (fb >> 4) + 16 * (2 * (i & 1) + (q >> 1)) + 4 * (fb & 3) + 2 * (kq & 1) + (kq >> 1);
    const uint32_t fkey = (uint32_t)__builtin_amdgcn_ds_bpermute(4 * ts, (int)mykey), fr = (uint32_t)__builtin_amdgcn_ds_bpermute(4 * ts, (int)ro);
    const int ft = 2047 - (int)(((fkey >> 2) & 0x7f8u) | (fkey & 7u));
    fo = (uint32_t)(ft * D_ + dd) * 2u;
    u32x2 fs;
    asm volatile("global_load_ushort %0, %4, %8\n\tglobal_load_ushort %1, %5, %9 sc1\n\tglobal_load_ushort %2, %6, %9 sc1\n\tglobal_load_dwordx2 %3, %7, %9 sc1"
                 : "=&v"(fx), "=&v"(fv), "=&v"(fn), "=&v"(fs) : "v"(fo), "v"(v_at(dd, ft >> 3) + 2u * (ft & 7)), "v"(O_XC + fr + 2u * dd), "v"(O_SC + 16u * dd), "s"(x), "s"(ws) : "memory");
    uint32_t t16 = (((T >> (3 * ((ft >> 1) & 3))) & LMASK) | (((T >> (12 + ((ft >> 1) & 3))) & 0x00010001u) << 14)) >> (16 * (ft & 1)) & 0xffffu;
    const uint32_t o = fkey >> 16;
    const float rs0 = bf2f((u16)((o & 0x8000u) ? (o & 0x7fffu) : (~o & 0xffffu)));
    rs = rbf(__builtin_amdgcn_rcpf(1.f + __expf(-rs0)));
    const uint32_t one = (i < 8) ? (0x3f80u << (16 * (i & 1))) : 0u;
    se = (u32x4){(i >> 1) == 0 ? one : 0u, (i >> 1) == 1 ? one : 0u, (i >> 1) == 2 ? one : 0u, (i >> 1) == 3 ? one : 0u};
#pragma unroll
    for (int m = 0; m < 9; ++m) sa[m] = O_SL + (uint32_t)(16 * m + (lane >> 2)) * SP + (uint32_t)(4 * fb + (lane & 3)) * 16u;
    asm volatile("" : "+v"(t16), "+v"(rs), "+v"(se), "+v"(sa[0]), "+v"(sa[1]), "+v"(sa[2]), "+v"(sa[3]), "+v"(sa[4]), "+v"(sa[5]), "+v"(sa[6]), "+v"(sa[7]), "+v"(sa[8]));
    SLEEP(SSLEEP);
    TS(0, 1);
    float sum;
#pragma unroll 1
    for (int spin = 0; spin < SPIN_CAP; ++spin) {
      u32x4 sv[9];
#pragma unroll
      for (int m = 0; m < 9; ++m) LD16(sv[m], sa[m]);
      asm volatile("s_waitcnt vmcnt(9)" : "+v"(fx), "+v"(fv), "+v"(fn), "+v"(fs));
      if (__builtin_expect(__any(fs[0] != T), 0)) {
        for (int k = 0; k < SPIN_CAP && fs[0] != T; ++k)
          asm volatile("global_load_dwordx2 %0, %1, %2 sc1\n\ts_waitcnt vmcnt(0)" : "=&v"(fs) : "v"(O_SC + 16u * dd), "s"(ws) : "memory");
      }
      const uint32_t f1 = fv ^ t16, f2 = (f1 & 7u) ? fv & ~7u : f1;
      hx = rbf(bf2f((u16)fx) + (float)__builtin_bit_cast(_Float16, (u16)f2) * __uint_as_float(fs[1]));
      xs = bf2f((u16)fn);
      asm volatile("" : "+v"(hx), "+v"(xs));
      f32x4 dv = {0.f, 0.f, 0.f, 0.f};
      static_for<0, 9>([&](auto mm) {
        constexpr int m = mm.value;
        asm volatile("s_waitcnt vmcnt(%1)" : "+v"(sv[m]) : "n"(8 - m));
        dv = MMB(sv[m], se, dv);
        __builtin_amdgcn_sched_barrier(0);
      });
      sum = fold16(fold32(dv[0], dv[1]), fold32(dv[2], dv[3]));
      if (__builtin_expect(!__any(sum != sum), 1)) break;
      uint32_t mx = 0u;
#pragma unroll
      for (int m = 0; m < 9; ++m) mx = max(mx, max4(sv[m]));
      if (!__any(mx == 0xffffffffu)) break;
      TSC(18);
      SLEEP(PSLEEP);
    }
    const float o1 = rbf(xs + rbf(rbf(sum) * rs));
    if (i < 8) asm volatile("global_store_short %0, %1, %2 sc1" : : "v"(fo), "v"((uint32_t)f2bf(hx + o1)), "s"(out) : "memory");
    TS(0, 2);
  }
  TSE();
  }
  }
}
#define BROT "192"
#define XLIM "0x100"
#define XNUM "0"
extern "C" __global__ __launch_bounds__(512, 4) void kf_all(KARGS) {
  __shared__ __attribute__((aligned(16))) char smem[SMEM];
  asm volatile("v_readfirstlane_b32 s98, v0\n\ts_load_dwordx16 s[64:79], s[0:1], 0x0\n\ts_load_dwordx4 s[80:83], s[0:1], 0x40\n\t"
               "s_add_u32 s97, s2, " BROT "\n\ts_and_b32 s97, s97, 0xff\n\ts_and_b32 s2, s2, 0x100\n\ts_or_b32 s2, s2, s97\n\t"
               "s_cmpk_lt_u32 s2, 0x100\n\ts_cbranch_scc0 1f\n\ts_cmpk_lt_u32 s98, 0x100\n\ts_cbranch_scc0 2f\n"
               "7:\n\ts_and_b32 s84, s97, 0xe0\n\ts_bfe_u32 s85, s97, 0x20003\n\ts_or_b32 s84, s84, s85\n\ts_and_b32 s85, s97, 7\n\ts_lshl2_add_u32 s84, s85, s84\n\t"
               "s_mulk_i32 s84, 0x2400\n\ts_and_b32 s96, s98, 0xc0\n\ts_mulk_i32 s96, 36\n\ts_add_u32 s84, s84, s96\n\tv_and_b32 v110, 63, v0\n\tv_and_b32 v111, 32, v0\n\t"
               "v_lshlrev_b32 v111, 5, v111\n\tv_lshl_add_u32 v111, v110, 2, v111\n\tv_lshl_add_u32 v110, v110, 4, s84\n\tv_add_u32 v111, s84, v111\n\t"
               "v_mov_b32 v109, 0\n\ts_cmpk_lt_u32 s2, " XLIM "\n\ts_waitcnt lgkmcnt(0)\n\ts_cbranch_scc0 8f\n\t"
               "global_load_dwordx4 v[100:103], v110, s[64:65]\n\tglobal_load_dwordx4 v[104:107], v110, s[64:65] offset:1152\n\t"
               "global_load_dword v108, v111, s[64:65] offset:1024\n8:\n\tglobal_load_dword v109, v109, s[82:83] sc1\n\t"
               "s_cmp_lg_u32 s97, s2\n\ts_cbranch_scc1 6f\n\ts_cmpk_lt_u32 s2, 0x90\n\ts_cbranch_scc1 kf_n\n\ts_branch kf_h\n"
               "1:\n\ts_bfe_u32 s84, s2, 0x50003\n\ts_and_b32 s85, s2, 7\n\ts_lshl_b32 s85, s85, 5\n\ts_or_b32 s84, s84, s85\n\t"
               "s_lshl_b32 s85, s84, 9\n\ts_lshl_b32 s84, s84, 2\n\tv_and_b32 v110, 0xff, v0\n\tv_bfe_u32 v107, v0, 7, 1\n\t"
               "v_lshl_add_u32 v110, v110, 1, s85\n\tv_lshl_add_u32 v107, v107, 1, s84\n\tv_mov_b32 v109, 0\n\ts_cmpk_lt_u32 s98, 0x100\n\ts_waitcnt lgkmcnt(0)\n\t"
               "global_load_ushort v100, v107, s[78:79]\n\tglobal_load_ushort v101, v110, s[66:67]\n\tglobal_load_ushort v102, v110, s[68:69]\n\t"
               "s_cbranch_scc0 3f\n"
               "4:\n\tglobal_load_ushort v103, v110, s[70:71]\n\tglobal_load_ushort v104, v110, s[72:73]\n\tglobal_load_ushort v105, v110, s[74:75]\n\t"
               "global_load_ushort v106, v110, s[76:77]\n\tglobal_load_ushort v107, v107, s[80:81]\n\tglobal_load_dword v109, v109, s[82:83] sc1\n\ts_cbranch_scc1 kf_c\n\ts_barrier\n\ts_branch kf_p\n"
               "3:\n\tglobal_load_dword v109, v109, s[82:83] sc1\n\ts_branch kf_u\n"
               "2:\n\ts_cmpk_lt_u32 s2, 0xc0\n\ts_cbranch_scc1 5f\n\t"
               "s_lshr_b32 s84, s2, 3\n\ts_and_b32 s85, s2, 7\n\ts_lshl3_add_u32 s84, s85, s84\n\ts_addk_i32 s84, 488\n\ts_lshl_b32 s85, s84, 8\n\ts_lshl_b32 s84, s84, 1\n\t"
               "v_and_b32 v110, 0x7f, v0\n\tv_lshl_add_u32 v110, v110, 1, s85\n\tv_mov_b32 v107, s84\n\tv_mov_b32 v109, 0\n\ts_cmp_eq_u32 s2, 0\n\ts_waitcnt lgkmcnt(0)\n\t"
               "global_load_ushort v100, v107, s[78:79]\n\tglobal_load_ushort v101, v110, s[66:67]\n\tglobal_load_ushort v102, v110, s[68:69]\n\ts_branch 4b\n"
               "5:\n\ts_add_u32 s97, s2, 160\n\ts_sub_u32 s99, s2, 32\n\ts_cmpk_lt_u32 s99, " XNUM "\n\ts_cbranch_scc1 7b\n\t"
               "s_cmpk_lt_u32 s2, 32\n\ts_cselect_b32 s99, 0, 64\n\ts_sub_u32 s99, s2, s99\n\t"
               "s_and_b32 s99, s99, 0x78\n\ts_lshl_b32 s99, s99, 5\n\ts_and_b32 s98, s98, 0xc0\n\ts_or_b32 s98, s98, s99\n\ts_lshl_b32 s98, s98, 1\n\t"
               "s_getpc_b64 s[96:97]\n\ts_add_u32 s96, s96, s98\n\ts_addc_u32 s97, s97, 0\n\t"
               "s_load_dword s99, s[96:97], 0x0\n\ts_load_dword s99, s[96:97], 0x40\n\ts_load_dword s99, s[96:97], 0x2000\n\ts_load_dword s99, s[96:97], 0x2040\n\t"
               "s_load_dword s99, s[96:97], 0x4000\n\ts_load_dword s99, s[96:97], 0x4040\n\ts_load_dword s99, s[96:97], 0x6000\n\ts_load_dword s99, s[96:97], 0x6040\n\t"
               "s_load_dword s99, s[96:97], 0x8000\n\ts_load_dword s99, s[96:97], 0x8040\n\t"
               "s_cmp_gt_i32 s98, kf_end-kf_all-0xa300\n\ts_cbranch_scc1 6f\n\t"
               "s_load_dword s99, s[96:97], 0xa000\n\ts_load_dword s99, s[96:97], 0xa040\n\t"
               "s_cmp_gt_i32 s98, kf_end-kf_all-0xc300\n\ts_cbranch_scc1 6f\n\t"
               "s_load_dword s99, s[96:97], 0xc000\n\ts_load_dword s99, s[96:97], 0xc040\n6:\n\ts_barrier\n\ts_branch kf_p"
               : : "s"(__builtin_amdgcn_kernarg_segment_ptr()), "s"(blockIdx.x), "v"(threadIdx.x),
                   "s"((uint32_t)(uintptr_t)(__attribute__((address_space(3))) char*)smem) : "scc", "s96", "s97", "s98", "s99", "v127");
}
extern "C" __global__ __launch_bounds__(512, 4) void kf_c(KARGS) {
  __shared__ __attribute__((aligned(16))) char smem[SMEM];
  role_body<0>(smem, KPASS);
}
extern "C" __global__ __launch_bounds__(512, 4) void kf_u(KARGS) {
  __shared__ __attribute__((aligned(16))) char smem[SMEM];
  role_body<3>(smem, KPASS);
}
extern "C" __global__ __launch_bounds__(512, 4) void kf_n(KARGS) {
  __shared__ __attribute__((aligned(16))) char smem[SMEM];
  role_body<1, 1>(smem, KPASS);
}
extern "C" __global__ __launch_bounds__(512, 4) void kf_h(KARGS) {
  __shared__ __attribute__((aligned(16))) char smem[SMEM];
  role_body<1, 0>(smem, KPASS);
}
extern "C" __global__ __launch_bounds__(512, 4) void kf_p(KARGS) {
  __shared__ __attribute__((aligned(16))) char smem[SMEM];
  role_body<2>(smem, KPASS);
}
extern "C" __global__ __launch_bounds__(512, 4) void kf_conv() { asm volatile(CONV_ASM ::: CONV_CLOB); }
extern "C" __global__ void kf_end() {}

extern "C" void kernel_launch(void* const* d_in, const int* in_sizes, int n_in,
                              void* d_out, int out_size, void* d_ws, size_t ws_size,
                              hipStream_t stream) {
  kf_all<<<512, 512, 0, stream>>>((const u16*)d_in[0], (const u16*)d_in[1], (const u16*)d_in[2], (const u16*)d_in[3], (const u16*)d_in[4],
                                  (const u16*)d_in[5], (const u16*)d_in[6], (const u16*)d_in[7], (const u16*)d_in[8], (char*)d_ws + (size_t)WSOFF * 1048576,
                                  (const u16*)d_in[10], (const u16*)d_in[11], (const u16*)d_in[12], (const u16*)d_in[13], (u16*)d_out);
}
```

```cpp
#include <hip/hip_runtime.h>
#include <hip/hip_bf16.h>
#include <stdint.h>$UNK

typedef __attribute__((ext_vector_type(8))) short bf16x8;
typedef __attribute__((ext_vector_type(4))) float f32x4;
typedef uint16_t u16;
typedef __attribute__((ext_vector_type(4))) uint32_t u32x4;
typedef __attribute__((ext_vector_type(2))) uint32_t u32x2;
#define TSD
#define TS(k, i)
#define TSC(i)
#define TSE()
#define TSP
#define TSA

constexpr int L_ = 2048, D_ = 576, N_ = 128, F_ = 4608, C_ = 8;
constexpr int NF = 144;
constexpr uint32_t O_EP = 0;
constexpr uint32_t O_KF = 4096;
constexpr uint32_t O_KEY = O_KF + 8192;
constexpr uint32_t O_SC = O_KEY + 32768;
constexpr uint32_t O_DN = O_SC + 9216;
constexpr uint32_t O_SU = O_DN + 20480;
constexpr uint32_t O_FB = O_SU + 262144;
constexpr uint32_t O_XC = O_FB + 8 * 1152;
constexpr uint32_t SP = 9216 + 128;
constexpr uint32_t O_SL = O_XC + 1024 * 1152;
constexpr uint32_t SLU = 336;
constexpr uint32_t O_U = O_SL + 256 * SLU * 16;
constexpr int UP = 584;
constexpr uint32_t VP = 8192 + 128, VPP = 4096 + 128;
constexpr uint32_t O_V = O_U + 256 * UP * 16, O_VP = O_V + 256 * VP;
static_assert(256 * SLU * 16 >= NF * SP && O_SL % 128 == 0 && O_U % 128 == 0 && O_V % 128 == 0, "");
__host__ __device__ __forceinline__ uint32_t u_at(int tg, int d) { return O_U + 16u * (uint32_t)((tg >> 1) * (UP * 2) + (d >> 2) * 8 + (tg & 1) * 4 + (d & 3)); }
__host__ __device__ __forceinline__ uint32_t v_at(int d, int tg) {
  return (d < 512) ? O_V + (uint32_t)(d >> 1) * VP + 16u * (uint32_t)(2 * tg + (d & 1)) : O_VP + (uint32_t)(d - 512) * VPP + 16u * (uint32_t)tg;
}
#define HAVE_VAT
#define HAVE_VTAG
__device__ __forceinline__ float bf2f(u16 v) { return __uint_as_float((uint32_t)v << 16); }
__device__ __forceinline__ float bflo(uint32_t v) { return __uint_as_float(v << 16); }
__device__ __forceinline__ float bfhi(uint32_t v) { return __uint_as_float(v & 0xffff0000u); }
__device__ __forceinline__ u16 f2bf(float f) { return __builtin_bit_cast(u16, (__bf16)f); }
__device__ __forceinline__ float rbf(float f) { return bf2f(f2bf(f)); }
typedef __attribute__((ext_vector_type(2))) float f32x2;
typedef __attribute__((ext_vector_type(2))) __bf16 bf16x2v;
__device__ __forceinline__ uint32_t pack2(float a, float b) {
  return __builtin_bit_cast(uint32_t, __builtin_convertvector((f32x2){a, b}, bf16x2v));
}

#define DPP_I(v, ctrl, rmask) __builtin_amdgcn_update_dpp(0, (v), (ctrl), (rmask), 0xf, true)
__device__ __forceinline__ float row_sum(float v) {
  v += __int_as_float(DPP_I(__float_as_int(v), 0x111, 0xf));
  v += __int_as_float(DPP_I(__float_as_int(v), 0x112, 0xf));
  v += __int_as_float(DPP_I(__float_as_int(v), 0x114, 0xf));
  v += __int_as_float(DPP_I(__float_as_int(v), 0x118, 0xf));
  return v;
}
__device__ __forceinline__ float rdl(float v, int l) { return __int_as_float(__builtin_amdgcn_readlane(__float_as_int(v), l)); }
__device__ __forceinline__ float wave_sum(float v) {
  v = row_sum(v);
  asm("s_nop 1\n\tv_add_f32_dpp %0, %0, %0 row_bcast:15 row_mask:0xa bank_mask:0xf\n\t"
      "s_nop 1\n\tv_add_f32_dpp %0, %0, %0 row_bcast:31 row_mask:0xc bank_mask:0xf\n\ts_nop 0" : "+v"(v));
  return rdl(v, 63);
}
__device__ __forceinline__ float fold32(float a, float b) {
  const auto r = __builtin_amdgcn_permlane32_swap(__float_as_uint(a), __float_as_uint(b), false, false);
  return __uint_as_float(r[0]) + __uint_as_float(r[1]);
}
__device__ __forceinline__ float fold16(float a, float b) {
  const auto r = __builtin_amdgcn_permlane16_swap(__float_as_uint(a), __float_as_uint(b), false, false);
  return __uint_as_float(r[0]) + __uint_as_float(r[1]);
}
__device__ __forceinline__ void wave_sum2(float& a, float& b) {
  float v = row_sum(fold32(a, b));
  asm("s_nop 1\n\tv_add_f32_dpp %0, %0, %0 row_bcast:15 row_mask:0xa bank_mask:0xf\n\ts_nop 0" : "+v"(v));
  a = rdl(v, 31); b = rdl(v, 63);
}
__device__ __forceinline__ void wave_sum4(float& a, float& b, float& c, float& d) {
  const float v = row_sum(fold16(fold32(a, b), fold32(c, d)));
  a = rdl(v, 15); c = rdl(v, 31); b = rdl(v, 47); d = rdl(v, 63);
}
__device__ __forceinline__ float dot2(uint32_t a, uint32_t b, float c) {
  return __builtin_amdgcn_fdot2_f32_bf16(__builtin_bit_cast(bf16x2v, a), __builtin_bit_cast(bf16x2v, b), c, false);
}
__device__ __forceinline__ uint32_t row_maxu(uint32_t v) {
  v = max(v, (uint32_t)DPP_I((int)v, 0xb1, 0xf));
  v = max(v, (uint32_t)DPP_I((int)v, 0x4e, 0xf));
  v = max(v, (uint32_t)DPP_I((int)v, 0x141, 0xf));
  v = max(v, (uint32_t)DPP_I((int)v, 0x140, 0xf));
  return v;
}
__device__ __forceinline__ uint32_t wave_maxu(uint32_t v) {
  v = max(v, (uint32_t)DPP_I((int)v, 0x111, 0xf));
  v = max(v, (uint32_t)DPP_I((int)v, 0x112, 0xf));
  v = max(v, (uint32_t)DPP_I((int)v, 0x114, 0xf));
  v = max(v, (uint32_t)DPP_I((int)v, 0x118, 0xf));
  v = max(v, (uint32_t)DPP_I((int)v, 0x142, 0xa));
  v = max(v, (uint32_t)DPP_I((int)v, 0x143, 0xc));
  return (uint32_t)__builtin_amdgcn_readlane((int)v, 63);
}
__device__ __forceinline__ void wave_lds_sync() {
  __builtin_amdgcn_s_waitcnt(0xc07f);
  __builtin_amdgcn_wave_barrier();
}

typedef __attribute__((ext_vector_type(8))) _Float16 f16x8;
typedef __attribute__((ext_vector_type(2))) _Float16 f16x2;
__device__ __forceinline__ uint32_t packh(f32x2 v) { return __builtin_bit_cast(uint32_t, __builtin_convertvector(v, f16x2)); }
__device__ __forceinline__ uint32_t packh(float a, float b) { return packh((f32x2){a, b}); }
constexpr int IP = 224, I_X = 128;
__device__ __forceinline__ f32x2 cmul(f32x2 z, f32x2 w) {
  f32x2 r;
  asm("v_pk_mul_f32 %0, %1, %2 op_sel_hi:[0,1]\n\tv_pk_fma_f32 %0, %1, %2, %0 op_sel:[1,1,0] op_sel_hi:[1,0,1] neg_lo:[0,1,0]" : "=&v"(r) : "v"(z), "v"(w));
  return r;
}
__device__ __forceinline__ f32x2 cexp2(float ar2, float air, float l) {
  const float t = air * l;
  return (f32x2){__builtin_amdgcn_cosf(t), __builtin_amdgcn_sinf(t)} * __builtin_amdgcn_exp2f(ar2 * l);
}
__device__ __forceinline__ float state_a(float ldt, float are, float aim, float& ar2, float& air, float& g) {
  const float dt = rbf(__expf(ldt));
  ar2 = dt * are * 1.4426950408889634f;
  air = dt * aim * 0.15915494309189535f;
  air -= __builtin_rintf(air);
  g = 0.f;
  if (__builtin_expect(__any(ar2 > 0.f), 0)) g = fminf(__uint_as_float(wave_maxu(__float_as_uint(fmaxf(ar2, 0.f)))), 1.f);
  return dt;
}
template <int N> __device__ __forceinline__ void put_im(char* p, int im, const f32x2* z) {
  *(u32x4*)p = (u32x4){packh(z[0][0], z[1][0]), packh(z[2][0], z[3][0]), packh(z[4][0], z[5][0]), packh(z[6][0], z[7][0])};
  *(u32x4*)(p + im) = (u32x4){packh(z[0][1], z[1][1]), packh(z[2][1], z[3][1]), packh(z[4][1], z[5][1]), packh(z[6][1], z[7][1])};
}
#define Y_ASM \
  "v_mov_b64 v[40:41], %0\nv_pk_mul_f32 v[42:43], v[40:41], %1 op_sel_hi:[0,1]\nv_pk_mul_f32 v[44:45], %0, %2 op_sel_hi:[0,1]\n" \
  "v_pk_fma_f32 v[42:43], v[40:41], %1, v[42:43] op_sel:[1,1,0] op_sel_hi:[1,0,1] neg_lo:[0,1,0]\n" \
  "v_pk_fma_f32 v[44:45], %0, %2, v[44:45] op_sel:[1,1,0] op_sel_hi:[1,0,1] neg_lo:[0,1,0]\nv_cvt_pk_f16_f32 v56, v40, v42\n" \
  "v_cvt_pk_f16_f32 v60, v41, v43\nv_pk_mul_f32 v[40:41], v[42:43], %1 op_sel_hi:[0,1]\nv_pk_mul_f32 v[48:49], v[44:45], %2 op_sel_hi:[0,1]\n" \
  "v_pk_fma_f32 v[40:41], v[42:43], %1, v[40:41] op_sel:[1,1,0] op_sel_hi:[1,0,1] neg_lo:[0,1,0]\n" \
  "v_pk_fma_f32 v[48:49], v[44:45], %2, v[48:49] op_sel:[1,1,0] op_sel_hi:[1,0,1] neg_lo:[0,1,0]\n" \
  "v_pk_mul_f32 v[42:43], v[40:41], %1 op_sel_hi:[0,1]\nv_pk_mul_f32 v[52:53], v[48:49], %2 op_sel_hi:[0,1]\n" \
  "v_pk_fma_f32 v[42:43], v[40:41], %1, v[42:43] op_sel:[1,1,0] op_sel_hi:[1,0,1] neg_lo:[0,1,0]\n" \
  "v_pk_fma_f32 v[52:53], v[48:49], %2, v[52:53] op_sel:[1,1,0] op_sel_hi:[1,0,1] neg_lo:[0,1,0]\nv_cvt_pk_f16_f32 v57, v40, v42\n" \
  "v_cvt_pk_f16_f32 v61, v41, v43\nv_pk_mul_f32 v[40:41], v[42:43], %1 op_sel_hi:[0,1]\n" \
  "v_pk_fma_f32 v[40:41], v[42:43], %1, v[40:41] op_sel:[1,1,0] op_sel_hi:[1,0,1] neg_lo:[0,1,0]\n" \
  "v_pk_mul_f32 v[42:43], v[40:41], %1 op_sel_hi:[0,1]\nv_pk_mul_f32 v[46:47], v[44:45], %1 op_sel_hi:[0,1]\n" \
  "v_pk_fma_f32 v[42:43], v[40:41], %1, v[42:43] op_sel:[1,1,0] op_sel_hi:[1,0,1] neg_lo:[0,1,0]\n" \
  "v_pk_fma_f32 v[46:47], v[44:45], %1, v[46:47] op_sel:[1,1,0] op_sel_hi:[1,0,1] neg_lo:[0,1,0]\nv_cvt_pk_f16_f32 v58, v40, v42\n" \
  "v_cvt_pk_f16_f32 v62, v41, v43\nv_cvt_pk_f16_f32 v64, v44, v46\nv_cvt_pk_f16_f32 v68, v45, v47\n" \
  "v_pk_mul_f32 v[40:41], v[42:43], %1 op_sel_hi:[0,1]\nv_pk_mul_f32 v[44:45], v[46:47], %1 op_sel_hi:[0,1]\n" \
  "v_pk_fma_f32 v[40:41], v[42:43], %1, v[40:41] op_sel:[1,1,0] op_sel_hi:[1,0,1] neg_lo:[0,1,0]\n" \
  "v_pk_fma_f32 v[44:45], v[46:47], %1, v[44:45] op_sel:[1,1,0] op_sel_hi:[1,0,1] neg_lo:[0,1,0]\n" \
  "v_pk_mul_f32 v[42:43], v[40:41], %1 op_sel_hi:[0,1]\nv_pk_mul_f32 v[46:47], v[44:45], %1 op_sel_hi:[0,1]\n" \
  "v_pk_fma_f32 v[42:43], v[40:41], %1, v[42:43] op_sel:[1,1,0] op_sel_hi:[1,0,1] neg_lo:[0,1,0]\n" \
  "v_pk_fma_f32 v[46:47], v[44:45], %1, v[46:47] op_sel:[1,1,0] op_sel_hi:[1,0,1] neg_lo:[0,1,0]\nv_cvt_pk_f16_f32 v59, v40, v42\n" \
  "v_cvt_pk_f16_f32 v63, v41, v43\nv_cvt_pk_f16_f32 v65, v44, v46\nv_cvt_pk_f16_f32 v69, v45, v47\n" \
  "v_pk_mul_f32 v[44:45], v[46:47], %1 op_sel_hi:[0,1]\nds_write_b128 %3, v[56:59] offset:0\n" \
  "v_pk_fma_f32 v[44:45], v[46:47], %1, v[44:45] op_sel:[1,1,0] op_sel_hi:[1,0,1] neg_lo:[0,1,0]\nds_write_b128 %3, v[60:63] offset:64\n" \
  "v_pk_mul_f32 v[46:47], v[44:45], %1 op_sel_hi:[0,1]\nv_pk_mul_f32 v[50:51], v[48:49], %1 op_sel_hi:[0,1]\n" \
  "v_pk_fma_f32 v[46:47], v[44:45], %1, v[46:47] op_sel:[1,1,0] op_sel_hi:[1,0,1] neg_lo:[0,1,0]\n" \
  "v_pk_fma_f32 v[50:51], v[48:49], %1, v[50:51] op_sel:[1,1,0] op_sel_hi:[1,0,1] neg_lo:[0,1,0]\nv_cvt_pk_f16_f32 v66, v44, v46\n" \
  "v_cvt_pk_f16_f32 v70, v45, v47\nv_cvt_pk_f16_f32 v56, v48, v50\nv_cvt_pk_f16_f32 v60, v49, v51\n" \
  "v_pk_mul_f32 v[44:45], v[46:47], %1 op_sel_hi:[0,1]\nv_pk_mul_f32 v[48:49], v[50:51], %1 op_sel_hi:[0,1]\n" \
  "v_pk_fma_f32 v[44:45], v[46:47], %1, v[44:45] op_sel:[1,1,0] op_sel_hi:[1,0,1] neg_lo:[0,1,0]\n" \
  "v_pk_fma_f32 v[48:49], v[50:51], %1, v[48:49] op_sel:[1,1,0] op_sel_hi:[1,0,1] neg_lo:[0,1,0]\n" \
  "v_pk_mul_f32 v[46:47], v[44:45], %1 op_sel_hi:[0,1]\nv_pk_mul_f32 v[50:51], v[48:49], %1 op_sel_hi:[0,1]\n" \
  "v_pk_fma_f32 v[46:47], v[44:45], %1, v[46:47] op_sel:[1,1,0] op_sel_hi:[1,0,1] neg_lo:[0,1,0]\n" \
  "v_pk_fma_f32 v[50:51], v[48:49], %1, v[50:51] op_sel:[1,1,0] op_sel_hi:[1,0,1] neg_lo:[0,1,0]\nv_cvt_pk_f16_f32 v67, v44, v46\n" \
  "v_cvt_pk_f16_f32 v71, v45, v47\nv_cvt_pk_f16_f32 v57, v48, v50\nv_cvt_pk_f16_f32 v61, v49, v51\n" \
  "v_pk_mul_f32 v[48:49], v[50:51], %1 op_sel_hi:[0,1]\nds_write_b128 %3, v[64:67] offset:16\n" \
  "v_pk_fma_f32 v[48:49], v[50:51], %1, v[48:49] op_sel:[1,1,0] op_sel_hi:[1,0,1] neg_lo:[0,1,0]\nds_write_b128 %3, v[68:71] offset:80\n" \
  "v_pk_mul_f32 v[50:51], v[48:49], %1 op_sel_hi:[0,1]\nv_pk_mul_f32 v[54:55], v[52:53], %1 op_sel_hi:[0,1]\n" \
  "v_pk_fma_f32 v[50:51], v[48:49], %1, v[50:51] op_sel:[1,1,0] op_sel_hi:[1,0,1] neg_lo:[0,1,0]\n" \
  "v_pk_fma_f32 v[54:55], v[52:53], %1, v[54:55] op_sel:[1,1,0] op_sel_hi:[1,0,1] neg_lo:[0,1,0]\nv_cvt_pk_f16_f32 v58, v48, v50\n" \
  "v_cvt_pk_f16_f32 v62, v49, v51\nv_cvt_pk_f16_f32 v64, v52, v54\nv_cvt_pk_f16_f32 v68, v53, v55\n" \
  "v_pk_mul_f32 v[48:49], v[50:51], %1 op_sel_hi:[0,1]\nv_pk_mul_f32 v[52:53], v[54:55], %1 op_sel_hi:[0,1]\n" \
  "v_pk_fma_f32 v[48:49], v[50:51], %1, v[48:49] op_sel:[1,1,0] op_sel_hi:[1,0,1] neg_lo:[0,1,0]\n" \
  "v_pk_fma_f32 v[52:53], v[54:55], %1, v[52:53] op_sel:[1,1,0] op_sel_hi:[1,0,1] neg_lo:[0,1,0]\n" \
  "v_pk_mul_f32 v[50:51], v[48:49], %1 op_sel_hi:[0,1]\nv_pk_mul_f32 v[54:55], v[52:53], %1 op_sel_hi:[0,1]\n" \
  "v_pk_fma_f32 v[50:51], v[48:49], %1, v[50:51] op_sel:[1,1,0] op_sel_hi:[1,0,1] neg_lo:[0,1,0]\n" \
  "v_pk_fma_f32 v[54:55], v[52:53], %1, v[54:55] op_sel:[1,1,0] op_sel_hi:[1,0,1] neg_lo:[0,1,0]\nv_cvt_pk_f16_f32 v59, v48, v50\n" \
  "v_cvt_pk_f16_f32 v63, v49, v51\nv_cvt_pk_f16_f32 v65, v52, v54\nv_cvt_pk_f16_f32 v69, v53, v55\n" \
  "v_pk_mul_f32 v[52:53], v[54:55], %1 op_sel_hi:[0,1]\nds_write_b128 %3, v[56:59] offset:32\n" \
  "v_pk_fma_f32 v[52:53], v[54:55], %1, v[52:53] op_sel:[1,1,0] op_sel_hi:[1,0,1] neg_lo:[0,1,0]\nds_write_b128 %3, v[60:63] offset:96\n" \
  "v_pk_mul_f32 v[54:55], v[52:53], %1 op_sel_hi:[0,1]\n" \
  "v_pk_fma_f32 v[54:55], v[52:53], %1, v[54:55] op_sel:[1,1,0] op_sel_hi:[1,0,1] neg_lo:[0,1,0]\nv_cvt_pk_f16_f32 v66, v52, v54\n" \
  "v_cvt_pk_f16_f32 v70, v53, v55\nv_pk_mul_f32 v[52:53], v[54:55], %1 op_sel_hi:[0,1]\n" \
  "v_pk_fma_f32 v[52:53], v[54:55], %1, v[52:53] op_sel:[1,1,0] op_sel_hi:[1,0,1] neg_lo:[0,1,0]\n" \
  "v_pk_mul_f32 v[54:55], v[52:53], %1 op_sel_hi:[0,1]\n" \
  "v_pk_fma_f32 v[54:55], v[52:53], %1, v[54:55] op_sel:[1,1,0] op_sel_hi:[1,0,1] neg_lo:[0,1,0]\nv_cvt_pk_f16_f32 v67, v52, v54\n" \
  "v_cvt_pk_f16_f32 v71, v53, v55\nds_write_b128 %3, v[64:67] offset:48\nds_write_b128 %3, v[68:71] offset:112\n"
#define Y_CLOB "memory", "v40", "v41", "v42", "v43", "v44", "v45", "v46", "v47", "v48", "v49", "v50", "v51", "v52", "v53", "v54", "v55", "v56", "v57", "v58", "v59", "v60", "v61", "v62", "v63", "v64", "v65", "v66", "v67", "v68", "v69", "v70", "v71"
#define T_ASM \
  "ds_read_b128 %4, %8\nds_read_b64_tr_b16 v[80:81], %7 offset:0\nds_read_b64_tr_b16 v[82:83], %7 offset:64\n" \
  "ds_read_b64_tr_b16 v[84:85], %5 offset:0\nds_read_b64_tr_b16 v[86:87], %5 offset:48\nds_read_b64_tr_b16 v[88:89], %6 offset:0\n" \
  "ds_read_b64_tr_b16 v[90:91], %6 offset:48\nds_read_b64_tr_b16 v[92:93], %7 offset:3584\nds_read_b64_tr_b16 v[94:95], %7 offset:3648\n" \
  "ds_read_b64_tr_b16 v[96:97], %5 offset:3584\nds_read_b64_tr_b16 v[98:99], %5 offset:3632\nds_read_b64_tr_b16 v[100:101], %6 offset:3584\n" \
  "ds_read_b64_tr_b16 v[102:103], %6 offset:3632\nds_read_b64_tr_b16 v[104:105], %7 offset:7168\nds_read_b64_tr_b16 v[106:107], %7 offset:7232\n" \
  "s_waitcnt lgkmcnt(8)\nv_mfma_f32_16x16x32_f16 %0, v[84:87], v[80:83], 0\nds_read_b64_tr_b16 v[108:109], %5 offset:7168\n" \
  "ds_read_b64_tr_b16 v[110:111], %5 offset:7216\nds_read_b64_tr_b16 v[112:113], %6 offset:7168\nv_mfma_f32_16x16x32_f16 %2, v[88:91], v[80:83], 0\n" \
  "ds_read_b64_tr_b16 v[114:115], %6 offset:7216\nds_read_b64_tr_b16 v[116:117], %7 offset:10752\nds_read_b64_tr_b16 v[118:119], %7 offset:10816\n" \
  "s_waitcnt lgkmcnt(8)\nv_mfma_f32_16x16x32_f16 %0, v[96:99], v[92:95], %0\nds_read_b64_tr_b16 v[120:121], %5 offset:10752\n" \
  "ds_read_b64_tr_b16 v[122:123], %5 offset:10800\nds_read_b64_tr_b16 v[124:125], %6 offset:10752\n" \
  "v_mfma_f32_16x16x32_f16 %2, v[100:103], v[92:95], %2\nds_read_b64_tr_b16 v[126:127], %6 offset:10800\n" \
  "ds_read_b64_tr_b16 v[80:81], %7 offset:14336\nds_read_b64_tr_b16 v[82:83], %7 offset:14400\ns_waitcnt lgkmcnt(8)\n" \
  "v_mfma_f32_16x16x32_f16 %0, v[108:111], v[104:107], %0\nds_read_b64_tr_b16 v[84:85], %5 offset:14336\n" \
  "ds_read_b64_tr_b16 v[86:87], %5 offset:14384\nds_read_b64_tr_b16 v[88:89], %6 offset:14336\n" \
  "v_mfma_f32_16x16x32_f16 %2, v[112:115], v[104:107], %2\nds_read_b64_tr_b16 v[90:91], %6 offset:14384\n" \
  "ds_read_b64_tr_b16 v[92:93], %7 offset:17920\nds_read_b64_tr_b16 v[94:95], %7 offset:17984\ns_waitcnt lgkmcnt(8)\n" \
  "v_mfma_f32_16x16x32_f16 %0, v[120:123], v[116:119], %0\nds_read_b64_tr_b16 v[96:97], %5 offset:17920\n" \
  "ds_read_b64_tr_b16 v[98:99], %5 offset:17968\nds_read_b64_tr_b16 v[100:101], %6 offset:17920\n" \
  "v_mfma_f32_16x16x32_f16 %2, v[124:127], v[116:119], %2\nds_read_b64_tr_b16 v[102:103], %6 offset:17968\n" \
  "ds_read_b64_tr_b16 v[104:105], %7 offset:21504\nds_read_b64_tr_b16 v[106:107], %7 offset:21568\ns_waitcnt lgkmcnt(8)\n" \
  "v_mfma_f32_16x16x32_f16 %1, v[84:87], v[80:83], 0\nds_read_b64_tr_b16 v[108:109], %5 offset:21504\nds_read_b64_tr_b16 v[110:111], %5 offset:21552\n" \
  "ds_read_b64_tr_b16 v[112:113], %6 offset:21504\nv_mfma_f32_16x16x32_f16 %3, v[88:91], v[80:83], 0\nds_read_b64_tr_b16 v[114:115], %6 offset:21552\n" \
  "ds_read_b64_tr_b16 v[116:117], %7 offset:25088\nds_read_b64_tr_b16 v[118:119], %7 offset:25152\ns_waitcnt lgkmcnt(8)\n" \
  "v_mfma_f32_16x16x32_f16 %1, v[96:99], v[92:95], %1\nds_read_b64_tr_b16 v[120:121], %5 offset:25088\n" \
  "ds_read_b64_tr_b16 v[122:123], %5 offset:25136\nds_read_b64_tr_b16 v[124:125], %6 offset:25088\n" \
  "v_mfma_f32_16x16x32_f16 %3, v[100:103], v[92:95], %3\nds_read_b64_tr_b16 v[126:127], %6 offset:25136\ns_waitcnt lgkmcnt(6)\n" \
  "v_mfma_f32_16x16x32_f16 %1, v[108:111], v[104:107], %1\nv_mfma_f32_16x16x32_f16 %3, v[112:115], v[104:107], %3\ns_waitcnt lgkmcnt(0)\n" \
  "v_mfma_f32_16x16x32_f16 %1, v[120:123], v[116:119], %1\nv_mfma_f32_16x16x32_f16 %3, v[124:127], v[116:119], %3\n"
#define T_CLOB "memory", "v80", "v81", "v82", "v83", "v84", "v85", "v86", "v87", "v88", "v89", "v90", "v91", "v92", "v93", "v94", "v95", "v96", "v97", "v98", "v99", "v100", "v101", "v102", "v103", "v104", "v105", "v106", "v107", "v108", "v109", "v110", "v111", "v112", "v113", "v114", "v115", "v116", "v117", "v118", "v119", "v120", "v121", "v122", "v123", "v124", "v125", "v126", "v127"
template <class F> __device__ __forceinline__ int gen_y(float ldt, float are, float aim, float bre, float bim, float cre, float cim, uint32_t im, F&& mid TSP) {
  float ar2, air, g;
  const float dt = state_a(ldt, are, aim, ar2, air, g);
  const float dbr = dt * bre, dbi = dt * bim, pr = cre * dbr - cim * dbi, pi = cre * dbi + cim * dbr;
  const f32x2 w8 = cexp2(ar2, -air, 8.f), w64 = cexp2(ar2, -air, 64.f);
  const float pm = fmaxf(fabsf(pr), fabsf(pi));
  int e = -4;
  if (__builtin_expect(__any(!(pm < 0x1p-4f) || ar2 > 0.f) || !__any(pm > 0x1p-12f), 0)) e = (int)(wave_maxu(__float_as_uint(pm)) >> 23) - 126 + (int)ceilf(g * 250.f);
  const f32x2 y0 = {__builtin_amdgcn_ldexpf(pr, 14 - e), __builtin_amdgcn_ldexpf(-pi, 14 - e)};
  mid();
  TS(0, 7);
  asm volatile(Y_ASM : : "v"(y0), "v"(w8), "v"(w64), "v"(im) : Y_CLOB);
  return e;
}
__device__ __forceinline__ int gen_x(float ldt, float are, float aim, char* im) {
  float ar2, air, g;
  state_a(ldt, are, aim, ar2, air, g);
  const int e = min((int)ceilf(g * 1810.f), 300);
  const float s = __builtin_amdgcn_ldexpf(1.f, 15 - e);
  const f32x2 w1 = cexp2(ar2, air, 1.f), w256 = cexp2(ar2, air, 256.f), w2 = cmul(w1, w1);
  f32x2 a[8], b[8], c[8];
  a[0] = (f32x2){s, 0.f}; a[1] = w1 * s; a[2] = w2 * s;
  b[0] = b[1] = w256 * s;
  b[2] = cmul(b[0], w2); a[3] = cmul(a[1], w2); a[4] = cmul(a[2], w2);
  b[3] = b[4] = b[5] = cmul(b[2], w2); a[5] = cmul(a[3], w2); a[6] = cmul(a[4], w2);
  b[6] = cmul(b[3], w2); a[7] = cmul(a[5], w2); c[0] = cmul(b[3], w256);
  b[7] = cmul(b[6], w2); c[2] = cmul(c[0], w256);
  put_im<0>(im + I_X, 48, a);
  c[1] = cmul(b[7], w256); c[3] = cmul(c[1], w256);
  put_im<0>(im + I_X + 16, 48, b);
#pragma unroll
  for (int k = 4; k < 8; ++k) c[k] = cmul(c[k - 1], w256);
  put_im<0>(im + I_X + 32, 48, c);
  return e;
}

template <int I> struct IC { static constexpr int value = I; };
template <int B, int E, class F> __device__ __forceinline__ void static_for(F&& f) {
  if constexpr (B < E) { f(IC<B>{}); static_for<B + 1, E>(f); }
}
typedef __attribute__((ext_vector_type(4))) short s16x4;
__device__ __forceinline__ u32x2 trd(uint32_t a) {
  return __builtin_bit_cast(u32x2, __builtin_amdgcn_ds_read_tr16_b64_v4i16((__attribute__((address_space(3))) s16x4*)(uintptr_t)a));
}
#define DPP_F(v, ctrl) __int_as_float(DPP_I(__float_as_int(v), (ctrl), 0xf))
__device__ __forceinline__ float s_add(float a, float b) { float r; asm("v_add_f32 %0, %1, %2" : "=v"(r) : "v"(a), "v"(b)); return r; }
__device__ __forceinline__ float s_fma(float a, float b, float c) { float r; asm("v_fma_f32 %0, %1, %2, %3" : "=v"(r) : "v"(a), "v"(b), "v"(c)); return r; }
#define BAR() asm volatile("s_waitcnt lgkmcnt(0)\n\ts_barrier" ::: "memory")
#define ST16(off, v) asm volatile("global_store_dwordx4 %0, %1, %2 sc1\n\ts_nop 1" : : "v"(off), "v"(v), "s"(ws) : "memory")
#define ST4(off, v) asm volatile("global_store_dword %0, %1, %2 sc1" : : "v"(off), "v"(v), "s"(ws) : "memory")
#define LD16(r, off) asm volatile("global_load_dwordx4 %0, %1, %2 sc1" : "=v"(r) : "v"(off), "s"(ws) : "memory")
#define LD16W(r, off) asm volatile("global_load_dwordx4 %0, %1, %2 sc1\n\ts_waitcnt vmcnt(0)" : "=&v"(r) : "v"(off), "s"(ws) : "memory")
#define DRAIN() asm volatile("s_waitcnt vmcnt(0)" ::: "memory")
#define CEX(a, b) { const uint32_t hi_ = max(a, b), lo_ = min(a, b); a = hi_; b = lo_; }
#define MMB(a_, b_, c_) __builtin_amdgcn_mfma_f32_16x16x32_bf16(__builtin_bit_cast(bf16x8, a_), __builtin_bit_cast(bf16x8, b_), c_, 0, 0, 0)
#define NTM 0
#define POLS(b) ((NTM & (b)) ? " nt" : "")
#define DPOL ""
#define DPOL2 ""
#define DPOL4 ""
#define XPOL ""
#define TPOL ""
constexpr int SPIN_CAP = 1 << 18;
constexpr uint32_t UMASK = 0x40074007u;
__device__ __forceinline__ uint32_t utag(uint32_t T, int j) {
  return ((T >> (3 * j)) & 0x00070007u) | (((j <= 2) ? (T << (2 - j)) : (T >> (j - 2))) & 0x40004000u);
}
__device__ __forceinline__ uint32_t max4(const u32x4& a) { return max(max(a[0], a[1]), max(a[2], a[3])); }
__device__ __forceinline__ uint32_t min4(const u32x4& a) { return min(min(a[0], a[1]), min(a[2], a[3])); }
__device__ __forceinline__ void until(uint32_t t) {
#pragma unroll 1
  for (int spin = 0; spin < 600 && (int)((uint32_t)__builtin_amdgcn_s_memrealtime() - t) < 0; ++spin) __builtin_amdgcn_s_sleep(1);
}
__device__ __forceinline__ uint32_t latest(uint32_t t) {
  const uint32_t t0 = (uint32_t)__builtin_amdgcn_readfirstlane(t) - 0x40000000u;
  return wave_maxu(t - t0) + t0;
}
__device__ __forceinline__ uint32_t gate(const char* ws, uint32_t po, uint32_t T) {
  u32x2 pv;
#pragma unroll 1
  for (int spin = 0; spin < SPIN_CAP; ++spin) {
    asm volatile("global_load_dwordx2 %0, %1, %2 sc1\n\ts_waitcnt vmcnt(0)" : "=&v"(pv) : "v"(po), "s"(ws) : "memory");
    if (!__any(pv[0] != T)) break;
    __builtin_amdgcn_s_sleep(2);
  }
  return pv[1];
}
__device__ __forceinline__ uint32_t gate2(const char* ws, uint32_t oa, uint32_t da, uint32_t ob, uint32_t T) {
  u32x2 pa, pb;
#pragma unroll 1
  for (int spin = 0; spin < SPIN_CAP; ++spin) {
    asm volatile("global_load_dwordx2 %0, %2, %4 sc1\n\tglobal_load_dwordx2 %1, %3, %4 sc1\n\ts_waitcnt vmcnt(0)" : "=&v"(pa), "=&v"(pb) : "v"(oa), "v"(ob), "s"(ws) : "memory");
    if (!__any(pa[0] != T || pb[0] != T)) break;
    __builtin_amdgcn_s_sleep(4);
  }
  pa[1] += da;
  return latest(((int)(pa[1] - pb[1]) > 0) ? pa[1] : pb[1]);
}
__device__ __forceinline__ uint32_t ahead(uint32_t c0, uint32_t t0, int dt) {
  const uint32_t c = (uint32_t)__builtin_amdgcn_s_memtime(), t = (uint32_t)__builtin_amdgcn_s_memrealtime();
  return t + (uint32_t)((float)((t - t0) * (uint32_t)(20 * dt)) * __builtin_amdgcn_rcpf((float)(c - c0)));
}
#define SIGNAL(off, pitch, dt) do { const uint32_t t_ = ahead(ck0, rt0, dt); \
    ST16((off) + (uint32_t)(lane >> 3) * (pitch) + 16u * (lane & 7), ((u32x4){T, t_, T, t_})); } while (0)
__device__ __forceinline__ void gsync(uint32_t a, uint32_t target) {
  uint32_t t, c, n = SPIN_CAP;
  uint64_t ex;
  asm volatile("s_waitcnt lgkmcnt(0)\n\ts_mov_b64 %3, exec\n\ts_mov_b64 exec, 1\n\tv_mov_b32 %0, 1\n\tds_add_u32 %4, %0\n\ts_mov_b64 exec, %3\n"
               "1:\n\ts_sleep 1\n\tds_read_b32 %0, %4\n\ts_sub_u32 %2, %2, 1\n\ts_cbranch_scc1 2f\n\ts_waitcnt lgkmcnt(0)\n\tv_readfirstlane_b32 %1, %0\n\t"
               "s_cmp_lt_u32 %1, %5\n\ts_cbranch_scc1 1b\n2:\n\ts_waitcnt lgkmcnt(0)"
               : "=&v"(t), "=&s"(c), "+s"(n), "=&s"(ex) : "v"(a), "n"(target) : "memory", "scc");
}
__device__ __forceinline__ void gsync0(uint32_t a, uint32_t target) {
  uint32_t t, c, n = SPIN_CAP;
  asm volatile("s_waitcnt lgkmcnt(0)\n\ts_mov_b64 exec, 1\n\tv_mov_b32 %0, 1\n\tds_add_u32 %3, %0\n\ts_mov_b64 exec, -1\n"
               "1:\n\tds_read_b32 %0, %3\n\ts_sub_u32 %2, %2, 1\n\ts_cbranch_scc1 2f\n\ts_waitcnt lgkmcnt(0)\n\tv_readfirstlane_b32 %1, %0\n\t"
               "s_cmp_lt_u32 %1, %4\n\ts_cbranch_scc1 1b\n2:\n\ts_waitcnt lgkmcnt(0)"
               : "=&v"(t), "=&s"(c), "+s"(n) : "v"(a), "n"(target) : "memory", "scc");
}
__device__ __forceinline__ void gwait(uint32_t a, uint32_t target) {
  uint32_t t, c, n = SPIN_CAP;
  asm volatile("1:\n\ts_sleep 2\n\tds_read_b32 %0, %3\n\ts_sub_u32 %2, %2, 1\n\ts_cbranch_scc1 2f\n\ts_waitcnt lgkmcnt(0)\n\tv_readfirstlane_b32 %1, %0\n\t"
               "s_cmp_lt_u32 %1, %4\n\ts_cbranch_scc1 1b\n2:\n\ts_waitcnt lgkmcnt(0)"
               : "=&v"(t), "=&s"(c), "+s"(n) : "v"(a), "n"(target) : "memory", "scc");
}
__device__ __forceinline__ void dma16(const void* base, uint32_t voff, uint32_t lds) {
  uint32_t keep;
  asm volatile("s_mov_b32 %0, m0\n\ts_mov_b32 m0, %3\n\ts_nop 0\n\tglobal_load_lds_dwordx4 %1, %2" DPOL "\n\ts_mov_b32 m0, %0" : "=&s"(keep) : "v"(voff), "s"(base), "s"(lds) : "memory");
}
__device__ __forceinline__ void dma16d(const void* base, uint32_t voff, uint32_t lds) {
  uint32_t keep;
  asm volatile("s_mov_b32 %0, m0\n\ts_mov_b32 m0, %3\n\ts_nop 0\n\tglobal_load_lds_dwordx4 %1, %2" DPOL2 "\n\ts_mov_b32 m0, %0" : "=&s"(keep) : "v"(voff), "s"(base), "s"(lds) : "memory");
}
__device__ __forceinline__ void nap(int n) {
  for (; n > 15; n -= 15) __builtin_amdgcn_s_sleep(15);
  switch (n) {
    case 1: __builtin_amdgcn_s_sleep(1); break; case 2: __builtin_amdgcn_s_sleep(2); break; case 3: __builtin_amdgcn_s_sleep(3); break;
    case 4: __builtin_amdgcn_s_sleep(4); break; case 5: __builtin_amdgcn_s_sleep(5); break; case 6: __builtin_amdgcn_s_sleep(6); break;
    case 7: __builtin_amdgcn_s_sleep(7); break; case 8: __builtin_amdgcn_s_sleep(8); break; case 9: __builtin_amdgcn_s_sleep(9); break;
    case 10: __builtin_amdgcn_s_sleep(10); break; case 11: __builtin_amdgcn_s_sleep(11); break; case 12: __builtin_amdgcn_s_sleep(12); break;
    case 13: __builtin_amdgcn_s_sleep(13); break; case 14: __builtin_amdgcn_s_sleep(14); break; case 15: __builtin_amdgcn_s_sleep(15); break;
    default: break;
  }
}
#define SLEEP(n) do { if ((n) > 0) __builtin_amdgcn_s_sleep(n); } while (0)
#define USLEEP 3
#define UPOS 1
#define QSLEEP 0
#define CSLEEP 4
#define WSLEEP 4
#define TSLEEP 0
#define VSLEEP 45
#define KSLEEP 2
#define SSLEEP 11
#define PSLEEP 2
#define PPRIO 0
#define APRIO 1
#define NTOUCH 4
#define SWZ 1
#define PREOUT 0
#define LFIRST 1
#define CBR "0"
#define WD 40
#define CD 3
#define OSLEEP 12
#define QPRIO 1
#define KPRIO 2
#define UDRAIN 0
#define GSLEEP 0
#define SIGPOS 0
#define LPRIO 3
#define TW 0
#define TPOS 0
#define R1EARLY 1
#define DPOS 0
#define NSLEEP 0
#define ASLEEP 40
#define FPRIO 1
#define PRBW 1
#define PRBPOS 1
#define TW2 6
#define DSLEEP 8
#define VD 400
#define XSLEEP 20
#define RPOS 1
#define KPRE 1
#define UD 40
#define XD 0
#define TRIG 1
#define CPF 0
#define RSLEEP 0
#define UPRIO 2
#define VD2 390
#define TWP 256
#define WDEPTH 6
#define PD 170
#define VDX 390
#define EARLY 0
#define ESLEEP 0
#define WDEPTH1 5
#define WD2 85
#define SD 58
#define KD 175
#define UFIRST 0
#define TX 1
#define QWAIT 4
#define OD 0
#define KBACK 6
#define DDEPTH 6
#define C3 1
#define XOFF 0
#define GD 105
#define XROT 2
#define WSOFF 0
#define T0 0
#define TF 192
#define DD 0
#define TDEPTH 1
#define TB1 0
#define TB1I 0
#define TB1X 2
#define B1SLEEP 0
#define DFIRST 0
#define T0SLEEP 0
#define BSLEEP 6
#define KN 2
#define TD 0
#define KE 0
#define SKIPB 0
#define NFIRST 0
#define PW1 16
#define PW2 38
#define KLATE 0
#define RPF 1
#define HSLEEP 6
#define LSLEEP 3
#define TSTOP 55
#define HSTOP 100
#define HL 0
#define VEARLY 0ull
#define FPR 3
#define SPR 3
#define DPRIO 0
#define T1 0
#define T1SLEEP 0
#define QPRIO2 3
#define HW 2
#define HN 3
#define RD 55
#define OPOS 1
#define OD2 0
#define NA 14
#define HA 2
#define HAX 0
#define RD2 80
#define ORD1 ((unsigned __int128)0x321087654u << 36 | 0x321087654ull)
#define RSL 10
#define WDEPTH2 3
#define RQ NA
#define HREV 1
#define QPRIO0 3
#define VPS 1
#define RW 0
#define NPRIO 0
#define XSL 10
#define FORCESLOW 0
#define FORCEFB 0
#define CONV_ASM \
  "s_cmp_eq_u32 s89, 1\ns_cbranch_scc1 1f\ns_cmp_gt_u32 s89, 1\ns_cbranch_scc1 9f\nds_read_b64 v[124:125], v3 offset:576\n" \
  "ds_read_b64 v[126:127], v3 offset:584\nds_read_b64 v[120:121], v3 offset:64\nds_read_b64 v[122:123], v3 offset:72\n" \
  "ds_read_b64 v[116:117], v3 offset:640\nds_read_b64 v[118:119], v3 offset:648\nds_read_b64 v[112:113], v3 offset:128\n" \
  "ds_read_b64 v[114:115], v3 offset:136\nds_read_b64 v[108:109], v3 offset:704\nds_read_b64 v[110:111], v3 offset:712\n" \
  "ds_read_b64 v[104:105], v3 offset:192\nds_read_b64 v[106:107], v3 offset:200\nds_read_b64 v[100:101], v3 offset:768\n" \
  "ds_read_b64 v[102:103], v3 offset:776\nds_read_b64 v[96:97], v3 offset:256\nds_read_b64 v[98:99], v3 offset:264\n" \
  "ds_read_b64 v[92:93], v3 offset:832\nds_read_b64 v[94:95], v3 offset:840\nds_read_b64 v[88:89], v3 offset:320\n" \
  "ds_read_b64 v[90:91], v3 offset:328\nds_read_b64 v[84:85], v3 offset:896\nds_read_b64 v[86:87], v3 offset:904\n" \
  "ds_read_b64 v[80:81], v3 offset:384\nds_read_b64 v[82:83], v3 offset:392\nds_read_b64 v[76:77], v3 offset:960\n" \
  "ds_read_b64 v[78:79], v3 offset:968\nds_read_b64 v[72:73], v3 offset:448\nds_read_b64 v[74:75], v3 offset:456\n" \
  "ds_read_b64 v[68:69], v3 offset:1024\nds_read_b64 v[70:71], v3 offset:1032\nds_read_b64 v[64:65], v3 offset:512\n" \
  "ds_read_b64 v[66:67], v3 offset:520\nds_read_b64 v[60:61], v3 offset:0\nds_read_b64 v[62:63], v3 offset:8\nds_read_b64 v[56:57], v3 offset:1088\n" \
  "ds_read_b64 v[58:59], v3 offset:1096\ns_waitcnt lgkmcnt(0)\ns_barrier\nds_read_b128 v[36:39], v2 offset:0\nds_read_b128 v[40:43], v2 offset:64\n" \
  "ds_read_b128 v[44:47], v2 offset:128\nds_read_b128 v[48:51], v2 offset:192\ns_waitcnt lgkmcnt(3)\n" \
  "v_mfma_f32_16x16x32_f16 v[12:15], v[124:127], v[36:39], 0\ns_setprio 1\nds_read_b128 v[52:55], v2 offset:256\n" \
  "v_mfma_f32_16x16x32_f16 v[16:19], v[120:123], v[36:39], 0\ns_waitcnt lgkmcnt(3)\nv_mfma_f32_16x16x32_f16 v[12:15], v[116:119], v[40:43], v[12:15]\n" \
  "ds_read_b128 v[36:39], v2 offset:320\nv_mfma_f32_16x16x32_f16 v[16:19], v[112:115], v[40:43], v[16:19]\ns_waitcnt lgkmcnt(3)\n" \
  "v_mfma_f32_16x16x32_f16 v[12:15], v[108:111], v[44:47], v[12:15]\nds_read_b128 v[40:43], v2 offset:384\n" \
  "v_mfma_f32_16x16x32_f16 v[16:19], v[104:107], v[44:47], v[16:19]\ns_waitcnt lgkmcnt(3)\n" \
  "v_mfma_f32_16x16x32_f16 v[12:15], v[100:103], v[48:51], v[12:15]\nds_read_b128 v[44:47], v2 offset:448\n" \
  "v_mfma_f32_16x16x32_f16 v[16:19], v[96:99], v[48:51], v[16:19]\ns_waitcnt lgkmcnt(3)\n" \
  "v_mfma_f32_16x16x32_f16 v[12:15], v[92:95], v[52:55], v[12:15]\nv_mfma_f32_16x16x32_f16 v[16:19], v[88:91], v[52:55], v[16:19]\n" \
  "s_waitcnt lgkmcnt(2)\nv_mfma_f32_16x16x32_f16 v[12:15], v[84:87], v[36:39], v[12:15]\nds_read_b128 v[48:51], v2 offset:512\n" \
  "v_mfma_f32_16x16x32_f16 v[16:19], v[80:83], v[36:39], v[16:19]\ns_waitcnt lgkmcnt(2)\n" \
  "v_mfma_f32_16x16x32_f16 v[12:15], v[76:79], v[40:43], v[12:15]\nds_read_b128 v[52:55], v2 offset:576\n" \
  "v_mfma_f32_16x16x32_f16 v[16:19], v[72:75], v[40:43], v[16:19]\ns_waitcnt lgkmcnt(2)\n" \
  "v_mfma_f32_16x16x32_f16 v[12:15], v[68:71], v[44:47], v[12:15]\nds_read_b128 v[36:39], v2 offset:640\n" \
  "v_mfma_f32_16x16x32_f16 v[16:19], v[64:67], v[44:47], v[16:19]\nv_mfma_f32_16x16x32_f16 v[20:23], v[60:63], v[44:47], 0\n" \
  "ds_read_b128 v[40:43], v2 offset:704\nv_mfma_f32_16x16x32_f16 v[28:31], v[56:59], v[44:47], 0\ns_waitcnt lgkmcnt(3)\n" \
  "v_mfma_f32_16x16x32_f16 v[16:19], v[124:127], v[48:51], v[16:19]\nds_read_b128 v[56:59], v2 offset:768\nv_cvt_pkrtz_f16_f32 v32, v12, v13\n" \
  "v_mfma_f32_16x16x32_f16 v[20:23], v[120:123], v[48:51], v[20:23]\nv_cvt_pkrtz_f16_f32 v33, v14, v15\ns_waitcnt lgkmcnt(3)\n" \
  "v_mfma_f32_16x16x32_f16 v[16:19], v[116:119], v[52:55], v[16:19]\nds_read_b128 v[44:47], v2 offset:832\n" \
  "v_pk_add_u16 v32, v32, 4 op_sel_hi:[1,0] clamp\nv_mfma_f32_16x16x32_f16 v[20:23], v[112:115], v[52:55], v[20:23]\n" \
  "v_pk_add_u16 v33, v33, 4 op_sel_hi:[1,0] clamp\ns_waitcnt lgkmcnt(3)\nv_mfma_f32_16x16x32_f16 v[16:19], v[108:111], v[36:39], v[16:19]\n" \
  "ds_read_b128 v[48:51], v2 offset:896\nds_write_b64 v4, v[32:33] offset:0\nv_mfma_f32_16x16x32_f16 v[20:23], v[104:107], v[36:39], v[20:23]\n" \
  "s_waitcnt lgkmcnt(3)\nv_mfma_f32_16x16x32_f16 v[16:19], v[100:103], v[40:43], v[16:19]\nds_read_b128 v[52:55], v2 offset:960\n" \
  "v_mfma_f32_16x16x32_f16 v[20:23], v[96:99], v[40:43], v[20:23]\nv_mfma_f32_16x16x32_f16 v[16:19], v[92:95], v[56:59], v[16:19]\n" \
  "v_mfma_f32_16x16x32_f16 v[20:23], v[88:91], v[56:59], v[20:23]\nds_read_b128 v[36:39], v2 offset:1024\ns_waitcnt lgkmcnt(3)\n" \
  "v_mfma_f32_16x16x32_f16 v[16:19], v[84:87], v[44:47], v[16:19]\nv_mfma_f32_16x16x32_f16 v[20:23], v[80:83], v[44:47], v[20:23]\n" \
  "ds_read_b128 v[40:43], v2 offset:1088\nds_read_b64 v[56:57], v6 offset:256\nds_read_b64 v[58:59], v6 offset:264\n" \
  "v_mfma_f32_16x16x32_f16 v[16:19], v[76:79], v[48:51], v[16:19]\nv_mfma_f32_16x16x32_f16 v[20:23], v[72:75], v[48:51], v[20:23]\n" \
  "ds_read_b128 v[44:47], v2 offset:1152\ns_waitcnt lgkmcnt(4)\nv_mfma_f32_16x16x32_f16 v[16:19], v[68:71], v[52:55], v[16:19]\n" \
  "v_mfma_f32_16x16x32_f16 v[20:23], v[64:67], v[52:55], v[20:23]\nds_read_b128 v[48:51], v2 offset:1216\n" \
  "v_mfma_f32_16x16x32_f16 v[24:27], v[60:63], v[52:55], 0\nv_mfma_f32_16x16x32_f16 v[20:23], v[124:127], v[36:39], v[20:23]\n" \
  "ds_read_b128 v[52:55], v2 offset:1280\nv_mfma_f32_16x16x32_f16 v[24:27], v[120:123], v[36:39], v[24:27]\ns_waitcnt lgkmcnt(5)\n" \
  "v_mfma_f32_16x16x32_f16 v[20:23], v[116:119], v[40:43], v[20:23]\nds_read_b128 v[36:39], v2 offset:1344\nv_cvt_pkrtz_f16_f32 v32, v16, v17\n" \
  "v_mfma_f32_16x16x32_f16 v[24:27], v[112:115], v[40:43], v[24:27]\nv_cvt_pkrtz_f16_f32 v33, v18, v19\ns_waitcnt lgkmcnt(3)\n" \
  "v_mfma_f32_16x16x32_f16 v[20:23], v[108:111], v[44:47], v[20:23]\nds_read_b128 v[40:43], v2 offset:1408\n" \
  "v_pk_add_u16 v32, v32, 4 op_sel_hi:[1,0] clamp\nds_read_b32 v35, v9 offset:32\nv_mfma_f32_16x16x32_f16 v[24:27], v[104:107], v[44:47], v[24:27]\n" \
  "v_pk_add_u16 v33, v33, 4 op_sel_hi:[1,0] clamp\ns_waitcnt lgkmcnt(3)\nv_mfma_f32_16x16x32_f16 v[20:23], v[100:103], v[48:51], v[20:23]\n" \
  "ds_read_b128 v[44:47], v2 offset:1472\nds_write_b64 v4, v[32:33] offset:1024\nv_mfma_f32_16x16x32_f16 v[24:27], v[96:99], v[48:51], v[24:27]\n" \
  "v_mfma_f32_16x16x32_f16 v[20:23], v[92:95], v[52:55], v[20:23]\nv_mfma_f32_16x16x32_f16 v[24:27], v[88:91], v[52:55], v[24:27]\n" \
  "ds_read_b128 v[48:51], v2 offset:1536\ns_waitcnt lgkmcnt(3)\nv_readfirstlane_b32 s85, v35\ns_cmp_lt_u32 s85, 4\ns_cbranch_scc1 21f\n22:\n" \
  "v_mfma_f32_16x16x32_f16 v[20:23], v[84:87], v[36:39], v[20:23]\nv_mfma_f32_16x16x32_f16 v[24:27], v[80:83], v[36:39], v[24:27]\n" \
  "ds_read_b128 v[52:55], v2 offset:1600\nv_mfma_f32_16x16x32_f16 v[20:23], v[76:79], v[40:43], v[20:23]\n" \
  "v_mfma_f32_16x16x32_f16 v[24:27], v[72:75], v[40:43], v[24:27]\nds_read_b128 v[36:39], v2 offset:1664\ns_waitcnt lgkmcnt(4)\n" \
  "v_mfma_f32_16x16x32_f16 v[20:23], v[68:71], v[44:47], v[20:23]\nv_mfma_f32_16x16x32_f16 v[24:27], v[64:67], v[44:47], v[24:27]\n" \
  "ds_read_b128 v[40:43], v2 offset:1728\nv_mfma_f32_16x16x32_f16 v[28:31], v[60:63], v[44:47], v[28:31]\nds_read_b64_tr_b16 v[60:61], v5 offset:0\n" \
  "ds_read_b64_tr_b16 v[62:63], v5 offset:32\ns_waitcnt lgkmcnt(4)\nv_mfma_f32_16x16x32_f16 v[24:27], v[124:127], v[48:51], v[24:27]\n" \
  "v_mfma_f32_16x16x32_f16 v[28:31], v[120:123], v[48:51], v[28:31]\nds_read_b64_tr_b16 v[44:45], v5 offset:256\n" \
  "ds_read_b64_tr_b16 v[46:47], v5 offset:288\nv_mfma_f32_16x16x32_f16 v[24:27], v[116:119], v[52:55], v[24:27]\n" \
  "ds_read_b64 v[120:121], v6 offset:320\nds_read_b64 v[122:123], v6 offset:328\nv_mfma_f32_16x16x32_f16 v[28:31], v[112:115], v[52:55], v[28:31]\n" \
  "ds_read_b128 v[48:51], v2 offset:1792\ns_waitcnt lgkmcnt(7)\nv_mfma_f32_16x16x32_f16 v[24:27], v[108:111], v[36:39], v[24:27]\n" \
  "ds_read_b128 v[112:115], v2 offset:1856\nv_mfma_f32_16x16x32_f16 v[28:31], v[104:107], v[36:39], v[28:31]\nds_read_b64 v[52:53], v6 offset:128\n" \
  "ds_read_b64 v[54:55], v6 offset:136\nv_mfma_f32_16x16x32_f16 v[24:27], v[100:103], v[40:43], v[24:27]\nds_read_b128 v[104:107], v2 offset:1920\n" \
  "v_mfma_f32_16x16x32_f16 v[28:31], v[96:99], v[40:43], v[28:31]\nds_read_b64 v[36:37], v6 offset:192\nds_read_b64 v[38:39], v6 offset:200\n" \
  "s_waitcnt lgkmcnt(9)\nv_mfma_f32_16x16x32_f16 v[20:23], v[60:63], v[56:59], v[20:23]\nds_read_b128 v[96:99], v2 offset:1984\ns_waitcnt lgkmcnt(7)\n" \
  "v_mfma_f32_16x16x32_f16 v[20:23], v[44:47], v[120:123], v[20:23]\nv_mfma_f32_16x16x32_f16 v[24:27], v[92:95], v[48:51], v[24:27]\n" \
  "ds_read_b128 v[40:43], v2 offset:2048\nv_mfma_f32_16x16x32_f16 v[28:31], v[88:91], v[48:51], v[28:31]\nds_read_b64_tr_b16 v[88:89], v5 offset:512\n" \
  "ds_read_b64_tr_b16 v[90:91], v5 offset:544\ns_waitcnt lgkmcnt(6)\nv_mfma_f32_16x16x32_f16 v[24:27], v[84:87], v[112:115], v[24:27]\n" \
  "v_mfma_f32_16x16x32_f16 v[28:31], v[80:83], v[112:115], v[28:31]\nv_cvt_pkrtz_f16_f32 v32, v20, v21\nv_cvt_pkrtz_f16_f32 v33, v22, v23\n" \
  "v_mfma_f32_16x16x32_f16 v[24:27], v[76:79], v[104:107], v[24:27]\nds_read_b64_tr_b16 v[48:49], v5 offset:768\n" \
  "ds_read_b64_tr_b16 v[50:51], v5 offset:800\nv_mfma_f32_16x16x32_f16 v[28:31], v[72:75], v[104:107], v[28:31]\n" \
  "ds_read_b128 v[80:83], v2 offset:2112\ns_waitcnt lgkmcnt(5)\nv_mfma_f32_16x16x32_f16 v[24:27], v[68:71], v[96:99], v[24:27]\n" \
  "ds_read_b128 v[112:115], v2 offset:2176\nv_pk_add_u16 v32, v32, 4 op_sel_hi:[1,0] clamp\n" \
  "v_mfma_f32_16x16x32_f16 v[28:31], v[64:67], v[96:99], v[28:31]\nds_read_b128 v[72:75], v2 offset:2240\n" \
  "v_pk_add_u16 v33, v33, 4 op_sel_hi:[1,0] clamp\nv_mfma_f32_16x16x32_f16 v[28:31], v[124:127], v[40:43], v[28:31]\n" \
  "ds_read_b128 v[104:107], v2 offset:2304\nds_write_b64 v4, v[32:33] offset:2048\nv_mfma_f32_16x16x32_f16 v[24:27], v[60:63], v[52:55], v[24:27]\n" \
  "ds_read_b128 v[64:67], v2 offset:2368\nds_read_b64 v[96:97], v6 offset:0\nds_read_b64 v[98:99], v6 offset:8\n" \
  "v_mfma_f32_16x16x32_f16 v[24:27], v[44:47], v[36:39], v[24:27]\nds_read_b128 v[124:127], v2 offset:2432\ns_waitcnt lgkmcnt(9)\n" \
  "v_mfma_f32_16x16x32_f16 v[24:27], v[88:91], v[56:59], v[24:27]\nds_read_b128 v[40:43], v2 offset:2496\n" \
  "v_mfma_f32_16x16x32_f16 v[24:27], v[48:51], v[120:123], v[24:27]\ns_waitcnt lgkmcnt(8)\n" \
  "v_mfma_f32_16x16x32_f16 v[28:31], v[116:119], v[80:83], v[28:31]\nds_read_b64 v[116:117], v6 offset:64\nds_read_b64 v[118:119], v6 offset:72\n" \
  "v_mfma_f32_16x16x32_f16 v[28:31], v[108:111], v[112:115], v[28:31]\nds_read_b64_tr_b16 v[80:81], v5 offset:1024\n" \
  "ds_read_b64_tr_b16 v[82:83], v5 offset:1056\ns_waitcnt lgkmcnt(10)\nv_mfma_f32_16x16x32_f16 v[28:31], v[100:103], v[72:75], v[28:31]\n" \
  "v_cvt_pkrtz_f16_f32 v32, v24, v25\nv_cvt_pkrtz_f16_f32 v33, v26, v27\nv_mfma_f32_16x16x32_f16 v[28:31], v[92:95], v[104:107], v[28:31]\n" \
  "v_pk_add_u16 v32, v32, 4 op_sel_hi:[1,0] clamp\ns_waitcnt lgkmcnt(5)\nv_mfma_f32_16x16x32_f16 v[28:31], v[84:87], v[64:67], v[28:31]\n" \
  "ds_read_b64_tr_b16 v[108:109], v5 offset:1280\nds_read_b64_tr_b16 v[110:111], v5 offset:1312\n" \
  "v_mfma_f32_16x16x32_f16 v[28:31], v[76:79], v[124:127], v[28:31]\nds_read_b64_tr_b16 v[112:113], v7 offset:0\n" \
  "ds_read_b64_tr_b16 v[114:115], v7 offset:64\ns_waitcnt lgkmcnt(6)\nv_mfma_f32_16x16x32_f16 v[28:31], v[68:71], v[40:43], v[28:31]\n" \
  "ds_read2_b32 v[100:101], v8 offset0:48 offset1:49\nds_read2_b32 v[102:103], v8 offset0:50 offset1:51\n" \
  "v_mfma_f32_16x16x32_f16 v[28:31], v[60:63], v[96:99], v[28:31]\nv_pk_add_u16 v33, v33, 4 op_sel_hi:[1,0] clamp\n" \
  "ds_write_b64 v4, v[32:33] offset:3072\nv_mfma_f32_16x16x32_f16 v[28:31], v[44:47], v[116:119], v[28:31]\n" \
  "v_mfma_f32_16x16x32_f16 v[28:31], v[88:91], v[52:55], v[28:31]\nv_mfma_f32_16x16x32_f16 v[28:31], v[48:51], v[36:39], v[28:31]\n" \
  "s_waitcnt lgkmcnt(5)\nv_mfma_f32_16x16x32_f16 v[28:31], v[80:83], v[56:59], v[28:31]\n" \
  "v_mfma_f32_16x16x32_f16 v[28:31], v[108:111], v[120:123], v[28:31]\ns_waitcnt lgkmcnt(1)\n" \
  "v_mfma_f32_16x16x32_f16 v[28:31], v[112:115], v[100:103], v[28:31]\ns_nop 7\nv_cvt_pkrtz_f16_f32 v32, v28, v29\nv_cvt_pkrtz_f16_f32 v33, v30, v31\n" \
  "v_pk_add_u16 v32, v32, 4 op_sel_hi:[1,0] clamp\nv_pk_add_u16 v33, v33, 4 op_sel_hi:[1,0] clamp\nds_write_b64 v4, v[32:33] offset:4096\n" \
  "s_setprio 0\ns_setpc_b64 s[90:91]\n21:\ns_movk_i32 s84, 0x4000\n23:\nds_read_b32 v35, v9 offset:32\ns_sub_u32 s84, s84, 1\ns_cbranch_scc1 22b\n" \
  "s_waitcnt lgkmcnt(0)\nv_readfirstlane_b32 s85, v35\ns_cmp_lt_u32 s85, 4\ns_cbranch_scc1 23b\ns_branch 22b\n1:\n" \
  "ds_read_b64 v[124:125], v3 offset:1088\nds_read_b64 v[126:127], v3 offset:1096\nds_read_b64 v[120:121], v3 offset:0\n" \
  "ds_read_b64 v[122:123], v3 offset:8\nds_read_b64 v[116:117], v3 offset:64\nds_read_b64 v[118:119], v3 offset:72\n" \
  "ds_read_b64 v[112:113], v3 offset:128\nds_read_b64 v[114:115], v3 offset:136\nds_read_b64 v[108:109], v3 offset:192\n" \
  "ds_read_b64 v[110:111], v3 offset:200\nds_read_b64 v[104:105], v3 offset:256\nds_read_b64 v[106:107], v3 offset:264\n" \
  "ds_read_b64 v[100:101], v3 offset:320\nds_read_b64 v[102:103], v3 offset:328\nds_read_b64 v[96:97], v3 offset:384\n" \
  "ds_read_b64 v[98:99], v3 offset:392\nds_read_b64 v[92:93], v3 offset:448\nds_read_b64 v[94:95], v3 offset:456\n" \
  "ds_read_b64 v[88:89], v3 offset:512\nds_read_b64 v[90:91], v3 offset:520\nds_read_b64 v[84:85], v3 offset:576\n" \
  "ds_read_b64 v[86:87], v3 offset:584\nds_read_b64 v[80:81], v3 offset:640\nds_read_b64 v[82:83], v3 offset:648\n" \
  "ds_read_b64 v[76:77], v3 offset:704\nds_read_b64 v[78:79], v3 offset:712\nds_read_b64 v[72:73], v3 offset:768\n" \
  "ds_read_b64 v[74:75], v3 offset:776\nds_read_b64 v[68:69], v3 offset:832\nds_read_b64 v[70:71], v3 offset:840\n" \
  "ds_read_b64 v[64:65], v3 offset:896\nds_read_b64 v[66:67], v3 offset:904\nds_read_b64 v[60:61], v3 offset:960\n" \
  "ds_read_b64 v[62:63], v3 offset:968\nds_read_b64 v[56:57], v3 offset:1024\nds_read_b64 v[58:59], v3 offset:1032\n" \
  "ds_read_b64 v[52:53], v6 offset:0\nds_read_b64 v[54:55], v6 offset:8\nds_read_b64 v[48:49], v6 offset:64\nds_read_b64 v[50:51], v6 offset:72\n" \
  "s_waitcnt lgkmcnt(0)\ns_barrier\nds_read_b128 v[28:31], v2 offset:960\nds_read_b128 v[32:35], v2 offset:1472\n" \
  "ds_read_b128 v[36:39], v2 offset:1984\nds_read_b128 v[40:43], v2 offset:2048\nds_read_b128 v[44:47], v2 offset:2112\ns_waitcnt lgkmcnt(3)\n" \
  "v_mfma_f32_16x16x32_f16 v[12:15], v[124:127], v[28:31], 0\ns_setprio 1\nds_read_b128 v[28:31], v2 offset:2176\n" \
  "v_mfma_f32_16x16x32_f16 v[16:19], v[124:127], v[32:35], 0\nds_read_b128 v[32:35], v2 offset:2240\ns_waitcnt lgkmcnt(3)\n" \
  "v_mfma_f32_16x16x32_f16 v[12:15], v[120:123], v[36:39], v[12:15]\nv_mfma_f32_16x16x32_f16 v[20:23], v[124:127], v[36:39], 0\n" \
  "ds_read_b128 v[124:127], v2 offset:2304\nds_read_b128 v[36:39], v2 offset:2368\nv_mfma_f32_16x16x32_f16 v[12:15], v[116:119], v[40:43], v[12:15]\n" \
  "ds_read_b128 v[40:43], v2 offset:2432\ns_waitcnt lgkmcnt(4)\nv_mfma_f32_16x16x32_f16 v[12:15], v[112:115], v[44:47], v[12:15]\n" \
  "ds_read_b128 v[44:47], v2 offset:2496\nv_mfma_f32_16x16x32_f16 v[12:15], v[108:111], v[28:31], v[12:15]\nds_read_b128 v[28:31], v2 offset:2560\n" \
  "s_waitcnt lgkmcnt(4)\nv_mfma_f32_16x16x32_f16 v[12:15], v[104:107], v[32:35], v[12:15]\nds_read_b128 v[32:35], v2 offset:2624\n" \
  "v_mfma_f32_16x16x32_f16 v[12:15], v[100:103], v[124:127], v[12:15]\ns_waitcnt lgkmcnt(3)\n" \
  "v_mfma_f32_16x16x32_f16 v[12:15], v[96:99], v[36:39], v[12:15]\nds_read_b128 v[124:127], v2 offset:2688\n" \
  "v_mfma_f32_16x16x32_f16 v[12:15], v[92:95], v[40:43], v[12:15]\ns_waitcnt lgkmcnt(3)\n" \
  "v_mfma_f32_16x16x32_f16 v[12:15], v[88:91], v[44:47], v[12:15]\nds_read_b128 v[36:39], v2 offset:2752\n" \
  "v_mfma_f32_16x16x32_f16 v[16:19], v[120:123], v[44:47], v[16:19]\ns_waitcnt lgkmcnt(3)\n" \
  "v_mfma_f32_16x16x32_f16 v[12:15], v[84:87], v[28:31], v[12:15]\nds_read_b128 v[40:43], v2 offset:2816\n" \
  "v_mfma_f32_16x16x32_f16 v[16:19], v[116:119], v[28:31], v[16:19]\ns_waitcnt lgkmcnt(3)\n" \
  "v_mfma_f32_16x16x32_f16 v[12:15], v[80:83], v[32:35], v[12:15]\nds_read_b128 v[44:47], v2 offset:2880\n" \
  "v_mfma_f32_16x16x32_f16 v[16:19], v[112:115], v[32:35], v[16:19]\ns_waitcnt lgkmcnt(3)\n" \
  "v_mfma_f32_16x16x32_f16 v[12:15], v[76:79], v[124:127], v[12:15]\nds_read_b128 v[28:31], v2 offset:2944\n" \
  "v_mfma_f32_16x16x32_f16 v[16:19], v[108:111], v[124:127], v[16:19]\ns_waitcnt lgkmcnt(3)\n" \
  "v_mfma_f32_16x16x32_f16 v[12:15], v[72:75], v[36:39], v[12:15]\nds_read_b128 v[32:35], v2 offset:3008\n" \
  "v_mfma_f32_16x16x32_f16 v[16:19], v[104:107], v[36:39], v[16:19]\ns_waitcnt lgkmcnt(3)\n" \
  "v_mfma_f32_16x16x32_f16 v[12:15], v[68:71], v[40:43], v[12:15]\nv_mfma_f32_16x16x32_f16 v[16:19], v[100:103], v[40:43], v[16:19]\n" \
  "ds_read_b128 v[124:127], v2 offset:3072\ns_waitcnt lgkmcnt(3)\nv_mfma_f32_16x16x32_f16 v[12:15], v[64:67], v[44:47], v[12:15]\n" \
  "v_mfma_f32_16x16x32_f16 v[16:19], v[96:99], v[44:47], v[16:19]\nds_read_b128 v[36:39], v2 offset:3136\ns_waitcnt lgkmcnt(3)\n" \
  "v_mfma_f32_16x16x32_f16 v[12:15], v[60:63], v[28:31], v[12:15]\nds_read_b64 v[40:41], v6 offset:128\nds_read_b64 v[42:43], v6 offset:136\n" \
  "v_mfma_f32_16x16x32_f16 v[16:19], v[92:95], v[28:31], v[16:19]\nds_read_b128 v[44:47], v2 offset:3200\ns_waitcnt lgkmcnt(4)\n" \
  "v_mfma_f32_16x16x32_f16 v[12:15], v[56:59], v[32:35], v[12:15]\nv_mfma_f32_16x16x32_f16 v[16:19], v[88:91], v[32:35], v[16:19]\n" \
  "ds_read_b128 v[28:31], v2 offset:3264\nv_mfma_f32_16x16x32_f16 v[20:23], v[120:123], v[32:35], v[20:23]\nds_read_b64 v[120:121], v6 offset:192\n" \
  "ds_read_b64 v[122:123], v6 offset:200\nv_mfma_f32_16x16x32_f16 v[16:19], v[84:87], v[124:127], v[16:19]\nds_read_b128 v[32:35], v2 offset:3328\n" \
  "v_mfma_f32_16x16x32_f16 v[20:23], v[116:119], v[124:127], v[20:23]\nds_read_b64 v[116:117], v6 offset:256\nds_read_b64 v[118:119], v6 offset:264\n" \
  "s_waitcnt lgkmcnt(6)\nv_mfma_f32_16x16x32_f16 v[16:19], v[80:83], v[36:39], v[16:19]\nds_read_b128 v[124:127], v2 offset:3392\n" \
  "v_mfma_f32_16x16x32_f16 v[20:23], v[112:115], v[36:39], v[20:23]\nds_read_b64 v[112:113], v6 offset:320\nds_read_b64 v[114:115], v6 offset:328\n" \
  "v_mfma_f32_16x16x32_f16 v[16:19], v[76:79], v[44:47], v[16:19]\nds_read_b128 v[36:39], v2 offset:3456\n" \
  "v_mfma_f32_16x16x32_f16 v[20:23], v[108:111], v[44:47], v[20:23]\ns_waitcnt lgkmcnt(6)\n" \
  "v_mfma_f32_16x16x32_f16 v[16:19], v[72:75], v[28:31], v[16:19]\nds_read_b128 v[108:111], v2 offset:3520\n" \
  "v_mfma_f32_16x16x32_f16 v[20:23], v[104:107], v[28:31], v[20:23]\nv_mfma_f32_16x16x32_f16 v[16:19], v[68:71], v[32:35], v[16:19]\n" \
  "ds_read_b128 v[44:47], v2 offset:3584\nv_mfma_f32_16x16x32_f16 v[20:23], v[100:103], v[32:35], v[20:23]\nds_read_b128 v[104:107], v2 offset:3648\n" \
  "ds_read_b32 v27, v9 offset:32\ns_waitcnt lgkmcnt(4)\nv_mfma_f32_16x16x32_f16 v[16:19], v[64:67], v[124:127], v[16:19]\n" \
  "ds_read_b128 v[28:31], v2 offset:3712\nv_mfma_f32_16x16x32_f16 v[20:23], v[96:99], v[124:127], v[20:23]\nds_read_b128 v[100:103], v2 offset:3776\n" \
  "ds_read2_b32 v[32:33], v8 offset0:32 offset1:33\nds_read2_b32 v[34:35], v8 offset0:34 offset1:35\n" \
  "v_mfma_f32_16x16x32_f16 v[16:19], v[60:63], v[36:39], v[16:19]\nds_read_b128 v[96:99], v2 offset:3840\n" \
  "v_mfma_f32_16x16x32_f16 v[20:23], v[92:95], v[36:39], v[20:23]\nds_read_b128 v[124:127], v2 offset:3904\ns_waitcnt lgkmcnt(8)\n" \
  "v_mfma_f32_16x16x32_f16 v[16:19], v[56:59], v[108:111], v[16:19]\nds_read_b128 v[92:95], v2 offset:3968\ns_waitcnt lgkmcnt(7)\n" \
  "v_readfirstlane_b32 s85, v27\ns_cmp_lt_u32 s85, 4\ns_cbranch_scc1 31f\n32:\nv_mfma_f32_16x16x32_f16 v[20:23], v[88:91], v[108:111], v[20:23]\n" \
  "ds_read_b128 v[36:39], v2 offset:4032\nds_read_b64_tr_b16 v[88:89], v5 offset:512\nds_read_b64_tr_b16 v[90:91], v5 offset:544\n" \
  "v_mfma_f32_16x16x32_f16 v[20:23], v[84:87], v[44:47], v[20:23]\nds_read_b64_tr_b16 v[108:109], v5 offset:768\n" \
  "ds_read_b64_tr_b16 v[110:111], v5 offset:800\nv_mfma_f32_16x16x32_f16 v[20:23], v[80:83], v[104:107], v[20:23]\n" \
  "ds_read_b64_tr_b16 v[84:85], v5 offset:1024\nds_read_b64_tr_b16 v[86:87], v5 offset:1056\ns_waitcnt lgkmcnt(12)\n" \
  "v_mfma_f32_16x16x32_f16 v[20:23], v[76:79], v[28:31], v[20:23]\nds_read_b64_tr_b16 v[44:45], v5 offset:1280\n" \
  "ds_read_b64_tr_b16 v[46:47], v5 offset:1312\nv_mfma_f32_16x16x32_f16 v[20:23], v[72:75], v[100:103], v[20:23]\ns_waitcnt lgkmcnt(10)\n" \
  "v_mfma_f32_16x16x32_f16 v[20:23], v[68:71], v[96:99], v[20:23]\nds_read_b64_tr_b16 v[80:81], v5 offset:1536\n" \
  "ds_read_b64_tr_b16 v[82:83], v5 offset:1568\nv_mfma_f32_16x16x32_f16 v[20:23], v[64:67], v[124:127], v[20:23]\n" \
  "ds_read_b64_tr_b16 v[104:105], v5 offset:1792\nds_read_b64_tr_b16 v[106:107], v5 offset:1824\ns_waitcnt lgkmcnt(12)\n" \
  "v_mfma_f32_16x16x32_f16 v[20:23], v[60:63], v[92:95], v[20:23]\nds_read_b64_tr_b16 v[76:77], v5 offset:2048\n" \
  "ds_read_b64_tr_b16 v[78:79], v5 offset:2080\nv_mfma_f32_16x16x32_f16 v[20:23], v[56:59], v[36:39], v[20:23]\ns_waitcnt lgkmcnt(10)\n" \
  "v_mfma_f32_16x16x32_f16 v[12:15], v[88:91], v[52:55], v[12:15]\nds_read_b64_tr_b16 v[28:29], v5 offset:2304\n" \
  "ds_read_b64_tr_b16 v[30:31], v5 offset:2336\nv_mfma_f32_16x16x32_f16 v[12:15], v[108:111], v[48:51], v[12:15]\n" \
  "ds_read_b64_tr_b16 v[72:73], v5 offset:2560\nds_read_b64_tr_b16 v[74:75], v5 offset:2592\ns_waitcnt lgkmcnt(10)\n" \
  "v_mfma_f32_16x16x32_f16 v[12:15], v[84:87], v[40:43], v[12:15]\nds_read_b64_tr_b16 v[100:101], v5 offset:2816\n" \
  "ds_read_b64_tr_b16 v[102:103], v5 offset:2848\nv_mfma_f32_16x16x32_f16 v[16:19], v[84:87], v[52:55], v[16:19]\n" \
  "ds_read_b64_tr_b16 v[68:69], v7 offset:0\nds_read_b64_tr_b16 v[70:71], v7 offset:64\n" \
  "v_mfma_f32_16x16x32_f16 v[12:15], v[44:47], v[120:123], v[12:15]\nv_mfma_f32_16x16x32_f16 v[16:19], v[44:47], v[48:51], v[16:19]\n" \
  "s_waitcnt lgkmcnt(10)\nv_mfma_f32_16x16x32_f16 v[12:15], v[80:83], v[116:119], v[12:15]\nds_read2_b32 v[96:97], v8 offset0:16 offset1:17\n" \
  "ds_read2_b32 v[98:99], v8 offset0:18 offset1:19\nv_mfma_f32_16x16x32_f16 v[16:19], v[80:83], v[40:43], v[16:19]\n" \
  "ds_read2_b32 v[64:65], v8 offset0:0 offset1:1\nds_read2_b32 v[66:67], v8 offset0:2 offset1:3\n" \
  "v_mfma_f32_16x16x32_f16 v[20:23], v[80:83], v[52:55], v[20:23]\nv_mfma_f32_16x16x32_f16 v[12:15], v[104:107], v[112:115], v[12:15]\n" \
  "v_mfma_f32_16x16x32_f16 v[16:19], v[104:107], v[120:123], v[16:19]\nv_mfma_f32_16x16x32_f16 v[20:23], v[104:107], v[48:51], v[20:23]\n" \
  "s_waitcnt lgkmcnt(10)\nv_mfma_f32_16x16x32_f16 v[16:19], v[76:79], v[116:119], v[16:19]\nds_read_b64_tr_b16 v[124:125], v7 offset:512\n" \
  "ds_read_b64_tr_b16 v[126:127], v7 offset:576\nv_mfma_f32_16x16x32_f16 v[20:23], v[76:79], v[40:43], v[20:23]\n" \
  "ds_read2_b32 v[60:61], v8 offset0:48 offset1:49\nds_read2_b32 v[62:63], v8 offset0:50 offset1:51\n" \
  "v_mfma_f32_16x16x32_f16 v[16:19], v[28:31], v[112:115], v[16:19]\nv_mfma_f32_16x16x32_f16 v[20:23], v[28:31], v[120:123], v[20:23]\n" \
  "s_waitcnt lgkmcnt(10)\nv_mfma_f32_16x16x32_f16 v[20:23], v[72:75], v[116:119], v[20:23]\nds_read_b64_tr_b16 v[92:93], v7 offset:1024\n" \
  "ds_read_b64_tr_b16 v[94:95], v7 offset:1088\nv_mfma_f32_16x16x32_f16 v[20:23], v[100:103], v[112:115], v[20:23]\n" \
  "ds_read_b64_tr_b16 v[56:57], v7 offset:1536\nds_read_b64_tr_b16 v[58:59], v7 offset:1600\ns_waitcnt lgkmcnt(10)\n" \
  "v_mfma_f32_16x16x32_f16 v[12:15], v[68:71], v[32:35], v[12:15]\nv_mfma_f32_16x16x32_f16 v[16:19], v[68:71], v[96:99], v[16:19]\n" \
  "s_waitcnt lgkmcnt(6)\nv_mfma_f32_16x16x32_f16 v[20:23], v[68:71], v[64:67], v[20:23]\ns_waitcnt lgkmcnt(2)\n" \
  "v_mfma_f32_16x16x32_f16 v[12:15], v[124:127], v[60:63], v[12:15]\nv_mfma_f32_16x16x32_f16 v[16:19], v[124:127], v[32:35], v[16:19]\n" \
  "v_mfma_f32_16x16x32_f16 v[20:23], v[124:127], v[96:99], v[20:23]\nv_mfma_f32_16x16x32_f16 v[16:19], v[92:95], v[60:63], v[16:19]\n" \
  "v_mfma_f32_16x16x32_f16 v[20:23], v[92:95], v[32:35], v[20:23]\ns_waitcnt lgkmcnt(0)\n" \
  "v_mfma_f32_16x16x32_f16 v[20:23], v[56:59], v[60:63], v[20:23]\ns_nop 1\nv_cvt_pkrtz_f16_f32 v24, v12, v13\nv_cvt_pkrtz_f16_f32 v25, v14, v15\n" \
  "v_pk_add_u16 v24, v24, 4 op_sel_hi:[1,0] clamp\nv_pk_add_u16 v25, v25, 4 op_sel_hi:[1,0] clamp\nds_write_b64 v4, v[24:25] offset:5120\n" \
  "v_cvt_pkrtz_f16_f32 v24, v16, v17\nv_cvt_pkrtz_f16_f32 v25, v18, v19\nv_pk_add_u16 v24, v24, 4 op_sel_hi:[1,0] clamp\n" \
  "v_pk_add_u16 v25, v25, 4 op_sel_hi:[1,0] clamp\nds_write_b64 v4, v[24:25] offset:6144\nv_cvt_pkrtz_f16_f32 v24, v20, v21\n" \
  "v_cvt_pkrtz_f16_f32 v25, v22, v23\nv_pk_add_u16 v24, v24, 4 op_sel_hi:[1,0] clamp\nv_pk_add_u16 v25, v25, 4 op_sel_hi:[1,0] clamp\n" \
  "ds_write_b64 v4, v[24:25] offset:7168\ns_setprio 0\ns_setpc_b64 s[90:91]\n31:\ns_movk_i32 s84, 0x4000\n33:\nds_read_b32 v27, v9 offset:32\n" \
  "s_sub_u32 s84, s84, 1\ns_cbranch_scc1 32b\ns_waitcnt lgkmcnt(0)\nv_readfirstlane_b32 s85, v27\ns_cmp_lt_u32 s85, 4\ns_cbranch_scc1 33b\n" \
  "s_branch 32b\n9:\ns_cmp_eq_u32 s89, 3\ns_cbranch_scc1 3f\ns_cmp_eq_u32 s89, 4\ns_cbranch_scc1 4f\ns_cmp_eq_u32 s89, 5\ns_cbranch_scc1 5f\n" \
  "ds_read_b64 v[124:125], v3 offset:576\nds_read_b64 v[126:127], v3 offset:584\nds_read_b64 v[120:121], v3 offset:64\n" \
  "ds_read_b64 v[122:123], v3 offset:72\nds_read_b64 v[116:117], v3 offset:640\nds_read_b64 v[118:119], v3 offset:648\n" \
  "ds_read_b64 v[112:113], v3 offset:128\nds_read_b64 v[114:115], v3 offset:136\nds_read_b64 v[108:109], v3 offset:704\n" \
  "ds_read_b64 v[110:111], v3 offset:712\nds_read_b64 v[104:105], v3 offset:192\nds_read_b64 v[106:107], v3 offset:200\n" \
  "ds_read_b64 v[100:101], v3 offset:768\nds_read_b64 v[102:103], v3 offset:776\nds_read_b64 v[96:97], v3 offset:256\n" \
  "ds_read_b64 v[98:99], v3 offset:264\nds_read_b64 v[92:93], v3 offset:832\nds_read_b64 v[94:95], v3 offset:840\n" \
  "ds_read_b64 v[88:89], v3 offset:320\nds_read_b64 v[90:91], v3 offset:328\nds_read_b64 v[84:85], v3 offset:896\n" \
  "ds_read_b64 v[86:87], v3 offset:904\nds_read_b64 v[80:81], v3 offset:384\nds_read_b64 v[82:83], v3 offset:392\n" \
  "ds_read_b64 v[76:77], v3 offset:960\nds_read_b64 v[78:79], v3 offset:968\nds_read_b64 v[72:73], v3 offset:448\n" \
  "ds_read_b64 v[74:75], v3 offset:456\nds_read_b64 v[68:69], v3 offset:1024\nds_read_b64 v[70:71], v3 offset:1032\n" \
  "ds_read_b64 v[64:65], v3 offset:512\nds_read_b64 v[66:67], v3 offset:520\ns_waitcnt lgkmcnt(0)\ns_barrier\nds_read_b128 v[24:27], v2 offset:0\n" \
  "ds_read_b128 v[28:31], v2 offset:64\nds_read_b128 v[32:35], v2 offset:128\nds_read_b128 v[36:39], v2 offset:192\ns_waitcnt lgkmcnt(3)\n" \
  "v_mfma_f32_16x16x32_f16 v[12:15], v[124:127], v[24:27], 0\ns_setprio 1\nds_read_b128 v[40:43], v2 offset:256\n" \
  "v_mfma_f32_16x16x32_f16 v[16:19], v[120:123], v[24:27], 0\ns_waitcnt lgkmcnt(3)\nv_mfma_f32_16x16x32_f16 v[12:15], v[116:119], v[28:31], v[12:15]\n" \
  "ds_read_b128 v[44:47], v2 offset:320\nv_mfma_f32_16x16x32_f16 v[16:19], v[112:115], v[28:31], v[16:19]\ns_waitcnt lgkmcnt(3)\n" \
  "v_mfma_f32_16x16x32_f16 v[12:15], v[108:111], v[32:35], v[12:15]\nds_read_b128 v[48:51], v2 offset:384\n" \
  "v_mfma_f32_16x16x32_f16 v[16:19], v[104:107], v[32:35], v[16:19]\ns_waitcnt lgkmcnt(3)\n" \
  "v_mfma_f32_16x16x32_f16 v[12:15], v[100:103], v[36:39], v[12:15]\nds_read_b128 v[52:55], v2 offset:448\n" \
  "v_mfma_f32_16x16x32_f16 v[16:19], v[96:99], v[36:39], v[16:19]\ns_waitcnt lgkmcnt(3)\n" \
  "v_mfma_f32_16x16x32_f16 v[12:15], v[92:95], v[40:43], v[12:15]\nds_read_b128 v[56:59], v2 offset:512\n" \
  "v_mfma_f32_16x16x32_f16 v[16:19], v[88:91], v[40:43], v[16:19]\nds_read_b128 v[60:63], v2 offset:576\ns_waitcnt lgkmcnt(3)\n" \
  "v_mfma_f32_16x16x32_f16 v[12:15], v[84:87], v[44:47], v[12:15]\nds_read_b128 v[120:123], v2 offset:640\n" \
  "v_mfma_f32_16x16x32_f16 v[16:19], v[80:83], v[44:47], v[16:19]\nds_read_b128 v[24:27], v2 offset:704\n" \
  "v_mfma_f32_16x16x32_f16 v[12:15], v[76:79], v[48:51], v[12:15]\nds_read_b128 v[112:115], v2 offset:768\n" \
  "v_mfma_f32_16x16x32_f16 v[16:19], v[72:75], v[48:51], v[16:19]\nds_read_b128 v[28:31], v2 offset:832\ns_waitcnt lgkmcnt(5)\n" \
  "v_mfma_f32_16x16x32_f16 v[12:15], v[68:71], v[52:55], v[12:15]\nds_read_b128 v[104:107], v2 offset:896\n" \
  "v_mfma_f32_16x16x32_f16 v[16:19], v[64:67], v[52:55], v[16:19]\nds_read_b128 v[32:35], v2 offset:960\n" \
  "v_mfma_f32_16x16x32_f16 v[16:19], v[124:127], v[56:59], v[16:19]\ns_waitcnt lgkmcnt(5)\n" \
  "v_mfma_f32_16x16x32_f16 v[16:19], v[116:119], v[60:63], v[16:19]\nv_mfma_f32_16x16x32_f16 v[16:19], v[108:111], v[120:123], v[16:19]\n" \
  "s_waitcnt lgkmcnt(3)\nv_mfma_f32_16x16x32_f16 v[16:19], v[100:103], v[24:27], v[16:19]\nv_cvt_pkrtz_f16_f32 v20, v12, v13\n" \
  "v_cvt_pkrtz_f16_f32 v21, v14, v15\nv_mfma_f32_16x16x32_f16 v[16:19], v[92:95], v[112:115], v[16:19]\n" \
  "v_pk_add_u16 v20, v20, 4 op_sel_hi:[1,0] clamp\ns_waitcnt lgkmcnt(2)\nv_mfma_f32_16x16x32_f16 v[16:19], v[84:87], v[28:31], v[16:19]\n" \
  "v_pk_add_u16 v21, v21, 4 op_sel_hi:[1,0] clamp\ns_waitcnt lgkmcnt(1)\nv_mfma_f32_16x16x32_f16 v[16:19], v[76:79], v[104:107], v[16:19]\n" \
  "ds_write_b64 v4, v[20:21] offset:0\ns_waitcnt lgkmcnt(1)\nv_mfma_f32_16x16x32_f16 v[16:19], v[68:71], v[32:35], v[16:19]\ns_nop 7\n" \
  "v_cvt_pkrtz_f16_f32 v20, v16, v17\nv_cvt_pkrtz_f16_f32 v21, v18, v19\nv_pk_add_u16 v20, v20, 4 op_sel_hi:[1,0] clamp\n" \
  "v_pk_add_u16 v21, v21, 4 op_sel_hi:[1,0] clamp\nds_write_b64 v4, v[20:21] offset:1024\ns_setprio 0\ns_setpc_b64 s[90:91]\n3:\n" \
  "ds_read_b64 v[124:125], v3 offset:0\nds_read_b64 v[126:127], v3 offset:8\nds_read_b64 v[120:121], v3 offset:64\n" \
  "ds_read_b64 v[122:123], v3 offset:72\nds_read_b64 v[116:117], v3 offset:128\nds_read_b64 v[118:119], v3 offset:136\n" \
  "ds_read_b64 v[112:113], v3 offset:192\nds_read_b64 v[114:115], v3 offset:200\nds_read_b64 v[108:109], v3 offset:256\n" \
  "ds_read_b64 v[110:111], v3 offset:264\nds_read_b64 v[104:105], v3 offset:320\nds_read_b64 v[106:107], v3 offset:328\n" \
  "ds_read_b64 v[100:101], v3 offset:384\nds_read_b64 v[102:103], v3 offset:392\nds_read_b64 v[96:97], v3 offset:448\n" \
  "ds_read_b64 v[98:99], v3 offset:456\nds_read_b64 v[92:93], v3 offset:512\nds_read_b64 v[94:95], v3 offset:520\n" \
  "ds_read_b64 v[88:89], v3 offset:576\nds_read_b64 v[90:91], v3 offset:584\nds_read_b64 v[84:85], v3 offset:640\n" \
  "ds_read_b64 v[86:87], v3 offset:648\nds_read_b64 v[80:81], v3 offset:704\nds_read_b64 v[82:83], v3 offset:712\n" \
  "ds_read_b64 v[76:77], v3 offset:768\nds_read_b64 v[78:79], v3 offset:776\nds_read_b64 v[72:73], v3 offset:832\n" \
  "ds_read_b64 v[74:75], v3 offset:840\nds_read_b64 v[68:69], v3 offset:896\nds_read_b64 v[70:71], v3 offset:904\n" \
  "ds_read_b64 v[64:65], v3 offset:960\nds_read_b64 v[66:67], v3 offset:968\nds_read_b64 v[60:61], v3 offset:1024\n" \
  "ds_read_b64 v[62:63], v3 offset:1032\nds_read_b64 v[56:57], v6 offset:256\nds_read_b64 v[58:59], v6 offset:264\n" \
  "ds_read_b64 v[52:53], v6 offset:128\nds_read_b64 v[54:55], v6 offset:136\nds_read_b64 v[48:49], v6 offset:320\n" \
  "ds_read_b64 v[50:51], v6 offset:328\nds_read_b64 v[44:45], v6 offset:192\nds_read_b64 v[46:47], v6 offset:200\ns_waitcnt lgkmcnt(0)\ns_barrier\n" \
  "ds_read_b128 v[24:27], v2 offset:448\nds_read_b128 v[28:31], v2 offset:512\nds_read_b128 v[32:35], v2 offset:576\n" \
  "ds_read_b128 v[36:39], v2 offset:640\nds_read_b128 v[40:43], v2 offset:704\ns_waitcnt lgkmcnt(3)\n" \
  "v_mfma_f32_16x16x32_f16 v[12:15], v[124:127], v[24:27], 0\ns_setprio 1\nds_read_b128 v[24:27], v2 offset:768\n" \
  "v_mfma_f32_16x16x32_f16 v[12:15], v[120:123], v[28:31], v[12:15]\nds_read_b128 v[28:31], v2 offset:832\ns_waitcnt lgkmcnt(3)\n" \
  "v_mfma_f32_16x16x32_f16 v[12:15], v[116:119], v[32:35], v[12:15]\nds_read_b128 v[32:35], v2 offset:896\n" \
  "v_mfma_f32_16x16x32_f16 v[12:15], v[112:115], v[36:39], v[12:15]\nds_read_b128 v[36:39], v2 offset:960\ns_waitcnt lgkmcnt(3)\n" \
  "v_mfma_f32_16x16x32_f16 v[12:15], v[108:111], v[40:43], v[12:15]\nds_read_b128 v[40:43], v2 offset:1024\n" \
  "v_mfma_f32_16x16x32_f16 v[12:15], v[104:107], v[24:27], v[12:15]\nds_read_b128 v[24:27], v2 offset:1088\ns_waitcnt lgkmcnt(3)\n" \
  "v_mfma_f32_16x16x32_f16 v[12:15], v[100:103], v[28:31], v[12:15]\nds_read_b128 v[28:31], v2 offset:1152\n" \
  "v_mfma_f32_16x16x32_f16 v[12:15], v[96:99], v[32:35], v[12:15]\ns_waitcnt lgkmcnt(3)\n" \
  "v_mfma_f32_16x16x32_f16 v[12:15], v[92:95], v[36:39], v[12:15]\nds_read_b128 v[32:35], v2 offset:1216\n" \
  "v_mfma_f32_16x16x32_f16 v[16:19], v[124:127], v[36:39], 0\ns_waitcnt lgkmcnt(3)\nv_mfma_f32_16x16x32_f16 v[12:15], v[88:91], v[40:43], v[12:15]\n" \
  "ds_read_b128 v[124:127], v2 offset:1280\nv_mfma_f32_16x16x32_f16 v[16:19], v[120:123], v[40:43], v[16:19]\ns_waitcnt lgkmcnt(3)\n" \
  "v_mfma_f32_16x16x32_f16 v[12:15], v[84:87], v[24:27], v[12:15]\nds_read_b128 v[36:39], v2 offset:1344\n" \
  "v_mfma_f32_16x16x32_f16 v[16:19], v[116:119], v[24:27], v[16:19]\ns_waitcnt lgkmcnt(3)\n" \
  "v_mfma_f32_16x16x32_f16 v[12:15], v[80:83], v[28:31], v[12:15]\nds_read_b128 v[120:123], v2 offset:1408\n" \
  "v_mfma_f32_16x16x32_f16 v[16:19], v[112:115], v[28:31], v[16:19]\ns_waitcnt lgkmcnt(3)\n" \
  "v_mfma_f32_16x16x32_f16 v[12:15], v[76:79], v[32:35], v[12:15]\nds_read_b128 v[40:43], v2 offset:1472\n" \
  "v_mfma_f32_16x16x32_f16 v[16:19], v[108:111], v[32:35], v[16:19]\ns_waitcnt lgkmcnt(3)\n" \
  "v_mfma_f32_16x16x32_f16 v[12:15], v[72:75], v[124:127], v[12:15]\nds_read_b128 v[116:119], v2 offset:1536\n" \
  "v_mfma_f32_16x16x32_f16 v[16:19], v[104:107], v[124:127], v[16:19]\nds_read_b128 v[24:27], v2 offset:1600\ns_waitcnt lgkmcnt(3)\n" \
  "v_mfma_f32_16x16x32_f16 v[12:15], v[68:71], v[36:39], v[12:15]\nds_read_b128 v[112:115], v2 offset:1664\nds_read_b32 v23, v9 offset:32\n" \
  "v_mfma_f32_16x16x32_f16 v[16:19], v[100:103], v[36:39], v[16:19]\nds_read_b128 v[28:31], v2 offset:1728\n" \
  "v_mfma_f32_16x16x32_f16 v[12:15], v[64:67], v[120:123], v[12:15]\nds_read_b128 v[108:111], v2 offset:1792\n" \
  "v_mfma_f32_16x16x32_f16 v[16:19], v[96:99], v[120:123], v[16:19]\nds_read_b128 v[32:35], v2 offset:1856\ns_waitcnt lgkmcnt(6)\n" \
  "v_mfma_f32_16x16x32_f16 v[12:15], v[60:63], v[40:43], v[12:15]\nds_read_b128 v[104:107], v2 offset:1920\n" \
  "v_mfma_f32_16x16x32_f16 v[16:19], v[92:95], v[40:43], v[16:19]\nds_read_b128 v[124:127], v2 offset:1984\ns_waitcnt lgkmcnt(5)\n" \
  "v_readfirstlane_b32 s85, v23\ns_cmp_lt_u32 s85, 3\ns_cbranch_scc1 51f\n52:\nv_mfma_f32_16x16x32_f16 v[16:19], v[88:91], v[116:119], v[16:19]\n" \
  "ds_read_b64_tr_b16 v[100:101], v5 offset:0\nds_read_b64_tr_b16 v[102:103], v5 offset:32\n" \
  "v_mfma_f32_16x16x32_f16 v[16:19], v[84:87], v[24:27], v[16:19]\nds_read_b64_tr_b16 v[36:37], v5 offset:256\n" \
  "ds_read_b64_tr_b16 v[38:39], v5 offset:288\nv_mfma_f32_16x16x32_f16 v[16:19], v[80:83], v[112:115], v[16:19]\n" \
  "ds_read_b64_tr_b16 v[96:97], v5 offset:512\nds_read_b64_tr_b16 v[98:99], v5 offset:544\ns_waitcnt lgkmcnt(9)\n" \
  "v_mfma_f32_16x16x32_f16 v[16:19], v[76:79], v[28:31], v[16:19]\nds_read_b64_tr_b16 v[120:121], v5 offset:768\n" \
  "ds_read_b64_tr_b16 v[122:123], v5 offset:800\nv_mfma_f32_16x16x32_f16 v[16:19], v[72:75], v[108:111], v[16:19]\ns_waitcnt lgkmcnt(9)\n" \
  "v_mfma_f32_16x16x32_f16 v[16:19], v[68:71], v[32:35], v[16:19]\nv_mfma_f32_16x16x32_f16 v[16:19], v[64:67], v[104:107], v[16:19]\n" \
  "s_waitcnt lgkmcnt(6)\nv_mfma_f32_16x16x32_f16 v[16:19], v[60:63], v[124:127], v[16:19]\n" \
  "v_mfma_f32_16x16x32_f16 v[12:15], v[100:103], v[56:59], v[12:15]\nv_mfma_f32_16x16x32_f16 v[16:19], v[100:103], v[52:55], v[16:19]\n" \
  "s_waitcnt lgkmcnt(2)\nv_mfma_f32_16x16x32_f16 v[12:15], v[36:39], v[48:51], v[12:15]\n" \
  "v_mfma_f32_16x16x32_f16 v[16:19], v[36:39], v[44:47], v[16:19]\nv_mfma_f32_16x16x32_f16 v[16:19], v[96:99], v[56:59], v[16:19]\n" \
  "s_waitcnt lgkmcnt(0)\nv_mfma_f32_16x16x32_f16 v[16:19], v[120:123], v[48:51], v[16:19]\ns_nop 3\nv_cvt_pkrtz_f16_f32 v20, v12, v13\n" \
  "v_cvt_pkrtz_f16_f32 v21, v14, v15\nv_pk_add_u16 v20, v20, 4 op_sel_hi:[1,0] clamp\nv_pk_add_u16 v21, v21, 4 op_sel_hi:[1,0] clamp\n" \
  "ds_write_b64 v4, v[20:21] offset:2048\nv_cvt_pkrtz_f16_f32 v20, v16, v17\nv_cvt_pkrtz_f16_f32 v21, v18, v19\n" \
  "v_pk_add_u16 v20, v20, 4 op_sel_hi:[1,0] clamp\nv_pk_add_u16 v21, v21, 4 op_sel_hi:[1,0] clamp\nds_write_b64 v4, v[20:21] offset:3072\n" \
  "s_setprio 0\ns_setpc_b64 s[90:91]\n51:\ns_movk_i32 s84, 0x4000\n53:\nds_read_b32 v23, v9 offset:32\ns_sub_u32 s84, s84, 1\ns_cbranch_scc1 52b\n" \
  "s_waitcnt lgkmcnt(0)\nv_readfirstlane_b32 s85, v23\ns_cmp_lt_u32 s85, 3\ns_cbranch_scc1 53b\ns_branch 52b\n4:\n" \
  "ds_read_b64 v[124:125], v3 offset:1088\nds_read_b64 v[126:127], v3 offset:1096\nds_read_b64 v[120:121], v3 offset:0\n" \
  "ds_read_b64 v[122:123], v3 offset:8\nds_read_b64 v[116:117], v3 offset:64\nds_read_b64 v[118:119], v3 offset:72\n" \
  "ds_read_b64 v[112:113], v3 offset:128\nds_read_b64 v[114:115], v3 offset:136\nds_read_b64 v[108:109], v3 offset:192\n" \
  "ds_read_b64 v[110:111], v3 offset:200\nds_read_b64 v[104:105], v3 offset:256\nds_read_b64 v[106:107], v3 offset:264\n" \
  "ds_read_b64 v[100:101], v3 offset:320\nds_read_b64 v[102:103], v3 offset:328\nds_read_b64 v[96:97], v3 offset:384\n" \
  "ds_read_b64 v[98:99], v3 offset:392\nds_read_b64 v[92:93], v3 offset:448\nds_read_b64 v[94:95], v3 offset:456\n" \
  "ds_read_b64 v[88:89], v3 offset:512\nds_read_b64 v[90:91], v3 offset:520\nds_read_b64 v[84:85], v3 offset:576\n" \
  "ds_read_b64 v[86:87], v3 offset:584\nds_read_b64 v[80:81], v3 offset:640\nds_read_b64 v[82:83], v3 offset:648\n" \
  "ds_read_b64 v[76:77], v3 offset:704\nds_read_b64 v[78:79], v3 offset:712\nds_read_b64 v[72:73], v3 offset:768\n" \
  "ds_read_b64 v[74:75], v3 offset:776\nds_read_b64 v[68:69], v3 offset:832\nds_read_b64 v[70:71], v3 offset:840\n" \
  "ds_read_b64 v[64:65], v3 offset:896\nds_read_b64 v[66:67], v3 offset:904\nds_read_b64 v[60:61], v3 offset:960\n" \
  "ds_read_b64 v[62:63], v3 offset:968\nds_read_b64 v[56:57], v3 offset:1024\nds_read_b64 v[58:59], v3 offset:1032\n" \
  "ds_read_b64 v[52:53], v6 offset:0\nds_read_b64 v[54:55], v6 offset:8\nds_read_b64 v[48:49], v6 offset:64\nds_read_b64 v[50:51], v6 offset:72\n" \
  "ds_read_b64 v[44:45], v6 offset:128\nds_read_b64 v[46:47], v6 offset:136\nds_read_b64 v[40:41], v6 offset:192\n" \
  "ds_read_b64 v[42:43], v6 offset:200\ns_waitcnt lgkmcnt(0)\ns_barrier\nds_read_b128 v[24:27], v2 offset:448\nds_read_b128 v[28:31], v2 offset:960\n" \
  "ds_read_b128 v[32:35], v2 offset:1472\nds_read_b128 v[36:39], v2 offset:1536\ns_waitcnt lgkmcnt(3)\n" \
  "v_mfma_f32_16x16x32_f16 v[12:15], v[124:127], v[24:27], 0\ns_setprio 1\nds_read_b128 v[24:27], v2 offset:1600\ns_waitcnt lgkmcnt(3)\n" \
  "v_mfma_f32_16x16x32_f16 v[16:19], v[124:127], v[28:31], 0\nds_read_b128 v[124:127], v2 offset:1664\ns_waitcnt lgkmcnt(3)\n" \
  "v_mfma_f32_16x16x32_f16 v[12:15], v[120:123], v[32:35], v[12:15]\nds_read_b128 v[28:31], v2 offset:1728\ns_waitcnt lgkmcnt(3)\n" \
  "v_mfma_f32_16x16x32_f16 v[12:15], v[116:119], v[36:39], v[12:15]\nds_read_b128 v[32:35], v2 offset:1792\ns_waitcnt lgkmcnt(3)\n" \
  "v_mfma_f32_16x16x32_f16 v[12:15], v[112:115], v[24:27], v[12:15]\nds_read_b128 v[36:39], v2 offset:1856\ns_waitcnt lgkmcnt(3)\n" \
  "v_mfma_f32_16x16x32_f16 v[12:15], v[108:111], v[124:127], v[12:15]\nds_read_b128 v[24:27], v2 offset:1920\ns_waitcnt lgkmcnt(3)\n" \
  "v_mfma_f32_16x16x32_f16 v[12:15], v[104:107], v[28:31], v[12:15]\nds_read_b128 v[124:127], v2 offset:1984\ns_waitcnt lgkmcnt(3)\n" \
  "v_mfma_f32_16x16x32_f16 v[12:15], v[100:103], v[32:35], v[12:15]\nds_read_b128 v[28:31], v2 offset:2048\ns_waitcnt lgkmcnt(3)\n" \
  "v_mfma_f32_16x16x32_f16 v[12:15], v[96:99], v[36:39], v[12:15]\nds_read_b128 v[32:35], v2 offset:2112\ns_waitcnt lgkmcnt(3)\n" \
  "v_mfma_f32_16x16x32_f16 v[12:15], v[92:95], v[24:27], v[12:15]\nds_read_b128 v[36:39], v2 offset:2176\ns_waitcnt lgkmcnt(3)\n" \
  "v_mfma_f32_16x16x32_f16 v[12:15], v[88:91], v[124:127], v[12:15]\nds_read_b128 v[24:27], v2 offset:2240\n" \
  "v_mfma_f32_16x16x32_f16 v[16:19], v[120:123], v[124:127], v[16:19]\nds_read_b64 v[120:121], v6 offset:256\nds_read_b64 v[122:123], v6 offset:264\n" \
  "s_waitcnt lgkmcnt(4)\nv_mfma_f32_16x16x32_f16 v[12:15], v[84:87], v[28:31], v[12:15]\nds_read_b128 v[124:127], v2 offset:2304\n" \
  "v_mfma_f32_16x16x32_f16 v[16:19], v[116:119], v[28:31], v[16:19]\nds_read_b64 v[116:117], v6 offset:320\nds_read_b64 v[118:119], v6 offset:328\n" \
  "v_mfma_f32_16x16x32_f16 v[12:15], v[80:83], v[32:35], v[12:15]\nds_read_b128 v[28:31], v2 offset:2368\n" \
  "v_mfma_f32_16x16x32_f16 v[16:19], v[112:115], v[32:35], v[16:19]\ns_waitcnt lgkmcnt(6)\n" \
  "v_mfma_f32_16x16x32_f16 v[12:15], v[76:79], v[36:39], v[12:15]\nds_read_b128 v[112:115], v2 offset:2432\n" \
  "v_mfma_f32_16x16x32_f16 v[16:19], v[108:111], v[36:39], v[16:19]\nds_read2_b32 v[32:33], v8 offset0:48 offset1:49\n" \
  "ds_read2_b32 v[34:35], v8 offset0:50 offset1:51\nv_mfma_f32_16x16x32_f16 v[12:15], v[72:75], v[24:27], v[12:15]\n" \
  "ds_read_b128 v[108:111], v2 offset:2496\nv_mfma_f32_16x16x32_f16 v[16:19], v[104:107], v[24:27], v[16:19]\n" \
  "ds_read2_b32 v[36:37], v8 offset0:32 offset1:33\nds_read2_b32 v[38:39], v8 offset0:34 offset1:35\ns_waitcnt lgkmcnt(6)\n" \
  "v_mfma_f32_16x16x32_f16 v[12:15], v[68:71], v[124:127], v[12:15]\nds_read_b128 v[104:107], v2 offset:2560\n" \
  "v_mfma_f32_16x16x32_f16 v[16:19], v[100:103], v[124:127], v[16:19]\nds_read_b128 v[24:27], v2 offset:2624\n" \
  "v_mfma_f32_16x16x32_f16 v[12:15], v[64:67], v[28:31], v[12:15]\nds_read_b128 v[100:103], v2 offset:2688\nds_read_b32 v23, v9 offset:32\n" \
  "v_mfma_f32_16x16x32_f16 v[16:19], v[96:99], v[28:31], v[16:19]\nds_read_b128 v[124:127], v2 offset:2752\ns_waitcnt lgkmcnt(7)\n" \
  "v_mfma_f32_16x16x32_f16 v[12:15], v[60:63], v[112:115], v[12:15]\nds_read_b128 v[96:99], v2 offset:2816\n" \
  "v_mfma_f32_16x16x32_f16 v[16:19], v[92:95], v[112:115], v[16:19]\nds_read_b128 v[28:31], v2 offset:2880\n" \
  "v_mfma_f32_16x16x32_f16 v[12:15], v[56:59], v[108:111], v[12:15]\nds_read_b128 v[92:95], v2 offset:2944\n" \
  "v_mfma_f32_16x16x32_f16 v[16:19], v[88:91], v[108:111], v[16:19]\nds_read_b128 v[112:115], v2 offset:3008\ns_waitcnt lgkmcnt(5)\n" \
  "v_readfirstlane_b32 s85, v23\ns_cmp_lt_u32 s85, 3\ns_cbranch_scc1 61f\n62:\nv_mfma_f32_16x16x32_f16 v[16:19], v[84:87], v[104:107], v[16:19]\n" \
  "ds_read_b64_tr_b16 v[88:89], v5 offset:0\nds_read_b64_tr_b16 v[90:91], v5 offset:32\n" \
  "v_mfma_f32_16x16x32_f16 v[16:19], v[80:83], v[24:27], v[16:19]\nds_read_b64_tr_b16 v[108:109], v5 offset:256\n" \
  "ds_read_b64_tr_b16 v[110:111], v5 offset:288\nv_mfma_f32_16x16x32_f16 v[16:19], v[76:79], v[100:103], v[16:19]\n" \
  "ds_read_b64_tr_b16 v[84:85], v5 offset:512\nds_read_b64_tr_b16 v[86:87], v5 offset:544\ns_waitcnt lgkmcnt(9)\n" \
  "v_mfma_f32_16x16x32_f16 v[16:19], v[72:75], v[124:127], v[16:19]\nds_read_b64_tr_b16 v[104:105], v5 offset:768\n" \
  "ds_read_b64_tr_b16 v[106:107], v5 offset:800\nv_mfma_f32_16x16x32_f16 v[16:19], v[68:71], v[96:99], v[16:19]\n" \
  "ds_read_b64_tr_b16 v[80:81], v5 offset:1024\nds_read_b64_tr_b16 v[82:83], v5 offset:1056\ns_waitcnt lgkmcnt(11)\n" \
  "v_mfma_f32_16x16x32_f16 v[16:19], v[64:67], v[28:31], v[16:19]\nds_read_b64_tr_b16 v[24:25], v5 offset:1280\n" \
  "ds_read_b64_tr_b16 v[26:27], v5 offset:1312\nv_mfma_f32_16x16x32_f16 v[16:19], v[60:63], v[92:95], v[16:19]\n" \
  "ds_read_b64_tr_b16 v[76:77], v5 offset:1536\nds_read_b64_tr_b16 v[78:79], v5 offset:1568\ns_waitcnt lgkmcnt(12)\n" \
  "v_mfma_f32_16x16x32_f16 v[16:19], v[56:59], v[112:115], v[16:19]\nds_read_b64_tr_b16 v[100:101], v5 offset:1792\n" \
  "ds_read_b64_tr_b16 v[102:103], v5 offset:1824\nv_mfma_f32_16x16x32_f16 v[12:15], v[88:91], v[52:55], v[12:15]\ns_waitcnt lgkmcnt(10)\n" \
  "v_mfma_f32_16x16x32_f16 v[12:15], v[108:111], v[48:51], v[12:15]\nds_read_b64_tr_b16 v[72:73], v7 offset:0\n" \
  "ds_read_b64_tr_b16 v[74:75], v7 offset:64\nv_mfma_f32_16x16x32_f16 v[12:15], v[84:87], v[44:47], v[12:15]\n" \
  "ds_read_b64_tr_b16 v[124:125], v7 offset:512\nds_read_b64_tr_b16 v[126:127], v7 offset:576\n" \
  "v_mfma_f32_16x16x32_f16 v[16:19], v[84:87], v[52:55], v[16:19]\ns_waitcnt lgkmcnt(10)\n" \
  "v_mfma_f32_16x16x32_f16 v[12:15], v[104:107], v[40:43], v[12:15]\nv_mfma_f32_16x16x32_f16 v[16:19], v[104:107], v[48:51], v[16:19]\n" \
  "v_mfma_f32_16x16x32_f16 v[12:15], v[80:83], v[120:123], v[12:15]\nv_mfma_f32_16x16x32_f16 v[16:19], v[80:83], v[44:47], v[16:19]\n" \
  "s_waitcnt lgkmcnt(6)\nv_mfma_f32_16x16x32_f16 v[12:15], v[24:27], v[116:119], v[12:15]\n" \
  "v_mfma_f32_16x16x32_f16 v[16:19], v[24:27], v[40:43], v[16:19]\nv_mfma_f32_16x16x32_f16 v[16:19], v[76:79], v[120:123], v[16:19]\n" \
  "s_waitcnt lgkmcnt(2)\nv_mfma_f32_16x16x32_f16 v[16:19], v[100:103], v[116:119], v[16:19]\n" \
  "v_mfma_f32_16x16x32_f16 v[12:15], v[72:75], v[32:35], v[12:15]\nv_mfma_f32_16x16x32_f16 v[16:19], v[72:75], v[36:39], v[16:19]\n" \
  "s_waitcnt lgkmcnt(0)\nv_mfma_f32_16x16x32_f16 v[16:19], v[124:127], v[32:35], v[16:19]\ns_nop 4\nv_cvt_pkrtz_f16_f32 v20, v12, v13\n" \
  "v_cvt_pkrtz_f16_f32 v21, v14, v15\nv_pk_add_u16 v20, v20, 4 op_sel_hi:[1,0] clamp\nv_pk_add_u16 v21, v21, 4 op_sel_hi:[1,0] clamp\n" \
  "ds_write_b64 v4, v[20:21] offset:4096\nv_cvt_pkrtz_f16_f32 v20, v16, v17\nv_cvt_pkrtz_f16_f32 v21, v18, v19\n" \
  "v_pk_add_u16 v20, v20, 4 op_sel_hi:[1,0] clamp\nv_pk_add_u16 v21, v21, 4 op_sel_hi:[1,0] clamp\nds_write_b64 v4, v[20:21] offset:5120\n" \
  "s_setprio 0\ns_setpc_b64 s[90:91]\n61:\ns_movk_i32 s84, 0x4000\n63:\nds_read_b32 v23, v9 offset:32\ns_sub_u32 s84, s84, 1\ns_cbranch_scc1 62b\n" \
  "s_waitcnt lgkmcnt(0)\nv_readfirstlane_b32 s85, v23\ns_cmp_lt_u32 s85, 3\ns_cbranch_scc1 63b\ns_branch 62b\n5:\n" \
  "ds_read_b64 v[124:125], v3 offset:1088\nds_read_b64 v[126:127], v3 offset:1096\nds_read_b64 v[120:121], v3 offset:0\n" \
  "ds_read_b64 v[122:123], v3 offset:8\nds_read_b64 v[116:117], v3 offset:64\nds_read_b64 v[118:119], v3 offset:72\n" \
  "ds_read_b64 v[112:113], v3 offset:128\nds_read_b64 v[114:115], v3 offset:136\nds_read_b64 v[108:109], v3 offset:192\n" \
  "ds_read_b64 v[110:111], v3 offset:200\nds_read_b64 v[104:105], v3 offset:256\nds_read_b64 v[106:107], v3 offset:264\n" \
  "ds_read_b64 v[100:101], v3 offset:320\nds_read_b64 v[102:103], v3 offset:328\nds_read_b64 v[96:97], v3 offset:384\n" \
  "ds_read_b64 v[98:99], v3 offset:392\nds_read_b64 v[92:93], v3 offset:448\nds_read_b64 v[94:95], v3 offset:456\n" \
  "ds_read_b64 v[88:89], v3 offset:512\nds_read_b64 v[90:91], v3 offset:520\nds_read_b64 v[84:85], v3 offset:576\n" \
  "ds_read_b64 v[86:87], v3 offset:584\nds_read_b64 v[80:81], v3 offset:640\nds_read_b64 v[82:83], v3 offset:648\n" \
  "ds_read_b64 v[76:77], v3 offset:704\nds_read_b64 v[78:79], v3 offset:712\nds_read_b64 v[72:73], v3 offset:768\n" \
  "ds_read_b64 v[74:75], v3 offset:776\nds_read_b64 v[68:69], v3 offset:832\nds_read_b64 v[70:71], v3 offset:840\n" \
  "ds_read_b64 v[64:65], v3 offset:896\nds_read_b64 v[66:67], v3 offset:904\nds_read_b64 v[60:61], v3 offset:960\n" \
  "ds_read_b64 v[62:63], v3 offset:968\nds_read_b64 v[56:57], v3 offset:1024\nds_read_b64 v[58:59], v3 offset:1032\n" \
  "ds_read_b64 v[52:53], v6 offset:0\nds_read_b64 v[54:55], v6 offset:8\nds_read_b64 v[48:49], v6 offset:64\nds_read_b64 v[50:51], v6 offset:72\n" \
  "ds_read_b64 v[44:45], v6 offset:128\nds_read_b64 v[46:47], v6 offset:136\nds_read_b64 v[40:41], v6 offset:192\n" \
  "ds_read_b64 v[42:43], v6 offset:200\ns_waitcnt lgkmcnt(0)\ns_barrier\nds_read_b128 v[24:27], v2 offset:1472\n" \
  "ds_read_b128 v[28:31], v2 offset:1984\nds_read_b128 v[32:35], v2 offset:2496\nds_read_b128 v[36:39], v2 offset:2560\ns_waitcnt lgkmcnt(3)\n" \
  "v_mfma_f32_16x16x32_f16 v[12:15], v[124:127], v[24:27], 0\ns_setprio 1\nds_read_b128 v[24:27], v2 offset:2624\ns_waitcnt lgkmcnt(3)\n" \
  "v_mfma_f32_16x16x32_f16 v[16:19], v[124:127], v[28:31], 0\nds_read_b128 v[124:127], v2 offset:2688\ns_waitcnt lgkmcnt(3)\n" \
  "v_mfma_f32_16x16x32_f16 v[12:15], v[120:123], v[32:35], v[12:15]\nds_read_b128 v[28:31], v2 offset:2752\ns_waitcnt lgkmcnt(3)\n" \
  "v_mfma_f32_16x16x32_f16 v[12:15], v[116:119], v[36:39], v[12:15]\nds_read_b128 v[32:35], v2 offset:2816\ns_waitcnt lgkmcnt(3)\n" \
  "v_mfma_f32_16x16x32_f16 v[12:15], v[112:115], v[24:27], v[12:15]\nds_read_b128 v[36:39], v2 offset:2880\ns_waitcnt lgkmcnt(3)\n" \
  "v_mfma_f32_16x16x32_f16 v[12:15], v[108:111], v[124:127], v[12:15]\nds_read_b128 v[24:27], v2 offset:2944\ns_waitcnt lgkmcnt(3)\n" \
  "v_mfma_f32_16x16x32_f16 v[12:15], v[104:107], v[28:31], v[12:15]\nds_read_b128 v[124:127], v2 offset:3008\ns_waitcnt lgkmcnt(3)\n" \
  "v_mfma_f32_16x16x32_f16 v[12:15], v[100:103], v[32:35], v[12:15]\nds_read_b128 v[28:31], v2 offset:3072\ns_waitcnt lgkmcnt(3)\n" \
  "v_mfma_f32_16x16x32_f16 v[12:15], v[96:99], v[36:39], v[12:15]\nds_read_b128 v[32:35], v2 offset:3136\ns_waitcnt lgkmcnt(3)\n" \
  "v_mfma_f32_16x16x32_f16 v[12:15], v[92:95], v[24:27], v[12:15]\nds_read_b128 v[36:39], v2 offset:3200\ns_waitcnt lgkmcnt(3)\n" \
  "v_mfma_f32_16x16x32_f16 v[12:15], v[88:91], v[124:127], v[12:15]\nds_read_b128 v[24:27], v2 offset:3264\n" \
  "v_mfma_f32_16x16x32_f16 v[16:19], v[120:123], v[124:127], v[16:19]\nds_read_b64 v[120:121], v6 offset:256\nds_read_b64 v[122:123], v6 offset:264\n" \
  "s_waitcnt lgkmcnt(4)\nv_mfma_f32_16x16x32_f16 v[12:15], v[84:87], v[28:31], v[12:15]\nds_read_b128 v[124:127], v2 offset:3328\n" \
  "v_mfma_f32_16x16x32_f16 v[16:19], v[116:119], v[28:31], v[16:19]\nds_read_b64 v[116:117], v6 offset:320\nds_read_b64 v[118:119], v6 offset:328\n" \
  "v_mfma_f32_16x16x32_f16 v[12:15], v[80:83], v[32:35], v[12:15]\nds_read_b128 v[28:31], v2 offset:3392\n" \
  "v_mfma_f32_16x16x32_f16 v[16:19], v[112:115], v[32:35], v[16:19]\ns_waitcnt lgkmcnt(6)\n" \
  "v_mfma_f32_16x16x32_f16 v[12:15], v[76:79], v[36:39], v[12:15]\nds_read_b128 v[112:115], v2 offset:3456\n" \
  "v_mfma_f32_16x16x32_f16 v[16:19], v[108:111], v[36:39], v[16:19]\nds_read2_b32 v[32:33], v8 offset0:16 offset1:17\n" \
  "ds_read2_b32 v[34:35], v8 offset0:18 offset1:19\nv_mfma_f32_16x16x32_f16 v[12:15], v[72:75], v[24:27], v[12:15]\n" \
  "ds_read_b128 v[108:111], v2 offset:3520\nv_mfma_f32_16x16x32_f16 v[16:19], v[104:107], v[24:27], v[16:19]\n" \
  "ds_read2_b32 v[36:37], v8 offset0:0 offset1:1\nds_read2_b32 v[38:39], v8 offset0:2 offset1:3\ns_waitcnt lgkmcnt(6)\n" \
  "v_mfma_f32_16x16x32_f16 v[12:15], v[68:71], v[124:127], v[12:15]\nds_read_b128 v[104:107], v2 offset:3584\n" \
  "v_mfma_f32_16x16x32_f16 v[16:19], v[100:103], v[124:127], v[16:19]\nds_read_b128 v[24:27], v2 offset:3648\n" \
  "ds_read2_b32 v[100:101], v8 offset0:32 offset1:33\nds_read2_b32 v[102:103], v8 offset0:34 offset1:35\n" \
  "v_mfma_f32_16x16x32_f16 v[12:15], v[64:67], v[28:31], v[12:15]\nds_read_b128 v[124:127], v2 offset:3712\nds_read_b32 v23, v9 offset:32\n" \
  "v_mfma_f32_16x16x32_f16 v[16:19], v[96:99], v[28:31], v[16:19]\nds_read_b128 v[96:99], v2 offset:3776\ns_waitcnt lgkmcnt(9)\n" \
  "v_mfma_f32_16x16x32_f16 v[12:15], v[60:63], v[112:115], v[12:15]\nds_read_b128 v[28:31], v2 offset:3840\n" \
  "v_mfma_f32_16x16x32_f16 v[16:19], v[92:95], v[112:115], v[16:19]\nds_read_b128 v[92:95], v2 offset:3904\n" \
  "v_mfma_f32_16x16x32_f16 v[12:15], v[56:59], v[108:111], v[12:15]\nds_read_b128 v[112:115], v2 offset:3968\n" \
  "v_mfma_f32_16x16x32_f16 v[16:19], v[88:91], v[108:111], v[16:19]\nds_read_b128 v[88:91], v2 offset:4032\ns_waitcnt lgkmcnt(5)\n" \
  "v_readfirstlane_b32 s85, v23\ns_cmp_lt_u32 s85, 3\ns_cbranch_scc1 71f\n72:\nv_mfma_f32_16x16x32_f16 v[16:19], v[84:87], v[104:107], v[16:19]\n" \
  "ds_read_b64_tr_b16 v[108:109], v5 offset:1024\nds_read_b64_tr_b16 v[110:111], v5 offset:1056\n" \
  "v_mfma_f32_16x16x32_f16 v[16:19], v[80:83], v[24:27], v[16:19]\nds_read_b64_tr_b16 v[84:85], v5 offset:1280\n" \
  "ds_read_b64_tr_b16 v[86:87], v5 offset:1312\nv_mfma_f32_16x16x32_f16 v[16:19], v[76:79], v[124:127], v[16:19]\n" \
  "ds_read_b64_tr_b16 v[104:105], v5 offset:1536\nds_read_b64_tr_b16 v[106:107], v5 offset:1568\ns_waitcnt lgkmcnt(9)\n" \
  "v_mfma_f32_16x16x32_f16 v[16:19], v[72:75], v[96:99], v[16:19]\nds_read_b64_tr_b16 v[80:81], v5 offset:1792\n" \
  "ds_read_b64_tr_b16 v[82:83], v5 offset:1824\nv_mfma_f32_16x16x32_f16 v[16:19], v[68:71], v[28:31], v[16:19]\n" \
  "ds_read_b64_tr_b16 v[24:25], v5 offset:2048\nds_read_b64_tr_b16 v[26:27], v5 offset:2080\ns_waitcnt lgkmcnt(11)\n" \
  "v_mfma_f32_16x16x32_f16 v[16:19], v[64:67], v[92:95], v[16:19]\nds_read_b64_tr_b16 v[76:77], v5 offset:2304\n" \
  "ds_read_b64_tr_b16 v[78:79], v5 offset:2336\nv_mfma_f32_16x16x32_f16 v[16:19], v[60:63], v[112:115], v[16:19]\n" \
  "ds_read_b64_tr_b16 v[124:125], v5 offset:2560\nds_read_b64_tr_b16 v[126:127], v5 offset:2592\ns_waitcnt lgkmcnt(12)\n" \
  "v_mfma_f32_16x16x32_f16 v[16:19], v[56:59], v[88:91], v[16:19]\nds_read_b64_tr_b16 v[72:73], v5 offset:2816\n" \
  "ds_read_b64_tr_b16 v[74:75], v5 offset:2848\nv_mfma_f32_16x16x32_f16 v[12:15], v[108:111], v[52:55], v[12:15]\ns_waitcnt lgkmcnt(10)\n" \
  "v_mfma_f32_16x16x32_f16 v[12:15], v[84:87], v[48:51], v[12:15]\nds_read_b64_tr_b16 v[96:97], v7 offset:0\n" \
  "ds_read_b64_tr_b16 v[98:99], v7 offset:64\nv_mfma_f32_16x16x32_f16 v[12:15], v[104:107], v[44:47], v[12:15]\n" \
  "ds_read_b64_tr_b16 v[68:69], v7 offset:512\nds_read_b64_tr_b16 v[70:71], v7 offset:576\n" \
  "v_mfma_f32_16x16x32_f16 v[16:19], v[104:107], v[52:55], v[16:19]\ns_waitcnt lgkmcnt(10)\n" \
  "v_mfma_f32_16x16x32_f16 v[12:15], v[80:83], v[40:43], v[12:15]\nds_read_b64_tr_b16 v[28:29], v7 offset:1024\n" \
  "ds_read_b64_tr_b16 v[30:31], v7 offset:1088\nv_mfma_f32_16x16x32_f16 v[16:19], v[80:83], v[48:51], v[16:19]\n" \
  "ds_read2_b32 v[64:65], v8 offset0:48 offset1:49\nds_read2_b32 v[66:67], v8 offset0:50 offset1:51\n" \
  "v_mfma_f32_16x16x32_f16 v[12:15], v[24:27], v[120:123], v[12:15]\nv_mfma_f32_16x16x32_f16 v[16:19], v[24:27], v[44:47], v[16:19]\n" \
  "s_waitcnt lgkmcnt(10)\nv_mfma_f32_16x16x32_f16 v[12:15], v[76:79], v[116:119], v[12:15]\nds_read_b64_tr_b16 v[92:93], v7 offset:1536\n" \
  "ds_read_b64_tr_b16 v[94:95], v7 offset:1600\nv_mfma_f32_16x16x32_f16 v[16:19], v[76:79], v[40:43], v[16:19]\n" \
  "v_mfma_f32_16x16x32_f16 v[16:19], v[124:127], v[120:123], v[16:19]\ns_waitcnt lgkmcnt(8)\n" \
  "v_mfma_f32_16x16x32_f16 v[16:19], v[72:75], v[116:119], v[16:19]\nv_mfma_f32_16x16x32_f16 v[12:15], v[96:99], v[32:35], v[12:15]\n" \
  "v_mfma_f32_16x16x32_f16 v[16:19], v[96:99], v[36:39], v[16:19]\ns_waitcnt lgkmcnt(4)\n" \
  "v_mfma_f32_16x16x32_f16 v[12:15], v[68:71], v[100:103], v[12:15]\nv_mfma_f32_16x16x32_f16 v[16:19], v[68:71], v[32:35], v[16:19]\n" \
  "s_waitcnt lgkmcnt(2)\nv_mfma_f32_16x16x32_f16 v[12:15], v[28:31], v[64:67], v[12:15]\n" \
  "v_mfma_f32_16x16x32_f16 v[16:19], v[28:31], v[100:103], v[16:19]\ns_waitcnt lgkmcnt(0)\n" \
  "v_mfma_f32_16x16x32_f16 v[16:19], v[92:95], v[64:67], v[16:19]\ns_nop 4\nv_cvt_pkrtz_f16_f32 v20, v12, v13\nv_cvt_pkrtz_f16_f32 v21, v14, v15\n" \
  "v_pk_add_u16 v20, v20, 4 op_sel_hi:[1,0] clamp\nv_pk_add_u16 v21, v21, 4 op_sel_hi:[1,0] clamp\nds_write_b64 v4, v[20:21] offset:6144\n" \
  "v_cvt_pkrtz_f16_f32 v20, v16, v17\nv_cvt_pkrtz_f16_f32 v21, v18, v19\nv_pk_add_u16 v20, v20, 4 op_sel_hi:[1,0] clamp\n" \
  "v_pk_add_u16 v21, v21, 4 op_sel_hi:[1,0] clamp\nds_write_b64 v4, v[20:21] offset:7168\ns_setprio 0\ns_setpc_b64 s[90:91]\n71:\n" \
  "s_movk_i32 s84, 0x4000\n73:\nds_read_b32 v23, v9 offset:32\ns_sub_u32 s84, s84, 1\ns_cbranch_scc1 72b\ns_waitcnt lgkmcnt(0)\n" \
  "v_readfirstlane_b32 s85, v23\ns_cmp_lt_u32 s85, 3\ns_cbranch_scc1 73b\ns_branch 72b\n"
#define CONV_CLOB "memory", "scc", "s84", "s85", "s90", "s91", "v12", "v13", "v14", "v15", "v16", "v17", "v18", "v19", "v20", "v21", "v22", "v23", "v24", "v25", "v26", "v27", "v28", "v29", "v30", "v31", "v32", "v33", "v34", "v35", "v36", "v37", "v38", "v39", "v40", "v41", "v42", "v43", "v44", "v45", "v46", "v47", "v48", "v49", "v50", "v51", "v52", "v53", "v54", "v55", "v56", "v57", "v58", "v59", "v60", "v61", "v62", "v63", "v64", "v65", "v66", "v67", "v68", "v69", "v70", "v71", "v72", "v73", "v74", "v75", "v76", "v77", "v78", "v79", "v80", "v81", "v82", "v83", "v84", "v85", "v86", "v87", "v88", "v89", "v90", "v91", "v92", "v93", "v94", "v95", "v96", "v97", "v98", "v99", "v100", "v101", "v102", "v103", "v104", "v105", "v106", "v107", "v108", "v109", "v110", "v111", "v112", "v113", "v114", "v115", "v116", "v117", "v118", "v119", "v120", "v121", "v122", "v123", "v124", "v125", "v126", "v127"
constexpr int TDP = 1216, T_3 = 4 * TDP, T_4 = T_3 + 512, TB = T_4 + 320;
constexpr int C_U = 480, UB = C_U + 4096 + 96, C_G5 = 0, C_G3 = 3200, C_G9 = 6400, C_G7 = C_G9 + 2096, GB = C_G7 + 2080 + 112;
constexpr int O_FL = 2 * TB, O_UL = O_FL + 64, O_IM = O_UL + 2 * UB, O_ST = O_IM + 2 * GB;
constexpr int X_TB = 10240, X_UL = X_TB + TB, X_IM = X_UL + UB, X_ST = X_IM + GB;
constexpr int X_WR = X_IM + 128 * IP;
#define X_RW(w) (LW + 9216 * (2 + ((w) >> 1)) + 2560 * ((w) & 1))
constexpr int RP = 1216;
constexpr int LW = 36864 + 512, LA = LW, LM = LW + 36864;
constexpr int L_SC = LM + 2048, SMEM = O_IM + 256 * IP;
static_assert(L_SC + 1152 <= SMEM && O_ST + 8192 <= SMEM && X_IM + 128 * IP <= LM && X_ST + 8192 <= LM && UB % 128 == 64 && GB % 128 == 64 && SMEM <= 79872, "");
struct TOps { uint32_t xa, xb, ya, pd, pf, pg, pe; };
__device__ __forceinline__ void table_ops(uint32_t im, uint32_t tb, int ct, int lane, TOps& t) {
  const int j = lane & 15, kq = lane >> 4, q4 = (lane >> 2) & 3, p4 = lane & 3, jj = j + 16 * ct;
  const uint32_t sa = im + (uint32_t)(IP * (lane >> 2));
  t.xa = sa + (uint32_t)(I_X + 8 * p4); t.xb = sa + (uint32_t)(I_X + 32 + 8 * (p4 & 1)); t.ya = sa + (uint32_t)(32 * ct + 8 * p4);
  t.pd = tb + (uint32_t)(2 * (541 - (8 * jj + 4 * (kq & 1) + 256 * (kq >> 1))));
  t.pf = tb + (uint32_t)((kq & 1) ? T_4 + 2 * (63 - jj) : T_3 + 2 * (127 - 2 * jj));
  t.pg = t.pf - ((kq & 1) ? 64 : 2);
  t.pe = tb + (uint32_t)(p4 * (TDP + 2) + ((lane < 12) ? 2 * (32 - q4) : 2 * (572 + (q4 & 1))));
  asm volatile("" : "+v"(t.xa), "+v"(t.xb), "+v"(t.ya), "+v"(t.pd), "+v"(t.pf), "+v"(t.pg), "+v"(t.pe));
}
typedef __attribute__((address_space(3))) char* lptr;
#define LP(T, a) ((__attribute__((address_space(3))) T*)(uintptr_t)(a))
__device__ __forceinline__ uint32_t table_tile(const TOps& t, int ct, int lane, uint32_t evp, float dsk TSP) {
  u32x4 ev;
  f32x4 ad[2], af[2];
  asm volatile(T_ASM : "=&v"(ad[0]), "=&v"(ad[1]), "=&v"(af[0]), "=&v"(af[1]), "=&v"(ev) : "v"(t.xa), "v"(t.xb), "v"(t.ya), "v"(evp) : T_CLOB);
  TS(0, 4);
  const int e0 = (int)(ev[0] + ev[2]), e1 = (int)(ev[1] + ev[3]);
  const int em = min(max(max(max(e0, e1), (int)((__float_as_uint(dsk) >> 23) & 0xffu) - 133), -110), 115);
  const int ts = ((uint32_t)(em + 12) <= 8u) ? 1 : -3 - em;
  float s0 = __builtin_amdgcn_ldexpf(1.f, e0 - 29 + ts), s1 = __builtin_amdgcn_ldexpf(1.f, e1 - 29 + ts);
  float d0 = (ct == 0 && lane == 0) ? __builtin_amdgcn_ldexpf(dsk, ts) : 0.f;
  asm volatile("s_nop 1" : "+v"(ad[0]), "+v"(ad[1]), "+v"(af[0]), "+v"(af[1]), "+v"(s0), "+v"(s1), "+v"(d0));
  const f32x4 k = ad[0] * s0 + (f32x4){d0, 0.f, 0.f, 0.f} + ad[1] * s1, f = af[0] * s0 + af[1] * s1;
  auto pk = [](float a, float b) { uint32_t r; asm("v_cvt_pk_f16_f32 %0, %1, %2" : "=v"(r) : "v"(a), "v"(b)); return r; };
  asm volatile("s_nop 0" : : "v"(k), "v"(f));
  const uint32_t p32 = pk(k[3], k[2]), p10 = pk(k[1], k[0]), p21 = pk(k[2], k[1]), p01 = pk(f[0], f[1]), p23 = pk(f[2], f[3]);
  *LP(u32x2, t.pd + 3 * TDP + 6) = (u32x2){p32, p10};
  *LP(uint32_t, t.pd + TDP + 2) = p32; *LP(uint32_t, t.pd + TDP + 6) = p10;
  *LP(uint32_t, t.pd + 2) = p21; *LP(uint32_t, t.pd + 2 * TDP + 6) = p21;
  *LP(u16, t.pd) = (u16)p32; *LP(u16, t.pd + 2 * TDP + 4) = (u16)p32;
  asm volatile("ds_write_b16_d16_hi %0, %1 offset:6\n\tds_write_b16_d16_hi %0, %1 offset:%2" : : "v"(t.pd), "v"(p10), "n"(2 * TDP + 10) : "memory");
  if (ct) {
    const uint32_t va = (uint32_t)__builtin_amdgcn_readlane((int)p32, 63), vb = (uint32_t)__builtin_amdgcn_readlane((int)p23, 15) >> 16;
    *LP(u16, t.pe) = (u16)((lane < 12) ? va : vb);
  }
  if (lane < 32) {
    *LP(u16, t.pf + 128) = (u16)p01; *LP(u16, t.pg + 128) = (u16)(p01 >> 16);
    *LP(u16, t.pf) = (u16)p23; *LP(u16, t.pg) = (u16)(p23 >> 16);
  }
  return (uint32_t)(127 + 5 - ts) << 23;
}
__device__ __forceinline__ uint32_t hadd(uint32_t a, uint32_t b) { return __builtin_bit_cast(uint32_t, __builtin_bit_cast(f16x2, a) + __builtin_bit_cast(f16x2, b)); }
template <bool FULL> __device__ __forceinline__ void sums(char* sm, const u32x4 p, const u32x4 c) {
  const uint32_t D[8] = {p[0], p[1], p[2], p[3], c[0], c[1], c[2], c[3]};
  uint32_t O[7];
#pragma unroll
  for (int j = 0; j < 7; ++j) O[j] = __builtin_amdgcn_alignbit(D[j + 1], D[j], 16);
  u32x4 a, b;
#pragma unroll
  for (int j = 0; j < 4; ++j) a[j] = hadd(D[j + 2], hadd(O[j + 1], O[j + 2]));
  *(u32x4*)(sm + C_G3 + 32) = a;
#pragma unroll
  for (int j = 0; j < 4; ++j) b[j] = hadd(a[j], hadd(D[j + 1], D[j + 3]));
  *(u32x4*)(sm + C_G5 + 32) = b;
  if (FULL) {
#pragma unroll
    for (int j = 0; j < 4; ++j) a[j] = hadd(b[j], hadd(O[j], O[j + 3]));
    *(u32x4*)(sm + C_G7 + 32) = a;
#pragma unroll
    for (int j = 0; j < 4; ++j) b[j] = hadd(a[j], hadd(D[j], D[j + 4]));
    *(u32x4*)(sm + C_G9 + 32) = b;
  }
}
__device__ __forceinline__ void count_up(uint32_t a) {
  uint32_t t;
  asm volatile("s_mov_b64 exec, 1\n\tv_mov_b32 %0, 1\n\tds_add_u32 %1, %0\n\ts_mov_b64 exec, -1" : "=&v"(t) : "v"(a) : "memory");
}
__device__ __forceinline__ int pad_at(int r) { return 16 * (r & 1) + 3200 * (r >> 1) - ((r >= 6) ? 3200 - 2096 : 0); }
struct COps { uint32_t b, a, o, g3, k3, g4, k4, f; };
__device__ __forceinline__ void conv_ops(uint32_t tb, uint32_t ub, uint32_t cb, int lane, COps& e) {
  const uint32_t j = lane & 15, kq = lane >> 4, q4 = (lane >> 2) & 3, p4 = lane & 3;
  e.b = ub + 32u * j + 16u * kq;
  e.a = tb + p4 * TDP + 32u - 8u * q4 + 16u * kq;
  e.g3 = cb + C_G5 + 8 + 64u * kq + 8u * (q4 + p4) + (q4 & 1u) * 3200u;
  e.g4 = cb + C_G9 + 8 + 128u * kq + 16u * q4 + 8u * p4 + (q4 & 1u) * 2096u;
  e.k3 = tb + T_3 + 120 - 8u * j + 16u * kq;
  e.k4 = tb + T_4 + 60 - 4u * j + 16u * kq;
  asm volatile("" : "+v"(e.b), "+v"(e.a), "+v"(e.g3), "+v"(e.g4), "+v"(e.k3), "+v"(e.k4));
}
#define CONV_RUN(prog, e, ...) do { int prog_ = (prog); \
    asm volatile("s_call_b64 s[90:91], kf_conv" : "+{s89}"(prog_), "+{v2}"(e.b), "+{v3}"(e.a), "+{v4}"(e.o), "+{v5}"(e.g3), "+{v6}"(e.k3), "+{v7}"(e.g4), \
                 "+{v8}"(e.k4), "+{v9}"(e.f) __VA_ARGS__ : : CONV_CLOB); } while (0)

template <bool E>
__device__ __forceinline__ void ln_stats(const uint32_t* v, int lane, float sc, f32x2* t, f32x4 z, f32x2& rc, f32x2& nc, float& e) {
  float s1a = z[0], s2a = z[1], s1b = z[2], s2b = z[3];
#pragma unroll
  for (int k = 0; k < 4; ++k) {
    s1a = dot2(v[k], 0x3f803f80u, s1a); s2a = dot2(v[k], v[k], s2a);
    s1b = dot2(v[4 + k], 0x3f803f80u, s1b); s2b = dot2(v[4 + k], v[4 + k], s2b);
  }
#pragma unroll
  for (int k = 0; k < 9; ++k) t[k] = (f32x2){bflo(v[k]), bfhi(v[k])};
  const float rs = row_sum(fold16(dot2(v[8], 0x3f803f80u, fold32(s1a, s1b)), dot2(v[8], v[8], fold32(s2a, s2b))));
  if (E) { e = row_sum(e); e += __int_as_float(__builtin_amdgcn_update_dpp(0, __float_as_int(e), 0x142, 0xa, 0xf, false)); }
  const f32x2 mean = (f32x2){rdl(rs, 15), rdl(rs, 47)} * (1.f / D_), m2 = mean * mean;
  f32x2 var = (f32x2){rdl(rs, 31), rdl(rs, 63)} * (1.f / D_) - m2;
  asm volatile("" : "+v"(t[0]), "+v"(t[1]), "+v"(t[2]), "+v"(t[3]), "+v"(t[4]), "+v"(t[5]), "+v"(t[6]), "+v"(t[7]), "+v"(t[8]));
  if (__builtin_expect(!(m2[0] <= 16.f * var[0] && m2[1] <= 16.f * var[1]), 0)) {
    float q[3] = {0.f, 0.f, 0.f};
#pragma unroll
    for (int k = 0; k < 9; ++k) {
      const f32x2 a = t[k] - ((k < 4 || (k == 8 && lane < 32)) ? mean[0] : mean[1]);
      q[k >> 2] += a[0] * a[0] + a[1] * a[1];
    }
    float qa = q[0] + ((lane < 32) ? q[2] : 0.f), qb = q[1] + ((lane < 32) ? 0.f : q[2]);
    wave_sum2(qa, qb);
    var = (f32x2){qa, qb} * (1.f / D_);
  }
  const uint32_t mp = pack2(mean[0], mean[1]), vp = pack2(var[0], var[1]);
  const f32x2 ve = (f32x2){bflo(vp), bfhi(vp)} + 1e-5f;
  const uint32_t e2 = pack2(ve[0], ve[1]);
  const uint32_t sp = pack2(__builtin_amdgcn_sqrtf(bflo(e2)), __builtin_amdgcn_sqrtf(bfhi(e2)));
  rc = (f32x2){__builtin_amdgcn_rcpf(bflo(sp)), __builtin_amdgcn_rcpf(bfhi(sp))} * sc;
  nc = -(f32x2){bflo(mp), bfhi(mp)} * rc;
}
template <bool H>
__device__ __forceinline__ void ln_apply(const f32x2* t, int lane, f32x2 rc, f32x2 nc, uint32_t* c) {
  const float nm[3] = {nc[0], nc[1], (lane < 32) ? nc[0] : nc[1]}, rr[3] = {rc[0], rc[1], (lane < 32) ? rc[0] : rc[1]};
  f32x2 dd[9];
#pragma unroll
  for (int k = 0; k < 9; ++k) dd[k] = __builtin_elementwise_fma(t[k], (f32x2){rr[k >> 2], rr[k >> 2]}, (f32x2){nm[k >> 2], nm[k >> 2]});
  asm volatile("" : "+v"(dd[0]), "+v"(dd[1]), "+v"(dd[2]), "+v"(dd[3]), "+v"(dd[4]), "+v"(dd[5]), "+v"(dd[6]), "+v"(dd[7]), "+v"(dd[8]));
#pragma unroll
  for (int k = 0; k < 9; ++k) {
    const f32x2 d = dd[k];
    typedef __attribute__((ext_vector_type(2))) uint16_t u16x2;
    if (H) c[k] = __builtin_bit_cast(uint32_t, __builtin_elementwise_add_sat(__builtin_bit_cast(u16x2, __builtin_amdgcn_cvt_pkrtz(d[0], d[1])), (u16x2){4, 4}));
    else c[k] = pack2(d[0], d[1]);
  }
}
__device__ __forceinline__ uint32_t med3u(uint32_t a, uint32_t b, uint32_t c) { return max(min(a, b), min(max(a, b), c)); }
__device__ __forceinline__ uint32_t oct_maxu(uint32_t v) {
  v = max(v, (uint32_t)DPP_I((int)v, 0xb1, 0xf));
  v = max(v, (uint32_t)DPP_I((int)v, 0x4e, 0xf));
  v = max(v, (uint32_t)DPP_I((int)v, 0x141, 0xf));
  return v;
}

#define KARGS const u16* __restrict__ x, const u16* __restrict__ Are, const u16* __restrict__ Aim, const u16* __restrict__ Bre, \
    const u16* __restrict__ Bim, const u16* __restrict__ Cre, const u16* __restrict__ Cim, const u16* __restrict__ logdt, \
    const u16* __restrict__ Dskip, char* __restrict__ ws, const u16* __restrict__ wr, const u16* __restrict__ wg, \
    const u16* __restrict__ wu, const u16* __restrict__ wd, u16* __restrict__ out
#define KPASS x, Are, Aim, Bre, Bim, Cre, Cim, logdt, Dskip, ws, wr, wg, wu, wd, out
template <int ROLE, int FFN = -1>
__device__ __forceinline__ void role_body(char* smem, KARGS) {
  const int tid = threadIdx.x, lane = tid & 63;
  const int w = __builtin_amdgcn_readfirstlane(tid >> 6), wl = w & 3;
  const int b = blockIdx.x & 255, bx = b & 7, bj = b >> 3;
  constexpr bool isl = ROLE == 1 || ROLE == 2;
  const int tg = isl ? 32 * (bj >> 2) + 4 * bx + (bj & 3) : 32 * bx + bj;
  u32x4 xa, xc;
  uint32_t ep, r0;
#define P_WAIT(n, ...) asm volatile("s_waitcnt vmcnt(" #n ")\n\tv_lshlrev_b32 %0, 16, v100\n\tv_lshlrev_b32 %1, 16, v101\n\tv_lshlrev_b32 %2, 16, v102" \
    : "=v"(ldt), "=v"(are), "=v"(aim) : : "v100", "v101", "v102" __VA_ARGS__)
#define P_MORE() asm volatile("v_lshlrev_b32 %0, 16, v103\n\tv_lshlrev_b32 %1, 16, v104\n\tv_lshlrev_b32 %2, 16, v105\n\tv_lshlrev_b32 %3, 16, v106" \
    : "=v"(bre), "=v"(bim), "=v"(cre), "=v"(cim) : : "v103", "v104", "v105", "v106")
  TSD;
  TS(0, 0);
  const uint32_t lb0 = (uint32_t)(uintptr_t)(__attribute__((address_space(3))) char*)smem;
  const int i = lane & 15, kq = lane >> 4;
  const u32x4 ones = {0xffffffffu, 0xffffffffu, 0xffffffffu, 0xffffffffu}, zero4 = {0u, 0u, 0u, 0u};
  auto u_again = [&](u32x4& uw, const u32x4& ut, uint32_t uo) {
#pragma unroll 1
    for (int spin = 0; spin < SPIN_CAP; ++spin) {
      const bool na = ((uw[0] | uw[1] | uw[2] | uw[3]) & UMASK) != 0u;
      if (__builtin_expect(!__any(na), 1)) break;
      TSC(19);
      SLEEP(USLEEP);
      if (na) { LD16W(uw, uo); uw ^= ut; }
    }
  };
  constexpr uint32_t S16 = 0x41800000u, LMASK = 0x00070007u;
  auto v_put = [&](u32x4 v, const u32x4& ut, bool other, uint32_t vo) {
    const bool second = __any(other || ((v[0] | v[1] | v[2] | v[3]) & 0x40004000u) != 0u);
    if (__builtin_expect(second, 0)) v = (v & ~LMASK) | (~ut & LMASK); else v = (v & ~LMASK) | ut;
    ST16(vo, v);
    return second;
  };
  auto v_end = [&](uint32_t T, int n) { DRAIN(); if (lane == 0) ST4(O_DN + 16u * n, T); };
  if constexpr (!isl) {
    const int ch = wl >> 1, tl = tid & 255;
    int* evp = (int*)(smem + O_FL + 16 * ch);
    float ldt, are, aim, dsk;
    if constexpr (ROLE == 0) {
      __builtin_amdgcn_s_setprio(KPRIO);
      COps e;
      conv_ops(lb0 + (uint32_t)(ch * TB), lb0 + (uint32_t)(O_UL + ch * UB), lb0 + (uint32_t)(O_IM + ch * GB), lane, e);
      e.o = lb0 + O_ST + (uint32_t)(16 * ((4 * i + 2 * (kq >> 1) + ch) ^ (i >> 1)) + 8 * (kq & 1));
      e.f = lb0 + O_FL;
      TOps t;
      table_ops(lb0 + (uint32_t)(O_IM + 128 * IP * ch), lb0 + (uint32_t)(TB * ch), wl & 1, lane, t);
      uint32_t pz = lb0 + O_IM + (uint32_t)(((lane >> 3) & 1) * GB + pad_at(lane & 7)), im = lb0 + (uint32_t)(O_IM + IP * tl);
      asm volatile("" : "+v"(e.o), "+v"(e.f), "+v"(pz), "+v"(im));
      float bre, bim, cre, cim;
      P_WAIT(0, , "v103", "v104", "v105", "v106", "v107", "v109");
      asm volatile("" : "={v109}"(ep));
      const uint32_t T = ((uint32_t)__builtin_amdgcn_readfirstlane(ep) + 1u) * 0x9E3779B1u;
      if (w == 3 && lane < 8) ST16(O_KEY + 128u * b + 16u * lane, ones);
      P_MORE();
      asm volatile("v_lshlrev_b32 %0, 16, v107" : "=v"(dsk) : : "v107");
      TS(0, 1);
      evp[wl & 1] = gen_y(ldt, are, aim, bre, bim, cre, cim, im, [] {} TSA);
      TS(0, 8);
      BAR();
      TS(0, 2);
      const uint32_t cs = table_tile(t, wl & 1, lane, lb0 + O_FL + 16 * ch, dsk TSA);
      *(uint32_t*)(smem + O_FL + 40 + 4 * ch) = cs;
      DRAIN();
      if ((wl & 1) && lane == 0) asm volatile("global_store_dwordx2 %0, %1, %2 sc1" : : "v"(O_SC + 16u * (2 * tg + ch)), "v"((u32x2){T, cs}), "s"(ws) : "memory");
      TS(0, 9);
      gsync0(lb0 + O_FL + 36, 4);
      TS(0, 3);
      *(u32x4*)(smem + O_ST + 16 * tl) = ones;
      *(u32x4*)(smem + O_ST + 4096 + 16 * tl) = ones;
      if (w == 0) *LP(u32x4, pz) = zero4;
      CONV_RUN(wl & 1, e);
      TS(0, 5);
    } else {
      __builtin_amdgcn_s_setprio(UPRIO);
#pragma unroll
      for (int k = 0; k < 3; ++k) *(u32x4*)(smem + ((16 * (tl + 256 * k) < O_FL) ? 16 * (tl + 256 * k) : O_FL + 32)) = zero4;
      if (w == 7) *(u32x4*)(smem + O_UL + (lane >> 5) * UB + 16 * min(lane & 31, 29)) = zero4;
      uint32_t uo = u_at(tl >> 1, 2 * tg + (tid & 1)), go = O_SU + (uint32_t)(256 * bx + 32 * wl + (lane & 31) + 4 * (lane & 32)) * 128u;
      uint32_t ul = (uint32_t)(O_UL + (tid & 1) * UB + C_U - 16 + 16 * (tl >> 1)), ug = (uint32_t)(O_IM + (tid & 1) * GB + 16 * (tl >> 1)), so = lb0 + O_ST + 16u * (uint32_t)(lane ^ (lane >> 3));
      uint32_t vo = O_V + (uint32_t)tg * VP + 16u * lane;
      asm volatile("" : "+v"(uo), "+v"(go), "+v"(ul), "+v"(ug), "+v"(so), "+v"(vo));
      P_WAIT(1, , "v109");
      TS(0, 1);
      evp[2 + (wl & 1)] = gen_x(ldt, are, aim, smem + O_IM + IP * tl);
      asm volatile("s_waitcnt vmcnt(0)" : "={v109}"(ep));
      const uint32_t T = ((uint32_t)__builtin_amdgcn_readfirstlane(ep) + 1u) * 0x9E3779B1u;
      TS(0, 8);
      BAR();
      TS(0, 2);
      u32x4 ut = {utag(T, 0), utag(T, 1), utag(T, 2), utag(T, 3)};
      asm volatile("" : "+v"(ut));
      SLEEP(GSLEEP);
      u32x2 pv;
      pv[1] = gate(ws, go, T);
      TS(0, 3);
      until(latest(pv[1]) + UD);
      TS(0, 7);
      u32x4 uw, ux;
      asm volatile("global_load_dwordx4 %0, %2, %4 sc1\n\tglobal_load_dwordx4 %1, %3, %4 sc1"
                   : "=&v"(uw), "=&v"(ux) : "v"(uo), "v"(uo + 64 * UP * 32), "s"(ws) : "memory");
      __builtin_amdgcn_s_setprio(FPR);
      asm volatile("s_waitcnt vmcnt(1)" : "+v"(uw));
      uw ^= ut;
      *(u32x4*)(smem + ul + 16) = uw;
      asm volatile("s_waitcnt vmcnt(0)" : "+v"(ux));
      TS(0, 11);
      ux ^= ut;
      *(u32x4*)(smem + ul + 2048 + 16) = ux;
      if (__builtin_expect(__any(((uw[0] | uw[1] | uw[2] | uw[3] | ux[0] | ux[1] | ux[2] | ux[3]) & UMASK) != 0u), 0)) {
        u_again(uw, ut, uo);
        u_again(ux, ut, uo + 64 * UP * 32);
        *(u32x4*)(smem + ul + 16) = uw;
        *(u32x4*)(smem + ul + 2048 + 16) = ux;
      }
      BAR();
      TS(0, 4);
      const u32x4 pw = *(const u32x4*)(smem + ul);
      if (wl < 2) {
        const u32x4 px = *(const u32x4*)(smem + ul + 2048);
        sums<true>(smem + ug, pw, uw);
        sums<false>(smem + ug + 2048, px, ux);
      } else sums<true>(smem + ug, pw, uw);
      count_up(lb0 + O_FL + 32);
      __builtin_amdgcn_s_setprio(SPR);
      TS(0, 5);
      const bool other = *(const uint32_t*)(smem + O_FL + 40 + 4 * (lane & 1)) != S16;
      bool second = false;
#pragma unroll
      for (int k = 0; k < 2; ++k) {
        const uint32_t o = ((k ? 0x3247u : 0x1065u) >> (4 * wl) & 7u) * 1024u;
        u32x4 v;
#pragma unroll 1
        for (int spin = 0; spin < SPIN_CAP; ++spin) {
          asm volatile("ds_read_b128 %0, %1\n\ts_waitcnt lgkmcnt(0)" : "=&v"(v) : "v"(so + o) : "memory");
          if (!__any(max4(v) == 0xffffffffu)) break;
          __builtin_amdgcn_s_sleep(VPS);
        }
        TS(0, 12 + k);
        second |= v_put(v, ut, other, vo + o);
        TS(0, 10 - 4 * k);
      }
      if (__builtin_expect(second, 0)) v_end(T, 4 * tg + wl);
    }
    TSE();
  } else {
  uint32_t* cand = (uint32_t*)(smem + LM);
  uint32_t* sel = cand + 128;
  uint32_t* kl = sel + 48;
  uint32_t* cnt = sel + 64;
  const uint32_t ca = lb0 + LM + 4 * (128 + 64);
  u16* hm = (u16*)(cnt + 12);
  uint32_t T;
  const bool ffn = (FFN < 0) ? bj < 18 : FFN, isx = FFN != 1 && bj >= 24;
  const int fb = 18 * bx + bj;
  constexpr bool ln2 = ROLE == 2;
  const int g0 = tg, g2 = tg;
  u32x4 bw[18];
#pragma unroll
  for (int s = 0; s < 9; ++s) bw[s] = (u32x4){0u, 0u, 0u, 0u};
  int tid2 = threadIdx.x;
  uint32_t ts1 = 0u;
  auto rest = [&] {
    if (ts1 && (int)((uint32_t)__builtin_amdgcn_s_memrealtime() - ts1) > 0) { TS(0, 8); until(ts1 + TSTOP + RD); if (RW) gwait(ca, 4); ts1 = 0u; }
  };
  auto gu_dma = [&](int q0, int q1, auto depth) {
    const char* gs = (const char*)((w < 2) ? wg : wu) + (size_t)((144 * (w & 1)) * (F_ * 2) + fb * 64);
    const uint32_t gv = (uint32_t)((lane >> 2) * (F_ * 2) + 16 * ((lane & 3) ^ (lane >> 5) ^ (w & 2)));
#pragma unroll 1
    for (int q = q0; q < q1; ++q) {
      const int k = (int)((((bj & 1) ? ORD1 : ((unsigned __int128)0x876543210u << 36 | 0x876543210ull)) >> (4 * (q - (q >= 9 ? 9 : 0)) + (q >= 9 ? 36 : 0))) & 15u), h = q >= 9;
      if (q >= RQ) rest();
      dma16(gs + (size_t)(288 * h + 16 * k) * (F_ * 2), gv, lb0 + LW * h + 9216 * w + 1024 * k);
      asm volatile("s_waitcnt vmcnt(%0)" : : "n"(depth.value) : "memory");
    }
  };
  auto down_dma = [&](int k0, int k1, int lane) {
#pragma unroll 1
    for (int k = k0; k < k1; ++k) {
      const int o = 9216 * wl + 2304 * k;
      const char* ds = (const char*)wd + (size_t)(fb * 36864 + o);
      const uint32_t dl = lb0 + o + 32 * ((4 * wl + k + 1) >> 1);
      if (ROLE == 1) rest();
      dma16d(ds, 16u * lane, dl);
      dma16d(ds + 1024, 16u * lane, dl + 1024);
      uint32_t keep;
      asm volatile("s_mov_b32 %0, m0\n\ts_mov_b32 m0, %3\n\ts_mov_b64 exec, 0xffff\n\tglobal_load_lds_dwordx4 %1, %2" DPOL2 "\n\ts_mov_b64 exec, -1\n\ts_mov_b32 m0, %0\n\ts_waitcnt vmcnt(%4)"
                   : "=&s"(keep) : "v"(16u * lane), "s"(ds + 2048), "s"(dl + 2048), "n"(ROLE == 1 ? DDEPTH : 40) : "memory");
    }
  };
  auto gu_read = [&](int h) {
    const uint32_t ra = lb0 + LW * h + (uint32_t)(((lane >> 1) & 1) * 18432 + (8 * kq + ((lane >> 2) & 3)) * 64 + ((w ^ (kq & 1) ^ (lane & 2)) * 16) + 8 * (lane & 1));
#pragma unroll
    for (int s = 0; s < 9; ++s) {
      const u32x2 h0 = trd(ra + 2048 * s), h1 = trd(ra + 2048 * s + 256);
      bw[9 * h + s] = (u32x4){h0[0], h0[1], h1[0], h1[1]};
    }
#pragma unroll
    for (int s = 0; s < 9; ++s) asm volatile("" : "+v"(bw[9 * h + s]));
  };
  uint32_t tv = 0u, tk = 0u;
  const uint32_t vearly = (uint32_t)((VEARLY >> (8 * (g0 >> 5))) & 0xffull);
  auto w_line = [&](int n) {
    const int q = min(lane, 62) / 9, r = min(7 * (n >> 2) + q, 287), h = r >= 144;
    return (const char*)((n & 1) ? wu : wg) + (uint32_t)((r + 144 * (h + ((n >> 1) & 1))) * (F_ * 2) + bx * 1152 + (min(lane, 62) - 9 * q) * 128);
  };
#define TOUCH(pa) asm volatile("global_load_dword v126, %0, off" DPOL4 : : "v"(pa) : "memory", "v126")
  uint32_t wv[5], xv[9];
  auto rows_xw = [&](int lane) {
    u32x4 wa, xr, xs;
    const uint32_t xo = (uint32_t)((8 * g2 + 2 * wl) * (D_ * 2));
    asm volatile("global_load_dwordx4 %0, %5, %9\n\tglobal_load_dword %1, %6, %9 offset:1024\n\t"
                 "global_load_dwordx4 %2, %7, %10\n\tglobal_load_dwordx4 %3, %7, %10 offset:1152\n\tglobal_load_dword %4, %8, %10 offset:1024\n\ts_waitcnt vmcnt(0)"
                 : "=&v"(wa), "=&v"(wv[4]), "=&v"(xr), "=&v"(xs), "=&v"(xv[8])
                 : "v"(16u * lane), "v"(4u * (lane & 31)), "v"(xo + 16u * lane), "v"(xo + 4u * lane + 32u * (lane & 32)), "s"(wr), "s"(x) : "memory");
#pragma unroll
    for (int k = 0; k < 4; ++k) { wv[k] = wa[k]; xv[k] = xr[k]; xv[4 + k] = xs[k]; }
  };
  auto ln1 = [&](int g, int o, uint32_t c0) {
    uint32_t uo = u_at(g, 64 * wl + lane);
    uint32_t ta = lb0 + (uint32_t)(o + ((lane >> 2) & 3) * RP + (64 * wl + 16 * kq + 4 * (lane & 3)) * 2);
    uint32_t la = (uint32_t)(o + 2 * wl * RP + 16 * lane), lt = (uint32_t)(o + (2 * wl + (lane >> 5)) * RP + 1024 + 4 * (lane & 31));
    uint32_t sg = O_SU + (uint32_t)(256 * (lane >> 3) + g) * 128u + 16u * (lane & 7);
    asm volatile("" : "+v"(uo), "+v"(ta), "+v"(la), "+v"(lt), "+v"(sg));
    f32x4 z4 = {0.f, 0.f, 0.f, 0.f};
    asm volatile("" : "+v"(z4));
    asm volatile("s_waitcnt vmcnt(0)" : "={v[100:103]}"(xa), "={v[104:107]}"(xc), "={v108}"(r0), "={v109}"(ep));
    TS(0, 1);
    const uint32_t v[9] = {xa[0], xa[1], xa[2], xa[3], xc[0], xc[1], xc[2], xc[3], r0};
    uint32_t c[9];
    f32x2 tf[9];
    T = ((uint32_t)__builtin_amdgcn_readfirstlane(ep) + 1u) * 0x9E3779B1u;
    if (wl == 3) { const uint32_t now = (uint32_t)__builtin_amdgcn_s_memrealtime() + SD; ST16(sg, ((u32x4){T, now, T, now})); }
    const uint32_t tj = ((T >> (3 * wl)) & 0x00070007u) | (((T >> (12 + wl)) & 0x00010001u) << 14);
    uint32_t tw[3] = {(tj & 0xffffu) * 0x10001u, (tj >> 16) * 0x10001u, 0u};
    tw[2] = (lane < 32) ? tw[0] : tw[1];
    asm volatile("" : "+v"(tw[0]), "+v"(tw[1]), "+v"(tw[2]));
    f32x2 rc, nc;
    float e_;
    ln_stats<false>(v, lane, 0.03125f, tf, z4, rc, nc, e_);
    ln_apply<true>(tf, lane, rc, nc, c);
#pragma unroll
    for (int k = 0; k < 9; ++k) c[k] = (c[k] & ~UMASK) | tw[k >> 2];
    *(u32x4*)(smem + la) = (u32x4){c[0], c[1], c[2], c[3]};
    *(u32x4*)(smem + la + RP) = (u32x4){c[4], c[5], c[6], c[7]};
    *(uint32_t*)(smem + lt) = c[8];
    u32x4 ha, hb;
    {
      uint32_t t, cc, n = SPIN_CAP;
      asm volatile("s_mov_b64 exec, 1\n\tv_mov_b32 %2, 1\n\tds_add_u32 %5, %2\n\ts_mov_b64 exec, -1\n"
                   "1:\n\tds_read_b32 %2, %5\n\tds_read_b64_tr_b16 v[40:41], %6\n\tds_read_b64_tr_b16 v[42:43], %6 offset:%7\n\tds_read_b64_tr_b16 v[44:45], %6 offset:512\n\t"
                   "ds_read_b64_tr_b16 v[46:47], %6 offset:%8\n\ts_sub_u32 %4, %4, 1\n\ts_cbranch_scc1 2f\n\ts_waitcnt lgkmcnt(4)\n\tv_readfirstlane_b32 %3, %2\n\t"
                   "s_cmp_lt_u32 %3, 4\n\ts_cbranch_scc1 1b\n2:\n\ts_waitcnt lgkmcnt(0)"
                   : "=&{v[40:43]}"(ha), "=&{v[44:47]}"(hb), "=&v"(t), "=&s"(cc), "+s"(n) : "v"(c0), "v"(ta), "n"(4 * RP), "n"(512 + 4 * RP) : "memory", "scc");
    }
    ST16(uo, ha);
    ST16(uo + 64 * 128, hb);
    if (wl == 0) {
      const u32x2 h4 = trd(ta + 1024), h5 = trd(ta + 1024 + 4 * RP);
      ST16(uo + 128 * 128, ((u32x4){h4[0], h4[1], h5[0], h5[1]}));
    }
  };
  const int dX = 512 - 24 + 8 * bx + bj;
  if constexpr (ROLE == 2) {
    const int tl = tid - 256;
    if (XOFF && (uint32_t)(bj - 4) < 8u) {
      __builtin_amdgcn_s_setprio(3);
      ln1(tg + 160, 10240, ca + 44);
      TS(0, 2);
    }
    if (!isx && T0) {
      SLEEP(T0SLEEP);
      if (wl < T0) TOUCH(w_line(24 * wl + bj));
      if (T1) { asm volatile("s_waitcnt vmcnt(0)" ::: "memory"); SLEEP(T1SLEEP); if (wl < T1) TOUCH(w_line(24 * (T0 + wl) + bj)); }
    }
    if (isx) {
      __builtin_amdgcn_s_setprio(QPRIO0);
      *(u32x4*)(smem + X_TB + 16 * tl) = zero4;
      *(u32x4*)(smem + X_TB + 16 * min(256 + tl, (TB + C_U) / 16 - 1)) = zero4;
      const int pr = (wl + XROT) & 3;
      COps e;
      conv_ops(lb0 + X_TB, lb0 + X_UL, lb0 + X_IM, lane, e);
      TOps t;
      table_ops(lb0 + X_IM, lb0 + X_TB, wl & 1, lane, t);
      e.o = lb0 + X_ST + (uint32_t)(16 * ((2 * i + (kq >> 1)) ^ ((i >> 2) & 1)) + 8 * (kq & 1));
      e.f = ca + 28 - 32;
      asm volatile("" : "+v"(e.o), "+v"(e.f));
      float ldt, are, aim, bre, bim, cre, cim, dsk;
      P_WAIT(0, , "v103", "v104", "v105", "v106", "v107", "v109");
      asm volatile("" : "={v109}"(ep));
      T = ((uint32_t)__builtin_amdgcn_readfirstlane(ep) + 1u) * 0x9E3779B1u;
      P_MORE();
      asm volatile("v_lshlrev_b32 %0, 16, v107" : "=v"(dsk) : : "v107");
      TS(0, 1);
      __builtin_amdgcn_s_setprio(QPRIO);
      int* evp = (int*)(sel + 44);
      char* im = smem + X_IM + IP * (tl & 127);
      evp[wl] = (wl < 2) ? gen_y(ldt, are, aim, bre, bim, cre, cim, lb0 + X_IM + IP * tl, [] {} TSA) : gen_x(ldt, are, aim, im);
      TS(0, 2);
      __builtin_amdgcn_s_setprio(QPRIO2);
      gsync(ca + 8, 4);
      if (wl < 2) evp[-4 + wl] = (int)table_tile(t, wl & 1, lane, lb0 + LM + 4 * (128 + 44), dsk TSA);
      if (wl < 2) gsync0(ca + 8, 8); else { count_up(ca + 8); SLEEP(XSL); gwait(ca + 8, 8); }
      const uint32_t cs = (uint32_t)__builtin_amdgcn_readfirstlane(evp[-4]);
      if (wl == 2 && lane == 0) asm volatile("global_store_dwordx2 %0, %1, %2 sc1" : : "v"(O_SC + 16u * dX), "v"((u32x2){T, cs}), "s"(ws) : "memory");
      if (wl == 3) *(u32x4*)(smem + X_IM + pad_at(lane & 7)) = zero4;
      TS(0, 3);
      CONV_RUN(2 + pr, e, , "+{v11}"(tid2));
      {
        const int lane = tid2 & 63;
        const u32x4 v = *(const u32x4*)(smem + X_ST + 2048 * pr + 1024 * (lane >> 5) + 16 * ((lane & 31) ^ ((lane >> 3) & 1)));
        if (__builtin_expect(v_put(v, (u32x4){utag(T, 0), utag(T, 1), utag(T, 2), utag(T, 3)}, cs != S16, v_at(dX, 64 * pr + lane)), 0)) {
          DRAIN();
          if (lane == 0) ST4(O_DN + 16u * (4 * (dX - 256) + pr), T);
        }
      }
      TS(0, 6);
    }
    __builtin_amdgcn_s_setprio(PPRIO);
    asm volatile("" : "+v"(tid2));
    if (isx) {
      const int lane = tid2 & 63;
      const u32x4 ra = *(const u32x4*)(smem + X_RW(wl) + 16 * lane), rb = *(const u32x4*)(smem + X_RW(wl) + 1024 + 16 * lane), rw = *(const u32x4*)(smem + X_WR + 16 * lane);
      const u32x2 rc = *(const u32x2*)(smem + X_RW(wl) + 2048 + 8 * lane);
#pragma unroll
      for (int k = 0; k < 4; ++k) { xv[k] = ra[k]; xv[4 + k] = rb[k]; wv[k] = rw[k]; }
      xv[8] = rc[0]; wv[4] = rc[1];
    } else rows_xw(tid2 & 63);
    if (!isx && !ffn) {
      SLEEP(RSLEEP);
#pragma unroll
      for (int k = 0; k < 7; ++k) ST16(O_SL + (uint32_t)((6 * bx + bj - 18) * 1792 + 256 * k - 256 + tid2) * 16u, ones);
      DRAIN();
    }
    if (!isx) BAR();
    const uint32_t tp = (uint32_t)__builtin_amdgcn_readfirstlane(cnt[4]);
    if (!isx) T = (uint32_t)__builtin_amdgcn_readfirstlane(sel[43]);
    tv = tp + UD + VD2 - vearly;
  } else {
    __builtin_amdgcn_s_setprio(3);
    if (tid < 12) cnt[tid] = 0u;
    BAR();
    if (tid < 20) ((uint32_t*)(hm + 320))[tid] = 0u;
    if (tid >= 184) *(u32x4*)(smem + L_SC - 16 * 184 + 16 * tid) = (u32x4){0x41804180u, 0x41804180u, 0x41804180u, 0x41804180u};
    uint32_t sink = 0;
    if (XOFF && isx) {
      asm volatile("s_waitcnt vmcnt(0)" : "={v109}"(ep));
      T = ((uint32_t)__builtin_amdgcn_readfirstlane(ep) + 1u) * 0x9E3779B1u;
    } else ln1(tg, 0, ca + 12);
    TS(0, 2);
    __builtin_amdgcn_s_setprio(NPRIO);
    if (!isx && 3 - w < TB1) TOUCH(w_line(24 * (3 - w) + 23 - bj));
    asm volatile("s_waitcnt vmcnt(0)" : "+v"(sink) : : "memory");
    TS(0, 13);
    if (w == 0) {
      const uint32_t xr = O_XC + (uint32_t)((511 - 2 * (g0 + w)) * 144 + lane) * 16u;
      ST16(xr, ones);
      ST16(xr + 1024u, ones);
      if (lane < 16) ST16(xr + 2048u, ones);
    }
    if (w == 2 && b < 72 && lane < 8) ST16(O_FB + (uint32_t)(8 * b + lane) * 16u, ones);
    if (isx) {
      rows_xw(lane);
      char* sp = smem + X_RW(w) + 16 * lane;
      *(u32x4*)sp = (u32x4){xv[0], xv[1], xv[2], xv[3]};
      *(u32x4*)(sp + 1024) = (u32x4){xv[4], xv[5], xv[6], xv[7]};
      *(u32x2*)(smem + X_RW(w) + 2048 + 8 * lane) = (u32x2){xv[8], wv[4]};
      if (w == 0) *(u32x4*)(smem + X_WR + 16 * lane) = (u32x4){wv[0], wv[1], wv[2], wv[3]};
    }
    SLEEP(ESLEEP);
    uint32_t tu = latest(gate(ws, O_SU + (uint32_t)(256 * bx + 64 * w + 4 * i + (bj & 3)) * 128u, T));
    asm volatile("s_mov_b64 exec, 1\n\tds_max_u32 %0, %1\n\ts_mov_b64 exec, -1" : : "v"(ca + 16), "v"(tu) : "memory");
    if (w == 0) sel[43] = T;
    if (isx) {
      const uint32_t xo = u_at(tid, dX);
      char* sm = smem + X_UL + C_U - 16 + 16 * tid;
      const u32x4 ut = {utag(T, 0), utag(T, 1), utag(T, 2), utag(T, 3)};
      __builtin_amdgcn_s_setprio(3);
      until(tu + UD + XD);
      u32x4 uw;
      asm volatile("global_load_dwordx4 %0, %1, %2 sc1" : "=&v"(uw) : "v"(xo), "s"(ws) : "memory");
      asm volatile("s_waitcnt vmcnt(0)" : "+v"(uw));
      uw ^= ut;
      u_again(uw, ut, xo);
      *(u32x4*)(sm + 16) = uw;
      BAR();
      if (w < 3) {
        const u32x4 pw = *(const u32x4*)sm;
        if (w < 2) sums<true>(smem + X_IM + 16 * tid, pw, uw); else sums<false>(smem + X_IM + 16 * tid, pw, uw);
        count_up(ca + 28);
      }
    } else {
      until(tu + UD + WD);
      BAR();
    }
    tu = (uint32_t)__builtin_amdgcn_readfirstlane(cnt[4]);
    TS(0, 3);
    __builtin_amdgcn_s_setprio(DPRIO);
    if (ffn) {
      ts1 = (tu + UD + VD2 - TSTOP) | 1u;
      gu_dma(0, NA, IC<WDEPTH>{});
      asm volatile("s_waitcnt vmcnt(%0)" : : "n"(NA - 9) : "memory");
      gsync0(ca + 4, 4);
      gu_read(0);
      asm volatile("s_waitcnt lgkmcnt(0)" ::: "memory");
      count_up(ca + 36);
      TS(0, 4);
      gu_dma(NA, 18, IC<WDEPTH2>{});
      gwait(ca + 36, 4);
      down_dma(0, KN, lane);
      asm volatile("s_waitcnt vmcnt(%0)" : : "n"(3 * KN) : "memory");
      gsync0(ca + 4, 8);
      gu_read(1);
      DRAIN();
      TS(0, 7);
    } else {
      if (!isx && C3) {
        const int n = 6 * min(tid, 191) + bj - 18, r = n >> 2;
        const char* wb = (const char*)((n & 1) ? wu : wg) + bx * 1152;
        const int c0 = (3 - (int)((uintptr_t)wb >> 7)) & 7;
        wb += (uint32_t)((r + 144 * ((r >= 144) + ((n >> 1) & 1))) * (F_ * 2) + 128 * c0);
        TOUCH(wb);
        if (c0 == 0) TOUCH(wb + 1024);
      }
      auto d_line = [&](int k) {
        const int n = min((k * 14 + bj - 18) * 64 + lane, 5183), q = n / 1296, r = n - 1296 * q;
        return (const char*)wd + (uint32_t)(bx * 5184 + (r / 18) * 72 + 18 * (HREV ? 3 - q : q) + r % 18) * 128u;
      };
      int k = w;
      if (w < HW) {
#pragma unroll 1
        for (; k < 6 && (int)((uint32_t)__builtin_amdgcn_s_memrealtime() - (tu + UD + VD2 - HSTOP)) < 0; k += HW) { TOUCH(d_line(k)); DRAIN(); }
      }
      TS(0, 4);
      until(tu + UD + VD2 + RD2);
      if (RW) gwait(ca, 4);
      if (w < HW) {
#pragma unroll 1
        for (; k < 6; k += HW) { TOUCH(d_line(k)); DRAIN(); }
      }
      TS(0, 7);
    }
    tv = tu + UD + VD2 - vearly;
    tk = tu + UD + VD2 + KD;
  }
  {
  const int tid = tid2, lane = tid & 63, i = lane & 15, kq = lane >> 4;
  __builtin_amdgcn_s_setprio(3);
  const int PL = ffn ? 6400 : LW + 6656;
  f32x2 xf[9];
  f32x4 za = {0.f, 0.f, 0.f, 0.f}, zb = za;
  uint32_t al = O_XC + 64u * (i >> 3) + 16u * kq + 128u * w;
  uint32_t ha = (ROLE == 1) ? (uint32_t)((4 * (kq & 1) + 2 * (kq >> 1) + (i >> 3)) * 80 + 16 * w + 2 * (i & 7)) : 0u;
  uint32_t h0a = (uint32_t)(min(i, 8) * 80 + 16 * kq), h1a = (uint32_t)(min(i ^ 8, 8) * 80 + 16 * kq);
  uint32_t so = O_SL + (uint32_t)fb * SP + ((ROLE == 2) ? 16u * tid - 4096u : 8192u + 16u * lane);
  uint32_t da = lb0 + (uint32_t)((8 * kq + ((lane >> 2) & 3)) * 1152 + 32 * ((8 * kq + ((lane >> 2) & 3) + 2) >> 2) + 8 * (lane & 3) + ((ROLE == 2) ? 128 * wl : 1024));
  if (ROLE == 2) asm volatile("" : "+v"(h0a), "+v"(h1a), "+v"(so), "+v"(da)); else asm volatile("" : "+v"(ha), "+v"(al), "+v"(da));
  uint32_t hv[9] = {}, nv[9] = {}, keya = 0u, keyb = 0u;
  int rka = 0, rkb = 0;
  auto put_row = [&](const uint32_t* r, int j, const void* base, uint32_t ro) {
    const u32x4 tl = {r[8], (uint32_t)DPP_I((int)r[8], 0x55, 0xf), (uint32_t)DPP_I((int)r[8], 0xaa, 0xf), (uint32_t)DPP_I((int)r[8], 0xff, 0xf)};
    asm volatile("global_store_dwordx4 %0, %1, %2 sc1\n\ts_nop 1" : : "v"(ro + 16u * lane), "v"((u32x4){r[4 * j], r[4 * j + 1], r[4 * j + 2], r[4 * j + 3]}), "s"(base) : "memory");
    if ((lane & 35) == 32 * j) asm volatile("global_store_dwordx4 %0, %1, %2 offset:1024 sc1\n\ts_nop 1" : : "v"(ro + 4u * (lane & 31)), "v"(tl), "s"(base) : "memory");
  };
  if (ln2) {
    const int po = PL - 2560 * (ROLE == 1);
    uint32_t pa = (uint32_t)(po + 9216 * wl + 32 * lane), pb = (uint32_t)(po + 9216 * wl + 2048 + 8 * (lane & 31)), ps = (lane < 32) ? 0u : 16u;
    uint32_t kid = (uint32_t)(((255 - g2) << 5) | (7 - 2 * wl - (lane >> 5)));
    const int d1 = tid - 256, ga = 0, d2 = 512 + 16 * wl + i, gb = 0;
    uint32_t vo = v_at(d1, g0 + ga), vo2 = v_at(d2, g0 + gb), px = (uint32_t)(PL - 2560 * ga + 4 * d1), py = (uint32_t)(PL - 2560 * gb + 4 * d2);
#pragma unroll
    for (int k = 0; k < 9; ++k) xf[k] = (f32x2){bflo(xv[k]), bfhi(xv[k])};
    asm volatile("" : "+v"(xf[0]), "+v"(xf[1]), "+v"(xf[2]), "+v"(xf[3]), "+v"(xf[4]), "+v"(xf[5]), "+v"(xf[6]), "+v"(xf[7]), "+v"(xf[8]));
    asm volatile("" : "+v"(za), "+v"(zb), "+v"(pa), "+v"(pb), "+v"(ps), "+v"(kid), "+v"(vo), "+v"(vo2), "+v"(px), "+v"(py));
    u32x4 ut = {utag(T, 0), utag(T, 1), utag(T, 2), utag(T, 3)};
    float sv[10], ws_ = dot2(wv[4], 0x3f803f80u, 0.f) * 0.5f;
#pragma unroll
    for (int k = 0; k < 4; ++k) ws_ = dot2(wv[k], 0x3f803f80u, ws_);
    ws_ = wave_sum(ws_);
#pragma unroll
    for (int k = 0; k < 10; ++k) sv[k] = 16.f;
    uint32_t ka_ = lb0 + LM + 4u * (128 + 48 + 2 * wl + (lane >> 5)), kb_ = lb0 + LM + 4u * (128 + 48);
    uint32_t ko_ = O_KEY + (uint32_t)(256 * (lane & 7) + 8 * (g2 & 31) + (g2 >> 5)) * 16u;
    asm volatile("" : "+v"(ut), "+v"(ws_), "+v"(sv[0]), "+v"(sv[1]), "+v"(sv[2]), "+v"(sv[3]), "+v"(sv[4]), "+v"(sv[5]), "+v"(sv[6]), "+v"(sv[7]), "+v"(sv[8]), "+v"(sv[9]),
                 "+v"(ka_), "+v"(kb_), "+v"(ko_));
    if (ffn) gwait(ca + 36, 4);
    TS(0, 7);
    until(tv);
    TS(0, 8);
    u32x4 va, vc, vb = ut;
    asm volatile("global_load_dwordx4 %0, %3, %6 sc1\n\tglobal_load_dwordx4 %1, %4, %6 sc1\n\ts_mov_b64 exec, 0xffff\n\tglobal_load_dwordx4 %2, %5, %6 sc1\n\ts_mov_b64 exec, -1\n\t"
                 "s_waitcnt vmcnt(2)" : "=&v"(va), "=&v"(vc), "+v"(vb) : "v"(vo), "v"(vo + 128 * VP), "v"(vo2), "s"(ws) : "memory");
    auto turn = [&] {
#pragma unroll
      for (int e = 0; e < 4; ++e) *(uint32_t*)(smem + px + 1024 + 9216 * e) = vc[e];
      if (lane < 16) {
#pragma unroll
        for (int e = 0; e < 4; ++e) *(uint32_t*)(smem + py + 9216 * e) = vb[e];
      }
    };
    va ^= ut;
#pragma unroll
    for (int e = 0; e < 4; ++e) *(uint32_t*)(smem + px + 9216 * e) = va[e];
    asm volatile("s_waitcnt vmcnt(0)" : "+v"(vc), "+v"(vb) : : "memory");
    vc ^= ut; vb ^= ut;
    turn();
    const u32x4 vs = va | vb | vc;
    if (__builtin_expect(__any(((vs[0] | vs[1] | vs[2] | vs[3]) & UMASK) != 0u), 0)) {
#pragma unroll 1
      for (int spin = 0; spin < 4; ++spin) {
        const bool na = ((va[0] | va[1] | va[2] | va[3]) & UMASK) != 0u, nb = ((vb[0] | vb[1] | vb[2] | vb[3]) & UMASK) != 0u, nc = ((vc[0] | vc[1] | vc[2] | vc[3]) & UMASK) != 0u;
        if (!__any(na || nb || nc)) break;
        TSC(16);
        if (na) LD16(va, vo);
        if (nb) LD16(vb, vo2);
        if (nc) LD16(vc, vo + 128 * VP);
        asm volatile("s_waitcnt vmcnt(0)" : "+v"(va), "+v"(vb), "+v"(vc) : : "memory");
        if (na) va ^= ut;
        if (nb) vb ^= ut;
        if (nc) vc ^= ut;
      }
      auto again = [&](u32x4& v, uint32_t o, int d) {
        const int J = g0 >> 5;
        const uint32_t dn = O_DN + 16u * (uint32_t)((d < 512) ? 4 * (d >> 1) + ((0x01013232 >> (4 * J)) & 3) : 4 * (d - 256) + (J >> 1));
        bool pend = ((v[0] | v[1] | v[2] | v[3]) & UMASK) != 0u;
#pragma unroll 1
        for (int spin = 0; spin < SPIN_CAP && __any(pend); ++spin) {
          TSC(16);
          SLEEP(PSLEEP);
          if (pend) {
            uint32_t fin = ~T;
            if ((v[0] & v[1] & v[2] & v[3] & LMASK) == LMASK)
              asm volatile("global_load_dword %0, %1, %2 sc1\n\ts_waitcnt vmcnt(0)" : "=&v"(fin) : "v"(dn), "s"(ws) : "memory");
            LD16W(v, o);
            v ^= ut;
            pend = ((v[0] | v[1] | v[2] | v[3]) & UMASK) != 0u;
            if (pend && fin == T) {
              u32x2 sc;
              for (int k = 0; k < SPIN_CAP; ++k) {
                asm volatile("global_load_dwordx2 %0, %1, %2 sc1\n\ts_waitcnt vmcnt(0)" : "=&v"(sc) : "v"(O_SC + 16u * d), "s"(ws) : "memory");
                if (sc[0] == T) break;
              }
              *(u16*)(smem + L_SC + 2 * d) = (u16)(sc[1] >> 16);
              cnt[10] = 1u;
              v = (v ^ ut) & ~LMASK;
              pend = false;
            }
          }
        }
      };
      again(va, vo, d1); again(vc, vo + 128 * VP, d1 + 256); again(vb, vo2, d2);
#pragma unroll
      for (int e = 0; e < 4; ++e) *(uint32_t*)(smem + px + 9216 * e) = va[e];
      turn();
    }
    u32x4 a0, a1;
    u32x2 a2, cf;
    {
      uint32_t cc, n = SPIN_CAP;
      asm volatile("s_mov_b64 exec, 1\n\tv_mov_b32 v90, 1\n\tds_add_u32 %6, v90\n\ts_mov_b64 exec, -1\n"
                   "1:\n\tds_read2_b32 %3, %6 offset1:10\n\tds_read_b128 %0, %7\n\tds_read_b128 %1, %7 offset:16\n\tds_read_b64 %2, %8\n\t"
                   "s_sub_u32 %5, %5, 1\n\ts_cbranch_scc1 2f\n\ts_waitcnt lgkmcnt(3)\n\tv_readfirstlane_b32 %4, v90\n\t"
                   "s_cmp_lt_u32 %4, 4\n\ts_cbranch_scc1 1b\n2:\n\ts_waitcnt lgkmcnt(0)"
                   : "=&v"(a0), "=&v"(a1), "=&v"(a2), "=&{v[90:91]}"(cf), "=&s"(cc), "+s"(n) : "v"(ca), "v"(lb0 + pa), "v"(lb0 + pb) : "memory", "scc");
    }
    TS(0, 9);
    if (__builtin_expect(__builtin_amdgcn_readfirstlane(cf[1]) != 0, 0)) {
      const u32x4 q = *(const u32x4*)(smem + L_SC + 16 * lane);
      const uint32_t r = *(const uint32_t*)(smem + L_SC + 1024 + 4 * (lane & 31));
#pragma unroll
      for (int k = 0; k < 4; ++k) { sv[2 * k] = bflo(q[k]); sv[2 * k + 1] = bfhi(q[k]); }
      sv[8] = bflo(r); sv[9] = bfhi(r);
    }
    const uint32_t av[10] = {a0[0], a0[1], a0[2], a0[3], a1[0], a1[1], a1[2], a1[3], a2[0] >> ps, a2[1] >> ps};
    f32x2 h[9];
#pragma unroll
    for (int k = 0; k < 10; ++k) {
      asm("v_fma_mix_f32 %0, %1, %2, %3 op_sel_hi:[1,0,0]" : "=v"(h[(k < 8) ? k >> 1 : 8][k & 1]) : "v"(av[k]), "v"(sv[k]), "v"(xf[(k < 8) ? k >> 1 : 8][k & 1]));
      if (k < 8) asm("v_fma_mix_f32 %0, %1, %2, %3 op_sel:[1,0,0] op_sel_hi:[1,0,0]" : "=v"(h[4 + (k >> 1)][k & 1]) : "v"(av[k]), "v"(sv[k]), "v"(xf[4 + (k >> 1)][k & 1]));
    }
#pragma unroll
    for (int k = 0; k < 9; ++k) hv[k] = pack2(h[k][0], h[k][1]);
    const float da = dot2(hv[2], wv[2], dot2(hv[0], wv[0], zb[0])) + dot2(hv[3], wv[3], dot2(hv[1], wv[1], zb[1]));
    const float db = dot2(hv[6], wv[2], dot2(hv[4], wv[0], zb[2])) + dot2(hv[7], wv[3], dot2(hv[5], wv[1], zb[3]));
    float dt = dot2(hv[8], wv[4], fold32(da, db));
    f32x2 rc, nc;
    ln_stats<true>(hv, lane, 1.f, xf, za, rc, nc, dt);
    dt = (lane < 32) ? dt * rc[0] + ws_ * nc[0] : dt * rc[1] + ws_ * nc[1];
    const uint32_t bb = pack2(dt, dt) & 0xffff0000u;
    const uint32_t kk = (bb ^ (((uint32_t)((int)bb >> 31) & 0x7fff0000u) | 0x80000000u)) | kid;
    u32x4 ka, kb;
    uint32_t old;
    asm volatile("s_mov_b64 exec, %5\n\tds_write_b32 %3, %4\n\ts_mov_b64 exec, 1\n\tv_mov_b32 %2, 1\n\tds_add_rtn_u32 %2, %7, %2\n\ts_mov_b64 exec, -1\n\t"
                 "ds_read_b128 %0, %6\n\tds_read_b128 %1, %6 offset:16\n\ts_waitcnt lgkmcnt(0)"
                 : "=&v"(ka), "=&v"(kb), "=&v"(old) : "v"(ka_), "v"(kk), "s"(0x8000000080000000ull), "v"(kb_), "v"(ca) : "memory");
    if (__builtin_amdgcn_readfirstlane(old) == 7) {
      uint32_t m1 = max(max(ka[0], ka[1]), ka[2]), m2 = med3u(ka[0], ka[1], ka[2]), m3 = min(min(ka[0], ka[1]), ka[2]);
#pragma unroll
      for (int e = 0; e < 5; ++e) {
        const uint32_t kx = e ? kb[e - 1] : ka[3];
        m3 = med3u(m2, m3, kx); m2 = med3u(m1, m2, kx); m1 = max(m1, kx);
      }
      if (lane < 8) ST16(ko_, ((u32x4){m1 | 24u, m2 | 16u, m3 | 8u, T & ~1u}));
    }
    TS(0, 10);
    keya = (uint32_t)__builtin_amdgcn_readlane(kk, 31); keyb = (uint32_t)__builtin_amdgcn_readlane(kk, 63);
    ln_apply<false>(xf, lane, rc, nc, nv);
    gwait(ca, 8);
    if (wl == 1) {
      const u32x4 kv = *(const u32x4*)&kl[4 * (lane & 1)];
      if (lane < 2) ST16(O_KF + (uint32_t)(2 * g2 + lane) * 16u, ((u32x4){__builtin_amdgcn_perm(kv[1], kv[0], 0x07060302u), T, __builtin_amdgcn_perm(kv[3], kv[2], 0x07060302u), T}));
      if (ROLE == 2 && b == 0 && lane == 0) ST4(O_EP, T * 0x0E8B2F51u);
    }
    const uint32_t ko = kl[lane & 7];
    rka = FORCEFB + __popcll(__ballot(ko > keya) & 0xffull); rkb = FORCEFB + __popcll(__ballot(ko > keyb) & 0xffull);
#pragma unroll
    for (int k = 0; k < 9; ++k) {
      const f32x2 o = xf[k] + (f32x2){bflo(nv[k]), bfhi(nv[k])};
      hv[k] = pack2(o[0], o[1]);
      nv[k] = min(nv[k], 0xfffffffeu);
    }
    if (rka < 2) put_row(nv, 0, ws, O_XC + (uint32_t)(1023 - 4 * g2 - rka) * 1152u);
    if (rkb < 2) put_row(nv, 1, ws, O_XC + (uint32_t)(1023 - 4 * g2 - rkb) * 1152u);
  }
  uint32_t k3rd = 0u, k4th = 0u;
  if constexpr (ROLE == 1) {
    u32x4 key;
    const uint32_t kof = O_KEY + (uint32_t)(256 * bx + tid) * 16u;
    until(tk);
    if (!ffn) SLEEP(NSLEEP);
    LD16W(key, kof);
    if (__builtin_expect(__any(key[3] != (T & ~1u)), 0)) {
#pragma unroll 1
      for (int spin = 0; spin < SPIN_CAP; ++spin) {
        const bool need = key[3] != (T & ~1u);
        if (!__any(need)) break;
        TSC(15);
        SLEEP(KBACK);
        if (need) LD16W(key, kof);
      }
    }
    TS(0, 11);
    const uint32_t m1 = oct_maxu(key[0]);
    const bool hit = key[0] == m1;
    const uint32_t k0 = hit ? key[1] : key[0], k1 = hit ? key[2] : key[1];
    const uint32_t m2 = oct_maxu(k0);
    if ((lane & 7) == 0) *(u32x2*)&cand[tid >> 2] = (u32x2){m1, m2};
    k3rd = (k0 == m2) ? k1 : k0;
    k4th = (k0 == m2) ? (hit ? 0u : key[2]) : k1;
    if (RPF && !ffn && w == (bj & 3)) {
      const uint32_t r1 = O_XC + ((m1 >> 3) & 0x3ffu) * 1152u + 128u * (lane & 7), r2 = O_XC + ((m2 >> 3) & 0x3ffu) * 1152u + 128u * (lane & 7);
      asm volatile("global_load_dword v126, %0, %3 sc1\n\tglobal_load_dword v126, %1, %3 sc1\n\tglobal_load_dword v126, %2, %3 offset:1024 sc1"
                   : : "v"(r1), "v"(r2), "v"(((lane & 1) ? r1 : r2) - 128u * (lane & 7)), "s"(ws) : "memory", "v126");
    }
  }
  auto pick = [&](const uint32_t* cd, uint32_t* sl) {
    BAR();
    const uint32_t c = cd[lane], own = cd[8 * w + (lane & 7)];
    const u32x4 la = *(const u32x4*)&cd[8 * w], lb = *(const u32x4*)&cd[8 * w + 4];
    uint32_t r = C_;
    asm("v_cmp_gt_u32 s[40:41], %1, %2\n\tv_cmp_gt_u32 s[42:43], %1, %3\n\tv_cmp_gt_u32 s[44:45], %1, %4\n\tv_cmp_gt_u32 s[46:47], %1, %5\n\t"
        "v_cmp_gt_u32 s[48:49], %1, %6\n\tv_cmp_gt_u32 s[50:51], %1, %7\n\tv_cmp_gt_u32 s[52:53], %1, %8\n\tv_cmp_gt_u32 s[54:55], %1, %9\n\t"
        "s_bcnt1_i32_b64 s40, s[40:41]\n\ts_bcnt1_i32_b64 s42, s[42:43]\n\tv_writelane_b32 %0, s40, 0\n\ts_bcnt1_i32_b64 s44, s[44:45]\n\t"
        "v_writelane_b32 %0, s42, 1\n\ts_bcnt1_i32_b64 s46, s[46:47]\n\tv_writelane_b32 %0, s44, 2\n\ts_bcnt1_i32_b64 s48, s[48:49]\n\t"
        "v_writelane_b32 %0, s46, 3\n\ts_bcnt1_i32_b64 s50, s[50:51]\n\tv_writelane_b32 %0, s48, 4\n\ts_bcnt1_i32_b64 s52, s[52:53]\n\t"
        "v_writelane_b32 %0, s50, 5\n\ts_bcnt1_i32_b64 s54, s[54:55]\n\tv_writelane_b32 %0, s52, 6\n\tv_writelane_b32 %0, s54, 7"
        : "+v"(r) : "v"(c), "v"(la[0]), "v"(la[1]), "v"(la[2]), "v"(la[3]), "v"(lb[0]), "v"(lb[1]), "v"(lb[2]), "v"(lb[3])
        : "scc", "s40", "s41", "s42", "s43", "s44", "s45", "s46", "s47", "s48", "s49", "s50", "s51", "s52", "s53", "s54", "s55");
    sl[min(r, (uint32_t)C_)] = own;
    BAR();
  };
  auto rows = [&](uint32_t ao, auto&& meanwhile) {
    u32x4 aa, ab, ac = __builtin_bit_cast(u32x4, zb);
    asm volatile("global_load_dwordx4 %0, %3, %4 sc1\n\tglobal_load_dwordx4 %1, %3, %4 offset:512 sc1\n\t"
                 "s_cmp_lg_u32 %5, 0\n\ts_cbranch_scc1 1f\n\tglobal_load_dwordx4 %2, %3, %4 offset:1024 sc1\n1:"
                 : "=&v"(aa), "=&v"(ab), "+v"(ac) : "v"(ao), "s"(ws), "s"(w) : "memory", "scc");
    __builtin_amdgcn_sched_barrier(0);
    meanwhile();
    asm volatile("s_waitcnt vmcnt(0)" : "+v"(aa), "+v"(ab), "+v"(ac) : : "memory");
    if (__builtin_expect(__any(max(max(max4(aa), max4(ab)), max4(ac)) == 0xffffffffu), 0)) {
#pragma unroll 1
      for (int spin = 0; spin < SPIN_CAP; ++spin) {
        const bool na = max4(aa) == 0xffffffffu, nb = max4(ab) == 0xffffffffu, nc = max4(ac) == 0xffffffffu;
        if (!__any(na || nb || nc)) break;
        TSC(14);
        SLEEP(PSLEEP);
        if (na) LD16W(aa, ao);
        if (nb) LD16W(ab, ao + 512u);
        if (nc) LD16W(ac, ao + 1024u);
      }
    }
    *(u32x4*)(smem + LA + 16 * tid) = aa;
    *(u32x4*)(smem + LA + 4096 + 16 * tid) = ab;
    if (w == 0) *(u32x4*)(smem + LA + 8192 + 16 * tid) = ac;
  };
  auto get_bd = [&](int t0, int t1) {
#pragma unroll
    for (int t = t0; t < t1; ++t) {
      const u32x2 h0 = trd(da + 512 * (t >> 2) + 32 * (t & 3)), h1 = trd(da + 512 * (t >> 2) + 32 * (t & 3) + 4608 + 32);
      bw[t] = (u32x4){h0[0], h0[1], h1[0], h1[1]};
    }
#pragma unroll
    for (int t = t0; t < t1; ++t) asm volatile("" : "+v"(bw[t]));
  };
  pick(cand, sel);
  uint32_t mykey = sel[lane & 7], ro = ((mykey >> 3) & 0x3ffu) * 1152u;
  TS(0, 12);
  auto check = [&] {
    cnt[(k3rd > (uint32_t)__builtin_amdgcn_readlane(mykey, 7)) ? 5 : 6] = 1u;
    const uint32_t m3 = oct_maxu(k3rd);
    if (k3rd == m3) k3rd = k4th;
    if ((lane & 7) == 0) cand[72 + (tid >> 3)] = m3;
    if (w == 0 && lane < 32) cand[(lane < 8) ? 64 + lane : 96 + lane] = (lane < 8) ? mykey : 0u;
  };
  if constexpr (ROLE == 1) {
    if (ffn && !FORCEFB) rows(ro + al, check); else { check(); DRAIN(); }
  } else if (ffn) {
    if (KN < 4) { SLEEP(LSLEEP); down_dma(KN, 4, lane); } else get_bd(0, 8);
  }
  BAR();
  TS(0, 5);
  auto put_out = [&](int j, uint32_t k0, int rk) {
    const uint64_t mine = __ballot(((mykey ^ k0) & ~24u) == 0u) & 0xffull;
    if (mine == 0ull) put_row(hv, j, out, (uint32_t)((8 * g2 + 2 * wl + j) * (D_ * 2)));
    else if (rk >= 2) put_row(nv, j, ws, O_FB + 1152u * (uint32_t)__builtin_ctzll(mine));
  };
  auto rest_out = [&] { put_out(0, keya, rka); put_out(1, keyb, rkb); };
  f32x4 lo, hi;
  auto gate_up = [&] {
    u32x4 av[5];
    const uint32_t aa = lb0 + LA + 16u * lane;
#pragma unroll
    for (int j = 0; j < 5; ++j) asm volatile("ds_read_b128 %0, %1 offset:%2" : "=v"(av[j]) : "v"(aa), "n"(1024 * j) : "memory");
    lo = (f32x4){0.f, 0.f, 0.f, 0.f}; hi = lo;
#pragma unroll
    for (int j = 0; j < 9; ++j) {
      if (j < 5) asm volatile("s_waitcnt lgkmcnt(4)" : "+v"(av[j % 5]));
      else if (j == 5) asm volatile("s_waitcnt lgkmcnt(2)" : "+v"(av[0]), "+v"(av[1]));
      else if (j == 7) asm volatile("s_waitcnt lgkmcnt(0)" : "+v"(av[2]), "+v"(av[3]));
      lo = MMB(av[j % 5], bw[2 * j], lo);
      hi = MMB(av[j % 5], bw[2 * j + 1], hi);
      __builtin_amdgcn_sched_barrier(0);
      if (j < 4) asm volatile("ds_read_b128 %0, %1 offset:%2" : "=v"(av[j]) : "v"(aa), "n"(1024 * (j + 5)) : "memory");
    }
  };
  uint32_t fl;
  asm volatile("ds_read_b32 %0, %1" : "=v"(fl) : "v"(ca + 20) : "memory");
  if (ROLE == 1) gate_up();
  asm volatile("s_waitcnt lgkmcnt(0)" : "+v"(fl));
  const bool slow = FORCESLOW || FORCEFB || __builtin_amdgcn_readfirstlane(fl) != 0;
  if (__builtin_expect(slow, 0)) {
    TSC(17);
    pick(cand + 64, sel + 16);
    mykey = sel[16 + (lane & 7)];
    ro = ((mykey >> 3) & 0x3ffu) * 1152u;
    if (ROLE == 1 && (k3rd > (uint32_t)__builtin_amdgcn_readlane(mykey, 7) || (mykey & 24u) == 8u)) cnt[11] = 1u;
    BAR();
    if (FORCESLOW > 1 || FORCEFB || __builtin_amdgcn_readfirstlane(cnt[11]) != 0) {
    u32x4 key;
#pragma unroll 1
    for (int spin = 0; spin < SPIN_CAP; ++spin) {
      LD16W(key, O_KF + 16u * tid);
      if (!__any(key[1] != T || key[3] != T)) break;
      SLEEP(KBACK);
    }
    const uint32_t ib = (uint32_t)(((255 - (tid >> 1)) << 5) | (7 - 4 * (tid & 1)));
    uint32_t k0 = (key[0] << 16) | ib, k1 = (key[0] & 0xffff0000u) | (ib - 1), k2 = (key[2] << 16) | (ib - 2), k3 = (key[2] & 0xffff0000u) | (ib - 3);
    CEX(k0, k1) CEX(k2, k3) CEX(k0, k2) CEX(k1, k3) CEX(k1, k2)
#pragma unroll 1
    for (int c = 0; c < C_; ++c) {
      const uint32_t mx = wave_maxu(k0);
      const bool hit = (k0 == mx);
      k0 = hit ? k1 : k0; k1 = hit ? k2 : k1; k2 = hit ? k3 : k2; k3 = hit ? 0u : k3;
      if (lane == 0) cand[64 + 8 * w + c] = mx;
    }
    pick(cand + 64, sel + 32);
    mykey = sel[32 + (lane & 7)];
    const uint32_t okey = sel[32 + (lane >> 3)];
    const uint64_t same = __ballot(((mykey ^ okey) & 0x1fe0u) == 0u && okey > mykey);
    const int sl = FORCEFB + __popcll((same >> (lane & 7)) & 0x0101010101010101ull);
    ro = (sl < 2) ? (((mykey >> 3) & 0x3ffu) + 3 - sl) * 1152u : O_FB - O_XC + 1152u * (lane & 7);
    }
    if (ln2) rest_out();
    if (ffn) {
      if (ROLE == 1) rows(ro + al, [] {});
      BAR();
      if (ROLE == 1) gate_up();
    }
  }
  if (OPOS == 0 && ROLE == 2 && ffn && !slow) { SLEEP(OD); rest_out(); }
  if (!ffn) {
    if (ln2 && !slow) { SLEEP(OD2); rest_out(); }
    TSE();
    return;
  }
  uint32_t fx = 0, fv = 0, fn = 0, fo = 0, sa[9];
  float rs, hx, xs;
  u32x4 se;
  if constexpr (ROLE == 1) {
    const f32x4 lh = (lane < 32) ? lo : hi;
    const float fa = fold32(lh[0], lh[2]), fb2 = fold32(lh[1], lh[3]);
    const float g = __int_as_float(__builtin_amdgcn_update_dpp(__float_as_int(fa), __float_as_int(fb2), 0x128, 0xf, 0xc, false));
    const float u = __int_as_float(__builtin_amdgcn_update_dpp(__float_as_int(fb2), __float_as_int(fa), 0x128, 0xf, 0x3, false));
    const uint32_t gu = pack2(g, u);
    const float gp = bflo(gu);
    const float sg = rbf(gp * __builtin_amdgcn_rcpf(1.f + __expf(-gp)));
    *(u16*)((char*)hm + ha) = f2bf(sg * bfhi(gu));
  } else if (KN < 4) {
    DRAIN();
    gsync0(ca, 12);
    get_bd(0, 8);
  }
  BAR();
  TS(0, 6);
  if (ROLE == 2 || w == 2) {
    const u32x4 a0 = *(const u32x4*)((char*)hm + h0a), a1 = *(const u32x4*)((char*)hm + h1a);
    if (ROLE == 1) get_bd(0, 4);
    const f32x4 z = {0.f, 0.f, 0.f, 0.f};
    f32x4 o[4];
#pragma unroll
    for (int t = 0; t < 2 * ROLE; ++t) o[t] = MMB(a1, bw[2 * t + 1], MMB(a0, bw[2 * t], z));
#pragma unroll
    for (int t = 0; t < ROLE; ++t) {
      u32x4 sv;
#pragma unroll
      for (int r = 0; r < 4; ++r) sv[r] = min(pack2(o[2 * t][r], o[2 * t + 1][r]), 0xfffffffeu);
      ST16(so + 4096u * t, sv);
    }
    TS(0, 13);
  }
  if (OPOS == 1 && ROLE == 2) {
    SLEEP(OSLEEP);
    if (!slow) rest_out();
  }
  if (ROLE == 1 && w == 1) {
    const int q = (fb & 15) >> 2, ts = 4 * (q & 1) + ((i & 7) >> 1);
    const int dd = 64 * (fb >> 4) + 16 * (2 * (i & 1) + (q >> 1)) + 4 * (fb & 3) + 2 * (kq & 1) + (kq >> 1);
    const uint32_t fkey = (uint32_t)__builtin_amdgcn_ds_bpermute(4 * ts, (int)mykey), fr = (uint32_t)__builtin_amdgcn_ds_bpermute(4 * ts, (int)ro);
    const int ft = 2047 - (int)(((fkey >> 2) & 0x7f8u) | (fkey & 7u));
    fo = (uint32_t)(ft * D_ + dd) * 2u;
    u32x2 fs;
    asm volatile("global_load_ushort %0, %4, %8\n\tglobal_load_ushort %1, %5, %9 sc1\n\tglobal_load_ushort %2, %6, %9 sc1\n\tglobal_load_dwordx2 %3, %7, %9 sc1"
                 : "=&v"(fx), "=&v"(fv), "=&v"(fn), "=&v"(fs) : "v"(fo), "v"(v_at(dd, ft >> 3) + 2u * (ft & 7)), "v"(O_XC + fr + 2u * dd), "v"(O_SC + 16u * dd), "s"(x), "s"(ws) : "memory");
    uint32_t t16 = (((T >> (3 * ((ft >> 1) & 3))) & LMASK) | (((T >> (12 + ((ft >> 1) & 3))) & 0x00010001u) << 14)) >> (16 * (ft & 1)) & 0xffffu;
    const uint32_t o = fkey >> 16;
    const float rs0 = bf2f((u16)((o & 0x8000u) ? (o & 0x7fffu) : (~o & 0xffffu)));
    rs = rbf(__builtin_amdgcn_rcpf(1.f + __expf(-rs0)));
    const uint32_t one = (i < 8) ? (0x3f80u << (16 * (i & 1))) : 0u;
    se = (u32x4){(i >> 1) == 0 ? one : 0u, (i >> 1) == 1 ? one : 0u, (i >> 1) == 2 ? one : 0u, (i >> 1) == 3 ? one : 0u};
#pragma unroll
    for (int m = 0; m < 9; ++m) sa[m] = O_SL + (uint32_t)(16 * m + (lane >> 2)) * SP + (uint32_t)(4 * fb + (lane & 3)) * 16u;
    asm volatile("" : "+v"(t16), "+v"(rs), "+v"(se), "+v"(sa[0]), "+v"(sa[1]), "+v"(sa[2]), "+v"(sa[3]), "+v"(sa[4]), "+v"(sa[5]), "+v"(sa[6]), "+v"(sa[7]), "+v"(sa[8]));
    SLEEP(SSLEEP);
    TS(0, 1);
    float sum;
#pragma unroll 1
    for (int spin = 0; spin < SPIN_CAP; ++spin) {
      u32x4 sv[9];
#pragma unroll
      for (int m = 0; m < 9; ++m) LD16(sv[m], sa[m]);
      asm volatile("s_waitcnt vmcnt(9)" : "+v"(fx), "+v"(fv), "+v"(fn), "+v"(fs));
      if (__builtin_expect(__any(fs[0] != T), 0)) {
        for (int k = 0; k < SPIN_CAP && fs[0] != T; ++k)
          asm volatile("global_load_dwordx2 %0, %1, %2 sc1\n\ts_waitcnt vmcnt(0)" : "=&v"(fs) : "v"(O_SC + 16u * dd), "s"(ws) : "memory");
      }
      const uint32_t f1 = fv ^ t16, f2 = (f1 & 7u) ? fv & ~7u : f1;
      hx = rbf(bf2f((u16)fx) + (float)__builtin_bit_cast(_Float16, (u16)f2) * __uint_as_float(fs[1]));
      xs = bf2f((u16)fn);
      asm volatile("" : "+v"(hx), "+v"(xs));
      f32x4 dv = {0.f, 0.f, 0.f, 0.f};
      static_for<0, 9>([&](auto mm) {
        constexpr int m = mm.value;
        asm volatile("s_waitcnt vmcnt(%1)" : "+v"(sv[m]) : "n"(8 - m));
        dv = MMB(sv[m], se, dv);
        __builtin_amdgcn_sched_barrier(0);
      });
      sum = fold16(fold32(dv[0], dv[1]), fold32(dv[2], dv[3]));
      if (__builtin_expect(!__any(sum != sum), 1)) break;
      uint32_t mx = 0u;
#pragma unroll
      for (int m = 0; m < 9; ++m) mx = max(mx, max4(sv[m]));
      if (!__any(mx == 0xffffffffu)) break;
      TSC(18);
      SLEEP(PSLEEP);
    }
    const float o1 = rbf(xs + rbf(rbf(sum) * rs));
    if (i < 8) asm volatile("global_store_short %0, %1, %2 sc1" : : "v"(fo), "v"((uint32_t)f2bf(hx + o1)), "s"(out) : "memory");
    TS(0, 2);
  }
  TSE();
  }
  }
}
#define BROT "192"
#define XLIM "0x100"
#define XNUM "0"
extern "C" __global__ __launch_bounds__(512, 4) void kf_all(KARGS) {
  __shared__ __attribute__((aligned(16))) char smem[SMEM];
  asm volatile("v_readfirstlane_b32 s98, v0\n\ts_load_dwordx16 s[64:79], s[0:1], 0x0\n\ts_load_dwordx4 s[80:83], s[0:1], 0x40\n\t"
               "s_add_u32 s97, s2, " BROT "\n\ts_and_b32 s97, s97, 0xff\n\ts_and_b32 s2, s2, 0x100\n\ts_or_b32 s2, s2, s97\n\t"
               "s_cmpk_lt_u32 s2, 0x100\n\ts_cbranch_scc0 1f\n\ts_cmpk_lt_u32 s98, 0x100\n\ts_cbranch_scc0 2f\n"
               "7:\n\ts_and_b32 s84, s97, 0xe0\n\ts_bfe_u32 s85, s97, 0x20003\n\ts_or_b32 s84, s84, s85\n\ts_and_b32 s85, s97, 7\n\ts_lshl2_add_u32 s84, s85, s84\n\t"
               "s_mulk_i32 s84, 0x2400\n\ts_and_b32 s96, s98, 0xc0\n\ts_mulk_i32 s96, 36\n\ts_add_u32 s84, s84, s96\n\tv_and_b32 v110, 63, v0\n\tv_and_b32 v111, 32, v0\n\t"
               "v_lshlrev_b32 v111, 5, v111\n\tv_lshl_add_u32 v111, v110, 2, v111\n\tv_lshl_add_u32 v110, v110, 4, s84\n\tv_add_u32 v111, s84, v111\n\t"
               "v_mov_b32 v109, 0\n\ts_cmpk_lt_u32 s2, " XLIM "\n\ts_waitcnt lgkmcnt(0)\n\ts_cbranch_scc0 8f\n\t"
               "global_load_dwordx4 v[100:103], v110, s[64:65]\n\tglobal_load_dwordx4 v[104:107], v110, s[64:65] offset:1152\n\t"
               "global_load_dword v108, v111, s[64:65] offset:1024\n8:\n\tglobal_load_dword v109, v109, s[82:83] sc1\n\t"
               "s_cmp_lg_u32 s97, s2\n\ts_cbranch_scc1 6f\n\ts_cmpk_lt_u32 s2, 0x90\n\ts_cbranch_scc1 kf_n\n\ts_branch kf_h\n"
               "1:\n\ts_bfe_u32 s84, s2, 0x50003\n\ts_and_b32 s85, s2, 7\n\ts_lshl_b32 s85, s85, 5\n\ts_or_b32 s84, s84, s85\n\t"
               "s_lshl_b32 s85, s84, 9\n\ts_lshl_b32 s84, s84, 2\n\tv_and_b32 v110, 0xff, v0\n\tv_bfe_u32 v107, v0, 7, 1\n\t"
               "v_lshl_add_u32 v110, v110, 1, s85\n\tv_lshl_add_u32 v107, v107, 1, s84\n\tv_mov_b32 v109, 0\n\ts_cmpk_lt_u32 s98, 0x100\n\ts_waitcnt lgkmcnt(0)\n\t"
               "global_load_ushort v100, v107, s[78:79]\n\tglobal_load_ushort v101, v110, s[66:67]\n\tglobal_load_ushort v102, v110, s[68:69]\n\t"
               "s_cbranch_scc0 3f\n"
               "4:\n\tglobal_load_ushort v103, v110, s[70:71]\n\tglobal_load_ushort v104, v110, s[72:73]\n\tglobal_load_ushort v105, v110, s[74:75]\n\t"
               "global_load_ushort v106, v110, s[76:77]\n\tglobal_load_ushort v107, v107, s[80:81]\n\tglobal_load_dword v109, v109, s[82:83] sc1\n\ts_cbranch_scc1 kf_c\n\ts_barrier\n\ts_branch kf_p\n"
               "3:\n\tglobal_load_dword v109, v109, s[82:83] sc1\n\ts_branch kf_u\n"
               "2:\n\ts_cmpk_lt_u32 s2, 0xc0\n\ts_cbranch_scc1 5f\n\t"
               "s_lshr_b32 s84, s2, 3\n\ts_and_b32 s85, s2, 7\n\ts_lshl3_add_u32 s84, s85, s84\n\ts_addk_i32 s84, 488\n\ts_lshl_b32 s85, s84, 8\n\ts_lshl_b32 s84, s84, 1\n\t"
               "v_and_b32 v110, 0x7f, v0\n\tv_lshl_add_u32 v110, v110, 1, s85\n\tv_mov_b32 v107, s84\n\tv_mov_b32 v109, 0\n\ts_cmp_eq_u32 s2, 0\n\ts_waitcnt lgkmcnt(0)\n\t"
               "global_load_ushort v100, v107, s[78:79]\n\tglobal_load_ushort v101, v110, s[66:67]\n\tglobal_load_ushort v102, v110, s[68:69]\n\ts_branch 4b\n"
               "5:\n\ts_add_u32 s97, s2, 160\n\ts_sub_u32 s99, s2, 32\n\ts_cmpk_lt_u32 s99, " XNUM "\n\ts_cbranch_scc1 7b\n\t"
               "s_cmpk_lt_u32 s2, 32\n\ts_cselect_b32 s99, 0, 64\n\ts_sub_u32 s99, s2, s99\n\t"
               "s_and_b32 s99, s99, 0x78\n\ts_lshl_b32 s99, s99, 5\n\ts_and_b32 s98, s98, 0xc0\n\ts_or_b32 s98, s98, s99\n\ts_lshl_b32 s98, s98, 1\n\t"
               "s_getpc_b64 s[96:97]\n\ts_add_u32 s96, s96, s98\n\ts_addc_u32 s97, s97, 0\n\t"
               "s_load_dword s99, s[96:97], 0x0\n\ts_load_dword s99, s[96:97], 0x40\n\ts_load_dword s99, s[96:97], 0x2000\n\ts_load_dword s99, s[96:97], 0x2040\n\t"
               "s_load_dword s99, s[96:97], 0x4000\n\ts_load_dword s99, s[96:97], 0x4040\n\ts_load_dword s99, s[96:97], 0x6000\n\ts_load_dword s99, s[96:97], 0x6040\n\t"
               "s_load_dword s99, s[96:97], 0x8000\n\ts_load_dword s99, s[96:97], 0x8040\n\t"
               "s_cmp_gt_i32 s98, kf_end-kf_all-0xa300\n\ts_cbranch_scc1 6f\n\t"
               "s_load_dword s99, s[96:97], 0xa000\n\ts_load_dword s99, s[96:97], 0xa040\n\t"
               "s_cmp_gt_i32 s98, kf_end-kf_all-0xc300\n\ts_cbranch_scc1 6f\n\t"
               "s_load_dword s99, s[96:97], 0xc000\n\ts_load_dword s99, s[96:97], 0xc040\n6:\n\ts_barrier\n\ts_branch kf_p"
               : : "s"(__builtin_amdgcn_kernarg_segment_ptr()), "s"(blockIdx.x), "v"(threadIdx.x),
                   "s"((uint32_t)(uintptr_t)(__attribute__((address_space(3))) char*)smem) : "scc", "s96", "s97", "s98", "s99", "v127");
}
extern "C" __global__ __launch_bounds__(512, 4) void kf_c(KARGS) {
  __shared__ __attribute__((aligned(16))) char smem[SMEM];
  role_body<0>(smem, KPASS);
}
extern "C" __global__ __launch_bounds__(512, 4) void kf_u(KARGS) {
  __shared__ __attribute__((aligned(16))) char smem[SMEM];
  role_body<3>(smem, KPASS);
}
extern "C" __global__ __launch_bounds__(512, 4) void kf_n(KARGS) {
  __shared__ __attribute__((aligned(16))) char smem[SMEM];
  role_body<1, 1>(smem, KPASS);
}
extern "C" __global__ __launch_bounds__(512, 4) void kf_h(KARGS) {
  __shared__ __attribute__((aligned(16))) char smem[SMEM];
  role_body<1, 0>(smem, KPASS);
}
extern "C" __global__ __launch_bounds__(512, 4) void kf_p(KARGS) {
  __shared__ __attribute__((aligned(16))) char smem[SMEM];
  role_body<2>(smem, KPASS);
}
extern "C" __global__ __launch_bounds__(512, 4) void kf_conv() { asm volatile(CONV_ASM ::: CONV_CLOB); }
extern "C" __global__ void kf_end() {}

extern "C" void kernel_launch(void* const* d_in, const int* in_sizes, int n_in,
                              void* d_out, int out_size, void* d_ws, size_t ws_size,
                              hipStream_t stream) {
  kf_all<<<512, 512, 0, stream>>>((const u16*)d_in[0], (const u16*)d_in[1], (const u16*)d_in[2], (const u16*)d_in[3], (const u16*)d_in[4],
                                  (const u16*)d_in[5], (const u16*)d_in[6], (const u16*)d_in[7], (const u16*)d_in[8], (char*)d_ws + (size_t)WSOFF * 1048576,
                                  (const u16*)d_in[10], (const u16*)d_in[11], (const u16*)d_in[12], (const u16*)d_in[13], (u16*)d_out);
}
```

```cpp
#include <hip/hip_runtime.h>
#include <hip/hip_bf16.h>
#include <stdint.h>

typedef __attribute__((ext_vector_type(8))) short bf16x8;
typedef __attribute__((ext_vector_type(4))) float f32x4;
typedef uint16_t u16;
typedef __attribute__((ext_vector_type(4))) uint32_t u32x4;
typedef __attribute__((ext_vector_type(2))) uint32_t u32x2;
#define TSD
#define TS(k, i)
#define TSC(i)
#define TSE()
#define TSP
#define TSA

constexpr int L_ = 2048, D_ = 576, N_ = 128, F_ = 4608, C_ = 8;
constexpr int NF = 144;
constexpr uint32_t O_EP = 0;
constexpr uint32_t O_KF = 4096;
constexpr uint32_t O_KEY = O_KF + 8192;
constexpr uint32_t O_SC = O_KEY + 32768;
constexpr uint32_t O_DN = O_SC + 9216;
constexpr uint32_t O_SU = O_DN + 20480;
constexpr uint32_t O_FB = O_SU + 262144;
constexpr uint32_t O_XC = O_FB + 8 * 1152;
constexpr uint32_t SP = 9216 + 128;
constexpr uint32_t O_SL = O_XC + 1024 * 1152;
constexpr uint32_t SLU = 336;
constexpr uint32_t O_U = O_SL + 256 * SLU * 16;
constexpr int UP = 584;
constexpr uint32_t VP = 8192 + 128, VPP = 4096 + 128;
constexpr uint32_t O_V = O_U + 256 * UP * 16, O_VP = O_V + 256 * VP;
static_assert(256 * SLU * 16 >= NF * SP && O_SL % 128 == 0 && O_U % 128 == 0 && O_V % 128 == 0, "");
__host__ __device__ __forceinline__ uint32_t u_at(int tg, int d) { return O_U + 16u * (uint32_t)((tg >> 1) * (UP * 2) + (d >> 2) * 8 + (tg & 1) * 4 + (d & 3)); }
__host__ __device__ __forceinline__ uint32_t v_at(int d, int tg) {
  return (d < 512) ? O_V + (uint32_t)(d >> 1) * VP + 16u * (uint32_t)(2 * tg + (d & 1)) : O_VP + (uint32_t)(d - 512) * VPP + 16u * (uint32_t)tg;
}
#define HAVE_VAT
#define HAVE_VTAG
__device__ __forceinline__ float bf2f(u16 v) { return __uint_as_float((uint32_t)v << 16); }
__device__ __forceinline__ float bflo(uint32_t v) { return __uint_as_float(v << 16); }
__device__ __forceinline__ float bfhi(uint32_t v) { return __uint_as_float(v & 0xffff0000u); }
__device__ __forceinline__ u16 f2bf(float f) { return __builtin_bit_cast(u16, (__bf16)f); }
__device__ __forceinline__ float rbf(float f) { return bf2f(f2bf(f)); }
typedef __attribute__((ext_vector_type(2))) float f32x2;
typedef __attribute__((ext_vector_type(2))) __bf16 bf16x2v;
__device__ __forceinline__ uint32_t pack2(float a, float b) {
  return __builtin_bit_cast(uint32_t, __builtin_convertvector((f32x2){a, b}, bf16x2v));
}

#define DPP_I(v, ctrl, rmask) __builtin_amdgcn_update_dpp(0, (v), (ctrl), (rmask), 0xf, true)
__device__ __forceinline__ float row_sum(float v) {
  v += __int_as_float(DPP_I(__float_as_int(v), 0x111, 0xf));
  v += __int_as_float(DPP_I(__float_as_int(v), 0x112, 0xf));
  v += __int_as_float(DPP_I(__float_as_int(v), 0x114, 0xf));
  v += __int_as_float(DPP_I(__float_as_int(v), 0x118, 0xf));
  return v;
}
__device__ __forceinline__ float rdl(float v, int l) { return __int_as_float(__builtin_amdgcn_readlane(__float_as_int(v), l)); }
__device__ __forceinline__ float wave_sum(float v) {
  v = row_sum(v);
  asm("s_nop 1\n\tv_add_f32_dpp %0, %0, %0 row_bcast:15 row_mask:0xa bank_mask:0xf\n\t"
      "s_nop 1\n\tv_add_f32_dpp %0, %0, %0 row_bcast:31 row_mask:0xc bank_mask:0xf\n\ts_nop 0" : "+v"(v));
  return rdl(v, 63);
}
__device__ __forceinline__ float fold32(float a, float b) {
  const auto r = __builtin_amdgcn_permlane32_swap(__float_as_uint(a), __float_as_uint(b), false, false);
  return __uint_as_float(r[0]) + __uint_as_float(r[1]);
}
__device__ __forceinline__ float fold16(float a, float b) {
  const auto r = __builtin_amdgcn_permlane16_swap(__float_as_uint(a), __float_as_uint(b), false, false);
  return __uint_as_float(r[0]) + __uint_as_float(r[1]);
}
__device__ __forceinline__ void wave_sum2(float& a, float& b) {
  float v = row_sum(fold32(a, b));
  asm("s_nop 1\n\tv_add_f32_dpp %0, %0, %0 row_bcast:15 row_mask:0xa bank_mask:0xf\n\ts_nop 0" : "+v"(v));
  a = rdl(v, 31); b = rdl(v, 63);
}
__device__ __forceinline__ float dot2(uint32_t a, uint32_t b, float c) {
  return __builtin_amdgcn_fdot2_f32_bf16(__builtin_bit_cast(bf16x2v, a), __builtin_bit_cast(bf16x2v, b), c, false);
}
__device__ __forceinline__ uint32_t row_maxu(uint32_t v) {
  v = max(v, (uint32_t)DPP_I((int)v, 0xb1, 0xf));
  v = max(v, (uint32_t)DPP_I((int)v, 0x4e, 0xf));
  v = max(v, (uint32_t)DPP_I((int)v, 0x141, 0xf));
  v = max(v, (uint32_t)DPP_I((int)v, 0x140, 0xf));
  return v;
}
__device__ __forceinline__ uint32_t wave_maxu(uint32_t v) {
  v = max(v, (uint32_t)DPP_I((int)v, 0x111, 0xf));
  v = max(v, (uint32_t)DPP_I((int)v, 0x112, 0xf));
  v = max(v, (uint32_t)DPP_I((int)v, 0x114, 0xf));
  v = max(v, (uint32_t)DPP_I((int)v, 0x118, 0xf));
  v = max(v, (uint32_t)DPP_I((int)v, 0x142, 0xa));
  v = max(v, (uint32_t)DPP_I((int)v, 0x143, 0xc));
  return (uint32_t)__builtin_amdgcn_readlane((int)v, 63);
}

typedef __attribute__((ext_vector_type(8))) _Float16 f16x8;
typedef __attribute__((ext_vector_type(2))) _Float16 f16x2;
__device__ __forceinline__ uint32_t packh(f32x2 v) { return __builtin_bit_cast(uint32_t, __builtin_convertvector(v, f16x2)); }
__device__ __forceinline__ uint32_t packh(float a, float b) { return packh((f32x2){a, b}); }
constexpr int IP = 224, I_X = 128;
__device__ __forceinline__ f32x2 cmul(f32x2 z, f32x2 w) {
  f32x2 r;
  asm("v_pk_mul_f32 %0, %1, %2 op_sel_hi:[0,1]\n\tv_pk_fma_f32 %0, %1, %2, %0 op_sel:[1,1,0] op_sel_hi:[1,0,1] neg_lo:[0,1,0]" : "=&v"(r) : "v"(z), "v"(w));
  return r;
}
__device__ __forceinline__ f32x2 cexp2(float ar2, float air, float l) {
  const float t = air * l;
  return (f32x2){__builtin_amdgcn_cosf(t), __builtin_amdgcn_sinf(t)} * __builtin_amdgcn_exp2f(ar2 * l);
}
__device__ __forceinline__ float state_a(float ldt, float are, float aim, float& ar2, float& air, float& g) {
  const float dt = rbf(__expf(ldt));
  ar2 = dt * are * 1.4426950408889634f;
  air = dt * aim * 0.15915494309189535f;
  air -= __builtin_rintf(air);
  g = 0.f;
  if (__builtin_expect(__any(ar2 > 0.f), 0)) g = fminf(__uint_as_float(wave_maxu(__float_as_uint(fmaxf(ar2, 0.f)))), 1.f);
  return dt;
}
template <int N> __device__ __forceinline__ void put_im(char* p, int im, const f32x2* z) {
  *(u32x4*)p = (u32x4){packh(z[0][0], z[1][0]), packh(z[2][0], z[3][0]), packh(z[4][0], z[5][0]), packh(z[6][0], z[7][0])};
  *(u32x4*)(p + im) = (u32x4){packh(z[0][1], z[1][1]), packh(z[2][1], z[3][1]), packh(z[4][1], z[5][1]), packh(z[6][1], z[7][1])};
}
#define Y_ASM \
  "v_mov_b64 v[40:41], %0\nv_pk_mul_f32 v[42:43], v[40:41], %1 op_sel_hi:[0,1]\nv_pk_mul_f32 v[44:45], %0, %2 op_sel_hi:[0,1]\n" \
  "v_pk_fma_f32 v[42:43], v[40:41], %1, v[42:43] op_sel:[1,1,0] op_sel_hi:[1,0,1] neg_lo:[0,1,0]\n" \
  "v_pk_fma_f32 v[44:45], %0, %2, v[44:45] op_sel:[1,1,0] op_sel_hi:[1,0,1] neg_lo:[0,1,0]\nv_cvt_pk_f16_f32 v56, v40, v42\n" \
  "v_cvt_pk_f16_f32 v60, v41, v43\nv_pk_mul_f32 v[40:41], v[42:43], %1 op_sel_hi:[0,1]\nv_pk_mul_f32 v[48:49], v[44:45], %2 op_sel_hi:[0,1]\n" \
  "v_pk_fma_f32 v[40:41], v[42:43], %1, v[40:41] op_sel:[1,1,0] op_sel_hi:[1,0,1] neg_lo:[0,1,0]\n" \
  "v_pk_fma_f32 v[48:49], v[44:45], %2, v[48:49] op_sel:[1,1,0] op_sel_hi:[1,0,1] neg_lo:[0,1,0]\n" \
  "v_pk_mul_f32 v[42:43], v[40:41], %1 op_sel_hi:[0,1]\nv_pk_mul_f32 v[52:53], v[48:49], %2 op_sel_hi:[0,1]\n" \
  "v_pk_fma_f32 v[42:43], v[40:41], %1, v[42:43] op_sel:[1,1,0] op_sel_hi:[1,0,1] neg_lo:[0,1,0]\n" \
  "v_pk_fma_f32 v[52:53], v[48:49], %2, v[52:53] op_sel:[1,1,0] op_sel_hi:[1,0,1] neg_lo:[0,1,0]\nv_cvt_pk_f16_f32 v57, v40, v42\n" \
  "v_cvt_pk_f16_f32 v61, v41, v43\nv_pk_mul_f32 v[40:41], v[42:43], %1 op_sel_hi:[0,1]\n" \
  "v_pk_fma_f32 v[40:41], v[42:43], %1, v[40:41] op_sel:[1,1,0] op_sel_hi:[1,0,1] neg_lo:[0,1,0]\n" \
  "v_pk_mul_f32 v[42:43], v[40:41], %1 op_sel_hi:[0,1]\nv_pk_mul_f32 v[46:47], v[44:45], %1 op_sel_hi:[0,1]\n" \
  "v_pk_fma_f32 v[42:43], v[40:41], %1, v[42:43] op_sel:[1,1,0] op_sel_hi:[1,0,1] neg_lo:[0,1,0]\n" \
  "v_pk_fma_f32 v[46:47], v[44:45], %1, v[46:47] op_sel:[1,1,0] op_sel_hi:[1,0,1] neg_lo:[0,1,0]\nv_cvt_pk_f16_f32 v58, v40, v42\n" \
  "v_cvt_pk_f16_f32 v62, v41, v43\nv_cvt_pk_f16_f32 v64, v44, v46\nv_cvt_pk_f16_f32 v68, v45, v47\n" \
  "v_pk_mul_f32 v[40:41], v[42:43], %1 op_sel_hi:[0,1]\nv_pk_mul_f32 v[44:45], v[46:47], %1 op_sel_hi:[0,1]\n" \
  "v_pk_fma_f32 v[40:41], v[42:43], %1, v[40:41] op_sel:[1,1,0] op_sel_hi:[1,0,1] neg_lo:[0,1,0]\n" \
  "v_pk_fma_f32 v[44:45], v[46:47], %1, v[44:45] op_sel:[1,1,0] op_sel_hi:[1,0,1] neg_lo:[0,1,0]\n" \
  "v_pk_mul_f32 v[42:43], v[40:41], %1 op_sel_hi:[0,1]\nv_pk_mul_f32 v[46:47], v[44:45], %1 op_sel_hi:[0,1]\n" \
  "v_pk_fma_f32 v[42:43], v[40:41], %1, v[42:43] op_sel:[1,1,0] op_sel_hi:[1,0,1] neg_lo:[0,1,0]\n" \
  "v_pk_fma_f32 v[46:47], v[44:45], %1, v[46:47] op_sel:[1,1,0] op_sel_hi:[1,0,1] neg_lo:[0,1,0]\nv_cvt_pk_f16_f32 v59, v40, v42\n" \
  "v_cvt_pk_f16_f32 v63, v41, v43\nv_cvt_pk_f16_f32 v65, v44, v46\nv_cvt_pk_f16_f32 v69, v45, v47\n" \
  "v_pk_mul_f32 v[44:45], v[46:47], %1 op_sel_hi:[0,1]\nds_write_b128 %3, v[56:59] offset:0\n" \
  "v_pk_fma_f32 v[44:45], v[46:47], %1, v[44:45] op_sel:[1,1,0] op_sel_hi:[1,0,1] neg_lo:[0,1,0]\nds_write_b128 %3, v[60:63] offset:64\n" \
  "v_pk_mul_f32 v[46:47], v[44:45], %1 op_sel_hi:[0,1]\nv_pk_mul_f32 v[50:51], v[48:49], %1 op_sel_hi:[0,1]\n" \
  "v_pk_fma_f32 v[46:47], v[44:45], %1, v[46:47] op_sel:[1,1,0] op_sel_hi:[1,0,1] neg_lo:[0,1,0]\n" \
  "v_pk_fma_f32 v[50:51], v[48:49], %1, v[50:51] op_sel:[1,1,0] op_sel_hi:[1,0,1] neg_lo:[0,1,0]\nv_cvt_pk_f16_f32 v66, v44, v46\n" \
  "v_cvt_pk_f16_f32 v70, v45, v47\nv_cvt_pk_f16_f32 v56, v48, v50\nv_cvt_pk_f16_f32 v60, v49, v51\n" \
  "v_pk_mul_f32 v[44:45], v[46:47], %1 op_sel_hi:[0,1]\nv_pk_mul_f32 v[48:49], v[50:51], %1 op_sel_hi:[0,1]\n" \
  "v_pk_fma_f32 v[44:45], v[46:47], %1, v[44:45] op_sel:[1,1,0] op_sel_hi:[1,0,1] neg_lo:[0,1,0]\n" \
  "v_pk_fma_f32 v[48:49], v[50:51], %1, v[48:49] op_sel:[1,1,0] op_sel_hi:[1,0,1] neg_lo:[0,1,0]\n" \
  "v_pk_mul_f32 v[46:47], v[44:45], %1 op_sel_hi:[0,1]\nv_pk_mul_f32 v[50:51], v[48:49], %1 op_sel_hi:[0,1]\n" \
  "v_pk_fma_f32 v[46:47], v[44:45], %1, v[46:47] op_sel:[1,1,0] op_sel_hi:[1,0,1] neg_lo:[0,1,0]\n" \
  "v_pk_fma_f32 v[50:51], v[48:49], %1, v[50:51] op_sel:[1,1,0] op_sel_hi:[1,0,1] neg_lo:[0,1,0]\nv_cvt_pk_f16_f32 v67, v44, v46\n" \
  "v_cvt_pk_f16_f32 v71, v45, v47\nv_cvt_pk_f16_f32 v57, v48, v50\nv_cvt_pk_f16_f32 v61, v49, v51\n" \
  "v_pk_mul_f32 v[48:49], v[50:51], %1 op_sel_hi:[0,1]\nds_write_b128 %3, v[64:67] offset:16\n" \
  "v_pk_fma_f32 v[48:49], v[50:51], %1, v[48:49] op_sel:[1,1,0] op_sel_hi:[1,0,1] neg_lo:[0,1,0]\nds_write_b128 %3, v[68:71] offset:80\n" \
  "v_pk_mul_f32 v[50:51], v[48:49], %1 op_sel_hi:[0,1]\nv_pk_mul_f32 v[54:55], v[52:53], %1 op_sel_hi:[0,1]\n" \
  "v_pk_fma_f32 v[50:51], v[48:49], %1, v[50:51] op_sel:[1,1,0] op_sel_hi:[1,0,1] neg_lo:[0,1,0]\n" \
  "v_pk_fma_f32 v[54:55], v[52:53], %1, v[54:55] op_sel:[1,1,0] op_sel_hi:[1,0,1] neg_lo:[0,1,0]\nv_cvt_pk_f16_f32 v58, v48, v50\n" \
  "v_cvt_pk_f16_f32 v62, v49, v51\nv_cvt_pk_f16_f32 v64, v52, v54\nv_cvt_pk_f16_f32 v68, v53, v55\n" \
  "v_pk_mul_f32 v[48:49], v[50:51], %1 op_sel_hi:[0,1]\nv_pk_mul_f32 v[52:53], v[54:55], %1 op_sel_hi:[0,1]\n" \
  "v_pk_fma_f32 v[48:49], v[50:51], %1, v[48:49] op_sel:[1,1,0] op_sel_hi:[1,0,1] neg_lo:[0,1,0]\n" \
  "v_pk_fma_f32 v[52:53], v[54:55], %1, v[52:53] op_sel:[1,1,0] op_sel_hi:[1,0,1] neg_lo:[0,1,0]\n" \
  "v_pk_mul_f32 v[50:51], v[48:49], %1 op_sel_hi:[0,1]\nv_pk_mul_f32 v[54:55], v[52:53], %1 op_sel_hi:[0,1]\n" \
  "v_pk_fma_f32 v[50:51], v[48:49], %1, v[50:51] op_sel:[1,1,0] op_sel_hi:[1,0,1] neg_lo:[0,1,0]\n" \
  "v_pk_fma_f32 v[54:55], v[52:53], %1, v[54:55] op_sel:[1,1,0] op_sel_hi:[1,0,1] neg_lo:[0,1,0]\nv_cvt_pk_f16_f32 v59, v48, v50\n" \
  "v_cvt_pk_f16_f32 v63, v49, v51\nv_cvt_pk_f16_f32 v65, v52, v54\nv_cvt_pk_f16_f32 v69, v53, v55\n" \
  "v_pk_mul_f32 v[52:53], v[54:55], %1 op_sel_hi:[0,1]\nds_write_b128 %3, v[56:59] offset:32\n" \
  "v_pk_fma_f32 v[52:53], v[54:55], %1, v[52:53] op_sel:[1,1,0] op_sel_hi:[1,0,1] neg_lo:[0,1,0]\nds_write_b128 %3, v[60:63] offset:96\n" \
  "v_pk_mul_f32 v[54:55], v[52:53], %1 op_sel_hi:[0,1]\n" \
  "v_pk_fma_f32 v[54:55], v[52:53], %1, v[54:55] op_sel:[1,1,0] op_sel_hi:[1,0,1] neg_lo:[0,1,0]\nv_cvt_pk_f16_f32 v66, v52, v54\n" \
  "v_cvt_pk_f16_f32 v70, v53, v55\nv_pk_mul_f32 v[52:53], v[54:55], %1 op_sel_hi:[0,1]\n" \
  "v_pk_fma_f32 v[52:53], v[54:55], %1, v[52:53] op_sel:[1,1,0] op_sel_hi:[1,0,1] neg_lo:[0,1,0]\n" \
  "v_pk_mul_f32 v[54:55], v[52:53], %1 op_sel_hi:[0,1]\n" \
  "v_pk_fma_f32 v[54:55], v[52:53], %1, v[54:55] op_sel:[1,1,0] op_sel_hi:[1,0,1] neg_lo:[0,1,0]\nv_cvt_pk_f16_f32 v67, v52, v54\n" \
  "v_cvt_pk_f16_f32 v71, v53, v55\nds_write_b128 %3, v[64:67] offset:48\nds_write_b128 %3, v[68:71] offset:112\n"
#define Y_CLOB "memory", "v40", "v41", "v42", "v43", "v44", "v45", "v46", "v47", "v48", "v49", "v50", "v51", "v52", "v53", "v54", "v55", "v56", "v57", "v58", "v59", "v60", "v61", "v62", "v63", "v64", "v65", "v66", "v67", "v68", "v69", "v70", "v71"
#define T_ASM \
  "ds_read_b128 %4, %8\nds_read_b64_tr_b16 v[80:81], %7 offset:0\nds_read_b64_tr_b16 v[82:83], %7 offset:64\n" \
  "ds_read_b64_tr_b16 v[84:85], %5 offset:0\nds_read_b64_tr_b16 v[86:87], %5 offset:48\nds_read_b64_tr_b16 v[88:89], %6 offset:0\n" \
  "ds_read_b64_tr_b16 v[90:91], %6 offset:48\nds_read_b64_tr_b16 v[92:93], %7 offset:3584\nds_read_b64_tr_b16 v[94:95], %7 offset:3648\n" \
  "ds_read_b64_tr_b16 v[96:97], %5 offset:3584\nds_read_b64_tr_b16 v[98:99], %5 offset:3632\nds_read_b64_tr_b16 v[100:101], %6 offset:3584\n" \
  "ds_read_b64_tr_b16 v[102:103], %6 offset:3632\nds_read_b64_tr_b16 v[104:105], %7 offset:7168\nds_read_b64_tr_b16 v[106:107], %7 offset:7232\n" \
  "s_waitcnt lgkmcnt(8)\nv_mfma_f32_16x16x32_f16 %0, v[84:87], v[80:83], 0\nds_read_b64_tr_b16 v[108:109], %5 offset:7168\n" \
  "ds_read_b64_tr_b16 v[110:111], %5 offset:7216\nds_read_b64_tr_b16 v[112:113], %6 offset:7168\nv_mfma_f32_16x16x32_f16 %2, v[88:91], v[80:83], 0\n" \
  "ds_read_b64_tr_b16 v[114:115], %6 offset:7216\nds_read_b64_tr_b16 v[116:117], %7 offset:10752\nds_read_b64_tr_b16 v[118:119], %7 offset:10816\n" \
  "s_waitcnt lgkmcnt(8)\nv_mfma_f32_16x16x32_f16 %0, v[96:99], v[92:95], %0\nds_read_b64_tr_b16 v[120:121], %5 offset:10752\n" \
  "ds_read_b64_tr_b16 v[122:123], %5 offset:10800\nds_read_b64_tr_b16 v[124:125], %6 offset:10752\n" \
  "v_mfma_f32_16x16x32_f16 %2, v[100:103], v[92:95], %2\nds_read_b64_tr_b16 v[126:127], %6 offset:10800\n" \
  "ds_read_b64_tr_b16 v[80:81], %7 offset:14336\nds_read_b64_tr_b16 v[82:83], %7 offset:14400\ns_waitcnt lgkmcnt(8)\n" \
  "v_mfma_f32_16x16x32_f16 %0, v[108:111], v[104:107], %0\nds_read_b64_tr_b16 v[84:85], %5 offset:14336\n" \
  "ds_read_b64_tr_b16 v[86:87], %5 offset:14384\nds_read_b64_tr_b16 v[88:89], %6 offset:14336\n" \
  "v_mfma_f32_16x16x32_f16 %2, v[112:115], v[104:107], %2\nds_read_b64_tr_b16 v[90:91], %6 offset:14384\n" \
  "ds_read_b64_tr_b16 v[92:93], %7 offset:17920\nds_read_b64_tr_b16 v[94:95], %7 offset:17984\ns_waitcnt lgkmcnt(8)\n" \
  "v_mfma_f32_16x16x32_f16 %0, v[120:123], v[116:119], %0\nds_read_b64_tr_b16 v[96:97], %5 offset:17920\n" \
  "ds_read_b64_tr_b16 v[98:99], %5 offset:17968\nds_read_b64_tr_b16 v[100:101], %6 offset:17920\n" \
  "v_mfma_f32_16x16x32_f16 %2, v[124:127], v[116:119], %2\nds_read_b64_tr_b16 v[102:103], %6 offset:17968\n" \
  "ds_read_b64_tr_b16 v[104:105], %7 offset:21504\nds_read_b64_tr_b16 v[106:107], %7 offset:21568\ns_waitcnt lgkmcnt(8)\n" \
  "v_mfma_f32_16x16x32_f16 %1, v[84:87], v[80:83], 0\nds_read_b64_tr_b16 v[108:109], %5 offset:21504\nds_read_b64_tr_b16 v[110:111], %5 offset:21552\n" \
  "ds_read_b64_tr_b16 v[112:113], %6 offset:21504\nv_mfma_f32_16x16x32_f16 %3, v[88:91], v[80:83], 0\nds_read_b64_tr_b16 v[114:115], %6 offset:21552\n" \
  "ds_read_b64_tr_b16 v[116:117], %7 offset:25088\nds_read_b64_tr_b16 v[118:119], %7 offset:25152\ns_waitcnt lgkmcnt(8)\n" \
  "v_mfma_f32_16x16x32_f16 %1, v[96:99], v[92:95], %1\nds_read_b64_tr_b16 v[120:121], %5 offset:25088\n" \
  "ds_read_b64_tr_b16 v[122:123], %5 offset:25136\nds_read_b64_tr_b16 v[124:125], %6 offset:25088\n" \
  "v_mfma_f32_16x16x32_f16 %3, v[100:103], v[92:95], %3\nds_read_b64_tr_b16 v[126:127], %6 offset:25136\ns_waitcnt lgkmcnt(6)\n" \
  "v_mfma_f32_16x16x32_f16 %1, v[108:111], v[104:107], %1\nv_mfma_f32_16x16x32_f16 %3, v[112:115], v[104:107], %3\ns_waitcnt lgkmcnt(0)\n" \
  "v_mfma_f32_16x16x32_f16 %1, v[120:123], v[116:119], %1\nv_mfma_f32_16x16x32_f16 %3, v[124:127], v[116:119], %3\n"
#define T_CLOB "memory", "v80", "v81", "v82", "v83", "v84", "v85", "v86", "v87", "v88", "v89", "v90", "v91", "v92", "v93", "v94", "v95", "v96", "v97", "v98", "v99", "v100", "v101", "v102", "v103", "v104", "v105", "v106", "v107", "v108", "v109", "v110", "v111", "v112", "v113", "v114", "v115", "v116", "v117", "v118", "v119", "v120", "v121", "v122", "v123", "v124", "v125", "v126", "v127"
template <class F> __device__ __forceinline__ int gen_y(float ldt, float are, float aim, float bre, float bim, float cre, float cim, uint32_t im, F&& mid TSP) {
  float ar2, air, g;
  const float dt = state_a(ldt, are, aim, ar2, air, g);
  const float dbr = dt * bre, dbi = dt * bim, pr = cre * dbr - cim * dbi, pi = cre * dbi + cim * dbr;
  const f32x2 w8 = cexp2(ar2, -air, 8.f), w64 = cexp2(ar2, -air, 64.f);
  const float pm = fmaxf(fabsf(pr), fabsf(pi));
  int e = -4;
  if (__builtin_expect(__any(!(pm < 0x1p-4f) || ar2 > 0.f) || !__any(pm > 0x1p-12f), 0)) e = (int)(wave_maxu(__float_as_uint(pm)) >> 23) - 126 + (int)ceilf(g * 250.f);
  const f32x2 y0 = {__builtin_amdgcn_ldexpf(pr, 14 - e), __builtin_amdgcn_ldexpf(-pi, 14 - e)};
  mid();
  TS(0, 7);
  asm volatile(Y_ASM : : "v"(y0), "v"(w8), "v"(w64), "v"(im) : Y_CLOB);
  return e;
}
__device__ __forceinline__ int gen_x(float ldt, float are, float aim, char* im) {
  float ar2, air, g;
  state_a(ldt, are, aim, ar2, air, g);
  const int e = min((int)ceilf(g * 1810.f), 300);
  const float s = __builtin_amdgcn_ldexpf(1.f, 15 - e);
  const f32x2 w1 = cexp2(ar2, air, 1.f), w256 = cexp2(ar2, air, 256.f), w2 = cmul(w1, w1);
  f32x2 a[8], b[8], c[8];
  a[0] = (f32x2){s, 0.f}; a[1] = w1 * s; a[2] = w2 * s;
  b[0] = b[1] = w256 * s;
  b[2] = cmul(b[0], w2); a[3] = cmul(a[1], w2); a[4] = cmul(a[2], w2);
  b[3] = b[4] = b[5] = cmul(b[2], w2); a[5] = cmul(a[3], w2); a[6] = cmul(a[4], w2);
  b[6] = cmul(b[3], w2); a[7] = cmul(a[5], w2); c[0] = cmul(b[3], w256);
  b[7] = cmul(b[6], w2); c[2] = cmul(c[0], w256);
  put_im<0>(im + I_X, 48, a);
  c[1] = cmul(b[7], w256); c[3] = cmul(c[1], w256);
  put_im<0>(im + I_X + 16, 48, b);
#pragma unroll
  for (int k = 4; k < 8; ++k) c[k] = cmul(c[k - 1], w256);
  put_im<0>(im + I_X + 32, 48, c);
  return e;
}

template <int I> struct IC { static constexpr int value = I; };
template <int B, int E, class F> __device__ __forceinline__ void static_for(F&& f) {
  if constexpr (B < E) { f(IC<B>{}); static_for<B + 1, E>(f); }
}
typedef __attribute__((ext_vector_type(4))) short s16x4;
__device__ __forceinline__ u32x2 trd(uint32_t a) {
  return __builtin_bit_cast(u32x2, __builtin_amdgcn_ds_read_tr16_b64_v4i16((__attribute__((address_space(3))) s16x4*)(uintptr_t)a));
}
#define BAR() asm volatile("s_waitcnt lgkmcnt(0)\n\ts_barrier" ::: "memory")
#define ST16(off, v) asm volatile("global_store_dwordx4 %0, %1, %2 sc1\n\ts_nop 1" : : "v"(off), "v"(v), "s"(ws) : "memory")
#define ST4(off, v) asm volatile("global_store_dword %0, %1, %2 sc1" : : "v"(off), "v"(v), "s"(ws) : "memory")
#define LD16(r, off) asm volatile("global_load_dwordx4 %0, %1, %2 sc1" : "=v"(r) : "v"(off), "s"(ws) : "memory")
#define LD16W(r, off) asm volatile("global_load_dwordx4 %0, %1, %2 sc1\n\ts_waitcnt vmcnt(0)" : "=&v"(r) : "v"(off), "s"(ws) : "memory")
#define DRAIN() asm volatile("s_waitcnt vmcnt(0)" ::: "memory")
#define CEX(a, b) { const uint32_t hi_ = max(a, b), lo_ = min(a, b); a = hi_; b = lo_; }
#define MMB(a_, b_, c_) __builtin_amdgcn_mfma_f32_16x16x32_bf16(__builtin_bit_cast(bf16x8, a_), __builtin_bit_cast(bf16x8, b_), c_, 0, 0, 0)
#define NTM 0
#define DPOL ""
#define DPOL2 ""
#define DPOL4 ""
constexpr int SPIN_CAP = 1 << 18;
constexpr uint32_t UMASK = 0x40074007u;
__device__ __forceinline__ uint32_t utag(uint32_t T, int j) {
  return ((T >> (3 * j)) & 0x00070007u) | (((j <= 2) ? (T << (2 - j)) : (T >> (j - 2))) & 0x40004000u);
}
__device__ __forceinline__ uint32_t max4(const u32x4& a) { return max(max(a[0], a[1]), max(a[2], a[3])); }
__device__ __forceinline__ void until(uint32_t t) {
#pragma unroll 1
  for (int spin = 0; spin < 600 && (int)((uint32_t)__builtin_amdgcn_s_memrealtime() - t) < 0; ++spin) __builtin_amdgcn_s_sleep(1);
}
__device__ __forceinline__ uint32_t latest(uint32_t t) {
  const uint32_t t0 = (uint32_t)__builtin_amdgcn_readfirstlane(t) - 0x40000000u;
  return wave_maxu(t - t0) + t0;
}
__device__ __forceinline__ uint32_t gate(const char* ws, uint32_t po, uint32_t T) {
  u32x2 pv;
#pragma unroll 1
  for (int spin = 0; spin < SPIN_CAP; ++spin) {
    asm volatile("global_load_dwordx2 %0, %1, %2 sc1\n\ts_waitcnt vmcnt(0)" : "=&v"(pv) : "v"(po), "s"(ws) : "memory");
    if (!__any(pv[0] != T)) break;
    __builtin_amdgcn_s_sleep(2);
  }
  return pv[1];
}
__device__ __forceinline__ uint32_t gate2(const char* ws, uint32_t oa, uint32_t da, uint32_t ob, uint32_t T) {
  u32x2 pa, pb;
#pragma unroll 1
  for (int spin = 0; spin < SPIN_CAP; ++spin) {
    asm volatile("global_load_dwordx2 %0, %2, %4 sc1\n\tglobal_load_dwordx2 %1, %3, %4 sc1\n\ts_waitcnt vmcnt(0)" : "=&v"(pa), "=&v"(pb) : "v"(oa), "v"(ob), "s"(ws) : "memory");
    if (!__any(pa[0] != T || pb[0] != T)) break;
    __builtin_amdgcn_s_sleep(4);
  }
  pa[1] += da;
  return latest(((int)(pa[1] - pb[1]) > 0) ? pa[1] : pb[1]);
}
__device__ __forceinline__ void gsync(uint32_t a, uint32_t target) {
  uint32_t t, c, n = SPIN_CAP;
  uint64_t ex;
  asm volatile("s_waitcnt lgkmcnt(0)\n\ts_mov_b64 %3, exec\n\ts_mov_b64 exec, 1\n\tv_mov_b32 %0, 1\n\tds_add_u32 %4, %0\n\ts_mov_b64 exec, %3\n"
               "1:\n\ts_sleep 1\n\tds_read_b32 %0, %4\n\ts_sub_u32 %2, %2, 1\n\ts_cbranch_scc1 2f\n\ts_waitcnt lgkmcnt(0)\n\tv_readfirstlane_b32 %1, %0\n\t"
               "s_cmp_lt_u32 %1, %5\n\ts_cbranch_scc1 1b\n2:\n\ts_waitcnt lgkmcnt(0)"
               : "=&v"(t), "=&s"(c), "+s"(n), "=&s"(ex) : "v"(a), "n"(target) : "memory", "scc");
}
__device__ __forceinline__ void gsync0(uint32_t a, uint32_t target) {
  uint32_t t, c, n = SPIN_CAP;
  asm volatile("s_waitcnt lgkmcnt(0)\n\ts_mov_b64 exec, 1\n\tv_mov_b32 %0, 1\n\tds_add_u32 %3, %0\n\ts_mov_b64 exec, -1\n"
               "1:\n\tds_read_b32 %0, %3\n\ts_sub_u32 %2, %2, 1\n\ts_cbranch_scc1 2f\n\ts_waitcnt lgkmcnt(0)\n\tv_readfirstlane_b32 %1, %0\n\t"
               "s_cmp_lt_u32 %1, %4\n\ts_cbranch_scc1 1b\n2:\n\ts_waitcnt lgkmcnt(0)"
               : "=&v"(t), "=&s"(c), "+s"(n) : "v"(a), "n"(target) : "memory", "scc");
}
__device__ __forceinline__ void gwait(uint32_t a, uint32_t target) {
  uint32_t t, c, n = SPIN_CAP;
  asm volatile("1:\n\ts_sleep 2\n\tds_read_b32 %0, %3\n\ts_sub_u32 %2, %2, 1\n\ts_cbranch_scc1 2f\n\ts_waitcnt lgkmcnt(0)\n\tv_readfirstlane_b32 %1, %0\n\t"
               "s_cmp_lt_u32 %1, %4\n\ts_cbranch_scc1 1b\n2:\n\ts_waitcnt lgkmcnt(0)"
               : "=&v"(t), "=&s"(c), "+s"(n) : "v"(a), "n"(target) : "memory", "scc");
}
__device__ __forceinline__ void gwait0(uint32_t a, uint32_t target) {
  uint32_t t, c, n = SPIN_CAP;
  asm volatile("1:\n\tds_read_b32 %0, %3\n\ts_sub_u32 %2, %2, 1\n\ts_cbranch_scc1 2f\n\ts_waitcnt lgkmcnt(0)\n\tv_readfirstlane_b32 %1, %0\n\t"
               "s_cmp_lt_u32 %1, %4\n\ts_cbranch_scc1 1b\n2:\n\ts_waitcnt lgkmcnt(0)"
               : "=&v"(t), "=&s"(c), "+s"(n) : "v"(a), "n"(target) : "memory", "scc");
}
__device__ __forceinline__ void dma16(const void* base, uint32_t voff, uint32_t lds) {
  uint32_t keep;
  asm volatile("s_mov_b32 %0, m0\n\ts_mov_b32 m0, %3\n\ts_nop 0\n\tglobal_load_lds_dwordx4 %1, %2" DPOL "\n\ts_mov_b32 m0, %0" : "=&s"(keep) : "v"(voff), "s"(base), "s"(lds) : "memory");
}
__device__ __forceinline__ void dma16d(const void* base, uint32_t voff, uint32_t lds) {
  uint32_t keep;
  asm volatile("s_mov_b32 %0, m0\n\ts_mov_b32 m0, %3\n\ts_nop 0\n\tglobal_load_lds_dwordx4 %1, %2" DPOL2 "\n\ts_mov_b32 m0, %0" : "=&s"(keep) : "v"(voff), "s"(base), "s"(lds) : "memory");
}
__device__ __forceinline__ void nap(int n) {
  for (; n > 15; n -= 15) __builtin_amdgcn_s_sleep(15);
  switch (n) {
    case 1: __builtin_amdgcn_s_sleep(1); break; case 2: __builtin_amdgcn_s_sleep(2); break; case 3: __builtin_amdgcn_s_sleep(3); break;
    case 4: __builtin_amdgcn_s_sleep(4); break; case 5: __builtin_amdgcn_s_sleep(5); break; case 6: __builtin_amdgcn_s_sleep(6); break;
    case 7: __builtin_amdgcn_s_sleep(7); break; case 8: __builtin_amdgcn_s_sleep(8); break; case 9: __builtin_amdgcn_s_sleep(9); break;
    case 10: __builtin_amdgcn_s_sleep(10); break; case 11: __builtin_amdgcn_s_sleep(11); break; case 12: __builtin_amdgcn_s_sleep(12); break;
    case 13: __builtin_amdgcn_s_sleep(13); break; case 14: __builtin_amdgcn_s_sleep(14); break; case 15: __builtin_amdgcn_s_sleep(15); break;
    default: break;
  }
}
#define SLEEP(n) do { if ((n) > 0) __builtin_amdgcn_s_sleep(n); } while (0)
#define USLEEP 3
#define SSLEEP 11
#define PSLEEP 2
#define PPRIO 0
#define LFIRST 1
#define WD 40
#define OSLEEP 12
#define QPRIO 1
#define KPRIO 2
#define GSLEEP 0
#define NSLEEP 0
#define UD 40
#define XD 0
#define RSLEEP 0
#define UPRIO 2
#define VD2 395
#define WDEPTH 4
#define ESLEEP 0
#define SD 58
#define KD 175
#define OD 0
#define KBACK 6
#define DDEPTH 6
#define C3 1
#define XOFF 0
#define XROT 2
#define WSOFF 0
#define T0 0
#define TB1 0
#define T0SLEEP 0
#define KN 2
#define RPF 1
#define LSLEEP 3
#define TSTOP 70
#define HSTOP 150
#define VEARLY 0ull
#define FPR 3
#define SPR 3
#define DPRIO 0
#define T1 0
#define T1SLEEP 0
#define QPRIO2 3
#define HW 2
#define RD 62
#define OPOS 1
#define OD2 0
#define NA 12
#define RD2 88
#define ORD1 ((unsigned __int128)0x321087654u << 36 | 0x321087654ull)
#define WDEPTH2 4
#define RQ NA
#define HREV 1
#define QPRIO0 3
#define VPS 1
#define RW 1
#define NPRIO 0
#define XSL 10
#define V2D 20
#define XF 0
#define N0 8
#define FORCESLOW 0
#define FORCEFB 0
#define CONV_ASM \
  "s_cmp_eq_u32 s89, 1\ns_cbranch_scc1 1f\ns_cmp_gt_u32 s89, 1\ns_cbranch_scc1 9f\nds_read_b64 v[124:125], v3 offset:576\n" \
  "ds_read_b64 v[126:127], v3 offset:584\nds_read_b64 v[120:121], v3 offset:64\nds_read_b64 v[122:123], v3 offset:72\n" \
  "ds_read_b64 v[116:117], v3 offset:640\nds_read_b64 v[118:119], v3 offset:648\nds_read_b64 v[112:113], v3 offset:128\n" \
  "ds_read_b64 v[114:115], v3 offset:136\nds_read_b64 v[108:109], v3 offset:704\nds_read_b64 v[110:111], v3 offset:712\n" \
  "ds_read_b64 v[104:105], v3 offset:192\nds_read_b64 v[106:107], v3 offset:200\nds_read_b64 v[100:101], v3 offset:768\n" \
  "ds_read_b64 v[102:103], v3 offset:776\nds_read_b64 v[96:97], v3 offset:256\nds_read_b64 v[98:99], v3 offset:264\n" \
  "ds_read_b64 v[92:93], v3 offset:832\nds_read_b64 v[94:95], v3 offset:840\nds_read_b64 v[88:89], v3 offset:320\n" \
  "ds_read_b64 v[90:91], v3 offset:328\nds_read_b64 v[84:85], v3 offset:896\nds_read_b64 v[86:87], v3 offset:904\n" \
  "ds_read_b64 v[80:81], v3 offset:384\nds_read_b64 v[82:83], v3 offset:392\nds_read_b64 v[76:77], v3 offset:960\n" \
  "ds_read_b64 v[78:79], v3 offset:968\nds_read_b64 v[72:73], v3 offset:448\nds_read_b64 v[74:75], v3 offset:456\n" \
  "ds_read_b64 v[68:69], v3 offset:1024\nds_read_b64 v[70:71], v3 offset:1032\nds_read_b64 v[64:65], v3 offset:512\n" \
  "ds_read_b64 v[66:67], v3 offset:520\nds_read_b64 v[60:61], v3 offset:0\nds_read_b64 v[62:63], v3 offset:8\nds_read_b64 v[56:57], v3 offset:1088\n" \
  "ds_read_b64 v[58:59], v3 offset:1096\ns_waitcnt lgkmcnt(0)\ns_barrier\nds_read_b128 v[36:39], v2 offset:0\nds_read_b128 v[40:43], v2 offset:64\n" \
  "ds_read_b128 v[44:47], v2 offset:128\nds_read_b128 v[48:51], v2 offset:192\ns_waitcnt lgkmcnt(3)\n" \
  "v_mfma_f32_16x16x32_f16 v[12:15], v[124:127], v[36:39], 0\ns_setprio 1\nds_read_b128 v[52:55], v2 offset:256\n" \
  "v_mfma_f32_16x16x32_f16 v[16:19], v[120:123], v[36:39], 0\ns_waitcnt lgkmcnt(3)\nv_mfma_f32_16x16x32_f16 v[12:15], v[116:119], v[40:43], v[12:15]\n" \
  "ds_read_b128 v[36:39], v2 offset:320\nv_mfma_f32_16x16x32_f16 v[16:19], v[112:115], v[40:43], v[16:19]\ns_waitcnt lgkmcnt(3)\n" \
  "v_mfma_f32_16x16x32_f16 v[12:15], v[108:111], v[44:47], v[12:15]\nds_read_b128 v[40:43], v2 offset:384\n" \
  "v_mfma_f32_16x16x32_f16 v[16:19], v[104:107], v[44:47], v[16:19]\ns_waitcnt lgkmcnt(3)\n" \
  "v_mfma_f32_16x16x32_f16 v[12:15], v[100:103], v[48:51], v[12:15]\nds_read_b128 v[44:47], v2 offset:448\n" \
  "v_mfma_f32_16x16x32_f16 v[16:19], v[96:99], v[48:51], v[16:19]\ns_waitcnt lgkmcnt(3)\n" \
  "v_mfma_f32_16x16x32_f16 v[12:15], v[92:95], v[52:55], v[12:15]\nv_mfma_f32_16x16x32_f16 v[16:19], v[88:91], v[52:55], v[16:19]\n" \
  "s_waitcnt lgkmcnt(2)\nv_mfma_f32_16x16x32_f16 v[12:15], v[84:87], v[36:39], v[12:15]\nds_read_b128 v[48:51], v2 offset:512\n" \
  "v_mfma_f32_16x16x32_f16 v[16:19], v[80:83], v[36:39], v[16:19]\ns_waitcnt lgkmcnt(2)\n" \
  "v_mfma_f32_16x16x32_f16 v[12:15], v[76:79], v[40:43], v[12:15]\nds_read_b128 v[52:55], v2 offset:576\n" \
  "v_mfma_f32_16x16x32_f16 v[16:19], v[72:75], v[40:43], v[16:19]\ns_waitcnt lgkmcnt(2)\n" \
  "v_mfma_f32_16x16x32_f16 v[12:15], v[68:71], v[44:47], v[12:15]\nds_read_b128 v[36:39], v2 offset:640\n" \
  "v_mfma_f32_16x16x32_f16 v[16:19], v[64:67], v[44:47], v[16:19]\nv_mfma_f32_16x16x32_f16 v[20:23], v[60:63], v[44:47], 0\n" \
  "ds_read_b128 v[40:43], v2 offset:704\nv_mfma_f32_16x16x32_f16 v[28:31], v[56:59], v[44:47], 0\ns_waitcnt lgkmcnt(3)\n" \
  "v_mfma_f32_16x16x32_f16 v[16:19], v[124:127], v[48:51], v[16:19]\nds_read_b128 v[56:59], v2 offset:768\nv_cvt_pkrtz_f16_f32 v32, v12, v13\n" \
  "v_mfma_f32_16x16x32_f16 v[20:23], v[120:123], v[48:51], v[20:23]\nv_cvt_pkrtz_f16_f32 v33, v14, v15\ns_waitcnt lgkmcnt(3)\n" \
  "v_mfma_f32_16x16x32_f16 v[16:19], v[116:119], v[52:55], v[16:19]\nds_read_b128 v[44:47], v2 offset:832\n" \
  "v_pk_add_u16 v32, v32, 4 op_sel_hi:[1,0] clamp\nv_mfma_f32_16x16x32_f16 v[20:23], v[112:115], v[52:55], v[20:23]\n" \
  "v_pk_add_u16 v33, v33, 4 op_sel_hi:[1,0] clamp\ns_waitcnt lgkmcnt(3)\nv_mfma_f32_16x16x32_f16 v[16:19], v[108:111], v[36:39], v[16:19]\n" \
  "ds_read_b128 v[48:51], v2 offset:896\nds_write_b64 v4, v[32:33] offset:0\nv_mfma_f32_16x16x32_f16 v[20:23], v[104:107], v[36:39], v[20:23]\n" \
  "s_waitcnt lgkmcnt(3)\nv_mfma_f32_16x16x32_f16 v[16:19], v[100:103], v[40:43], v[16:19]\nds_read_b128 v[52:55], v2 offset:960\n" \
  "v_mfma_f32_16x16x32_f16 v[20:23], v[96:99], v[40:43], v[20:23]\nv_mfma_f32_16x16x32_f16 v[16:19], v[92:95], v[56:59], v[16:19]\n" \
  "v_mfma_f32_16x16x32_f16 v[20:23], v[88:91], v[56:59], v[20:23]\nds_read_b128 v[36:39], v2 offset:1024\ns_waitcnt lgkmcnt(3)\n" \
  "v_mfma_f32_16x16x32_f16 v[16:19], v[84:87], v[44:47], v[16:19]\nv_mfma_f32_16x16x32_f16 v[20:23], v[80:83], v[44:47], v[20:23]\n" \
  "ds_read_b128 v[40:43], v2 offset:1088\nds_read_b64 v[56:57], v6 offset:256\nds_read_b64 v[58:59], v6 offset:264\n" \
  "v_mfma_f32_16x16x32_f16 v[16:19], v[76:79], v[48:51], v[16:19]\nv_mfma_f32_16x16x32_f16 v[20:23], v[72:75], v[48:51], v[20:23]\n" \
  "ds_read_b128 v[44:47], v2 offset:1152\ns_waitcnt lgkmcnt(4)\nv_mfma_f32_16x16x32_f16 v[16:19], v[68:71], v[52:55], v[16:19]\n" \
  "v_mfma_f32_16x16x32_f16 v[20:23], v[64:67], v[52:55], v[20:23]\nds_read_b128 v[48:51], v2 offset:1216\n" \
  "v_mfma_f32_16x16x32_f16 v[24:27], v[60:63], v[52:55], 0\nv_mfma_f32_16x16x32_f16 v[20:23], v[124:127], v[36:39], v[20:23]\n" \
  "ds_read_b128 v[52:55], v2 offset:1280\nv_mfma_f32_16x16x32_f16 v[24:27], v[120:123], v[36:39], v[24:27]\ns_waitcnt lgkmcnt(5)\n" \
  "v_mfma_f32_16x16x32_f16 v[20:23], v[116:119], v[40:43], v[20:23]\nds_read_b128 v[36:39], v2 offset:1344\nv_cvt_pkrtz_f16_f32 v32, v16, v17\n" \
  "v_mfma_f32_16x16x32_f16 v[24:27], v[112:115], v[40:43], v[24:27]\nv_cvt_pkrtz_f16_f32 v33, v18, v19\ns_waitcnt lgkmcnt(3)\n" \
  "v_mfma_f32_16x16x32_f16 v[20:23], v[108:111], v[44:47], v[20:23]\nds_read_b128 v[40:43], v2 offset:1408\n" \
  "v_pk_add_u16 v32, v32, 4 op_sel_hi:[1,0] clamp\nds_read_b32 v35, v9 offset:32\nv_mfma_f32_16x16x32_f16 v[24:27], v[104:107], v[44:47], v[24:27]\n" \
  "v_pk_add_u16 v33, v33, 4 op_sel_hi:[1,0] clamp\ns_waitcnt lgkmcnt(3)\nv_mfma_f32_16x16x32_f16 v[20:23], v[100:103], v[48:51], v[20:23]\n" \
  "ds_read_b128 v[44:47], v2 offset:1472\nds_write_b64 v4, v[32:33] offset:1024\nv_mfma_f32_16x16x32_f16 v[24:27], v[96:99], v[48:51], v[24:27]\n" \
  "v_mfma_f32_16x16x32_f16 v[20:23], v[92:95], v[52:55], v[20:23]\nv_mfma_f32_16x16x32_f16 v[24:27], v[88:91], v[52:55], v[24:27]\n" \
  "ds_read_b128 v[48:51], v2 offset:1536\ns_waitcnt lgkmcnt(3)\nv_readfirstlane_b32 s85, v35\ns_cmp_lt_u32 s85, 4\ns_cbranch_scc1 21f\n22:\n" \
  "v_mfma_f32_16x16x32_f16 v[20:23], v[84:87], v[36:39], v[20:23]\nv_mfma_f32_16x16x32_f16 v[24:27], v[80:83], v[36:39], v[24:27]\n" \
  "ds_read_b128 v[52:55], v2 offset:1600\nv_mfma_f32_16x16x32_f16 v[20:23], v[76:79], v[40:43], v[20:23]\n" \
  "v_mfma_f32_16x16x32_f16 v[24:27], v[72:75], v[40:43], v[24:27]\nds_read_b128 v[36:39], v2 offset:1664\ns_waitcnt lgkmcnt(4)\n" \
  "v_mfma_f32_16x16x32_f16 v[20:23], v[68:71], v[44:47], v[20:23]\nv_mfma_f32_16x16x32_f16 v[24:27], v[64:67], v[44:47], v[24:27]\n" \
  "ds_read_b128 v[40:43], v2 offset:1728\nv_mfma_f32_16x16x32_f16 v[28:31], v[60:63], v[44:47], v[28:31]\nds_read_b64_tr_b16 v[60:61], v5 offset:0\n" \
  "ds_read_b64_tr_b16 v[62:63], v5 offset:32\ns_waitcnt lgkmcnt(4)\nv_mfma_f32_16x16x32_f16 v[24:27], v[124:127], v[48:51], v[24:27]\n" \
  "v_mfma_f32_16x16x32_f16 v[28:31], v[120:123], v[48:51], v[28:31]\nds_read_b64_tr_b16 v[44:45], v5 offset:256\n" \
  "ds_read_b64_tr_b16 v[46:47], v5 offset:288\nv_mfma_f32_16x16x32_f16 v[24:27], v[116:119], v[52:55], v[24:27]\n" \
  "ds_read_b64 v[120:121], v6 offset:320\nds_read_b64 v[122:123], v6 offset:328\nv_mfma_f32_16x16x32_f16 v[28:31], v[112:115], v[52:55], v[28:31]\n" \
  "ds_read_b128 v[48:51], v2 offset:1792\ns_waitcnt lgkmcnt(7)\nv_mfma_f32_16x16x32_f16 v[24:27], v[108:111], v[36:39], v[24:27]\n" \
  "ds_read_b128 v[112:115], v2 offset:1856\nv_mfma_f32_16x16x32_f16 v[28:31], v[104:107], v[36:39], v[28:31]\nds_read_b64 v[52:53], v6 offset:128\n" \
  "ds_read_b64 v[54:55], v6 offset:136\nv_mfma_f32_16x16x32_f16 v[24:27], v[100:103], v[40:43], v[24:27]\nds_read_b128 v[104:107], v2 offset:1920\n" \
  "v_mfma_f32_16x16x32_f16 v[28:31], v[96:99], v[40:43], v[28:31]\nds_read_b64 v[36:37], v6 offset:192\nds_read_b64 v[38:39], v6 offset:200\n" \
  "s_waitcnt lgkmcnt(9)\nv_mfma_f32_16x16x32_f16 v[20:23], v[60:63], v[56:59], v[20:23]\nds_read_b128 v[96:99], v2 offset:1984\ns_waitcnt lgkmcnt(7)\n" \
  "v_mfma_f32_16x16x32_f16 v[20:23], v[44:47], v[120:123], v[20:23]\nv_mfma_f32_16x16x32_f16 v[24:27], v[92:95], v[48:51], v[24:27]\n" \
  "ds_read_b128 v[40:43], v2 offset:2048\nv_mfma_f32_16x16x32_f16 v[28:31], v[88:91], v[48:51], v[28:31]\nds_read_b64_tr_b16 v[88:89], v5 offset:512\n" \
  "ds_read_b64_tr_b16 v[90:91], v5 offset:544\ns_waitcnt lgkmcnt(6)\nv_mfma_f32_16x16x32_f16 v[24:27], v[84:87], v[112:115], v[24:27]\n" \
  "v_mfma_f32_16x16x32_f16 v[28:31], v[80:83], v[112:115], v[28:31]\nv_cvt_pkrtz_f16_f32 v32, v20, v21\nv_cvt_pkrtz_f16_f32 v33, v22, v23\n" \
  "v_mfma_f32_16x16x32_f16 v[24:27], v[76:79], v[104:107], v[24:27]\nds_read_b64_tr_b16 v[48:49], v5 offset:768\n" \
  "ds_read_b64_tr_b16 v[50:51], v5 offset:800\nv_mfma_f32_16x16x32_f16 v[28:31], v[72:75], v[104:107], v[28:31]\n" \
  "ds_read_b128 v[80:83], v2 offset:2112\ns_waitcnt lgkmcnt(5)\nv_mfma_f32_16x16x32_f16 v[24:27], v[68:71], v[96:99], v[24:27]\n" \
  "ds_read_b128 v[112:115], v2 offset:2176\nv_pk_add_u16 v32, v32, 4 op_sel_hi:[1,0] clamp\n" \
  "v_mfma_f32_16x16x32_f16 v[28:31], v[64:67], v[96:99], v[28:31]\nds_read_b128 v[72:75], v2 offset:2240\n" \
  "v_pk_add_u16 v33, v33, 4 op_sel_hi:[1,0] clamp\nv_mfma_f32_16x16x32_f16 v[28:31], v[124:127], v[40:43], v[28:31]\n" \
  "ds_read_b128 v[104:107], v2 offset:2304\nds_write_b64 v4, v[32:33] offset:2048\nv_mfma_f32_16x16x32_f16 v[24:27], v[60:63], v[52:55], v[24:27]\n" \
  "ds_read_b128 v[64:67], v2 offset:2368\nds_read_b64 v[96:97], v6 offset:0\nds_read_b64 v[98:99], v6 offset:8\n" \
  "v_mfma_f32_16x16x32_f16 v[24:27], v[44:47], v[36:39], v[24:27]\nds_read_b128 v[124:127], v2 offset:2432\ns_waitcnt lgkmcnt(9)\n" \
  "v_mfma_f32_16x16x32_f16 v[24:27], v[88:91], v[56:59], v[24:27]\nds_read_b128 v[40:43], v2 offset:2496\n" \
  "v_mfma_f32_16x16x32_f16 v[24:27], v[48:51], v[120:123], v[24:27]\ns_waitcnt lgkmcnt(8)\n" \
  "v_mfma_f32_16x16x32_f16 v[28:31], v[116:119], v[80:83], v[28:31]\nds_read_b64 v[116:117], v6 offset:64\nds_read_b64 v[118:119], v6 offset:72\n" \
  "v_mfma_f32_16x16x32_f16 v[28:31], v[108:111], v[112:115], v[28:31]\nds_read_b64_tr_b16 v[80:81], v5 offset:1024\n" \
  "ds_read_b64_tr_b16 v[82:83], v5 offset:1056\ns_waitcnt lgkmcnt(10)\nv_mfma_f32_16x16x32_f16 v[28:31], v[100:103], v[72:75], v[28:31]\n" \
  "v_cvt_pkrtz_f16_f32 v32, v24, v25\nv_cvt_pkrtz_f16_f32 v33, v26, v27\nv_mfma_f32_16x16x32_f16 v[28:31], v[92:95], v[104:107], v[28:31]\n" \
  "v_pk_add_u16 v32, v32, 4 op_sel_hi:[1,0] clamp\ns_waitcnt lgkmcnt(5)\nv_mfma_f32_16x16x32_f16 v[28:31], v[84:87], v[64:67], v[28:31]\n" \
  "ds_read_b64_tr_b16 v[108:109], v5 offset:1280\nds_read_b64_tr_b16 v[110:111], v5 offset:1312\n" \
  "v_mfma_f32_16x16x32_f16 v[28:31], v[76:79], v[124:127], v[28:31]\nds_read_b64_tr_b16 v[112:113], v7 offset:0\n" \
  "ds_read_b64_tr_b16 v[114:115], v7 offset:64\ns_waitcnt lgkmcnt(6)\nv_mfma_f32_16x16x32_f16 v[28:31], v[68:71], v[40:43], v[28:31]\n" \
  "ds_read2_b32 v[100:101], v8 offset0:48 offset1:49\nds_read2_b32 v[102:103], v8 offset0:50 offset1:51\n" \
  "v_mfma_f32_16x16x32_f16 v[28:31], v[60:63], v[96:99], v[28:31]\nv_pk_add_u16 v33, v33, 4 op_sel_hi:[1,0] clamp\n" \
  "ds_write_b64 v4, v[32:33] offset:3072\nv_mfma_f32_16x16x32_f16 v[28:31], v[44:47], v[116:119], v[28:31]\n" \
  "v_mfma_f32_16x16x32_f16 v[28:31], v[88:91], v[52:55], v[28:31]\nv_mfma_f32_16x16x32_f16 v[28:31], v[48:51], v[36:39], v[28:31]\n" \
  "s_waitcnt lgkmcnt(5)\nv_mfma_f32_16x16x32_f16 v[28:31], v[80:83], v[56:59], v[28:31]\n" \
  "v_mfma_f32_16x16x32_f16 v[28:31], v[108:111], v[120:123], v[28:31]\ns_waitcnt lgkmcnt(1)\n" \
  "v_mfma_f32_16x16x32_f16 v[28:31], v[112:115], v[100:103], v[28:31]\ns_nop 7\nv_cvt_pkrtz_f16_f32 v32, v28, v29\nv_cvt_pkrtz_f16_f32 v33, v30, v31\n" \
  "v_pk_add_u16 v32, v32, 4 op_sel_hi:[1,0] clamp\nv_pk_add_u16 v33, v33, 4 op_sel_hi:[1,0] clamp\nds_write_b64 v4, v[32:33] offset:4096\n" \
  "s_setprio 0\ns_setpc_b64 s[90:91]\n21:\ns_movk_i32 s84, 0x4000\n23:\nds_read_b32 v35, v9 offset:32\ns_sub_u32 s84, s84, 1\ns_cbranch_scc1 22b\n" \
  "s_waitcnt lgkmcnt(0)\nv_readfirstlane_b32 s85, v35\ns_cmp_lt_u32 s85, 4\ns_cbranch_scc1 23b\ns_branch 22b\n1:\n" \
  "ds_read_b64 v[124:125], v3 offset:1088\nds_read_b64 v[126:127], v3 offset:1096\nds_read_b64 v[120:121], v3 offset:0\n" \
  "ds_read_b64 v[122:123], v3 offset:8\nds_read_b64 v[116:117], v3 offset:64\nds_read_b64 v[118:119], v3 offset:72\n" \
  "ds_read_b64 v[112:113], v3 offset:128\nds_read_b64 v[114:115], v3 offset:136\nds_read_b64 v[108:109], v3 offset:192\n" \
  "ds_read_b64 v[110:111], v3 offset:200\nds_read_b64 v[104:105], v3 offset:256\nds_read_b64 v[106:107], v3 offset:264\n" \
  "ds_read_b64 v[100:101], v3 offset:320\nds_read_b64 v[102:103], v3 offset:328\nds_read_b64 v[96:97], v3 offset:384\n" \
  "ds_read_b64 v[98:99], v3 offset:392\nds_read_b64 v[92:93], v3 offset:448\nds_read_b64 v[94:95], v3 offset:456\n" \
  "ds_read_b64 v[88:89], v3 offset:512\nds_read_b64 v[90:91], v3 offset:520\nds_read_b64 v[84:85], v3 offset:576\n" \
  "ds_read_b64 v[86:87], v3 offset:584\nds_read_b64 v[80:81], v3 offset:640\nds_read_b64 v[82:83], v3 offset:648\n" \
  "ds_read_b64 v[76:77], v3 offset:704\nds_read_b64 v[78:79], v3 offset:712\nds_read_b64 v[72:73], v3 offset:768\n" \
  "ds_read_b64 v[74:75], v3 offset:776\nds_read_b64 v[68:69], v3 offset:832\nds_read_b64 v[70:71], v3 offset:840\n" \
  "ds_read_b64 v[64:65], v3 offset:896\nds_read_b64 v[66:67], v3 offset:904\nds_read_b64 v[60:61], v3 offset:960\n" \
  "ds_read_b64 v[62:63], v3 offset:968\nds_read_b64 v[56:57], v3 offset:1024\nds_read_b64 v[58:59], v3 offset:1032\n" \
  "ds_read_b64 v[52:53], v6 offset:0\nds_read_b64 v[54:55], v6 offset:8\nds_read_b64 v[48:49], v6 offset:64\nds_read_b64 v[50:51], v6 offset:72\n" \
  "s_waitcnt lgkmcnt(0)\ns_barrier\nds_read_b128 v[28:31], v2 offset:960\nds_read_b128 v[32:35], v2 offset:1472\n" \
  "ds_read_b128 v[36:39], v2 offset:1984\nds_read_b128 v[40:43], v2 offset:2048\nds_read_b128 v[44:47], v2 offset:2112\ns_waitcnt lgkmcnt(3)\n" \
  "v_mfma_f32_16x16x32_f16 v[12:15], v[124:127], v[28:31], 0\ns_setprio 1\nds_read_b128 v[28:31], v2 offset:2176\n" \
  "v_mfma_f32_16x16x32_f16 v[16:19], v[124:127], v[32:35], 0\nds_read_b128 v[32:35], v2 offset:2240\ns_waitcnt lgkmcnt(3)\n" \
  "v_mfma_f32_16x16x32_f16 v[12:15], v[120:123], v[36:39], v[12:15]\nv_mfma_f32_16x16x32_f16 v[20:23], v[124:127], v[36:39], 0\n" \
  "ds_read_b128 v[124:127], v2 offset:2304\nds_read_b128 v[36:39], v2 offset:2368\nv_mfma_f32_16x16x32_f16 v[12:15], v[116:119], v[40:43], v[12:15]\n" \
  "ds_read_b128 v[40:43], v2 offset:2432\ns_waitcnt lgkmcnt(4)\nv_mfma_f32_16x16x32_f16 v[12:15], v[112:115], v[44:47], v[12:15]\n" \
  "ds_read_b128 v[44:47], v2 offset:2496\nv_mfma_f32_16x16x32_f16 v[12:15], v[108:111], v[28:31], v[12:15]\nds_read_b128 v[28:31], v2 offset:2560\n" \
  "s_waitcnt lgkmcnt(4)\nv_mfma_f32_16x16x32_f16 v[12:15], v[104:107], v[32:35], v[12:15]\nds_read_b128 v[32:35], v2 offset:2624\n" \
  "v_mfma_f32_16x16x32_f16 v[12:15], v[100:103], v[124:127], v[12:15]\ns_waitcnt lgkmcnt(3)\n" \
  "v_mfma_f32_16x16x32_f16 v[12:15], v[96:99], v[36:39], v[12:15]\nds_read_b128 v[124:127], v2 offset:2688\n" \
  "v_mfma_f32_16x16x32_f16 v[12:15], v[92:95], v[40:43], v[12:15]\ns_waitcnt lgkmcnt(3)\n" \
  "v_mfma_f32_16x16x32_f16 v[12:15], v[88:91], v[44:47], v[12:15]\nds_read_b128 v[36:39], v2 offset:2752\n" \
  "v_mfma_f32_16x16x32_f16 v[16:19], v[120:123], v[44:47], v[16:19]\ns_waitcnt lgkmcnt(3)\n" \
  "v_mfma_f32_16x16x32_f16 v[12:15], v[84:87], v[28:31], v[12:15]\nds_read_b128 v[40:43], v2 offset:2816\n" \
  "v_mfma_f32_16x16x32_f16 v[16:19], v[116:119], v[28:31], v[16:19]\ns_waitcnt lgkmcnt(3)\n" \
  "v_mfma_f32_16x16x32_f16 v[12:15], v[80:83], v[32:35], v[12:15]\nds_read_b128 v[44:47], v2 offset:2880\n" \
  "v_mfma_f32_16x16x32_f16 v[16:19], v[112:115], v[32:35], v[16:19]\ns_waitcnt lgkmcnt(3)\n" \
  "v_mfma_f32_16x16x32_f16 v[12:15], v[76:79], v[124:127], v[12:15]\nds_read_b128 v[28:31], v2 offset:2944\n" \
  "v_mfma_f32_16x16x32_f16 v[16:19], v[108:111], v[124:127], v[16:19]\ns_waitcnt lgkmcnt(3)\n" \
  "v_mfma_f32_16x16x32_f16 v[12:15], v[72:75], v[36:39], v[12:15]\nds_read_b128 v[32:35], v2 offset:3008\n" \
  "v_mfma_f32_16x16x32_f16 v[16:19], v[104:107], v[36:39], v[16:19]\ns_waitcnt lgkmcnt(3)\n" \
  "v_mfma_f32_16x16x32_f16 v[12:15], v[68:71], v[40:43], v[12:15]\nv_mfma_f32_16x16x32_f16 v[16:19], v[100:103], v[40:43], v[16:19]\n" \
  "ds_read_b128 v[124:127], v2 offset:3072\ns_waitcnt lgkmcnt(3)\nv_mfma_f32_16x16x32_f16 v[12:15], v[64:67], v[44:47], v[12:15]\n" \
  "v_mfma_f32_16x16x32_f16 v[16:19], v[96:99], v[44:47], v[16:19]\nds_read_b128 v[36:39], v2 offset:3136\ns_waitcnt lgkmcnt(3)\n" \
  "v_mfma_f32_16x16x32_f16 v[12:15], v[60:63], v[28:31], v[12:15]\nds_read_b64 v[40:41], v6 offset:128\nds_read_b64 v[42:43], v6 offset:136\n" \
  "v_mfma_f32_16x16x32_f16 v[16:19], v[92:95], v[28:31], v[16:19]\nds_read_b128 v[44:47], v2 offset:3200\ns_waitcnt lgkmcnt(4)\n" \
  "v_mfma_f32_16x16x32_f16 v[12:15], v[56:59], v[32:35], v[12:15]\nv_mfma_f32_16x16x32_f16 v[16:19], v[88:91], v[32:35], v[16:19]\n" \
  "ds_read_b128 v[28:31], v2 offset:3264\nv_mfma_f32_16x16x32_f16 v[20:23], v[120:123], v[32:35], v[20:23]\nds_read_b64 v[120:121], v6 offset:192\n" \
  "ds_read_b64 v[122:123], v6 offset:200\nv_mfma_f32_16x16x32_f16 v[16:19], v[84:87], v[124:127], v[16:19]\nds_read_b128 v[32:35], v2 offset:3328\n" \
  "v_mfma_f32_16x16x32_f16 v[20:23], v[116:119], v[124:127], v[20:23]\nds_read_b64 v[116:117], v6 offset:256\nds_read_b64 v[118:119], v6 offset:264\n" \
  "s_waitcnt lgkmcnt(6)\nv_mfma_f32_16x16x32_f16 v[16:19], v[80:83], v[36:39], v[16:19]\nds_read_b128 v[124:127], v2 offset:3392\n" \
  "v_mfma_f32_16x16x32_f16 v[20:23], v[112:115], v[36:39], v[20:23]\nds_read_b64 v[112:113], v6 offset:320\nds_read_b64 v[114:115], v6 offset:328\n" \
  "v_mfma_f32_16x16x32_f16 v[16:19], v[76:79], v[44:47], v[16:19]\nds_read_b128 v[36:39], v2 offset:3456\n" \
  "v_mfma_f32_16x16x32_f16 v[20:23], v[108:111], v[44:47], v[20:23]\ns_waitcnt lgkmcnt(6)\n" \
  "v_mfma_f32_16x16x32_f16 v[16:19], v[72:75], v[28:31], v[16:19]\nds_read_b128 v[108:111], v2 offset:3520\n" \
  "v_mfma_f32_16x16x32_f16 v[20:23], v[104:107], v[28:31], v[20:23]\nv_mfma_f32_16x16x32_f16 v[16:19], v[68:71], v[32:35], v[16:19]\n" \
  "ds_read_b128 v[44:47], v2 offset:3584\nv_mfma_f32_16x16x32_f16 v[20:23], v[100:103], v[32:35], v[20:23]\nds_read_b128 v[104:107], v2 offset:3648\n" \
  "ds_read_b32 v27, v9 offset:32\ns_waitcnt lgkmcnt(4)\nv_mfma_f32_16x16x32_f16 v[16:19], v[64:67], v[124:127], v[16:19]\n" \
  "ds_read_b128 v[28:31], v2 offset:3712\nv_mfma_f32_16x16x32_f16 v[20:23], v[96:99], v[124:127], v[20:23]\nds_read_b128 v[100:103], v2 offset:3776\n" \
  "ds_read2_b32 v[32:33], v8 offset0:32 offset1:33\nds_read2_b32 v[34:35], v8 offset0:34 offset1:35\n" \
  "v_mfma_f32_16x16x32_f16 v[16:19], v[60:63], v[36:39], v[16:19]\nds_read_b128 v[96:99], v2 offset:3840\n" \
  "v_mfma_f32_16x16x32_f16 v[20:23], v[92:95], v[36:39], v[20:23]\nds_read_b128 v[124:127], v2 offset:3904\ns_waitcnt lgkmcnt(8)\n" \
  "v_mfma_f32_16x16x32_f16 v[16:19], v[56:59], v[108:111], v[16:19]\nds_read_b128 v[92:95], v2 offset:3968\ns_waitcnt lgkmcnt(7)\n" \
  "v_readfirstlane_b32 s85, v27\ns_cmp_lt_u32 s85, 4\ns_cbranch_scc1 31f\n32:\nv_mfma_f32_16x16x32_f16 v[20:23], v[88:91], v[108:111], v[20:23]\n" \
  "ds_read_b128 v[36:39], v2 offset:4032\nds_read_b64_tr_b16 v[88:89], v5 offset:512\nds_read_b64_tr_b16 v[90:91], v5 offset:544\n" \
  "v_mfma_f32_16x16x32_f16 v[20:23], v[84:87], v[44:47], v[20:23]\nds_read_b64_tr_b16 v[108:109], v5 offset:768\n" \
  "ds_read_b64_tr_b16 v[110:111], v5 offset:800\nv_mfma_f32_16x16x32_f16 v[20:23], v[80:83], v[104:107], v[20:23]\n" \
  "ds_read_b64_tr_b16 v[84:85], v5 offset:1024\nds_read_b64_tr_b16 v[86:87], v5 offset:1056\ns_waitcnt lgkmcnt(12)\n" \
  "v_mfma_f32_16x16x32_f16 v[20:23], v[76:79], v[28:31], v[20:23]\nds_read_b64_tr_b16 v[44:45], v5 offset:1280\n" \
  "ds_read_b64_tr_b16 v[46:47], v5 offset:1312\nv_mfma_f32_16x16x32_f16 v[20:23], v[72:75], v[100:103], v[20:23]\ns_waitcnt lgkmcnt(10)\n" \
  "v_mfma_f32_16x16x32_f16 v[20:23], v[68:71], v[96:99], v[20:23]\nds_read_b64_tr_b16 v[80:81], v5 offset:1536\n" \
  "ds_read_b64_tr_b16 v[82:83], v5 offset:1568\nv_mfma_f32_16x16x32_f16 v[20:23], v[64:67], v[124:127], v[20:23]\n" \
  "ds_read_b64_tr_b16 v[104:105], v5 offset:1792\nds_read_b64_tr_b16 v[106:107], v5 offset:1824\ns_waitcnt lgkmcnt(12)\n" \
  "v_mfma_f32_16x16x32_f16 v[20:23], v[60:63], v[92:95], v[20:23]\nds_read_b64_tr_b16 v[76:77], v5 offset:2048\n" \
  "ds_read_b64_tr_b16 v[78:79], v5 offset:2080\nv_mfma_f32_16x16x32_f16 v[20:23], v[56:59], v[36:39], v[20:23]\ns_waitcnt lgkmcnt(10)\n" \
  "v_mfma_f32_16x16x32_f16 v[12:15], v[88:91], v[52:55], v[12:15]\nds_read_b64_tr_b16 v[28:29], v5 offset:2304\n" \
  "ds_read_b64_tr_b16 v[30:31], v5 offset:2336\nv_mfma_f32_16x16x32_f16 v[12:15], v[108:111], v[48:51], v[12:15]\n" \
  "ds_read_b64_tr_b16 v[72:73], v5 offset:2560\nds_read_b64_tr_b16 v[74:75], v5 offset:2592\ns_waitcnt lgkmcnt(10)\n" \
  "v_mfma_f32_16x16x32_f16 v[12:15], v[84:87], v[40:43], v[12:15]\nds_read_b64_tr_b16 v[100:101], v5 offset:2816\n" \
  "ds_read_b64_tr_b16 v[102:103], v5 offset:2848\nv_mfma_f32_16x16x32_f16 v[16:19], v[84:87], v[52:55], v[16:19]\n" \
  "ds_read_b64_tr_b16 v[68:69], v7 offset:0\nds_read_b64_tr_b16 v[70:71], v7 offset:64\n" \
  "v_mfma_f32_16x16x32_f16 v[12:15], v[44:47], v[120:123], v[12:15]\nv_mfma_f32_16x16x32_f16 v[16:19], v[44:47], v[48:51], v[16:19]\n" \
  "s_waitcnt lgkmcnt(10)\nv_mfma_f32_16x16x32_f16 v[12:15], v[80:83], v[116:119], v[12:15]\nds_read2_b32 v[96:97], v8 offset0:16 offset1:17\n" \
  "ds_read2_b32 v[98:99], v8 offset0:18 offset1:19\nv_mfma_f32_16x16x32_f16 v[16:19], v[80:83], v[40:43], v[16:19]\n" \
  "ds_read2_b32 v[64:65], v8 offset0:0 offset1:1\nds_read2_b32 v[66:67], v8 offset0:2 offset1:3\n" \
  "v_mfma_f32_16x16x32_f16 v[20:23], v[80:83], v[52:55], v[20:23]\nv_mfma_f32_16x16x32_f16 v[12:15], v[104:107], v[112:115], v[12:15]\n" \
  "v_mfma_f32_16x16x32_f16 v[16:19], v[104:107], v[120:123], v[16:19]\nv_mfma_f32_16x16x32_f16 v[20:23], v[104:107], v[48:51], v[20:23]\n" \
  "s_waitcnt lgkmcnt(10)\nv_mfma_f32_16x16x32_f16 v[16:19], v[76:79], v[116:119], v[16:19]\nds_read_b64_tr_b16 v[124:125], v7 offset:512\n" \
  "ds_read_b64_tr_b16 v[126:127], v7 offset:576\nv_mfma_f32_16x16x32_f16 v[20:23], v[76:79], v[40:43], v[20:23]\n" \
  "ds_read2_b32 v[60:61], v8 offset0:48 offset1:49\nds_read2_b32 v[62:63], v8 offset0:50 offset1:51\n" \
  "v_mfma_f32_16x16x32_f16 v[16:19], v[28:31], v[112:115], v[16:19]\nv_mfma_f32_16x16x32_f16 v[20:23], v[28:31], v[120:123], v[20:23]\n" \
  "s_waitcnt lgkmcnt(10)\nv_mfma_f32_16x16x32_f16 v[20:23], v[72:75], v[116:119], v[20:23]\nds_read_b64_tr_b16 v[92:93], v7 offset:1024\n" \
  "ds_read_b64_tr_b16 v[94:95], v7 offset:1088\nv_mfma_f32_16x16x32_f16 v[20:23], v[100:103], v[112:115], v[20:23]\n" \
  "ds_read_b64_tr_b16 v[56:57], v7 offset:1536\nds_read_b64_tr_b16 v[58:59], v7 offset:1600\ns_waitcnt lgkmcnt(10)\n" \
  "v_mfma_f32_16x16x32_f16 v[12:15], v[68:71], v[32:35], v[12:15]\nv_mfma_f32_16x16x32_f16 v[16:19], v[68:71], v[96:99], v[16:19]\n" \
  "s_waitcnt lgkmcnt(6)\nv_mfma_f32_16x16x32_f16 v[20:23], v[68:71], v[64:67], v[20:23]\ns_waitcnt lgkmcnt(2)\n" \
  "v_mfma_f32_16x16x32_f16 v[12:15], v[124:127], v[60:63], v[12:15]\nv_mfma_f32_16x16x32_f16 v[16:19], v[124:127], v[32:35], v[16:19]\n" \
  "v_mfma_f32_16x16x32_f16 v[20:23], v[124:127], v[96:99], v[20:23]\nv_mfma_f32_16x16x32_f16 v[16:19], v[92:95], v[60:63], v[16:19]\n" \
  "v_mfma_f32_16x16x32_f16 v[20:23], v[92:95], v[32:35], v[20:23]\ns_waitcnt lgkmcnt(0)\n" \
  "v_mfma_f32_16x16x32_f16 v[20:23], v[56:59], v[60:63], v[20:23]\ns_nop 1\nv_cvt_pkrtz_f16_f32 v24, v12, v13\nv_cvt_pkrtz_f16_f32 v25, v14, v15\n" \
  "v_pk_add_u16 v24, v24, 4 op_sel_hi:[1,0] clamp\nv_pk_add_u16 v25, v25, 4 op_sel_hi:[1,0] clamp\nds_write_b64 v4, v[24:25] offset:5120\n" \
  "v_cvt_pkrtz_f16_f32 v24, v16, v17\nv_cvt_pkrtz_f16_f32 v25, v18, v19\nv_pk_add_u16 v24, v24, 4 op_sel_hi:[1,0] clamp\n" \
  "v_pk_add_u16 v25, v25, 4 op_sel_hi:[1,0] clamp\nds_write_b64 v4, v[24:25] offset:6144\nv_cvt_pkrtz_f16_f32 v24, v20, v21\n" \
  "v_cvt_pkrtz_f16_f32 v25, v22, v23\nv_pk_add_u16 v24, v24, 4 op_sel_hi:[1,0] clamp\nv_pk_add_u16 v25, v25, 4 op_sel_hi:[1,0] clamp\n" \
  "ds_write_b64 v4, v[24:25] offset:7168\ns_setprio 0\ns_setpc_b64 s[90:91]\n31:\ns_movk_i32 s84, 0x4000\n33:\nds_read_b32 v27, v9 offset:32\n" \
  "s_sub_u32 s84, s84, 1\ns_cbranch_scc1 32b\ns_waitcnt lgkmcnt(0)\nv_readfirstlane_b32 s85, v27\ns_cmp_lt_u32 s85, 4\ns_cbranch_scc1 33b\n" \
  "s_branch 32b\n9:\ns_cmp_eq_u32 s89, 3\ns_cbranch_scc1 3f\ns_cmp_eq_u32 s89, 4\ns_cbranch_scc1 4f\ns_cmp_eq_u32 s89, 5\ns_cbranch_scc1 5f\n" \
  "ds_read_b64 v[124:125], v3 offset:576\nds_read_b64 v[126:127], v3 offset:584\nds_read_b64 v[120:121], v3 offset:64\n" \
  "ds_read_b64 v[122:123], v3 offset:72\nds_read_b64 v[116:117], v3 offset:640\nds_read_b64 v[118:119], v3 offset:648\n" \
  "ds_read_b64 v[112:113], v3 offset:128\nds_read_b64 v[114:115], v3 offset:136\nds_read_b64 v[108:109], v3 offset:704\n" \
  "ds_read_b64 v[110:111], v3 offset:712\nds_read_b64 v[104:105], v3 offset:192\nds_read_b64 v[106:107], v3 offset:200\n" \
  "ds_read_b64 v[100:101], v3 offset:768\nds_read_b64 v[102:103], v3 offset:776\nds_read_b64 v[96:97], v3 offset:256\n" \
  "ds_read_b64 v[98:99], v3 offset:264\nds_read_b64 v[92:93], v3 offset:832\nds_read_b64 v[94:95], v3 offset:840\n" \
  "ds_read_b64 v[88:89], v3 offset:320\nds_read_b64 v[90:91], v3 offset:328\nds_read_b64 v[84:85], v3 offset:896\n" \
  "ds_read_b64 v[86:87], v3 offset:904\nds_read_b64 v[80:81], v3 offset:384\nds_read_b64 v[82:83], v3 offset:392\n" \
  "ds_read_b64 v[76:77], v3 offset:960\nds_read_b64 v[78:79], v3 offset:968\nds_read_b64 v[72:73], v3 offset:448\n" \
  "ds_read_b64 v[74:75], v3 offset:456\nds_read_b64 v[68:69], v3 offset:1024\nds_read_b64 v[70:71], v3 offset:1032\n" \
  "ds_read_b64 v[64:65], v3 offset:512\nds_read_b64 v[66:67], v3 offset:520\ns_waitcnt lgkmcnt(0)\ns_barrier\nds_read_b128 v[24:27], v2 offset:0\n" \
  "ds_read_b128 v[28:31], v2 offset:64\nds_read_b128 v[32:35], v2 offset:128\nds_read_b128 v[36:39], v2 offset:192\ns_waitcnt lgkmcnt(3)\n" \
  "v_mfma_f32_16x16x32_f16 v[12:15], v[124:127], v[24:27], 0\ns_setprio 1\nds_read_b128 v[40:43], v2 offset:256\n" \
  "v_mfma_f32_16x16x32_f16 v[16:19], v[120:123], v[24:27], 0\ns_waitcnt lgkmcnt(3)\nv_mfma_f32_16x16x32_f16 v[12:15], v[116:119], v[28:31], v[12:15]\n" \
  "ds_read_b128 v[44:47], v2 offset:320\nv_mfma_f32_16x16x32_f16 v[16:19], v[112:115], v[28:31], v[16:19]\ns_waitcnt lgkmcnt(3)\n" \
  "v_mfma_f32_16x16x32_f16 v[12:15], v[108:111], v[32:35], v[12:15]\nds_read_b128 v[48:51], v2 offset:384\n" \
  "v_mfma_f32_16x16x32_f16 v[16:19], v[104:107], v[32:35], v[16:19]\ns_waitcnt lgkmcnt(3)\n" \
  "v_mfma_f32_16x16x32_f16 v[12:15], v[100:103], v[36:39], v[12:15]\nds_read_b128 v[52:55], v2 offset:448\n" \
  "v_mfma_f32_16x16x32_f16 v[16:19], v[96:99], v[36:39], v[16:19]\ns_waitcnt lgkmcnt(3)\n" \
  "v_mfma_f32_16x16x32_f16 v[12:15], v[92:95], v[40:43], v[12:15]\nds_read_b128 v[56:59], v2 offset:512\n" \
  "v_mfma_f32_16x16x32_f16 v[16:19], v[88:91], v[40:43], v[16:19]\nds_read_b128 v[60:63], v2 offset:576\ns_waitcnt lgkmcnt(3)\n" \
  "v_mfma_f32_16x16x32_f16 v[12:15], v[84:87], v[44:47], v[12:15]\nds_read_b128 v[120:123], v2 offset:640\n" \
  "v_mfma_f32_16x16x32_f16 v[16:19], v[80:83], v[44:47], v[16:19]\nds_read_b128 v[24:27], v2 offset:704\n" \
  "v_mfma_f32_16x16x32_f16 v[12:15], v[76:79], v[48:51], v[12:15]\nds_read_b128 v[112:115], v2 offset:768\n" \
  "v_mfma_f32_16x16x32_f16 v[16:19], v[72:75], v[48:51], v[16:19]\nds_read_b128 v[28:31], v2 offset:832\ns_waitcnt lgkmcnt(5)\n" \
  "v_mfma_f32_16x16x32_f16 v[12:15], v[68:71], v[52:55], v[12:15]\nds_read_b128 v[104:107], v2 offset:896\n" \
  "v_mfma_f32_16x16x32_f16 v[16:19], v[64:67], v[52:55], v[16:19]\nds_read_b128 v[32:35], v2 offset:960\n" \
  "v_mfma_f32_16x16x32_f16 v[16:19], v[124:127], v[56:59], v[16:19]\ns_waitcnt lgkmcnt(5)\n" \
  "v_mfma_f32_16x16x32_f16 v[16:19], v[116:119], v[60:63], v[16:19]\nv_mfma_f32_16x16x32_f16 v[16:19], v[108:111], v[120:123], v[16:19]\n" \
  "s_waitcnt lgkmcnt(3)\nv_mfma_f32_16x16x32_f16 v[16:19], v[100:103], v[24:27], v[16:19]\nv_cvt_pkrtz_f16_f32 v20, v12, v13\n" \
  "v_cvt_pkrtz_f16_f32 v21, v14, v15\nv_mfma_f32_16x16x32_f16 v[16:19], v[92:95], v[112:115], v[16:19]\n" \
  "v_pk_add_u16 v20, v20, 4 op_sel_hi:[1,0] clamp\ns_waitcnt lgkmcnt(2)\nv_mfma_f32_16x16x32_f16 v[16:19], v[84:87], v[28:31], v[16:19]\n" \
  "v_pk_add_u16 v21, v21, 4 op_sel_hi:[1,0] clamp\ns_waitcnt lgkmcnt(1)\nv_mfma_f32_16x16x32_f16 v[16:19], v[76:79], v[104:107], v[16:19]\n" \
  "ds_write_b64 v4, v[20:21] offset:0\ns_waitcnt lgkmcnt(1)\nv_mfma_f32_16x16x32_f16 v[16:19], v[68:71], v[32:35], v[16:19]\ns_nop 7\n" \
  "v_cvt_pkrtz_f16_f32 v20, v16, v17\nv_cvt_pkrtz_f16_f32 v21, v18, v19\nv_pk_add_u16 v20, v20, 4 op_sel_hi:[1,0] clamp\n" \
  "v_pk_add_u16 v21, v21, 4 op_sel_hi:[1,0] clamp\nds_write_b64 v4, v[20:21] offset:1024\ns_setprio 0\ns_setpc_b64 s[90:91]\n3:\n" \
  "ds_read_b64 v[124:125], v3 offset:0\nds_read_b64 v[126:127], v3 offset:8\nds_read_b64 v[120:121], v3 offset:64\n" \
  "ds_read_b64 v[122:123], v3 offset:72\nds_read_b64 v[116:117], v3 offset:128\nds_read_b64 v[118:119], v3 offset:136\n" \
  "ds_read_b64 v[112:113], v3 offset:192\nds_read_b64 v[114:115], v3 offset:200\nds_read_b64 v[108:109], v3 offset:256\n" \
  "ds_read_b64 v[110:111], v3 offset:264\nds_read_b64 v[104:105], v3 offset:320\nds_read_b64 v[106:107], v3 offset:328\n" \
  "ds_read_b64 v[100:101], v3 offset:384\nds_read_b64 v[102:103], v3 offset:392\nds_read_b64 v[96:97], v3 offset:448\n" \
  "ds_read_b64 v[98:99], v3 offset:456\nds_read_b64 v[92:93], v3 offset:512\nds_read_b64 v[94:95], v3 offset:520\n" \
  "ds_read_b64 v[88:89], v3 offset:576\nds_read_b64 v[90:91], v3 offset:584\nds_read_b64 v[84:85], v3 offset:640\n" \
  "ds_read_b64 v[86:87], v3 offset:648\nds_read_b64 v[80:81], v3 offset:704\nds_read_b64 v[82:83], v3 offset:712\n" \
  "ds_read_b64 v[76:77], v3 offset:768\nds_read_b64 v[78:79], v3 offset:776\nds_read_b64 v[72:73], v3 offset:832\n" \
  "ds_read_b64 v[74:75], v3 offset:840\nds_read_b64 v[68:69], v3 offset:896\nds_read_b64 v[70:71], v3 offset:904\n" \
  "ds_read_b64 v[64:65], v3 offset:960\nds_read_b64 v[66:67], v3 offset:968\nds_read_b64 v[60:61], v3 offset:1024\n" \
  "ds_read_b64 v[62:63], v3 offset:1032\nds_read_b64 v[56:57], v6 offset:256\nds_read_b64 v[58:59], v6 offset:264\n" \
  "ds_read_b64 v[52:53], v6 offset:128\nds_read_b64 v[54:55], v6 offset:136\nds_read_b64 v[48:49], v6 offset:320\n" \
  "ds_read_b64 v[50:51], v6 offset:328\nds_read_b64 v[44:45], v6 offset:192\nds_read_b64 v[46:47], v6 offset:200\ns_waitcnt lgkmcnt(0)\ns_barrier\n" \
  "ds_read_b128 v[24:27], v2 offset:448\nds_read_b128 v[28:31], v2 offset:512\nds_read_b128 v[32:35], v2 offset:576\n" \
  "ds_read_b128 v[36:39], v2 offset:640\nds_read_b128 v[40:43], v2 offset:704\ns_waitcnt lgkmcnt(3)\n" \
  "v_mfma_f32_16x16x32_f16 v[12:15], v[124:127], v[24:27], 0\ns_setprio 1\nds_read_b128 v[24:27], v2 offset:768\n" \
  "v_mfma_f32_16x16x32_f16 v[12:15], v[120:123], v[28:31], v[12:15]\nds_read_b128 v[28:31], v2 offset:832\ns_waitcnt lgkmcnt(3)\n" \
  "v_mfma_f32_16x16x32_f16 v[12:15], v[116:119], v[32:35], v[12:15]\nds_read_b128 v[32:35], v2 offset:896\n" \
  "v_mfma_f32_16x16x32_f16 v[12:15], v[112:115], v[36:39], v[12:15]\nds_read_b128 v[36:39], v2 offset:960\ns_waitcnt lgkmcnt(3)\n" \
  "v_mfma_f32_16x16x32_f16 v[12:15], v[108:111], v[40:43], v[12:15]\nds_read_b128 v[40:43], v2 offset:1024\n" \
  "v_mfma_f32_16x16x32_f16 v[12:15], v[104:107], v[24:27], v[12:15]\nds_read_b128 v[24:27], v2 offset:1088\ns_waitcnt lgkmcnt(3)\n" \
  "v_mfma_f32_16x16x32_f16 v[12:15], v[100:103], v[28:31], v[12:15]\nds_read_b128 v[28:31], v2 offset:1152\n" \
  "v_mfma_f32_16x16x32_f16 v[12:15], v[96:99], v[32:35], v[12:15]\ns_waitcnt lgkmcnt(3)\n" \
  "v_mfma_f32_16x16x32_f16 v[12:15], v[92:95], v[36:39], v[12:15]\nds_read_b128 v[32:35], v2 offset:1216\n" \
  "v_mfma_f32_16x16x32_f16 v[16:19], v[124:127], v[36:39], 0\ns_waitcnt lgkmcnt(3)\nv_mfma_f32_16x16x32_f16 v[12:15], v[88:91], v[40:43], v[12:15]\n" \
  "ds_read_b128 v[124:127], v2 offset:1280\nv_mfma_f32_16x16x32_f16 v[16:19], v[120:123], v[40:43], v[16:19]\ns_waitcnt lgkmcnt(3)\n" \
  "v_mfma_f32_16x16x32_f16 v[12:15], v[84:87], v[24:27], v[12:15]\nds_read_b128 v[36:39], v2 offset:1344\n" \
  "v_mfma_f32_16x16x32_f16 v[16:19], v[116:119], v[24:27], v[16:19]\ns_waitcnt lgkmcnt(3)\n" \
  "v_mfma_f32_16x16x32_f16 v[12:15], v[80:83], v[28:31], v[12:15]\nds_read_b128 v[120:123], v2 offset:1408\n" \
  "v_mfma_f32_16x16x32_f16 v[16:19], v[112:115], v[28:31], v[16:19]\ns_waitcnt lgkmcnt(3)\n" \
  "v_mfma_f32_16x16x32_f16 v[12:15], v[76:79], v[32:35], v[12:15]\nds_read_b128 v[40:43], v2 offset:1472\n" \
  "v_mfma_f32_16x16x32_f16 v[16:19], v[108:111], v[32:35], v[16:19]\ns_waitcnt lgkmcnt(3)\n" \
  "v_mfma_f32_16x16x32_f16 v[12:15], v[72:75], v[124:127], v[12:15]\nds_read_b128 v[116:119], v2 offset:1536\n" \
  "v_mfma_f32_16x16x32_f16 v[16:19], v[104:107], v[124:127], v[16:19]\nds_read_b128 v[24:27], v2 offset:1600\ns_waitcnt lgkmcnt(3)\n" \
  "v_mfma_f32_16x16x32_f16 v[12:15], v[68:71], v[36:39], v[12:15]\nds_read_b128 v[112:115], v2 offset:1664\nds_read_b32 v23, v9 offset:32\n" \
  "v_mfma_f32_16x16x32_f16 v[16:19], v[100:103], v[36:39], v[16:19]\nds_read_b128 v[28:31], v2 offset:1728\n" \
  "v_mfma_f32_16x16x32_f16 v[12:15], v[64:67], v[120:123], v[12:15]\nds_read_b128 v[108:111], v2 offset:1792\n" \
  "v_mfma_f32_16x16x32_f16 v[16:19], v[96:99], v[120:123], v[16:19]\nds_read_b128 v[32:35], v2 offset:1856\ns_waitcnt lgkmcnt(6)\n" \
  "v_mfma_f32_16x16x32_f16 v[12:15], v[60:63], v[40:43], v[12:15]\nds_read_b128 v[104:107], v2 offset:1920\n" \
  "v_mfma_f32_16x16x32_f16 v[16:19], v[92:95], v[40:43], v[16:19]\nds_read_b128 v[124:127], v2 offset:1984\ns_waitcnt lgkmcnt(5)\n" \
  "v_readfirstlane_b32 s85, v23\ns_cmp_lt_u32 s85, 3\ns_cbranch_scc1 51f\n52:\nv_mfma_f32_16x16x32_f16 v[16:19], v[88:91], v[116:119], v[16:19]\n" \
  "ds_read_b64_tr_b16 v[100:101], v5 offset:0\nds_read_b64_tr_b16 v[102:103], v5 offset:32\n" \
  "v_mfma_f32_16x16x32_f16 v[16:19], v[84:87], v[24:27], v[16:19]\nds_read_b64_tr_b16 v[36:37], v5 offset:256\n" \
  "ds_read_b64_tr_b16 v[38:39], v5 offset:288\nv_mfma_f32_16x16x32_f16 v[16:19], v[80:83], v[112:115], v[16:19]\n" \
  "ds_read_b64_tr_b16 v[96:97], v5 offset:512\nds_read_b64_tr_b16 v[98:99], v5 offset:544\ns_waitcnt lgkmcnt(9)\n" \
  "v_mfma_f32_16x16x32_f16 v[16:19], v[76:79], v[28:31], v[16:19]\nds_read_b64_tr_b16 v[120:121], v5 offset:768\n" \
  "ds_read_b64_tr_b16 v[122:123], v5 offset:800\nv_mfma_f32_16x16x32_f16 v[16:19], v[72:75], v[108:111], v[16:19]\ns_waitcnt lgkmcnt(9)\n" \
  "v_mfma_f32_16x16x32_f16 v[16:19], v[68:71], v[32:35], v[16:19]\nv_mfma_f32_16x16x32_f16 v[16:19], v[64:67], v[104:107], v[16:19]\n" \
  "s_waitcnt lgkmcnt(6)\nv_mfma_f32_16x16x32_f16 v[16:19], v[60:63], v[124:127], v[16:19]\n" \
  "v_mfma_f32_16x16x32_f16 v[12:15], v[100:103], v[56:59], v[12:15]\nv_mfma_f32_16x16x32_f16 v[16:19], v[100:103], v[52:55], v[16:19]\n" \
  "s_waitcnt lgkmcnt(2)\nv_mfma_f32_16x16x32_f16 v[12:15], v[36:39], v[48:51], v[12:15]\n" \
  "v_mfma_f32_16x16x32_f16 v[16:19], v[36:39], v[44:47], v[16:19]\nv_mfma_f32_16x16x32_f16 v[16:19], v[96:99], v[56:59], v[16:19]\n" \
  "s_waitcnt lgkmcnt(0)\nv_mfma_f32_16x16x32_f16 v[16:19], v[120:123], v[48:51], v[16:19]\ns_nop 3\nv_cvt_pkrtz_f16_f32 v20, v12, v13\n" \
  "v_cvt_pkrtz_f16_f32 v21, v14, v15\nv_pk_add_u16 v20, v20, 4 op_sel_hi:[1,0] clamp\nv_pk_add_u16 v21, v21, 4 op_sel_hi:[1,0] clamp\n" \
  "ds_write_b64 v4, v[20:21] offset:2048\nv_cvt_pkrtz_f16_f32 v20, v16, v17\nv_cvt_pkrtz_f16_f32 v21, v18, v19\n" \
  "v_pk_add_u16 v20, v20, 4 op_sel_hi:[1,0] clamp\nv_pk_add_u16 v21, v21, 4 op_sel_hi:[1,0] clamp\nds_write_b64 v4, v[20:21] offset:3072\n" \
  "s_setprio 0\ns_setpc_b64 s[90:91]\n51:\ns_movk_i32 s84, 0x4000\n53:\nds_read_b32 v23, v9 offset:32\ns_sub_u32 s84, s84, 1\ns_cbranch_scc1 52b\n" \
  "s_waitcnt lgkmcnt(0)\nv_readfirstlane_b32 s85, v23\ns_cmp_lt_u32 s85, 3\ns_cbranch_scc1 53b\ns_branch 52b\n4:\n" \
  "ds_read_b64 v[124:125], v3 offset:1088\nds_read_b64 v[126:127], v3 offset:1096\nds_read_b64 v[120:121], v3 offset:0\n" \
  "ds_read_b64 v[122:123], v3 offset:8\nds_read_b64 v[116:117], v3 offset:64\nds_read_b64 v[118:119], v3 offset:72\n" \
  "ds_read_b64 v[112:113], v3 offset:128\nds_read_b64 v[114:115], v3 offset:136\nds_read_b64 v[108:109], v3 offset:192\n" \
  "ds_read_b64 v[110:111], v3 offset:200\nds_read_b64 v[104:105], v3 offset:256\nds_read_b64 v[106:107], v3 offset:264\n" \
  "ds_read_b64 v[100:101], v3 offset:320\nds_read_b64 v[102:103], v3 offset:328\nds_read_b64 v[96:97], v3 offset:384\n" \
  "ds_read_b64 v[98:99], v3 offset:392\nds_read_b64 v[92:93], v3 offset:448\nds_read_b64 v[94:95], v3 offset:456\n" \
  "ds_read_b64 v[88:89], v3 offset:512\nds_read_b64 v[90:91], v3 offset:520\nds_read_b64 v[84:85], v3 offset:576\n" \
  "ds_read_b64 v[86:87], v3 offset:584\nds_read_b64 v[80:81], v3 offset:640\nds_read_b64 v[82:83], v3 offset:648\n" \
  "ds_read_b64 v[76:77], v3 offset:704\nds_read_b64 v[78:79], v3 offset:712\nds_read_b64 v[72:73], v3 offset:768\n" \
  "ds_read_b64 v[74:75], v3 offset:776\nds_read_b64 v[68:69], v3 offset:832\nds_read_b64 v[70:71], v3 offset:840\n" \
  "ds_read_b64 v[64:65], v3 offset:896\nds_read_b64 v[66:67], v3 offset:904\nds_read_b64 v[60:61], v3 offset:960\n" \
  "ds_read_b64 v[62:63], v3 offset:968\nds_read_b64 v[56:57], v3 offset:1024\nds_read_b64 v[58:59], v3 offset:1032\n" \
  "ds_read_b64 v[52:53], v6 offset:0\nds_read_b64 v[54:55], v6 offset:8\nds_read_b64 v[48:49], v6 offset:64\nds_read_b64 v[50:51], v6 offset:72\n" \
  "ds_read_b64 v[44:45], v6 offset:128\nds_read_b64 v[46:47], v6 offset:136\nds_read_b64 v[40:41], v6 offset:192\n" \
  "ds_read_b64 v[42:43], v6 offset:200\ns_waitcnt lgkmcnt(0)\ns_barrier\nds_read_b128 v[24:27], v2 offset:448\nds_read_b128 v[28:31], v2 offset:960\n" \
  "ds_read_b128 v[32:35], v2 offset:1472\nds_read_b128 v[36:39], v2 offset:1536\ns_waitcnt lgkmcnt(3)\n" \
  "v_mfma_f32_16x16x32_f16 v[12:15], v[124:127], v[24:27], 0\ns_setprio 1\nds_read_b128 v[24:27], v2 offset:1600\ns_waitcnt lgkmcnt(3)\n" \
  "v_mfma_f32_16x16x32_f16 v[16:19], v[124:127], v[28:31], 0\nds_read_b128 v[124:127], v2 offset:1664\ns_waitcnt lgkmcnt(3)\n" \
  "v_mfma_f32_16x16x32_f16 v[12:15], v[120:123], v[32:35], v[12:15]\nds_read_b128 v[28:31], v2 offset:1728\ns_waitcnt lgkmcnt(3)\n" \
  "v_mfma_f32_16x16x32_f16 v[12:15], v[116:119], v[36:39], v[12:15]\nds_read_b128 v[32:35], v2 offset:1792\ns_waitcnt lgkmcnt(3)\n" \
  "v_mfma_f32_16x16x32_f16 v[12:15], v[112:115], v[24:27], v[12:15]\nds_read_b128 v[36:39], v2 offset:1856\ns_waitcnt lgkmcnt(3)\n" \
  "v_mfma_f32_16x16x32_f16 v[12:15], v[108:111], v[124:127], v[12:15]\nds_read_b128 v[24:27], v2 offset:1920\ns_waitcnt lgkmcnt(3)\n" \
  "v_mfma_f32_16x16x32_f16 v[12:15], v[104:107], v[28:31], v[12:15]\nds_read_b128 v[124:127], v2 offset:1984\ns_waitcnt lgkmcnt(3)\n" \
  "v_mfma_f32_16x16x32_f16 v[12:15], v[100:103], v[32:35], v[12:15]\nds_read_b128 v[28:31], v2 offset:2048\ns_waitcnt lgkmcnt(3)\n" \
  "v_mfma_f32_16x16x32_f16 v[12:15], v[96:99], v[36:39], v[12:15]\nds_read_b128 v[32:35], v2 offset:2112\ns_waitcnt lgkmcnt(3)\n" \
  "v_mfma_f32_16x16x32_f16 v[12:15], v[92:95], v[24:27], v[12:15]\nds_read_b128 v[36:39], v2 offset:2176\ns_waitcnt lgkmcnt(3)\n" \
  "v_mfma_f32_16x16x32_f16 v[12:15], v[88:91], v[124:127], v[12:15]\nds_read_b128 v[24:27], v2 offset:2240\n" \
  "v_mfma_f32_16x16x32_f16 v[16:19], v[120:123], v[124:127], v[16:19]\nds_read_b64 v[120:121], v6 offset:256\nds_read_b64 v[122:123], v6 offset:264\n" \
  "s_waitcnt lgkmcnt(4)\nv_mfma_f32_16x16x32_f16 v[12:15], v[84:87], v[28:31], v[12:15]\nds_read_b128 v[124:127], v2 offset:2304\n" \
  "v_mfma_f32_16x16x32_f16 v[16:19], v[116:119], v[28:31], v[16:19]\nds_read_b64 v[116:117], v6 offset:320\nds_read_b64 v[118:119], v6 offset:328\n" \
  "v_mfma_f32_16x16x32_f16 v[12:15], v[80:83], v[32:35], v[12:15]\nds_read_b128 v[28:31], v2 offset:2368\n" \
  "v_mfma_f32_16x16x32_f16 v[16:19], v[112:115], v[32:35], v[16:19]\ns_waitcnt lgkmcnt(6)\n" \
  "v_mfma_f32_16x16x32_f16 v[12:15], v[76:79], v[36:39], v[12:15]\nds_read_b128 v[112:115], v2 offset:2432\n" \
  "v_mfma_f32_16x16x32_f16 v[16:19], v[108:111], v[36:39], v[16:19]\nds_read2_b32 v[32:33], v8 offset0:48 offset1:49\n" \
  "ds_read2_b32 v[34:35], v8 offset0:50 offset1:51\nv_mfma_f32_16x16x32_f16 v[12:15], v[72:75], v[24:27], v[12:15]\n" \
  "ds_read_b128 v[108:111], v2 offset:2496\nv_mfma_f32_16x16x32_f16 v[16:19], v[104:107], v[24:27], v[16:19]\n" \
  "ds_read2_b32 v[36:37], v8 offset0:32 offset1:33\nds_read2_b32 v[38:39], v8 offset0:34 offset1:35\ns_waitcnt lgkmcnt(6)\n" \
  "v_mfma_f32_16x16x32_f16 v[12:15], v[68:71], v[124:127], v[12:15]\nds_read_b128 v[104:107], v2 offset:2560\n" \
  "v_mfma_f32_16x16x32_f16 v[16:19], v[100:103], v[124:127], v[16:19]\nds_read_b128 v[24:27], v2 offset:2624\n" \
  "v_mfma_f32_16x16x32_f16 v[12:15], v[64:67], v[28:31], v[12:15]\nds_read_b128 v[100:103], v2 offset:2688\nds_read_b32 v23, v9 offset:32\n" \
  "v_mfma_f32_16x16x32_f16 v[16:19], v[96:99], v[28:31], v[16:19]\nds_read_b128 v[124:127], v2 offset:2752\ns_waitcnt lgkmcnt(7)\n" \
  "v_mfma_f32_16x16x32_f16 v[12:15], v[60:63], v[112:115], v[12:15]\nds_read_b128 v[96:99], v2 offset:2816\n" \
  "v_mfma_f32_16x16x32_f16 v[16:19], v[92:95], v[112:115], v[16:19]\nds_read_b128 v[28:31], v2 offset:2880\n" \
  "v_mfma_f32_16x16x32_f16 v[12:15], v[56:59], v[108:111], v[12:15]\nds_read_b128 v[92:95], v2 offset:2944\n" \
  "v_mfma_f32_16x16x32_f16 v[16:19], v[88:91], v[108:111], v[16:19]\nds_read_b128 v[112:115], v2 offset:3008\ns_waitcnt lgkmcnt(5)\n" \
  "v_readfirstlane_b32 s85, v23\ns_cmp_lt_u32 s85, 3\ns_cbranch_scc1 61f\n62:\nv_mfma_f32_16x16x32_f16 v[16:19], v[84:87], v[104:107], v[16:19]\n" \
  "ds_read_b64_tr_b16 v[88:89], v5 offset:0\nds_read_b64_tr_b16 v[90:91], v5 offset:32\n" \
  "v_mfma_f32_16x16x32_f16 v[16:19], v[80:83], v[24:27], v[16:19]\nds_read_b64_tr_b16 v[108:109], v5 offset:256\n" \
  "ds_read_b64_tr_b16 v[110:111], v5 offset:288\nv_mfma_f32_16x16x32_f16 v[16:19], v[76:79], v[100:103], v[16:19]\n" \
  "ds_read_b64_tr_b16 v[84:85], v5 offset:512\nds_read_b64_tr_b16 v[86:87], v5 offset:544\ns_waitcnt lgkmcnt(9)\n" \
  "v_mfma_f32_16x16x32_f16 v[16:19], v[72:75], v[124:127], v[16:19]\nds_read_b64_tr_b16 v[104:105], v5 offset:768\n" \
  "ds_read_b64_tr_b16 v[106:107], v5 offset:800\nv_mfma_f32_16x16x32_f16 v[16:19], v[68:71], v[96:99], v[16:19]\n" \
  "ds_read_b64_tr_b16 v[80:81], v5 offset:1024\nds_read_b64_tr_b16 v[82:83], v5 offset:1056\ns_waitcnt lgkmcnt(11)\n" \
  "v_mfma_f32_16x16x32_f16 v[16:19], v[64:67], v[28:31], v[16:19]\nds_read_b64_tr_b16 v[24:25], v5 offset:1280\n" \
  "ds_read_b64_tr_b16 v[26:27], v5 offset:1312\nv_mfma_f32_16x16x32_f16 v[16:19], v[60:63], v[92:95], v[16:19]\n" \
  "ds_read_b64_tr_b16 v[76:77], v5 offset:1536\nds_read_b64_tr_b16 v[78:79], v5 offset:1568\ns_waitcnt lgkmcnt(12)\n" \
  "v_mfma_f32_16x16x32_f16 v[16:19], v[56:59], v[112:115], v[16:19]\nds_read_b64_tr_b16 v[100:101], v5 offset:1792\n" \
  "ds_read_b64_tr_b16 v[102:103], v5 offset:1824\nv_mfma_f32_16x16x32_f16 v[12:15], v[88:91], v[52:55], v[12:15]\ns_waitcnt lgkmcnt(10)\n" \
  "v_mfma_f32_16x16x32_f16 v[12:15], v[108:111], v[48:51], v[12:15]\nds_read_b64_tr_b16 v[72:73], v7 offset:0\n" \
  "ds_read_b64_tr_b16 v[74:75], v7 offset:64\nv_mfma_f32_16x16x32_f16 v[12:15], v[84:87], v[44:47], v[12:15]\n" \
  "ds_read_b64_tr_b16 v[124:125], v7 offset:512\nds_read_b64_tr_b16 v[126:127], v7 offset:576\n" \
  "v_mfma_f32_16x16x32_f16 v[16:19], v[84:87], v[52:55], v[16:19]\ns_waitcnt lgkmcnt(10)\n" \
  "v_mfma_f32_16x16x32_f16 v[12:15], v[104:107], v[40:43], v[12:15]\nv_mfma_f32_16x16x32_f16 v[16:19], v[104:107], v[48:51], v[16:19]\n" \
  "v_mfma_f32_16x16x32_f16 v[12:15], v[80:83], v[120:123], v[12:15]\nv_mfma_f32_16x16x32_f16 v[16:19], v[80:83], v[44:47], v[16:19]\n" \
  "s_waitcnt lgkmcnt(6)\nv_mfma_f32_16x16x32_f16 v[12:15], v[24:27], v[116:119], v[12:15]\n" \
  "v_mfma_f32_16x16x32_f16 v[16:19], v[24:27], v[40:43], v[16:19]\nv_mfma_f32_16x16x32_f16 v[16:19], v[76:79], v[120:123], v[16:19]\n" \
  "s_waitcnt lgkmcnt(2)\nv_mfma_f32_16x16x32_f16 v[16:19], v[100:103], v[116:119], v[16:19]\n" \
  "v_mfma_f32_16x16x32_f16 v[12:15], v[72:75], v[32:35], v[12:15]\nv_mfma_f32_16x16x32_f16 v[16:19], v[72:75], v[36:39], v[16:19]\n" \
  "s_waitcnt lgkmcnt(0)\nv_mfma_f32_16x16x32_f16 v[16:19], v[124:127], v[32:35], v[16:19]\ns_nop 4\nv_cvt_pkrtz_f16_f32 v20, v12, v13\n" \
  "v_cvt_pkrtz_f16_f32 v21, v14, v15\nv_pk_add_u16 v20, v20, 4 op_sel_hi:[1,0] clamp\nv_pk_add_u16 v21, v21, 4 op_sel_hi:[1,0] clamp\n" \
  "ds_write_b64 v4, v[20:21] offset:4096\nv_cvt_pkrtz_f16_f32 v20, v16, v17\nv_cvt_pkrtz_f16_f32 v21, v18, v19\n" \
  "v_pk_add_u16 v20, v20, 4 op_sel_hi:[1,0] clamp\nv_pk_add_u16 v21, v21, 4 op_sel_hi:[1,0] clamp\nds_write_b64 v4, v[20:21] offset:5120\n" \
  "s_setprio 0\ns_setpc_b64 s[90:91]\n61:\ns_movk_i32 s84, 0x4000\n63:\nds_read_b32 v23, v9 offset:32\ns_sub_u32 s84, s84, 1\ns_cbranch_scc1 62b\n" \
  "s_waitcnt lgkmcnt(0)\nv_readfirstlane_b32 s85, v23\ns_cmp_lt_u32 s85, 3\ns_cbranch_scc1 63b\ns_branch 62b\n5:\n" \
  "ds_read_b64 v[124:125], v3 offset:1088\nds_read_b64 v[126:127], v3 offset:1096\nds_read_b64 v[120:121], v3 offset:0\n" \
  "ds_read_b64 v[122:123], v3 offset:8\nds_read_b64 v[116:117], v3 offset:64\nds_read_b64 v[118:119], v3 offset:72\n" \
  "ds_read_b64 v[112:113], v3 offset:128\nds_read_b64 v[114:115], v3 offset:136\nds_read_b64 v[108:109], v3 offset:192\n" \
  "ds_read_b64 v[110:111], v3 offset:200\nds_read_b64 v[104:105], v3 offset:256\nds_read_b64 v[106:107], v3 offset:264\n" \
  "ds_read_b64 v[100:101], v3 offset:320\nds_read_b64 v[102:103], v3 offset:328\nds_read_b64 v[96:97], v3 offset:384\n" \
  "ds_read_b64 v[98:99], v3 offset:392\nds_read_b64 v[92:93], v3 offset:448\nds_read_b64 v[94:95], v3 offset:456\n" \
  "ds_read_b64 v[88:89], v3 offset:512\nds_read_b64 v[90:91], v3 offset:520\nds_read_b64 v[84:85], v3 offset:576\n" \
  "ds_read_b64 v[86:87], v3 offset:584\nds_read_b64 v[80:81], v3 offset:640\nds_read_b64 v[82:83], v3 offset:648\n" \
  "ds_read_b64 v[76:77], v3 offset:704\nds_read_b64 v[78:79], v3 offset:712\nds_read_b64 v[72:73], v3 offset:768\n" \
  "ds_read_b64 v[74:75], v3 offset:776\nds_read_b64 v[68:69], v3 offset:832\nds_read_b64 v[70:71], v3 offset:840\n" \
  "ds_read_b64 v[64:65], v3 offset:896\nds_read_b64 v[66:67], v3 offset:904\nds_read_b64 v[60:61], v3 offset:960\n" \
  "ds_read_b64 v[62:63], v3 offset:968\nds_read_b64 v[56:57], v3 offset:1024\nds_read_b64 v[58:59], v3 offset:1032\n" \
  "ds_read_b64 v[52:53], v6 offset:0\nds_read_b64 v[54:55], v6 offset:8\nds_read_b64 v[48:49], v6 offset:64\nds_read_b64 v[50:51], v6 offset:72\n" \
  "ds_read_b64 v[44:45], v6 offset:128\nds_read_b64 v[46:47], v6 offset:136\nds_read_b64 v[40:41], v6 offset:192\n" \
  "ds_read_b64 v[42:43], v6 offset:200\ns_waitcnt lgkmcnt(0)\ns_barrier\nds_read_b128 v[24:27], v2 offset:1472\n" \
  "ds_read_b128 v[28:31], v2 offset:1984\nds_read_b128 v[32:35], v2 offset:2496\nds_read_b128 v[36:39], v2 offset:2560\ns_waitcnt lgkmcnt(3)\n" \
  "v_mfma_f32_16x16x32_f16 v[12:15], v[124:127], v[24:27], 0\ns_setprio 1\nds_read_b128 v[24:27], v2 offset:2624\ns_waitcnt lgkmcnt(3)\n" \
  "v_mfma_f32_16x16x32_f16 v[16:19], v[124:127], v[28:31], 0\nds_read_b128 v[124:127], v2 offset:2688\ns_waitcnt lgkmcnt(3)\n" \
  "v_mfma_f32_16x16x32_f16 v[12:15], v[120:123], v[32:35], v[12:15]\nds_read_b128 v[28:31], v2 offset:2752\ns_waitcnt lgkmcnt(3)\n" \
  "v_mfma_f32_16x16x32_f16 v[12:15], v[116:119], v[36:39], v[12:15]\nds_read_b128 v[32:35], v2 offset:2816\ns_waitcnt lgkmcnt(3)\n" \
  "v_mfma_f32_16x16x32_f16 v[12:15], v[112:115], v[24:27], v[12:15]\nds_read_b128 v[36:39], v2 offset:2880\ns_waitcnt lgkmcnt(3)\n" \
  "v_mfma_f32_16x16x32_f16 v[12:15], v[108:111], v[124:127], v[12:15]\nds_read_b128 v[24:27], v2 offset:2944\ns_waitcnt lgkmcnt(3)\n" \
  "v_mfma_f32_16x16x32_f16 v[12:15], v[104:107], v[28:31], v[12:15]\nds_read_b128 v[124:127], v2 offset:3008\ns_waitcnt lgkmcnt(3)\n" \
  "v_mfma_f32_16x16x32_f16 v[12:15], v[100:103], v[32:35], v[12:15]\nds_read_b128 v[28:31], v2 offset:3072\ns_waitcnt lgkmcnt(3)\n" \
  "v_mfma_f32_16x16x32_f16 v[12:15], v[96:99], v[36:39], v[12:15]\nds_read_b128 v[32:35], v2 offset:3136\ns_waitcnt lgkmcnt(3)\n" \
  "v_mfma_f32_16x16x32_f16 v[12:15], v[92:95], v[24:27], v[12:15]\nds_read_b128 v[36:39], v2 offset:3200\ns_waitcnt lgkmcnt(3)\n" \
  "v_mfma_f32_16x16x32_f16 v[12:15], v[88:91], v[124:127], v[12:15]\nds_read_b128 v[24:27], v2 offset:3264\n" \
  "v_mfma_f32_16x16x32_f16 v[16:19], v[120:123], v[124:127], v[16:19]\nds_read_b64 v[120:121], v6 offset:256\nds_read_b64 v[122:123], v6 offset:264\n" \
  "s_waitcnt lgkmcnt(4)\nv_mfma_f32_16x16x32_f16 v[12:15], v[84:87], v[28:31], v[12:15]\nds_read_b128 v[124:127], v2 offset:3328\n" \
  "v_mfma_f32_16x16x32_f16 v[16:19], v[116:119], v[28:31], v[16:19]\nds_read_b64 v[116:117], v6 offset:320\nds_read_b64 v[118:119], v6 offset:328\n" \
  "v_mfma_f32_16x16x32_f16 v[12:15], v[80:83], v[32:35], v[12:15]\nds_read_b128 v[28:31], v2 offset:3392\n" \
  "v_mfma_f32_16x16x32_f16 v[16:19], v[112:115], v[32:35], v[16:19]\ns_waitcnt lgkmcnt(6)\n" \
  "v_mfma_f32_16x16x32_f16 v[12:15], v[76:79], v[36:39], v[12:15]\nds_read_b128 v[112:115], v2 offset:3456\n" \
  "v_mfma_f32_16x16x32_f16 v[16:19], v[108:111], v[36:39], v[16:19]\nds_read2_b32 v[32:33], v8 offset0:16 offset1:17\n" \
  "ds_read2_b32 v[34:35], v8 offset0:18 offset1:19\nv_mfma_f32_16x16x32_f16 v[12:15], v[72:75], v[24:27], v[12:15]\n" \
  "ds_read_b128 v[108:111], v2 offset:3520\nv_mfma_f32_16x16x32_f16 v[16:19], v[104:107], v[24:27], v[16:19]\n" \
  "ds_read2_b32 v[36:37], v8 offset0:0 offset1:1\nds_read2_b32 v[38:39], v8 offset0:2 offset1:3\ns_waitcnt lgkmcnt(6)\n" \
  "v_mfma_f32_16x16x32_f16 v[12:15], v[68:71], v[124:127], v[12:15]\nds_read_b128 v[104:107], v2 offset:3584\n" \
  "v_mfma_f32_16x16x32_f16 v[16:19], v[100:103], v[124:127], v[16:19]\nds_read_b128 v[24:27], v2 offset:3648\n" \
  "ds_read2_b32 v[100:101], v8 offset0:32 offset1:33\nds_read2_b32 v[102:103], v8 offset0:34 offset1:35\n" \
  "v_mfma_f32_16x16x32_f16 v[12:15], v[64:67], v[28:31], v[12:15]\nds_read_b128 v[124:127], v2 offset:3712\nds_read_b32 v23, v9 offset:32\n" \
  "v_mfma_f32_16x16x32_f16 v[16:19], v[96:99], v[28:31], v[16:19]\nds_read_b128 v[96:99], v2 offset:3776\ns_waitcnt lgkmcnt(9)\n" \
  "v_mfma_f32_16x16x32_f16 v[12:15], v[60:63], v[112:115], v[12:15]\nds_read_b128 v[28:31], v2 offset:3840\n" \
  "v_mfma_f32_16x16x32_f16 v[16:19], v[92:95], v[112:115], v[16:19]\nds_read_b128 v[92:95], v2 offset:3904\n" \
  "v_mfma_f32_16x16x32_f16 v[12:15], v[56:59], v[108:111], v[12:15]\nds_read_b128 v[112:115], v2 offset:3968\n" \
  "v_mfma_f32_16x16x32_f16 v[16:19], v[88:91], v[108:111], v[16:19]\nds_read_b128 v[88:91], v2 offset:4032\ns_waitcnt lgkmcnt(5)\n" \
  "v_readfirstlane_b32 s85, v23\ns_cmp_lt_u32 s85, 3\ns_cbranch_scc1 71f\n72:\nv_mfma_f32_16x16x32_f16 v[16:19], v[84:87], v[104:107], v[16:19]\n" \
  "ds_read_b64_tr_b16 v[108:109], v5 offset:1024\nds_read_b64_tr_b16 v[110:111], v5 offset:1056\n" \
  "v_mfma_f32_16x16x32_f16 v[16:19], v[80:83], v[24:27], v[16:19]\nds_read_b64_tr_b16 v[84:85], v5 offset:1280\n" \
  "ds_read_b64_tr_b16 v[86:87], v5 offset:1312\nv_mfma_f32_16x16x32_f16 v[16:19], v[76:79], v[124:127], v[16:19]\n" \
  "ds_read_b64_tr_b16 v[104:105], v5 offset:1536\nds_read_b64_tr_b16 v[106:107], v5 offset:1568\ns_waitcnt lgkmcnt(9)\n" \
  "v_mfma_f32_16x16x32_f16 v[16:19], v[72:75], v[96:99], v[16:19]\nds_read_b64_tr_b16 v[80:81], v5 offset:1792\n" \
  "ds_read_b64_tr_b16 v[82:83], v5 offset:1824\nv_mfma_f32_16x16x32_f16 v[16:19], v[68:71], v[28:31], v[16:19]\n" \
  "ds_read_b64_tr_b16 v[24:25], v5 offset:2048\nds_read_b64_tr_b16 v[26:27], v5 offset:2080\ns_waitcnt lgkmcnt(11)\n" \
  "v_mfma_f32_16x16x32_f16 v[16:19], v[64:67], v[92:95], v[16:19]\nds_read_b64_tr_b16 v[76:77], v5 offset:2304\n" \
  "ds_read_b64_tr_b16 v[78:79], v5 offset:2336\nv_mfma_f32_16x16x32_f16 v[16:19], v[60:63], v[112:115], v[16:19]\n" \
  "ds_read_b64_tr_b16 v[124:125], v5 offset:2560\nds_read_b64_tr_b16 v[126:127], v5 offset:2592\ns_waitcnt lgkmcnt(12)\n" \
  "v_mfma_f32_16x16x32_f16 v[16:19], v[56:59], v[88:91], v[16:19]\nds_read_b64_tr_b16 v[72:73], v5 offset:2816\n" \
  "ds_read_b64_tr_b16 v[74:75], v5 offset:2848\nv_mfma_f32_16x16x32_f16 v[12:15], v[108:111], v[52:55], v[12:15]\ns_waitcnt lgkmcnt(10)\n" \
  "v_mfma_f32_16x16x32_f16 v[12:15], v[84:87], v[48:51], v[12:15]\nds_read_b64_tr_b16 v[96:97], v7 offset:0\n" \
  "ds_read_b64_tr_b16 v[98:99], v7 offset:64\nv_mfma_f32_16x16x32_f16 v[12:15], v[104:107], v[44:47], v[12:15]\n" \
  "ds_read_b64_tr_b16 v[68:69], v7 offset:512\nds_read_b64_tr_b16 v[70:71], v7 offset:576\n" \
  "v_mfma_f32_16x16x32_f16 v[16:19], v[104:107], v[52:55], v[16:19]\ns_waitcnt lgkmcnt(10)\n" \
  "v_mfma_f32_16x16x32_f16 v[12:15], v[80:83], v[40:43], v[12:15]\nds_read_b64_tr_b16 v[28:29], v7 offset:1024\n" \
  "ds_read_b64_tr_b16 v[30:31], v7 offset:1088\nv_mfma_f32_16x16x32_f16 v[16:19], v[80:83], v[48:51], v[16:19]\n" \
  "ds_read2_b32 v[64:65], v8 offset0:48 offset1:49\nds_read2_b32 v[66:67], v8 offset0:50 offset1:51\n" \
  "v_mfma_f32_16x16x32_f16 v[12:15], v[24:27], v[120:123], v[12:15]\nv_mfma_f32_16x16x32_f16 v[16:19], v[24:27], v[44:47], v[16:19]\n" \
  "s_waitcnt lgkmcnt(10)\nv_mfma_f32_16x16x32_f16 v[12:15], v[76:79], v[116:119], v[12:15]\nds_read_b64_tr_b16 v[92:93], v7 offset:1536\n" \
  "ds_read_b64_tr_b16 v[94:95], v7 offset:1600\nv_mfma_f32_16x16x32_f16 v[16:19], v[76:79], v[40:43], v[16:19]\n" \
  "v_mfma_f32_16x16x32_f16 v[16:19], v[124:127], v[120:123], v[16:19]\ns_waitcnt lgkmcnt(8)\n" \
  "v_mfma_f32_16x16x32_f16 v[16:19], v[72:75], v[116:119], v[16:19]\nv_mfma_f32_16x16x32_f16 v[12:15], v[96:99], v[32:35], v[12:15]\n" \
  "v_mfma_f32_16x16x32_f16 v[16:19], v[96:99], v[36:39], v[16:19]\ns_waitcnt lgkmcnt(4)\n" \
  "v_mfma_f32_16x16x32_f16 v[12:15], v[68:71], v[100:103], v[12:15]\nv_mfma_f32_16x16x32_f16 v[16:19], v[68:71], v[32:35], v[16:19]\n" \
  "s_waitcnt lgkmcnt(2)\nv_mfma_f32_16x16x32_f16 v[12:15], v[28:31], v[64:67], v[12:15]\n" \
  "v_mfma_f32_16x16x32_f16 v[16:19], v[28:31], v[100:103], v[16:19]\ns_waitcnt lgkmcnt(0)\n" \
  "v_mfma_f32_16x16x32_f16 v[16:19], v[92:95], v[64:67], v[16:19]\ns_nop 4\nv_cvt_pkrtz_f16_f32 v20, v12, v13\nv_cvt_pkrtz_f16_f32 v21, v14, v15\n" \
  "v_pk_add_u16 v20, v20, 4 op_sel_hi:[1,0] clamp\nv_pk_add_u16 v21, v21, 4 op_sel_hi:[1,0] clamp\nds_write_b64 v4, v[20:21] offset:6144\n" \
  "v_cvt_pkrtz_f16_f32 v20, v16, v17\nv_cvt_pkrtz_f16_f32 v21, v18, v19\nv_pk_add_u16 v20, v20, 4 op_sel_hi:[1,0] clamp\n" \
  "v_pk_add_u16 v21, v21, 4 op_sel_hi:[1,0] clamp\nds_write_b64 v4, v[20:21] offset:7168\ns_setprio 0\ns_setpc_b64 s[90:91]\n71:\n" \
  "s_movk_i32 s84, 0x4000\n73:\nds_read_b32 v23, v9 offset:32\ns_sub_u32 s84, s84, 1\ns_cbranch_scc1 72b\ns_waitcnt lgkmcnt(0)\n" \
  "v_readfirstlane_b32 s85, v23\ns_cmp_lt_u32 s85, 3\ns_cbranch_scc1 73b\ns_branch 72b\n"
#define CONV_CLOB "memory", "scc", "s84", "s85", "s90", "s91", "v12", "v13", "v14", "v15", "v16", "v17", "v18", "v19", "v20", "v21", "v22", "v23", "v24", "v25", "v26", "v27", "v28", "v29", "v30", "v31", "v32", "v33", "v34", "v35", "v36", "v37", "v38", "v39", "v40", "v41", "v42", "v43", "v44", "v45", "v46", "v47", "v48", "v49", "v50", "v51", "v52", "v53", "v54", "v55", "v56", "v57", "v58", "v59", "v60", "v61", "v62", "v63", "v64", "v65", "v66", "v67", "v68", "v69", "v70", "v71", "v72", "v73", "v74", "v75", "v76", "v77", "v78", "v79", "v80", "v81", "v82", "v83", "v84", "v85", "v86", "v87", "v88", "v89", "v90", "v91", "v92", "v93", "v94", "v95", "v96", "v97", "v98", "v99", "v100", "v101", "v102", "v103", "v104", "v105", "v106", "v107", "v108", "v109", "v110", "v111", "v112", "v113", "v114", "v115", "v116", "v117", "v118", "v119", "v120", "v121", "v122", "v123", "v124", "v125", "v126", "v127"
constexpr int TDP = 1216, T_3 = 4 * TDP, T_4 = T_3 + 512, TB = T_4 + 320;
constexpr int C_U = 480, UB = C_U + 4096 + 96, C_G5 = 0, C_G3 = 3200, C_G9 = 6400, C_G7 = C_G9 + 2096, GB = C_G7 + 2080 + 112;
constexpr int O_FL = 2 * TB, O_UL = O_FL + 64, O_IM = O_UL + 2 * UB, O_ST = O_IM + 2 * GB;
constexpr int X_TB = 10240, X_UL = X_TB + TB, X_IM = X_UL + UB, X_ST = X_IM + GB;
constexpr int X_WR = X_IM + 128 * IP;
#define X_RW(w) (LW + 9216 * (2 + ((w) >> 1)) + 2560 * ((w) & 1))
constexpr int RP = 1216;
constexpr int LW = 36864 + 512, LA = LW, LM = LW + 36864;
constexpr int L_SC = LM + 2048, SMEM = O_IM + 256 * IP;
static_assert(L_SC + 1152 <= SMEM && O_ST + 8192 <= SMEM && X_IM + 128 * IP <= LM && X_ST + 8192 <= LM && UB % 128 == 64 && GB % 128 == 64 && SMEM <= 79872, "");
struct TOps { uint32_t xa, xb, ya, pd, pf, pg, pe; };
__device__ __forceinline__ void table_ops(uint32_t im, uint32_t tb, int ct, int lane, TOps& t) {
  const int j = lane & 15, kq = lane >> 4, q4 = (lane >> 2) & 3, p4 = lane & 3, jj = j + 16 * ct;
  const uint32_t sa = im + (uint32_t)(IP * (lane >> 2));
  t.xa = sa + (uint32_t)(I_X + 8 * p4); t.xb = sa + (uint32_t)(I_X + 32 + 8 * (p4 & 1)); t.ya = sa + (uint32_t)(32 * ct + 8 * p4);
  t.pd = tb + (uint32_t)(2 * (541 - (8 * jj + 4 * (kq & 1) + 256 * (kq >> 1))));
  t.pf = tb + (uint32_t)((kq & 1) ? T_4 + 2 * (63 - jj) : T_3 + 2 * (127 - 2 * jj));
  t.pg = t.pf - ((kq & 1) ? 64 : 2);
  t.pe = tb + (uint32_t)(p4 * (TDP + 2) + ((lane < 12) ? 2 * (32 - q4) : 2 * (572 + (q4 & 1))));
  asm volatile("" : "+v"(t.xa), "+v"(t.xb), "+v"(t.ya), "+v"(t.pd), "+v"(t.pf), "+v"(t.pg), "+v"(t.pe));
}
typedef __attribute__((address_space(3))) char* lptr;
#define LP(T, a) ((__attribute__((address_space(3))) T*)(uintptr_t)(a))
__device__ __forceinline__ uint32_t table_tile(const TOps& t, int ct, int lane, uint32_t evp, float dsk TSP) {
  u32x4 ev;
  f32x4 ad[2], af[2];
  asm volatile(T_ASM : "=&v"(ad[0]), "=&v"(ad[1]), "=&v"(af[0]), "=&v"(af[1]), "=&v"(ev) : "v"(t.xa), "v"(t.xb), "v"(t.ya), "v"(evp) : T_CLOB);
  TS(0, 4);
  const int e0 = (int)(ev[0] + ev[2]), e1 = (int)(ev[1] + ev[3]);
  const int em = min(max(max(max(e0, e1), (int)((__float_as_uint(dsk) >> 23) & 0xffu) - 133), -110), 115);
  const int ts = ((uint32_t)(em + 12) <= 8u) ? 1 : -3 - em;
  float s0 = __builtin_amdgcn_ldexpf(1.f, e0 - 29 + ts), s1 = __builtin_amdgcn_ldexpf(1.f, e1 - 29 + ts);
  float d0 = (ct == 0 && lane == 0) ? __builtin_amdgcn_ldexpf(dsk, ts) : 0.f;
  asm volatile("s_nop 1" : "+v"(ad[0]), "+v"(ad[1]), "+v"(af[0]), "+v"(af[1]), "+v"(s0), "+v"(s1), "+v"(d0));
  const f32x4 k = ad[0] * s0 + (f32x4){d0, 0.f, 0.f, 0.f} + ad[1] * s1, f = af[0] * s0 + af[1] * s1;
  auto pk = [](float a, float b) { uint32_t r; asm("v_cvt_pk_f16_f32 %0, %1, %2" : "=v"(r) : "v"(a), "v"(b)); return r; };
  asm volatile("s_nop 0" : : "v"(k), "v"(f));
  const uint32_t p32 = pk(k[3], k[2]), p10 = pk(k[1], k[0]), p21 = pk(k[2], k[1]), p01 = pk(f[0], f[1]), p23 = pk(f[2], f[3]);
  *LP(u32x2, t.pd + 3 * TDP + 6) = (u32x2){p32, p10};
  *LP(uint32_t, t.pd + TDP + 2) = p32; *LP(uint32_t, t.pd + TDP + 6) = p10;
  *LP(uint32_t, t.pd + 2) = p21; *LP(uint32_t, t.pd + 2 * TDP + 6) = p21;
  *LP(u16, t.pd) = (u16)p32; *LP(u16, t.pd + 2 * TDP + 4) = (u16)p32;
  asm volatile("ds_write_b16_d16_hi %0, %1 offset:6\n\tds_write_b16_d16_hi %0, %1 offset:%2" : : "v"(t.pd), "v"(p10), "n"(2 * TDP + 10) : "memory");
  if (ct) {
    const uint32_t va = (uint32_t)__builtin_amdgcn_readlane((int)p32, 63), vb = (uint32_t)__builtin_amdgcn_readlane((int)p23, 15) >> 16;
    *LP(u16, t.pe) = (u16)((lane < 12) ? va : vb);
  }
  if (lane < 32) {
    *LP(u16, t.pf + 128) = (u16)p01; *LP(u16, t.pg + 128) = (u16)(p01 >> 16);
    *LP(u16, t.pf) = (u16)p23; *LP(u16, t.pg) = (u16)(p23 >> 16);
  }
  return (uint32_t)(127 + 5 - ts) << 23;
}
__device__ __forceinline__ uint32_t hadd(uint32_t a, uint32_t b) { return __builtin_bit_cast(uint32_t, __builtin_bit_cast(f16x2, a) + __builtin_bit_cast(f16x2, b)); }
template <bool FULL> __device__ __forceinline__ void sums(char* sm, const u32x4 p, const u32x4 c) {
  const uint32_t D[8] = {p[0], p[1], p[2], p[3], c[0], c[1], c[2], c[3]};
  uint32_t O[7];
#pragma unroll
  for (int j = 0; j < 7; ++j) O[j] = __builtin_amdgcn_alignbit(D[j + 1], D[j], 16);
  u32x4 a, b;
#pragma unroll
  for (int j = 0; j < 4; ++j) a[j] = hadd(D[j + 2], hadd(O[j + 1], O[j + 2]));
  *(u32x4*)(sm + C_G3 + 32) = a;
#pragma unroll
  for (int j = 0; j < 4; ++j) b[j] = hadd(a[j], hadd(D[j + 1], D[j + 3]));
  *(u32x4*)(sm + C_G5 + 32) = b;
  if (FULL) {
#pragma unroll
    for (int j = 0; j < 4; ++j) a[j] = hadd(b[j], hadd(O[j], O[j + 3]));
    *(u32x4*)(sm + C_G7 + 32) = a;
#pragma unroll
    for (int j = 0; j < 4; ++j) b[j] = hadd(a[j], hadd(D[j], D[j + 4]));
    *(u32x4*)(sm + C_G9 + 32) = b;
  }
}
__device__ __forceinline__ void count_up(uint32_t a) {
  uint32_t t;
  asm volatile("s_mov_b64 exec, 1\n\tv_mov_b32 %0, 1\n\tds_add_u32 %1, %0\n\ts_mov_b64 exec, -1" : "=&v"(t) : "v"(a) : "memory");
}
__device__ __forceinline__ int pad_at(int r) { return 16 * (r & 1) + 3200 * (r >> 1) - ((r >= 6) ? 3200 - 2096 : 0); }
struct COps { uint32_t b, a, o, g3, k3, g4, k4, f; };
__device__ __forceinline__ void conv_ops(uint32_t tb, uint32_t ub, uint32_t cb, int lane, COps& e) {
  const uint32_t j = lane & 15, kq = lane >> 4, q4 = (lane >> 2) & 3, p4 = lane & 3;
  e.b = ub + 32u * j + 16u * kq;
  e.a = tb + p4 * TDP + 32u - 8u * q4 + 16u * kq;
  e.g3 = cb + C_G5 + 8 + 64u * kq + 8u * (q4 + p4) + (q4 & 1u) * 3200u;
  e.g4 = cb + C_G9 + 8 + 128u * kq + 16u * q4 + 8u * p4 + (q4 & 1u) * 2096u;
  e.k3 = tb + T_3 + 120 - 8u * j + 16u * kq;
  e.k4 = tb + T_4 + 60 - 4u * j + 16u * kq;
  asm volatile("" : "+v"(e.b), "+v"(e.a), "+v"(e.g3), "+v"(e.g4), "+v"(e.k3), "+v"(e.k4));
}
#define CONV_RUN(prog, e, ...) do { int prog_ = (prog); \
    asm volatile("s_call_b64 s[90:91], kf_conv" : "+{s89}"(prog_), "+{v2}"(e.b), "+{v3}"(e.a), "+{v4}"(e.o), "+{v5}"(e.g3), "+{v6}"(e.k3), "+{v7}"(e.g4), \
                 "+{v8}"(e.k4), "+{v9}"(e.f) __VA_ARGS__ : : CONV_CLOB); } while (0)

template <bool E>
__device__ __forceinline__ void ln_stats(const uint32_t* v, int lane, float sc, f32x2* t, f32x4 z, f32x2& rc, f32x2& nc, float& e) {
  float s1a = z[0], s2a = z[1], s1b = z[2], s2b = z[3];
#pragma unroll
  for (int k = 0; k < 4; ++k) {
    s1a = dot2(v[k], 0x3f803f80u, s1a); s2a = dot2(v[k], v[k], s2a);
    s1b = dot2(v[4 + k], 0x3f803f80u, s1b); s2b = dot2(v[4 + k], v[4 + k], s2b);
  }
#pragma unroll
  for (int k = 0; k < 9; ++k) t[k] = (f32x2){bflo(v[k]), bfhi(v[k])};
  const float rs = row_sum(fold16(dot2(v[8], 0x3f803f80u, fold32(s1a, s1b)), dot2(v[8], v[8], fold32(s2a, s2b))));
  if (E) { e = row_sum(e); e += __int_as_float(__builtin_amdgcn_update_dpp(0, __float_as_int(e), 0x142, 0xa, 0xf, false)); }
  const f32x2 mean = (f32x2){rdl(rs, 15), rdl(rs, 47)} * (1.f / D_), m2 = mean * mean;
  f32x2 var = (f32x2){rdl(rs, 31), rdl(rs, 63)} * (1.f / D_) - m2;
  asm volatile("" : "+v"(t[0]), "+v"(t[1]), "+v"(t[2]), "+v"(t[3]), "+v"(t[4]), "+v"(t[5]), "+v"(t[6]), "+v"(t[7]), "+v"(t[8]));
  if (__builtin_expect(!(m2[0] <= 16.f * var[0] && m2[1] <= 16.f * var[1]), 0)) {
    float q[3] = {0.f, 0.f, 0.f};
#pragma unroll
    for (int k = 0; k < 9; ++k) {
      const f32x2 a = t[k] - ((k < 4 || (k == 8 && lane < 32)) ? mean[0] : mean[1]);
      q[k >> 2] += a[0] * a[0] + a[1] * a[1];
    }
    float qa = q[0] + ((lane < 32) ? q[2] : 0.f), qb = q[1] + ((lane < 32) ? 0.f : q[2]);
    wave_sum2(qa, qb);
    var = (f32x2){qa, qb} * (1.f / D_);
  }
  const uint32_t mp = pack2(mean[0], mean[1]), vp = pack2(var[0], var[1]);
  const f32x2 ve = (f32x2){bflo(vp), bfhi(vp)} + 1e-5f;
  const uint32_t e2 = pack2(ve[0], ve[1]);
  const uint32_t sp = pack2(__builtin_amdgcn_sqrtf(bflo(e2)), __builtin_amdgcn_sqrtf(bfhi(e2)));
  rc = (f32x2){__builtin_amdgcn_rcpf(bflo(sp)), __builtin_amdgcn_rcpf(bfhi(sp))} * sc;
  nc = -(f32x2){bflo(mp), bfhi(mp)} * rc;
}
template <bool H>
__device__ __forceinline__ void ln_apply(const f32x2* t, int lane, f32x2 rc, f32x2 nc, uint32_t* c) {
  const float nm[3] = {nc[0], nc[1], (lane < 32) ? nc[0] : nc[1]}, rr[3] = {rc[0], rc[1], (lane < 32) ? rc[0] : rc[1]};
  f32x2 dd[9];
#pragma unroll
  for (int k = 0; k < 9; ++k) dd[k] = __builtin_elementwise_fma(t[k], (f32x2){rr[k >> 2], rr[k >> 2]}, (f32x2){nm[k >> 2], nm[k >> 2]});
  asm volatile("" : "+v"(dd[0]), "+v"(dd[1]), "+v"(dd[2]), "+v"(dd[3]), "+v"(dd[4]), "+v"(dd[5]), "+v"(dd[6]), "+v"(dd[7]), "+v"(dd[8]));
#pragma unroll
  for (int k = 0; k < 9; ++k) {
    const f32x2 d = dd[k];
    typedef __attribute__((ext_vector_type(2))) uint16_t u16x2;
    if (H) c[k] = __builtin_bit_cast(uint32_t, __builtin_elementwise_add_sat(__builtin_bit_cast(u16x2, __builtin_amdgcn_cvt_pkrtz(d[0], d[1])), (u16x2){4, 4}));
    else c[k] = pack2(d[0], d[1]);
  }
}
__device__ __forceinline__ uint32_t med3u(uint32_t a, uint32_t b, uint32_t c) { return max(min(a, b), min(max(a, b), c)); }
__device__ __forceinline__ uint32_t oct_maxu(uint32_t v) {
  v = max(v, (uint32_t)DPP_I((int)v, 0xb1, 0xf));
  v = max(v, (uint32_t)DPP_I((int)v, 0x4e, 0xf));
  v = max(v, (uint32_t)DPP_I((int)v, 0x141, 0xf));
  return v;
}

#define KARGS const u16* __restrict__ x, const u16* __restrict__ Are, const u16* __restrict__ Aim, const u16* __restrict__ Bre, \
    const u16* __restrict__ Bim, const u16* __restrict__ Cre, const u16* __restrict__ Cim, const u16* __restrict__ logdt, \
    const u16* __restrict__ Dskip, char* __restrict__ ws, const u16* __restrict__ wr, const u16* __restrict__ wg, \
    const u16* __restrict__ wu, const u16* __restrict__ wd, u16* __restrict__ out
#define KPASS x, Are, Aim, Bre, Bim, Cre, Cim, logdt, Dskip, ws, wr, wg, wu, wd, out
template <int ROLE, int FFN = -1>
__device__ __forceinline__ void role_body(char* smem, KARGS) {
  const int tid = threadIdx.x, lane = tid & 63;
  const int w = __builtin_amdgcn_readfirstlane(tid >> 6), wl = w & 3;
  const int b = blockIdx.x & 255, bx = b & 7, bj = b >> 3;
  constexpr bool isl = ROLE == 1 || ROLE == 2;
  const int tg = isl ? 32 * (bj >> 2) + 4 * bx + (bj & 3) : 32 * bx + bj;
  u32x4 xa, xc;
  uint32_t ep, r0;
#define P_WAIT(n, ...) asm volatile("s_waitcnt vmcnt(" #n ")\n\tv_lshlrev_b32 %0, 16, v100\n\tv_lshlrev_b32 %1, 16, v101\n\tv_lshlrev_b32 %2, 16, v102" \
    : "=v"(ldt), "=v"(are), "=v"(aim) : : "v100", "v101", "v102" __VA_ARGS__)
#define P_MORE() asm volatile("v_lshlrev_b32 %0, 16, v103\n\tv_lshlrev_b32 %1, 16, v104\n\tv_lshlrev_b32 %2, 16, v105\n\tv_lshlrev_b32 %3, 16, v106" \
    : "=v"(bre), "=v"(bim), "=v"(cre), "=v"(cim) : : "v103", "v104", "v105", "v106")
  TSD;
  TS(0, 0);
  const uint32_t lb0 = (uint32_t)(uintptr_t)(__attribute__((address_space(3))) char*)smem;
  const int i = lane & 15, kq = lane >> 4;
  const u32x4 ones = {0xffffffffu, 0xffffffffu, 0xffffffffu, 0xffffffffu}, zero4 = {0u, 0u, 0u, 0u};
  auto u_again = [&](u32x4& uw, const u32x4& ut, uint32_t uo) {
#pragma unroll 1
    for (int spin = 0; spin < SPIN_CAP; ++spin) {
      const bool na = ((uw[0] | uw[1] | uw[2] | uw[3]) & UMASK) != 0u;
      if (__builtin_expect(!__any(na), 1)) break;
      TSC(19);
      SLEEP(USLEEP);
      if (na) { LD16W(uw, uo); uw ^= ut; }
    }
  };
  constexpr uint32_t S16 = 0x41800000u, LMASK = 0x00070007u;
  auto v_put = [&](u32x4 v, const u32x4& ut, bool other, uint32_t vo) {
    const bool second = __any(other || ((v[0] | v[1] | v[2] | v[3]) & 0x40004000u) != 0u);
    if (__builtin_expect(second, 0)) v = (v & ~LMASK) | (~ut & LMASK); else v = (v & ~LMASK) | ut;
    ST16(vo, v);
    return second;
  };
  auto v_end = [&](uint32_t T, int n) { DRAIN(); if (lane == 0) ST4(O_DN + 16u * n, T); };
  if constexpr (!isl) {
    const int ch = wl >> 1, tl = tid & 255;
    int* evp = (int*)(smem + O_FL + 16 * ch);
    float ldt, are, aim, dsk;
    if constexpr (ROLE == 0) {
      __builtin_amdgcn_s_setprio(KPRIO);
      COps e;
      conv_ops(lb0 + (uint32_t)(ch * TB), lb0 + (uint32_t)(O_UL + ch * UB), lb0 + (uint32_t)(O_IM + ch * GB), lane, e);
      e.o = lb0 + O_ST + (uint32_t)(16 * ((4 * i + 2 * (kq >> 1) + ch) ^ (i >> 1)) + 8 * (kq & 1));
      e.f = lb0 + O_FL;
      TOps t;
      table_ops(lb0 + (uint32_t)(O_IM + 128 * IP * ch), lb0 + (uint32_t)(TB * ch), wl & 1, lane, t);
      uint32_t pz = lb0 + O_IM + (uint32_t)(((lane >> 3) & 1) * GB + pad_at(lane & 7)), im = lb0 + (uint32_t)(O_IM + IP * tl);
      asm volatile("" : "+v"(e.o), "+v"(e.f), "+v"(pz), "+v"(im));
      float bre, bim, cre, cim;
      P_WAIT(0, , "v103", "v104", "v105", "v106", "v107", "v109");
      asm volatile("" : "={v109}"(ep));
      const uint32_t T = ((uint32_t)__builtin_amdgcn_readfirstlane(ep) + 1u) * 0x9E3779B1u;
      if (w == 3 && lane < 8) ST16(O_KEY + 128u * b + 16u * lane, ones);
      P_MORE();
      asm volatile("v_lshlrev_b32 %0, 16, v107" : "=v"(dsk) : : "v107");
      TS(0, 1);
      evp[wl & 1] = gen_y(ldt, are, aim, bre, bim, cre, cim, im, [] {} TSA);
      TS(0, 8);
      BAR();
      TS(0, 2);
      const uint32_t cs = table_tile(t, wl & 1, lane, lb0 + O_FL + 16 * ch, dsk TSA);
      *(uint32_t*)(smem + O_FL + 40 + 4 * ch) = cs;
      DRAIN();
      if ((wl & 1) && lane == 0) asm volatile("global_store_dwordx2 %0, %1, %2 sc1" : : "v"(O_SC + 16u * (2 * tg + ch)), "v"((u32x2){T, cs}), "s"(ws) : "memory");
      TS(0, 9);
      gsync0(lb0 + O_FL + 36, 4);
      TS(0, 3);
      *(u32x4*)(smem + O_ST + 16 * tl) = ones;
      *(u32x4*)(smem + O_ST + 4096 + 16 * tl) = ones;
      if (w == 0) *LP(u32x4, pz) = zero4;
      CONV_RUN(wl & 1, e);
      TS(0, 5);
    } else {
      __builtin_amdgcn_s_setprio(UPRIO);
#pragma unroll
      for (int k = 0; k < 3; ++k) *(u32x4*)(smem + ((16 * (tl + 256 * k) < O_FL) ? 16 * (tl + 256 * k) : O_FL + 32)) = zero4;
      if (w == 7) *(u32x4*)(smem + O_UL + (lane >> 5) * UB + 16 * min(lane & 31, 29)) = zero4;
      uint32_t uo = u_at(tl >> 1, 2 * tg + (tid & 1)), go = O_SU + (uint32_t)(256 * bx + 32 * wl + (lane & 31) + 4 * (lane & 32)) * 128u;
      uint32_t ul = (uint32_t)(O_UL + (tid & 1) * UB + C_U - 16 + 16 * (tl >> 1)), ug = (uint32_t)(O_IM + (tid & 1) * GB + 16 * (tl >> 1)), so = lb0 + O_ST + 16u * (uint32_t)(lane ^ (lane >> 3));
      uint32_t vo = O_V + (uint32_t)tg * VP + 16u * lane;
      asm volatile("" : "+v"(uo), "+v"(go), "+v"(ul), "+v"(ug), "+v"(so), "+v"(vo));
      P_WAIT(1, , "v109");
      TS(0, 1);
      evp[2 + (wl & 1)] = gen_x(ldt, are, aim, smem + O_IM + IP * tl);
      asm volatile("s_waitcnt vmcnt(0)" : "={v109}"(ep));
      const uint32_t T = ((uint32_t)__builtin_amdgcn_readfirstlane(ep) + 1u) * 0x9E3779B1u;
      TS(0, 8);
      BAR();
      TS(0, 2);
      u32x4 ut = {utag(T, 0), utag(T, 1), utag(T, 2), utag(T, 3)};
      asm volatile("" : "+v"(ut));
      SLEEP(GSLEEP);
      u32x2 pv;
      pv[1] = gate(ws, go, T);
      TS(0, 3);
      until(latest(pv[1]) + UD);
      TS(0, 7);
      u32x4 uw, ux;
      asm volatile("global_load_dwordx4 %0, %2, %4 sc1\n\tglobal_load_dwordx4 %1, %3, %4 sc1"
                   : "=&v"(uw), "=&v"(ux) : "v"(uo), "v"(uo + 64 * UP * 32), "s"(ws) : "memory");
      __builtin_amdgcn_s_setprio(FPR);
      asm volatile("s_waitcnt vmcnt(1)" : "+v"(uw));
      uw ^= ut;
      *(u32x4*)(smem + ul + 16) = uw;
      asm volatile("s_waitcnt vmcnt(0)" : "+v"(ux));
      TS(0, 11);
      ux ^= ut;
      *(u32x4*)(smem + ul + 2048 + 16) = ux;
      if (__builtin_expect(__any(((uw[0] | uw[1] | uw[2] | uw[3] | ux[0] | ux[1] | ux[2] | ux[3]) & UMASK) != 0u), 0)) {
        u_again(uw, ut, uo);
        u_again(ux, ut, uo + 64 * UP * 32);
        *(u32x4*)(smem + ul + 16) = uw;
        *(u32x4*)(smem + ul + 2048 + 16) = ux;
      }
      BAR();
      TS(0, 4);
      const u32x4 pw = *(const u32x4*)(smem + ul);
      if (wl < 2) {
        const u32x4 px = *(const u32x4*)(smem + ul + 2048);
        sums<true>(smem + ug, pw, uw);
        sums<false>(smem + ug + 2048, px, ux);
      } else sums<true>(smem + ug, pw, uw);
      count_up(lb0 + O_FL + 32);
      __builtin_amdgcn_s_setprio(SPR);
      TS(0, 5);
      const bool other = *(const uint32_t*)(smem + O_FL + 40 + 4 * (lane & 1)) != S16;
      bool second = false;
#pragma unroll
      for (int k = 0; k < 2; ++k) {
        const uint32_t o = ((k ? 0x3247u : 0x1065u) >> (4 * wl) & 7u) * 1024u;
        u32x4 v;
#pragma unroll 1
        for (int spin = 0; spin < SPIN_CAP; ++spin) {
          asm volatile("ds_read_b128 %0, %1\n\ts_waitcnt lgkmcnt(0)" : "=&v"(v) : "v"(so + o) : "memory");
          if (!__any(max4(v) == 0xffffffffu)) break;
          __builtin_amdgcn_s_sleep(VPS);
        }
        TS(0, 12 + k);
        second |= v_put(v, ut, other, vo + o);
        TS(0, 10 - 4 * k);
      }
      if (__builtin_expect(second, 0)) v_end(T, 4 * tg + wl);
    }
    TSE();
  } else {
  uint32_t* cand = (uint32_t*)(smem + LM);
  uint32_t* sel = cand + 128;
  uint32_t* kl = sel + 48;
  uint32_t* cnt = sel + 64;
  const uint32_t ca = lb0 + LM + 4 * (128 + 64);
  u16* hm = (u16*)(cnt + 12);
  uint32_t T;
  const bool ffn = (FFN < 0) ? bj < 18 : FFN, isx = FFN != 1 && bj >= 24;
  const int fb = 18 * bx + bj;
  constexpr bool ln2 = ROLE == 2;
  const int g0 = tg, g2 = tg;
  u32x4 bw[18];
#pragma unroll
  for (int s = 0; s < 9; ++s) bw[s] = (u32x4){0u, 0u, 0u, 0u};
  int tid2 = threadIdx.x;
  uint32_t ts1 = 0u;
  auto rest = [&] {
    if (ts1 && (int)((uint32_t)__builtin_amdgcn_s_memrealtime() - ts1) > 0) { TS(0, 8); until(ts1 + TSTOP + RD); if (RW) gwait(ca, 4); ts1 = 0u; }
  };
  auto gu_dma = [&](int q0, int q1, auto depth) {
    const char* gs = (const char*)((w < 2) ? wg : wu) + (size_t)((144 * (w & 1)) * (F_ * 2) + fb * 64);
    const uint32_t gv = (uint32_t)((lane >> 2) * (F_ * 2) + 16 * ((lane & 3) ^ (lane >> 5) ^ (w & 2)));
#pragma unroll 1
    for (int q = q0; q < q1; ++q) {
      const int k = (int)((((bj & 1) ? ORD1 : ((unsigned __int128)0x876543210u << 36 | 0x876543210ull)) >> (4 * (q - (q >= 9 ? 9 : 0)) + (q >= 9 ? 36 : 0))) & 15u), h = q >= 9;
      if (q >= RQ) rest();
      dma16(gs + (size_t)(288 * h + 16 * k) * (F_ * 2), gv, lb0 + LW * h + 9216 * w + 1024 * k);
      asm volatile("s_waitcnt vmcnt(%0)" : : "n"(depth.value) : "memory");
    }
  };
  auto down_dma = [&](int k0, int k1, int lane) {
#pragma unroll 1
    for (int k = k0; k < k1; ++k) {
      const int o = 9216 * wl + 2304 * k;
      const char* ds = (const char*)wd + (size_t)(fb * 36864 + o);
      const uint32_t dl = lb0 + o + 32 * ((4 * wl + k + 1) >> 1);
      if (ROLE == 1) rest();
      dma16d(ds, 16u * lane, dl);
      dma16d(ds + 1024, 16u * lane, dl + 1024);
      uint32_t keep;
      asm volatile("s_mov_b32 %0, m0\n\ts_mov_b32 m0, %3\n\ts_mov_b64 exec, 0xffff\n\tglobal_load_lds_dwordx4 %1, %2" DPOL2 "\n\ts_mov_b64 exec, -1\n\ts_mov_b32 m0, %0\n\ts_waitcnt vmcnt(%4)"
                   : "=&s"(keep) : "v"(16u * lane), "s"(ds + 2048), "s"(dl + 2048), "n"(ROLE == 1 ? DDEPTH : 40) : "memory");
    }
  };
  auto gu_read = [&](int h) {
    const uint32_t ra = lb0 + LW * h + (uint32_t)(((lane >> 1) & 1) * 18432 + (8 * kq + ((lane >> 2) & 3)) * 64 + ((w ^ (kq & 1) ^ (lane & 2)) * 16) + 8 * (lane & 1));
#pragma unroll
    for (int s = 0; s < 9; ++s) {
      const u32x2 h0 = trd(ra + 2048 * s), h1 = trd(ra + 2048 * s + 256);
      bw[9 * h + s] = (u32x4){h0[0], h0[1], h1[0], h1[1]};
    }
#pragma unroll
    for (int s = 0; s < 9; ++s) asm volatile("" : "+v"(bw[9 * h + s]));
  };
  uint32_t tv = 0u, tk = 0u;
  const uint32_t vearly = (uint32_t)((VEARLY >> (8 * (g0 >> 5))) & 0xffull);
  auto w_line = [&](int n) {
    const int q = min(lane, 62) / 9, r = min(7 * (n >> 2) + q, 287), h = r >= 144;
    return (const char*)((n & 1) ? wu : wg) + (uint32_t)((r + 144 * (h + ((n >> 1) & 1))) * (F_ * 2) + bx * 1152 + (min(lane, 62) - 9 * q) * 128);
  };
#define TOUCH(pa) asm volatile("global_load_dword v126, %0, off" DPOL4 : : "v"(pa) : "memory", "v126")
  uint32_t wv[5], xv[9];
  auto rows_xw = [&](int lane) {
    u32x4 wa, xr, xs;
    const uint32_t xo = (uint32_t)((8 * g2 + 2 * wl) * (D_ * 2));
    asm volatile("global_load_dwordx4 %0, %5, %9\n\tglobal_load_dword %1, %6, %9 offset:1024\n\t"
                 "global_load_dwordx4 %2, %7, %10\n\tglobal_load_dwordx4 %3, %7, %10 offset:1152\n\tglobal_load_dword %4, %8, %10 offset:1024\n\ts_waitcnt vmcnt(0)"
                 : "=&v"(wa), "=&v"(wv[4]), "=&v"(xr), "=&v"(xs), "=&v"(xv[8])
                 : "v"(16u * lane), "v"(4u * (lane & 31)), "v"(xo + 16u * lane), "v"(xo + 4u * lane + 32u * (lane & 32)), "s"(wr), "s"(x) : "memory");
#pragma unroll
    for (int k = 0; k < 4; ++k) { wv[k] = wa[k]; xv[k] = xr[k]; xv[4 + k] = xs[k]; }
  };
  auto ln1 = [&](int g, int o, uint32_t c0) {
    uint32_t uo = u_at(g, 64 * wl + lane);
    uint32_t ta = lb0 + (uint32_t)(o + ((lane >> 2) & 3) * RP + (64 * wl + 16 * kq + 4 * (lane & 3)) * 2);
    uint32_t la = (uint32_t)(o + 2 * wl * RP + 16 * lane), lt = (uint32_t)(o + (2 * wl + (lane >> 5)) * RP + 1024 + 4 * (lane & 31));
    uint32_t sg = O_SU + (uint32_t)(256 * (lane >> 3) + g) * 128u + 16u * (lane & 7);
    asm volatile("" : "+v"(uo), "+v"(ta), "+v"(la), "+v"(lt), "+v"(sg));
    f32x4 z4 = {0.f, 0.f, 0.f, 0.f};
    asm volatile("" : "+v"(z4));
    asm volatile("s_waitcnt vmcnt(0)" : "={v[100:103]}"(xa), "={v[104:107]}"(xc), "={v108}"(r0), "={v109}"(ep));
    TS(0, 1);
    const uint32_t v[9] = {xa[0], xa[1], xa[2], xa[3], xc[0], xc[1], xc[2], xc[3], r0};
    uint32_t c[9];
    f32x2 tf[9];
    T = ((uint32_t)__builtin_amdgcn_readfirstlane(ep) + 1u) * 0x9E3779B1u;
    if (wl == 3) { const uint32_t now = (uint32_t)__builtin_amdgcn_s_memrealtime() + SD; ST16(sg, ((u32x4){T, now, T, now})); }
    const uint32_t tj = ((T >> (3 * wl)) & 0x00070007u) | (((T >> (12 + wl)) & 0x00010001u) << 14);
    uint32_t tw[3] = {(tj & 0xffffu) * 0x10001u, (tj >> 16) * 0x10001u, 0u};
    tw[2] = (lane < 32) ? tw[0] : tw[1];
    asm volatile("" : "+v"(tw[0]), "+v"(tw[1]), "+v"(tw[2]));
    f32x2 rc, nc;
    float e_;
    ln_stats<false>(v, lane, 0.03125f, tf, z4, rc, nc, e_);
    ln_apply<true>(tf, lane, rc, nc, c);
#pragma unroll
    for (int k = 0; k < 9; ++k) c[k] = (c[k] & ~UMASK) | tw[k >> 2];
    *(u32x4*)(smem + la) = (u32x4){c[0], c[1], c[2], c[3]};
    *(u32x4*)(smem + la + RP) = (u32x4){c[4], c[5], c[6], c[7]};
    *(uint32_t*)(smem + lt) = c[8];
    u32x4 ha, hb;
    {
      uint32_t t, cc, n = SPIN_CAP;
      asm volatile("s_mov_b64 exec, 1\n\tv_mov_b32 %2, 1\n\tds_add_u32 %5, %2\n\ts_mov_b64 exec, -1\n"
                   "1:\n\tds_read_b32 %2, %5\n\tds_read_b64_tr_b16 v[40:41], %6\n\tds_read_b64_tr_b16 v[42:43], %6 offset:%7\n\tds_read_b64_tr_b16 v[44:45], %6 offset:512\n\t"
                   "ds_read_b64_tr_b16 v[46:47], %6 offset:%8\n\ts_sub_u32 %4, %4, 1\n\ts_cbranch_scc1 2f\n\ts_waitcnt lgkmcnt(4)\n\tv_readfirstlane_b32 %3, %2\n\t"
                   "s_cmp_lt_u32 %3, 4\n\ts_cbranch_scc1 1b\n2:\n\ts_waitcnt lgkmcnt(0)"
                   : "=&{v[40:43]}"(ha), "=&{v[44:47]}"(hb), "=&v"(t), "=&s"(cc), "+s"(n) : "v"(c0), "v"(ta), "n"(4 * RP), "n"(512 + 4 * RP) : "memory", "scc");
    }
    ST16(uo, ha);
    ST16(uo + 64 * 128, hb);
    if (wl == 0) {
      const u32x2 h4 = trd(ta + 1024), h5 = trd(ta + 1024 + 4 * RP);
      ST16(uo + 128 * 128, ((u32x4){h4[0], h4[1], h5[0], h5[1]}));
    }
  };
  const int dX = 512 - 24 + 8 * bx + bj;
  if constexpr (ROLE == 2) {
    const int tl = tid - 256;
    if (XOFF && (uint32_t)(bj - 4) < 8u) {
      __builtin_amdgcn_s_setprio(3);
      ln1(tg + 160, 10240, ca + 32);
      TS(0, 2);
    }
    if (!isx && T0) {
      SLEEP(T0SLEEP);
      if (wl < T0) TOUCH(w_line(24 * wl + bj));
      if (T1) { asm volatile("s_waitcnt vmcnt(0)" ::: "memory"); SLEEP(T1SLEEP); if (wl < T1) TOUCH(w_line(24 * (T0 + wl) + bj)); }
    }
    if (isx) {
      __builtin_amdgcn_s_setprio(QPRIO0);
      *(u32x4*)(smem + X_TB + 16 * tl) = zero4;
      *(u32x4*)(smem + X_TB + 16 * min(256 + tl, (TB + C_U) / 16 - 1)) = zero4;
      const int pr = (wl + XROT) & 3;
      COps e;
      conv_ops(lb0 + X_TB, lb0 + X_UL, lb0 + X_IM, lane, e);
      TOps t;
      table_ops(lb0 + X_IM, lb0 + X_TB, wl & 1, lane, t);
      e.o = lb0 + X_ST + (uint32_t)(16 * ((2 * i + (kq >> 1)) ^ ((i >> 2) & 1)) + 8 * (kq & 1));
      e.f = ca + 28 - 32;
      asm volatile("" : "+v"(e.o), "+v"(e.f));
      float ldt, are, aim, bre, bim, cre, cim, dsk;
      P_WAIT(0, , "v103", "v104", "v105", "v106", "v107", "v109");
      asm volatile("" : "={v109}"(ep));
      T = ((uint32_t)__builtin_amdgcn_readfirstlane(ep) + 1u) * 0x9E3779B1u;
      P_MORE();
      asm volatile("v_lshlrev_b32 %0, 16, v107" : "=v"(dsk) : : "v107");
      TS(0, 1);
      __builtin_amdgcn_s_setprio(QPRIO);
      int* evp = (int*)(sel + 44);
      char* im = smem + X_IM + IP * (tl & 127);
      evp[wl] = (wl < 2) ? gen_y(ldt, are, aim, bre, bim, cre, cim, lb0 + X_IM + IP * tl, [] {} TSA) : gen_x(ldt, are, aim, im);
      TS(0, 2);
      __builtin_amdgcn_s_setprio(QPRIO2);
      gsync(ca + 8, 4);
      if (wl < 2) evp[-4 + wl] = (int)table_tile(t, wl & 1, lane, lb0 + LM + 4 * (128 + 44), dsk TSA);
      if (wl < 2) gsync0(ca + 8, 8); else { count_up(ca + 8); SLEEP(XSL); gwait(ca + 8, 8); }
      const uint32_t cs = (uint32_t)__builtin_amdgcn_readfirstlane(evp[-4]);
      if (wl == 2 && lane == 0) asm volatile("global_store_dwordx2 %0, %1, %2 sc1" : : "v"(O_SC + 16u * dX), "v"((u32x2){T, cs}), "s"(ws) : "memory");
      if (wl == 3) *(u32x4*)(smem + X_IM + pad_at(lane & 7)) = zero4;
      TS(0, 3);
      CONV_RUN(2 + pr, e, , "+{v11}"(tid2));
      {
        const int lane = tid2 & 63;
        const u32x4 v = *(const u32x4*)(smem + X_ST + 2048 * pr + 1024 * (lane >> 5) + 16 * ((lane & 31) ^ ((lane >> 3) & 1)));
        if (__builtin_expect(v_put(v, (u32x4){utag(T, 0), utag(T, 1), utag(T, 2), utag(T, 3)}, cs != S16, v_at(dX, 64 * pr + lane)), 0)) {
          DRAIN();
          if (lane == 0) ST4(O_DN + 16u * (4 * (dX - 256) + pr), T);
        }
      }
      TS(0, 6);
    }
    __builtin_amdgcn_s_setprio(PPRIO);
    asm volatile("" : "+v"(tid2));
    if (isx) {
      const int lane = tid2 & 63;
      const u32x4 ra = *(const u32x4*)(smem + X_RW(wl) + 16 * lane), rb = *(const u32x4*)(smem + X_RW(wl) + 1024 + 16 * lane), rw = *(const u32x4*)(smem + X_WR + 16 * lane);
      const u32x2 rc = *(const u32x2*)(smem + X_RW(wl) + 2048 + 8 * lane);
#pragma unroll
      for (int k = 0; k < 4; ++k) { xv[k] = ra[k]; xv[4 + k] = rb[k]; wv[k] = rw[k]; }
      xv[8] = rc[0]; wv[4] = rc[1];
    } else rows_xw(tid2 & 63);
    if (!isx && !ffn) {
      SLEEP(RSLEEP);
#pragma unroll
      for (int k = 0; k < 7; ++k) ST16(O_SL + (uint32_t)((6 * bx + bj - 18) * 1792 + 256 * k - 256 + tid2) * 16u, ones);
      DRAIN();
    }
    if (!isx) BAR();
    const uint32_t tp = (uint32_t)__builtin_amdgcn_readfirstlane(cnt[4]);
    if (!isx) T = (uint32_t)__builtin_amdgcn_readfirstlane(sel[43]);
    tv = tp + UD + VD2 - vearly;
  } else {
    __builtin_amdgcn_s_setprio(3);
    if (tid < 12) cnt[tid] = 0u;
    BAR();
    if (tid < 20) ((uint32_t*)(hm + 320))[tid] = 0u;
    if (tid >= 184) *(u32x4*)(smem + L_SC - 16 * 184 + 16 * tid) = (u32x4){0x41804180u, 0x41804180u, 0x41804180u, 0x41804180u};
    uint32_t sink = 0;
    if (XOFF && isx) {
      asm volatile("s_waitcnt vmcnt(0)" : "={v109}"(ep));
      T = ((uint32_t)__builtin_amdgcn_readfirstlane(ep) + 1u) * 0x9E3779B1u;
    } else ln1(tg, 0, ca + 12);
    TS(0, 2);
    __builtin_amdgcn_s_setprio(NPRIO);
    if (!isx && 3 - w < TB1) TOUCH(w_line(24 * (3 - w) + 23 - bj));
    asm volatile("s_waitcnt vmcnt(0)" : "+v"(sink) : : "memory");
    TS(0, 13);
    if (w == 0) {
      const uint32_t xr = O_XC + (uint32_t)((511 - 2 * (g0 + w)) * 144 + lane) * 16u;
      ST16(xr, ones);
      ST16(xr + 1024u, ones);
      if (lane < 16) ST16(xr + 2048u, ones);
    }
    if (w == 2 && b < 72 && lane < 8) ST16(O_FB + (uint32_t)(8 * b + lane) * 16u, ones);
    if (isx) {
      rows_xw(lane);
      char* sp = smem + X_RW(w) + 16 * lane;
      *(u32x4*)sp = (u32x4){xv[0], xv[1], xv[2], xv[3]};
      *(u32x4*)(sp + 1024) = (u32x4){xv[4], xv[5], xv[6], xv[7]};
      *(u32x2*)(smem + X_RW(w) + 2048 + 8 * lane) = (u32x2){xv[8], wv[4]};
      if (w == 0) *(u32x4*)(smem + X_WR + 16 * lane) = (u32x4){wv[0], wv[1], wv[2], wv[3]};
    }
    SLEEP(ESLEEP);
    uint32_t tu = latest(gate(ws, O_SU + (uint32_t)(256 * bx + 64 * w + 4 * i + (bj & 3)) * 128u, T));
    asm volatile("s_mov_b64 exec, 1\n\tds_max_u32 %0, %1\n\ts_mov_b64 exec, -1" : : "v"(ca + 16), "v"(tu) : "memory");
    if (w == 0) sel[43] = T;
    if (isx) {
      const uint32_t xo = u_at(tid, dX);
      char* sm = smem + X_UL + C_U - 16 + 16 * tid;
      const u32x4 ut = {utag(T, 0), utag(T, 1), utag(T, 2), utag(T, 3)};
      __builtin_amdgcn_s_setprio(3);
      until(tu + UD + XD);
      u32x4 uw;
      asm volatile("global_load_dwordx4 %0, %1, %2 sc1" : "=&v"(uw) : "v"(xo), "s"(ws) : "memory");
      asm volatile("s_waitcnt vmcnt(0)" : "+v"(uw));
      uw ^= ut;
      u_again(uw, ut, xo);
      *(u32x4*)(sm + 16) = uw;
      if (XF) { gsync0(ca + 12, XOFF ? 4 : 8); gwait(ca + 8, 8); } else BAR();
      if (w < 3) {
        const u32x4 pw = *(const u32x4*)sm;
        if (w < 2) sums<true>(smem + X_IM + 16 * tid, pw, uw); else sums<false>(smem + X_IM + 16 * tid, pw, uw);
        count_up(ca + 28);
      }
      if (XF) BAR();
    } else {
      until(tu + UD + WD);
      BAR();
    }
    tu = (uint32_t)__builtin_amdgcn_readfirstlane(cnt[4]);
    TS(0, 3);
    __builtin_amdgcn_s_setprio(DPRIO);
    if (ffn) {
      ts1 = (tu + UD + VD2 - TSTOP) | 1u;
      gu_dma(0, N0, IC<40>{});
      gu_dma(N0, NA, IC<WDEPTH>{});
      asm volatile("s_waitcnt vmcnt(%0)" : : "n"(NA - 9) : "memory");
      gsync0(ca + 4, 4);
      gu_read(0);
      asm volatile("s_waitcnt lgkmcnt(0)" ::: "memory");
      count_up(ca + 36);
      TS(0, 4);
      gu_dma(NA, 18, IC<WDEPTH2>{});
      gwait(ca + 36, 4);
      down_dma(0, KN, lane);
      asm volatile("s_waitcnt vmcnt(%0)" : : "n"(3 * KN) : "memory");
      count_up(ca + 4);
      TS(0, 7);
    } else {
      if (!isx && C3) {
        const int n = 6 * min(tid, 191) + bj - 18, r = n >> 2;
        const char* wb = (const char*)((n & 1) ? wu : wg) + bx * 1152;
        const int c0 = (3 - (int)((uintptr_t)wb >> 7)) & 7;
        wb += (uint32_t)((r + 144 * ((r >= 144) + ((n >> 1) & 1))) * (F_ * 2) + 128 * c0);
        TOUCH(wb);
        if (c0 == 0) TOUCH(wb + 1024);
      }
      auto d_line = [&](int k) {
        const int n = min((k * 14 + bj - 18) * 64 + lane, 5183), q = n / 1296, r = n - 1296 * q;
        return (const char*)wd + (uint32_t)(bx * 5184 + (r / 18) * 72 + 18 * (HREV ? 3 - q : q) + r % 18) * 128u;
      };
      int k = w;
      if (w < HW) {
#pragma unroll 1
        for (; k < 6 && (int)((uint32_t)__builtin_amdgcn_s_memrealtime() - (tu + UD + VD2 - HSTOP)) < 0; k += HW) { TOUCH(d_line(k)); DRAIN(); }
      }
      TS(0, 4);
      until(tu + UD + VD2 + RD2);
      if (RW) gwait(ca, 4);
      if (w < HW) {
#pragma unroll 1
        for (; k < 6; k += HW) { TOUCH(d_line(k)); DRAIN(); }
      }
      TS(0, 7);
    }
    tv = tu + UD + VD2 - vearly;
    tk = tu + UD + VD2 + KD;
  }
  {
  const int tid = tid2, lane = tid & 63, i = lane & 15, kq = lane >> 4;
  __builtin_amdgcn_s_setprio(3);
  const int PL = ffn ? 6400 : LW + 6656;
  f32x2 xf[9];
  f32x4 za = {0.f, 0.f, 0.f, 0.f}, zb = za;
  uint32_t al = O_XC + 64u * (i >> 3) + 16u * kq + 128u * w;
  uint32_t ha = (ROLE == 1) ? (uint32_t)((4 * (kq & 1) + 2 * (kq >> 1) + (i >> 3)) * 80 + 16 * w + 2 * (i & 7)) : 0u;
  uint32_t h0a = (uint32_t)(min(i, 8) * 80 + 16 * kq), h1a = (uint32_t)(min(i ^ 8, 8) * 80 + 16 * kq);
  uint32_t so = O_SL + (uint32_t)fb * SP + ((ROLE == 2) ? 16u * tid - 4096u : 8192u + 16u * lane);
  uint32_t da = lb0 + (uint32_t)((8 * kq + ((lane >> 2) & 3)) * 1152 + 32 * ((8 * kq + ((lane >> 2) & 3) + 2) >> 2) + 8 * (lane & 3) + ((ROLE == 2) ? 128 * wl : 1024));
  if (ROLE == 2) asm volatile("" : "+v"(h0a), "+v"(h1a), "+v"(so), "+v"(da)); else asm volatile("" : "+v"(ha), "+v"(al), "+v"(da));
  uint32_t hv[9] = {}, nv[9] = {}, keya = 0u, keyb = 0u;
  int rka = 0, rkb = 0;
  auto put_row = [&](const uint32_t* r, int j, const void* base, uint32_t ro) {
    const u32x4 tl = {r[8], (uint32_t)DPP_I((int)r[8], 0x55, 0xf), (uint32_t)DPP_I((int)r[8], 0xaa, 0xf), (uint32_t)DPP_I((int)r[8], 0xff, 0xf)};
    asm volatile("global_store_dwordx4 %0, %1, %2 sc1\n\ts_nop 1" : : "v"(ro + 16u * lane), "v"((u32x4){r[4 * j], r[4 * j + 1], r[4 * j + 2], r[4 * j + 3]}), "s"(base) : "memory");
    if ((lane & 35) == 32 * j) asm volatile("global_store_dwordx4 %0, %1, %2 offset:1024 sc1\n\ts_nop 1" : : "v"(ro + 4u * (lane & 31)), "v"(tl), "s"(base) : "memory");
  };
  if (ln2) {
    const int po = PL - 2560 * (ROLE == 1);
    uint32_t pa = (uint32_t)(po + 9216 * wl + 32 * lane), pb = (uint32_t)(po + 9216 * wl + 2048 + 8 * (lane & 31)), ps = (lane < 32) ? 0u : 16u;
    uint32_t kid = (uint32_t)(((255 - g2) << 5) | (7 - 2 * wl - (lane >> 5)));
    const int tl = tid - 256, t2 = tl + 128, d1 = 64 * (tl / 48) + tl % 48, d3 = 64 * (t2 / 48) + t2 % 48;
    const int d2 = (tl < 128) ? 64 * (tl >> 4) + 48 + (tl & 15) : min(384 + tl, 575);
    uint32_t vo = v_at(d1, g0), vo2 = v_at(d2, g0), vo3 = v_at(d3, g0), px = (uint32_t)(PL + 4 * d1), py = (uint32_t)(PL + 4 * d2), pz = (uint32_t)(PL + 4 * d3);
#pragma unroll
    for (int k = 0; k < 9; ++k) xf[k] = (f32x2){bflo(xv[k]), bfhi(xv[k])};
    asm volatile("" : "+v"(xf[0]), "+v"(xf[1]), "+v"(xf[2]), "+v"(xf[3]), "+v"(xf[4]), "+v"(xf[5]), "+v"(xf[6]), "+v"(xf[7]), "+v"(xf[8]));
    asm volatile("" : "+v"(za), "+v"(zb), "+v"(pa), "+v"(pb), "+v"(ps), "+v"(kid), "+v"(vo), "+v"(vo2), "+v"(vo3), "+v"(px), "+v"(py), "+v"(pz));
    u32x4 ut = {utag(T, 0), utag(T, 1), utag(T, 2), utag(T, 3)};
    float sv[10], ws_ = dot2(wv[4], 0x3f803f80u, 0.f) * 0.5f;
#pragma unroll
    for (int k = 0; k < 4; ++k) ws_ = dot2(wv[k], 0x3f803f80u, ws_);
    ws_ = wave_sum(ws_);
#pragma unroll
    for (int k = 0; k < 10; ++k) sv[k] = 16.f;
    uint32_t ka_ = lb0 + LM + 4u * (128 + 48 + 2 * wl + (lane >> 5)), kb_ = lb0 + LM + 4u * (128 + 48);
    uint32_t ko_ = O_KEY + (uint32_t)(256 * (lane & 7) + 8 * (g2 & 31) + (g2 >> 5)) * 16u;
    asm volatile("" : "+v"(ut), "+v"(ws_), "+v"(sv[0]), "+v"(sv[1]), "+v"(sv[2]), "+v"(sv[3]), "+v"(sv[4]), "+v"(sv[5]), "+v"(sv[6]), "+v"(sv[7]), "+v"(sv[8]), "+v"(sv[9]),
                 "+v"(ka_), "+v"(kb_), "+v"(ko_));
    if (ffn) gwait(ca + 36, 4);
    TS(0, 7);
    until(tv);
    TS(0, 8);
    u32x4 va, vc = ut, vb = ut;
    asm volatile("global_load_dwordx4 %0, %2, %4 sc1\n\ts_cmp_lt_u32 %5, 2\n\ts_cbranch_scc1 1f\n\tglobal_load_dwordx4 %1, %3, %4 sc1\n1:"
                 : "=&v"(va), "+v"(vc) : "v"(vo), "v"(vo3), "s"(ws), "s"(wl) : "memory", "scc");
    until(tv + V2D);
    asm volatile("s_cmp_eq_u32 %5, 3\n\ts_cbranch_scc1 1f\n\tglobal_load_dwordx4 %2, %3, %4 sc1\n\ts_waitcnt vmcnt(1)\n\ts_branch 2f\n1:\n\ts_waitcnt vmcnt(0)\n2:"
                 : "+v"(va), "+v"(vc), "+v"(vb) : "v"(vo2), "s"(ws), "s"(wl) : "memory", "scc");
    auto turn1 = [&] {
#pragma unroll
      for (int e = 0; e < 4; ++e) *(uint32_t*)(smem + px + 9216 * e) = va[e];
      if (wl >= 2) {
#pragma unroll
        for (int e = 0; e < 4; ++e) *(uint32_t*)(smem + pz + 9216 * e) = vc[e];
      }
    };
    auto turn = [&] {
      if (wl < 3) {
#pragma unroll
        for (int e = 0; e < 4; ++e) *(uint32_t*)(smem + py + 9216 * e) = vb[e];
      }
    };
    va ^= ut; vc ^= ut;
    turn1();
    asm volatile("s_waitcnt vmcnt(0)" : "+v"(vb) : : "memory");
    vb ^= ut;
    turn();
    const u32x4 vs = va | vb | vc;
    if (__builtin_expect(__any(((vs[0] | vs[1] | vs[2] | vs[3]) & UMASK) != 0u), 0)) {
#pragma unroll 1
      for (int spin = 0; spin < 4; ++spin) {
        const bool na = ((va[0] | va[1] | va[2] | va[3]) & UMASK) != 0u, nb = ((vb[0] | vb[1] | vb[2] | vb[3]) & UMASK) != 0u, nc = ((vc[0] | vc[1] | vc[2] | vc[3]) & UMASK) != 0u;
        if (!__any(na || nb || nc)) break;
        TSC(16);
        if (na) LD16(va, vo);
        if (nb) LD16(vb, vo2);
        if (nc) LD16(vc, vo3);
        asm volatile("s_waitcnt vmcnt(0)" : "+v"(va), "+v"(vb), "+v"(vc) : : "memory");
        if (na) va ^= ut;
        if (nb) vb ^= ut;
        if (nc) vc ^= ut;
      }
      auto again = [&](u32x4& v, uint32_t o, int d) {
        const int J = g0 >> 5;
        const uint32_t dn = O_DN + 16u * (uint32_t)((d < 512) ? 4 * (d >> 1) + ((0x01013232 >> (4 * J)) & 3) : 4 * (d - 256) + (J >> 1));
        bool pend = ((v[0] | v[1] | v[2] | v[3]) & UMASK) != 0u;
#pragma unroll 1
        for (int spin = 0; spin < SPIN_CAP && __any(pend); ++spin) {
          TSC(16);
          SLEEP(PSLEEP);
          if (pend) {
            uint32_t fin = ~T;
            if ((v[0] & v[1] & v[2] & v[3] & LMASK) == LMASK)
              asm volatile("global_load_dword %0, %1, %2 sc1\n\ts_waitcnt vmcnt(0)" : "=&v"(fin) : "v"(dn), "s"(ws) : "memory");
            LD16W(v, o);
            v ^= ut;
            pend = ((v[0] | v[1] | v[2] | v[3]) & UMASK) != 0u;
            if (pend && fin == T) {
              u32x2 sc;
              for (int k = 0; k < SPIN_CAP; ++k) {
                asm volatile("global_load_dwordx2 %0, %1, %2 sc1\n\ts_waitcnt vmcnt(0)" : "=&v"(sc) : "v"(O_SC + 16u * d), "s"(ws) : "memory");
                if (sc[0] == T) break;
              }
              *(u16*)(smem + L_SC + 2 * d) = (u16)(sc[1] >> 16);
              cnt[10] = 1u;
              v = (v ^ ut) & ~LMASK;
              pend = false;
            }
          }
        }
      };
      again(va, vo, d1); again(vc, vo3, d3); again(vb, vo2, d2);
      turn1();
      turn();
    }
    u32x4 a0, a1;
    u32x2 a2, cf;
    {
      uint32_t cc, n = SPIN_CAP;
      asm volatile("s_mov_b64 exec, 1\n\tv_mov_b32 v90, 1\n\tds_add_u32 %6, v90\n\ts_mov_b64 exec, -1\n"
                   "1:\n\tds_read2_b32 %3, %6 offset1:10\n\tds_read_b128 %0, %7\n\tds_read_b128 %1, %7 offset:16\n\tds_read_b64 %2, %8\n\t"
                   "s_sub_u32 %5, %5, 1\n\ts_cbranch_scc1 2f\n\ts_waitcnt lgkmcnt(3)\n\tv_readfirstlane_b32 %4, v90\n\t"
                   "s_cmp_lt_u32 %4, 4\n\ts_cbranch_scc1 1b\n2:\n\ts_waitcnt lgkmcnt(0)"
                   : "=&v"(a0), "=&v"(a1), "=&v"(a2), "=&{v[90:91]}"(cf), "=&s"(cc), "+s"(n) : "v"(ca), "v"(lb0 + pa), "v"(lb0 + pb) : "memory", "scc");
    }
    TS(0, 9);
    if (__builtin_expect(__builtin_amdgcn_readfirstlane(cf[1]) != 0, 0)) {
      const u32x4 q = *(const u32x4*)(smem + L_SC + 16 * lane);
      const uint32_t r = *(const uint32_t*)(smem + L_SC + 1024 + 4 * (lane & 31));
#pragma unroll
      for (int k = 0; k < 4; ++k) { sv[2 * k] = bflo(q[k]); sv[2 * k + 1] = bfhi(q[k]); }
      sv[8] = bflo(r); sv[9] = bfhi(r);
    }
    const uint32_t av[10] = {a0[0], a0[1], a0[2], a0[3], a1[0], a1[1], a1[2], a1[3], a2[0] >> ps, a2[1] >> ps};
    f32x2 h[9];
#pragma unroll
    for (int k = 0; k < 10; ++k) {
      asm("v_fma_mix_f32 %0, %1, %2, %3 op_sel_hi:[1,0,0]" : "=v"(h[(k < 8) ? k >> 1 : 8][k & 1]) : "v"(av[k]), "v"(sv[k]), "v"(xf[(k < 8) ? k >> 1 : 8][k & 1]));
      if (k < 8) asm("v_fma_mix_f32 %0, %1, %2, %3 op_sel:[1,0,0] op_sel_hi:[1,0,0]" : "=v"(h[4 + (k >> 1)][k & 1]) : "v"(av[k]), "v"(sv[k]), "v"(xf[4 + (k >> 1)][k & 1]));
    }
#pragma unroll
    for (int k = 0; k < 9; ++k) hv[k] = pack2(h[k][0], h[k][1]);
    const float da = dot2(hv[2], wv[2], dot2(hv[0], wv[0], zb[0])) + dot2(hv[3], wv[3], dot2(hv[1], wv[1], zb[1]));
    const float db = dot2(hv[6], wv[2], dot2(hv[4], wv[0], zb[2])) + dot2(hv[7], wv[3], dot2(hv[5], wv[1], zb[3]));
    float dt = dot2(hv[8], wv[4], fold32(da, db));
    f32x2 rc, nc;
    ln_stats<true>(hv, lane, 1.f, xf, za, rc, nc, dt);
    dt = (lane < 32) ? dt * rc[0] + ws_ * nc[0] : dt * rc[1] + ws_ * nc[1];
    const uint32_t bb = pack2(dt, dt) & 0xffff0000u;
    const uint32_t kk = (bb ^ (((uint32_t)((int)bb >> 31) & 0x7fff0000u) | 0x80000000u)) | kid;
    u32x4 ka, kb;
    uint32_t old;
    asm volatile("s_mov_b64 exec, %5\n\tds_write_b32 %3, %4\n\ts_mov_b64 exec, 1\n\tv_mov_b32 %2, 1\n\tds_add_rtn_u32 %2, %7, %2\n\ts_mov_b64 exec, -1\n\t"
                 "ds_read_b128 %0, %6\n\tds_read_b128 %1, %6 offset:16\n\ts_waitcnt lgkmcnt(0)"
                 : "=&v"(ka), "=&v"(kb), "=&v"(old) : "v"(ka_), "v"(kk), "s"(0x8000000080000000ull), "v"(kb_), "v"(ca) : "memory");
    if (__builtin_amdgcn_readfirstlane(old) == 7) {
      uint32_t m1 = max(max(ka[0], ka[1]), ka[2]), m2 = med3u(ka[0], ka[1], ka[2]), m3 = min(min(ka[0], ka[1]), ka[2]);
#pragma unroll
      for (int e = 0; e < 5; ++e) {
        const uint32_t kx = e ? kb[e - 1] : ka[3];
        m3 = med3u(m2, m3, kx); m2 = med3u(m1, m2, kx); m1 = max(m1, kx);
      }
      if (lane < 8) ST16(ko_, ((u32x4){m1 | 24u, m2 | 16u, m3 | 8u, T & ~1u}));
    }
    TS(0, 10);
    keya = (uint32_t)__builtin_amdgcn_readlane(kk, 31); keyb = (uint32_t)__builtin_amdgcn_readlane(kk, 63);
    ln_apply<false>(xf, lane, rc, nc, nv);
    gwait(ca, 8);
    if (wl == 1) {
      const u32x4 kv = *(const u32x4*)&kl[4 * (lane & 1)];
      if (lane < 2) ST16(O_KF + (uint32_t)(2 * g2 + lane) * 16u, ((u32x4){__builtin_amdgcn_perm(kv[1], kv[0], 0x07060302u), T, __builtin_amdgcn_perm(kv[3], kv[2], 0x07060302u), T}));
      if (ROLE == 2 && b == 0 && lane == 0) ST4(O_EP, T * 0x0E8B2F51u);
    }
    const uint32_t ko = kl[lane & 7];
    rka = FORCEFB + __popcll(__ballot(ko > keya) & 0xffull); rkb = FORCEFB + __popcll(__ballot(ko > keyb) & 0xffull);
#pragma unroll
    for (int k = 0; k < 9; ++k) {
      const f32x2 o = xf[k] + (f32x2){bflo(nv[k]), bfhi(nv[k])};
      hv[k] = pack2(o[0], o[1]);
      nv[k] = min(nv[k], 0xfffffffeu);
    }
    if (rka < 2) put_row(nv, 0, ws, O_XC + (uint32_t)(1023 - 4 * g2 - rka) * 1152u);
    if (rkb < 2) put_row(nv, 1, ws, O_XC + (uint32_t)(1023 - 4 * g2 - rkb) * 1152u);
  }
  uint32_t k3rd = 0u, k4th = 0u;
  if constexpr (ROLE == 1) {
    u32x4 key;
    const uint32_t kof = O_KEY + (uint32_t)(256 * bx + tid) * 16u;
    until(tk);
    if (!ffn) SLEEP(NSLEEP);
    LD16(key, kof);
    if (ffn) {
      gwait0(ca + 4, 8);
      gu_read(1);
    }
    asm volatile("s_waitcnt vmcnt(0)" : "+v"(key) : : "memory");
    if (__builtin_expect(__any(key[3] != (T & ~1u)), 0)) {
#pragma unroll 1
      for (int spin = 0; spin < SPIN_CAP; ++spin) {
        const bool need = key[3] != (T & ~1u);
        if (!__any(need)) break;
        TSC(15);
        SLEEP(KBACK);
        if (need) LD16W(key, kof);
      }
    }
    TS(0, 11);
    const uint32_t m1 = oct_maxu(key[0]);
    const bool hit = key[0] == m1;
    const uint32_t k0 = hit ? key[1] : key[0], k1 = hit ? key[2] : key[1];
    const uint32_t m2 = oct_maxu(k0);
    if ((lane & 7) == 0) *(u32x2*)&cand[tid >> 2] = (u32x2){m1, m2};
    k3rd = (k0 == m2) ? k1 : k0;
    k4th = (k0 == m2) ? (hit ? 0u : key[2]) : k1;
    if (RPF && !ffn && w == (bj & 3)) {
      const uint32_t r1 = O_XC + ((m1 >> 3) & 0x3ffu) * 1152u + 128u * (lane & 7), r2 = O_XC + ((m2 >> 3) & 0x3ffu) * 1152u + 128u * (lane & 7);
      asm volatile("global_load_dword v126, %0, %3 sc1\n\tglobal_load_dword v126, %1, %3 sc1\n\tglobal_load_dword v126, %2, %3 offset:1024 sc1"
                   : : "v"(r1), "v"(r2), "v"(((lane & 1) ? r1 : r2) - 128u * (lane & 7)), "s"(ws) : "memory", "v126");
    }
  }
  auto pick = [&](const uint32_t* cd, uint32_t* sl) {
    BAR();
    const uint32_t c = cd[lane], own = cd[8 * w + (lane & 7)];
    const u32x4 la = *(const u32x4*)&cd[8 * w], lb = *(const u32x4*)&cd[8 * w + 4];
    uint32_t r = C_;
    asm("v_cmp_gt_u32 s[40:41], %1, %2\n\tv_cmp_gt_u32 s[42:43], %1, %3\n\tv_cmp_gt_u32 s[44:45], %1, %4\n\tv_cmp_gt_u32 s[46:47], %1, %5\n\t"
        "v_cmp_gt_u32 s[48:49], %1, %6\n\tv_cmp_gt_u32 s[50:51], %1, %7\n\tv_cmp_gt_u32 s[52:53], %1, %8\n\tv_cmp_gt_u32 s[54:55], %1, %9\n\t"
        "s_bcnt1_i32_b64 s40, s[40:41]\n\ts_bcnt1_i32_b64 s42, s[42:43]\n\tv_writelane_b32 %0, s40, 0\n\ts_bcnt1_i32_b64 s44, s[44:45]\n\t"
        "v_writelane_b32 %0, s42, 1\n\ts_bcnt1_i32_b64 s46, s[46:47]\n\tv_writelane_b32 %0, s44, 2\n\ts_bcnt1_i32_b64 s48, s[48:49]\n\t"
        "v_writelane_b32 %0, s46, 3\n\ts_bcnt1_i32_b64 s50, s[50:51]\n\tv_writelane_b32 %0, s48, 4\n\ts_bcnt1_i32_b64 s52, s[52:53]\n\t"
        "v_writelane_b32 %0, s50, 5\n\ts_bcnt1_i32_b64 s54, s[54:55]\n\tv_writelane_b32 %0, s52, 6\n\tv_writelane_b32 %0, s54, 7"
        : "+v"(r) : "v"(c), "v"(la[0]), "v"(la[1]), "v"(la[2]), "v"(la[3]), "v"(lb[0]), "v"(lb[1]), "v"(lb[2]), "v"(lb[3])
        : "scc", "s40", "s41", "s42", "s43", "s44", "s45", "s46", "s47", "s48", "s49", "s50", "s51", "s52", "s53", "s54", "s55");
    sl[min(r, (uint32_t)C_)] = own;
    BAR();
  };
  auto rows = [&](uint32_t ao, auto&& meanwhile) {
    u32x4 aa, ab, ac = __builtin_bit_cast(u32x4, zb);
    asm volatile("global_load_dwordx4 %0, %3, %4 sc1\n\tglobal_load_dwordx4 %1, %3, %4 offset:512 sc1\n\t"
                 "s_cmp_lg_u32 %5, 0\n\ts_cbranch_scc1 1f\n\tglobal_load_dwordx4 %2, %3, %4 offset:1024 sc1\n1:"
                 : "=&v"(aa), "=&v"(ab), "+v"(ac) : "v"(ao), "s"(ws), "s"(w) : "memory", "scc");
    __builtin_amdgcn_sched_barrier(0);
    meanwhile();
    asm volatile("s_waitcnt vmcnt(0)" : "+v"(aa), "+v"(ab), "+v"(ac) : : "memory");
    if (__builtin_expect(__any(max(max(max4(aa), max4(ab)), max4(ac)) == 0xffffffffu), 0)) {
#pragma unroll 1
      for (int spin = 0; spin < SPIN_CAP; ++spin) {
        const bool na = max4(aa) == 0xffffffffu, nb = max4(ab) == 0xffffffffu, nc = max4(ac) == 0xffffffffu;
        if (!__any(na || nb || nc)) break;
        TSC(14);
        SLEEP(PSLEEP);
        if (na) LD16W(aa, ao);
        if (nb) LD16W(ab, ao + 512u);
        if (nc) LD16W(ac, ao + 1024u);
      }
    }
    *(u32x4*)(smem + LA + 16 * tid) = aa;
    *(u32x4*)(smem + LA + 4096 + 16 * tid) = ab;
    if (w == 0) *(u32x4*)(smem + LA + 8192 + 16 * tid) = ac;
  };
  auto get_bd = [&](int t0, int t1) {
#pragma unroll
    for (int t = t0; t < t1; ++t) {
      const u32x2 h0 = trd(da + 512 * (t >> 2) + 32 * (t & 3)), h1 = trd(da + 512 * (t >> 2) + 32 * (t & 3) + 4608 + 32);
      bw[t] = (u32x4){h0[0], h0[1], h1[0], h1[1]};
    }
#pragma unroll
    for (int t = t0; t < t1; ++t) asm volatile("" : "+v"(bw[t]));
  };
  pick(cand, sel);
  uint32_t mykey = sel[lane & 7], ro = ((mykey >> 3) & 0x3ffu) * 1152u;
  TS(0, 12);
  auto check = [&] {
    cnt[(k3rd > (uint32_t)__builtin_amdgcn_readlane(mykey, 7)) ? 5 : 6] = 1u;
    const uint32_t m3 = oct_maxu(k3rd);
    if (k3rd == m3) k3rd = k4th;
    if ((lane & 7) == 0) cand[72 + (tid >> 3)] = m3;
    if (w == 0 && lane < 32) cand[(lane < 8) ? 64 + lane : 96 + lane] = (lane < 8) ? mykey : 0u;
  };
  if constexpr (ROLE == 1) {
    if (ffn && !FORCEFB) rows(ro + al, check); else { check(); DRAIN(); }
  } else if (ffn) {
    if (KN < 4) { SLEEP(LSLEEP); down_dma(KN, 4, lane); } else get_bd(0, 8);
  }
  BAR();
  TS(0, 5);
  auto put_out = [&](int j, uint32_t k0, int rk) {
    const uint64_t mine = __ballot(((mykey ^ k0) & ~24u) == 0u) & 0xffull;
    if (mine == 0ull) put_row(hv, j, out, (uint32_t)((8 * g2 + 2 * wl + j) * (D_ * 2)));
    else if (rk >= 2) put_row(nv, j, ws, O_FB + 1152u * (uint32_t)__builtin_ctzll(mine));
  };
  auto rest_out = [&] { put_out(0, keya, rka); put_out(1, keyb, rkb); };
  f32x4 lo, hi;
  auto gate_up = [&] {
    u32x4 av[5];
    const uint32_t aa = lb0 + LA + 16u * lane;
#pragma unroll
    for (int j = 0; j < 5; ++j) asm volatile("ds_read_b128 %0, %1 offset:%2" : "=v"(av[j]) : "v"(aa), "n"(1024 * j) : "memory");
    lo = (f32x4){0.f, 0.f, 0.f, 0.f}; hi = lo;
#pragma unroll
    for (int j = 0; j < 9; ++j) {
      if (j < 5) asm volatile("s_waitcnt lgkmcnt(4)" : "+v"(av[j % 5]));
      else if (j == 5) asm volatile("s_waitcnt lgkmcnt(2)" : "+v"(av[0]), "+v"(av[1]));
      else if (j == 7) asm volatile("s_waitcnt lgkmcnt(0)" : "+v"(av[2]), "+v"(av[3]));
      lo = MMB(av[j % 5], bw[2 * j], lo);
      hi = MMB(av[j % 5], bw[2 * j + 1], hi);
      __builtin_amdgcn_sched_barrier(0);
      if (j < 4) asm volatile("ds_read_b128 %0, %1 offset:%2" : "=v"(av[j]) : "v"(aa), "n"(1024 * (j + 5)) : "memory");
    }
  };
  uint32_t fl;
  asm volatile("ds_read_b32 %0, %1" : "=v"(fl) : "v"(ca + 20) : "memory");
  if (ROLE == 1) gate_up();
  asm volatile("s_waitcnt lgkmcnt(0)" : "+v"(fl));
  const bool slow = FORCESLOW || FORCEFB || __builtin_amdgcn_readfirstlane(fl) != 0;
  if (__builtin_expect(slow, 0)) {
    TSC(17);
    pick(cand + 64, sel + 16);
    mykey = sel[16 + (lane & 7)];
    ro = ((mykey >> 3) & 0x3ffu) * 1152u;
    if (ROLE == 1 && (k3rd > (uint32_t)__builtin_amdgcn_readlane(mykey, 7) || (mykey & 24u) == 8u)) cnt[11] = 1u;
    BAR();
    if (FORCESLOW > 1 || FORCEFB || __builtin_amdgcn_readfirstlane(cnt[11]) != 0) {
    u32x4 key;
#pragma unroll 1
    for (int spin = 0; spin < SPIN_CAP; ++spin) {
      LD16W(key, O_KF + 16u * tid);
      if (!__any(key[1] != T || key[3] != T)) break;
      SLEEP(KBACK);
    }
    const uint32_t ib = (uint32_t)(((255 - (tid >> 1)) << 5) | (7 - 4 * (tid & 1)));
    uint32_t k0 = (key[0] << 16) | ib, k1 = (key[0] & 0xffff0000u) | (ib - 1), k2 = (key[2] << 16) | (ib - 2), k3 = (key[2] & 0xffff0000u) | (ib - 3);
    CEX(k0, k1) CEX(k2, k3) CEX(k0, k2) CEX(k1, k3) CEX(k1, k2)
#pragma unroll 1
    for (int c = 0; c < C_; ++c) {
      const uint32_t mx = wave_maxu(k0);
      const bool hit = (k0 == mx);
      k0 = hit ? k1 : k0; k1 = hit ? k2 : k1; k2 = hit ? k3 : k2; k3 = hit ? 0u : k3;
      if (lane == 0) cand[64 + 8 * w + c] = mx;
    }
    pick(cand + 64, sel + 32);
    mykey = sel[32 + (lane & 7)];
    const uint32_t okey = sel[32 + (lane >> 3)];
    const uint64_t same = __ballot(((mykey ^ okey) & 0x1fe0u) == 0u && okey > mykey);
    const int sl = FORCEFB + __popcll((same >> (lane & 7)) & 0x0101010101010101ull);
    ro = (sl < 2) ? (((mykey >> 3) & 0x3ffu) + 3 - sl) * 1152u : O_FB - O_XC + 1152u * (lane & 7);
    }
    if (ln2) rest_out();
    if (ffn) {
      if (ROLE == 1) rows(ro + al, [] {});
      BAR();
      if (ROLE == 1) gate_up();
    }
  }
  if (OPOS == 0 && ROLE == 2 && ffn && !slow) { SLEEP(OD); rest_out(); }
  if (!ffn) {
    if (ln2 && !slow) { SLEEP(OD2); rest_out(); }
    TSE();
    return;
  }
  uint32_t fx = 0, fv = 0, fn = 0, fo = 0, sa[9];
  float rs, hx, xs;
  u32x4 se;
  if constexpr (ROLE == 1) {
    const f32x4 lh = (lane < 32) ? lo : hi;
    const float fa = fold32(lh[0], lh[2]), fb2 = fold32(lh[1], lh[3]);
    const float g = __int_as_float(__builtin_amdgcn_update_dpp(__float_as_int(fa), __float_as_int(fb2), 0x128, 0xf, 0xc, false));
    const float u = __int_as_float(__builtin_amdgcn_update_dpp(__float_as_int(fb2), __float_as_int(fa), 0x128, 0xf, 0x3, false));
    const uint32_t gu = pack2(g, u);
    const float gp = bflo(gu);
    const float sg = rbf(gp * __builtin_amdgcn_rcpf(1.f + __expf(-gp)));
    *(u16*)((char*)hm + ha) = f2bf(sg * bfhi(gu));
  } else if (KN < 4) {
    DRAIN();
    gsync0(ca, 12);
    get_bd(0, 8);
  }
  BAR();
  TS(0, 6);
  if (ROLE == 2 || w == 2) {
    const u32x4 a0 = *(const u32x4*)((char*)hm + h0a), a1 = *(const u32x4*)((char*)hm + h1a);
    if (ROLE == 1) get_bd(0, 4);
    const f32x4 z = {0.f, 0.f, 0.f, 0.f};
    f32x4 o[4];
#pragma unroll
    for (int t = 0; t < 2 * ROLE; ++t) o[t] = MMB(a1, bw[2 * t + 1], MMB(a0, bw[2 * t], z));
#pragma unroll
    for (int t = 0; t < ROLE; ++t) {
      u32x4 sv;
#pragma unroll
      for (int r = 0; r < 4; ++r) sv[r] = min(pack2(o[2 * t][r], o[2 * t + 1][r]), 0xfffffffeu);
      ST16(so + 4096u * t, sv);
    }
    TS(0, 13);
  }
  if (OPOS == 1 && ROLE == 2) {
    SLEEP(OSLEEP);
    if (!slow) rest_out();
  }
  if (ROLE == 1 && w == 1) {
    const int q = (fb & 15) >> 2, ts = 4 * (q & 1) + ((i & 7) >> 1);
    const int dd = 64 * (fb >> 4) + 16 * (2 * (i & 1) + (q >> 1)) + 4 * (fb & 3) + 2 * (kq & 1) + (kq >> 1);
    const uint32_t fkey = (uint32_t)__builtin_amdgcn_ds_bpermute(4 * ts, (int)mykey), fr = (uint32_t)__builtin_amdgcn_ds_bpermute(4 * ts, (int)ro);
    const int ft = 2047 - (int)(((fkey >> 2) & 0x7f8u) | (fkey & 7u));
    fo = (uint32_t)(ft * D_ + dd) * 2u;
    u32x2 fs;
    asm volatile("global_load_ushort %0, %4, %8\n\tglobal_load_ushort %1, %5, %9 sc1\n\tglobal_load_ushort %2, %6, %9 sc1\n\tglobal_load_dwordx2 %3, %7, %9 sc1"
                 : "=&v"(fx), "=&v"(fv), "=&v"(fn), "=&v"(fs) : "v"(fo), "v"(v_at(dd, ft >> 3) + 2u * (ft & 7)), "v"(O_XC + fr + 2u * dd), "v"(O_SC + 16u * dd), "s"(x), "s"(ws) : "memory");
    uint32_t t16 = (((T >> (3 * ((ft >> 1) & 3))) & LMASK) | (((T >> (12 + ((ft >> 1) & 3))) & 0x00010001u) << 14)) >> (16 * (ft & 1)) & 0xffffu;
    const uint32_t o = fkey >> 16;
    const float rs0 = bf2f((u16)((o & 0x8000u) ? (o & 0x7fffu) : (~o & 0xffffu)));
    rs = rbf(__builtin_amdgcn_rcpf(1.f + __expf(-rs0)));
    const uint32_t one = (i < 8) ? (0x3f80u << (16 * (i & 1))) : 0u;
    se = (u32x4){(i >> 1) == 0 ? one : 0u, (i >> 1) == 1 ? one : 0u, (i >> 1) == 2 ? one : 0u, (i >> 1) == 3 ? one : 0u};
#pragma unroll
    for (int m = 0; m < 9; ++m) sa[m] = O_SL + (uint32_t)(16 * m + (lane >> 2)) * SP + (uint32_t)(4 * fb + (lane & 3)) * 16u;
    asm volatile("" : "+v"(t16), "+v"(rs), "+v"(se), "+v"(sa[0]), "+v"(sa[1]), "+v"(sa[2]), "+v"(sa[3]), "+v"(sa[4]), "+v"(sa[5]), "+v"(sa[6]), "+v"(sa[7]), "+v"(sa[8]));
    SLEEP(SSLEEP);
    TS(0, 1);
    float sum;
#pragma unroll 1
    for (int spin = 0; spin < SPIN_CAP; ++spin) {
      u32x4 sv[9];
#pragma unroll
      for (int m = 0; m < 9; ++m) LD16(sv[m], sa[m]);
      asm volatile("s_waitcnt vmcnt(9)" : "+v"(fx), "+v"(fv), "+v"(fn), "+v"(fs));
      if (__builtin_expect(__any(fs[0] != T), 0)) {
        for (int k = 0; k < SPIN_CAP && fs[0] != T; ++k)
          asm volatile("global_load_dwordx2 %0, %1, %2 sc1\n\ts_waitcnt vmcnt(0)" : "=&v"(fs) : "v"(O_SC + 16u * dd), "s"(ws) : "memory");
      }
      const uint32_t f1 = fv ^ t16, f2 = (f1 & 7u) ? fv & ~7u : f1;
      hx = rbf(bf2f((u16)fx) + (float)__builtin_bit_cast(_Float16, (u16)f2) * __uint_as_float(fs[1]));
      xs = bf2f((u16)fn);
      asm volatile("" : "+v"(hx), "+v"(xs));
      f32x4 dv = {0.f, 0.f, 0.f, 0.f};
      static_for<0, 9>([&](auto mm) {
        constexpr int m = mm.value;
        asm volatile("s_waitcnt vmcnt(%1)" : "+v"(sv[m]) : "n"(8 - m));
        dv = MMB(sv[m], se, dv);
        __builtin_amdgcn_sched_barrier(0);
      });
      sum = fold16(fold32(dv[0], dv[1]), fold32(dv[2], dv[3]));
      if (__builtin_expect(!__any(sum != sum), 1)) break;
      uint32_t mx = 0u;
#pragma unroll
      for (int m = 0; m < 9; ++m) mx = max(mx, max4(sv[m]));
      if (!__any(mx == 0xffffffffu)) break;
      TSC(18);
      SLEEP(PSLEEP);
    }
    const float o1 = rbf(xs + rbf(rbf(sum) * rs));
    if (i < 8) asm volatile("global_store_short %0, %1, %2 sc1" : : "v"(fo), "v"((uint32_t)f2bf(hx + o1)), "s"(out) : "memory");
    TS(0, 2);
  }
  TSE();
  }
  }
}
#define BROT "192"
#define XLIM "0x100"
#define XNUM "0"
extern "C" __global__ __launch_bounds__(512, 4) void kf_all(KARGS) {
  __shared__ __attribute__((aligned(16))) char smem[SMEM];
  asm volatile("v_readfirstlane_b32 s98, v0\n\ts_load_dwordx16 s[64:79], s[0:1], 0x0\n\ts_load_dwordx4 s[80:83], s[0:1], 0x40\n\t"
               "s_add_u32 s97, s2, " BROT "\n\ts_and_b32 s97, s97, 0xff\n\ts_and_b32 s2, s2, 0x100\n\ts_or_b32 s2, s2, s97\n\t"
               "s_cmpk_lt_u32 s2, 0x100\n\ts_cbranch_scc0 1f\n\ts_cmpk_lt_u32 s98, 0x100\n\ts_cbranch_scc0 2f\n"
               "7:\n\ts_and_b32 s84, s97, 0xe0\n\ts_bfe_u32 s85, s97, 0x20003\n\ts_or_b32 s84, s84, s85\n\ts_and_b32 s85, s97, 7\n\ts_lshl2_add_u32 s84, s85, s84\n\t"
               "s_mulk_i32 s84, 0x2400\n\ts_and_b32 s96, s98, 0xc0\n\ts_mulk_i32 s96, 36\n\ts_add_u32 s84, s84, s96\n\tv_and_b32 v110, 63, v0\n\tv_and_b32 v111, 32, v0\n\t"
               "v_lshlrev_b32 v111, 5, v111\n\tv_lshl_add_u32 v111, v110, 2, v111\n\tv_lshl_add_u32 v110, v110, 4, s84\n\tv_add_u32 v111, s84, v111\n\t"
               "v_mov_b32 v109, 0\n\ts_cmpk_lt_u32 s2, " XLIM "\n\ts_waitcnt lgkmcnt(0)\n\ts_cbranch_scc0 8f\n\t"
               "global_load_dwordx4 v[100:103], v110, s[64:65]\n\tglobal_load_dwordx4 v[104:107], v110, s[64:65] offset:1152\n\t"
               "global_load_dword v108, v111, s[64:65] offset:1024\n8:\n\tglobal_load_dword v109, v109, s[82:83] sc1\n\t"
               "s_cmp_lg_u32 s97, s2\n\ts_cbranch_scc1 6f\n\ts_cmpk_lt_u32 s2, 0x90\n\ts_cbranch_scc1 kf_n\n\ts_branch kf_h\n"
               "1:\n\ts_bfe_u32 s84, s2, 0x50003\n\ts_and_b32 s85, s2, 7\n\ts_lshl_b32 s85, s85, 5\n\ts_or_b32 s84, s84, s85\n\t"
               "s_lshl_b32 s85, s84, 9\n\ts_lshl_b32 s84, s84, 2\n\tv_and_b32 v110, 0xff, v0\n\tv_bfe_u32 v107, v0, 7, 1\n\t"
               "v_lshl_add_u32 v110, v110, 1, s85\n\tv_lshl_add_u32 v107, v107, 1, s84\n\tv_mov_b32 v109, 0\n\ts_cmpk_lt_u32 s98, 0x100\n\ts_waitcnt lgkmcnt(0)\n\t"
               "global_load_ushort v100, v107, s[78:79]\n\tglobal_load_ushort v101, v110, s[66:67]\n\tglobal_load_ushort v102, v110, s[68:69]\n\t"
               "s_cbranch_scc0 3f\n"
               "4:\n\tglobal_load_ushort v103, v110, s[70:71]\n\tglobal_load_ushort v104, v110, s[72:73]\n\tglobal_load_ushort v105, v110, s[74:75]\n\t"
               "global_load_ushort v106, v110, s[76:77]\n\tglobal_load_ushort v107, v107, s[80:81]\n\tglobal_load_dword v109, v109, s[82:83] sc1\n\ts_cbranch_scc1 kf_c\n\ts_barrier\n\ts_branch kf_p\n"
               "3:\n\tglobal_load_dword v109, v109, s[82:83] sc1\n\ts_branch kf_u\n"
               "2:\n\ts_cmpk_lt_u32 s2, 0xc0\n\ts_cbranch_scc1 5f\n\t"
               "s_lshr_b32 s84, s2, 3\n\ts_and_b32 s85, s2, 7\n\ts_lshl3_add_u32 s84, s85, s84\n\ts_addk_i32 s84, 488\n\ts_lshl_b32 s85, s84, 8\n\ts_lshl_b32 s84, s84, 1\n\t"
               "v_and_b32 v110, 0x7f, v0\n\tv_lshl_add_u32 v110, v110, 1, s85\n\tv_mov_b32 v107, s84\n\tv_mov_b32 v109, 0\n\ts_cmp_eq_u32 s2, 0\n\ts_waitcnt lgkmcnt(0)\n\t"
               "global_load_ushort v100, v107, s[78:79]\n\tglobal_load_ushort v101, v110, s[66:67]\n\tglobal_load_ushort v102, v110, s[68:69]\n\ts_branch 4b\n"
               "5:\n\ts_add_u32 s97, s2, 160\n\ts_sub_u32 s99, s2, 32\n\ts_cmpk_lt_u32 s99, " XNUM "\n\ts_cbranch_scc1 7b\n\t"
               "s_cmpk_lt_u32 s2, 32\n\ts_cselect_b32 s99, 0, 64\n\ts_sub_u32 s99, s2, s99\n\t"
               "s_and_b32 s99, s99, 0x78\n\ts_lshl_b32 s99, s99, 5\n\ts_and_b32 s98, s98, 0xc0\n\ts_or_b32 s98, s98, s99\n\ts_lshl_b32 s98, s98, 1\n\t"
               "s_getpc_b64 s[96:97]\n\ts_add_u32 s96, s96, s98\n\ts_addc_u32 s97, s97, 0\n\t"
               "s_load_dword s99, s[96:97], 0x0\n\ts_load_dword s99, s[96:97], 0x40\n\ts_load_dword s99, s[96:97], 0x2000\n\ts_load_dword s99, s[96:97], 0x2040\n\t"
               "s_load_dword s99, s[96:97], 0x4000\n\ts_load_dword s99, s[96:97], 0x4040\n\ts_load_dword s99, s[96:97], 0x6000\n\ts_load_dword s99, s[96:97], 0x6040\n\t"
               "s_load_dword s99, s[96:97], 0x8000\n\ts_load_dword s99, s[96:97], 0x8040\n\t"
               "s_cmp_gt_i32 s98, kf_end-kf_all-0xa300\n\ts_cbranch_scc1 6f\n\t"
               "s_load_dword s99, s[96:97], 0xa000\n\ts_load_dword s99, s[96:97], 0xa040\n\t"
               "s_cmp_gt_i32 s98, kf_end-kf_all-0xc300\n\ts_cbranch_scc1 6f\n\t"
               "s_load_dword s99, s[96:97], 0xc000\n\ts_load_dword s99, s[96:97], 0xc040\n6:\n\ts_barrier\n\ts_branch kf_p"
               : : "s"(__builtin_amdgcn_kernarg_segment_ptr()), "s"(blockIdx.x), "v"(threadIdx.x),
                   "s"((uint32_t)(uintptr_t)(__attribute__((address_space(3))) char*)smem) : "scc", "s96", "s97", "s98", "s99", "v127");
}
extern "C" __global__ __launch_bounds__(512, 4) void kf_c(KARGS) {
  __shared__ __attribute__((aligned(16))) char smem[SMEM];
  role_body<0>(smem, KPASS);
}
extern "C" __global__ __launch_bounds__(512, 4) void kf_u(KARGS) {
  __shared__ __attribute__((aligned(16))) char smem[SMEM];
  role_body<3>(smem, KPASS);
}
extern "C" __global__ __launch_bounds__(512, 4) void kf_n(KARGS) {
  __shared__ __attribute__((aligned(16))) char smem[SMEM];
  role_body<1, 1>(smem, KPASS);
}
extern "C" __global__ __launch_bounds__(512, 4) void kf_h(KARGS) {
  __shared__ __attribute__((aligned(16))) char smem[SMEM];
  role_body<1, 0>(smem, KPASS);
}
extern "C" __global__ __launch_bounds__(512, 4) void kf_p(KARGS) {
  __shared__ __attribute__((aligned(16))) char smem[SMEM];
  role_body<2>(smem, KPASS);
}
extern "C" __global__ __launch_bounds__(512, 4) void kf_conv() { asm volatile(CONV_ASM ::: CONV_CLOB); }
extern "C" __global__ void kf_end() {}

extern "C" void kernel_launch(void* const* d_in, const int* in_sizes, int n_in,
                              void* d_out, int out_size, void* d_ws, size_t ws_size,
                              hipStream_t stream) {
  kf_all<<<512, 512, 0, stream>>>((const u16*)d_in[0], (const u16*)d_in[1], (const u16*)d_in[2], (const u16*)d_in[3], (const u16*)d_in[4],
                                  (const u16*)d_in[5], (const u16*)d_in[6], (const u16*)d_in[7], (const u16*)d_in[8], (char*)d_ws + (size_t)WSOFF * 1048576,
                                  (const u16*)d_in[10], (const u16*)d_in[11], (const u16*)d_in[12], (const u16*)d_in[13], (u16*)d_out);
}
```

```cpp
#include <hip/hip_runtime.h>
#include <hip/hip_bf16.h>
#include <stdint.h>
#define LPOS 1
#define LSLEEP 1

typedef __attribute__((ext_vector_type(8))) short bf16x8;
typedef __attribute__((ext_vector_type(4))) float f32x4;
typedef uint16_t u16;
typedef __attribute__((ext_vector_type(4))) uint32_t u32x4;
typedef __attribute__((ext_vector_type(2))) uint32_t u32x2;
#define TSD
#define TS(k, i)
#define TSC(i)
#define TSE()
#define TSP
#define TSA

constexpr int L_ = 2048, D_ = 576, N_ = 128, F_ = 4608, C_ = 8;
constexpr int NF = 144;
constexpr uint32_t O_EP = 0;
constexpr uint32_t O_KF = 4096;
constexpr uint32_t O_KEY = O_KF + 8192;
constexpr uint32_t O_SC = O_KEY + 32768;
constexpr uint32_t O_DN = O_SC + 9216;
constexpr uint32_t O_SU = O_DN + 20480;
constexpr uint32_t O_FB = O_SU + 262144;
constexpr uint32_t O_XC = O_FB + 8 * 1152;
constexpr uint32_t SP = 9216 + 128;
constexpr uint32_t O_SL = O_XC + 1024 * 1152;
constexpr uint32_t SLU = 336;
constexpr uint32_t O_U = O_SL + 256 * SLU * 16;
constexpr int UP = 584;
constexpr uint32_t VP = 8192 + 128, VPP = 4096 + 128;
constexpr uint32_t O_V = O_U + 256 * UP * 16, O_VP = O_V + 256 * VP;
static_assert(256 * SLU * 16 >= NF * SP && O_SL % 128 == 0 && O_U % 128 == 0 && O_V % 128 == 0, "");
__host__ __device__ __forceinline__ uint32_t u_at(int tg, int d) { return O_U + 16u * (uint32_t)((tg >> 1) * (UP * 2) + (d >> 2) * 8 + (tg & 1) * 4 + (d & 3)); }
__host__ __device__ __forceinline__ uint32_t v_at(int d, int tg) {
  return (d < 512) ? O_V + (uint32_t)(d >> 1) * VP + 16u * (uint32_t)(2 * tg + (d & 1)) : O_VP + (uint32_t)(d - 512) * VPP + 16u * (uint32_t)tg;
}
#define HAVE_VAT
#define HAVE_VTAG
__device__ __forceinline__ float bf2f(u16 v) { return __uint_as_float((uint32_t)v << 16); }
__device__ __forceinline__ float bflo(uint32_t v) { return __uint_as_float(v << 16); }
__device__ __forceinline__ float bfhi(uint32_t v) { return __uint_as_float(v & 0xffff0000u); }
__device__ __forceinline__ u16 f2bf(float f) { return __builtin_bit_cast(u16, (__bf16)f); }
__device__ __forceinline__ float rbf(float f) { return bf2f(f2bf(f)); }
typedef __attribute__((ext_vector_type(2))) float f32x2;
typedef __attribute__((ext_vector_type(2))) __bf16 bf16x2v;
__device__ __forceinline__ uint32_t pack2(float a, float b) {
  return __builtin_bit_cast(uint32_t, __builtin_convertvector((f32x2){a, b}, bf16x2v));
}

#define DPP_I(v, ctrl, rmask) __builtin_amdgcn_update_dpp(0, (v), (ctrl), (rmask), 0xf, true)
__device__ __forceinline__ float row_sum(float v) {
  v += __int_as_float(DPP_I(__float_as_int(v), 0x111, 0xf));
  v += __int_as_float(DPP_I(__float_as_int(v), 0x112, 0xf));
  v += __int_as_float(DPP_I(__float_as_int(v), 0x114, 0xf));
  v += __int_as_float(DPP_I(__float_as_int(v), 0x118, 0xf));
  return v;
}
__device__ __forceinline__ float rdl(float v, int l) { return __int_as_float(__builtin_amdgcn_readlane(__float_as_int(v), l)); }
__device__ __forceinline__ float wave_sum(float v) {
  v = row_sum(v);
  asm("s_nop 1\n\tv_add_f32_dpp %0, %0, %0 row_bcast:15 row_mask:0xa bank_mask:0xf\n\t"
      "s_nop 1\n\tv_add_f32_dpp %0, %0, %0 row_bcast:31 row_mask:0xc bank_mask:0xf\n\ts_nop 0" : "+v"(v));
  return rdl(v, 63);
}
__device__ __forceinline__ float fold32(float a, float b) {
  const auto r = __builtin_amdgcn_permlane32_swap(__float_as_uint(a), __float_as_uint(b), false, false);
  return __uint_as_float(r[0]) + __uint_as_float(r[1]);
}
__device__ __forceinline__ float fold16(float a, float b) {
  const auto r = __builtin_amdgcn_permlane16_swap(__float_as_uint(a), __float_as_uint(b), false, false);
  return __uint_as_float(r[0]) + __uint_as_float(r[1]);
}
__device__ __forceinline__ void wave_sum2(float& a, float& b) {
  float v = row_sum(fold32(a, b));
  asm("s_nop 1\n\tv_add_f32_dpp %0, %0, %0 row_bcast:15 row_mask:0xa bank_mask:0xf\n\ts_nop 0" : "+v"(v));
  a = rdl(v, 31); b = rdl(v, 63);
}
__device__ __forceinline__ float dot2(uint32_t a, uint32_t b, float c) {
  return __builtin_amdgcn_fdot2_f32_bf16(__builtin_bit_cast(bf16x2v, a), __builtin_bit_cast(bf16x2v, b), c, false);
}
__device__ __forceinline__ uint32_t row_maxu(uint32_t v) {
  v = max(v, (uint32_t)DPP_I((int)v, 0xb1, 0xf));
  v = max(v, (uint32_t)DPP_I((int)v, 0x4e, 0xf));
  v = max(v, (uint32_t)DPP_I((int)v, 0x141, 0xf));
  v = max(v, (uint32_t)DPP_I((int)v, 0x140, 0xf));
  return v;
}
__device__ __forceinline__ uint32_t wave_maxu(uint32_t v) {
  v = max(v, (uint32_t)DPP_I((int)v, 0x111, 0xf));
  v = max(v, (uint32_t)DPP_I((int)v, 0x112, 0xf));
  v = max(v, (uint32_t)DPP_I((int)v, 0x114, 0xf));
  v = max(v, (uint32_t)DPP_I((int)v, 0x118, 0xf));
  v = max(v, (uint32_t)DPP_I((int)v, 0x142, 0xa));
  v = max(v, (uint32_t)DPP_I((int)v, 0x143, 0xc));
  return (uint32_t)__builtin_amdgcn_readlane((int)v, 63);
}

typedef __attribute__((ext_vector_type(8))) _Float16 f16x8;
typedef __attribute__((ext_vector_type(2))) _Float16 f16x2;
__device__ __forceinline__ uint32_t packh(f32x2 v) { return __builtin_bit_cast(uint32_t, __builtin_convertvector(v, f16x2)); }
__device__ __forceinline__ uint32_t packh(float a, float b) { return packh((f32x2){a, b}); }
constexpr int IP = 224, I_X = 128;
__device__ __forceinline__ f32x2 cmul(f32x2 z, f32x2 w) {
  f32x2 r;
  asm("v_pk_mul_f32 %0, %1, %2 op_sel_hi:[0,1]\n\tv_pk_fma_f32 %0, %1, %2, %0 op_sel:[1,1,0] op_sel_hi:[1,0,1] neg_lo:[0,1,0]" : "=&v"(r) : "v"(z), "v"(w));
  return r;
}
__device__ __forceinline__ f32x2 cexp2(float ar2, float air, float l) {
  const float t = air * l;
  return (f32x2){__builtin_amdgcn_cosf(t), __builtin_amdgcn_sinf(t)} * __builtin_amdgcn_exp2f(ar2 * l);
}
__device__ __forceinline__ float state_a(float ldt, float are, float aim, float& ar2, float& air, float& g) {
  const float dt = rbf(__expf(ldt));
  ar2 = dt * are * 1.4426950408889634f;
  air = dt * aim * 0.15915494309189535f;
  air -= __builtin_rintf(air);
  g = 0.f;
  if (__builtin_expect(__any(ar2 > 0.f), 0)) g = fminf(__uint_as_float(wave_maxu(__float_as_uint(fmaxf(ar2, 0.f)))), 1.f);
  return dt;
}
template <int N> __device__ __forceinline__ void put_im(char* p, int im, const f32x2* z) {
  *(u32x4*)p = (u32x4){packh(z[0][0], z[1][0]), packh(z[2][0], z[3][0]), packh(z[4][0], z[5][0]), packh(z[6][0], z[7][0])};
  *(u32x4*)(p + im) = (u32x4){packh(z[0][1], z[1][1]), packh(z[2][1], z[3][1]), packh(z[4][1], z[5][1]), packh(z[6][1], z[7][1])};
}
#define Y_ASM \
  "v_mov_b64 v[40:41], %0\nv_pk_mul_f32 v[42:43], v[40:41], %1 op_sel_hi:[0,1]\nv_pk_mul_f32 v[44:45], %0, %2 op_sel_hi:[0,1]\n" \
  "v_pk_fma_f32 v[42:43], v[40:41], %1, v[42:43] op_sel:[1,1,0] op_sel_hi:[1,0,1] neg_lo:[0,1,0]\n" \
  "v_pk_fma_f32 v[44:45], %0, %2, v[44:45] op_sel:[1,1,0] op_sel_hi:[1,0,1] neg_lo:[0,1,0]\nv_cvt_pk_f16_f32 v56, v40, v42\n" \
  "v_cvt_pk_f16_f32 v60, v41, v43\nv_pk_mul_f32 v[40:41], v[42:43], %1 op_sel_hi:[0,1]\nv_pk_mul_f32 v[48:49], v[44:45], %2 op_sel_hi:[0,1]\n" \
  "v_pk_fma_f32 v[40:41], v[42:43], %1, v[40:41] op_sel:[1,1,0] op_sel_hi:[1,0,1] neg_lo:[0,1,0]\n" \
  "v_pk_fma_f32 v[48:49], v[44:45], %2, v[48:49] op_sel:[1,1,0] op_sel_hi:[1,0,1] neg_lo:[0,1,0]\n" \
  "v_pk_mul_f32 v[42:43], v[40:41], %1 op_sel_hi:[0,1]\nv_pk_mul_f32 v[52:53], v[48:49], %2 op_sel_hi:[0,1]\n" \
  "v_pk_fma_f32 v[42:43], v[40:41], %1, v[42:43] op_sel:[1,1,0] op_sel_hi:[1,0,1] neg_lo:[0,1,0]\n" \
  "v_pk_fma_f32 v[52:53], v[48:49], %2, v[52:53] op_sel:[1,1,0] op_sel_hi:[1,0,1] neg_lo:[0,1,0]\nv_cvt_pk_f16_f32 v57, v40, v42\n" \
  "v_cvt_pk_f16_f32 v61, v41, v43\nv_pk_mul_f32 v[40:41], v[42:43], %1 op_sel_hi:[0,1]\n" \
  "v_pk_fma_f32 v[40:41], v[42:43], %1, v[40:41] op_sel:[1,1,0] op_sel_hi:[1,0,1] neg_lo:[0,1,0]\n" \
  "v_pk_mul_f32 v[42:43], v[40:41], %1 op_sel_hi:[0,1]\nv_pk_mul_f32 v[46:47], v[44:45], %1 op_sel_hi:[0,1]\n" \
  "v_pk_fma_f32 v[42:43], v[40:41], %1, v[42:43] op_sel:[1,1,0] op_sel_hi:[1,0,1] neg_lo:[0,1,0]\n" \
  "v_pk_fma_f32 v[46:47], v[44:45], %1, v[46:47] op_sel:[1,1,0] op_sel_hi:[1,0,1] neg_lo:[0,1,0]\nv_cvt_pk_f16_f32 v58, v40, v42\n" \
  "v_cvt_pk_f16_f32 v62, v41, v43\nv_cvt_pk_f16_f32 v64, v44, v46\nv_cvt_pk_f16_f32 v68, v45, v47\n" \
  "v_pk_mul_f32 v[40:41], v[42:43], %1 op_sel_hi:[0,1]\nv_pk_mul_f32 v[44:45], v[46:47], %1 op_sel_hi:[0,1]\n" \
  "v_pk_fma_f32 v[40:41], v[42:43], %1, v[40:41] op_sel:[1,1,0] op_sel_hi:[1,0,1] neg_lo:[0,1,0]\n" \
  "v_pk_fma_f32 v[44:45], v[46:47], %1, v[44:45] op_sel:[1,1,0] op_sel_hi:[1,0,1] neg_lo:[0,1,0]\n" \
  "v_pk_mul_f32 v[42:43], v[40:41], %1 op_sel_hi:[0,1]\nv_pk_mul_f32 v[46:47], v[44:45], %1 op_sel_hi:[0,1]\n" \
  "v_pk_fma_f32 v[42:43], v[40:41], %1, v[42:43] op_sel:[1,1,0] op_sel_hi:[1,0,1] neg_lo:[0,1,0]\n" \
  "v_pk_fma_f32 v[46:47], v[44:45], %1, v[46:47] op_sel:[1,1,0] op_sel_hi:[1,0,1] neg_lo:[0,1,0]\nv_cvt_pk_f16_f32 v59, v40, v42\n" \
  "v_cvt_pk_f16_f32 v63, v41, v43\nv_cvt_pk_f16_f32 v65, v44, v46\nv_cvt_pk_f16_f32 v69, v45, v47\n" \
  "v_pk_mul_f32 v[44:45], v[46:47], %1 op_sel_hi:[0,1]\nds_write_b128 %3, v[56:59] offset:0\n" \
  "v_pk_fma_f32 v[44:45], v[46:47], %1, v[44:45] op_sel:[1,1,0] op_sel_hi:[1,0,1] neg_lo:[0,1,0]\nds_write_b128 %3, v[60:63] offset:64\n" \
  "v_pk_mul_f32 v[46:47], v[44:45], %1 op_sel_hi:[0,1]\nv_pk_mul_f32 v[50:51], v[48:49], %1 op_sel_hi:[0,1]\n" \
  "v_pk_fma_f32 v[46:47], v[44:45], %1, v[46:47] op_sel:[1,1,0] op_sel_hi:[1,0,1] neg_lo:[0,1,0]\n" \
  "v_pk_fma_f32 v[50:51], v[48:49], %1, v[50:51] op_sel:[1,1,0] op_sel_hi:[1,0,1] neg_lo:[0,1,0]\nv_cvt_pk_f16_f32 v66, v44, v46\n" \
  "v_cvt_pk_f16_f32 v70, v45, v47\nv_cvt_pk_f16_f32 v56, v48, v50\nv_cvt_pk_f16_f32 v60, v49, v51\n" \
  "v_pk_mul_f32 v[44:45], v[46:47], %1 op_sel_hi:[0,1]\nv_pk_mul_f32 v[48:49], v[50:51], %1 op_sel_hi:[0,1]\n" \
  "v_pk_fma_f32 v[44:45], v[46:47], %1, v[44:45] op_sel:[1,1,0] op_sel_hi:[1,0,1] neg_lo:[0,1,0]\n" \
  "v_pk_fma_f32 v[48:49], v[50:51], %1, v[48:49] op_sel:[1,1,0] op_sel_hi:[1,0,1] neg_lo:[0,1,0]\n" \
  "v_pk_mul_f32 v[46:47], v[44:45], %1 op_sel_hi:[0,1]\nv_pk_mul_f32 v[50:51], v[48:49], %1 op_sel_hi:[0,1]\n" \
  "v_pk_fma_f32 v[46:47], v[44:45], %1, v[46:47] op_sel:[1,1,0] op_sel_hi:[1,0,1] neg_lo:[0,1,0]\n" \
  "v_pk_fma_f32 v[50:51], v[48:49], %1, v[50:51] op_sel:[1,1,0] op_sel_hi:[1,0,1] neg_lo:[0,1,0]\nv_cvt_pk_f16_f32 v67, v44, v46\n" \
  "v_cvt_pk_f16_f32 v71, v45, v47\nv_cvt_pk_f16_f32 v57, v48, v50\nv_cvt_pk_f16_f32 v61, v49, v51\n" \
  "v_pk_mul_f32 v[48:49], v[50:51], %1 op_sel_hi:[0,1]\nds_write_b128 %3, v[64:67] offset:16\n" \
  "v_pk_fma_f32 v[48:49], v[50:51], %1, v[48:49] op_sel:[1,1,0] op_sel_hi:[1,0,1] neg_lo:[0,1,0]\nds_write_b128 %3, v[68:71] offset:80\n" \
  "v_pk_mul_f32 v[50:51], v[48:49], %1 op_sel_hi:[0,1]\nv_pk_mul_f32 v[54:55], v[52:53], %1 op_sel_hi:[0,1]\n" \
  "v_pk_fma_f32 v[50:51], v[48:49], %1, v[50:51] op_sel:[1,1,0] op_sel_hi:[1,0,1] neg_lo:[0,1,0]\n" \
  "v_pk_fma_f32 v[54:55], v[52:53], %1, v[54:55] op_sel:[1,1,0] op_sel_hi:[1,0,1] neg_lo:[0,1,0]\nv_cvt_pk_f16_f32 v58, v48, v50\n" \
  "v_cvt_pk_f16_f32 v62, v49, v51\nv_cvt_pk_f16_f32 v64, v52, v54\nv_cvt_pk_f16_f32 v68, v53, v55\n" \
  "v_pk_mul_f32 v[48:49], v[50:51], %1 op_sel_hi:[0,1]\nv_pk_mul_f32 v[52:53], v[54:55], %1 op_sel_hi:[0,1]\n" \
  "v_pk_fma_f32 v[48:49], v[50:51], %1, v[48:49] op_sel:[1,1,0] op_sel_hi:[1,0,1] neg_lo:[0,1,0]\n" \
  "v_pk_fma_f32 v[52:53], v[54:55], %1, v[52:53] op_sel:[1,1,0] op_sel_hi:[1,0,1] neg_lo:[0,1,0]\n" \
  "v_pk_mul_f32 v[50:51], v[48:49], %1 op_sel_hi:[0,1]\nv_pk_mul_f32 v[54:55], v[52:53], %1 op_sel_hi:[0,1]\n" \
  "v_pk_fma_f32 v[50:51], v[48:49], %1, v[50:51] op_sel:[1,1,0] op_sel_hi:[1,0,1] neg_lo:[0,1,0]\n" \
  "v_pk_fma_f32 v[54:55], v[52:53], %1, v[54:55] op_sel:[1,1,0] op_sel_hi:[1,0,1] neg_lo:[0,1,0]\nv_cvt_pk_f16_f32 v59, v48, v50\n" \
  "v_cvt_pk_f16_f32 v63, v49, v51\nv_cvt_pk_f16_f32 v65, v52, v54\nv_cvt_pk_f16_f32 v69, v53, v55\n" \
  "v_pk_mul_f32 v[52:53], v[54:55], %1 op_sel_hi:[0,1]\nds_write_b128 %3, v[56:59] offset:32\n" \
  "v_pk_fma_f32 v[52:53], v[54:55], %1, v[52:53] op_sel:[1,1,0] op_sel_hi:[1,0,1] neg_lo:[0,1,0]\nds_write_b128 %3, v[60:63] offset:96\n" \
  "v_pk_mul_f32 v[54:55], v[52:53], %1 op_sel_hi:[0,1]\n" \
  "v_pk_fma_f32 v[54:55], v[52:53], %1, v[54:55] op_sel:[1,1,0] op_sel_hi:[1,0,1] neg_lo:[0,1,0]\nv_cvt_pk_f16_f32 v66, v52, v54\n" \
  "v_cvt_pk_f16_f32 v70, v53, v55\nv_pk_mul_f32 v[52:53], v[54:55], %1 op_sel_hi:[0,1]\n" \
  "v_pk_fma_f32 v[52:53], v[54:55], %1, v[52:53] op_sel:[1,1,0] op_sel_hi:[1,0,1] neg_lo:[0,1,0]\n" \
  "v_pk_mul_f32 v[54:55], v[52:53], %1 op_sel_hi:[0,1]\n" \
  "v_pk_fma_f32 v[54:55], v[52:53], %1, v[54:55] op_sel:[1,1,0] op_sel_hi:[1,0,1] neg_lo:[0,1,0]\nv_cvt_pk_f16_f32 v67, v52, v54\n" \
  "v_cvt_pk_f16_f32 v71, v53, v55\nds_write_b128 %3, v[64:67] offset:48\nds_write_b128 %3, v[68:71] offset:112\n"
#define Y_CLOB "memory", "v40", "v41", "v42", "v43", "v44", "v45", "v46", "v47", "v48", "v49", "v50", "v51", "v52", "v53", "v54", "v55", "v56", "v57", "v58", "v59", "v60", "v61", "v62", "v63", "v64", "v65", "v66", "v67", "v68", "v69", "v70", "v71"
#define T_ASM \
  "ds_read_b128 %4, %8\nds_read_b64_tr_b16 v[80:81], %7 offset:0\nds_read_b64_tr_b16 v[82:83], %7 offset:64\n" \
  "ds_read_b64_tr_b16 v[84:85], %5 offset:0\nds_read_b64_tr_b16 v[86:87], %5 offset:48\nds_read_b64_tr_b16 v[88:89], %6 offset:0\n" \
  "ds_read_b64_tr_b16 v[90:91], %6 offset:48\nds_read_b64_tr_b16 v[92:93], %7 offset:3584\nds_read_b64_tr_b16 v[94:95], %7 offset:3648\n" \
  "ds_read_b64_tr_b16 v[96:97], %5 offset:3584\nds_read_b64_tr_b16 v[98:99], %5 offset:3632\nds_read_b64_tr_b16 v[100:101], %6 offset:3584\n" \
  "ds_read_b64_tr_b16 v[102:103], %6 offset:3632\nds_read_b64_tr_b16 v[104:105], %7 offset:7168\nds_read_b64_tr_b16 v[106:107], %7 offset:7232\n" \
  "s_waitcnt lgkmcnt(8)\nv_mfma_f32_16x16x32_f16 %0, v[84:87], v[80:83], 0\nds_read_b64_tr_b16 v[108:109], %5 offset:7168\n" \
  "ds_read_b64_tr_b16 v[110:111], %5 offset:7216\nds_read_b64_tr_b16 v[112:113], %6 offset:7168\nv_mfma_f32_16x16x32_f16 %2, v[88:91], v[80:83], 0\n" \
  "ds_read_b64_tr_b16 v[114:115], %6 offset:7216\nds_read_b64_tr_b16 v[116:117], %7 offset:10752\nds_read_b64_tr_b16 v[118:119], %7 offset:10816\n" \
  "s_waitcnt lgkmcnt(8)\nv_mfma_f32_16x16x32_f16 %0, v[96:99], v[92:95], %0\nds_read_b64_tr_b16 v[120:121], %5 offset:10752\n" \
  "ds_read_b64_tr_b16 v[122:123], %5 offset:10800\nds_read_b64_tr_b16 v[124:125], %6 offset:10752\n" \
  "v_mfma_f32_16x16x32_f16 %2, v[100:103], v[92:95], %2\nds_read_b64_tr_b16 v[126:127], %6 offset:10800\n" \
  "ds_read_b64_tr_b16 v[80:81], %7 offset:14336\nds_read_b64_tr_b16 v[82:83], %7 offset:14400\ns_waitcnt lgkmcnt(8)\n" \
  "v_mfma_f32_16x16x32_f16 %0, v[108:111], v[104:107], %0\nds_read_b64_tr_b16 v[84:85], %5 offset:14336\n" \
  "ds_read_b64_tr_b16 v[86:87], %5 offset:14384\nds_read_b64_tr_b16 v[88:89], %6 offset:14336\n" \
  "v_mfma_f32_16x16x32_f16 %2, v[112:115], v[104:107], %2\nds_read_b64_tr_b16 v[90:91], %6 offset:14384\n" \
  "ds_read_b64_tr_b16 v[92:93], %7 offset:17920\nds_read_b64_tr_b16 v[94:95], %7 offset:17984\ns_waitcnt lgkmcnt(8)\n" \
  "v_mfma_f32_16x16x32_f16 %0, v[120:123], v[116:119], %0\nds_read_b64_tr_b16 v[96:97], %5 offset:17920\n" \
  "ds_read_b64_tr_b16 v[98:99], %5 offset:17968\nds_read_b64_tr_b16 v[100:101], %6 offset:17920\n" \
  "v_mfma_f32_16x16x32_f16 %2, v[124:127], v[116:119], %2\nds_read_b64_tr_b16 v[102:103], %6 offset:17968\n" \
  "ds_read_b64_tr_b16 v[104:105], %7 offset:21504\nds_read_b64_tr_b16 v[106:107], %7 offset:21568\ns_waitcnt lgkmcnt(8)\n" \
  "v_mfma_f32_16x16x32_f16 %1, v[84:87], v[80:83], 0\nds_read_b64_tr_b16 v[108:109], %5 offset:21504\nds_read_b64_tr_b16 v[110:111], %5 offset:21552\n" \
  "ds_read_b64_tr_b16 v[112:113], %6 offset:21504\nv_mfma_f32_16x16x32_f16 %3, v[88:91], v[80:83], 0\nds_read_b64_tr_b16 v[114:115], %6 offset:21552\n" \
  "ds_read_b64_tr_b16 v[116:117], %7 offset:25088\nds_read_b64_tr_b16 v[118:119], %7 offset:25152\ns_waitcnt lgkmcnt(8)\n" \
  "v_mfma_f32_16x16x32_f16 %1, v[96:99], v[92:95], %1\nds_read_b64_tr_b16 v[120:121], %5 offset:25088\n" \
  "ds_read_b64_tr_b16 v[122:123], %5 offset:25136\nds_read_b64_tr_b16 v[124:125], %6 offset:25088\n" \
  "v_mfma_f32_16x16x32_f16 %3, v[100:103], v[92:95], %3\nds_read_b64_tr_b16 v[126:127], %6 offset:25136\ns_waitcnt lgkmcnt(6)\n" \
  "v_mfma_f32_16x16x32_f16 %1, v[108:111], v[104:107], %1\nv_mfma_f32_16x16x32_f16 %3, v[112:115], v[104:107], %3\ns_waitcnt lgkmcnt(0)\n" \
  "v_mfma_f32_16x16x32_f16 %1, v[120:123], v[116:119], %1\nv_mfma_f32_16x16x32_f16 %3, v[124:127], v[116:119], %3\n"
#define T_CLOB "memory", "v80", "v81", "v82", "v83", "v84", "v85", "v86", "v87", "v88", "v89", "v90", "v91", "v92", "v93", "v94", "v95", "v96", "v97", "v98", "v99", "v100", "v101", "v102", "v103", "v104", "v105", "v106", "v107", "v108", "v109", "v110", "v111", "v112", "v113", "v114", "v115", "v116", "v117", "v118", "v119", "v120", "v121", "v122", "v123", "v124", "v125", "v126", "v127"
template <class F> __device__ __forceinline__ int gen_y(float ldt, float are, float aim, float bre, float bim, float cre, float cim, uint32_t im, F&& mid TSP) {
  float ar2, air, g;
  const float dt = state_a(ldt, are, aim, ar2, air, g);
  const float dbr = dt * bre, dbi = dt * bim, pr = cre * dbr - cim * dbi, pi = cre * dbi + cim * dbr;
  const f32x2 w8 = cexp2(ar2, -air, 8.f), w64 = cexp2(ar2, -air, 64.f);
  const float pm = fmaxf(fabsf(pr), fabsf(pi));
  int e = -4;
  if (__builtin_expect(__any(!(pm < 0x1p-4f) || ar2 > 0.f) || !__any(pm > 0x1p-12f), 0)) e = (int)(wave_maxu(__float_as_uint(pm)) >> 23) - 126 + (int)ceilf(g * 250.f);
  const f32x2 y0 = {__builtin_amdgcn_ldexpf(pr, 14 - e), __builtin_amdgcn_ldexpf(-pi, 14 - e)};
  mid();
  TS(0, 7);
  asm volatile(Y_ASM : : "v"(y0), "v"(w8), "v"(w64), "v"(im) : Y_CLOB);
  return e;
}
__device__ __forceinline__ int gen_x(float ldt, float are, float aim, char* im) {
  float ar2, air, g;
  state_a(ldt, are, aim, ar2, air, g);
  const int e = min((int)ceilf(g * 1810.f), 300);
  const float s = __builtin_amdgcn_ldexpf(1.f, 15 - e);
  const f32x2 w1 = cexp2(ar2, air, 1.f), w256 = cexp2(ar2, air, 256.f), w2 = cmul(w1, w1);
  f32x2 a[8], b[8], c[8];
  a[0] = (f32x2){s, 0.f}; a[1] = w1 * s; a[2] = w2 * s;
  b[0] = b[1] = w256 * s;
  b[2] = cmul(b[0], w2); a[3] = cmul(a[1], w2); a[4] = cmul(a[2], w2);
  b[3] = b[4] = b[5] = cmul(b[2], w2); a[5] = cmul(a[3], w2); a[6] = cmul(a[4], w2);
  b[6] = cmul(b[3], w2); a[7] = cmul(a[5], w2); c[0] = cmul(b[3], w256);
  b[7] = cmul(b[6], w2); c[2] = cmul(c[0], w256);
  put_im<0>(im + I_X, 48, a);
  c[1] = cmul(b[7], w256); c[3] = cmul(c[1], w256);
  put_im<0>(im + I_X + 16, 48, b);
#pragma unroll
  for (int k = 4; k < 8; ++k) c[k] = cmul(c[k - 1], w256);
  put_im<0>(im + I_X + 32, 48, c);
  return e;
}

template <int I> struct IC { static constexpr int value = I; };
template <int B, int E, class F> __device__ __forceinline__ void static_for(F&& f) {
  if constexpr (B < E) { f(IC<B>{}); static_for<B + 1, E>(f); }
}
typedef __attribute__((ext_vector_type(4))) short s16x4;
__device__ __forceinline__ u32x2 trd(uint32_t a) {
  return __builtin_bit_cast(u32x2, __builtin_amdgcn_ds_read_tr16_b64_v4i16((__attribute__((address_space(3))) s16x4*)(uintptr_t)a));
}
#define BAR() asm volatile("s_waitcnt lgkmcnt(0)\n\ts_barrier" ::: "memory")
#define ST16(off, v) asm volatile("global_store_dwordx4 %0, %1, %2 sc1\n\ts_nop 1" : : "v"(off), "v"(v), "s"(ws) : "memory")
#define ST4(off, v) asm volatile("global_store_dword %0, %1, %2 sc1" : : "v"(off), "v"(v), "s"(ws) : "memory")
#define LD16(r, off) asm volatile("global_load_dwordx4 %0, %1, %2 sc1" : "=v"(r) : "v"(off), "s"(ws) : "memory")
#define LD16W(r, off) asm volatile("global_load_dwordx4 %0, %1, %2 sc1\n\ts_waitcnt vmcnt(0)" : "=&v"(r) : "v"(off), "s"(ws) : "memory")
#define DRAIN() asm volatile("s_waitcnt vmcnt(0)" ::: "memory")
#define CEX(a, b) { const uint32_t hi_ = max(a, b), lo_ = min(a, b); a = hi_; b = lo_; }
#define MMB(a_, b_, c_) __builtin_amdgcn_mfma_f32_16x16x32_bf16(__builtin_bit_cast(bf16x8, a_), __builtin_bit_cast(bf16x8, b_), c_, 0, 0, 0)
#define NTM 0
#define DPOL ""
#define DPOL2 ""
#define DPOL4 ""
constexpr int SPIN_CAP = 1 << 18;
constexpr uint32_t UMASK = 0x40074007u;
__device__ __forceinline__ uint32_t utag(uint32_t T, int j) {
  return ((T >> (3 * j)) & 0x00070007u) | (((j <= 2) ? (T << (2 - j)) : (T >> (j - 2))) & 0x40004000u);
}
__device__ __forceinline__ uint32_t max4(const u32x4& a) { return max(max(a[0], a[1]), max(a[2], a[3])); }
__device__ __forceinline__ void until(uint32_t t) {
#pragma unroll 1
  for (int spin = 0; spin < 600 && (int)((uint32_t)__builtin_amdgcn_s_memrealtime() - t) < 0; ++spin) __builtin_amdgcn_s_sleep(1);
}
__device__ __forceinline__ uint32_t latest(uint32_t t) {
  const uint32_t t0 = (uint32_t)__builtin_amdgcn_readfirstlane(t) - 0x40000000u;
  return wave_maxu(t - t0) + t0;
}
__device__ __forceinline__ uint32_t gate(const char* ws, uint32_t po, uint32_t T) {
  u32x2 pv;
#pragma unroll 1
  for (int spin = 0; spin < SPIN_CAP; ++spin) {
    asm volatile("global_load_dwordx2 %0, %1, %2 sc1\n\ts_waitcnt vmcnt(0)" : "=&v"(pv) : "v"(po), "s"(ws) : "memory");
    if (!__any(pv[0] != T)) break;
    __builtin_amdgcn_s_sleep(2);
  }
  return pv[1];
}
__device__ __forceinline__ uint32_t gate2(const char* ws, uint32_t oa, uint32_t da, uint32_t ob, uint32_t T) {
  u32x2 pa, pb;
#pragma unroll 1
  for (int spin = 0; spin < SPIN_CAP; ++spin) {
    asm volatile("global_load_dwordx2 %0, %2, %4 sc1\n\tglobal_load_dwordx2 %1, %3, %4 sc1\n\ts_waitcnt vmcnt(0)" : "=&v"(pa), "=&v"(pb) : "v"(oa), "v"(ob), "s"(ws) : "memory");
    if (!__any(pa[0] != T || pb[0] != T)) break;
    __builtin_amdgcn_s_sleep(4);
  }
  pa[1] += da;
  return latest(((int)(pa[1] - pb[1]) > 0) ? pa[1] : pb[1]);
}
__device__ __forceinline__ void gsync(uint32_t a, uint32_t target) {
  uint32_t t, c, n = SPIN_CAP;
  uint64_t ex;
  asm volatile("s_waitcnt lgkmcnt(0)\n\ts_mov_b64 %3, exec\n\ts_mov_b64 exec, 1\n\tv_mov_b32 %0, 1\n\tds_add_u32 %4, %0\n\ts_mov_b64 exec, %3\n"
               "1:\n\ts_sleep 1\n\tds_read_b32 %0, %4\n\ts_sub_u32 %2, %2, 1\n\ts_cbranch_scc1 2f\n\ts_waitcnt lgkmcnt(0)\n\tv_readfirstlane_b32 %1, %0\n\t"
               "s_cmp_lt_u32 %1, %5\n\ts_cbranch_scc1 1b\n2:\n\ts_waitcnt lgkmcnt(0)"
               : "=&v"(t), "=&s"(c), "+s"(n), "=&s"(ex) : "v"(a), "n"(target) : "memory", "scc");
}
__device__ __forceinline__ void gsync0(uint32_t a, uint32_t target) {
  uint32_t t, c, n = SPIN_CAP;
  asm volatile("s_waitcnt lgkmcnt(0)\n\ts_mov_b64 exec, 1\n\tv_mov_b32 %0, 1\n\tds_add_u32 %3, %0\n\ts_mov_b64 exec, -1\n"
               "1:\n\tds_read_b32 %0, %3\n\ts_sub_u32 %2, %2, 1\n\ts_cbranch_scc1 2f\n\ts_waitcnt lgkmcnt(0)\n\tv_readfirstlane_b32 %1, %0\n\t"
               "s_cmp_lt_u32 %1, %4\n\ts_cbranch_scc1 1b\n2:\n\ts_waitcnt lgkmcnt(0)"
               : "=&v"(t), "=&s"(c), "+s"(n) : "v"(a), "n"(target) : "memory", "scc");
}
__device__ __forceinline__ void gwait(uint32_t a, uint32_t target) {
  uint32_t t, c, n = SPIN_CAP;
  asm volatile("1:\n\ts_sleep 2\n\tds_read_b32 %0, %3\n\ts_sub_u32 %2, %2, 1\n\ts_cbranch_scc1 2f\n\ts_waitcnt lgkmcnt(0)\n\tv_readfirstlane_b32 %1, %0\n\t"
               "s_cmp_lt_u32 %1, %4\n\ts_cbranch_scc1 1b\n2:\n\ts_waitcnt lgkmcnt(0)"
               : "=&v"(t), "=&s"(c), "+s"(n) : "v"(a), "n"(target) : "memory", "scc");
}
__device__ __forceinline__ void gwait0(uint32_t a, uint32_t target) {
  uint32_t t, c, n = SPIN_CAP;
  asm volatile("1:\n\tds_read_b32 %0, %3\n\ts_sub_u32 %2, %2, 1\n\ts_cbranch_scc1 2f\n\ts_waitcnt lgkmcnt(0)\n\tv_readfirstlane_b32 %1, %0\n\t"
               "s_cmp_lt_u32 %1, %4\n\ts_cbranch_scc1 1b\n2:\n\ts_waitcnt lgkmcnt(0)"
               : "=&v"(t), "=&s"(c), "+s"(n) : "v"(a), "n"(target) : "memory", "scc");
}
__device__ __forceinline__ void dma16(const void* base, uint32_t voff, uint32_t lds) {
  uint32_t keep;
  asm volatile("s_mov_b32 %0, m0\n\ts_mov_b32 m0, %3\n\ts_nop 0\n\tglobal_load_lds_dwordx4 %1, %2" DPOL "\n\ts_mov_b32 m0, %0" : "=&s"(keep) : "v"(voff), "s"(base), "s"(lds) : "memory");
}
__device__ __forceinline__ void dma16d(const void* base, uint32_t voff, uint32_t lds) {
  uint32_t keep;
  asm volatile("s_mov_b32 %0, m0\n\ts_mov_b32 m0, %3\n\ts_nop 0\n\tglobal_load_lds_dwordx4 %1, %2" DPOL2 "\n\ts_mov_b32 m0, %0" : "=&s"(keep) : "v"(voff), "s"(base), "s"(lds) : "memory");
}
__device__ __forceinline__ void nap(int n) {
  for (; n > 15; n -= 15) __builtin_amdgcn_s_sleep(15);
  switch (n) {
    case 1: __builtin_amdgcn_s_sleep(1); break; case 2: __builtin_amdgcn_s_sleep(2); break; case 3: __builtin_amdgcn_s_sleep(3); break;
    case 4: __builtin_amdgcn_s_sleep(4); break; case 5: __builtin_amdgcn_s_sleep(5); break; case 6: __builtin_amdgcn_s_sleep(6); break;
    case 7: __builtin_amdgcn_s_sleep(7); break; case 8: __builtin_amdgcn_s_sleep(8); break; case 9: __builtin_amdgcn_s_sleep(9); break;
    case 10: __builtin_amdgcn_s_sleep(10); break; case 11: __builtin_amdgcn_s_sleep(11); break; case 12: __builtin_amdgcn_s_sleep(12); break;
    case 13: __builtin_amdgcn_s_sleep(13); break; case 14: __builtin_amdgcn_s_sleep(14); break; case 15: __builtin_amdgcn_s_sleep(15); break;
    default: break;
  }
}
#define SLEEP(n) do { if ((n) > 0) __builtin_amdgcn_s_sleep(n); } while (0)
#define USLEEP 3
#define SSLEEP 11
#define PSLEEP 2
#define PPRIO 0
#define LFIRST 1
#define WD 40
#define OSLEEP 12
#define QPRIO 1
#define KPRIO 2
#define GSLEEP 0
#define NSLEEP 0
#define UD 40
#define XD 0
#define RSLEEP 0
#define UPRIO 2
#define VD2 395
#define WDEPTH 4
#define ESLEEP 0
#define SD 58
#define KD 175
#define OD 0
#define KBACK 6
#define DDEPTH 6
#define C3 1
#define XOFF 0
#define XROT 2
#define WSOFF 0
#define T0 0
#define TB1 0
#define T0SLEEP 0
#define KN 2
#define RPF 1
#define TSTOP 70
#define HSTOP 110
#define VEARLY 0ull
#define FPR 3
#define SPR 3
#define DPRIO 0
#define T1 0
#define T1SLEEP 0
#define QPRIO2 3
#define HW 2
#define RD 62
#define OPOS 1
#define OD2 0
#define NA 12
#define RD2 88
#define ORD1 ((unsigned __int128)0x321087654u << 36 | 0x321087654ull)
#define WDEPTH2 4
#define RQ NA
#define HREV 1
#define QPRIO0 3
#define VPS 1
#define RW 1
#define NPRIO 0
#define XSL 10
#define V2D 20
#define XF 0
#define N0 8
#define FORCESLOW 0
#define FORCEFB 0
#define CONV_ASM \
  "s_cmp_eq_u32 s89, 1\ns_cbranch_scc1 1f\ns_cmp_gt_u32 s89, 1\ns_cbranch_scc1 9f\nds_read_b64 v[124:125], v3 offset:576\n" \
  "ds_read_b64 v[126:127], v3 offset:584\nds_read_b64 v[120:121], v3 offset:64\nds_read_b64 v[122:123], v3 offset:72\n" \
  "ds_read_b64 v[116:117], v3 offset:640\nds_read_b64 v[118:119], v3 offset:648\nds_read_b64 v[112:113], v3 offset:128\n" \
  "ds_read_b64 v[114:115], v3 offset:136\nds_read_b64 v[108:109], v3 offset:704\nds_read_b64 v[110:111], v3 offset:712\n" \
  "ds_read_b64 v[104:105], v3 offset:192\nds_read_b64 v[106:107], v3 offset:200\nds_read_b64 v[100:101], v3 offset:768\n" \
  "ds_read_b64 v[102:103], v3 offset:776\nds_read_b64 v[96:97], v3 offset:256\nds_read_b64 v[98:99], v3 offset:264\n" \
  "ds_read_b64 v[92:93], v3 offset:832\nds_read_b64 v[94:95], v3 offset:840\nds_read_b64 v[88:89], v3 offset:320\n" \
  "ds_read_b64 v[90:91], v3 offset:328\nds_read_b64 v[84:85], v3 offset:896\nds_read_b64 v[86:87], v3 offset:904\n" \
  "ds_read_b64 v[80:81], v3 offset:384\nds_read_b64 v[82:83], v3 offset:392\nds_read_b64 v[76:77], v3 offset:960\n" \
  "ds_read_b64 v[78:79], v3 offset:968\nds_read_b64 v[72:73], v3 offset:448\nds_read_b64 v[74:75], v3 offset:456\n" \
  "ds_read_b64 v[68:69], v3 offset:1024\nds_read_b64 v[70:71], v3 offset:1032\nds_read_b64 v[64:65], v3 offset:512\n" \
  "ds_read_b64 v[66:67], v3 offset:520\nds_read_b64 v[60:61], v3 offset:0\nds_read_b64 v[62:63], v3 offset:8\nds_read_b64 v[56:57], v3 offset:1088\n" \
  "ds_read_b64 v[58:59], v3 offset:1096\ns_waitcnt lgkmcnt(0)\ns_barrier\nds_read_b128 v[36:39], v2 offset:0\nds_read_b128 v[40:43], v2 offset:64\n" \
  "ds_read_b128 v[44:47], v2 offset:128\nds_read_b128 v[48:51], v2 offset:192\ns_waitcnt lgkmcnt(3)\n" \
  "v_mfma_f32_16x16x32_f16 v[12:15], v[124:127], v[36:39], 0\ns_setprio 1\nds_read_b128 v[52:55], v2 offset:256\n" \
  "v_mfma_f32_16x16x32_f16 v[16:19], v[120:123], v[36:39], 0\ns_waitcnt lgkmcnt(3)\nv_mfma_f32_16x16x32_f16 v[12:15], v[116:119], v[40:43], v[12:15]\n" \
  "ds_read_b128 v[36:39], v2 offset:320\nv_mfma_f32_16x16x32_f16 v[16:19], v[112:115], v[40:43], v[16:19]\ns_waitcnt lgkmcnt(3)\n" \
  "v_mfma_f32_16x16x32_f16 v[12:15], v[108:111], v[44:47], v[12:15]\nds_read_b128 v[40:43], v2 offset:384\n" \
  "v_mfma_f32_16x16x32_f16 v[16:19], v[104:107], v[44:47], v[16:19]\ns_waitcnt lgkmcnt(3)\n" \
  "v_mfma_f32_16x16x32_f16 v[12:15], v[100:103], v[48:51], v[12:15]\nds_read_b128 v[44:47], v2 offset:448\n" \
  "v_mfma_f32_16x16x32_f16 v[16:19], v[96:99], v[48:51], v[16:19]\ns_waitcnt lgkmcnt(3)\n" \
  "v_mfma_f32_16x16x32_f16 v[12:15], v[92:95], v[52:55], v[12:15]\nv_mfma_f32_16x16x32_f16 v[16:19], v[88:91], v[52:55], v[16:19]\n" \
  "s_waitcnt lgkmcnt(2)\nv_mfma_f32_16x16x32_f16 v[12:15], v[84:87], v[36:39], v[12:15]\nds_read_b128 v[48:51], v2 offset:512\n" \
  "v_mfma_f32_16x16x32_f16 v[16:19], v[80:83], v[36:39], v[16:19]\ns_waitcnt lgkmcnt(2)\n" \
  "v_mfma_f32_16x16x32_f16 v[12:15], v[76:79], v[40:43], v[12:15]\nds_read_b128 v[52:55], v2 offset:576\n" \
  "v_mfma_f32_16x16x32_f16 v[16:19], v[72:75], v[40:43], v[16:19]\ns_waitcnt lgkmcnt(2)\n" \
  "v_mfma_f32_16x16x32_f16 v[12:15], v[68:71], v[44:47], v[12:15]\nds_read_b128 v[36:39], v2 offset:640\n" \
  "v_mfma_f32_16x16x32_f16 v[16:19], v[64:67], v[44:47], v[16:19]\nv_mfma_f32_16x16x32_f16 v[20:23], v[60:63], v[44:47], 0\n" \
  "ds_read_b128 v[40:43], v2 offset:704\nv_mfma_f32_16x16x32_f16 v[28:31], v[56:59], v[44:47], 0\ns_waitcnt lgkmcnt(3)\n" \
  "v_mfma_f32_16x16x32_f16 v[16:19], v[124:127], v[48:51], v[16:19]\nds_read_b128 v[56:59], v2 offset:768\nv_cvt_pkrtz_f16_f32 v32, v12, v13\n" \
  "v_mfma_f32_16x16x32_f16 v[20:23], v[120:123], v[48:51], v[20:23]\nv_cvt_pkrtz_f16_f32 v33, v14, v15\ns_waitcnt lgkmcnt(3)\n" \
  "v_mfma_f32_16x16x32_f16 v[16:19], v[116:119], v[52:55], v[16:19]\nds_read_b128 v[44:47], v2 offset:832\n" \
  "v_pk_add_u16 v32, v32, 4 op_sel_hi:[1,0] clamp\nv_mfma_f32_16x16x32_f16 v[20:23], v[112:115], v[52:55], v[20:23]\n" \
  "v_pk_add_u16 v33, v33, 4 op_sel_hi:[1,0] clamp\ns_waitcnt lgkmcnt(3)\nv_mfma_f32_16x16x32_f16 v[16:19], v[108:111], v[36:39], v[16:19]\n" \
  "ds_read_b128 v[48:51], v2 offset:896\nds_write_b64 v4, v[32:33] offset:0\nv_mfma_f32_16x16x32_f16 v[20:23], v[104:107], v[36:39], v[20:23]\n" \
  "s_waitcnt lgkmcnt(3)\nv_mfma_f32_16x16x32_f16 v[16:19], v[100:103], v[40:43], v[16:19]\nds_read_b128 v[52:55], v2 offset:960\n" \
  "v_mfma_f32_16x16x32_f16 v[20:23], v[96:99], v[40:43], v[20:23]\nv_mfma_f32_16x16x32_f16 v[16:19], v[92:95], v[56:59], v[16:19]\n" \
  "v_mfma_f32_16x16x32_f16 v[20:23], v[88:91], v[56:59], v[20:23]\nds_read_b128 v[36:39], v2 offset:1024\ns_waitcnt lgkmcnt(3)\n" \
  "v_mfma_f32_16x16x32_f16 v[16:19], v[84:87], v[44:47], v[16:19]\nv_mfma_f32_16x16x32_f16 v[20:23], v[80:83], v[44:47], v[20:23]\n" \
  "ds_read_b128 v[40:43], v2 offset:1088\nds_read_b64 v[56:57], v6 offset:256\nds_read_b64 v[58:59], v6 offset:264\n" \
  "v_mfma_f32_16x16x32_f16 v[16:19], v[76:79], v[48:51], v[16:19]\nv_mfma_f32_16x16x32_f16 v[20:23], v[72:75], v[48:51], v[20:23]\n" \
  "ds_read_b128 v[44:47], v2 offset:1152\ns_waitcnt lgkmcnt(4)\nv_mfma_f32_16x16x32_f16 v[16:19], v[68:71], v[52:55], v[16:19]\n" \
  "v_mfma_f32_16x16x32_f16 v[20:23], v[64:67], v[52:55], v[20:23]\nds_read_b128 v[48:51], v2 offset:1216\n" \
  "v_mfma_f32_16x16x32_f16 v[24:27], v[60:63], v[52:55], 0\nv_mfma_f32_16x16x32_f16 v[20:23], v[124:127], v[36:39], v[20:23]\n" \
  "ds_read_b128 v[52:55], v2 offset:1280\nv_mfma_f32_16x16x32_f16 v[24:27], v[120:123], v[36:39], v[24:27]\ns_waitcnt lgkmcnt(5)\n" \
  "v_mfma_f32_16x16x32_f16 v[20:23], v[116:119], v[40:43], v[20:23]\nds_read_b128 v[36:39], v2 offset:1344\nv_cvt_pkrtz_f16_f32 v32, v16, v17\n" \
  "v_mfma_f32_16x16x32_f16 v[24:27], v[112:115], v[40:43], v[24:27]\nv_cvt_pkrtz_f16_f32 v33, v18, v19\ns_waitcnt lgkmcnt(3)\n" \
  "v_mfma_f32_16x16x32_f16 v[20:23], v[108:111], v[44:47], v[20:23]\nds_read_b128 v[40:43], v2 offset:1408\n" \
  "v_pk_add_u16 v32, v32, 4 op_sel_hi:[1,0] clamp\nds_read_b32 v35, v9 offset:32\nv_mfma_f32_16x16x32_f16 v[24:27], v[104:107], v[44:47], v[24:27]\n" \
  "v_pk_add_u16 v33, v33, 4 op_sel_hi:[1,0] clamp\ns_waitcnt lgkmcnt(3)\nv_mfma_f32_16x16x32_f16 v[20:23], v[100:103], v[48:51], v[20:23]\n" \
  "ds_read_b128 v[44:47], v2 offset:1472\nds_write_b64 v4, v[32:33] offset:1024\nv_mfma_f32_16x16x32_f16 v[24:27], v[96:99], v[48:51], v[24:27]\n" \
  "v_mfma_f32_16x16x32_f16 v[20:23], v[92:95], v[52:55], v[20:23]\nv_mfma_f32_16x16x32_f16 v[24:27], v[88:91], v[52:55], v[24:27]\n" \
  "ds_read_b128 v[48:51], v2 offset:1536\ns_waitcnt lgkmcnt(3)\nv_readfirstlane_b32 s85, v35\ns_cmp_lt_u32 s85, 4\ns_cbranch_scc1 21f\n22:\n" \
  "v_mfma_f32_16x16x32_f16 v[20:23], v[84:87], v[36:39], v[20:23]\nv_mfma_f32_16x16x32_f16 v[24:27], v[80:83], v[36:39], v[24:27]\n" \
  "ds_read_b128 v[52:55], v2 offset:1600\nv_mfma_f32_16x16x32_f16 v[20:23], v[76:79], v[40:43], v[20:23]\n" \
  "v_mfma_f32_16x16x32_f16 v[24:27], v[72:75], v[40:43], v[24:27]\nds_read_b128 v[36:39], v2 offset:1664\ns_waitcnt lgkmcnt(4)\n" \
  "v_mfma_f32_16x16x32_f16 v[20:23], v[68:71], v[44:47], v[20:23]\nv_mfma_f32_16x16x32_f16 v[24:27], v[64:67], v[44:47], v[24:27]\n" \
  "ds_read_b128 v[40:43], v2 offset:1728\nv_mfma_f32_16x16x32_f16 v[28:31], v[60:63], v[44:47], v[28:31]\nds_read_b64_tr_b16 v[60:61], v5 offset:0\n" \
  "ds_read_b64_tr_b16 v[62:63], v5 offset:32\ns_waitcnt lgkmcnt(4)\nv_mfma_f32_16x16x32_f16 v[24:27], v[124:127], v[48:51], v[24:27]\n" \
  "v_mfma_f32_16x16x32_f16 v[28:31], v[120:123], v[48:51], v[28:31]\nds_read_b64_tr_b16 v[44:45], v5 offset:256\n" \
  "ds_read_b64_tr_b16 v[46:47], v5 offset:288\nv_mfma_f32_16x16x32_f16 v[24:27], v[116:119], v[52:55], v[24:27]\n" \
  "ds_read_b64 v[120:121], v6 offset:320\nds_read_b64 v[122:123], v6 offset:328\nv_mfma_f32_16x16x32_f16 v[28:31], v[112:115], v[52:55], v[28:31]\n" \
  "ds_read_b128 v[48:51], v2 offset:1792\ns_waitcnt lgkmcnt(7)\nv_mfma_f32_16x16x32_f16 v[24:27], v[108:111], v[36:39], v[24:27]\n" \
  "ds_read_b128 v[112:115], v2 offset:1856\nv_mfma_f32_16x16x32_f16 v[28:31], v[104:107], v[36:39], v[28:31]\nds_read_b64 v[52:53], v6 offset:128\n" \
  "ds_read_b64 v[54:55], v6 offset:136\nv_mfma_f32_16x16x32_f16 v[24:27], v[100:103], v[40:43], v[24:27]\nds_read_b128 v[104:107], v2 offset:1920\n" \
  "v_mfma_f32_16x16x32_f16 v[28:31], v[96:99], v[40:43], v[28:31]\nds_read_b64 v[36:37], v6 offset:192\nds_read_b64 v[38:39], v6 offset:200\n" \
  "s_waitcnt lgkmcnt(9)\nv_mfma_f32_16x16x32_f16 v[20:23], v[60:63], v[56:59], v[20:23]\nds_read_b128 v[96:99], v2 offset:1984\ns_waitcnt lgkmcnt(7)\n" \
  "v_mfma_f32_16x16x32_f16 v[20:23], v[44:47], v[120:123], v[20:23]\nv_mfma_f32_16x16x32_f16 v[24:27], v[92:95], v[48:51], v[24:27]\n" \
  "ds_read_b128 v[40:43], v2 offset:2048\nv_mfma_f32_16x16x32_f16 v[28:31], v[88:91], v[48:51], v[28:31]\nds_read_b64_tr_b16 v[88:89], v5 offset:512\n" \
  "ds_read_b64_tr_b16 v[90:91], v5 offset:544\ns_waitcnt lgkmcnt(6)\nv_mfma_f32_16x16x32_f16 v[24:27], v[84:87], v[112:115], v[24:27]\n" \
  "v_mfma_f32_16x16x32_f16 v[28:31], v[80:83], v[112:115], v[28:31]\nv_cvt_pkrtz_f16_f32 v32, v20, v21\nv_cvt_pkrtz_f16_f32 v33, v22, v23\n" \
  "v_mfma_f32_16x16x32_f16 v[24:27], v[76:79], v[104:107], v[24:27]\nds_read_b64_tr_b16 v[48:49], v5 offset:768\n" \
  "ds_read_b64_tr_b16 v[50:51], v5 offset:800\nv_mfma_f32_16x16x32_f16 v[28:31], v[72:75], v[104:107], v[28:31]\n" \
  "ds_read_b128 v[80:83], v2 offset:2112\ns_waitcnt lgkmcnt(5)\nv_mfma_f32_16x16x32_f16 v[24:27], v[68:71], v[96:99], v[24:27]\n" \
  "ds_read_b128 v[112:115], v2 offset:2176\nv_pk_add_u16 v32, v32, 4 op_sel_hi:[1,0] clamp\n" \
  "v_mfma_f32_16x16x32_f16 v[28:31], v[64:67], v[96:99], v[28:31]\nds_read_b128 v[72:75], v2 offset:2240\n" \
  "v_pk_add_u16 v33, v33, 4 op_sel_hi:[1,0] clamp\nv_mfma_f32_16x16x32_f16 v[28:31], v[124:127], v[40:43], v[28:31]\n" \
  "ds_read_b128 v[104:107], v2 offset:2304\nds_write_b64 v4, v[32:33] offset:2048\nv_mfma_f32_16x16x32_f16 v[24:27], v[60:63], v[52:55], v[24:27]\n" \
  "ds_read_b128 v[64:67], v2 offset:2368\nds_read_b64 v[96:97], v6 offset:0\nds_read_b64 v[98:99], v6 offset:8\n" \
  "v_mfma_f32_16x16x32_f16 v[24:27], v[44:47], v[36:39], v[24:27]\nds_read_b128 v[124:127], v2 offset:2432\ns_waitcnt lgkmcnt(9)\n" \
  "v_mfma_f32_16x16x32_f16 v[24:27], v[88:91], v[56:59], v[24:27]\nds_read_b128 v[40:43], v2 offset:2496\n" \
  "v_mfma_f32_16x16x32_f16 v[24:27], v[48:51], v[120:123], v[24:27]\ns_waitcnt lgkmcnt(8)\n" \
  "v_mfma_f32_16x16x32_f16 v[28:31], v[116:119], v[80:83], v[28:31]\nds_read_b64 v[116:117], v6 offset:64\nds_read_b64 v[118:119], v6 offset:72\n" \
  "v_mfma_f32_16x16x32_f16 v[28:31], v[108:111], v[112:115], v[28:31]\nds_read_b64_tr_b16 v[80:81], v5 offset:1024\n" \
  "ds_read_b64_tr_b16 v[82:83], v5 offset:1056\ns_waitcnt lgkmcnt(10)\nv_mfma_f32_16x16x32_f16 v[28:31], v[100:103], v[72:75], v[28:31]\n" \
  "v_cvt_pkrtz_f16_f32 v32, v24, v25\nv_cvt_pkrtz_f16_f32 v33, v26, v27\nv_mfma_f32_16x16x32_f16 v[28:31], v[92:95], v[104:107], v[28:31]\n" \
  "v_pk_add_u16 v32, v32, 4 op_sel_hi:[1,0] clamp\ns_waitcnt lgkmcnt(5)\nv_mfma_f32_16x16x32_f16 v[28:31], v[84:87], v[64:67], v[28:31]\n" \
  "ds_read_b64_tr_b16 v[108:109], v5 offset:1280\nds_read_b64_tr_b16 v[110:111], v5 offset:1312\n" \
  "v_mfma_f32_16x16x32_f16 v[28:31], v[76:79], v[124:127], v[28:31]\nds_read_b64_tr_b16 v[112:113], v7 offset:0\n" \
  "ds_read_b64_tr_b16 v[114:115], v7 offset:64\ns_waitcnt lgkmcnt(6)\nv_mfma_f32_16x16x32_f16 v[28:31], v[68:71], v[40:43], v[28:31]\n" \
  "ds_read2_b32 v[100:101], v8 offset0:48 offset1:49\nds_read2_b32 v[102:103], v8 offset0:50 offset1:51\n" \
  "v_mfma_f32_16x16x32_f16 v[28:31], v[60:63], v[96:99], v[28:31]\nv_pk_add_u16 v33, v33, 4 op_sel_hi:[1,0] clamp\n" \
  "ds_write_b64 v4, v[32:33] offset:3072\nv_mfma_f32_16x16x32_f16 v[28:31], v[44:47], v[116:119], v[28:31]\n" \
  "v_mfma_f32_16x16x32_f16 v[28:31], v[88:91], v[52:55], v[28:31]\nv_mfma_f32_16x16x32_f16 v[28:31], v[48:51], v[36:39], v[28:31]\n" \
  "s_waitcnt lgkmcnt(5)\nv_mfma_f32_16x16x32_f16 v[28:31], v[80:83], v[56:59], v[28:31]\n" \
  "v_mfma_f32_16x16x32_f16 v[28:31], v[108:111], v[120:123], v[28:31]\ns_waitcnt lgkmcnt(1)\n" \
  "v_mfma_f32_16x16x32_f16 v[28:31], v[112:115], v[100:103], v[28:31]\ns_nop 7\nv_cvt_pkrtz_f16_f32 v32, v28, v29\nv_cvt_pkrtz_f16_f32 v33, v30, v31\n" \
  "v_pk_add_u16 v32, v32, 4 op_sel_hi:[1,0] clamp\nv_pk_add_u16 v33, v33, 4 op_sel_hi:[1,0] clamp\nds_write_b64 v4, v[32:33] offset:4096\n" \
  "s_setprio 0\ns_setpc_b64 s[90:91]\n21:\ns_movk_i32 s84, 0x4000\n23:\nds_read_b32 v35, v9 offset:32\ns_sub_u32 s84, s84, 1\ns_cbranch_scc1 22b\n" \
  "s_waitcnt lgkmcnt(0)\nv_readfirstlane_b32 s85, v35\ns_cmp_lt_u32 s85, 4\ns_cbranch_scc1 23b\ns_branch 22b\n1:\n" \
  "ds_read_b64 v[124:125], v3 offset:1088\nds_read_b64 v[126:127], v3 offset:1096\nds_read_b64 v[120:121], v3 offset:0\n" \
  "ds_read_b64 v[122:123], v3 offset:8\nds_read_b64 v[116:117], v3 offset:64\nds_read_b64 v[118:119], v3 offset:72\n" \
  "ds_read_b64 v[112:113], v3 offset:128\nds_read_b64 v[114:115], v3 offset:136\nds_read_b64 v[108:109], v3 offset:192\n" \
  "ds_read_b64 v[110:111], v3 offset:200\nds_read_b64 v[104:105], v3 offset:256\nds_read_b64 v[106:107], v3 offset:264\n" \
  "ds_read_b64 v[100:101], v3 offset:320\nds_read_b64 v[102:103], v3 offset:328\nds_read_b64 v[96:97], v3 offset:384\n" \
  "ds_read_b64 v[98:99], v3 offset:392\nds_read_b64 v[92:93], v3 offset:448\nds_read_b64 v[94:95], v3 offset:456\n" \
  "ds_read_b64 v[88:89], v3 offset:512\nds_read_b64 v[90:91], v3 offset:520\nds_read_b64 v[84:85], v3 offset:576\n" \
  "ds_read_b64 v[86:87], v3 offset:584\nds_read_b64 v[80:81], v3 offset:640\nds_read_b64 v[82:83], v3 offset:648\n" \
  "ds_read_b64 v[76:77], v3 offset:704\nds_read_b64 v[78:79], v3 offset:712\nds_read_b64 v[72:73], v3 offset:768\n" \
  "ds_read_b64 v[74:75], v3 offset:776\nds_read_b64 v[68:69], v3 offset:832\nds_read_b64 v[70:71], v3 offset:840\n" \
  "ds_read_b64 v[64:65], v3 offset:896\nds_read_b64 v[66:67], v3 offset:904\nds_read_b64 v[60:61], v3 offset:960\n" \
  "ds_read_b64 v[62:63], v3 offset:968\nds_read_b64 v[56:57], v3 offset:1024\nds_read_b64 v[58:59], v3 offset:1032\n" \
  "ds_read_b64 v[52:53], v6 offset:0\nds_read_b64 v[54:55], v6 offset:8\nds_read_b64 v[48:49], v6 offset:64\nds_read_b64 v[50:51], v6 offset:72\n" \
  "s_waitcnt lgkmcnt(0)\ns_barrier\nds_read_b128 v[28:31], v2 offset:960\nds_read_b128 v[32:35], v2 offset:1472\n" \
  "ds_read_b128 v[36:39], v2 offset:1984\nds_read_b128 v[40:43], v2 offset:2048\nds_read_b128 v[44:47], v2 offset:2112\ns_waitcnt lgkmcnt(3)\n" \
  "v_mfma_f32_16x16x32_f16 v[12:15], v[124:127], v[28:31], 0\ns_setprio 1\nds_read_b128 v[28:31], v2 offset:2176\n" \
  "v_mfma_f32_16x16x32_f16 v[16:19], v[124:127], v[32:35], 0\nds_read_b128 v[32:35], v2 offset:2240\ns_waitcnt lgkmcnt(3)\n" \
  "v_mfma_f32_16x16x32_f16 v[12:15], v[120:123], v[36:39], v[12:15]\nv_mfma_f32_16x16x32_f16 v[20:23], v[124:127], v[36:39], 0\n" \
  "ds_read_b128 v[124:127], v2 offset:2304\nds_read_b128 v[36:39], v2 offset:2368\nv_mfma_f32_16x16x32_f16 v[12:15], v[116:119], v[40:43], v[12:15]\n" \
  "ds_read_b128 v[40:43], v2 offset:2432\ns_waitcnt lgkmcnt(4)\nv_mfma_f32_16x16x32_f16 v[12:15], v[112:115], v[44:47], v[12:15]\n" \
  "ds_read_b128 v[44:47], v2 offset:2496\nv_mfma_f32_16x16x32_f16 v[12:15], v[108:111], v[28:31], v[12:15]\nds_read_b128 v[28:31], v2 offset:2560\n" \
  "s_waitcnt lgkmcnt(4)\nv_mfma_f32_16x16x32_f16 v[12:15], v[104:107], v[32:35], v[12:15]\nds_read_b128 v[32:35], v2 offset:2624\n" \
  "v_mfma_f32_16x16x32_f16 v[12:15], v[100:103], v[124:127], v[12:15]\ns_waitcnt lgkmcnt(3)\n" \
  "v_mfma_f32_16x16x32_f16 v[12:15], v[96:99], v[36:39], v[12:15]\nds_read_b128 v[124:127], v2 offset:2688\n" \
  "v_mfma_f32_16x16x32_f16 v[12:15], v[92:95], v[40:43], v[12:15]\ns_waitcnt lgkmcnt(3)\n" \
  "v_mfma_f32_16x16x32_f16 v[12:15], v[88:91], v[44:47], v[12:15]\nds_read_b128 v[36:39], v2 offset:2752\n" \
  "v_mfma_f32_16x16x32_f16 v[16:19], v[120:123], v[44:47], v[16:19]\ns_waitcnt lgkmcnt(3)\n" \
  "v_mfma_f32_16x16x32_f16 v[12:15], v[84:87], v[28:31], v[12:15]\nds_read_b128 v[40:43], v2 offset:2816\n" \
  "v_mfma_f32_16x16x32_f16 v[16:19], v[116:119], v[28:31], v[16:19]\ns_waitcnt lgkmcnt(3)\n" \
  "v_mfma_f32_16x16x32_f16 v[12:15], v[80:83], v[32:35], v[12:15]\nds_read_b128 v[44:47], v2 offset:2880\n" \
  "v_mfma_f32_16x16x32_f16 v[16:19], v[112:115], v[32:35], v[16:19]\ns_waitcnt lgkmcnt(3)\n" \
  "v_mfma_f32_16x16x32_f16 v[12:15], v[76:79], v[124:127], v[12:15]\nds_read_b128 v[28:31], v2 offset:2944\n" \
  "v_mfma_f32_16x16x32_f16 v[16:19], v[108:111], v[124:127], v[16:19]\ns_waitcnt lgkmcnt(3)\n" \
  "v_mfma_f32_16x16x32_f16 v[12:15], v[72:75], v[36:39], v[12:15]\nds_read_b128 v[32:35], v2 offset:3008\n" \
  "v_mfma_f32_16x16x32_f16 v[16:19], v[104:107], v[36:39], v[16:19]\ns_waitcnt lgkmcnt(3)\n" \
  "v_mfma_f32_16x16x32_f16 v[12:15], v[68:71], v[40:43], v[12:15]\nv_mfma_f32_16x16x32_f16 v[16:19], v[100:103], v[40:43], v[16:19]\n" \
  "ds_read_b128 v[124:127], v2 offset:3072\ns_waitcnt lgkmcnt(3)\nv_mfma_f32_16x16x32_f16 v[12:15], v[64:67], v[44:47], v[12:15]\n" \
  "v_mfma_f32_16x16x32_f16 v[16:19], v[96:99], v[44:47], v[16:19]\nds_read_b128 v[36:39], v2 offset:3136\ns_waitcnt lgkmcnt(3)\n" \
  "v_mfma_f32_16x16x32_f16 v[12:15], v[60:63], v[28:31], v[12:15]\nds_read_b64 v[40:41], v6 offset:128\nds_read_b64 v[42:43], v6 offset:136\n" \
  "v_mfma_f32_16x16x32_f16 v[16:19], v[92:95], v[28:31], v[16:19]\nds_read_b128 v[44:47], v2 offset:3200\ns_waitcnt lgkmcnt(4)\n" \
  "v_mfma_f32_16x16x32_f16 v[12:15], v[56:59], v[32:35], v[12:15]\nv_mfma_f32_16x16x32_f16 v[16:19], v[88:91], v[32:35], v[16:19]\n" \
  "ds_read_b128 v[28:31], v2 offset:3264\nv_mfma_f32_16x16x32_f16 v[20:23], v[120:123], v[32:35], v[20:23]\nds_read_b64 v[120:121], v6 offset:192\n" \
  "ds_read_b64 v[122:123], v6 offset:200\nv_mfma_f32_16x16x32_f16 v[16:19], v[84:87], v[124:127], v[16:19]\nds_read_b128 v[32:35], v2 offset:3328\n" \
  "v_mfma_f32_16x16x32_f16 v[20:23], v[116:119], v[124:127], v[20:23]\nds_read_b64 v[116:117], v6 offset:256\nds_read_b64 v[118:119], v6 offset:264\n" \
  "s_waitcnt lgkmcnt(6)\nv_mfma_f32_16x16x32_f16 v[16:19], v[80:83], v[36:39], v[16:19]\nds_read_b128 v[124:127], v2 offset:3392\n" \
  "v_mfma_f32_16x16x32_f16 v[20:23], v[112:115], v[36:39], v[20:23]\nds_read_b64 v[112:113], v6 offset:320\nds_read_b64 v[114:115], v6 offset:328\n" \
  "v_mfma_f32_16x16x32_f16 v[16:19], v[76:79], v[44:47], v[16:19]\nds_read_b128 v[36:39], v2 offset:3456\n" \
  "v_mfma_f32_16x16x32_f16 v[20:23], v[108:111], v[44:47], v[20:23]\ns_waitcnt lgkmcnt(6)\n" \
  "v_mfma_f32_16x16x32_f16 v[16:19], v[72:75], v[28:31], v[16:19]\nds_read_b128 v[108:111], v2 offset:3520\n" \
  "v_mfma_f32_16x16x32_f16 v[20:23], v[104:107], v[28:31], v[20:23]\nv_mfma_f32_16x16x32_f16 v[16:19], v[68:71], v[32:35], v[16:19]\n" \
  "ds_read_b128 v[44:47], v2 offset:3584\nv_mfma_f32_16x16x32_f16 v[20:23], v[100:103], v[32:35], v[20:23]\nds_read_b128 v[104:107], v2 offset:3648\n" \
  "ds_read_b32 v27, v9 offset:32\ns_waitcnt lgkmcnt(4)\nv_mfma_f32_16x16x32_f16 v[16:19], v[64:67], v[124:127], v[16:19]\n" \
  "ds_read_b128 v[28:31], v2 offset:3712\nv_mfma_f32_16x16x32_f16 v[20:23], v[96:99], v[124:127], v[20:23]\nds_read_b128 v[100:103], v2 offset:3776\n" \
  "ds_read2_b32 v[32:33], v8 offset0:32 offset1:33\nds_read2_b32 v[34:35], v8 offset0:34 offset1:35\n" \
  "v_mfma_f32_16x16x32_f16 v[16:19], v[60:63], v[36:39], v[16:19]\nds_read_b128 v[96:99], v2 offset:3840\n" \
  "v_mfma_f32_16x16x32_f16 v[20:23], v[92:95], v[36:39], v[20:23]\nds_read_b128 v[124:127], v2 offset:3904\ns_waitcnt lgkmcnt(8)\n" \
  "v_mfma_f32_16x16x32_f16 v[16:19], v[56:59], v[108:111], v[16:19]\nds_read_b128 v[92:95], v2 offset:3968\ns_waitcnt lgkmcnt(7)\n" \
  "v_readfirstlane_b32 s85, v27\ns_cmp_lt_u32 s85, 4\ns_cbranch_scc1 31f\n32:\nv_mfma_f32_16x16x32_f16 v[20:23], v[88:91], v[108:111], v[20:23]\n" \
  "ds_read_b128 v[36:39], v2 offset:4032\nds_read_b64_tr_b16 v[88:89], v5 offset:512\nds_read_b64_tr_b16 v[90:91], v5 offset:544\n" \
  "v_mfma_f32_16x16x32_f16 v[20:23], v[84:87], v[44:47], v[20:23]\nds_read_b64_tr_b16 v[108:109], v5 offset:768\n" \
  "ds_read_b64_tr_b16 v[110:111], v5 offset:800\nv_mfma_f32_16x16x32_f16 v[20:23], v[80:83], v[104:107], v[20:23]\n" \
  "ds_read_b64_tr_b16 v[84:85], v5 offset:1024\nds_read_b64_tr_b16 v[86:87], v5 offset:1056\ns_waitcnt lgkmcnt(12)\n" \
  "v_mfma_f32_16x16x32_f16 v[20:23], v[76:79], v[28:31], v[20:23]\nds_read_b64_tr_b16 v[44:45], v5 offset:1280\n" \
  "ds_read_b64_tr_b16 v[46:47], v5 offset:1312\nv_mfma_f32_16x16x32_f16 v[20:23], v[72:75], v[100:103], v[20:23]\ns_waitcnt lgkmcnt(10)\n" \
  "v_mfma_f32_16x16x32_f16 v[20:23], v[68:71], v[96:99], v[20:23]\nds_read_b64_tr_b16 v[80:81], v5 offset:1536\n" \
  "ds_read_b64_tr_b16 v[82:83], v5 offset:1568\nv_mfma_f32_16x16x32_f16 v[20:23], v[64:67], v[124:127], v[20:23]\n" \
  "ds_read_b64_tr_b16 v[104:105], v5 offset:1792\nds_read_b64_tr_b16 v[106:107], v5 offset:1824\ns_waitcnt lgkmcnt(12)\n" \
  "v_mfma_f32_16x16x32_f16 v[20:23], v[60:63], v[92:95], v[20:23]\nds_read_b64_tr_b16 v[76:77], v5 offset:2048\n" \
  "ds_read_b64_tr_b16 v[78:79], v5 offset:2080\nv_mfma_f32_16x16x32_f16 v[20:23], v[56:59], v[36:39], v[20:23]\ns_waitcnt lgkmcnt(10)\n" \
  "v_mfma_f32_16x16x32_f16 v[12:15], v[88:91], v[52:55], v[12:15]\nds_read_b64_tr_b16 v[28:29], v5 offset:2304\n" \
  "ds_read_b64_tr_b16 v[30:31], v5 offset:2336\nv_mfma_f32_16x16x32_f16 v[12:15], v[108:111], v[48:51], v[12:15]\n" \
  "ds_read_b64_tr_b16 v[72:73], v5 offset:2560\nds_read_b64_tr_b16 v[74:75], v5 offset:2592\ns_waitcnt lgkmcnt(10)\n" \
  "v_mfma_f32_16x16x32_f16 v[12:15], v[84:87], v[40:43], v[12:15]\nds_read_b64_tr_b16 v[100:101], v5 offset:2816\n" \
  "ds_read_b64_tr_b16 v[102:103], v5 offset:2848\nv_mfma_f32_16x16x32_f16 v[16:19], v[84:87], v[52:55], v[16:19]\n" \
  "ds_read_b64_tr_b16 v[68:69], v7 offset:0\nds_read_b64_tr_b16 v[70:71], v7 offset:64\n" \
  "v_mfma_f32_16x16x32_f16 v[12:15], v[44:47], v[120:123], v[12:15]\nv_mfma_f32_16x16x32_f16 v[16:19], v[44:47], v[48:51], v[16:19]\n" \
  "s_waitcnt lgkmcnt(10)\nv_mfma_f32_16x16x32_f16 v[12:15], v[80:83], v[116:119], v[12:15]\nds_read2_b32 v[96:97], v8 offset0:16 offset1:17\n" \
  "ds_read2_b32 v[98:99], v8 offset0:18 offset1:19\nv_mfma_f32_16x16x32_f16 v[16:19], v[80:83], v[40:43], v[16:19]\n" \
  "ds_read2_b32 v[64:65], v8 offset0:0 offset1:1\nds_read2_b32 v[66:67], v8 offset0:2 offset1:3\n" \
  "v_mfma_f32_16x16x32_f16 v[20:23], v[80:83], v[52:55], v[20:23]\nv_mfma_f32_16x16x32_f16 v[12:15], v[104:107], v[112:115], v[12:15]\n" \
  "v_mfma_f32_16x16x32_f16 v[16:19], v[104:107], v[120:123], v[16:19]\nv_mfma_f32_16x16x32_f16 v[20:23], v[104:107], v[48:51], v[20:23]\n" \
  "s_waitcnt lgkmcnt(10)\nv_mfma_f32_16x16x32_f16 v[16:19], v[76:79], v[116:119], v[16:19]\nds_read_b64_tr_b16 v[124:125], v7 offset:512\n" \
  "ds_read_b64_tr_b16 v[126:127], v7 offset:576\nv_mfma_f32_16x16x32_f16 v[20:23], v[76:79], v[40:43], v[20:23]\n" \
  "ds_read2_b32 v[60:61], v8 offset0:48 offset1:49\nds_read2_b32 v[62:63], v8 offset0:50 offset1:51\n" \
  "v_mfma_f32_16x16x32_f16 v[16:19], v[28:31], v[112:115], v[16:19]\nv_mfma_f32_16x16x32_f16 v[20:23], v[28:31], v[120:123], v[20:23]\n" \
  "s_waitcnt lgkmcnt(10)\nv_mfma_f32_16x16x32_f16 v[20:23], v[72:75], v[116:119], v[20:23]\nds_read_b64_tr_b16 v[92:93], v7 offset:1024\n" \
  "ds_read_b64_tr_b16 v[94:95], v7 offset:1088\nv_mfma_f32_16x16x32_f16 v[20:23], v[100:103], v[112:115], v[20:23]\n" \
  "ds_read_b64_tr_b16 v[56:57], v7 offset:1536\nds_read_b64_tr_b16 v[58:59], v7 offset:1600\ns_waitcnt lgkmcnt(10)\n" \
  "v_mfma_f32_16x16x32_f16 v[12:15], v[68:71], v[32:35], v[12:15]\nv_mfma_f32_16x16x32_f16 v[16:19], v[68:71], v[96:99], v[16:19]\n" \
  "s_waitcnt lgkmcnt(6)\nv_mfma_f32_16x16x32_f16 v[20:23], v[68:71], v[64:67], v[20:23]\ns_waitcnt lgkmcnt(2)\n" \
  "v_mfma_f32_16x16x32_f16 v[12:15], v[124:127], v[60:63], v[12:15]\nv_mfma_f32_16x16x32_f16 v[16:19], v[124:127], v[32:35], v[16:19]\n" \
  "v_mfma_f32_16x16x32_f16 v[20:23], v[124:127], v[96:99], v[20:23]\nv_mfma_f32_16x16x32_f16 v[16:19], v[92:95], v[60:63], v[16:19]\n" \
  "v_mfma_f32_16x16x32_f16 v[20:23], v[92:95], v[32:35], v[20:23]\ns_waitcnt lgkmcnt(0)\n" \
  "v_mfma_f32_16x16x32_f16 v[20:23], v[56:59], v[60:63], v[20:23]\ns_nop 1\nv_cvt_pkrtz_f16_f32 v24, v12, v13\nv_cvt_pkrtz_f16_f32 v25, v14, v15\n" \
  "v_pk_add_u16 v24, v24, 4 op_sel_hi:[1,0] clamp\nv_pk_add_u16 v25, v25, 4 op_sel_hi:[1,0] clamp\nds_write_b64 v4, v[24:25] offset:5120\n" \
  "v_cvt_pkrtz_f16_f32 v24, v16, v17\nv_cvt_pkrtz_f16_f32 v25, v18, v19\nv_pk_add_u16 v24, v24, 4 op_sel_hi:[1,0] clamp\n" \
  "v_pk_add_u16 v25, v25, 4 op_sel_hi:[1,0] clamp\nds_write_b64 v4, v[24:25] offset:6144\nv_cvt_pkrtz_f16_f32 v24, v20, v21\n" \
  "v_cvt_pkrtz_f16_f32 v25, v22, v23\nv_pk_add_u16 v24, v24, 4 op_sel_hi:[1,0] clamp\nv_pk_add_u16 v25, v25, 4 op_sel_hi:[1,0] clamp\n" \
  "ds_write_b64 v4, v[24:25] offset:7168\ns_setprio 0\ns_setpc_b64 s[90:91]\n31:\ns_movk_i32 s84, 0x4000\n33:\nds_read_b32 v27, v9 offset:32\n" \
  "s_sub_u32 s84, s84, 1\ns_cbranch_scc1 32b\ns_waitcnt lgkmcnt(0)\nv_readfirstlane_b32 s85, v27\ns_cmp_lt_u32 s85, 4\ns_cbranch_scc1 33b\n" \
  "s_branch 32b\n9:\ns_cmp_eq_u32 s89, 3\ns_cbranch_scc1 3f\ns_cmp_eq_u32 s89, 4\ns_cbranch_scc1 4f\ns_cmp_eq_u32 s89, 5\ns_cbranch_scc1 5f\n" \
  "ds_read_b64 v[124:125], v3 offset:576\nds_read_b64 v[126:127], v3 offset:584\nds_read_b64 v[120:121], v3 offset:64\n" \
  "ds_read_b64 v[122:123], v3 offset:72\nds_read_b64 v[116:117], v3 offset:640\nds_read_b64 v[118:119], v3 offset:648\n" \
  "ds_read_b64 v[112:113], v3 offset:128\nds_read_b64 v[114:115], v3 offset:136\nds_read_b64 v[108:109], v3 offset:704\n" \
  "ds_read_b64 v[110:111], v3 offset:712\nds_read_b64 v[104:105], v3 offset:192\nds_read_b64 v[106:107], v3 offset:200\n" \
  "ds_read_b64 v[100:101], v3 offset:768\nds_read_b64 v[102:103], v3 offset:776\nds_read_b64 v[96:97], v3 offset:256\n" \
  "ds_read_b64 v[98:99], v3 offset:264\nds_read_b64 v[92:93], v3 offset:832\nds_read_b64 v[94:95], v3 offset:840\n" \
  "ds_read_b64 v[88:89], v3 offset:320\nds_read_b64 v[90:91], v3 offset:328\nds_read_b64 v[84:85], v3 offset:896\n" \
  "ds_read_b64 v[86:87], v3 offset:904\nds_read_b64 v[80:81], v3 offset:384\nds_read_b64 v[82:83], v3 offset:392\n" \
  "ds_read_b64 v[76:77], v3 offset:960\nds_read_b64 v[78:79], v3 offset:968\nds_read_b64 v[72:73], v3 offset:448\n" \
  "ds_read_b64 v[74:75], v3 offset:456\nds_read_b64 v[68:69], v3 offset:1024\nds_read_b64 v[70:71], v3 offset:1032\n" \
  "ds_read_b64 v[64:65], v3 offset:512\nds_read_b64 v[66:67], v3 offset:520\ns_waitcnt lgkmcnt(0)\ns_barrier\nds_read_b128 v[24:27], v2 offset:0\n" \
  "ds_read_b128 v[28:31], v2 offset:64\nds_read_b128 v[32:35], v2 offset:128\nds_read_b128 v[36:39], v2 offset:192\ns_waitcnt lgkmcnt(3)\n" \
  "v_mfma_f32_16x16x32_f16 v[12:15], v[124:127], v[24:27], 0\ns_setprio 1\nds_read_b128 v[40:43], v2 offset:256\n" \
  "v_mfma_f32_16x16x32_f16 v[16:19], v[120:123], v[24:27], 0\ns_waitcnt lgkmcnt(3)\nv_mfma_f32_16x16x32_f16 v[12:15], v[116:119], v[28:31], v[12:15]\n" \
  "ds_read_b128 v[44:47], v2 offset:320\nv_mfma_f32_16x16x32_f16 v[16:19], v[112:115], v[28:31], v[16:19]\ns_waitcnt lgkmcnt(3)\n" \
  "v_mfma_f32_16x16x32_f16 v[12:15], v[108:111], v[32:35], v[12:15]\nds_read_b128 v[48:51], v2 offset:384\n" \
  "v_mfma_f32_16x16x32_f16 v[16:19], v[104:107], v[32:35], v[16:19]\ns_waitcnt lgkmcnt(3)\n" \
  "v_mfma_f32_16x16x32_f16 v[12:15], v[100:103], v[36:39], v[12:15]\nds_read_b128 v[52:55], v2 offset:448\n" \
  "v_mfma_f32_16x16x32_f16 v[16:19], v[96:99], v[36:39], v[16:19]\ns_waitcnt lgkmcnt(3)\n" \
  "v_mfma_f32_16x16x32_f16 v[12:15], v[92:95], v[40:43], v[12:15]\nds_read_b128 v[56:59], v2 offset:512\n" \
  "v_mfma_f32_16x16x32_f16 v[16:19], v[88:91], v[40:43], v[16:19]\nds_read_b128 v[60:63], v2 offset:576\ns_waitcnt lgkmcnt(3)\n" \
  "v_mfma_f32_16x16x32_f16 v[12:15], v[84:87], v[44:47], v[12:15]\nds_read_b128 v[120:123], v2 offset:640\n" \
  "v_mfma_f32_16x16x32_f16 v[16:19], v[80:83], v[44:47], v[16:19]\nds_read_b128 v[24:27], v2 offset:704\n" \
  "v_mfma_f32_16x16x32_f16 v[12:15], v[76:79], v[48:51], v[12:15]\nds_read_b128 v[112:115], v2 offset:768\n" \
  "v_mfma_f32_16x16x32_f16 v[16:19], v[72:75], v[48:51], v[16:19]\nds_read_b128 v[28:31], v2 offset:832\ns_waitcnt lgkmcnt(5)\n" \
  "v_mfma_f32_16x16x32_f16 v[12:15], v[68:71], v[52:55], v[12:15]\nds_read_b128 v[104:107], v2 offset:896\n" \
  "v_mfma_f32_16x16x32_f16 v[16:19], v[64:67], v[52:55], v[16:19]\nds_read_b128 v[32:35], v2 offset:960\n" \
  "v_mfma_f32_16x16x32_f16 v[16:19], v[124:127], v[56:59], v[16:19]\ns_waitcnt lgkmcnt(5)\n" \
  "v_mfma_f32_16x16x32_f16 v[16:19], v[116:119], v[60:63], v[16:19]\nv_mfma_f32_16x16x32_f16 v[16:19], v[108:111], v[120:123], v[16:19]\n" \
  "s_waitcnt lgkmcnt(3)\nv_mfma_f32_16x16x32_f16 v[16:19], v[100:103], v[24:27], v[16:19]\nv_cvt_pkrtz_f16_f32 v20, v12, v13\n" \
  "v_cvt_pkrtz_f16_f32 v21, v14, v15\nv_mfma_f32_16x16x32_f16 v[16:19], v[92:95], v[112:115], v[16:19]\n" \
  "v_pk_add_u16 v20, v20, 4 op_sel_hi:[1,0] clamp\ns_waitcnt lgkmcnt(2)\nv_mfma_f32_16x16x32_f16 v[16:19], v[84:87], v[28:31], v[16:19]\n" \
  "v_pk_add_u16 v21, v21, 4 op_sel_hi:[1,0] clamp\ns_waitcnt lgkmcnt(1)\nv_mfma_f32_16x16x32_f16 v[16:19], v[76:79], v[104:107], v[16:19]\n" \
  "ds_write_b64 v4, v[20:21] offset:0\ns_waitcnt lgkmcnt(1)\nv_mfma_f32_16x16x32_f16 v[16:19], v[68:71], v[32:35], v[16:19]\ns_nop 7\n" \
  "v_cvt_pkrtz_f16_f32 v20, v16, v17\nv_cvt_pkrtz_f16_f32 v21, v18, v19\nv_pk_add_u16 v20, v20, 4 op_sel_hi:[1,0] clamp\n" \
  "v_pk_add_u16 v21, v21, 4 op_sel_hi:[1,0] clamp\nds_write_b64 v4, v[20:21] offset:1024\ns_setprio 0\ns_setpc_b64 s[90:91]\n3:\n" \
  "ds_read_b64 v[124:125], v3 offset:0\nds_read_b64 v[126:127], v3 offset:8\nds_read_b64 v[120:121], v3 offset:64\n" \
  "ds_read_b64 v[122:123], v3 offset:72\nds_read_b64 v[116:117], v3 offset:128\nds_read_b64 v[118:119], v3 offset:136\n" \
  "ds_read_b64 v[112:113], v3 offset:192\nds_read_b64 v[114:115], v3 offset:200\nds_read_b64 v[108:109], v3 offset:256\n" \
  "ds_read_b64 v[110:111], v3 offset:264\nds_read_b64 v[104:105], v3 offset:320\nds_read_b64 v[106:107], v3 offset:328\n" \
  "ds_read_b64 v[100:101], v3 offset:384\nds_read_b64 v[102:103], v3 offset:392\nds_read_b64 v[96:97], v3 offset:448\n" \
  "ds_read_b64 v[98:99], v3 offset:456\nds_read_b64 v[92:93], v3 offset:512\nds_read_b64 v[94:95], v3 offset:520\n" \
  "ds_read_b64 v[88:89], v3 offset:576\nds_read_b64 v[90:91], v3 offset:584\nds_read_b64 v[84:85], v3 offset:640\n" \
  "ds_read_b64 v[86:87], v3 offset:648\nds_read_b64 v[80:81], v3 offset:704\nds_read_b64 v[82:83], v3 offset:712\n" \
  "ds_read_b64 v[76:77], v3 offset:768\nds_read_b64 v[78:79], v3 offset:776\nds_read_b64 v[72:73], v3 offset:832\n" \
  "ds_read_b64 v[74:75], v3 offset:840\nds_read_b64 v[68:69], v3 offset:896\nds_read_b64 v[70:71], v3 offset:904\n" \
  "ds_read_b64 v[64:65], v3 offset:960\nds_read_b64 v[66:67], v3 offset:968\nds_read_b64 v[60:61], v3 offset:1024\n" \
  "ds_read_b64 v[62:63], v3 offset:1032\nds_read_b64 v[56:57], v6 offset:256\nds_read_b64 v[58:59], v6 offset:264\n" \
  "ds_read_b64 v[52:53], v6 offset:128\nds_read_b64 v[54:55], v6 offset:136\nds_read_b64 v[48:49], v6 offset:320\n" \
  "ds_read_b64 v[50:51], v6 offset:328\nds_read_b64 v[44:45], v6 offset:192\nds_read_b64 v[46:47], v6 offset:200\ns_waitcnt lgkmcnt(0)\ns_barrier\n" \
  "ds_read_b128 v[24:27], v2 offset:448\nds_read_b128 v[28:31], v2 offset:512\nds_read_b128 v[32:35], v2 offset:576\n" \
  "ds_read_b128 v[36:39], v2 offset:640\nds_read_b128 v[40:43], v2 offset:704\ns_waitcnt lgkmcnt(3)\n" \
  "v_mfma_f32_16x16x32_f16 v[12:15], v[124:127], v[24:27], 0\ns_setprio 1\nds_read_b128 v[24:27], v2 offset:768\n" \
  "v_mfma_f32_16x16x32_f16 v[12:15], v[120:123], v[28:31], v[12:15]\nds_read_b128 v[28:31], v2 offset:832\ns_waitcnt lgkmcnt(3)\n" \
  "v_mfma_f32_16x16x32_f16 v[12:15], v[116:119], v[32:35], v[12:15]\nds_read_b128 v[32:35], v2 offset:896\n" \
  "v_mfma_f32_16x16x32_f16 v[12:15], v[112:115], v[36:39], v[12:15]\nds_read_b128 v[36:39], v2 offset:960\ns_waitcnt lgkmcnt(3)\n" \
  "v_mfma_f32_16x16x32_f16 v[12:15], v[108:111], v[40:43], v[12:15]\nds_read_b128 v[40:43], v2 offset:1024\n" \
  "v_mfma_f32_16x16x32_f16 v[12:15], v[104:107], v[24:27], v[12:15]\nds_read_b128 v[24:27], v2 offset:1088\ns_waitcnt lgkmcnt(3)\n" \
  "v_mfma_f32_16x16x32_f16 v[12:15], v[100:103], v[28:31], v[12:15]\nds_read_b128 v[28:31], v2 offset:1152\n" \
  "v_mfma_f32_16x16x32_f16 v[12:15], v[96:99], v[32:35], v[12:15]\ns_waitcnt lgkmcnt(3)\n" \
  "v_mfma_f32_16x16x32_f16 v[12:15], v[92:95], v[36:39], v[12:15]\nds_read_b128 v[32:35], v2 offset:1216\n" \
  "v_mfma_f32_16x16x32_f16 v[16:19], v[124:127], v[36:39], 0\ns_waitcnt lgkmcnt(3)\nv_mfma_f32_16x16x32_f16 v[12:15], v[88:91], v[40:43], v[12:15]\n" \
  "ds_read_b128 v[124:127], v2 offset:1280\nv_mfma_f32_16x16x32_f16 v[16:19], v[120:123], v[40:43], v[16:19]\ns_waitcnt lgkmcnt(3)\n" \
  "v_mfma_f32_16x16x32_f16 v[12:15], v[84:87], v[24:27], v[12:15]\nds_read_b128 v[36:39], v2 offset:1344\n" \
  "v_mfma_f32_16x16x32_f16 v[16:19], v[116:119], v[24:27], v[16:19]\ns_waitcnt lgkmcnt(3)\n" \
  "v_mfma_f32_16x16x32_f16 v[12:15], v[80:83], v[28:31], v[12:15]\nds_read_b128 v[120:123], v2 offset:1408\n" \
  "v_mfma_f32_16x16x32_f16 v[16:19], v[112:115], v[28:31], v[16:19]\ns_waitcnt lgkmcnt(3)\n" \
  "v_mfma_f32_16x16x32_f16 v[12:15], v[76:79], v[32:35], v[12:15]\nds_read_b128 v[40:43], v2 offset:1472\n" \
  "v_mfma_f32_16x16x32_f16 v[16:19], v[108:111], v[32:35], v[16:19]\ns_waitcnt lgkmcnt(3)\n" \
  "v_mfma_f32_16x16x32_f16 v[12:15], v[72:75], v[124:127], v[12:15]\nds_read_b128 v[116:119], v2 offset:1536\n" \
  "v_mfma_f32_16x16x32_f16 v[16:19], v[104:107], v[124:127], v[16:19]\nds_read_b128 v[24:27], v2 offset:1600\ns_waitcnt lgkmcnt(3)\n" \
  "v_mfma_f32_16x16x32_f16 v[12:15], v[68:71], v[36:39], v[12:15]\nds_read_b128 v[112:115], v2 offset:1664\nds_read_b32 v23, v9 offset:32\n" \
  "v_mfma_f32_16x16x32_f16 v[16:19], v[100:103], v[36:39], v[16:19]\nds_read_b128 v[28:31], v2 offset:1728\n" \
  "v_mfma_f32_16x16x32_f16 v[12:15], v[64:67], v[120:123], v[12:15]\nds_read_b128 v[108:111], v2 offset:1792\n" \
  "v_mfma_f32_16x16x32_f16 v[16:19], v[96:99], v[120:123], v[16:19]\nds_read_b128 v[32:35], v2 offset:1856\ns_waitcnt lgkmcnt(6)\n" \
  "v_mfma_f32_16x16x32_f16 v[12:15], v[60:63], v[40:43], v[12:15]\nds_read_b128 v[104:107], v2 offset:1920\n" \
  "v_mfma_f32_16x16x32_f16 v[16:19], v[92:95], v[40:43], v[16:19]\nds_read_b128 v[124:127], v2 offset:1984\ns_waitcnt lgkmcnt(5)\n" \
  "v_readfirstlane_b32 s85, v23\ns_cmp_lt_u32 s85, 3\ns_cbranch_scc1 51f\n52:\nv_mfma_f32_16x16x32_f16 v[16:19], v[88:91], v[116:119], v[16:19]\n" \
  "ds_read_b64_tr_b16 v[100:101], v5 offset:0\nds_read_b64_tr_b16 v[102:103], v5 offset:32\n" \
  "v_mfma_f32_16x16x32_f16 v[16:19], v[84:87], v[24:27], v[16:19]\nds_read_b64_tr_b16 v[36:37], v5 offset:256\n" \
  "ds_read_b64_tr_b16 v[38:39], v5 offset:288\nv_mfma_f32_16x16x32_f16 v[16:19], v[80:83], v[112:115], v[16:19]\n" \
  "ds_read_b64_tr_b16 v[96:97], v5 offset:512\nds_read_b64_tr_b16 v[98:99], v5 offset:544\ns_waitcnt lgkmcnt(9)\n" \
  "v_mfma_f32_16x16x32_f16 v[16:19], v[76:79], v[28:31], v[16:19]\nds_read_b64_tr_b16 v[120:121], v5 offset:768\n" \
  "ds_read_b64_tr_b16 v[122:123], v5 offset:800\nv_mfma_f32_16x16x32_f16 v[16:19], v[72:75], v[108:111], v[16:19]\ns_waitcnt lgkmcnt(9)\n" \
  "v_mfma_f32_16x16x32_f16 v[16:19], v[68:71], v[32:35], v[16:19]\nv_mfma_f32_16x16x32_f16 v[16:19], v[64:67], v[104:107], v[16:19]\n" \
  "s_waitcnt lgkmcnt(6)\nv_mfma_f32_16x16x32_f16 v[16:19], v[60:63], v[124:127], v[16:19]\n" \
  "v_mfma_f32_16x16x32_f16 v[12:15], v[100:103], v[56:59], v[12:15]\nv_mfma_f32_16x16x32_f16 v[16:19], v[100:103], v[52:55], v[16:19]\n" \
  "s_waitcnt lgkmcnt(2)\nv_mfma_f32_16x16x32_f16 v[12:15], v[36:39], v[48:51], v[12:15]\n" \
  "v_mfma_f32_16x16x32_f16 v[16:19], v[36:39], v[44:47], v[16:19]\nv_mfma_f32_16x16x32_f16 v[16:19], v[96:99], v[56:59], v[16:19]\n" \
  "s_waitcnt lgkmcnt(0)\nv_mfma_f32_16x16x32_f16 v[16:19], v[120:123], v[48:51], v[16:19]\ns_nop 3\nv_cvt_pkrtz_f16_f32 v20, v12, v13\n" \
  "v_cvt_pkrtz_f16_f32 v21, v14, v15\nv_pk_add_u16 v20, v20, 4 op_sel_hi:[1,0] clamp\nv_pk_add_u16 v21, v21, 4 op_sel_hi:[1,0] clamp\n" \
  "ds_write_b64 v4, v[20:21] offset:2048\nv_cvt_pkrtz_f16_f32 v20, v16, v17\nv_cvt_pkrtz_f16_f32 v21, v18, v19\n" \
  "v_pk_add_u16 v20, v20, 4 op_sel_hi:[1,0] clamp\nv_pk_add_u16 v21, v21, 4 op_sel_hi:[1,0] clamp\nds_write_b64 v4, v[20:21] offset:3072\n" \
  "s_setprio 0\ns_setpc_b64 s[90:91]\n51:\ns_movk_i32 s84, 0x4000\n53:\nds_read_b32 v23, v9 offset:32\ns_sub_u32 s84, s84, 1\ns_cbranch_scc1 52b\n" \
  "s_waitcnt lgkmcnt(0)\nv_readfirstlane_b32 s85, v23\ns_cmp_lt_u32 s85, 3\ns_cbranch_scc1 53b\ns_branch 52b\n4:\n" \
  "ds_read_b64 v[124:125], v3 offset:1088\nds_read_b64 v[126:127], v3 offset:1096\nds_read_b64 v[120:121], v3 offset:0\n" \
  "ds_read_b64 v[122:123], v3 offset:8\nds_read_b64 v[116:117], v3 offset:64\nds_read_b64 v[118:119], v3 offset:72\n" \
  "ds_read_b64 v[112:113], v3 offset:128\nds_read_b64 v[114:115], v3 offset:136\nds_read_b64 v[108:109], v3 offset:192\n" \
  "ds_read_b64 v[110:111], v3 offset:200\nds_read_b64 v[104:105], v3 offset:256\nds_read_b64 v[106:107], v3 offset:264\n" \
  "ds_read_b64 v[100:101], v3 offset:320\nds_read_b64 v[102:103], v3 offset:328\nds_read_b64 v[96:97], v3 offset:384\n" \
  "ds_read_b64 v[98:99], v3 offset:392\nds_read_b64 v[92:93], v3 offset:448\nds_read_b64 v[94:95], v3 offset:456\n" \
  "ds_read_b64 v[88:89], v3 offset:512\nds_read_b64 v[90:91], v3 offset:520\nds_read_b64 v[84:85], v3 offset:576\n" \
  "ds_read_b64 v[86:87], v3 offset:584\nds_read_b64 v[80:81], v3 offset:640\nds_read_b64 v[82:83], v3 offset:648\n" \
  "ds_read_b64 v[76:77], v3 offset:704\nds_read_b64 v[78:79], v3 offset:712\nds_read_b64 v[72:73], v3 offset:768\n" \
  "ds_read_b64 v[74:75], v3 offset:776\nds_read_b64 v[68:69], v3 offset:832\nds_read_b64 v[70:71], v3 offset:840\n" \
  "ds_read_b64 v[64:65], v3 offset:896\nds_read_b64 v[66:67], v3 offset:904\nds_read_b64 v[60:61], v3 offset:960\n" \
  "ds_read_b64 v[62:63], v3 offset:968\nds_read_b64 v[56:57], v3 offset:1024\nds_read_b64 v[58:59], v3 offset:1032\n" \
  "ds_read_b64 v[52:53], v6 offset:0\nds_read_b64 v[54:55], v6 offset:8\nds_read_b64 v[48:49], v6 offset:64\nds_read_b64 v[50:51], v6 offset:72\n" \
  "ds_read_b64 v[44:45], v6 offset:128\nds_read_b64 v[46:47], v6 offset:136\nds_read_b64 v[40:41], v6 offset:192\n" \
  "ds_read_b64 v[42:43], v6 offset:200\ns_waitcnt lgkmcnt(0)\ns_barrier\nds_read_b128 v[24:27], v2 offset:448\nds_read_b128 v[28:31], v2 offset:960\n" \
  "ds_read_b128 v[32:35], v2 offset:1472\nds_read_b128 v[36:39], v2 offset:1536\ns_waitcnt lgkmcnt(3)\n" \
  "v_mfma_f32_16x16x32_f16 v[12:15], v[124:127], v[24:27], 0\ns_setprio 1\nds_read_b128 v[24:27], v2 offset:1600\ns_waitcnt lgkmcnt(3)\n" \
  "v_mfma_f32_16x16x32_f16 v[16:19], v[124:127], v[28:31], 0\nds_read_b128 v[124:127], v2 offset:1664\ns_waitcnt lgkmcnt(3)\n" \
  "v_mfma_f32_16x16x32_f16 v[12:15], v[120:123], v[32:35], v[12:15]\nds_read_b128 v[28:31], v2 offset:1728\ns_waitcnt lgkmcnt(3)\n" \
  "v_mfma_f32_16x16x32_f16 v[12:15], v[116:119], v[36:39], v[12:15]\nds_read_b128 v[32:35], v2 offset:1792\ns_waitcnt lgkmcnt(3)\n" \
  "v_mfma_f32_16x16x32_f16 v[12:15], v[112:115], v[24:27], v[12:15]\nds_read_b128 v[36:39], v2 offset:1856\ns_waitcnt lgkmcnt(3)\n" \
  "v_mfma_f32_16x16x32_f16 v[12:15], v[108:111], v[124:127], v[12:15]\nds_read_b128 v[24:27], v2 offset:1920\ns_waitcnt lgkmcnt(3)\n" \
  "v_mfma_f32_16x16x32_f16 v[12:15], v[104:107], v[28:31], v[12:15]\nds_read_b128 v[124:127], v2 offset:1984\ns_waitcnt lgkmcnt(3)\n" \
  "v_mfma_f32_16x16x32_f16 v[12:15], v[100:103], v[32:35], v[12:15]\nds_read_b128 v[28:31], v2 offset:2048\ns_waitcnt lgkmcnt(3)\n" \
  "v_mfma_f32_16x16x32_f16 v[12:15], v[96:99], v[36:39], v[12:15]\nds_read_b128 v[32:35], v2 offset:2112\ns_waitcnt lgkmcnt(3)\n" \
  "v_mfma_f32_16x16x32_f16 v[12:15], v[92:95], v[24:27], v[12:15]\nds_read_b128 v[36:39], v2 offset:2176\ns_waitcnt lgkmcnt(3)\n" \
  "v_mfma_f32_16x16x32_f16 v[12:15], v[88:91], v[124:127], v[12:15]\nds_read_b128 v[24:27], v2 offset:2240\n" \
  "v_mfma_f32_16x16x32_f16 v[16:19], v[120:123], v[124:127], v[16:19]\nds_read_b64 v[120:121], v6 offset:256\nds_read_b64 v[122:123], v6 offset:264\n" \
  "s_waitcnt lgkmcnt(4)\nv_mfma_f32_16x16x32_f16 v[12:15], v[84:87], v[28:31], v[12:15]\nds_read_b128 v[124:127], v2 offset:2304\n" \
  "v_mfma_f32_16x16x32_f16 v[16:19], v[116:119], v[28:31], v[16:19]\nds_read_b64 v[116:117], v6 offset:320\nds_read_b64 v[118:119], v6 offset:328\n" \
  "v_mfma_f32_16x16x32_f16 v[12:15], v[80:83], v[32:35], v[12:15]\nds_read_b128 v[28:31], v2 offset:2368\n" \
  "v_mfma_f32_16x16x32_f16 v[16:19], v[112:115], v[32:35], v[16:19]\ns_waitcnt lgkmcnt(6)\n" \
  "v_mfma_f32_16x16x32_f16 v[12:15], v[76:79], v[36:39], v[12:15]\nds_read_b128 v[112:115], v2 offset:2432\n" \
  "v_mfma_f32_16x16x32_f16 v[16:19], v[108:111], v[36:39], v[16:19]\nds_read2_b32 v[32:33], v8 offset0:48 offset1:49\n" \
  "ds_read2_b32 v[34:35], v8 offset0:50 offset1:51\nv_mfma_f32_16x16x32_f16 v[12:15], v[72:75], v[24:27], v[12:15]\n" \
  "ds_read_b128 v[108:111], v2 offset:2496\nv_mfma_f32_16x16x32_f16 v[16:19], v[104:107], v[24:27], v[16:19]\n" \
  "ds_read2_b32 v[36:37], v8 offset0:32 offset1:33\nds_read2_b32 v[38:39], v8 offset0:34 offset1:35\ns_waitcnt lgkmcnt(6)\n" \
  "v_mfma_f32_16x16x32_f16 v[12:15], v[68:71], v[124:127], v[12:15]\nds_read_b128 v[104:107], v2 offset:2560\n" \
  "v_mfma_f32_16x16x32_f16 v[16:19], v[100:103], v[124:127], v[16:19]\nds_read_b128 v[24:27], v2 offset:2624\n" \
  "v_mfma_f32_16x16x32_f16 v[12:15], v[64:67], v[28:31], v[12:15]\nds_read_b128 v[100:103], v2 offset:2688\nds_read_b32 v23, v9 offset:32\n" \
  "v_mfma_f32_16x16x32_f16 v[16:19], v[96:99], v[28:31], v[16:19]\nds_read_b128 v[124:127], v2 offset:2752\ns_waitcnt lgkmcnt(7)\n" \
  "v_mfma_f32_16x16x32_f16 v[12:15], v[60:63], v[112:115], v[12:15]\nds_read_b128 v[96:99], v2 offset:2816\n" \
  "v_mfma_f32_16x16x32_f16 v[16:19], v[92:95], v[112:115], v[16:19]\nds_read_b128 v[28:31], v2 offset:2880\n" \
  "v_mfma_f32_16x16x32_f16 v[12:15], v[56:59], v[108:111], v[12:15]\nds_read_b128 v[92:95], v2 offset:2944\n" \
  "v_mfma_f32_16x16x32_f16 v[16:19], v[88:91], v[108:111], v[16:19]\nds_read_b128 v[112:115], v2 offset:3008\ns_waitcnt lgkmcnt(5)\n" \
  "v_readfirstlane_b32 s85, v23\ns_cmp_lt_u32 s85, 3\ns_cbranch_scc1 61f\n62:\nv_mfma_f32_16x16x32_f16 v[16:19], v[84:87], v[104:107], v[16:19]\n" \
  "ds_read_b64_tr_b16 v[88:89], v5 offset:0\nds_read_b64_tr_b16 v[90:91], v5 offset:32\n" \
  "v_mfma_f32_16x16x32_f16 v[16:19], v[80:83], v[24:27], v[16:19]\nds_read_b64_tr_b16 v[108:109], v5 offset:256\n" \
  "ds_read_b64_tr_b16 v[110:111], v5 offset:288\nv_mfma_f32_16x16x32_f16 v[16:19], v[76:79], v[100:103], v[16:19]\n" \
  "ds_read_b64_tr_b16 v[84:85], v5 offset:512\nds_read_b64_tr_b16 v[86:87], v5 offset:544\ns_waitcnt lgkmcnt(9)\n" \
  "v_mfma_f32_16x16x32_f16 v[16:19], v[72:75], v[124:127], v[16:19]\nds_read_b64_tr_b16 v[104:105], v5 offset:768\n" \
  "ds_read_b64_tr_b16 v[106:107], v5 offset:800\nv_mfma_f32_16x16x32_f16 v[16:19], v[68:71], v[96:99], v[16:19]\n" \
  "ds_read_b64_tr_b16 v[80:81], v5 offset:1024\nds_read_b64_tr_b16 v[82:83], v5 offset:1056\ns_waitcnt lgkmcnt(11)\n" \
  "v_mfma_f32_16x16x32_f16 v[16:19], v[64:67], v[28:31], v[16:19]\nds_read_b64_tr_b16 v[24:25], v5 offset:1280\n" \
  "ds_read_b64_tr_b16 v[26:27], v5 offset:1312\nv_mfma_f32_16x16x32_f16 v[16:19], v[60:63], v[92:95], v[16:19]\n" \
  "ds_read_b64_tr_b16 v[76:77], v5 offset:1536\nds_read_b64_tr_b16 v[78:79], v5 offset:1568\ns_waitcnt lgkmcnt(12)\n" \
  "v_mfma_f32_16x16x32_f16 v[16:19], v[56:59], v[112:115], v[16:19]\nds_read_b64_tr_b16 v[100:101], v5 offset:1792\n" \
  "ds_read_b64_tr_b16 v[102:103], v5 offset:1824\nv_mfma_f32_16x16x32_f16 v[12:15], v[88:91], v[52:55], v[12:15]\ns_waitcnt lgkmcnt(10)\n" \
  "v_mfma_f32_16x16x32_f16 v[12:15], v[108:111], v[48:51], v[12:15]\nds_read_b64_tr_b16 v[72:73], v7 offset:0\n" \
  "ds_read_b64_tr_b16 v[74:75], v7 offset:64\nv_mfma_f32_16x16x32_f16 v[12:15], v[84:87], v[44:47], v[12:15]\n" \
  "ds_read_b64_tr_b16 v[124:125], v7 offset:512\nds_read_b64_tr_b16 v[126:127], v7 offset:576\n" \
  "v_mfma_f32_16x16x32_f16 v[16:19], v[84:87], v[52:55], v[16:19]\ns_waitcnt lgkmcnt(10)\n" \
  "v_mfma_f32_16x16x32_f16 v[12:15], v[104:107], v[40:43], v[12:15]\nv_mfma_f32_16x16x32_f16 v[16:19], v[104:107], v[48:51], v[16:19]\n" \
  "v_mfma_f32_16x16x32_f16 v[12:15], v[80:83], v[120:123], v[12:15]\nv_mfma_f32_16x16x32_f16 v[16:19], v[80:83], v[44:47], v[16:19]\n" \
  "s_waitcnt lgkmcnt(6)\nv_mfma_f32_16x16x32_f16 v[12:15], v[24:27], v[116:119], v[12:15]\n" \
  "v_mfma_f32_16x16x32_f16 v[16:19], v[24:27], v[40:43], v[16:19]\nv_mfma_f32_16x16x32_f16 v[16:19], v[76:79], v[120:123], v[16:19]\n" \
  "s_waitcnt lgkmcnt(2)\nv_mfma_f32_16x16x32_f16 v[16:19], v[100:103], v[116:119], v[16:19]\n" \
  "v_mfma_f32_16x16x32_f16 v[12:15], v[72:75], v[32:35], v[12:15]\nv_mfma_f32_16x16x32_f16 v[16:19], v[72:75], v[36:39], v[16:19]\n" \
  "s_waitcnt lgkmcnt(0)\nv_mfma_f32_16x16x32_f16 v[16:19], v[124:127], v[32:35], v[16:19]\ns_nop 4\nv_cvt_pkrtz_f16_f32 v20, v12, v13\n" \
  "v_cvt_pkrtz_f16_f32 v21, v14, v15\nv_pk_add_u16 v20, v20, 4 op_sel_hi:[1,0] clamp\nv_pk_add_u16 v21, v21, 4 op_sel_hi:[1,0] clamp\n" \
  "ds_write_b64 v4, v[20:21] offset:4096\nv_cvt_pkrtz_f16_f32 v20, v16, v17\nv_cvt_pkrtz_f16_f32 v21, v18, v19\n" \
  "v_pk_add_u16 v20, v20, 4 op_sel_hi:[1,0] clamp\nv_pk_add_u16 v21, v21, 4 op_sel_hi:[1,0] clamp\nds_write_b64 v4, v[20:21] offset:5120\n" \
  "s_setprio 0\ns_setpc_b64 s[90:91]\n61:\ns_movk_i32 s84, 0x4000\n63:\nds_read_b32 v23, v9 offset:32\ns_sub_u32 s84, s84, 1\ns_cbranch_scc1 62b\n" \
  "s_waitcnt lgkmcnt(0)\nv_readfirstlane_b32 s85, v23\ns_cmp_lt_u32 s85, 3\ns_cbranch_scc1 63b\ns_branch 62b\n5:\n" \
  "ds_read_b64 v[124:125], v3 offset:1088\nds_read_b64 v[126:127], v3 offset:1096\nds_read_b64 v[120:121], v3 offset:0\n" \
  "ds_read_b64 v[122:123], v3 offset:8\nds_read_b64 v[116:117], v3 offset:64\nds_read_b64 v[118:119], v3 offset:72\n" \
  "ds_read_b64 v[112:113], v3 offset:128\nds_read_b64 v[114:115], v3 offset:136\nds_read_b64 v[108:109], v3 offset:192\n" \
  "ds_read_b64 v[110:111], v3 offset:200\nds_read_b64 v[104:105], v3 offset:256\nds_read_b64 v[106:107], v3 offset:264\n" \
  "ds_read_b64 v[100:101], v3 offset:320\nds_read_b64 v[102:103], v3 offset:328\nds_read_b64 v[96:97], v3 offset:384\n" \
  "ds_read_b64 v[98:99], v3 offset:392\nds_read_b64 v[92:93], v3 offset:448\nds_read_b64 v[94:95], v3 offset:456\n" \
  "ds_read_b64 v[88:89], v3 offset:512\nds_read_b64 v[90:91], v3 offset:520\nds_read_b64 v[84:85], v3 offset:576\n" \
  "ds_read_b64 v[86:87], v3 offset:584\nds_read_b64 v[80:81], v3 offset:640\nds_read_b64 v[82:83], v3 offset:648\n" \
  "ds_read_b64 v[76:77], v3 offset:704\nds_read_b64 v[78:79], v3 offset:712\nds_read_b64 v[72:73], v3 offset:768\n" \
  "ds_read_b64 v[74:75], v3 offset:776\nds_read_b64 v[68:69], v3 offset:832\nds_read_b64 v[70:71], v3 offset:840\n" \
  "ds_read_b64 v[64:65], v3 offset:896\nds_read_b64 v[66:67], v3 offset:904\nds_read_b64 v[60:61], v3 offset:960\n" \
  "ds_read_b64 v[62:63], v3 offset:968\nds_read_b64 v[56:57], v3 offset:1024\nds_read_b64 v[58:59], v3 offset:1032\n" \
  "ds_read_b64 v[52:53], v6 offset:0\nds_read_b64 v[54:55], v6 offset:8\nds_read_b64 v[48:49], v6 offset:64\nds_read_b64 v[50:51], v6 offset:72\n" \
  "ds_read_b64 v[44:45], v6 offset:128\nds_read_b64 v[46:47], v6 offset:136\nds_read_b64 v[40:41], v6 offset:192\n" \
  "ds_read_b64 v[42:43], v6 offset:200\ns_waitcnt lgkmcnt(0)\ns_barrier\nds_read_b128 v[24:27], v2 offset:1472\n" \
  "ds_read_b128 v[28:31], v2 offset:1984\nds_read_b128 v[32:35], v2 offset:2496\nds_read_b128 v[36:39], v2 offset:2560\ns_waitcnt lgkmcnt(3)\n" \
  "v_mfma_f32_16x16x32_f16 v[12:15], v[124:127], v[24:27], 0\ns_setprio 1\nds_read_b128 v[24:27], v2 offset:2624\ns_waitcnt lgkmcnt(3)\n" \
  "v_mfma_f32_16x16x32_f16 v[16:19], v[124:127], v[28:31], 0\nds_read_b128 v[124:127], v2 offset:2688\ns_waitcnt lgkmcnt(3)\n" \
  "v_mfma_f32_16x16x32_f16 v[12:15], v[120:123], v[32:35], v[12:15]\nds_read_b128 v[28:31], v2 offset:2752\ns_waitcnt lgkmcnt(3)\n" \
  "v_mfma_f32_16x16x32_f16 v[12:15], v[116:119], v[36:39], v[12:15]\nds_read_b128 v[32:35], v2 offset:2816\ns_waitcnt lgkmcnt(3)\n" \
  "v_mfma_f32_16x16x32_f16 v[12:15], v[112:115], v[24:27], v[12:15]\nds_read_b128 v[36:39], v2 offset:2880\ns_waitcnt lgkmcnt(3)\n" \
  "v_mfma_f32_16x16x32_f16 v[12:15], v[108:111], v[124:127], v[12:15]\nds_read_b128 v[24:27], v2 offset:2944\ns_waitcnt lgkmcnt(3)\n" \
  "v_mfma_f32_16x16x32_f16 v[12:15], v[104:107], v[28:31], v[12:15]\nds_read_b128 v[124:127], v2 offset:3008\ns_waitcnt lgkmcnt(3)\n" \
  "v_mfma_f32_16x16x32_f16 v[12:15], v[100:103], v[32:35], v[12:15]\nds_read_b128 v[28:31], v2 offset:3072\ns_waitcnt lgkmcnt(3)\n" \
  "v_mfma_f32_16x16x32_f16 v[12:15], v[96:99], v[36:39], v[12:15]\nds_read_b128 v[32:35], v2 offset:3136\ns_waitcnt lgkmcnt(3)\n" \
  "v_mfma_f32_16x16x32_f16 v[12:15], v[92:95], v[24:27], v[12:15]\nds_read_b128 v[36:39], v2 offset:3200\ns_waitcnt lgkmcnt(3)\n" \
  "v_mfma_f32_16x16x32_f16 v[12:15], v[88:91], v[124:127], v[12:15]\nds_read_b128 v[24:27], v2 offset:3264\n" \
  "v_mfma_f32_16x16x32_f16 v[16:19], v[120:123], v[124:127], v[16:19]\nds_read_b64 v[120:121], v6 offset:256\nds_read_b64 v[122:123], v6 offset:264\n" \
  "s_waitcnt lgkmcnt(4)\nv_mfma_f32_16x16x32_f16 v[12:15], v[84:87], v[28:31], v[12:15]\nds_read_b128 v[124:127], v2 offset:3328\n" \
  "v_mfma_f32_16x16x32_f16 v[16:19], v[116:119], v[28:31], v[16:19]\nds_read_b64 v[116:117], v6 offset:320\nds_read_b64 v[118:119], v6 offset:328\n" \
  "v_mfma_f32_16x16x32_f16 v[12:15], v[80:83], v[32:35], v[12:15]\nds_read_b128 v[28:31], v2 offset:3392\n" \
  "v_mfma_f32_16x16x32_f16 v[16:19], v[112:115], v[32:35], v[16:19]\ns_waitcnt lgkmcnt(6)\n" \
  "v_mfma_f32_16x16x32_f16 v[12:15], v[76:79], v[36:39], v[12:15]\nds_read_b128 v[112:115], v2 offset:3456\n" \
  "v_mfma_f32_16x16x32_f16 v[16:19], v[108:111], v[36:39], v[16:19]\nds_read2_b32 v[32:33], v8 offset0:16 offset1:17\n" \
  "ds_read2_b32 v[34:35], v8 offset0:18 offset1:19\nv_mfma_f32_16x16x32_f16 v[12:15], v[72:75], v[24:27], v[12:15]\n" \
  "ds_read_b128 v[108:111], v2 offset:3520\nv_mfma_f32_16x16x32_f16 v[16:19], v[104:107], v[24:27], v[16:19]\n" \
  "ds_read2_b32 v[36:37], v8 offset0:0 offset1:1\nds_read2_b32 v[38:39], v8 offset0:2 offset1:3\ns_waitcnt lgkmcnt(6)\n" \
  "v_mfma_f32_16x16x32_f16 v[12:15], v[68:71], v[124:127], v[12:15]\nds_read_b128 v[104:107], v2 offset:3584\n" \
  "v_mfma_f32_16x16x32_f16 v[16:19], v[100:103], v[124:127], v[16:19]\nds_read_b128 v[24:27], v2 offset:3648\n" \
  "ds_read2_b32 v[100:101], v8 offset0:32 offset1:33\nds_read2_b32 v[102:103], v8 offset0:34 offset1:35\n" \
  "v_mfma_f32_16x16x32_f16 v[12:15], v[64:67], v[28:31], v[12:15]\nds_read_b128 v[124:127], v2 offset:3712\nds_read_b32 v23, v9 offset:32\n" \
  "v_mfma_f32_16x16x32_f16 v[16:19], v[96:99], v[28:31], v[16:19]\nds_read_b128 v[96:99], v2 offset:3776\ns_waitcnt lgkmcnt(9)\n" \
  "v_mfma_f32_16x16x32_f16 v[12:15], v[60:63], v[112:115], v[12:15]\nds_read_b128 v[28:31], v2 offset:3840\n" \
  "v_mfma_f32_16x16x32_f16 v[16:19], v[92:95], v[112:115], v[16:19]\nds_read_b128 v[92:95], v2 offset:3904\n" \
  "v_mfma_f32_16x16x32_f16 v[12:15], v[56:59], v[108:111], v[12:15]\nds_read_b128 v[112:115], v2 offset:3968\n" \
  "v_mfma_f32_16x16x32_f16 v[16:19], v[88:91], v[108:111], v[16:19]\nds_read_b128 v[88:91], v2 offset:4032\ns_waitcnt lgkmcnt(5)\n" \
  "v_readfirstlane_b32 s85, v23\ns_cmp_lt_u32 s85, 3\ns_cbranch_scc1 71f\n72:\nv_mfma_f32_16x16x32_f16 v[16:19], v[84:87], v[104:107], v[16:19]\n" \
  "ds_read_b64_tr_b16 v[108:109], v5 offset:1024\nds_read_b64_tr_b16 v[110:111], v5 offset:1056\n" \
  "v_mfma_f32_16x16x32_f16 v[16:19], v[80:83], v[24:27], v[16:19]\nds_read_b64_tr_b16 v[84:85], v5 offset:1280\n" \
  "ds_read_b64_tr_b16 v[86:87], v5 offset:1312\nv_mfma_f32_16x16x32_f16 v[16:19], v[76:79], v[124:127], v[16:19]\n" \
  "ds_read_b64_tr_b16 v[104:105], v5 offset:1536\nds_read_b64_tr_b16 v[106:107], v5 offset:1568\ns_waitcnt lgkmcnt(9)\n" \
  "v_mfma_f32_16x16x32_f16 v[16:19], v[72:75], v[96:99], v[16:19]\nds_read_b64_tr_b16 v[80:81], v5 offset:1792\n" \
  "ds_read_b64_tr_b16 v[82:83], v5 offset:1824\nv_mfma_f32_16x16x32_f16 v[16:19], v[68:71], v[28:31], v[16:19]\n" \
  "ds_read_b64_tr_b16 v[24:25], v5 offset:2048\nds_read_b64_tr_b16 v[26:27], v5 offset:2080\ns_waitcnt lgkmcnt(11)\n" \
  "v_mfma_f32_16x16x32_f16 v[16:19], v[64:67], v[92:95], v[16:19]\nds_read_b64_tr_b16 v[76:77], v5 offset:2304\n" \
  "ds_read_b64_tr_b16 v[78:79], v5 offset:2336\nv_mfma_f32_16x16x32_f16 v[16:19], v[60:63], v[112:115], v[16:19]\n" \
  "ds_read_b64_tr_b16 v[124:125], v5 offset:2560\nds_read_b64_tr_b16 v[126:127], v5 offset:2592\ns_waitcnt lgkmcnt(12)\n" \
  "v_mfma_f32_16x16x32_f16 v[16:19], v[56:59], v[88:91], v[16:19]\nds_read_b64_tr_b16 v[72:73], v5 offset:2816\n" \
  "ds_read_b64_tr_b16 v[74:75], v5 offset:2848\nv_mfma_f32_16x16x32_f16 v[12:15], v[108:111], v[52:55], v[12:15]\ns_waitcnt lgkmcnt(10)\n" \
  "v_mfma_f32_16x16x32_f16 v[12:15], v[84:87], v[48:51], v[12:15]\nds_read_b64_tr_b16 v[96:97], v7 offset:0\n" \
  "ds_read_b64_tr_b16 v[98:99], v7 offset:64\nv_mfma_f32_16x16x32_f16 v[12:15], v[104:107], v[44:47], v[12:15]\n" \
  "ds_read_b64_tr_b16 v[68:69], v7 offset:512\nds_read_b64_tr_b16 v[70:71], v7 offset:576\n" \
  "v_mfma_f32_16x16x32_f16 v[16:19], v[104:107], v[52:55], v[16:19]\ns_waitcnt lgkmcnt(10)\n" \
  "v_mfma_f32_16x16x32_f16 v[12:15], v[80:83], v[40:43], v[12:15]\nds_read_b64_tr_b16 v[28:29], v7 offset:1024\n" \
  "ds_read_b64_tr_b16 v[30:31], v7 offset:1088\nv_mfma_f32_16x16x32_f16 v[16:19], v[80:83], v[48:51], v[16:19]\n" \
  "ds_read2_b32 v[64:65], v8 offset0:48 offset1:49\nds_read2_b32 v[66:67], v8 offset0:50 offset1:51\n" \
  "v_mfma_f32_16x16x32_f16 v[12:15], v[24:27], v[120:123], v[12:15]\nv_mfma_f32_16x16x32_f16 v[16:19], v[24:27], v[44:47], v[16:19]\n" \
  "s_waitcnt lgkmcnt(10)\nv_mfma_f32_16x16x32_f16 v[12:15], v[76:79], v[116:119], v[12:15]\nds_read_b64_tr_b16 v[92:93], v7 offset:1536\n" \
  "ds_read_b64_tr_b16 v[94:95], v7 offset:1600\nv_mfma_f32_16x16x32_f16 v[16:19], v[76:79], v[40:43], v[16:19]\n" \
  "v_mfma_f32_16x16x32_f16 v[16:19], v[124:127], v[120:123], v[16:19]\ns_waitcnt lgkmcnt(8)\n" \
  "v_mfma_f32_16x16x32_f16 v[16:19], v[72:75], v[116:119], v[16:19]\nv_mfma_f32_16x16x32_f16 v[12:15], v[96:99], v[32:35], v[12:15]\n" \
  "v_mfma_f32_16x16x32_f16 v[16:19], v[96:99], v[36:39], v[16:19]\ns_waitcnt lgkmcnt(4)\n" \
  "v_mfma_f32_16x16x32_f16 v[12:15], v[68:71], v[100:103], v[12:15]\nv_mfma_f32_16x16x32_f16 v[16:19], v[68:71], v[32:35], v[16:19]\n" \
  "s_waitcnt lgkmcnt(2)\nv_mfma_f32_16x16x32_f16 v[12:15], v[28:31], v[64:67], v[12:15]\n" \
  "v_mfma_f32_16x16x32_f16 v[16:19], v[28:31], v[100:103], v[16:19]\ns_waitcnt lgkmcnt(0)\n" \
  "v_mfma_f32_16x16x32_f16 v[16:19], v[92:95], v[64:67], v[16:19]\ns_nop 4\nv_cvt_pkrtz_f16_f32 v20, v12, v13\nv_cvt_pkrtz_f16_f32 v21, v14, v15\n" \
  "v_pk_add_u16 v20, v20, 4 op_sel_hi:[1,0] clamp\nv_pk_add_u16 v21, v21, 4 op_sel_hi:[1,0] clamp\nds_write_b64 v4, v[20:21] offset:6144\n" \
  "v_cvt_pkrtz_f16_f32 v20, v16, v17\nv_cvt_pkrtz_f16_f32 v21, v18, v19\nv_pk_add_u16 v20, v20, 4 op_sel_hi:[1,0] clamp\n" \
  "v_pk_add_u16 v21, v21, 4 op_sel_hi:[1,0] clamp\nds_write_b64 v4, v[20:21] offset:7168\ns_setprio 0\ns_setpc_b64 s[90:91]\n71:\n" \
  "s_movk_i32 s84, 0x4000\n73:\nds_read_b32 v23, v9 offset:32\ns_sub_u32 s84, s84, 1\ns_cbranch_scc1 72b\ns_waitcnt lgkmcnt(0)\n" \
  "v_readfirstlane_b32 s85, v23\ns_cmp_lt_u32 s85, 3\ns_cbranch_scc1 73b\ns_branch 72b\n"
#define CONV_CLOB "memory", "scc", "s84", "s85", "s90", "s91", "v12", "v13", "v14", "v15", "v16", "v17", "v18", "v19", "v20", "v21", "v22", "v23", "v24", "v25", "v26", "v27", "v28", "v29", "v30", "v31", "v32", "v33", "v34", "v35", "v36", "v37", "v38", "v39", "v40", "v41", "v42", "v43", "v44", "v45", "v46", "v47", "v48", "v49", "v50", "v51", "v52", "v53", "v54", "v55", "v56", "v57", "v58", "v59", "v60", "v61", "v62", "v63", "v64", "v65", "v66", "v67", "v68", "v69", "v70", "v71", "v72", "v73", "v74", "v75", "v76", "v77", "v78", "v79", "v80", "v81", "v82", "v83", "v84", "v85", "v86", "v87", "v88", "v89", "v90", "v91", "v92", "v93", "v94", "v95", "v96", "v97", "v98", "v99", "v100", "v101", "v102", "v103", "v104", "v105", "v106", "v107", "v108", "v109", "v110", "v111", "v112", "v113", "v114", "v115", "v116", "v117", "v118", "v119", "v120", "v121", "v122", "v123", "v124", "v125", "v126", "v127"
constexpr int TDP = 1216, T_3 = 4 * TDP, T_4 = T_3 + 512, TB = T_4 + 320;
constexpr int C_U = 480, UB = C_U + 4096 + 96, C_G5 = 0, C_G3 = 3200, C_G9 = 6400, C_G7 = C_G9 + 2096, GB = C_G7 + 2080 + 112;
constexpr int O_FL = 2 * TB, O_UL = O_FL + 64, O_IM = O_UL + 2 * UB, O_ST = O_IM + 2 * GB;
constexpr int X_TB = 10240, X_UL = X_TB + TB, X_IM = X_UL + UB, X_ST = X_IM + GB;
constexpr int X_WR = X_IM + 128 * IP;
#define X_RW(w) (LW + 9216 * (2 + ((w) >> 1)) + 2560 * ((w) & 1))
constexpr int RP = 1216;
constexpr int LW = 36864 + 512, LA = LW, LM = LW + 36864;
constexpr int L_SC = LM + 2048, SMEM = O_IM + 256 * IP;
static_assert(L_SC + 1152 <= SMEM && O_ST + 8192 <= SMEM && X_IM + 128 * IP <= LM && X_ST + 8192 <= LM && UB % 128 == 64 && GB % 128 == 64 && SMEM <= 79872, "");
struct TOps { uint32_t xa, xb, ya, pd, pf, pg, pe; };
__device__ __forceinline__ void table_ops(uint32_t im, uint32_t tb, int ct, int lane, TOps& t) {
  const int j = lane & 15, kq = lane >> 4, q4 = (lane >> 2) & 3, p4 = lane & 3, jj = j + 16 * ct;
  const uint32_t sa = im + (uint32_t)(IP * (lane >> 2));
  t.xa = sa + (uint32_t)(I_X + 8 * p4); t.xb = sa + (uint32_t)(I_X + 32 + 8 * (p4 & 1)); t.ya = sa + (uint32_t)(32 * ct + 8 * p4);
  t.pd = tb + (uint32_t)(2 * (541 - (8 * jj + 4 * (kq & 1) + 256 * (kq >> 1))));
  t.pf = tb + (uint32_t)((kq & 1) ? T_4 + 2 * (63 - jj) : T_3 + 2 * (127 - 2 * jj));
  t.pg = t.pf - ((kq & 1) ? 64 : 2);
  t.pe = tb + (uint32_t)(p4 * (TDP + 2) + ((lane < 12) ? 2 * (32 - q4) : 2 * (572 + (q4 & 1))));
  asm volatile("" : "+v"(t.xa), "+v"(t.xb), "+v"(t.ya), "+v"(t.pd), "+v"(t.pf), "+v"(t.pg), "+v"(t.pe));
}
typedef __attribute__((address_space(3))) char* lptr;
#define LP(T, a) ((__attribute__((address_space(3))) T*)(uintptr_t)(a))
__device__ __forceinline__ uint32_t table_tile(const TOps& t, int ct, int lane, uint32_t evp, float dsk TSP) {
  u32x4 ev;
  f32x4 ad[2], af[2];
  asm volatile(T_ASM : "=&v"(ad[0]), "=&v"(ad[1]), "=&v"(af[0]), "=&v"(af[1]), "=&v"(ev) : "v"(t.xa), "v"(t.xb), "v"(t.ya), "v"(evp) : T_CLOB);
  TS(0, 4);
  const int e0 = (int)(ev[0] + ev[2]), e1 = (int)(ev[1] + ev[3]);
  const int em = min(max(max(max(e0, e1), (int)((__float_as_uint(dsk) >> 23) & 0xffu) - 133), -110), 115);
  const int ts = ((uint32_t)(em + 12) <= 8u) ? 1 : -3 - em;
  float s0 = __builtin_amdgcn_ldexpf(1.f, e0 - 29 + ts), s1 = __builtin_amdgcn_ldexpf(1.f, e1 - 29 + ts);
  float d0 = (ct == 0 && lane == 0) ? __builtin_amdgcn_ldexpf(dsk, ts) : 0.f;
  asm volatile("s_nop 1" : "+v"(ad[0]), "+v"(ad[1]), "+v"(af[0]), "+v"(af[1]), "+v"(s0), "+v"(s1), "+v"(d0));
  const f32x4 k = ad[0] * s0 + (f32x4){d0, 0.f, 0.f, 0.f} + ad[1] * s1, f = af[0] * s0 + af[1] * s1;
  auto pk = [](float a, float b) { uint32_t r; asm("v_cvt_pk_f16_f32 %0, %1, %2" : "=v"(r) : "v"(a), "v"(b)); return r; };
  asm volatile("s_nop 0" : : "v"(k), "v"(f));
  const uint32_t p32 = pk(k[3], k[2]), p10 = pk(k[1], k[0]), p21 = pk(k[2], k[1]), p01 = pk(f[0], f[1]), p23 = pk(f[2], f[3]);
  *LP(u32x2, t.pd + 3 * TDP + 6) = (u32x2){p32, p10};
  *LP(uint32_t, t.pd + TDP + 2) = p32; *LP(uint32_t, t.pd + TDP + 6) = p10;
  *LP(uint32_t, t.pd + 2) = p21; *LP(uint32_t, t.pd + 2 * TDP + 6) = p21;
  *LP(u16, t.pd) = (u16)p32; *LP(u16, t.pd + 2 * TDP + 4) = (u16)p32;
  asm volatile("ds_write_b16_d16_hi %0, %1 offset:6\n\tds_write_b16_d16_hi %0, %1 offset:%2" : : "v"(t.pd), "v"(p10), "n"(2 * TDP + 10) : "memory");
  if (ct) {
    const uint32_t va = (uint32_t)__builtin_amdgcn_readlane((int)p32, 63), vb = (uint32_t)__builtin_amdgcn_readlane((int)p23, 15) >> 16;
    *LP(u16, t.pe) = (u16)((lane < 12) ? va : vb);
  }
  if (lane < 32) {
    *LP(u16, t.pf + 128) = (u16)p01; *LP(u16, t.pg + 128) = (u16)(p01 >> 16);
    *LP(u16, t.pf) = (u16)p23; *LP(u16, t.pg) = (u16)(p23 >> 16);
  }
  return (uint32_t)(127 + 5 - ts) << 23;
}
__device__ __forceinline__ uint32_t hadd(uint32_t a, uint32_t b) { return __builtin_bit_cast(uint32_t, __builtin_bit_cast(f16x2, a) + __builtin_bit_cast(f16x2, b)); }
template <bool FULL> __device__ __forceinline__ void sums(char* sm, const u32x4 p, const u32x4 c) {
  const uint32_t D[8] = {p[0], p[1], p[2], p[3], c[0], c[1], c[2], c[3]};
  uint32_t O[7];
#pragma unroll
  for (int j = 0; j < 7; ++j) O[j] = __builtin_amdgcn_alignbit(D[j + 1], D[j], 16);
  u32x4 a, b;
#pragma unroll
  for (int j = 0; j < 4; ++j) a[j] = hadd(D[j + 2], hadd(O[j + 1], O[j + 2]));
  *(u32x4*)(sm + C_G3 + 32) = a;
#pragma unroll
  for (int j = 0; j < 4; ++j) b[j] = hadd(a[j], hadd(D[j + 1], D[j + 3]));
  *(u32x4*)(sm + C_G5 + 32) = b;
  if (FULL) {
#pragma unroll
    for (int j = 0; j < 4; ++j) a[j] = hadd(b[j], hadd(O[j], O[j + 3]));
    *(u32x4*)(sm + C_G7 + 32) = a;
#pragma unroll
    for (int j = 0; j < 4; ++j) b[j] = hadd(a[j], hadd(D[j], D[j + 4]));
    *(u32x4*)(sm + C_G9 + 32) = b;
  }
}
__device__ __forceinline__ void count_up(uint32_t a) {
  uint32_t t;
  asm volatile("s_mov_b64 exec, 1\n\tv_mov_b32 %0, 1\n\tds_add_u32 %1, %0\n\ts_mov_b64 exec, -1" : "=&v"(t) : "v"(a) : "memory");
}
__device__ __forceinline__ int pad_at(int r) { return 16 * (r & 1) + 3200 * (r >> 1) - ((r >= 6) ? 3200 - 2096 : 0); }
struct COps { uint32_t b, a, o, g3, k3, g4, k4, f; };
__device__ __forceinline__ void conv_ops(uint32_t tb, uint32_t ub, uint32_t cb, int lane, COps& e) {
  const uint32_t j = lane & 15, kq = lane >> 4, q4 = (lane >> 2) & 3, p4 = lane & 3;
  e.b = ub + 32u * j + 16u * kq;
  e.a = tb + p4 * TDP + 32u - 8u * q4 + 16u * kq;
  e.g3 = cb + C_G5 + 8 + 64u * kq + 8u * (q4 + p4) + (q4 & 1u) * 3200u;
  e.g4 = cb + C_G9 + 8 + 128u * kq + 16u * q4 + 8u * p4 + (q4 & 1u) * 2096u;
  e.k3 = tb + T_3 + 120 - 8u * j + 16u * kq;
  e.k4 = tb + T_4 + 60 - 4u * j + 16u * kq;
  asm volatile("" : "+v"(e.b), "+v"(e.a), "+v"(e.g3), "+v"(e.g4), "+v"(e.k3), "+v"(e.k4));
}
#define CONV_RUN(prog, e, ...) do { int prog_ = (prog); \
    asm volatile("s_call_b64 s[90:91], kf_conv" : "+{s89}"(prog_), "+{v2}"(e.b), "+{v3}"(e.a), "+{v4}"(e.o), "+{v5}"(e.g3), "+{v6}"(e.k3), "+{v7}"(e.g4), \
                 "+{v8}"(e.k4), "+{v9}"(e.f) __VA_ARGS__ : : CONV_CLOB); } while (0)

template <bool E>
__device__ __forceinline__ void ln_stats(const uint32_t* v, int lane, float sc, f32x2* t, f32x4 z, f32x2& rc, f32x2& nc, float& e) {
  float s1a = z[0], s2a = z[1], s1b = z[2], s2b = z[3];
#pragma unroll
  for (int k = 0; k < 4; ++k) {
    s1a = dot2(v[k], 0x3f803f80u, s1a); s2a = dot2(v[k], v[k], s2a);
    s1b = dot2(v[4 + k], 0x3f803f80u, s1b); s2b = dot2(v[4 + k], v[4 + k], s2b);
  }
#pragma unroll
  for (int k = 0; k < 9; ++k) t[k] = (f32x2){bflo(v[k]), bfhi(v[k])};
  const float rs = row_sum(fold16(dot2(v[8], 0x3f803f80u, fold32(s1a, s1b)), dot2(v[8], v[8], fold32(s2a, s2b))));
  if (E) { e = row_sum(e); e += __int_as_float(__builtin_amdgcn_update_dpp(0, __float_as_int(e), 0x142, 0xa, 0xf, false)); }
  const f32x2 mean = (f32x2){rdl(rs, 15), rdl(rs, 47)} * (1.f / D_), m2 = mean * mean;
  f32x2 var = (f32x2){rdl(rs, 31), rdl(rs, 63)} * (1.f / D_) - m2;
  asm volatile("" : "+v"(t[0]), "+v"(t[1]), "+v"(t[2]), "+v"(t[3]), "+v"(t[4]), "+v"(t[5]), "+v"(t[6]), "+v"(t[7]), "+v"(t[8]));
  if (__builtin_expect(!(m2[0] <= 16.f * var[0] && m2[1] <= 16.f * var[1]), 0)) {
    float q[3] = {0.f, 0.f, 0.f};
#pragma unroll
    for (int k = 0; k < 9; ++k) {
      const f32x2 a = t[k] - ((k < 4 || (k == 8 && lane < 32)) ? mean[0] : mean[1]);
      q[k >> 2] += a[0] * a[0] + a[1] * a[1];
    }
    float qa = q[0] + ((lane < 32) ? q[2] : 0.f), qb = q[1] + ((lane < 32) ? 0.f : q[2]);
    wave_sum2(qa, qb);
    var = (f32x2){qa, qb} * (1.f / D_);
  }
  const uint32_t mp = pack2(mean[0], mean[1]), vp = pack2(var[0], var[1]);
  const f32x2 ve = (f32x2){bflo(vp), bfhi(vp)} + 1e-5f;
  const uint32_t e2 = pack2(ve[0], ve[1]);
  const uint32_t sp = pack2(__builtin_amdgcn_sqrtf(bflo(e2)), __builtin_amdgcn_sqrtf(bfhi(e2)));
  rc = (f32x2){__builtin_amdgcn_rcpf(bflo(sp)), __builtin_amdgcn_rcpf(bfhi(sp))} * sc;
  nc = -(f32x2){bflo(mp), bfhi(mp)} * rc;
}
template <bool H>
__device__ __forceinline__ void ln_apply(const f32x2* t, int lane, f32x2 rc, f32x2 nc, uint32_t* c) {
  const float nm[3] = {nc[0], nc[1], (lane < 32) ? nc[0] : nc[1]}, rr[3] = {rc[0], rc[1], (lane < 32) ? rc[0] : rc[1]};
  f32x2 dd[9];
#pragma unroll
  for (int k = 0; k < 9; ++k) dd[k] = __builtin_elementwise_fma(t[k], (f32x2){rr[k >> 2], rr[k >> 2]}, (f32x2){nm[k >> 2], nm[k >> 2]});
  asm volatile("" : "+v"(dd[0]), "+v"(dd[1]), "+v"(dd[2]), "+v"(dd[3]), "+v"(dd[4]), "+v"(dd[5]), "+v"(dd[6]), "+v"(dd[7]), "+v"(dd[8]));
#pragma unroll
  for (int k = 0; k < 9; ++k) {
    const f32x2 d = dd[k];
    typedef __attribute__((ext_vector_type(2))) uint16_t u16x2;
    if (H) c[k] = __builtin_bit_cast(uint32_t, __builtin_elementwise_add_sat(__builtin_bit_cast(u16x2, __builtin_amdgcn_cvt_pkrtz(d[0], d[1])), (u16x2){4, 4}));
    else c[k] = pack2(d[0], d[1]);
  }
}
__device__ __forceinline__ uint32_t med3u(uint32_t a, uint32_t b, uint32_t c) { return max(min(a, b), min(max(a, b), c)); }
__device__ __forceinline__ uint32_t oct_maxu(uint32_t v) {
  v = max(v, (uint32_t)DPP_I((int)v, 0xb1, 0xf));
  v = max(v, (uint32_t)DPP_I((int)v, 0x4e, 0xf));
  v = max(v, (uint32_t)DPP_I((int)v, 0x141, 0xf));
  return v;
}

#define KARGS const u16* __restrict__ x, const u16* __restrict__ Are, const u16* __restrict__ Aim, const u16* __restrict__ Bre, \
    const u16* __restrict__ Bim, const u16* __restrict__ Cre, const u16* __restrict__ Cim, const u16* __restrict__ logdt, \
    const u16* __restrict__ Dskip, char* __restrict__ ws, const u16* __restrict__ wr, const u16* __restrict__ wg, \
    const u16* __restrict__ wu, const u16* __restrict__ wd, u16* __restrict__ out
#define KPASS x, Are, Aim, Bre, Bim, Cre, Cim, logdt, Dskip, ws, wr, wg, wu, wd, out
template <int ROLE, int FFN = -1>
__device__ __forceinline__ void role_body(char* smem, KARGS) {
  const int tid = threadIdx.x, lane = tid & 63;
  const int w = __builtin_amdgcn_readfirstlane(tid >> 6), wl = w & 3;
  const int b = blockIdx.x & 255, bx = b & 7, bj = b >> 3;
  constexpr bool isl = ROLE == 1 || ROLE == 2;
  const int tg = isl ? 32 * (bj >> 2) + 4 * bx + (bj & 3) : 32 * bx + bj;
  u32x4 xa, xc;
  uint32_t ep, r0;
#define P_WAIT(n, ...) asm volatile("s_waitcnt vmcnt(" #n ")\n\tv_lshlrev_b32 %0, 16, v100\n\tv_lshlrev_b32 %1, 16, v101\n\tv_lshlrev_b32 %2, 16, v102" \
    : "=v"(ldt), "=v"(are), "=v"(aim) : : "v100", "v101", "v102" __VA_ARGS__)
#define P_MORE() asm volatile("v_lshlrev_b32 %0, 16, v103\n\tv_lshlrev_b32 %1, 16, v104\n\tv_lshlrev_b32 %2, 16, v105\n\tv_lshlrev_b32 %3, 16, v106" \
    : "=v"(bre), "=v"(bim), "=v"(cre), "=v"(cim) : : "v103", "v104", "v105", "v106")
  TSD;
  TS(0, 0);
  const uint32_t lb0 = (uint32_t)(uintptr_t)(__attribute__((address_space(3))) char*)smem;
  const int i = lane & 15, kq = lane >> 4;
  const u32x4 ones = {0xffffffffu, 0xffffffffu, 0xffffffffu, 0xffffffffu}, zero4 = {0u, 0u, 0u, 0u};
  auto u_again = [&](u32x4& uw, const u32x4& ut, uint32_t uo) {
#pragma unroll 1
    for (int spin = 0; spin < SPIN_CAP; ++spin) {
      const bool na = ((uw[0] | uw[1] | uw[2] | uw[3]) & UMASK) != 0u;
      if (__builtin_expect(!__any(na), 1)) break;
      TSC(19);
      SLEEP(USLEEP);
      if (na) { LD16W(uw, uo); uw ^= ut; }
    }
  };
  constexpr uint32_t S16 = 0x41800000u, LMASK = 0x00070007u;
  auto v_put = [&](u32x4 v, const u32x4& ut, bool other, uint32_t vo) {
    const bool second = __any(other || ((v[0] | v[1] | v[2] | v[3]) & 0x40004000u) != 0u);
    if (__builtin_expect(second, 0)) v = (v & ~LMASK) | (~ut & LMASK); else v = (v & ~LMASK) | ut;
    ST16(vo, v);
    return second;
  };
  auto v_end = [&](uint32_t T, int n) { DRAIN(); if (lane == 0) ST4(O_DN + 16u * n, T); };
  if constexpr (!isl) {
    const int ch = wl >> 1, tl = tid & 255;
    int* evp = (int*)(smem + O_FL + 16 * ch);
    float ldt, are, aim, dsk;
    if constexpr (ROLE == 0) {
      __builtin_amdgcn_s_setprio(KPRIO);
      COps e;
      conv_ops(lb0 + (uint32_t)(ch * TB), lb0 + (uint32_t)(O_UL + ch * UB), lb0 + (uint32_t)(O_IM + ch * GB), lane, e);
      e.o = lb0 + O_ST + (uint32_t)(16 * ((4 * i + 2 * (kq >> 1) + ch) ^ (i >> 1)) + 8 * (kq & 1));
      e.f = lb0 + O_FL;
      TOps t;
      table_ops(lb0 + (uint32_t)(O_IM + 128 * IP * ch), lb0 + (uint32_t)(TB * ch), wl & 1, lane, t);
      uint32_t pz = lb0 + O_IM + (uint32_t)(((lane >> 3) & 1) * GB + pad_at(lane & 7)), im = lb0 + (uint32_t)(O_IM + IP * tl);
      asm volatile("" : "+v"(e.o), "+v"(e.f), "+v"(pz), "+v"(im));
      float bre, bim, cre, cim;
      P_WAIT(0, , "v103", "v104", "v105", "v106", "v107", "v109");
      asm volatile("" : "={v109}"(ep));
      const uint32_t T = ((uint32_t)__builtin_amdgcn_readfirstlane(ep) + 1u) * 0x9E3779B1u;
      if (w == 3 && lane < 8) ST16(O_KEY + 128u * b + 16u * lane, ones);
      P_MORE();
      asm volatile("v_lshlrev_b32 %0, 16, v107" : "=v"(dsk) : : "v107");
      TS(0, 1);
      evp[wl & 1] = gen_y(ldt, are, aim, bre, bim, cre, cim, im, [] {} TSA);
      TS(0, 8);
      BAR();
      TS(0, 2);
      const uint32_t cs = table_tile(t, wl & 1, lane, lb0 + O_FL + 16 * ch, dsk TSA);
      *(uint32_t*)(smem + O_FL + 40 + 4 * ch) = cs;
      DRAIN();
      if ((wl & 1) && lane == 0) asm volatile("global_store_dwordx2 %0, %1, %2 sc1" : : "v"(O_SC + 16u * (2 * tg + ch)), "v"((u32x2){T, cs}), "s"(ws) : "memory");
      TS(0, 9);
      gsync0(lb0 + O_FL + 36, 4);
      TS(0, 3);
      *(u32x4*)(smem + O_ST + 16 * tl) = ones;
      *(u32x4*)(smem + O_ST + 4096 + 16 * tl) = ones;
      if (w == 0) *LP(u32x4, pz) = zero4;
      CONV_RUN(wl & 1, e);
      TS(0, 5);
    } else {
      __builtin_amdgcn_s_setprio(UPRIO);
#pragma unroll
      for (int k = 0; k < 3; ++k) *(u32x4*)(smem + ((16 * (tl + 256 * k) < O_FL) ? 16 * (tl + 256 * k) : O_FL + 32)) = zero4;
      if (w == 7) *(u32x4*)(smem + O_UL + (lane >> 5) * UB + 16 * min(lane & 31, 29)) = zero4;
      uint32_t uo = u_at(tl >> 1, 2 * tg + (tid & 1)), go = O_SU + (uint32_t)(256 * bx + 32 * wl + (lane & 31) + 4 * (lane & 32)) * 128u;
      uint32_t ul = (uint32_t)(O_UL + (tid & 1) * UB + C_U - 16 + 16 * (tl >> 1)), ug = (uint32_t)(O_IM + (tid & 1) * GB + 16 * (tl >> 1)), so = lb0 + O_ST + 16u * (uint32_t)(lane ^ (lane >> 3));
      uint32_t vo = O_V + (uint32_t)tg * VP + 16u * lane;
      asm volatile("" : "+v"(uo), "+v"(go), "+v"(ul), "+v"(ug), "+v"(so), "+v"(vo));
      P_WAIT(1, , "v109");
      TS(0, 1);
      evp[2 + (wl & 1)] = gen_x(ldt, are, aim, smem + O_IM + IP * tl);
      asm volatile("s_waitcnt vmcnt(0)" : "={v109}"(ep));
      const uint32_t T = ((uint32_t)__builtin_amdgcn_readfirstlane(ep) + 1u) * 0x9E3779B1u;
      TS(0, 8);
      BAR();
      TS(0, 2);
      u32x4 ut = {utag(T, 0), utag(T, 1), utag(T, 2), utag(T, 3)};
      asm volatile("" : "+v"(ut));
      SLEEP(GSLEEP);
      u32x2 pv;
      pv[1] = gate(ws, go, T);
      TS(0, 3);
      until(latest(pv[1]) + UD);
      TS(0, 7);
      u32x4 uw, ux;
      asm volatile("global_load_dwordx4 %0, %2, %4 sc1\n\tglobal_load_dwordx4 %1, %3, %4 sc1"
                   : "=&v"(uw), "=&v"(ux) : "v"(uo), "v"(uo + 64 * UP * 32), "s"(ws) : "memory");
      __builtin_amdgcn_s_setprio(FPR);
      asm volatile("s_waitcnt vmcnt(1)" : "+v"(uw));
      uw ^= ut;
      *(u32x4*)(smem + ul + 16) = uw;
      asm volatile("s_waitcnt vmcnt(0)" : "+v"(ux));
      TS(0, 11);
      ux ^= ut;
      *(u32x4*)(smem + ul + 2048 + 16) = ux;
      if (__builtin_expect(__any(((uw[0] | uw[1] | uw[2] | uw[3] | ux[0] | ux[1] | ux[2] | ux[3]) & UMASK) != 0u), 0)) {
        u_again(uw, ut, uo);
        u_again(ux, ut, uo + 64 * UP * 32);
        *(u32x4*)(smem + ul + 16) = uw;
        *(u32x4*)(smem + ul + 2048 + 16) = ux;
      }
      BAR();
      TS(0, 4);
      const u32x4 pw = *(const u32x4*)(smem + ul);
      if (wl < 2) {
        const u32x4 px = *(const u32x4*)(smem + ul + 2048);
        sums<true>(smem + ug, pw, uw);
        sums<false>(smem + ug + 2048, px, ux);
      } else sums<true>(smem + ug, pw, uw);
      count_up(lb0 + O_FL + 32);
      __builtin_amdgcn_s_setprio(SPR);
      TS(0, 5);
      const bool other = *(const uint32_t*)(smem + O_FL + 40 + 4 * (lane & 1)) != S16;
      bool second = false;
#pragma unroll
      for (int k = 0; k < 2; ++k) {
        const uint32_t o = ((k ? 0x3247u : 0x1065u) >> (4 * wl) & 7u) * 1024u;
        u32x4 v;
#pragma unroll 1
        for (int spin = 0; spin < SPIN_CAP; ++spin) {
          asm volatile("ds_read_b128 %0, %1\n\ts_waitcnt lgkmcnt(0)" : "=&v"(v) : "v"(so + o) : "memory");
          if (!__any(max4(v) == 0xffffffffu)) break;
          __builtin_amdgcn_s_sleep(VPS);
        }
        TS(0, 12 + k);
        second |= v_put(v, ut, other, vo + o);
        TS(0, 10 - 4 * k);
      }
      if (__builtin_expect(second, 0)) v_end(T, 4 * tg + wl);
    }
    TSE();
  } else {
  uint32_t* cand = (uint32_t*)(smem + LM);
  uint32_t* sel = cand + 128;
  uint32_t* kl = sel + 48;
  uint32_t* cnt = sel + 64;
  const uint32_t ca = lb0 + LM + 4 * (128 + 64);
  u16* hm = (u16*)(cnt + 12);
  uint32_t T;
  const bool ffn = (FFN < 0) ? bj < 18 : FFN, isx = FFN != 1 && bj >= 24;
  const int fb = 18 * bx + bj;
  constexpr bool ln2 = ROLE == 2;
  const int g0 = tg, g2 = tg;
  u32x4 bw[18];
#pragma unroll
  for (int s = 0; s < 9; ++s) bw[s] = (u32x4){0u, 0u, 0u, 0u};
  int tid2 = threadIdx.x;
  uint32_t ts1 = 0u;
  auto rest = [&] {
    if (ts1 && (int)((uint32_t)__builtin_amdgcn_s_memrealtime() - ts1) > 0) { TS(0, 8); until(ts1 + TSTOP + RD); if (RW) gwait(ca, 4); ts1 = 0u; }
  };
  auto gu_dma = [&](int q0, int q1, auto depth) {
    const char* gs = (const char*)((w < 2) ? wg : wu) + (size_t)((144 * (w & 1)) * (F_ * 2) + fb * 64);
    const uint32_t gv = (uint32_t)((lane >> 2) * (F_ * 2) + 16 * ((lane & 3) ^ (lane >> 5) ^ (w & 2)));
#pragma unroll 1
    for (int q = q0; q < q1; ++q) {
      const int k = (int)((((bj & 1) ? ORD1 : ((unsigned __int128)0x876543210u << 36 | 0x876543210ull)) >> (4 * (q - (q >= 9 ? 9 : 0)) + (q >= 9 ? 36 : 0))) & 15u), h = q >= 9;
      if (q >= RQ) rest();
      dma16(gs + (size_t)(288 * h + 16 * k) * (F_ * 2), gv, lb0 + LW * h + 9216 * w + 1024 * k);
      asm volatile("s_waitcnt vmcnt(%0)" : : "n"(depth.value) : "memory");
    }
  };
  auto down_dma = [&](int k0, int k1, int lane) {
#pragma unroll 1
    for (int k = k0; k < k1; ++k) {
      const int o = 9216 * wl + 2304 * k;
      const char* ds = (const char*)wd + (size_t)(fb * 36864 + o);
      const uint32_t dl = lb0 + o + 32 * ((4 * wl + k + 1) >> 1);
      if (ROLE == 1) rest();
      dma16d(ds, 16u * lane, dl);
      dma16d(ds + 1024, 16u * lane, dl + 1024);
      uint32_t keep;
      asm volatile("s_mov_b32 %0, m0\n\ts_mov_b32 m0, %3\n\ts_mov_b64 exec, 0xffff\n\tglobal_load_lds_dwordx4 %1, %2" DPOL2 "\n\ts_mov_b64 exec, -1\n\ts_mov_b32 m0, %0\n\ts_waitcnt vmcnt(%4)"
                   : "=&s"(keep) : "v"(16u * lane), "s"(ds + 2048), "s"(dl + 2048), "n"(ROLE == 1 ? DDEPTH : 40) : "memory");
    }
  };
  auto gu_read = [&](int h) {
    const uint32_t ra = lb0 + LW * h + (uint32_t)(((lane >> 1) & 1) * 18432 + (8 * kq + ((lane >> 2) & 3)) * 64 + ((w ^ (kq & 1) ^ (lane & 2)) * 16) + 8 * (lane & 1));
#pragma unroll
    for (int s = 0; s < 9; ++s) {
      const u32x2 h0 = trd(ra + 2048 * s), h1 = trd(ra + 2048 * s + 256);
      bw[9 * h + s] = (u32x4){h0[0], h0[1], h1[0], h1[1]};
    }
#pragma unroll
    for (int s = 0; s < 9; ++s) asm volatile("" : "+v"(bw[9 * h + s]));
  };
  uint32_t tv = 0u, tk = 0u;
  const uint32_t vearly = (uint32_t)((VEARLY >> (8 * (g0 >> 5))) & 0xffull);
  auto w_line = [&](int n) {
    const int q = min(lane, 62) / 9, r = min(7 * (n >> 2) + q, 287), h = r >= 144;
    return (const char*)((n & 1) ? wu : wg) + (uint32_t)((r + 144 * (h + ((n >> 1) & 1))) * (F_ * 2) + bx * 1152 + (min(lane, 62) - 9 * q) * 128);
  };
#define TOUCH(pa) asm volatile("global_load_dword v126, %0, off" DPOL4 : : "v"(pa) : "memory", "v126")
  uint32_t wv[5], xv[9];
  auto rows_xw = [&](int lane) {
    u32x4 wa, xr, xs;
    const uint32_t xo = (uint32_t)((8 * g2 + 2 * wl) * (D_ * 2));
    asm volatile("global_load_dwordx4 %0, %5, %9\n\tglobal_load_dword %1, %6, %9 offset:1024\n\t"
                 "global_load_dwordx4 %2, %7, %10\n\tglobal_load_dwordx4 %3, %7, %10 offset:1152\n\tglobal_load_dword %4, %8, %10 offset:1024\n\ts_waitcnt vmcnt(0)"
                 : "=&v"(wa), "=&v"(wv[4]), "=&v"(xr), "=&v"(xs), "=&v"(xv[8])
                 : "v"(16u * lane), "v"(4u * (lane & 31)), "v"(xo + 16u * lane), "v"(xo + 4u * lane + 32u * (lane & 32)), "s"(wr), "s"(x) : "memory");
#pragma unroll
    for (int k = 0; k < 4; ++k) { wv[k] = wa[k]; xv[k] = xr[k]; xv[4 + k] = xs[k]; }
  };
  auto ln1 = [&](int g, int o, uint32_t c0) {
    uint32_t uo = u_at(g, 64 * wl + lane);
    uint32_t ta = lb0 + (uint32_t)(o + ((lane >> 2) & 3) * RP + (64 * wl + 16 * kq + 4 * (lane & 3)) * 2);
    uint32_t la = (uint32_t)(o + 2 * wl * RP + 16 * lane), lt = (uint32_t)(o + (2 * wl + (lane >> 5)) * RP + 1024 + 4 * (lane & 31));
    uint32_t sg = O_SU + (uint32_t)(256 * (lane >> 3) + g) * 128u + 16u * (lane & 7);
    asm volatile("" : "+v"(uo), "+v"(ta), "+v"(la), "+v"(lt), "+v"(sg));
    f32x4 z4 = {0.f, 0.f, 0.f, 0.f};
    asm volatile("" : "+v"(z4));
    asm volatile("s_waitcnt vmcnt(0)" : "={v[100:103]}"(xa), "={v[104:107]}"(xc), "={v108}"(r0), "={v109}"(ep));
    TS(0, 1);
    const uint32_t v[9] = {xa[0], xa[1], xa[2], xa[3], xc[0], xc[1], xc[2], xc[3], r0};
    uint32_t c[9];
    f32x2 tf[9];
    T = ((uint32_t)__builtin_amdgcn_readfirstlane(ep) + 1u) * 0x9E3779B1u;
    if (wl == 3) { const uint32_t now = (uint32_t)__builtin_amdgcn_s_memrealtime() + SD; ST16(sg, ((u32x4){T, now, T, now})); }
    const uint32_t tj = ((T >> (3 * wl)) & 0x00070007u) | (((T >> (12 + wl)) & 0x00010001u) << 14);
    uint32_t tw[3] = {(tj & 0xffffu) * 0x10001u, (tj >> 16) * 0x10001u, 0u};
    tw[2] = (lane < 32) ? tw[0] : tw[1];
    asm volatile("" : "+v"(tw[0]), "+v"(tw[1]), "+v"(tw[2]));
    f32x2 rc, nc;
    float e_;
    ln_stats<false>(v, lane, 0.03125f, tf, z4, rc, nc, e_);
    ln_apply<true>(tf, lane, rc, nc, c);
#pragma unroll
    for (int k = 0; k < 9; ++k) c[k] = (c[k] & ~UMASK) | tw[k >> 2];
    *(u32x4*)(smem + la) = (u32x4){c[0], c[1], c[2], c[3]};
    *(u32x4*)(smem + la + RP) = (u32x4){c[4], c[5], c[6], c[7]};
    *(uint32_t*)(smem + lt) = c[8];
    u32x4 ha, hb;
    {
      uint32_t t, cc, n = SPIN_CAP;
      asm volatile("s_mov_b64 exec, 1\n\tv_mov_b32 %2, 1\n\tds_add_u32 %5, %2\n\ts_mov_b64 exec, -1\n"
                   "1:\n\tds_read_b32 %2, %5\n\tds_read_b64_tr_b16 v[40:41], %6\n\tds_read_b64_tr_b16 v[42:43], %6 offset:%7\n\tds_read_b64_tr_b16 v[44:45], %6 offset:512\n\t"
                   "ds_read_b64_tr_b16 v[46:47], %6 offset:%8\n\ts_sub_u32 %4, %4, 1\n\ts_cbranch_scc1 2f\n\ts_waitcnt lgkmcnt(4)\n\tv_readfirstlane_b32 %3, %2\n\t"
                   "s_cmp_lt_u32 %3, 4\n\ts_cbranch_scc1 1b\n2:\n\ts_waitcnt lgkmcnt(0)"
                   : "=&{v[40:43]}"(ha), "=&{v[44:47]}"(hb), "=&v"(t), "=&s"(cc), "+s"(n) : "v"(c0), "v"(ta), "n"(4 * RP), "n"(512 + 4 * RP) : "memory", "scc");
    }
    ST16(uo, ha);
    ST16(uo + 64 * 128, hb);
    if (wl == 0) {
      const u32x2 h4 = trd(ta + 1024), h5 = trd(ta + 1024 + 4 * RP);
      ST16(uo + 128 * 128, ((u32x4){h4[0], h4[1], h5[0], h5[1]}));
    }
  };
  const int dX = 512 - 24 + 8 * bx + bj;
  if constexpr (ROLE == 2) {
    const int tl = tid - 256;
    if (XOFF && (uint32_t)(bj - 4) < 8u) {
      __builtin_amdgcn_s_setprio(3);
      ln1(tg + 160, 10240, ca + 32);
      TS(0, 2);
    }
    if (!isx && T0) {
      SLEEP(T0SLEEP);
      if (wl < T0) TOUCH(w_line(24 * wl + bj));
      if (T1) { asm volatile("s_waitcnt vmcnt(0)" ::: "memory"); SLEEP(T1SLEEP); if (wl < T1) TOUCH(w_line(24 * (T0 + wl) + bj)); }
    }
    if (isx) {
      __builtin_amdgcn_s_setprio(QPRIO0);
      *(u32x4*)(smem + X_TB + 16 * tl) = zero4;
      *(u32x4*)(smem + X_TB + 16 * min(256 + tl, (TB + C_U) / 16 - 1)) = zero4;
      const int pr = (wl + XROT) & 3;
      COps e;
      conv_ops(lb0 + X_TB, lb0 + X_UL, lb0 + X_IM, lane, e);
      TOps t;
      table_ops(lb0 + X_IM, lb0 + X_TB, wl & 1, lane, t);
      e.o = lb0 + X_ST + (uint32_t)(16 * ((2 * i + (kq >> 1)) ^ ((i >> 2) & 1)) + 8 * (kq & 1));
      e.f = ca + 28 - 32;
      asm volatile("" : "+v"(e.o), "+v"(e.f));
      float ldt, are, aim, bre, bim, cre, cim, dsk;
      P_WAIT(0, , "v103", "v104", "v105", "v106", "v107", "v109");
      asm volatile("" : "={v109}"(ep));
      T = ((uint32_t)__builtin_amdgcn_readfirstlane(ep) + 1u) * 0x9E3779B1u;
      P_MORE();
      asm volatile("v_lshlrev_b32 %0, 16, v107" : "=v"(dsk) : : "v107");
      TS(0, 1);
      __builtin_amdgcn_s_setprio(QPRIO);
      int* evp = (int*)(sel + 44);
      char* im = smem + X_IM + IP * (tl & 127);
      evp[wl] = (wl < 2) ? gen_y(ldt, are, aim, bre, bim, cre, cim, lb0 + X_IM + IP * tl, [] {} TSA) : gen_x(ldt, are, aim, im);
      TS(0, 2);
      __builtin_amdgcn_s_setprio(QPRIO2);
      gsync(ca + 8, 4);
      if (wl < 2) evp[-4 + wl] = (int)table_tile(t, wl & 1, lane, lb0 + LM + 4 * (128 + 44), dsk TSA);
      if (wl < 2) gsync0(ca + 8, 8); else { count_up(ca + 8); SLEEP(XSL); gwait(ca + 8, 8); }
      const uint32_t cs = (uint32_t)__builtin_amdgcn_readfirstlane(evp[-4]);
      if (wl == 2 && lane == 0) asm volatile("global_store_dwordx2 %0, %1, %2 sc1" : : "v"(O_SC + 16u * dX), "v"((u32x2){T, cs}), "s"(ws) : "memory");
      if (wl == 3) *(u32x4*)(smem + X_IM + pad_at(lane & 7)) = zero4;
      TS(0, 3);
      CONV_RUN(2 + pr, e, , "+{v11}"(tid2));
      {
        const int lane = tid2 & 63;
        const u32x4 v = *(const u32x4*)(smem + X_ST + 2048 * pr + 1024 * (lane >> 5) + 16 * ((lane & 31) ^ ((lane >> 3) & 1)));
        if (__builtin_expect(v_put(v, (u32x4){utag(T, 0), utag(T, 1), utag(T, 2), utag(T, 3)}, cs != S16, v_at(dX, 64 * pr + lane)), 0)) {
          DRAIN();
          if (lane == 0) ST4(O_DN + 16u * (4 * (dX - 256) + pr), T);
        }
      }
      TS(0, 6);
    }
    __builtin_amdgcn_s_setprio(PPRIO);
    asm volatile("" : "+v"(tid2));
    if (isx) {
      const int lane = tid2 & 63;
      const u32x4 ra = *(const u32x4*)(smem + X_RW(wl) + 16 * lane), rb = *(const u32x4*)(smem + X_RW(wl) + 1024 + 16 * lane), rw = *(const u32x4*)(smem + X_WR + 16 * lane);
      const u32x2 rc = *(const u32x2*)(smem + X_RW(wl) + 2048 + 8 * lane);
#pragma unroll
      for (int k = 0; k < 4; ++k) { xv[k] = ra[k]; xv[4 + k] = rb[k]; wv[k] = rw[k]; }
      xv[8] = rc[0]; wv[4] = rc[1];
    } else rows_xw(tid2 & 63);
    if (!isx && !ffn) {
      SLEEP(RSLEEP);
#pragma unroll
      for (int k = 0; k < 7; ++k) ST16(O_SL + (uint32_t)((6 * bx + bj - 18) * 1792 + 256 * k - 256 + tid2) * 16u, ones);
      DRAIN();
    }
    if (!isx) BAR();
    const uint32_t tp = (uint32_t)__builtin_amdgcn_readfirstlane(cnt[4]);
    if (!isx) T = (uint32_t)__builtin_amdgcn_readfirstlane(sel[43]);
    tv = tp + UD + VD2 - vearly;
  } else {
    __builtin_amdgcn_s_setprio(3);
    if (tid < 12) cnt[tid] = 0u;
    BAR();
    if (tid < 20) ((uint32_t*)(hm + 320))[tid] = 0u;
    if (tid >= 184) *(u32x4*)(smem + L_SC - 16 * 184 + 16 * tid) = (u32x4){0x41804180u, 0x41804180u, 0x41804180u, 0x41804180u};
    uint32_t sink = 0;
    if (XOFF && isx) {
      asm volatile("s_waitcnt vmcnt(0)" : "={v109}"(ep));
      T = ((uint32_t)__builtin_amdgcn_readfirstlane(ep) + 1u) * 0x9E3779B1u;
    } else ln1(tg, 0, ca + 12);
    TS(0, 2);
    __builtin_amdgcn_s_setprio(NPRIO);
    if (!isx && 3 - w < TB1) TOUCH(w_line(24 * (3 - w) + 23 - bj));
    asm volatile("s_waitcnt vmcnt(0)" : "+v"(sink) : : "memory");
    TS(0, 13);
    if (w == 0) {
      const uint32_t xr = O_XC + (uint32_t)((511 - 2 * (g0 + w)) * 144 + lane) * 16u;
      ST16(xr, ones);
      ST16(xr + 1024u, ones);
      if (lane < 16) ST16(xr + 2048u, ones);
    }
    if (w == 2 && b < 72 && lane < 8) ST16(O_FB + (uint32_t)(8 * b + lane) * 16u, ones);
    if (isx) {
      rows_xw(lane);
      char* sp = smem + X_RW(w) + 16 * lane;
      *(u32x4*)sp = (u32x4){xv[0], xv[1], xv[2], xv[3]};
      *(u32x4*)(sp + 1024) = (u32x4){xv[4], xv[5], xv[6], xv[7]};
      *(u32x2*)(smem + X_RW(w) + 2048 + 8 * lane) = (u32x2){xv[8], wv[4]};
      if (w == 0) *(u32x4*)(smem + X_WR + 16 * lane) = (u32x4){wv[0], wv[1], wv[2], wv[3]};
    }
    SLEEP(ESLEEP);
    uint32_t tu = latest(gate(ws, O_SU + (uint32_t)(256 * bx + 64 * w + 4 * i + (bj & 3)) * 128u, T));
    asm volatile("s_mov_b64 exec, 1\n\tds_max_u32 %0, %1\n\ts_mov_b64 exec, -1" : : "v"(ca + 16), "v"(tu) : "memory");
    if (w == 0) sel[43] = T;
    if (isx) {
      const uint32_t xo = u_at(tid, dX);
      char* sm = smem + X_UL + C_U - 16 + 16 * tid;
      const u32x4 ut = {utag(T, 0), utag(T, 1), utag(T, 2), utag(T, 3)};
      __builtin_amdgcn_s_setprio(3);
      until(tu + UD + XD);
      u32x4 uw;
      asm volatile("global_load_dwordx4 %0, %1, %2 sc1" : "=&v"(uw) : "v"(xo), "s"(ws) : "memory");
      asm volatile("s_waitcnt vmcnt(0)" : "+v"(uw));
      uw ^= ut;
      u_again(uw, ut, xo);
      *(u32x4*)(sm + 16) = uw;
      if (XF) { gsync0(ca + 12, XOFF ? 4 : 8); gwait(ca + 8, 8); } else BAR();
      if (w < 3) {
        const u32x4 pw = *(const u32x4*)sm;
        if (w < 2) sums<true>(smem + X_IM + 16 * tid, pw, uw); else sums<false>(smem + X_IM + 16 * tid, pw, uw);
        count_up(ca + 28);
      }
      if (XF) BAR();
    } else {
      until(tu + UD + WD);
      BAR();
    }
    tu = (uint32_t)__builtin_amdgcn_readfirstlane(cnt[4]);
    TS(0, 3);
    __builtin_amdgcn_s_setprio(DPRIO);
    if (ffn) {
      ts1 = (tu + UD + VD2 - TSTOP) | 1u;
      gu_dma(0, N0, IC<40>{});
      gu_dma(N0, NA, IC<WDEPTH>{});
      asm volatile("s_waitcnt vmcnt(%0)" : : "n"(NA - 9) : "memory");
      gsync0(ca + 4, 4);
      gu_read(0);
      asm volatile("s_waitcnt lgkmcnt(0)" ::: "memory");
      count_up(ca + 36);
      TS(0, 4);
      gu_dma(NA, 18, IC<WDEPTH2>{});
      gwait(ca + 36, 4);
      down_dma(0, KN, lane);
      asm volatile("s_waitcnt vmcnt(%0)" : : "n"(3 * KN) : "memory");
      count_up(ca + 4);
      TS(0, 7);
    } else {
      if (!isx && C3) {
        const int n = 6 * min(tid, 191) + bj - 18, r = n >> 2;
        const char* wb = (const char*)((n & 1) ? wu : wg) + bx * 1152;
        const int c0 = (3 - (int)((uintptr_t)wb >> 7)) & 7;
        wb += (uint32_t)((r + 144 * ((r >= 144) + ((n >> 1) & 1))) * (F_ * 2) + 128 * c0);
        TOUCH(wb);
        if (c0 == 0) TOUCH(wb + 1024);
      }
      auto d_line = [&](int k) {
        const int n = min((k * 14 + bj - 18) * 64 + lane, 5183), q = n / 1296, r = n - 1296 * q;
        return (const char*)wd + (uint32_t)(bx * 5184 + (r / 18) * 72 + 18 * (HREV ? 3 - q : q) + r % 18) * 128u;
      };
      int k = w;
      if (w < HW) {
#pragma unroll 1
        for (; k < 6 && (int)((uint32_t)__builtin_amdgcn_s_memrealtime() - (tu + UD + VD2 - HSTOP)) < 0; k += HW) { TOUCH(d_line(k)); DRAIN(); }
      }
      TS(0, 4);
      until(tu + UD + VD2 + RD2);
      if (RW) gwait(ca, 4);
      if (w < HW) {
#pragma unroll 1
        for (; k < 6; k += HW) { TOUCH(d_line(k)); DRAIN(); }
      }
      TS(0, 7);
    }
    tv = tu + UD + VD2 - vearly;
    tk = tu + UD + VD2 + KD;
  }
  {
  const int tid = tid2, lane = tid & 63, i = lane & 15, kq = lane >> 4;
  __builtin_amdgcn_s_setprio(3);
  const int PL = ffn ? 6400 : LW + 6656;
  f32x2 xf[9];
  f32x4 za = {0.f, 0.f, 0.f, 0.f}, zb = za;
  uint32_t al = O_XC + 64u * (i >> 3) + 16u * kq + 128u * w;
  uint32_t ha = (ROLE == 1) ? (uint32_t)((4 * (kq & 1) + 2 * (kq >> 1) + (i >> 3)) * 80 + 16 * w + 2 * (i & 7)) : 0u;
  uint32_t h0a = (uint32_t)(min(i, 8) * 80 + 16 * kq), h1a = (uint32_t)(min(i ^ 8, 8) * 80 + 16 * kq);
  uint32_t so = O_SL + (uint32_t)fb * SP + ((ROLE == 2) ? 16u * tid - 4096u : 8192u + 16u * lane);
  uint32_t da = lb0 + (uint32_t)((8 * kq + ((lane >> 2) & 3)) * 1152 + 32 * ((8 * kq + ((lane >> 2) & 3) + 2) >> 2) + 8 * (lane & 3) + ((ROLE == 2) ? 128 * wl : 1024));
  if (ROLE == 2) asm volatile("" : "+v"(h0a), "+v"(h1a), "+v"(so), "+v"(da)); else asm volatile("" : "+v"(ha), "+v"(al), "+v"(da));
  uint32_t hv[9] = {}, nv[9] = {}, keya = 0u, keyb = 0u;
  int rka = 0, rkb = 0;
  auto put_row = [&](const uint32_t* r, int j, const void* base, uint32_t ro) {
    const u32x4 tl = {r[8], (uint32_t)DPP_I((int)r[8], 0x55, 0xf), (uint32_t)DPP_I((int)r[8], 0xaa, 0xf), (uint32_t)DPP_I((int)r[8], 0xff, 0xf)};
    asm volatile("global_store_dwordx4 %0, %1, %2 sc1\n\ts_nop 1" : : "v"(ro + 16u * lane), "v"((u32x4){r[4 * j], r[4 * j + 1], r[4 * j + 2], r[4 * j + 3]}), "s"(base) : "memory");
    if ((lane & 35) == 32 * j) asm volatile("global_store_dwordx4 %0, %1, %2 offset:1024 sc1\n\ts_nop 1" : : "v"(ro + 4u * (lane & 31)), "v"(tl), "s"(base) : "memory");
  };
  if (ln2) {
    const int po = PL - 2560 * (ROLE == 1);
    uint32_t pa = (uint32_t)(po + 9216 * wl + 32 * lane), pb = (uint32_t)(po + 9216 * wl + 2048 + 8 * (lane & 31)), ps = (lane < 32) ? 0u : 16u;
    uint32_t kid = (uint32_t)(((255 - g2) << 5) | (7 - 2 * wl - (lane >> 5)));
    const int tl = tid - 256, t2 = tl + 128, d1 = 64 * (tl / 48) + tl % 48, d3 = 64 * (t2 / 48) + t2 % 48;
    const int d2 = (tl < 128) ? 64 * (tl >> 4) + 48 + (tl & 15) : min(384 + tl, 575);
    uint32_t vo = v_at(d1, g0), vo2 = v_at(d2, g0), vo3 = v_at(d3, g0), px = (uint32_t)(PL + 4 * d1), py = (uint32_t)(PL + 4 * d2), pz = (uint32_t)(PL + 4 * d3);
#pragma unroll
    for (int k = 0; k < 9; ++k) xf[k] = (f32x2){bflo(xv[k]), bfhi(xv[k])};
    asm volatile("" : "+v"(xf[0]), "+v"(xf[1]), "+v"(xf[2]), "+v"(xf[3]), "+v"(xf[4]), "+v"(xf[5]), "+v"(xf[6]), "+v"(xf[7]), "+v"(xf[8]));
    asm volatile("" : "+v"(za), "+v"(zb), "+v"(pa), "+v"(pb), "+v"(ps), "+v"(kid), "+v"(vo), "+v"(vo2), "+v"(vo3), "+v"(px), "+v"(py), "+v"(pz));
    u32x4 ut = {utag(T, 0), utag(T, 1), utag(T, 2), utag(T, 3)};
    float sv[10], ws_ = dot2(wv[4], 0x3f803f80u, 0.f) * 0.5f;
#pragma unroll
    for (int k = 0; k < 4; ++k) ws_ = dot2(wv[k], 0x3f803f80u, ws_);
    ws_ = wave_sum(ws_);
#pragma unroll
    for (int k = 0; k < 10; ++k) sv[k] = 16.f;
    uint32_t ka_ = lb0 + LM + 4u * (128 + 48 + 2 * wl + (lane >> 5)), kb_ = lb0 + LM + 4u * (128 + 48);
    uint32_t ko_ = O_KEY + (uint32_t)(256 * (lane & 7) + 8 * (g2 & 31) + (g2 >> 5)) * 16u;
    asm volatile("" : "+v"(ut), "+v"(ws_), "+v"(sv[0]), "+v"(sv[1]), "+v"(sv[2]), "+v"(sv[3]), "+v"(sv[4]), "+v"(sv[5]), "+v"(sv[6]), "+v"(sv[7]), "+v"(sv[8]), "+v"(sv[9]),
                 "+v"(ka_), "+v"(kb_), "+v"(ko_));
    if (ffn) gwait(ca + 36, 4);
    TS(0, 7);
    until(tv);
    TS(0, 8);
    u32x4 va, vc = ut, vb = ut;
    asm volatile("global_load_dwordx4 %0, %2, %4 sc1\n\ts_cmp_lt_u32 %5, 2\n\ts_cbranch_scc1 1f\n\tglobal_load_dwordx4 %1, %3, %4 sc1\n1:"
                 : "=&v"(va), "+v"(vc) : "v"(vo), "v"(vo3), "s"(ws), "s"(wl) : "memory", "scc");
    until(tv + V2D);
    asm volatile("s_cmp_eq_u32 %5, 3\n\ts_cbranch_scc1 1f\n\tglobal_load_dwordx4 %2, %3, %4 sc1\n\ts_waitcnt vmcnt(1)\n\ts_branch 2f\n1:\n\ts_waitcnt vmcnt(0)\n2:"
                 : "+v"(va), "+v"(vc), "+v"(vb) : "v"(vo2), "s"(ws), "s"(wl) : "memory", "scc");
    auto turn1 = [&] {
#pragma unroll
      for (int e = 0; e < 4; ++e) *(uint32_t*)(smem + px + 9216 * e) = va[e];
      if (wl >= 2) {
#pragma unroll
        for (int e = 0; e < 4; ++e) *(uint32_t*)(smem + pz + 9216 * e) = vc[e];
      }
    };
    auto turn = [&] {
      if (wl < 3) {
#pragma unroll
        for (int e = 0; e < 4; ++e) *(uint32_t*)(smem + py + 9216 * e) = vb[e];
      }
    };
    va ^= ut; vc ^= ut;
    turn1();
    asm volatile("s_waitcnt vmcnt(0)" : "+v"(vb) : : "memory");
    vb ^= ut;
    turn();
    const u32x4 vs = va | vb | vc;
    if (__builtin_expect(__any(((vs[0] | vs[1] | vs[2] | vs[3]) & UMASK) != 0u), 0)) {
#pragma unroll 1
      for (int spin = 0; spin < 4; ++spin) {
        const bool na = ((va[0] | va[1] | va[2] | va[3]) & UMASK) != 0u, nb = ((vb[0] | vb[1] | vb[2] | vb[3]) & UMASK) != 0u, nc = ((vc[0] | vc[1] | vc[2] | vc[3]) & UMASK) != 0u;
        if (!__any(na || nb || nc)) break;
        TSC(16);
        if (na) LD16(va, vo);
        if (nb) LD16(vb, vo2);
        if (nc) LD16(vc, vo3);
        asm volatile("s_waitcnt vmcnt(0)" : "+v"(va), "+v"(vb), "+v"(vc) : : "memory");
        if (na) va ^= ut;
        if (nb) vb ^= ut;
        if (nc) vc ^= ut;
      }
      auto again = [&](u32x4& v, uint32_t o, int d) {
        const int J = g0 >> 5;
        const uint32_t dn = O_DN + 16u * (uint32_t)((d < 512) ? 4 * (d >> 1) + ((0x01013232 >> (4 * J)) & 3) : 4 * (d - 256) + (J >> 1));
        bool pend = ((v[0] | v[1] | v[2] | v[3]) & UMASK) != 0u;
#pragma unroll 1
        for (int spin = 0; spin < SPIN_CAP && __any(pend); ++spin) {
          TSC(16);
          SLEEP(PSLEEP);
          if (pend) {
            uint32_t fin = ~T;
            if ((v[0] & v[1] & v[2] & v[3] & LMASK) == LMASK)
              asm volatile("global_load_dword %0, %1, %2 sc1\n\ts_waitcnt vmcnt(0)" : "=&v"(fin) : "v"(dn), "s"(ws) : "memory");
            LD16W(v, o);
            v ^= ut;
            pend = ((v[0] | v[1] | v[2] | v[3]) & UMASK) != 0u;
            if (pend && fin == T) {
              u32x2 sc;
              for (int k = 0; k < SPIN_CAP; ++k) {
                asm volatile("global_load_dwordx2 %0, %1, %2 sc1\n\ts_waitcnt vmcnt(0)" : "=&v"(sc) : "v"(O_SC + 16u * d), "s"(ws) : "memory");
                if (sc[0] == T) break;
              }
              *(u16*)(smem + L_SC + 2 * d) = (u16)(sc[1] >> 16);
              cnt[10] = 1u;
              v = (v ^ ut) & ~LMASK;
              pend = false;
            }
          }
        }
      };
      again(va, vo, d1); again(vc, vo3, d3); again(vb, vo2, d2);
      turn1();
      turn();
    }
    u32x4 a0, a1;
    u32x2 a2, cf;
    {
      uint32_t cc, n = SPIN_CAP;
      asm volatile("s_mov_b64 exec, 1\n\tv_mov_b32 v90, 1\n\tds_add_u32 %6, v90\n\ts_mov_b64 exec, -1\n"
                   "1:\n\tds_read2_b32 %3, %6 offset1:10\n\tds_read_b128 %0, %7\n\tds_read_b128 %1, %7 offset:16\n\tds_read_b64 %2, %8\n\t"
                   "s_sub_u32 %5, %5, 1\n\ts_cbranch_scc1 2f\n\ts_waitcnt lgkmcnt(3)\n\tv_readfirstlane_b32 %4, v90\n\t"
                   "s_cmp_lt_u32 %4, 4\n\ts_cbranch_scc1 1b\n2:\n\ts_waitcnt lgkmcnt(0)"
                   : "=&v"(a0), "=&v"(a1), "=&v"(a2), "=&{v[90:91]}"(cf), "=&s"(cc), "+s"(n) : "v"(ca), "v"(lb0 + pa), "v"(lb0 + pb) : "memory", "scc");
    }
    TS(0, 9);
    if (__builtin_expect(__builtin_amdgcn_readfirstlane(cf[1]) != 0, 0)) {
      const u32x4 q = *(const u32x4*)(smem + L_SC + 16 * lane);
      const uint32_t r = *(const uint32_t*)(smem + L_SC + 1024 + 4 * (lane & 31));
#pragma unroll
      for (int k = 0; k < 4; ++k) { sv[2 * k] = bflo(q[k]); sv[2 * k + 1] = bfhi(q[k]); }
      sv[8] = bflo(r); sv[9] = bfhi(r);
    }
    const uint32_t av[10] = {a0[0], a0[1], a0[2], a0[3], a1[0], a1[1], a1[2], a1[3], a2[0] >> ps, a2[1] >> ps};
    f32x2 h[9];
#pragma unroll
    for (int k = 0; k < 10; ++k) {
      asm("v_fma_mix_f32 %0, %1, %2, %3 op_sel_hi:[1,0,0]" : "=v"(h[(k < 8) ? k >> 1 : 8][k & 1]) : "v"(av[k]), "v"(sv[k]), "v"(xf[(k < 8) ? k >> 1 : 8][k & 1]));
      if (k < 8) asm("v_fma_mix_f32 %0, %1, %2, %3 op_sel:[1,0,0] op_sel_hi:[1,0,0]" : "=v"(h[4 + (k >> 1)][k & 1]) : "v"(av[k]), "v"(sv[k]), "v"(xf[4 + (k >> 1)][k & 1]));
    }
#pragma unroll
    for (int k = 0; k < 9; ++k) hv[k] = pack2(h[k][0], h[k][1]);
    const float da = dot2(hv[2], wv[2], dot2(hv[0], wv[0], zb[0])) + dot2(hv[3], wv[3], dot2(hv[1], wv[1], zb[1]));
    const float db = dot2(hv[6], wv[2], dot2(hv[4], wv[0], zb[2])) + dot2(hv[7], wv[3], dot2(hv[5], wv[1], zb[3]));
    float dt = dot2(hv[8], wv[4], fold32(da, db));
    f32x2 rc, nc;
    ln_stats<true>(hv, lane, 1.f, xf, za, rc, nc, dt);
    dt = (lane < 32) ? dt * rc[0] + ws_ * nc[0] : dt * rc[1] + ws_ * nc[1];
    const uint32_t bb = pack2(dt, dt) & 0xffff0000u;
    const uint32_t kk = (bb ^ (((uint32_t)((int)bb >> 31) & 0x7fff0000u) | 0x80000000u)) | kid;
    u32x4 ka, kb;
    uint32_t old;
    asm volatile("s_mov_b64 exec, %5\n\tds_write_b32 %3, %4\n\ts_mov_b64 exec, 1\n\tv_mov_b32 %2, 1\n\tds_add_rtn_u32 %2, %7, %2\n\ts_mov_b64 exec, -1\n\t"
                 "ds_read_b128 %0, %6\n\tds_read_b128 %1, %6 offset:16\n\ts_waitcnt lgkmcnt(0)"
                 : "=&v"(ka), "=&v"(kb), "=&v"(old) : "v"(ka_), "v"(kk), "s"(0x8000000080000000ull), "v"(kb_), "v"(ca) : "memory");
    if (__builtin_amdgcn_readfirstlane(old) == 7) {
      uint32_t m1 = max(max(ka[0], ka[1]), ka[2]), m2 = med3u(ka[0], ka[1], ka[2]), m3 = min(min(ka[0], ka[1]), ka[2]);
#pragma unroll
      for (int e = 0; e < 5; ++e) {
        const uint32_t kx = e ? kb[e - 1] : ka[3];
        m3 = med3u(m2, m3, kx); m2 = med3u(m1, m2, kx); m1 = max(m1, kx);
      }
      if (lane < 8) ST16(ko_, ((u32x4){m1 | 24u, m2 | 16u, m3 | 8u, T & ~1u}));
    }
    TS(0, 10);
    keya = (uint32_t)__builtin_amdgcn_readlane(kk, 31); keyb = (uint32_t)__builtin_amdgcn_readlane(kk, 63);
    ln_apply<false>(xf, lane, rc, nc, nv);
    gwait(ca, 8);
    if (wl == 1) {
      const u32x4 kv = *(const u32x4*)&kl[4 * (lane & 1)];
      if (lane < 2) ST16(O_KF + (uint32_t)(2 * g2 + lane) * 16u, ((u32x4){__builtin_amdgcn_perm(kv[1], kv[0], 0x07060302u), T, __builtin_amdgcn_perm(kv[3], kv[2], 0x07060302u), T}));
      if (ROLE == 2 && b == 0 && lane == 0) ST4(O_EP, T * 0x0E8B2F51u);
    }
    const uint32_t ko = kl[lane & 7];
    rka = FORCEFB + __popcll(__ballot(ko > keya) & 0xffull); rkb = FORCEFB + __popcll(__ballot(ko > keyb) & 0xffull);
#pragma unroll
    for (int k = 0; k < 9; ++k) {
      const f32x2 o = xf[k] + (f32x2){bflo(nv[k]), bfhi(nv[k])};
      hv[k] = pack2(o[0], o[1]);
      nv[k] = min(nv[k], 0xfffffffeu);
    }
    if (rka < 2) put_row(nv, 0, ws, O_XC + (uint32_t)(1023 - 4 * g2 - rka) * 1152u);
    if (rkb < 2) put_row(nv, 1, ws, O_XC + (uint32_t)(1023 - 4 * g2 - rkb) * 1152u);
    if (!LPOS && ffn) down_dma(KN, 4, lane);
  }
  uint32_t k3rd = 0u, k4th = 0u;
  if constexpr (ROLE == 1) {
    u32x4 key;
    const uint32_t kof = O_KEY + (uint32_t)(256 * bx + tid) * 16u;
    until(tk);
    if (!ffn) SLEEP(NSLEEP);
    LD16(key, kof);
    if (ffn) {
      gwait0(ca + 4, 8);
      gu_read(1);
    }
    asm volatile("s_waitcnt vmcnt(0)" : "+v"(key) : : "memory");
    if (__builtin_expect(__any(key[3] != (T & ~1u)), 0)) {
#pragma unroll 1
      for (int spin = 0; spin < SPIN_CAP; ++spin) {
        const bool need = key[3] != (T & ~1u);
        if (!__any(need)) break;
        TSC(15);
        SLEEP(KBACK);
        if (need) LD16W(key, kof);
      }
    }
    TS(0, 11);
    const uint32_t m1 = oct_maxu(key[0]);
    const bool hit = key[0] == m1;
    const uint32_t k0 = hit ? key[1] : key[0], k1 = hit ? key[2] : key[1];
    const uint32_t m2 = oct_maxu(k0);
    if ((lane & 7) == 0) *(u32x2*)&cand[tid >> 2] = (u32x2){m1, m2};
    k3rd = (k0 == m2) ? k1 : k0;
    k4th = (k0 == m2) ? (hit ? 0u : key[2]) : k1;
    if (RPF == 1 && !ffn && w == (bj & 3)) {
      const uint32_t r1 = O_XC + ((m1 >> 3) & 0x3ffu) * 1152u + 128u * (lane & 7), r2 = O_XC + ((m2 >> 3) & 0x3ffu) * 1152u + 128u * (lane & 7);
      asm volatile("global_load_dword v126, %0, %3 sc1\n\tglobal_load_dword v126, %1, %3 sc1\n\tglobal_load_dword v126, %2, %3 offset:1024 sc1"
                   : : "v"(r1), "v"(r2), "v"(((lane & 1) ? r1 : r2) - 128u * (lane & 7)), "s"(ws) : "memory", "v126");
    }
  }
  auto pick = [&](const uint32_t* cd, uint32_t* sl, bool pf = false) {
    BAR();
    if (RPF == 2 && ROLE == 2 && pf && ffn && w == 7) {
      const uint32_t r1 = O_XC + ((cd[16 * (bj & 3) + (lane >> 2)] >> 3) & 0x3ffu) * 1152u + 128u * (lane & 3);
      asm volatile("global_load_dword v126, %0, %1 sc1\n\tglobal_load_dword v126, %0, %1 offset:512 sc1\n\tglobal_load_dword v126, %0, %1 offset:640 sc1"
                   : : "v"(r1), "s"(ws) : "memory", "v126");
    }
    const uint32_t c = cd[lane], own = cd[8 * w + (lane & 7)];
    const u32x4 la = *(const u32x4*)&cd[8 * w], lb = *(const u32x4*)&cd[8 * w + 4];
    uint32_t r = C_;
    asm("v_cmp_gt_u32 s[40:41], %1, %2\n\tv_cmp_gt_u32 s[42:43], %1, %3\n\tv_cmp_gt_u32 s[44:45], %1, %4\n\tv_cmp_gt_u32 s[46:47], %1, %5\n\t"
        "v_cmp_gt_u32 s[48:49], %1, %6\n\tv_cmp_gt_u32 s[50:51], %1, %7\n\tv_cmp_gt_u32 s[52:53], %1, %8\n\tv_cmp_gt_u32 s[54:55], %1, %9\n\t"
        "s_bcnt1_i32_b64 s40, s[40:41]\n\ts_bcnt1_i32_b64 s42, s[42:43]\n\tv_writelane_b32 %0, s40, 0\n\ts_bcnt1_i32_b64 s44, s[44:45]\n\t"
        "v_writelane_b32 %0, s42, 1\n\ts_bcnt1_i32_b64 s46, s[46:47]\n\tv_writelane_b32 %0, s44, 2\n\ts_bcnt1_i32_b64 s48, s[48:49]\n\t"
        "v_writelane_b32 %0, s46, 3\n\ts_bcnt1_i32_b64 s50, s[50:51]\n\tv_writelane_b32 %0, s48, 4\n\ts_bcnt1_i32_b64 s52, s[52:53]\n\t"
        "v_writelane_b32 %0, s50, 5\n\ts_bcnt1_i32_b64 s54, s[54:55]\n\tv_writelane_b32 %0, s52, 6\n\tv_writelane_b32 %0, s54, 7"
        : "+v"(r) : "v"(c), "v"(la[0]), "v"(la[1]), "v"(la[2]), "v"(la[3]), "v"(lb[0]), "v"(lb[1]), "v"(lb[2]), "v"(lb[3])
        : "scc", "s40", "s41", "s42", "s43", "s44", "s45", "s46", "s47", "s48", "s49", "s50", "s51", "s52", "s53", "s54", "s55");
    sl[min(r, (uint32_t)C_)] = own;
    BAR();
  };
  auto rows = [&](uint32_t ao, auto&& meanwhile) {
    u32x4 aa, ab, ac = __builtin_bit_cast(u32x4, zb);
    asm volatile("global_load_dwordx4 %0, %3, %4 sc1\n\tglobal_load_dwordx4 %1, %3, %4 offset:512 sc1\n\t"
                 "s_cmp_lg_u32 %5, 0\n\ts_cbranch_scc1 1f\n\tglobal_load_dwordx4 %2, %3, %4 offset:1024 sc1\n1:"
                 : "=&v"(aa), "=&v"(ab), "+v"(ac) : "v"(ao), "s"(ws), "s"(w) : "memory", "scc");
    __builtin_amdgcn_sched_barrier(0);
    meanwhile();
    asm volatile("s_waitcnt vmcnt(0)" : "+v"(aa), "+v"(ab), "+v"(ac) : : "memory");
    if (__builtin_expect(__any(max(max(max4(aa), max4(ab)), max4(ac)) == 0xffffffffu), 0)) {
#pragma unroll 1
      for (int spin = 0; spin < SPIN_CAP; ++spin) {
        const bool na = max4(aa) == 0xffffffffu, nb = max4(ab) == 0xffffffffu, nc = max4(ac) == 0xffffffffu;
        if (!__any(na || nb || nc)) break;
        TSC(14);
        SLEEP(PSLEEP);
        if (na) LD16W(aa, ao);
        if (nb) LD16W(ab, ao + 512u);
        if (nc) LD16W(ac, ao + 1024u);
      }
    }
    *(u32x4*)(smem + LA + 16 * tid) = aa;
    *(u32x4*)(smem + LA + 4096 + 16 * tid) = ab;
    if (w == 0) *(u32x4*)(smem + LA + 8192 + 16 * tid) = ac;
  };
  auto rows_dma = [&](uint32_t ao) {
    uint32_t dl = lb0 + LA + 1024u * w, keep;
    asm volatile("s_mov_b32 %0, m0\n\ts_mov_b32 m0, %1\n\ts_add_u32 %1, %1, 4096 - 512\n\tglobal_load_lds_dwordx4 %2, %3 sc1\n\ts_mov_b32 m0, %1\n\ts_add_u32 %1, %1, 4096 - 512\n\t"
                 "global_load_lds_dwordx4 %2, %3 offset:512 sc1\n\ts_cmp_lg_u32 %4, 0\n\ts_cbranch_scc1 1f\n\ts_mov_b32 m0, %1\n\ts_nop 0\n\t"
                 "global_load_lds_dwordx4 %2, %3 offset:1024 sc1\n1:\n\ts_mov_b32 m0, %0" : "=&s"(keep), "+s"(dl) : "v"(ao), "s"(ws), "s"(w) : "memory", "scc");
  };
  auto get_bd = [&](int t0, int t1) {
#pragma unroll
    for (int t = t0; t < t1; ++t) {
      const u32x2 h0 = trd(da + 512 * (t >> 2) + 32 * (t & 3)), h1 = trd(da + 512 * (t >> 2) + 32 * (t & 3) + 4608 + 32);
      bw[t] = (u32x4){h0[0], h0[1], h1[0], h1[1]};
    }
#pragma unroll
    for (int t = t0; t < t1; ++t) asm volatile("" : "+v"(bw[t]));
  };
  pick(cand, sel, true);
  uint32_t mykey = sel[lane & 7], ro = ((mykey >> 3) & 0x3ffu) * 1152u;
  TS(0, 12);
  auto check = [&] {
    cnt[(k3rd > (uint32_t)__builtin_amdgcn_readlane(mykey, 7)) ? 5 : 6] = 1u;
    const uint32_t m3 = oct_maxu(k3rd);
    if (k3rd == m3) k3rd = k4th;
    if ((lane & 7) == 0) cand[72 + (tid >> 3)] = m3;
    if (w == 0 && lane < 32) cand[(lane < 8) ? 64 + lane : 96 + lane] = (lane < 8) ? mykey : 0u;
  };
  if constexpr (ROLE == 1) {
    if (ffn && !FORCEFB) rows_dma(ro + al);
    check();
    DRAIN();
  } else if (ffn) {
    if (LPOS) { SLEEP(LSLEEP); down_dma(KN, 4, lane); } else { DRAIN(); gsync0(ca, 12); get_bd(0, 8); }
  }
  BAR();
  TS(0, 5);
  auto put_out = [&](int j, uint32_t k0, int rk) {
    const uint64_t mine = __ballot(((mykey ^ k0) & ~24u) == 0u) & 0xffull;
    if (mine == 0ull) put_row(hv, j, out, (uint32_t)((8 * g2 + 2 * wl + j) * (D_ * 2)));
    else if (rk >= 2) put_row(nv, j, ws, O_FB + 1152u * (uint32_t)__builtin_ctzll(mine));
  };
  auto rest_out = [&] { put_out(0, keya, rka); put_out(1, keyb, rkb); };
  f32x4 lo, hi;
  auto gate_up = [&] {
    u32x4 av[5];
    const uint32_t aa = lb0 + LA + 16u * lane;
#pragma unroll
    for (int j = 0; j < 5; ++j) asm volatile("ds_read_b128 %0, %1 offset:%2" : "=v"(av[j]) : "v"(aa), "n"(1024 * j) : "memory");
    lo = (f32x4){0.f, 0.f, 0.f, 0.f}; hi = lo;
#pragma unroll
    for (int j = 0; j < 9; ++j) {
      if (j < 5) asm volatile("s_waitcnt lgkmcnt(4)" : "+v"(av[j % 5]));
      else if (j == 5) asm volatile("s_waitcnt lgkmcnt(2)" : "+v"(av[0]), "+v"(av[1]));
      else if (j == 7) asm volatile("s_waitcnt lgkmcnt(0)" : "+v"(av[2]), "+v"(av[3]));
      lo = MMB(av[j % 5], bw[2 * j], lo);
      hi = MMB(av[j % 5], bw[2 * j + 1], hi);
      __builtin_amdgcn_sched_barrier(0);
      if (j < 4) asm volatile("ds_read_b128 %0, %1 offset:%2" : "=v"(av[j]) : "v"(aa), "n"(1024 * (j + 5)) : "memory");
    }
  };
  uint32_t fl;
  asm volatile("ds_read_b32 %0, %1" : "=v"(fl) : "v"(ca + 20) : "memory");
  if (ROLE == 1) gate_up();
  asm volatile("s_waitcnt lgkmcnt(0)" : "+v"(fl));
  const bool slow = FORCESLOW || FORCEFB || __builtin_amdgcn_readfirstlane(fl) != 0;
  if (__builtin_expect(slow, 0)) {
    TSC(17);
    pick(cand + 64, sel + 16);
    mykey = sel[16 + (lane & 7)];
    ro = ((mykey >> 3) & 0x3ffu) * 1152u;
    if (ROLE == 1 && (k3rd > (uint32_t)__builtin_amdgcn_readlane(mykey, 7) || (mykey & 24u) == 8u)) cnt[11] = 1u;
    BAR();
    if (FORCESLOW > 1 || FORCEFB || __builtin_amdgcn_readfirstlane(cnt[11]) != 0) {
    u32x4 key;
#pragma unroll 1
    for (int spin = 0; spin < SPIN_CAP; ++spin) {
      LD16W(key, O_KF + 16u * tid);
      if (!__any(key[1] != T || key[3] != T)) break;
      SLEEP(KBACK);
    }
    const uint32_t ib = (uint32_t)(((255 - (tid >> 1)) << 5) | (7 - 4 * (tid & 1)));
    uint32_t k0 = (key[0] << 16) | ib, k1 = (key[0] & 0xffff0000u) | (ib - 1), k2 = (key[2] << 16) | (ib - 2), k3 = (key[2] & 0xffff0000u) | (ib - 3);
    CEX(k0, k1) CEX(k2, k3) CEX(k0, k2) CEX(k1, k3) CEX(k1, k2)
#pragma unroll 1
    for (int c = 0; c < C_; ++c) {
      const uint32_t mx = wave_maxu(k0);
      const bool hit = (k0 == mx);
      k0 = hit ? k1 : k0; k1 = hit ? k2 : k1; k2 = hit ? k3 : k2; k3 = hit ? 0u : k3;
      if (lane == 0) cand[64 + 8 * w + c] = mx;
    }
    pick(cand + 64, sel + 32);
    mykey = sel[32 + (lane & 7)];
    const uint32_t okey = sel[32 + (lane >> 3)];
    const uint64_t same = __ballot(((mykey ^ okey) & 0x1fe0u) == 0u && okey > mykey);
    const int sl = FORCEFB + __popcll((same >> (lane & 7)) & 0x0101010101010101ull);
    ro = (sl < 2) ? (((mykey >> 3) & 0x3ffu) + 3 - sl) * 1152u : O_FB - O_XC + 1152u * (lane & 7);
    }
    if (ln2) rest_out();
    if (ffn) {
      if (ROLE == 1) rows(ro + al, [] {});
      BAR();
      if (ROLE == 1) gate_up();
    }
  }
  if (ROLE == 1 && ffn && !slow && __builtin_expect(__any(lo[0] != lo[0] || lo[1] != lo[1] || lo[2] != lo[2] || lo[3] != lo[3] ||
                                                           hi[0] != hi[0] || hi[1] != hi[1] || hi[2] != hi[2] || hi[3] != hi[3]), 0)) {
    gsync0(ca + 4, 12);
    rows(ro + al, [] {});
    gsync0(ca + 4, 16);
    gate_up();
  }
  if (OPOS == 0 && ROLE == 2 && ffn && !slow) { SLEEP(OD); rest_out(); }
  if (!ffn) {
    if (ln2 && !slow) { SLEEP(OD2); rest_out(); }
    TSE();
    return;
  }
  uint32_t fx = 0, fv = 0, fn = 0, fo = 0, sa[9];
  float rs, hx, xs;
  u32x4 se;
  if constexpr (ROLE == 1) {
    const f32x4 lh = (lane < 32) ? lo : hi;
    const float fa = fold32(lh[0], lh[2]), fb2 = fold32(lh[1], lh[3]);
    const float g = __int_as_float(__builtin_amdgcn_update_dpp(__float_as_int(fa), __float_as_int(fb2), 0x128, 0xf, 0xc, false));
    const float u = __int_as_float(__builtin_amdgcn_update_dpp(__float_as_int(fb2), __float_as_int(fa), 0x128, 0xf, 0x3, false));
    const uint32_t gu = pack2(g, u);
    const float gp = bflo(gu);
    const float sg = rbf(gp * __builtin_amdgcn_rcpf(1.f + __expf(-gp)));
    *(u16*)((char*)hm + ha) = f2bf(sg * bfhi(gu));
  } else if (LPOS) {
    DRAIN();
    gsync0(ca, 12);
    get_bd(0, 8);
  }
  BAR();
  TS(0, 6);
  if (ROLE == 2 || w == 2) {
    const u32x4 a0 = *(const u32x4*)((char*)hm + h0a), a1 = *(const u32x4*)((char*)hm + h1a);
    if (ROLE == 1) get_bd(0, 4);
    const f32x4 z = {0.f, 0.f, 0.f, 0.f};
    f32x4 o[4];
#pragma unroll
    for (int t = 0; t < 2 * ROLE; ++t) o[t] = MMB(a1, bw[2 * t + 1], MMB(a0, bw[2 * t], z));
#pragma unroll
    for (int t = 0; t < ROLE; ++t) {
      u32x4 sv;
#pragma unroll
      for (int r = 0; r < 4; ++r) sv[r] = min(pack2(o[2 * t][r], o[2 * t + 1][r]), 0xfffffffeu);
      ST16(so + 4096u * t, sv);
    }
    TS(0, 13);
  }
  if (OPOS == 1 && ROLE == 2) {
    SLEEP(OSLEEP);
    if (!slow) rest_out();
  }
  if (ROLE == 1 && w == 1) {
    const int q = (fb & 15) >> 2, ts = 4 * (q & 1) + ((i & 7) >> 1);
    const int dd = 64 * (fb >> 4) + 16 * (2 * (i & 1) + (q >> 1)) + 4 * (fb & 3) + 2 * (kq & 1) + (kq >> 1);
    const uint32_t fkey = (uint32_t)__builtin_amdgcn_ds_bpermute(4 * ts, (int)mykey), fr = (uint32_t)__builtin_amdgcn_ds_bpermute(4 * ts, (int)ro);
    const int ft = 2047 - (int)(((fkey >> 2) & 0x7f8u) | (fkey & 7u));
    fo = (uint32_t)(ft * D_ + dd) * 2u;
    u32x2 fs;
    asm volatile("global_load_ushort %0, %4, %8\n\tglobal_load_ushort %1, %5, %9 sc1\n\tglobal_load_ushort %2, %6, %9 sc1\n\tglobal_load_dwordx2 %3, %7, %9 sc1"
                 : "=&v"(fx), "=&v"(fv), "=&v"(fn), "=&v"(fs) : "v"(fo), "v"(v_at(dd, ft >> 3) + 2u * (ft & 7)), "v"(O_XC + fr + 2u * dd), "v"(O_SC + 16u * dd), "s"(x), "s"(ws) : "memory");
    uint32_t t16 = (((T >> (3 * ((ft >> 1) & 3))) & LMASK) | (((T >> (12 + ((ft >> 1) & 3))) & 0x00010001u) << 14)) >> (16 * (ft & 1)) & 0xffffu;
    const uint32_t o = fkey >> 16;
    const float rs0 = bf2f((u16)((o & 0x8000u) ? (o & 0x7fffu) : (~o & 0xffffu)));
    rs = rbf(__builtin_amdgcn_rcpf(1.f + __expf(-rs0)));
    const uint32_t one = (i < 8) ? (0x3f80u << (16 * (i & 1))) : 0u;
    se = (u32x4){(i >> 1) == 0 ? one : 0u, (i >> 1) == 1 ? one : 0u, (i >> 1) == 2 ? one : 0u, (i >> 1) == 3 ? one : 0u};
#pragma unroll
    for (int m = 0; m < 9; ++m) sa[m] = O_SL + (uint32_t)(16 * m + (lane >> 2)) * SP + (uint32_t)(4 * fb + (lane & 3)) * 16u;
    asm volatile("" : "+v"(t16), "+v"(rs), "+v"(se), "+v"(sa[0]), "+v"(sa[1]), "+v"(sa[2]), "+v"(sa[3]), "+v"(sa[4]), "+v"(sa[5]), "+v"(sa[6]), "+v"(sa[7]), "+v"(sa[8]));
    SLEEP(SSLEEP);
    TS(0, 1);
    float sum;
#pragma unroll 1
    for (int spin = 0; spin < SPIN_CAP; ++spin) {
      u32x4 sv[9];
#pragma unroll
      for (int m = 0; m < 9; ++m) LD16(sv[m], sa[m]);
      asm volatile("s_waitcnt vmcnt(9)" : "+v"(fx), "+v"(fv), "+v"(fn), "+v"(fs));
      if (__builtin_expect(__any(fs[0] != T), 0)) {
        for (int k = 0; k < SPIN_CAP && fs[0] != T; ++k)
          asm volatile("global_load_dwordx2 %0, %1, %2 sc1\n\ts_waitcnt vmcnt(0)" : "=&v"(fs) : "v"(O_SC + 16u * dd), "s"(ws) : "memory");
      }
      const uint32_t f1 = fv ^ t16, f2 = (f1 & 7u) ? fv & ~7u : f1;
      hx = rbf(bf2f((u16)fx) + (float)__builtin_bit_cast(_Float16, (u16)f2) * __uint_as_float(fs[1]));
      xs = bf2f((u16)fn);
      asm volatile("" : "+v"(hx), "+v"(xs));
      f32x4 dv = {0.f, 0.f, 0.f, 0.f};
      static_for<0, 9>([&](auto mm) {
        constexpr int m = mm.value;
        asm volatile("s_waitcnt vmcnt(%1)" : "+v"(sv[m]) : "n"(8 - m));
        dv = MMB(sv[m], se, dv);
        __builtin_amdgcn_sched_barrier(0);
      });
      sum = fold16(fold32(dv[0], dv[1]), fold32(dv[2], dv[3]));
      if (__builtin_expect(!__any(sum != sum), 1)) break;
      uint32_t mx = 0u;
#pragma unroll
      for (int m = 0; m < 9; ++m) mx = max(mx, max4(sv[m]));
      if (!__any(mx == 0xffffffffu)) break;
      TSC(18);
      SLEEP(PSLEEP);
    }
    const float o1 = rbf(xs + rbf(rbf(sum) * rs));
    if (i < 8) asm volatile("global_store_short %0, %1, %2 sc1" : : "v"(fo), "v"((uint32_t)f2bf(hx + o1)), "s"(out) : "memory");
    TS(0, 2);
  }
  TSE();
  }
  }
}
#define BROT "192"
#define CPF_V ""
#define CPF_A "s_getpc_b64 s[96:97]\n\ts_lshl_b32 s99, s2, 4\n\ts_lshl_b32 s84, s98, 7\n\ts_add_u32 s99, s99, s84\n\tv_and_b32 v112, 1, v0\n\t" \
              "v_lshl_add_u32 v112, v112, 12, s99\n\tv_min_u32 v112, kf_end-kf_all-0x80, v112\n\ts_mov_b64 exec, 3\n\tglobal_load_dword v112, v112, s[96:97]\n\ts_mov_b64 exec, -1\n\t"
extern "C" __global__ __launch_bounds__(512, 4) void kf_all(KARGS) {
  __shared__ __attribute__((aligned(16))) char smem[SMEM];
  asm volatile("s_load_dwordx16 s[64:79], s[0:1], 0x0\n\ts_load_dwordx4 s[80:83], s[0:1], 0x40\n\tv_readfirstlane_b32 s98, v0\n\t"
               "s_add_u32 s84, s2, " BROT "\n\ts_xor_b32 s84, s84, s2\n\ts_bitset0_b32 s84, 8\n\ts_bitset0_b32 s84, 9\n\ts_xor_b32 s2, s2, s84\n\t"
               "v_mov_b32 v109, 0\n\ts_and_b32 s85, s2, 7\n\ts_bitcmp1_b32 s2, 8\n\ts_cbranch_scc1 1f\n\t" CPF_A "s_bitcmp1_b32 s98, 8\n\ts_cbranch_scc1 2f\n\t"
               "s_andn2_b32 s84, s2, 31\n\ts_lshl2_add_u32 s84, s85, s84\n\ts_bfe_u32 s85, s2, 0x20003\n\ts_or_b32 s84, s84, s85\n\t"
               "s_mulk_i32 s84, 0x2400\n\ts_mulk_i32 s98, 36\n\ts_add_u32 s84, s84, s98\n\tv_and_b32 v110, 63, v0\n\tv_and_b32 v111, 32, v0\n\t"
               "v_lshlrev_b32 v111, 5, v111\n\tv_lshl_add_u32 v111, v110, 2, v111\n\tv_lshl_add_u32 v110, v110, 4, s84\n\tv_add_u32 v111, s84, v111\n\t"
               "s_cmpk_lt_u32 s2, 0x90\n\ts_waitcnt lgkmcnt(0)\n\t"
               "global_load_dwordx4 v[100:103], v110, s[64:65]\n\tglobal_load_dwordx4 v[104:107], v110, s[64:65] offset:1152\n\t"
               "global_load_dword v108, v111, s[64:65] offset:1024\n\tglobal_load_dword v109, v109, s[82:83] sc1\n\t"
               "s_cbranch_scc1 kf_n\n\ts_branch kf_h\n"
               "1:\n\ts_bfe_u32 s84, s2, 0x50003\n\ts_lshl_b32 s85, s85, 5\n\ts_or_b32 s84, s84, s85\n\ts_lshl_b32 s84, s84, 2\n\t"
               "v_and_b32 v110, 0xff, v0\n\tv_bfe_u32 v107, v0, 7, 1\n"
               "4:\n\ts_lshl_b32 s85, s84, 7\n\tv_lshl_add_u32 v110, v110, 1, s85\n\tv_lshl_add_u32 v107, v107, 1, s84\n\ts_and_b32 s85, s98, s2\n\ts_waitcnt lgkmcnt(0)\n\t"
               "global_load_ushort v100, v107, s[78:79]\n\tglobal_load_ushort v101, v110, s[66:67]\n\tglobal_load_ushort v102, v110, s[68:69]\n\t"
               "global_load_dword v109, v109, s[82:83] sc1\n\ts_bitcmp1_b32 s85, 8\n\ts_cbranch_scc1 kf_u\n\t"
               "global_load_ushort v103, v110, s[70:71]\n\tglobal_load_ushort v104, v110, s[72:73]\n\tglobal_load_ushort v105, v110, s[74:75]\n\t"
               "global_load_ushort v106, v110, s[76:77]\n\tglobal_load_ushort v107, v107, s[80:81]\n\ts_bitcmp1_b32 s2, 8\n\ts_cbranch_scc1 kf_c\n"
               "6:\n\ts_barrier\n\ts_branch kf_p\n"
               "2:\n\t" CPF_V "s_cmpk_lt_u32 s2, 0xc0\n\ts_cbranch_scc1 6b\n\t"
               "s_lshr_b32 s84, s2, 3\n\ts_lshl3_add_u32 s84, s85, s84\n\ts_lshl1_add_u32 s84, s84, 976\n\t"
               "v_and_b32 v110, 0x7f, v0\n\tv_mov_b32 v107, 0\n\ts_branch 4b"
               : : "s"(__builtin_amdgcn_kernarg_segment_ptr()), "s"(blockIdx.x), "v"(threadIdx.x),
                   "s"((uint32_t)(uintptr_t)(__attribute__((address_space(3))) char*)smem) : "scc", "s96", "s97", "s98", "s99", "v127");
}
extern "C" __global__ __launch_bounds__(512, 4) void kf_c(KARGS) {
  __shared__ __attribute__((aligned(16))) char smem[SMEM];
  role_body<0>(smem, KPASS);
}
extern "C" __global__ __launch_bounds__(512, 4) void kf_u(KARGS) {
  __shared__ __attribute__((aligned(16))) char smem[SMEM];
  role_body<3>(smem, KPASS);
}
extern "C" __global__ __launch_bounds__(512, 4) void kf_n(KARGS) {
  __shared__ __attribute__((aligned(16))) char smem[SMEM];
  role_body<1, 1>(smem, KPASS);
}
extern "C" __global__ __launch_bounds__(512, 4) void kf_h(KARGS) {
  __shared__ __attribute__((aligned(16))) char smem[SMEM];
  role_body<1, 0>(smem, KPASS);
}
extern "C" __global__ __launch_bounds__(512, 4) void kf_p(KARGS) {
  __shared__ __attribute__((aligned(16))) char smem[SMEM];
  role_body<2>(smem, KPASS);
}
extern "C" __global__ __launch_bounds__(512, 4) void kf_conv() { asm volatile(CONV_ASM ::: CONV_CLOB); }
extern "C" __global__ void kf_end() {}

extern "C" void kernel_launch(void* const* d_in, const int* in_sizes, int n_in,
                              void* d_out, int out_size, void* d_ws, size_t ws_size,
                              hipStream_t stream) {
  kf_all<<<512, 512, 0, stream>>>((const u16*)d_in[0], (const u16*)d_in[1], (const u16*)d_in[2], (const u16*)d_in[3], (const u16*)d_in[4],
                                  (const u16*)d_in[5], (const u16*)d_in[6], (const u16*)d_in[7], (const u16*)d_in[8], (char*)d_ws + (size_t)WSOFF * 1048576,
                                  (const u16*)d_in[10], (const u16*)d_in[11], (const u16*)d_in[12], (const u16*)d_in[13], (u16*)d_out);
}
```

```cpp
#include <hip/hip_runtime.h>
#include <hip/hip_bf16.h>
#include <stdint.h>
#define HA 2
#define RSL 10

typedef __attribute__((ext_vector_type(8))) short bf16x8;
typedef __attribute__((ext_vector_type(4))) float f32x4;
typedef uint16_t u16;
typedef __attribute__((ext_vector_type(4))) uint32_t u32x4;
typedef __attribute__((ext_vector_type(2))) uint32_t u32x2;
#define TSD uint32_t pr0_ = 0u, pr1_ = 0u
__device__ unsigned long long g_prb[4096];
#define TS(k, i) do { if ((i) == 0 && ROLE == 1) asm volatile("s_waitcnt lgkmcnt(0)\n\ts_or_b32 %0, s92, 1\n\ts_or_b32 %1, s92, 1" : "=s"(pr0_), "=s"(pr1_) : : "scc"); } while (0)
#define TSC(i)
#define TSE() do { if (__builtin_amdgcn_mbcnt_hi(-1, __builtin_amdgcn_mbcnt_lo(-1, 0)) == 0) \
    __hip_atomic_store(&g_prb[blockIdx.x * 8 + w], ((unsigned long long)pr1_ << 32) | pr0_, __ATOMIC_RELAXED, __HIP_MEMORY_SCOPE_AGENT); } while (0)
#define TSP
#define TSA

constexpr int L_ = 2048, D_ = 576, N_ = 128, F_ = 4608, C_ = 8;
constexpr int NF = 144;
constexpr uint32_t O_EP = 0;
constexpr uint32_t O_KF = 4096;
constexpr uint32_t O_KEY = O_KF + 8192;
constexpr uint32_t O_SV = O_KEY + 32768;
constexpr uint32_t O_SU = O_SV + 8 * 320 * 128;
constexpr uint32_t O_FB = O_SU + 262144;
constexpr uint32_t O_XC = O_FB + 8 * 1152;
constexpr uint32_t SP = 9216 + 128;
constexpr uint32_t O_SL = O_XC + 1024 * 1152;
constexpr uint32_t SLU = 336;
constexpr uint32_t O_U = O_SL + 256 * SLU * 16;
constexpr int UP = 584;
constexpr uint32_t VP = 8192 + 128, VPP = 4096 + 128;
constexpr uint32_t O_V = O_U + 256 * UP * 16, O_VP = O_V + 256 * VP;
static_assert(256 * SLU * 16 >= NF * SP && O_SL % 128 == 0 && O_U % 128 == 0 && O_V % 128 == 0, "");
__host__ __device__ __forceinline__ uint32_t u_at(int tg, int d) { return O_U + 16u * (uint32_t)((tg >> 1) * (UP * 2) + (d >> 2) * 8 + (tg & 1) * 4 + (d & 3)); }
__host__ __device__ __forceinline__ uint32_t v_at(int d, int tg) {
  return (d < 512) ? O_V + (uint32_t)(d >> 1) * VP + 16u * (uint32_t)(2 * tg + (d & 1)) : O_VP + (uint32_t)(d - 512) * VPP + 16u * (uint32_t)tg;
}
#define HAVE_VAT
__device__ __forceinline__ float bf2f(u16 v) { return __uint_as_float((uint32_t)v << 16); }
__device__ __forceinline__ float bflo(uint32_t v) { return __uint_as_float(v << 16); }
__device__ __forceinline__ float bfhi(uint32_t v) { return __uint_as_float(v & 0xffff0000u); }
__device__ __forceinline__ u16 f2bf(float f) { return __builtin_bit_cast(u16, (__bf16)f); }
__device__ __forceinline__ float rbf(float f) { return bf2f(f2bf(f)); }
typedef __attribute__((ext_vector_type(2))) float f32x2;
typedef __attribute__((ext_vector_type(2))) __bf16 bf16x2v;
__device__ __forceinline__ uint32_t pack2(float a, float b) {
  return __builtin_bit_cast(uint32_t, __builtin_convertvector((f32x2){a, b}, bf16x2v));
}

#define DPP_I(v, ctrl, rmask) __builtin_amdgcn_update_dpp(0, (v), (ctrl), (rmask), 0xf, true)
__device__ __forceinline__ float row_sum(float v) {
  v += __int_as_float(DPP_I(__float_as_int(v), 0x111, 0xf));
  v += __int_as_float(DPP_I(__float_as_int(v), 0x112, 0xf));
  v += __int_as_float(DPP_I(__float_as_int(v), 0x114, 0xf));
  v += __int_as_float(DPP_I(__float_as_int(v), 0x118, 0xf));
  return v;
}
__device__ __forceinline__ float rdl(float v, int l) { return __int_as_float(__builtin_amdgcn_readlane(__float_as_int(v), l)); }
__device__ __forceinline__ float wave_sum(float v) {
  v = row_sum(v);
  asm("s_nop 1\n\tv_add_f32_dpp %0, %0, %0 row_bcast:15 row_mask:0xa bank_mask:0xf\n\t"
      "s_nop 1\n\tv_add_f32_dpp %0, %0, %0 row_bcast:31 row_mask:0xc bank_mask:0xf\n\ts_nop 0" : "+v"(v));
  return rdl(v, 63);
}
__device__ __forceinline__ float fold32(float a, float b) {
  const auto r = __builtin_amdgcn_permlane32_swap(__float_as_uint(a), __float_as_uint(b), false, false);
  return __uint_as_float(r[0]) + __uint_as_float(r[1]);
}
__device__ __forceinline__ float fold16(float a, float b) {
  const auto r = __builtin_amdgcn_permlane16_swap(__float_as_uint(a), __float_as_uint(b), false, false);
  return __uint_as_float(r[0]) + __uint_as_float(r[1]);
}
__device__ __forceinline__ void wave_sum2(float& a, float& b) {
  float v = row_sum(fold32(a, b));
  asm("s_nop 1\n\tv_add_f32_dpp %0, %0, %0 row_bcast:15 row_mask:0xa bank_mask:0xf\n\ts_nop 0" : "+v"(v));
  a = rdl(v, 31); b = rdl(v, 63);
}
__device__ __forceinline__ void wave_sum4(float& a, float& b, float& c, float& d) {
  const float v = row_sum(fold16(fold32(a, b), fold32(c, d)));
  a = rdl(v, 15); c = rdl(v, 31); b = rdl(v, 47); d = rdl(v, 63);
}
__device__ __forceinline__ float dot2(uint32_t a, uint32_t b, float c) {
  return __builtin_amdgcn_fdot2_f32_bf16(__builtin_bit_cast(bf16x2v, a), __builtin_bit_cast(bf16x2v, b), c, false);
}
__device__ __forceinline__ uint32_t row_maxu(uint32_t v) {
  v = max(v, (uint32_t)DPP_I((int)v, 0xb1, 0xf));
  v = max(v, (uint32_t)DPP_I((int)v, 0x4e, 0xf));
  v = max(v, (uint32_t)DPP_I((int)v, 0x141, 0xf));
  v = max(v, (uint32_t)DPP_I((int)v, 0x140, 0xf));
  return v;
}
__device__ __forceinline__ uint32_t wave_maxu(uint32_t v) {
  v = max(v, (uint32_t)DPP_I((int)v, 0x111, 0xf));
  v = max(v, (uint32_t)DPP_I((int)v, 0x112, 0xf));
  v = max(v, (uint32_t)DPP_I((int)v, 0x114, 0xf));
  v = max(v, (uint32_t)DPP_I((int)v, 0x118, 0xf));
  v = max(v, (uint32_t)DPP_I((int)v, 0x142, 0xa));
  v = max(v, (uint32_t)DPP_I((int)v, 0x143, 0xc));
  return (uint32_t)__builtin_amdgcn_readlane((int)v, 63);
}
__device__ __forceinline__ void wave_lds_sync() {
  __builtin_amdgcn_s_waitcnt(0xc07f);
  __builtin_amdgcn_wave_barrier();
}

typedef __attribute__((ext_vector_type(8))) _Float16 f16x8;
typedef __attribute__((ext_vector_type(2))) _Float16 f16x2;
__device__ __forceinline__ uint32_t packh(f32x2 v) { return __builtin_bit_cast(uint32_t, __builtin_convertvector(v, f16x2)); }
__device__ __forceinline__ uint32_t packh(float a, float b) { return packh((f32x2){a, b}); }
constexpr int IP = 224, I_X = 128;
__device__ __forceinline__ f32x2 cmul(f32x2 z, f32x2 w) {
  f32x2 r;
  asm("v_pk_mul_f32 %0, %1, %2 op_sel_hi:[0,1]\n\tv_pk_fma_f32 %0, %1, %2, %0 op_sel:[1,1,0] op_sel_hi:[1,0,1] neg_lo:[0,1,0]" : "=&v"(r) : "v"(z), "v"(w));
  return r;
}
__device__ __forceinline__ f32x2 cexp2(float ar2, float air, float l) {
  const float t = air * l;
  return (f32x2){__builtin_amdgcn_cosf(t), __builtin_amdgcn_sinf(t)} * __builtin_amdgcn_exp2f(ar2 * l);
}
__device__ __forceinline__ float state_a(float ldt, float are, float aim, float& ar2, float& air, float& g) {
  const float dt = rbf(__expf(ldt));
  ar2 = dt * are * 1.4426950408889634f;
  air = dt * aim * 0.15915494309189535f;
  air -= __builtin_rintf(air);
  g = 0.f;
  if (__builtin_expect(__any(ar2 > 0.f), 0)) g = fminf(__uint_as_float(wave_maxu(__float_as_uint(fmaxf(ar2, 0.f)))), 1.f);
  return dt;
}
template <int N> __device__ __forceinline__ void put_im(char* p, int im, const f32x2* z) {
  *(u32x4*)p = (u32x4){packh(z[0][0], z[1][0]), packh(z[2][0], z[3][0]), packh(z[4][0], z[5][0]), packh(z[6][0], z[7][0])};
  *(u32x4*)(p + im) = (u32x4){packh(z[0][1], z[1][1]), packh(z[2][1], z[3][1]), packh(z[4][1], z[5][1]), packh(z[6][1], z[7][1])};
}
#define Y_ASM \
  "v_mov_b64 v[40:41], %0\nv_pk_mul_f32 v[42:43], v[40:41], %1 op_sel_hi:[0,1]\nv_pk_mul_f32 v[44:45], %0, %2 op_sel_hi:[0,1]\n" \
  "v_pk_fma_f32 v[42:43], v[40:41], %1, v[42:43] op_sel:[1,1,0] op_sel_hi:[1,0,1] neg_lo:[0,1,0]\n" \
  "v_pk_fma_f32 v[44:45], %0, %2, v[44:45] op_sel:[1,1,0] op_sel_hi:[1,0,1] neg_lo:[0,1,0]\nv_cvt_pk_f16_f32 v56, v40, v42\n" \
  "v_cvt_pk_f16_f32 v60, v41, v43\nv_pk_mul_f32 v[40:41], v[42:43], %1 op_sel_hi:[0,1]\nv_pk_mul_f32 v[48:49], v[44:45], %2 op_sel_hi:[0,1]\n" \
  "v_pk_fma_f32 v[40:41], v[42:43], %1, v[40:41] op_sel:[1,1,0] op_sel_hi:[1,0,1] neg_lo:[0,1,0]\n" \
  "v_pk_fma_f32 v[48:49], v[44:45], %2, v[48:49] op_sel:[1,1,0] op_sel_hi:[1,0,1] neg_lo:[0,1,0]\n" \
  "v_pk_mul_f32 v[42:43], v[40:41], %1 op_sel_hi:[0,1]\nv_pk_mul_f32 v[52:53], v[48:49], %2 op_sel_hi:[0,1]\n" \
  "v_pk_fma_f32 v[42:43], v[40:41], %1, v[42:43] op_sel:[1,1,0] op_sel_hi:[1,0,1] neg_lo:[0,1,0]\n" \
  "v_pk_fma_f32 v[52:53], v[48:49], %2, v[52:53] op_sel:[1,1,0] op_sel_hi:[1,0,1] neg_lo:[0,1,0]\nv_cvt_pk_f16_f32 v57, v40, v42\n" \
  "v_cvt_pk_f16_f32 v61, v41, v43\nv_pk_mul_f32 v[40:41], v[42:43], %1 op_sel_hi:[0,1]\n" \
  "v_pk_fma_f32 v[40:41], v[42:43], %1, v[40:41] op_sel:[1,1,0] op_sel_hi:[1,0,1] neg_lo:[0,1,0]\n" \
  "v_pk_mul_f32 v[42:43], v[40:41], %1 op_sel_hi:[0,1]\nv_pk_mul_f32 v[46:47], v[44:45], %1 op_sel_hi:[0,1]\n" \
  "v_pk_fma_f32 v[42:43], v[40:41], %1, v[42:43] op_sel:[1,1,0] op_sel_hi:[1,0,1] neg_lo:[0,1,0]\n" \
  "v_pk_fma_f32 v[46:47], v[44:45], %1, v[46:47] op_sel:[1,1,0] op_sel_hi:[1,0,1] neg_lo:[0,1,0]\nv_cvt_pk_f16_f32 v58, v40, v42\n" \
  "v_cvt_pk_f16_f32 v62, v41, v43\nv_cvt_pk_f16_f32 v64, v44, v46\nv_cvt_pk_f16_f32 v68, v45, v47\n" \
  "v_pk_mul_f32 v[40:41], v[42:43], %1 op_sel_hi:[0,1]\nv_pk_mul_f32 v[44:45], v[46:47], %1 op_sel_hi:[0,1]\n" \
  "v_pk_fma_f32 v[40:41], v[42:43], %1, v[40:41] op_sel:[1,1,0] op_sel_hi:[1,0,1] neg_lo:[0,1,0]\n" \
  "v_pk_fma_f32 v[44:45], v[46:47], %1, v[44:45] op_sel:[1,1,0] op_sel_hi:[1,0,1] neg_lo:[0,1,0]\n" \
  "v_pk_mul_f32 v[42:43], v[40:41], %1 op_sel_hi:[0,1]\nv_pk_mul_f32 v[46:47], v[44:45], %1 op_sel_hi:[0,1]\n" \
  "v_pk_fma_f32 v[42:43], v[40:41], %1, v[42:43] op_sel:[1,1,0] op_sel_hi:[1,0,1] neg_lo:[0,1,0]\n" \
  "v_pk_fma_f32 v[46:47], v[44:45], %1, v[46:47] op_sel:[1,1,0] op_sel_hi:[1,0,1] neg_lo:[0,1,0]\nv_cvt_pk_f16_f32 v59, v40, v42\n" \
  "v_cvt_pk_f16_f32 v63, v41, v43\nv_cvt_pk_f16_f32 v65, v44, v46\nv_cvt_pk_f16_f32 v69, v45, v47\n" \
  "v_pk_mul_f32 v[44:45], v[46:47], %1 op_sel_hi:[0,1]\nds_write_b128 %3, v[56:59] offset:0\n" \
  "v_pk_fma_f32 v[44:45], v[46:47], %1, v[44:45] op_sel:[1,1,0] op_sel_hi:[1,0,1] neg_lo:[0,1,0]\nds_write_b128 %3, v[60:63] offset:64\n" \
  "v_pk_mul_f32 v[46:47], v[44:45], %1 op_sel_hi:[0,1]\nv_pk_mul_f32 v[50:51], v[48:49], %1 op_sel_hi:[0,1]\n" \
  "v_pk_fma_f32 v[46:47], v[44:45], %1, v[46:47] op_sel:[1,1,0] op_sel_hi:[1,0,1] neg_lo:[0,1,0]\n" \
  "v_pk_fma_f32 v[50:51], v[48:49], %1, v[50:51] op_sel:[1,1,0] op_sel_hi:[1,0,1] neg_lo:[0,1,0]\nv_cvt_pk_f16_f32 v66, v44, v46\n" \
  "v_cvt_pk_f16_f32 v70, v45, v47\nv_cvt_pk_f16_f32 v56, v48, v50\nv_cvt_pk_f16_f32 v60, v49, v51\n" \
  "v_pk_mul_f32 v[44:45], v[46:47], %1 op_sel_hi:[0,1]\nv_pk_mul_f32 v[48:49], v[50:51], %1 op_sel_hi:[0,1]\n" \
  "v_pk_fma_f32 v[44:45], v[46:47], %1, v[44:45] op_sel:[1,1,0] op_sel_hi:[1,0,1] neg_lo:[0,1,0]\n" \
  "v_pk_fma_f32 v[48:49], v[50:51], %1, v[48:49] op_sel:[1,1,0] op_sel_hi:[1,0,1] neg_lo:[0,1,0]\n" \
  "v_pk_mul_f32 v[46:47], v[44:45], %1 op_sel_hi:[0,1]\nv_pk_mul_f32 v[50:51], v[48:49], %1 op_sel_hi:[0,1]\n" \
  "v_pk_fma_f32 v[46:47], v[44:45], %1, v[46:47] op_sel:[1,1,0] op_sel_hi:[1,0,1] neg_lo:[0,1,0]\n" \
  "v_pk_fma_f32 v[50:51], v[48:49], %1, v[50:51] op_sel:[1,1,0] op_sel_hi:[1,0,1] neg_lo:[0,1,0]\nv_cvt_pk_f16_f32 v67, v44, v46\n" \
  "v_cvt_pk_f16_f32 v71, v45, v47\nv_cvt_pk_f16_f32 v57, v48, v50\nv_cvt_pk_f16_f32 v61, v49, v51\n" \
  "v_pk_mul_f32 v[48:49], v[50:51], %1 op_sel_hi:[0,1]\nds_write_b128 %3, v[64:67] offset:16\n" \
  "v_pk_fma_f32 v[48:49], v[50:51], %1, v[48:49] op_sel:[1,1,0] op_sel_hi:[1,0,1] neg_lo:[0,1,0]\nds_write_b128 %3, v[68:71] offset:80\n" \
  "v_pk_mul_f32 v[50:51], v[48:49], %1 op_sel_hi:[0,1]\nv_pk_mul_f32 v[54:55], v[52:53], %1 op_sel_hi:[0,1]\n" \
  "v_pk_fma_f32 v[50:51], v[48:49], %1, v[50:51] op_sel:[1,1,0] op_sel_hi:[1,0,1] neg_lo:[0,1,0]\n" \
  "v_pk_fma_f32 v[54:55], v[52:53], %1, v[54:55] op_sel:[1,1,0] op_sel_hi:[1,0,1] neg_lo:[0,1,0]\nv_cvt_pk_f16_f32 v58, v48, v50\n" \
  "v_cvt_pk_f16_f32 v62, v49, v51\nv_cvt_pk_f16_f32 v64, v52, v54\nv_cvt_pk_f16_f32 v68, v53, v55\n" \
  "v_pk_mul_f32 v[48:49], v[50:51], %1 op_sel_hi:[0,1]\nv_pk_mul_f32 v[52:53], v[54:55], %1 op_sel_hi:[0,1]\n" \
  "v_pk_fma_f32 v[48:49], v[50:51], %1, v[48:49] op_sel:[1,1,0] op_sel_hi:[1,0,1] neg_lo:[0,1,0]\n" \
  "v_pk_fma_f32 v[52:53], v[54:55], %1, v[52:53] op_sel:[1,1,0] op_sel_hi:[1,0,1] neg_lo:[0,1,0]\n" \
  "v_pk_mul_f32 v[50:51], v[48:49], %1 op_sel_hi:[0,1]\nv_pk_mul_f32 v[54:55], v[52:53], %1 op_sel_hi:[0,1]\n" \
  "v_pk_fma_f32 v[50:51], v[48:49], %1, v[50:51] op_sel:[1,1,0] op_sel_hi:[1,0,1] neg_lo:[0,1,0]\n" \
  "v_pk_fma_f32 v[54:55], v[52:53], %1, v[54:55] op_sel:[1,1,0] op_sel_hi:[1,0,1] neg_lo:[0,1,0]\nv_cvt_pk_f16_f32 v59, v48, v50\n" \
  "v_cvt_pk_f16_f32 v63, v49, v51\nv_cvt_pk_f16_f32 v65, v52, v54\nv_cvt_pk_f16_f32 v69, v53, v55\n" \
  "v_pk_mul_f32 v[52:53], v[54:55], %1 op_sel_hi:[0,1]\nds_write_b128 %3, v[56:59] offset:32\n" \
  "v_pk_fma_f32 v[52:53], v[54:55], %1, v[52:53] op_sel:[1,1,0] op_sel_hi:[1,0,1] neg_lo:[0,1,0]\nds_write_b128 %3, v[60:63] offset:96\n" \
  "v_pk_mul_f32 v[54:55], v[52:53], %1 op_sel_hi:[0,1]\n" \
  "v_pk_fma_f32 v[54:55], v[52:53], %1, v[54:55] op_sel:[1,1,0] op_sel_hi:[1,0,1] neg_lo:[0,1,0]\nv_cvt_pk_f16_f32 v66, v52, v54\n" \
  "v_cvt_pk_f16_f32 v70, v53, v55\nv_pk_mul_f32 v[52:53], v[54:55], %1 op_sel_hi:[0,1]\n" \
  "v_pk_fma_f32 v[52:53], v[54:55], %1, v[52:53] op_sel:[1,1,0] op_sel_hi:[1,0,1] neg_lo:[0,1,0]\n" \
  "v_pk_mul_f32 v[54:55], v[52:53], %1 op_sel_hi:[0,1]\n" \
  "v_pk_fma_f32 v[54:55], v[52:53], %1, v[54:55] op_sel:[1,1,0] op_sel_hi:[1,0,1] neg_lo:[0,1,0]\nv_cvt_pk_f16_f32 v67, v52, v54\n" \
  "v_cvt_pk_f16_f32 v71, v53, v55\nds_write_b128 %3, v[64:67] offset:48\nds_write_b128 %3, v[68:71] offset:112\n"
#define Y_CLOB "memory", "v40", "v41", "v42", "v43", "v44", "v45", "v46", "v47", "v48", "v49", "v50", "v51", "v52", "v53", "v54", "v55", "v56", "v57", "v58", "v59", "v60", "v61", "v62", "v63", "v64", "v65", "v66", "v67", "v68", "v69", "v70", "v71"
#define T_ASM \
  "ds_read_b128 %4, %8\nds_read_b64_tr_b16 v[80:81], %7 offset:0\nds_read_b64_tr_b16 v[82:83], %7 offset:64\n" \
  "ds_read_b64_tr_b16 v[84:85], %5 offset:0\nds_read_b64_tr_b16 v[86:87], %5 offset:48\nds_read_b64_tr_b16 v[88:89], %6 offset:0\n" \
  "ds_read_b64_tr_b16 v[90:91], %6 offset:48\nds_read_b64_tr_b16 v[92:93], %7 offset:3584\nds_read_b64_tr_b16 v[94:95], %7 offset:3648\n" \
  "ds_read_b64_tr_b16 v[96:97], %5 offset:3584\nds_read_b64_tr_b16 v[98:99], %5 offset:3632\nds_read_b64_tr_b16 v[100:101], %6 offset:3584\n" \
  "ds_read_b64_tr_b16 v[102:103], %6 offset:3632\nds_read_b64_tr_b16 v[104:105], %7 offset:7168\nds_read_b64_tr_b16 v[106:107], %7 offset:7232\n" \
  "s_waitcnt lgkmcnt(8)\nv_mfma_f32_16x16x32_f16 %0, v[84:87], v[80:83], 0\nds_read_b64_tr_b16 v[108:109], %5 offset:7168\n" \
  "ds_read_b64_tr_b16 v[110:111], %5 offset:7216\nds_read_b64_tr_b16 v[112:113], %6 offset:7168\nv_mfma_f32_16x16x32_f16 %2, v[88:91], v[80:83], 0\n" \
  "ds_read_b64_tr_b16 v[114:115], %6 offset:7216\nds_read_b64_tr_b16 v[116:117], %7 offset:10752\nds_read_b64_tr_b16 v[118:119], %7 offset:10816\n" \
  "s_waitcnt lgkmcnt(8)\nv_mfma_f32_16x16x32_f16 %0, v[96:99], v[92:95], %0\nds_read_b64_tr_b16 v[120:121], %5 offset:10752\n" \
  "ds_read_b64_tr_b16 v[122:123], %5 offset:10800\nds_read_b64_tr_b16 v[124:125], %6 offset:10752\n" \
  "v_mfma_f32_16x16x32_f16 %2, v[100:103], v[92:95], %2\nds_read_b64_tr_b16 v[126:127], %6 offset:10800\n" \
  "ds_read_b64_tr_b16 v[80:81], %7 offset:14336\nds_read_b64_tr_b16 v[82:83], %7 offset:14400\ns_waitcnt lgkmcnt(8)\n" \
  "v_mfma_f32_16x16x32_f16 %0, v[108:111], v[104:107], %0\nds_read_b64_tr_b16 v[84:85], %5 offset:14336\n" \
  "ds_read_b64_tr_b16 v[86:87], %5 offset:14384\nds_read_b64_tr_b16 v[88:89], %6 offset:14336\n" \
  "v_mfma_f32_16x16x32_f16 %2, v[112:115], v[104:107], %2\nds_read_b64_tr_b16 v[90:91], %6 offset:14384\n" \
  "ds_read_b64_tr_b16 v[92:93], %7 offset:17920\nds_read_b64_tr_b16 v[94:95], %7 offset:17984\ns_waitcnt lgkmcnt(8)\n" \
  "v_mfma_f32_16x16x32_f16 %0, v[120:123], v[116:119], %0\nds_read_b64_tr_b16 v[96:97], %5 offset:17920\n" \
  "ds_read_b64_tr_b16 v[98:99], %5 offset:17968\nds_read_b64_tr_b16 v[100:101], %6 offset:17920\n" \
  "v_mfma_f32_16x16x32_f16 %2, v[124:127], v[116:119], %2\nds_read_b64_tr_b16 v[102:103], %6 offset:17968\n" \
  "ds_read_b64_tr_b16 v[104:105], %7 offset:21504\nds_read_b64_tr_b16 v[106:107], %7 offset:21568\ns_waitcnt lgkmcnt(8)\n" \
  "v_mfma_f32_16x16x32_f16 %1, v[84:87], v[80:83], 0\nds_read_b64_tr_b16 v[108:109], %5 offset:21504\nds_read_b64_tr_b16 v[110:111], %5 offset:21552\n" \
  "ds_read_b64_tr_b16 v[112:113], %6 offset:21504\nv_mfma_f32_16x16x32_f16 %3, v[88:91], v[80:83], 0\nds_read_b64_tr_b16 v[114:115], %6 offset:21552\n" \
  "ds_read_b64_tr_b16 v[116:117], %7 offset:25088\nds_read_b64_tr_b16 v[118:119], %7 offset:25152\ns_waitcnt lgkmcnt(8)\n" \
  "v_mfma_f32_16x16x32_f16 %1, v[96:99], v[92:95], %1\nds_read_b64_tr_b16 v[120:121], %5 offset:25088\n" \
  "ds_read_b64_tr_b16 v[122:123], %5 offset:25136\nds_read_b64_tr_b16 v[124:125], %6 offset:25088\n" \
  "v_mfma_f32_16x16x32_f16 %3, v[100:103], v[92:95], %3\nds_read_b64_tr_b16 v[126:127], %6 offset:25136\ns_waitcnt lgkmcnt(6)\n" \
  "v_mfma_f32_16x16x32_f16 %1, v[108:111], v[104:107], %1\nv_mfma_f32_16x16x32_f16 %3, v[112:115], v[104:107], %3\ns_waitcnt lgkmcnt(0)\n" \
  "v_mfma_f32_16x16x32_f16 %1, v[120:123], v[116:119], %1\nv_mfma_f32_16x16x32_f16 %3, v[124:127], v[116:119], %3\n"
#define T_CLOB "memory", "v80", "v81", "v82", "v83", "v84", "v85", "v86", "v87", "v88", "v89", "v90", "v91", "v92", "v93", "v94", "v95", "v96", "v97", "v98", "v99", "v100", "v101", "v102", "v103", "v104", "v105", "v106", "v107", "v108", "v109", "v110", "v111", "v112", "v113", "v114", "v115", "v116", "v117", "v118", "v119", "v120", "v121", "v122", "v123", "v124", "v125", "v126", "v127"
template <class F> __device__ __forceinline__ int gen_y(float ldt, float are, float aim, float bre, float bim, float cre, float cim, uint32_t im, F&& mid TSP) {
  float ar2, air, g;
  const float dt = state_a(ldt, are, aim, ar2, air, g);
  const float dbr = dt * bre, dbi = dt * bim, pr = cre * dbr - cim * dbi, pi = cre * dbi + cim * dbr;
  const f32x2 w8 = cexp2(ar2, -air, 8.f), w64 = cexp2(ar2, -air, 64.f);
  const int e = (int)(wave_maxu(__float_as_uint(fmaxf(fabsf(pr), fabsf(pi)))) >> 23) - 126 + (int)ceilf(g * 250.f);
  const f32x2 y0 = {__builtin_amdgcn_ldexpf(pr, 14 - e), __builtin_amdgcn_ldexpf(-pi, 14 - e)};
  mid();
  asm volatile(Y_ASM : : "v"(y0), "v"(w8), "v"(w64), "v"(im) : Y_CLOB);
  return e;
}
__device__ __forceinline__ int gen_x(float ldt, float are, float aim, char* im) {
  float ar2, air, g;
  state_a(ldt, are, aim, ar2, air, g);
  const int e = min((int)ceilf(g * 1810.f), 300);
  const float s = __builtin_amdgcn_ldexpf(1.f, 15 - e);
  const f32x2 w1 = cexp2(ar2, air, 1.f), w256 = cexp2(ar2, air, 256.f), w2 = cmul(w1, w1);
  f32x2 a[8], b[8], c[8];
  a[0] = (f32x2){s, 0.f}; a[1] = w1 * s; a[2] = w2 * s;
  b[0] = b[1] = w256 * s;
  b[2] = cmul(b[0], w2); a[3] = cmul(a[1], w2); a[4] = cmul(a[2], w2);
  b[3] = b[4] = b[5] = cmul(b[2], w2); a[5] = cmul(a[3], w2); a[6] = cmul(a[4], w2);
  b[6] = cmul(b[3], w2); a[7] = cmul(a[5], w2); c[0] = cmul(b[3], w256);
  b[7] = cmul(b[6], w2); c[2] = cmul(c[0], w256);
  put_im<0>(im + I_X, 48, a);
  c[1] = cmul(b[7], w256); c[3] = cmul(c[1], w256);
  put_im<0>(im + I_X + 16, 48, b);
#pragma unroll
  for (int k = 4; k < 8; ++k) c[k] = cmul(c[k - 1], w256);
  put_im<0>(im + I_X + 32, 48, c);
  return e;
}

template <int I> struct IC { static constexpr int value = I; };
template <int B, int E, class F> __device__ __forceinline__ void static_for(F&& f) {
  if constexpr (B < E) { f(IC<B>{}); static_for<B + 1, E>(f); }
}
typedef __attribute__((ext_vector_type(4))) short s16x4;
__device__ __forceinline__ u32x2 trd(uint32_t a) {
  return __builtin_bit_cast(u32x2, __builtin_amdgcn_ds_read_tr16_b64_v4i16((__attribute__((address_space(3))) s16x4*)(uintptr_t)a));
}
#define DPP_F(v, ctrl) __int_as_float(DPP_I(__float_as_int(v), (ctrl), 0xf))
__device__ __forceinline__ float s_add(float a, float b) { float r; asm("v_add_f32 %0, %1, %2" : "=v"(r) : "v"(a), "v"(b)); return r; }
__device__ __forceinline__ float s_fma(float a, float b, float c) { float r; asm("v_fma_f32 %0, %1, %2, %3" : "=v"(r) : "v"(a), "v"(b), "v"(c)); return r; }
#define BAR() asm volatile("s_waitcnt lgkmcnt(0)\n\ts_barrier" ::: "memory")
#define ST16(off, v) asm volatile("global_store_dwordx4 %0, %1, %2 sc1\n\ts_nop 1" : : "v"(off), "v"(v), "s"(ws) : "memory")
#define ST4(off, v) asm volatile("global_store_dword %0, %1, %2 sc1" : : "v"(off), "v"(v), "s"(ws) : "memory")
#define LD16(r, off) asm volatile("global_load_dwordx4 %0, %1, %2 sc1" : "=v"(r) : "v"(off), "s"(ws) : "memory")
#define LD16W(r, off) asm volatile("global_load_dwordx4 %0, %1, %2 sc1\n\ts_waitcnt vmcnt(0)" : "=&v"(r) : "v"(off), "s"(ws) : "memory")
#define DRAIN() asm volatile("s_waitcnt vmcnt(0)" ::: "memory")
#define CEX(a, b) { const uint32_t hi_ = max(a, b), lo_ = min(a, b); a = hi_; b = lo_; }
#define MMB(a_, b_, c_) __builtin_amdgcn_mfma_f32_16x16x32_bf16(__builtin_bit_cast(bf16x8, a_), __builtin_bit_cast(bf16x8, b_), c_, 0, 0, 0)
#define DPOL ""
#define XPOL ""
#define TPOL ""
constexpr int SPIN_CAP = 1 << 18;
constexpr uint32_t UMASK = 0x40074007u;
__device__ __forceinline__ uint32_t utag(uint32_t T, int j) {
  return ((T >> (3 * j)) & 0x00070007u) | (((j <= 2) ? (T << (2 - j)) : (T >> (j - 2))) & 0x40004000u);
}
__device__ __forceinline__ uint32_t max4(const u32x4& a) { return max(max(a[0], a[1]), max(a[2], a[3])); }
__device__ __forceinline__ uint32_t min4(const u32x4& a) { return min(min(a[0], a[1]), min(a[2], a[3])); }
__device__ __forceinline__ void until(uint32_t t) {
#pragma unroll 1
  for (int spin = 0; spin < 200 && (int)((uint32_t)__builtin_amdgcn_s_memrealtime() - t) < 0; ++spin) __builtin_amdgcn_s_sleep(1);
}
__device__ __forceinline__ uint32_t latest(uint32_t t) {
  const uint32_t t0 = (uint32_t)__builtin_amdgcn_readfirstlane(t) - 0x40000000u;
  return wave_maxu(t - t0) + t0;
}
__device__ __forceinline__ uint32_t gate(const char* ws, uint32_t po, uint32_t T) {
  u32x2 pv;
#pragma unroll 1
  for (int spin = 0; spin < SPIN_CAP; ++spin) {
    asm volatile("global_load_dwordx2 %0, %1, %2 sc1\n\ts_waitcnt vmcnt(0)" : "=&v"(pv) : "v"(po), "s"(ws) : "memory");
    if (!__any(pv[0] != T)) break;
    __builtin_amdgcn_s_sleep(2);
  }
  return pv[1];
}
__device__ __forceinline__ uint32_t gate2(const char* ws, uint32_t oa, uint32_t da, uint32_t ob, uint32_t T) {
  u32x2 pa, pb;
#pragma unroll 1
  for (int spin = 0; spin < SPIN_CAP; ++spin) {
    asm volatile("global_load_dwordx2 %0, %2, %4 sc1\n\tglobal_load_dwordx2 %1, %3, %4 sc1\n\ts_waitcnt vmcnt(0)" : "=&v"(pa), "=&v"(pb) : "v"(oa), "v"(ob), "s"(ws) : "memory");
    if (!__any(pa[0] != T || pb[0] != T)) break;
    __builtin_amdgcn_s_sleep(4);
  }
  pa[1] += da;
  return latest(((int)(pa[1] - pb[1]) > 0) ? pa[1] : pb[1]);
}
__device__ __forceinline__ uint32_t ahead(uint32_t c0, uint32_t t0, int dt) {
  const uint32_t c = (uint32_t)__builtin_amdgcn_s_memtime(), t = (uint32_t)__builtin_amdgcn_s_memrealtime();
  return t + (uint32_t)((float)((t - t0) * (uint32_t)(20 * dt)) * __builtin_amdgcn_rcpf((float)(c - c0)));
}
#define SIGNAL(off, pitch, dt) do { const uint32_t t_ = ahead(ck0, rt0, dt); \
    ST16((off) + (uint32_t)(lane >> 3) * (pitch) + 16u * (lane & 7), ((u32x4){T, t_, T, t_})); } while (0)
__device__ __forceinline__ void gsync(uint32_t a, uint32_t target) {
  uint32_t t, c, n = SPIN_CAP;
  uint64_t ex;
  asm volatile("s_waitcnt lgkmcnt(0)\n\ts_mov_b64 %3, exec\n\ts_mov_b64 exec, 1\n\tv_mov_b32 %0, 1\n\tds_add_u32 %4, %0\n\ts_mov_b64 exec, %3\n"
               "1:\n\ts_sleep 1\n\tds_read_b32 %0, %4\n\ts_sub_u32 %2, %2, 1\n\ts_cbranch_scc1 2f\n\ts_waitcnt lgkmcnt(0)\n\tv_readfirstlane_b32 %1, %0\n\t"
               "s_cmp_lt_u32 %1, %5\n\ts_cbranch_scc1 1b\n2:\n\ts_waitcnt lgkmcnt(0)"
               : "=&v"(t), "=&s"(c), "+s"(n), "=&s"(ex) : "v"(a), "n"(target) : "memory", "scc");
}
__device__ __forceinline__ void gsync0(uint32_t a, uint32_t target) {
  uint32_t t, c, n = SPIN_CAP;
  asm volatile("s_waitcnt lgkmcnt(0)\n\ts_mov_b64 exec, 1\n\tv_mov_b32 %0, 1\n\tds_add_u32 %3, %0\n\ts_mov_b64 exec, -1\n"
               "1:\n\tds_read_b32 %0, %3\n\ts_sub_u32 %2, %2, 1\n\ts_cbranch_scc1 2f\n\ts_waitcnt lgkmcnt(0)\n\tv_readfirstlane_b32 %1, %0\n\t"
               "s_cmp_lt_u32 %1, %4\n\ts_cbranch_scc1 1b\n2:\n\ts_waitcnt lgkmcnt(0)"
               : "=&v"(t), "=&s"(c), "+s"(n) : "v"(a), "n"(target) : "memory", "scc");
}
__device__ __forceinline__ void gwait(uint32_t a, uint32_t target) {
  uint32_t t, c, n = SPIN_CAP;
  asm volatile("1:\n\ts_sleep 2\n\tds_read_b32 %0, %3\n\ts_sub_u32 %2, %2, 1\n\ts_cbranch_scc1 2f\n\ts_waitcnt lgkmcnt(0)\n\tv_readfirstlane_b32 %1, %0\n\t"
               "s_cmp_lt_u32 %1, %4\n\ts_cbranch_scc1 1b\n2:\n\ts_waitcnt lgkmcnt(0)"
               : "=&v"(t), "=&s"(c), "+s"(n) : "v"(a), "n"(target) : "memory", "scc");
}
__device__ __forceinline__ void dma16(const void* base, uint32_t voff, uint32_t lds) {
  uint32_t keep;
  asm volatile("s_mov_b32 %0, m0\n\ts_mov_b32 m0, %3\n\ts_nop 0\n\tglobal_load_lds_dwordx4 %1, %2" DPOL "\n\ts_mov_b32 m0, %0" : "=&s"(keep) : "v"(voff), "s"(base), "s"(lds) : "memory");
}
__device__ __forceinline__ void nap(int n) {
  for (; n > 15; n -= 15) __builtin_amdgcn_s_sleep(15);
  switch (n) {
    case 1: __builtin_amdgcn_s_sleep(1); break; case 2: __builtin_amdgcn_s_sleep(2); break; case 3: __builtin_amdgcn_s_sleep(3); break;
    case 4: __builtin_amdgcn_s_sleep(4); break; case 5: __builtin_amdgcn_s_sleep(5); break; case 6: __builtin_amdgcn_s_sleep(6); break;
    case 7: __builtin_amdgcn_s_sleep(7); break; case 8: __builtin_amdgcn_s_sleep(8); break; case 9: __builtin_amdgcn_s_sleep(9); break;
    case 10: __builtin_amdgcn_s_sleep(10); break; case 11: __builtin_amdgcn_s_sleep(11); break; case 12: __builtin_amdgcn_s_sleep(12); break;
    case 13: __builtin_amdgcn_s_sleep(13); break; case 14: __builtin_amdgcn_s_sleep(14); break; case 15: __builtin_amdgcn_s_sleep(15); break;
    default: break;
  }
}
#define SLEEP(n) do { if ((n) > 0) __builtin_amdgcn_s_sleep(n); } while (0)
#define USLEEP 3
#define UPOS 1
#define QSLEEP 0
#define CSLEEP 4
#define WSLEEP 4
#define TSLEEP 0
#define VSLEEP 45
#define KSLEEP 2
#define SSLEEP 11
#define PSLEEP 2
#define PPRIO 0
#define APRIO 1
#define NTOUCH 4
#define SWZ 1
#define PREOUT 0
#define LFIRST 1
#define CBR "0"
#define WD 60
#define CD 3
#define OSLEEP 12
#define QPRIO 1
#define KPRIO 2
#define UDRAIN 0
#define GSLEEP 0
#define SIGPOS 0
#define LPRIO 3
#define TW 0
#define TPOS 0
#define R1EARLY 1
#define DPOS 0
#define NSLEEP 0
#define ASLEEP 40
#define FPRIO 1
#define PRBW 1
#define PRBPOS 1
#define TW2 6
#define DSLEEP 8
#define VD 400
#define XSLEEP 20
#define RPOS 1
#define KPRE 1
#define UD 40
#define XD 0
#define TRIG 1
#define CPF 0
#define RSLEEP 0
#define UPRIO 2
#define VD2 270
#define TWP 256
#define WDEPTH 8
#define PD 170
#define VDX 390
#define EARLY 0
#define ESLEEP 0
#define WDEPTH1 5
#define WD2 85
#define SD 70
#define KD 200
#define UFIRST 0
#define TX 1
#define QWAIT 4
#define OD 0
#define KBACK 6
#define DDEPTH 6
#define C3 1
#define XOFF 1
#define GD 105
#define XROT 2
#define WSOFF 0
#define T0 0
#define TF 192
#define DD 0
#define TDEPTH 1
#define TB1 0
#define TB1I 0
#define TB1X 2
#define B1SLEEP 0
#define DFIRST 0
#define T0SLEEP 0
#define BSLEEP 6
#define KN 3
#define TD 0
#define KE 0
#define SKIPB 0
#define NFIRST 0
#define PW1 16
#define PW2 38
#define KLATE 0
#define RPF 1
#define HSLEEP 6
#define LSLEEP 2
#define TSTOP 150
#define HSTOP 20
#define HL 0
#define VEARLY 0ull
#define FPR 3
#define SPR 3
#define DPRIO 0
#define T1 0
#define T1SLEEP 0
#define QPRIO2 3
#define HW 2
#define HN 3
#define RD 60
#define OPOS 0
#define OD2 8
#define NA 12
#define HAX 0
#define RD2 60
#define ORD1 ((unsigned __int128)0x687210543u << 36 | 0x321087654ull)
#define FORCESLOW 0
#define FORCEFB 0
#define CONV_ASM \
  "s_cmp_eq_u32 s89, 1\ns_cbranch_scc1 1f\ns_cmp_gt_u32 s89, 1\ns_cbranch_scc1 9f\nds_read_b64 v[124:125], v3 offset:576\n" \
  "ds_read_b64 v[126:127], v3 offset:584\nds_read_b64 v[120:121], v3 offset:64\nds_read_b64 v[122:123], v3 offset:72\n" \
  "ds_read_b64 v[116:117], v3 offset:640\nds_read_b64 v[118:119], v3 offset:648\nds_read_b64 v[112:113], v3 offset:128\n" \
  "ds_read_b64 v[114:115], v3 offset:136\nds_read_b64 v[108:109], v3 offset:704\nds_read_b64 v[110:111], v3 offset:712\n" \
  "ds_read_b64 v[104:105], v3 offset:192\nds_read_b64 v[106:107], v3 offset:200\nds_read_b64 v[100:101], v3 offset:768\n" \
  "ds_read_b64 v[102:103], v3 offset:776\nds_read_b64 v[96:97], v3 offset:256\nds_read_b64 v[98:99], v3 offset:264\n" \
  "ds_read_b64 v[92:93], v3 offset:832\nds_read_b64 v[94:95], v3 offset:840\nds_read_b64 v[88:89], v3 offset:320\n" \
  "ds_read_b64 v[90:91], v3 offset:328\nds_read_b64 v[84:85], v3 offset:896\nds_read_b64 v[86:87], v3 offset:904\n" \
  "ds_read_b64 v[80:81], v3 offset:384\nds_read_b64 v[82:83], v3 offset:392\nds_read_b64 v[76:77], v3 offset:960\n" \
  "ds_read_b64 v[78:79], v3 offset:968\nds_read_b64 v[72:73], v3 offset:448\nds_read_b64 v[74:75], v3 offset:456\n" \
  "ds_read_b64 v[68:69], v3 offset:1024\nds_read_b64 v[70:71], v3 offset:1032\nds_read_b64 v[64:65], v3 offset:512\n" \
  "ds_read_b64 v[66:67], v3 offset:520\nds_read_b64 v[60:61], v3 offset:0\nds_read_b64 v[62:63], v3 offset:8\nds_read_b64 v[56:57], v3 offset:1088\n" \
  "ds_read_b64 v[58:59], v3 offset:1096\ns_waitcnt lgkmcnt(0)\ns_barrier\nds_read_b128 v[36:39], v2 offset:0\nds_read_b128 v[40:43], v2 offset:64\n" \
  "ds_read_b128 v[44:47], v2 offset:128\nds_read_b128 v[48:51], v2 offset:192\ns_waitcnt lgkmcnt(3)\n" \
  "v_mfma_f32_16x16x32_f16 v[12:15], v[124:127], v[36:39], 0\ns_setprio 1\nds_read_b128 v[52:55], v2 offset:256\n" \
  "v_mfma_f32_16x16x32_f16 v[16:19], v[120:123], v[36:39], 0\ns_waitcnt lgkmcnt(3)\nv_mfma_f32_16x16x32_f16 v[12:15], v[116:119], v[40:43], v[12:15]\n" \
  "ds_read_b128 v[36:39], v2 offset:320\nv_mfma_f32_16x16x32_f16 v[16:19], v[112:115], v[40:43], v[16:19]\ns_waitcnt lgkmcnt(3)\n" \
  "v_mfma_f32_16x16x32_f16 v[12:15], v[108:111], v[44:47], v[12:15]\nds_read_b128 v[40:43], v2 offset:384\n" \
  "v_mfma_f32_16x16x32_f16 v[16:19], v[104:107], v[44:47], v[16:19]\ns_waitcnt lgkmcnt(3)\n" \
  "v_mfma_f32_16x16x32_f16 v[12:15], v[100:103], v[48:51], v[12:15]\nds_read_b128 v[44:47], v2 offset:448\n" \
  "v_mfma_f32_16x16x32_f16 v[16:19], v[96:99], v[48:51], v[16:19]\ns_waitcnt lgkmcnt(3)\n" \
  "v_mfma_f32_16x16x32_f16 v[12:15], v[92:95], v[52:55], v[12:15]\nv_mfma_f32_16x16x32_f16 v[16:19], v[88:91], v[52:55], v[16:19]\n" \
  "s_waitcnt lgkmcnt(2)\nv_mfma_f32_16x16x32_f16 v[12:15], v[84:87], v[36:39], v[12:15]\nds_read_b128 v[48:51], v2 offset:512\n" \
  "v_mfma_f32_16x16x32_f16 v[16:19], v[80:83], v[36:39], v[16:19]\ns_waitcnt lgkmcnt(2)\n" \
  "v_mfma_f32_16x16x32_f16 v[12:15], v[76:79], v[40:43], v[12:15]\nds_read_b128 v[52:55], v2 offset:576\n" \
  "v_mfma_f32_16x16x32_f16 v[16:19], v[72:75], v[40:43], v[16:19]\ns_waitcnt lgkmcnt(2)\n" \
  "v_mfma_f32_16x16x32_f16 v[12:15], v[68:71], v[44:47], v[12:15]\nds_read_b128 v[36:39], v2 offset:640\n" \
  "v_mfma_f32_16x16x32_f16 v[16:19], v[64:67], v[44:47], v[16:19]\nv_mfma_f32_16x16x32_f16 v[20:23], v[60:63], v[44:47], 0\n" \
  "ds_read_b128 v[40:43], v2 offset:704\nv_mfma_f32_16x16x32_f16 v[28:31], v[56:59], v[44:47], 0\ns_waitcnt lgkmcnt(3)\n" \
  "v_mfma_f32_16x16x32_f16 v[16:19], v[124:127], v[48:51], v[16:19]\nds_read_b128 v[56:59], v2 offset:768\nv_mul_f32 v12, v10, v12\n" \
  "v_mfma_f32_16x16x32_f16 v[20:23], v[120:123], v[48:51], v[20:23]\nv_mul_f32 v13, v10, v13\ns_waitcnt lgkmcnt(3)\n" \
  "v_mfma_f32_16x16x32_f16 v[16:19], v[116:119], v[52:55], v[16:19]\nds_read_b128 v[44:47], v2 offset:832\nv_mul_f32 v14, v10, v14\n" \
  "v_mfma_f32_16x16x32_f16 v[20:23], v[112:115], v[52:55], v[20:23]\nv_mul_f32 v15, v10, v15\ns_waitcnt lgkmcnt(3)\n" \
  "v_mfma_f32_16x16x32_f16 v[16:19], v[108:111], v[36:39], v[16:19]\nds_read_b128 v[48:51], v2 offset:896\nv_cvt_pk_bf16_f32 v32, v12, v13\n" \
  "v_mfma_f32_16x16x32_f16 v[20:23], v[104:107], v[36:39], v[20:23]\nv_cvt_pk_bf16_f32 v33, v14, v15\ns_waitcnt lgkmcnt(3)\n" \
  "v_mfma_f32_16x16x32_f16 v[16:19], v[100:103], v[40:43], v[16:19]\nds_read_b128 v[52:55], v2 offset:960\nds_write_b64 v4, v[32:33] offset:0\n" \
  "v_mfma_f32_16x16x32_f16 v[20:23], v[96:99], v[40:43], v[20:23]\ns_waitcnt lgkmcnt(3)\n" \
  "v_mfma_f32_16x16x32_f16 v[16:19], v[92:95], v[56:59], v[16:19]\nv_mfma_f32_16x16x32_f16 v[20:23], v[88:91], v[56:59], v[20:23]\n" \
  "ds_read_b128 v[36:39], v2 offset:1024\nv_mfma_f32_16x16x32_f16 v[16:19], v[84:87], v[44:47], v[16:19]\n" \
  "v_mfma_f32_16x16x32_f16 v[20:23], v[80:83], v[44:47], v[20:23]\nds_read_b128 v[40:43], v2 offset:1088\ns_waitcnt lgkmcnt(3)\n" \
  "v_mfma_f32_16x16x32_f16 v[16:19], v[76:79], v[48:51], v[16:19]\nds_read_b64 v[56:57], v6 offset:256\nds_read_b64 v[58:59], v6 offset:264\n" \
  "v_mfma_f32_16x16x32_f16 v[20:23], v[72:75], v[48:51], v[20:23]\nds_read_b128 v[44:47], v2 offset:1152\n" \
  "v_mfma_f32_16x16x32_f16 v[16:19], v[68:71], v[52:55], v[16:19]\nv_mfma_f32_16x16x32_f16 v[20:23], v[64:67], v[52:55], v[20:23]\n" \
  "ds_read_b128 v[48:51], v2 offset:1216\nv_mfma_f32_16x16x32_f16 v[24:27], v[60:63], v[52:55], 0\ns_waitcnt lgkmcnt(4)\n" \
  "v_mfma_f32_16x16x32_f16 v[20:23], v[124:127], v[36:39], v[20:23]\nds_read_b128 v[52:55], v2 offset:1280\n" \
  "v_mfma_f32_16x16x32_f16 v[24:27], v[120:123], v[36:39], v[24:27]\nv_mfma_f32_16x16x32_f16 v[20:23], v[116:119], v[40:43], v[20:23]\n" \
  "ds_read_b128 v[36:39], v2 offset:1344\nv_mul_f32 v16, v10, v16\nv_mfma_f32_16x16x32_f16 v[24:27], v[112:115], v[40:43], v[24:27]\n" \
  "v_mul_f32 v17, v10, v17\ns_waitcnt lgkmcnt(3)\nv_mfma_f32_16x16x32_f16 v[20:23], v[108:111], v[44:47], v[20:23]\n" \
  "ds_read_b128 v[40:43], v2 offset:1408\nv_mul_f32 v18, v10, v18\nds_read_b32 v35, v9 offset:32\n" \
  "v_mfma_f32_16x16x32_f16 v[24:27], v[104:107], v[44:47], v[24:27]\nv_mul_f32 v19, v10, v19\ns_waitcnt lgkmcnt(3)\n" \
  "v_mfma_f32_16x16x32_f16 v[20:23], v[100:103], v[48:51], v[20:23]\nds_read_b128 v[44:47], v2 offset:1472\nv_cvt_pk_bf16_f32 v32, v16, v17\n" \
  "v_mfma_f32_16x16x32_f16 v[24:27], v[96:99], v[48:51], v[24:27]\nv_cvt_pk_bf16_f32 v33, v18, v19\nds_write_b64 v4, v[32:33] offset:1024\n" \
  "v_mfma_f32_16x16x32_f16 v[20:23], v[92:95], v[52:55], v[20:23]\nv_mfma_f32_16x16x32_f16 v[24:27], v[88:91], v[52:55], v[24:27]\n" \
  "ds_read_b128 v[48:51], v2 offset:1536\ns_waitcnt lgkmcnt(3)\nv_readfirstlane_b32 s85, v35\ns_cmp_lt_u32 s85, 4\ns_cbranch_scc1 21f\n22:\n" \
  "v_mfma_f32_16x16x32_f16 v[20:23], v[84:87], v[36:39], v[20:23]\nv_mfma_f32_16x16x32_f16 v[24:27], v[80:83], v[36:39], v[24:27]\n" \
  "ds_read_b128 v[52:55], v2 offset:1600\nv_mfma_f32_16x16x32_f16 v[20:23], v[76:79], v[40:43], v[20:23]\n" \
  "v_mfma_f32_16x16x32_f16 v[24:27], v[72:75], v[40:43], v[24:27]\nds_read_b128 v[36:39], v2 offset:1664\ns_waitcnt lgkmcnt(4)\n" \
  "v_mfma_f32_16x16x32_f16 v[20:23], v[68:71], v[44:47], v[20:23]\nv_mfma_f32_16x16x32_f16 v[24:27], v[64:67], v[44:47], v[24:27]\n" \
  "ds_read_b128 v[40:43], v2 offset:1728\nv_mfma_f32_16x16x32_f16 v[28:31], v[60:63], v[44:47], v[28:31]\nds_read_b64_tr_b16 v[60:61], v5 offset:0\n" \
  "ds_read_b64_tr_b16 v[62:63], v5 offset:32\ns_waitcnt lgkmcnt(4)\nv_mfma_f32_16x16x32_f16 v[24:27], v[124:127], v[48:51], v[24:27]\n" \
  "v_mfma_f32_16x16x32_f16 v[28:31], v[120:123], v[48:51], v[28:31]\nds_read_b64_tr_b16 v[44:45], v5 offset:256\n" \
  "ds_read_b64_tr_b16 v[46:47], v5 offset:288\nv_mfma_f32_16x16x32_f16 v[24:27], v[116:119], v[52:55], v[24:27]\n" \
  "ds_read_b64 v[120:121], v6 offset:320\nds_read_b64 v[122:123], v6 offset:328\nv_mfma_f32_16x16x32_f16 v[28:31], v[112:115], v[52:55], v[28:31]\n" \
  "ds_read_b128 v[48:51], v2 offset:1792\ns_waitcnt lgkmcnt(7)\nv_mfma_f32_16x16x32_f16 v[24:27], v[108:111], v[36:39], v[24:27]\n" \
  "ds_read_b128 v[112:115], v2 offset:1856\nv_mfma_f32_16x16x32_f16 v[28:31], v[104:107], v[36:39], v[28:31]\nds_read_b64 v[52:53], v6 offset:128\n" \
  "ds_read_b64 v[54:55], v6 offset:136\nv_mfma_f32_16x16x32_f16 v[24:27], v[100:103], v[40:43], v[24:27]\nds_read_b128 v[104:107], v2 offset:1920\n" \
  "v_mfma_f32_16x16x32_f16 v[28:31], v[96:99], v[40:43], v[28:31]\nds_read_b64 v[36:37], v6 offset:192\nds_read_b64 v[38:39], v6 offset:200\n" \
  "s_waitcnt lgkmcnt(9)\nv_mfma_f32_16x16x32_f16 v[20:23], v[60:63], v[56:59], v[20:23]\nds_read_b128 v[96:99], v2 offset:1984\ns_waitcnt lgkmcnt(7)\n" \
  "v_mfma_f32_16x16x32_f16 v[20:23], v[44:47], v[120:123], v[20:23]\nv_mfma_f32_16x16x32_f16 v[24:27], v[92:95], v[48:51], v[24:27]\n" \
  "ds_read_b128 v[40:43], v2 offset:2048\nv_mfma_f32_16x16x32_f16 v[28:31], v[88:91], v[48:51], v[28:31]\nds_read_b64_tr_b16 v[88:89], v5 offset:512\n" \
  "ds_read_b64_tr_b16 v[90:91], v5 offset:544\ns_waitcnt lgkmcnt(6)\nv_mfma_f32_16x16x32_f16 v[24:27], v[84:87], v[112:115], v[24:27]\n" \
  "v_mfma_f32_16x16x32_f16 v[28:31], v[80:83], v[112:115], v[28:31]\nv_mul_f32 v20, v10, v20\nv_mul_f32 v21, v10, v21\n" \
  "v_mfma_f32_16x16x32_f16 v[24:27], v[76:79], v[104:107], v[24:27]\nds_read_b64_tr_b16 v[48:49], v5 offset:768\n" \
  "ds_read_b64_tr_b16 v[50:51], v5 offset:800\nv_mfma_f32_16x16x32_f16 v[28:31], v[72:75], v[104:107], v[28:31]\n" \
  "ds_read_b128 v[80:83], v2 offset:2112\ns_waitcnt lgkmcnt(5)\nv_mfma_f32_16x16x32_f16 v[24:27], v[68:71], v[96:99], v[24:27]\n" \
  "ds_read_b128 v[112:115], v2 offset:2176\nv_mul_f32 v22, v10, v22\nv_mfma_f32_16x16x32_f16 v[28:31], v[64:67], v[96:99], v[28:31]\n" \
  "ds_read_b128 v[72:75], v2 offset:2240\nv_mul_f32 v23, v10, v23\nv_mfma_f32_16x16x32_f16 v[28:31], v[124:127], v[40:43], v[28:31]\n" \
  "ds_read_b128 v[104:107], v2 offset:2304\nv_cvt_pk_bf16_f32 v32, v20, v21\nv_mfma_f32_16x16x32_f16 v[24:27], v[60:63], v[52:55], v[24:27]\n" \
  "ds_read_b128 v[64:67], v2 offset:2368\nv_cvt_pk_bf16_f32 v33, v22, v23\nv_mfma_f32_16x16x32_f16 v[24:27], v[44:47], v[36:39], v[24:27]\n" \
  "ds_read_b128 v[96:99], v2 offset:2432\ns_waitcnt lgkmcnt(6)\nv_mfma_f32_16x16x32_f16 v[24:27], v[88:91], v[56:59], v[24:27]\n" \
  "ds_read_b128 v[124:127], v2 offset:2496\nds_write_b64 v4, v[32:33] offset:2048\nv_mfma_f32_16x16x32_f16 v[24:27], v[48:51], v[120:123], v[24:27]\n" \
  "ds_read_b64 v[40:41], v6 offset:0\nds_read_b64 v[42:43], v6 offset:8\ns_waitcnt lgkmcnt(8)\n" \
  "v_mfma_f32_16x16x32_f16 v[28:31], v[116:119], v[80:83], v[28:31]\nds_read_b64 v[116:117], v6 offset:64\nds_read_b64 v[118:119], v6 offset:72\n" \
  "v_mfma_f32_16x16x32_f16 v[28:31], v[108:111], v[112:115], v[28:31]\nds_read_b64_tr_b16 v[80:81], v5 offset:1024\n" \
  "ds_read_b64_tr_b16 v[82:83], v5 offset:1056\ns_waitcnt lgkmcnt(10)\nv_mfma_f32_16x16x32_f16 v[28:31], v[100:103], v[72:75], v[28:31]\n" \
  "v_mul_f32 v24, v10, v24\nv_mul_f32 v25, v10, v25\nv_mfma_f32_16x16x32_f16 v[28:31], v[92:95], v[104:107], v[28:31]\nv_mul_f32 v26, v10, v26\n" \
  "s_waitcnt lgkmcnt(8)\nv_mfma_f32_16x16x32_f16 v[28:31], v[84:87], v[64:67], v[28:31]\nds_read_b64_tr_b16 v[108:109], v5 offset:1280\n" \
  "ds_read_b64_tr_b16 v[110:111], v5 offset:1312\nv_mfma_f32_16x16x32_f16 v[28:31], v[76:79], v[96:99], v[28:31]\n" \
  "ds_read_b64_tr_b16 v[112:113], v7 offset:0\nds_read_b64_tr_b16 v[114:115], v7 offset:64\ns_waitcnt lgkmcnt(8)\n" \
  "v_mfma_f32_16x16x32_f16 v[28:31], v[68:71], v[124:127], v[28:31]\nds_read2_b32 v[100:101], v8 offset0:48 offset1:49\n" \
  "ds_read2_b32 v[102:103], v8 offset0:50 offset1:51\nv_mfma_f32_16x16x32_f16 v[28:31], v[60:63], v[40:43], v[28:31]\nv_mul_f32 v27, v10, v27\n" \
  "s_waitcnt lgkmcnt(6)\nv_mfma_f32_16x16x32_f16 v[28:31], v[44:47], v[116:119], v[28:31]\nv_cvt_pk_bf16_f32 v32, v24, v25\n" \
  "v_cvt_pk_bf16_f32 v33, v26, v27\nv_mfma_f32_16x16x32_f16 v[28:31], v[88:91], v[52:55], v[28:31]\nds_write_b64 v4, v[32:33] offset:3072\n" \
  "v_mfma_f32_16x16x32_f16 v[28:31], v[48:51], v[36:39], v[28:31]\nv_mfma_f32_16x16x32_f16 v[28:31], v[80:83], v[56:59], v[28:31]\n" \
  "s_waitcnt lgkmcnt(3)\nv_mfma_f32_16x16x32_f16 v[28:31], v[108:111], v[120:123], v[28:31]\ns_waitcnt lgkmcnt(1)\n" \
  "v_mfma_f32_16x16x32_f16 v[28:31], v[112:115], v[100:103], v[28:31]\ns_nop 7\nv_mul_f32 v28, v10, v28\nv_mul_f32 v29, v10, v29\n" \
  "v_mul_f32 v30, v10, v30\nv_mul_f32 v31, v10, v31\nv_cvt_pk_bf16_f32 v32, v28, v29\nv_cvt_pk_bf16_f32 v33, v30, v31\n" \
  "ds_write_b64 v4, v[32:33] offset:4096\ns_setprio 0\ns_setpc_b64 s[90:91]\n21:\ns_movk_i32 s84, 0x4000\n23:\nds_read_b32 v35, v9 offset:32\n" \
  "s_sub_u32 s84, s84, 1\ns_cbranch_scc1 22b\ns_waitcnt lgkmcnt(0)\nv_readfirstlane_b32 s85, v35\ns_cmp_lt_u32 s85, 4\ns_cbranch_scc1 23b\n" \
  "s_branch 22b\n1:\nds_read_b64 v[124:125], v3 offset:1088\nds_read_b64 v[126:127], v3 offset:1096\nds_read_b64 v[120:121], v3 offset:0\n" \
  "ds_read_b64 v[122:123], v3 offset:8\nds_read_b64 v[116:117], v3 offset:64\nds_read_b64 v[118:119], v3 offset:72\n" \
  "ds_read_b64 v[112:113], v3 offset:128\nds_read_b64 v[114:115], v3 offset:136\nds_read_b64 v[108:109], v3 offset:192\n" \
  "ds_read_b64 v[110:111], v3 offset:200\nds_read_b64 v[104:105], v3 offset:256\nds_read_b64 v[106:107], v3 offset:264\n" \
  "ds_read_b64 v[100:101], v3 offset:320\nds_read_b64 v[102:103], v3 offset:328\nds_read_b64 v[96:97], v3 offset:384\n" \
  "ds_read_b64 v[98:99], v3 offset:392\nds_read_b64 v[92:93], v3 offset:448\nds_read_b64 v[94:95], v3 offset:456\n" \
  "ds_read_b64 v[88:89], v3 offset:512\nds_read_b64 v[90:91], v3 offset:520\nds_read_b64 v[84:85], v3 offset:576\n" \
  "ds_read_b64 v[86:87], v3 offset:584\nds_read_b64 v[80:81], v3 offset:640\nds_read_b64 v[82:83], v3 offset:648\n" \
  "ds_read_b64 v[76:77], v3 offset:704\nds_read_b64 v[78:79], v3 offset:712\nds_read_b64 v[72:73], v3 offset:768\n" \
  "ds_read_b64 v[74:75], v3 offset:776\nds_read_b64 v[68:69], v3 offset:832\nds_read_b64 v[70:71], v3 offset:840\n" \
  "ds_read_b64 v[64:65], v3 offset:896\nds_read_b64 v[66:67], v3 offset:904\nds_read_b64 v[60:61], v3 offset:960\n" \
  "ds_read_b64 v[62:63], v3 offset:968\nds_read_b64 v[56:57], v3 offset:1024\nds_read_b64 v[58:59], v3 offset:1032\n" \
  "ds_read_b64 v[52:53], v6 offset:0\nds_read_b64 v[54:55], v6 offset:8\nds_read_b64 v[48:49], v6 offset:64\nds_read_b64 v[50:51], v6 offset:72\n" \
  "s_waitcnt lgkmcnt(0)\ns_barrier\nds_read_b128 v[28:31], v2 offset:960\nds_read_b128 v[32:35], v2 offset:1472\n" \
  "ds_read_b128 v[36:39], v2 offset:1984\nds_read_b128 v[40:43], v2 offset:2048\nds_read_b128 v[44:47], v2 offset:2112\ns_waitcnt lgkmcnt(3)\n" \
  "v_mfma_f32_16x16x32_f16 v[12:15], v[124:127], v[28:31], 0\ns_setprio 1\nds_read_b128 v[28:31], v2 offset:2176\n" \
  "v_mfma_f32_16x16x32_f16 v[16:19], v[124:127], v[32:35], 0\nds_read_b128 v[32:35], v2 offset:2240\ns_waitcnt lgkmcnt(3)\n" \
  "v_mfma_f32_16x16x32_f16 v[12:15], v[120:123], v[36:39], v[12:15]\nv_mfma_f32_16x16x32_f16 v[20:23], v[124:127], v[36:39], 0\n" \
  "ds_read_b128 v[124:127], v2 offset:2304\nds_read_b128 v[36:39], v2 offset:2368\nv_mfma_f32_16x16x32_f16 v[12:15], v[116:119], v[40:43], v[12:15]\n" \
  "ds_read_b128 v[40:43], v2 offset:2432\ns_waitcnt lgkmcnt(4)\nv_mfma_f32_16x16x32_f16 v[12:15], v[112:115], v[44:47], v[12:15]\n" \
  "ds_read_b128 v[44:47], v2 offset:2496\nv_mfma_f32_16x16x32_f16 v[12:15], v[108:111], v[28:31], v[12:15]\nds_read_b128 v[28:31], v2 offset:2560\n" \
  "s_waitcnt lgkmcnt(4)\nv_mfma_f32_16x16x32_f16 v[12:15], v[104:107], v[32:35], v[12:15]\nds_read_b128 v[32:35], v2 offset:2624\n" \
  "v_mfma_f32_16x16x32_f16 v[12:15], v[100:103], v[124:127], v[12:15]\ns_waitcnt lgkmcnt(3)\n" \
  "v_mfma_f32_16x16x32_f16 v[12:15], v[96:99], v[36:39], v[12:15]\nds_read_b128 v[124:127], v2 offset:2688\n" \
  "v_mfma_f32_16x16x32_f16 v[12:15], v[92:95], v[40:43], v[12:15]\ns_waitcnt lgkmcnt(3)\n" \
  "v_mfma_f32_16x16x32_f16 v[12:15], v[88:91], v[44:47], v[12:15]\nds_read_b128 v[36:39], v2 offset:2752\n" \
  "v_mfma_f32_16x16x32_f16 v[16:19], v[120:123], v[44:47], v[16:19]\ns_waitcnt lgkmcnt(3)\n" \
  "v_mfma_f32_16x16x32_f16 v[12:15], v[84:87], v[28:31], v[12:15]\nds_read_b128 v[40:43], v2 offset:2816\n" \
  "v_mfma_f32_16x16x32_f16 v[16:19], v[116:119], v[28:31], v[16:19]\ns_waitcnt lgkmcnt(3)\n" \
  "v_mfma_f32_16x16x32_f16 v[12:15], v[80:83], v[32:35], v[12:15]\nds_read_b128 v[44:47], v2 offset:2880\n" \
  "v_mfma_f32_16x16x32_f16 v[16:19], v[112:115], v[32:35], v[16:19]\ns_waitcnt lgkmcnt(3)\n" \
  "v_mfma_f32_16x16x32_f16 v[12:15], v[76:79], v[124:127], v[12:15]\nds_read_b128 v[28:31], v2 offset:2944\n" \
  "v_mfma_f32_16x16x32_f16 v[16:19], v[108:111], v[124:127], v[16:19]\ns_waitcnt lgkmcnt(3)\n" \
  "v_mfma_f32_16x16x32_f16 v[12:15], v[72:75], v[36:39], v[12:15]\nds_read_b128 v[32:35], v2 offset:3008\n" \
  "v_mfma_f32_16x16x32_f16 v[16:19], v[104:107], v[36:39], v[16:19]\ns_waitcnt lgkmcnt(3)\n" \
  "v_mfma_f32_16x16x32_f16 v[12:15], v[68:71], v[40:43], v[12:15]\nv_mfma_f32_16x16x32_f16 v[16:19], v[100:103], v[40:43], v[16:19]\n" \
  "ds_read_b128 v[124:127], v2 offset:3072\ns_waitcnt lgkmcnt(3)\nv_mfma_f32_16x16x32_f16 v[12:15], v[64:67], v[44:47], v[12:15]\n" \
  "v_mfma_f32_16x16x32_f16 v[16:19], v[96:99], v[44:47], v[16:19]\nds_read_b128 v[36:39], v2 offset:3136\ns_waitcnt lgkmcnt(3)\n" \
  "v_mfma_f32_16x16x32_f16 v[12:15], v[60:63], v[28:31], v[12:15]\nds_read_b64 v[40:41], v6 offset:128\nds_read_b64 v[42:43], v6 offset:136\n" \
  "v_mfma_f32_16x16x32_f16 v[16:19], v[92:95], v[28:31], v[16:19]\nds_read_b128 v[44:47], v2 offset:3200\ns_waitcnt lgkmcnt(4)\n" \
  "v_mfma_f32_16x16x32_f16 v[12:15], v[56:59], v[32:35], v[12:15]\nv_mfma_f32_16x16x32_f16 v[16:19], v[88:91], v[32:35], v[16:19]\n" \
  "ds_read_b128 v[28:31], v2 offset:3264\nv_mfma_f32_16x16x32_f16 v[20:23], v[120:123], v[32:35], v[20:23]\nds_read_b64 v[120:121], v6 offset:192\n" \
  "ds_read_b64 v[122:123], v6 offset:200\nv_mfma_f32_16x16x32_f16 v[16:19], v[84:87], v[124:127], v[16:19]\nds_read_b128 v[32:35], v2 offset:3328\n" \
  "v_mfma_f32_16x16x32_f16 v[20:23], v[116:119], v[124:127], v[20:23]\nds_read_b64 v[116:117], v6 offset:256\nds_read_b64 v[118:119], v6 offset:264\n" \
  "s_waitcnt lgkmcnt(6)\nv_mfma_f32_16x16x32_f16 v[16:19], v[80:83], v[36:39], v[16:19]\nds_read_b128 v[124:127], v2 offset:3392\n" \
  "v_mfma_f32_16x16x32_f16 v[20:23], v[112:115], v[36:39], v[20:23]\nds_read_b64 v[112:113], v6 offset:320\nds_read_b64 v[114:115], v6 offset:328\n" \
  "v_mfma_f32_16x16x32_f16 v[16:19], v[76:79], v[44:47], v[16:19]\nds_read_b128 v[36:39], v2 offset:3456\n" \
  "v_mfma_f32_16x16x32_f16 v[20:23], v[108:111], v[44:47], v[20:23]\ns_waitcnt lgkmcnt(6)\n" \
  "v_mfma_f32_16x16x32_f16 v[16:19], v[72:75], v[28:31], v[16:19]\nds_read_b128 v[108:111], v2 offset:3520\n" \
  "v_mfma_f32_16x16x32_f16 v[20:23], v[104:107], v[28:31], v[20:23]\nv_mfma_f32_16x16x32_f16 v[16:19], v[68:71], v[32:35], v[16:19]\n" \
  "ds_read_b128 v[44:47], v2 offset:3584\nv_mfma_f32_16x16x32_f16 v[20:23], v[100:103], v[32:35], v[20:23]\nds_read_b128 v[104:107], v2 offset:3648\n" \
  "ds_read_b32 v27, v9 offset:32\ns_waitcnt lgkmcnt(4)\nv_mfma_f32_16x16x32_f16 v[16:19], v[64:67], v[124:127], v[16:19]\n" \
  "ds_read_b128 v[28:31], v2 offset:3712\nv_mfma_f32_16x16x32_f16 v[20:23], v[96:99], v[124:127], v[20:23]\nds_read_b128 v[100:103], v2 offset:3776\n" \
  "ds_read2_b32 v[32:33], v8 offset0:32 offset1:33\nds_read2_b32 v[34:35], v8 offset0:34 offset1:35\n" \
  "v_mfma_f32_16x16x32_f16 v[16:19], v[60:63], v[36:39], v[16:19]\nds_read_b128 v[96:99], v2 offset:3840\n" \
  "v_mfma_f32_16x16x32_f16 v[20:23], v[92:95], v[36:39], v[20:23]\nds_read_b128 v[124:127], v2 offset:3904\ns_waitcnt lgkmcnt(8)\n" \
  "v_mfma_f32_16x16x32_f16 v[16:19], v[56:59], v[108:111], v[16:19]\nds_read_b128 v[92:95], v2 offset:3968\ns_waitcnt lgkmcnt(7)\n" \
  "v_readfirstlane_b32 s85, v27\ns_cmp_lt_u32 s85, 4\ns_cbranch_scc1 31f\n32:\nv_mfma_f32_16x16x32_f16 v[20:23], v[88:91], v[108:111], v[20:23]\n" \
  "ds_read_b128 v[36:39], v2 offset:4032\nds_read_b64_tr_b16 v[88:89], v5 offset:512\nds_read_b64_tr_b16 v[90:91], v5 offset:544\n" \
  "v_mfma_f32_16x16x32_f16 v[20:23], v[84:87], v[44:47], v[20:23]\nds_read_b64_tr_b16 v[108:109], v5 offset:768\n" \
  "ds_read_b64_tr_b16 v[110:111], v5 offset:800\nv_mfma_f32_16x16x32_f16 v[20:23], v[80:83], v[104:107], v[20:23]\n" \
  "ds_read_b64_tr_b16 v[84:85], v5 offset:1024\nds_read_b64_tr_b16 v[86:87], v5 offset:1056\ns_waitcnt lgkmcnt(12)\n" \
  "v_mfma_f32_16x16x32_f16 v[20:23], v[76:79], v[28:31], v[20:23]\nds_read_b64_tr_b16 v[44:45], v5 offset:1280\n" \
  "ds_read_b64_tr_b16 v[46:47], v5 offset:1312\nv_mfma_f32_16x16x32_f16 v[20:23], v[72:75], v[100:103], v[20:23]\ns_waitcnt lgkmcnt(10)\n" \
  "v_mfma_f32_16x16x32_f16 v[20:23], v[68:71], v[96:99], v[20:23]\nds_read_b64_tr_b16 v[80:81], v5 offset:1536\n" \
  "ds_read_b64_tr_b16 v[82:83], v5 offset:1568\nv_mfma_f32_16x16x32_f16 v[20:23], v[64:67], v[124:127], v[20:23]\n" \
  "ds_read_b64_tr_b16 v[104:105], v5 offset:1792\nds_read_b64_tr_b16 v[106:107], v5 offset:1824\ns_waitcnt lgkmcnt(12)\n" \
  "v_mfma_f32_16x16x32_f16 v[20:23], v[60:63], v[92:95], v[20:23]\nds_read_b64_tr_b16 v[76:77], v5 offset:2048\n" \
  "ds_read_b64_tr_b16 v[78:79], v5 offset:2080\nv_mfma_f32_16x16x32_f16 v[20:23], v[56:59], v[36:39], v[20:23]\ns_waitcnt lgkmcnt(10)\n" \
  "v_mfma_f32_16x16x32_f16 v[12:15], v[88:91], v[52:55], v[12:15]\nds_read_b64_tr_b16 v[28:29], v5 offset:2304\n" \
  "ds_read_b64_tr_b16 v[30:31], v5 offset:2336\nv_mfma_f32_16x16x32_f16 v[12:15], v[108:111], v[48:51], v[12:15]\n" \
  "ds_read_b64_tr_b16 v[72:73], v5 offset:2560\nds_read_b64_tr_b16 v[74:75], v5 offset:2592\ns_waitcnt lgkmcnt(10)\n" \
  "v_mfma_f32_16x16x32_f16 v[12:15], v[84:87], v[40:43], v[12:15]\nds_read_b64_tr_b16 v[100:101], v5 offset:2816\n" \
  "ds_read_b64_tr_b16 v[102:103], v5 offset:2848\nv_mfma_f32_16x16x32_f16 v[16:19], v[84:87], v[52:55], v[16:19]\n" \
  "ds_read_b64_tr_b16 v[68:69], v7 offset:0\nds_read_b64_tr_b16 v[70:71], v7 offset:64\n" \
  "v_mfma_f32_16x16x32_f16 v[12:15], v[44:47], v[120:123], v[12:15]\nv_mfma_f32_16x16x32_f16 v[16:19], v[44:47], v[48:51], v[16:19]\n" \
  "s_waitcnt lgkmcnt(10)\nv_mfma_f32_16x16x32_f16 v[12:15], v[80:83], v[116:119], v[12:15]\nds_read2_b32 v[96:97], v8 offset0:16 offset1:17\n" \
  "ds_read2_b32 v[98:99], v8 offset0:18 offset1:19\nv_mfma_f32_16x16x32_f16 v[16:19], v[80:83], v[40:43], v[16:19]\n" \
  "ds_read2_b32 v[64:65], v8 offset0:0 offset1:1\nds_read2_b32 v[66:67], v8 offset0:2 offset1:3\n" \
  "v_mfma_f32_16x16x32_f16 v[20:23], v[80:83], v[52:55], v[20:23]\nv_mfma_f32_16x16x32_f16 v[12:15], v[104:107], v[112:115], v[12:15]\n" \
  "v_mfma_f32_16x16x32_f16 v[16:19], v[104:107], v[120:123], v[16:19]\nv_mfma_f32_16x16x32_f16 v[20:23], v[104:107], v[48:51], v[20:23]\n" \
  "s_waitcnt lgkmcnt(10)\nv_mfma_f32_16x16x32_f16 v[16:19], v[76:79], v[116:119], v[16:19]\nds_read_b64_tr_b16 v[124:125], v7 offset:512\n" \
  "ds_read_b64_tr_b16 v[126:127], v7 offset:576\nv_mfma_f32_16x16x32_f16 v[20:23], v[76:79], v[40:43], v[20:23]\n" \
  "ds_read2_b32 v[60:61], v8 offset0:48 offset1:49\nds_read2_b32 v[62:63], v8 offset0:50 offset1:51\n" \
  "v_mfma_f32_16x16x32_f16 v[16:19], v[28:31], v[112:115], v[16:19]\nv_mfma_f32_16x16x32_f16 v[20:23], v[28:31], v[120:123], v[20:23]\n" \
  "s_waitcnt lgkmcnt(10)\nv_mfma_f32_16x16x32_f16 v[20:23], v[72:75], v[116:119], v[20:23]\nds_read_b64_tr_b16 v[92:93], v7 offset:1024\n" \
  "ds_read_b64_tr_b16 v[94:95], v7 offset:1088\nv_mfma_f32_16x16x32_f16 v[20:23], v[100:103], v[112:115], v[20:23]\n" \
  "ds_read_b64_tr_b16 v[56:57], v7 offset:1536\nds_read_b64_tr_b16 v[58:59], v7 offset:1600\ns_waitcnt lgkmcnt(10)\n" \
  "v_mfma_f32_16x16x32_f16 v[12:15], v[68:71], v[32:35], v[12:15]\nv_mfma_f32_16x16x32_f16 v[16:19], v[68:71], v[96:99], v[16:19]\n" \
  "s_waitcnt lgkmcnt(6)\nv_mfma_f32_16x16x32_f16 v[20:23], v[68:71], v[64:67], v[20:23]\ns_waitcnt lgkmcnt(2)\n" \
  "v_mfma_f32_16x16x32_f16 v[12:15], v[124:127], v[60:63], v[12:15]\nv_mfma_f32_16x16x32_f16 v[16:19], v[124:127], v[32:35], v[16:19]\n" \
  "v_mfma_f32_16x16x32_f16 v[20:23], v[124:127], v[96:99], v[20:23]\nv_mfma_f32_16x16x32_f16 v[16:19], v[92:95], v[60:63], v[16:19]\n" \
  "v_mfma_f32_16x16x32_f16 v[20:23], v[92:95], v[32:35], v[20:23]\ns_waitcnt lgkmcnt(0)\n" \
  "v_mfma_f32_16x16x32_f16 v[20:23], v[56:59], v[60:63], v[20:23]\ns_nop 1\nv_mul_f32 v12, v10, v12\nv_mul_f32 v13, v10, v13\n" \
  "v_mul_f32 v14, v10, v14\nv_mul_f32 v15, v10, v15\nv_cvt_pk_bf16_f32 v24, v12, v13\nv_cvt_pk_bf16_f32 v25, v14, v15\n" \
  "ds_write_b64 v4, v[24:25] offset:5120\nv_mul_f32 v16, v10, v16\nv_mul_f32 v17, v10, v17\nv_mul_f32 v18, v10, v18\nv_mul_f32 v19, v10, v19\n" \
  "v_cvt_pk_bf16_f32 v24, v16, v17\nv_cvt_pk_bf16_f32 v25, v18, v19\nds_write_b64 v4, v[24:25] offset:6144\nv_mul_f32 v20, v10, v20\n" \
  "v_mul_f32 v21, v10, v21\nv_mul_f32 v22, v10, v22\nv_mul_f32 v23, v10, v23\nv_cvt_pk_bf16_f32 v24, v20, v21\nv_cvt_pk_bf16_f32 v25, v22, v23\n" \
  "ds_write_b64 v4, v[24:25] offset:7168\ns_setprio 0\ns_setpc_b64 s[90:91]\n31:\ns_movk_i32 s84, 0x4000\n33:\nds_read_b32 v27, v9 offset:32\n" \
  "s_sub_u32 s84, s84, 1\ns_cbranch_scc1 32b\ns_waitcnt lgkmcnt(0)\nv_readfirstlane_b32 s85, v27\ns_cmp_lt_u32 s85, 4\ns_cbranch_scc1 33b\n" \
  "s_branch 32b\n9:\ns_cmp_eq_u32 s89, 3\ns_cbranch_scc1 3f\ns_cmp_eq_u32 s89, 4\ns_cbranch_scc1 4f\ns_cmp_eq_u32 s89, 5\ns_cbranch_scc1 5f\n" \
  "ds_read_b64 v[124:125], v3 offset:576\nds_read_b64 v[126:127], v3 offset:584\nds_read_b64 v[120:121], v3 offset:64\n" \
  "ds_read_b64 v[122:123], v3 offset:72\nds_read_b64 v[116:117], v3 offset:640\nds_read_b64 v[118:119], v3 offset:648\n" \
  "ds_read_b64 v[112:113], v3 offset:128\nds_read_b64 v[114:115], v3 offset:136\nds_read_b64 v[108:109], v3 offset:704\n" \
  "ds_read_b64 v[110:111], v3 offset:712\nds_read_b64 v[104:105], v3 offset:192\nds_read_b64 v[106:107], v3 offset:200\n" \
  "ds_read_b64 v[100:101], v3 offset:768\nds_read_b64 v[102:103], v3 offset:776\nds_read_b64 v[96:97], v3 offset:256\n" \
  "ds_read_b64 v[98:99], v3 offset:264\nds_read_b64 v[92:93], v3 offset:832\nds_read_b64 v[94:95], v3 offset:840\n" \
  "ds_read_b64 v[88:89], v3 offset:320\nds_read_b64 v[90:91], v3 offset:328\nds_read_b64 v[84:85], v3 offset:896\n" \
  "ds_read_b64 v[86:87], v3 offset:904\nds_read_b64 v[80:81], v3 offset:384\nds_read_b64 v[82:83], v3 offset:392\n" \
  "ds_read_b64 v[76:77], v3 offset:960\nds_read_b64 v[78:79], v3 offset:968\nds_read_b64 v[72:73], v3 offset:448\n" \
  "ds_read_b64 v[74:75], v3 offset:456\nds_read_b64 v[68:69], v3 offset:1024\nds_read_b64 v[70:71], v3 offset:1032\n" \
  "ds_read_b64 v[64:65], v3 offset:512\nds_read_b64 v[66:67], v3 offset:520\ns_waitcnt lgkmcnt(0)\ns_barrier\nds_read_b128 v[24:27], v2 offset:0\n" \
  "ds_read_b128 v[28:31], v2 offset:64\nds_read_b128 v[32:35], v2 offset:128\nds_read_b128 v[36:39], v2 offset:192\ns_waitcnt lgkmcnt(3)\n" \
  "v_mfma_f32_16x16x32_f16 v[12:15], v[124:127], v[24:27], 0\ns_setprio 1\nds_read_b128 v[40:43], v2 offset:256\n" \
  "v_mfma_f32_16x16x32_f16 v[16:19], v[120:123], v[24:27], 0\ns_waitcnt lgkmcnt(3)\nv_mfma_f32_16x16x32_f16 v[12:15], v[116:119], v[28:31], v[12:15]\n" \
  "ds_read_b128 v[44:47], v2 offset:320\nv_mfma_f32_16x16x32_f16 v[16:19], v[112:115], v[28:31], v[16:19]\ns_waitcnt lgkmcnt(3)\n" \
  "v_mfma_f32_16x16x32_f16 v[12:15], v[108:111], v[32:35], v[12:15]\nds_read_b128 v[48:51], v2 offset:384\n" \
  "v_mfma_f32_16x16x32_f16 v[16:19], v[104:107], v[32:35], v[16:19]\ns_waitcnt lgkmcnt(3)\n" \
  "v_mfma_f32_16x16x32_f16 v[12:15], v[100:103], v[36:39], v[12:15]\nds_read_b128 v[52:55], v2 offset:448\n" \
  "v_mfma_f32_16x16x32_f16 v[16:19], v[96:99], v[36:39], v[16:19]\ns_waitcnt lgkmcnt(3)\n" \
  "v_mfma_f32_16x16x32_f16 v[12:15], v[92:95], v[40:43], v[12:15]\nds_read_b128 v[56:59], v2 offset:512\n" \
  "v_mfma_f32_16x16x32_f16 v[16:19], v[88:91], v[40:43], v[16:19]\nds_read_b128 v[60:63], v2 offset:576\ns_waitcnt lgkmcnt(3)\n" \
  "v_mfma_f32_16x16x32_f16 v[12:15], v[84:87], v[44:47], v[12:15]\nds_read_b128 v[120:123], v2 offset:640\n" \
  "v_mfma_f32_16x16x32_f16 v[16:19], v[80:83], v[44:47], v[16:19]\nds_read_b128 v[24:27], v2 offset:704\n" \
  "v_mfma_f32_16x16x32_f16 v[12:15], v[76:79], v[48:51], v[12:15]\nds_read_b128 v[112:115], v2 offset:768\n" \
  "v_mfma_f32_16x16x32_f16 v[16:19], v[72:75], v[48:51], v[16:19]\nds_read_b128 v[28:31], v2 offset:832\ns_waitcnt lgkmcnt(5)\n" \
  "v_mfma_f32_16x16x32_f16 v[12:15], v[68:71], v[52:55], v[12:15]\nds_read_b128 v[104:107], v2 offset:896\n" \
  "v_mfma_f32_16x16x32_f16 v[16:19], v[64:67], v[52:55], v[16:19]\nds_read_b128 v[32:35], v2 offset:960\n" \
  "v_mfma_f32_16x16x32_f16 v[16:19], v[124:127], v[56:59], v[16:19]\ns_waitcnt lgkmcnt(5)\n" \
  "v_mfma_f32_16x16x32_f16 v[16:19], v[116:119], v[60:63], v[16:19]\nv_mfma_f32_16x16x32_f16 v[16:19], v[108:111], v[120:123], v[16:19]\n" \
  "s_waitcnt lgkmcnt(3)\nv_mfma_f32_16x16x32_f16 v[16:19], v[100:103], v[24:27], v[16:19]\nv_mul_f32 v12, v10, v12\nv_mul_f32 v13, v10, v13\n" \
  "v_mfma_f32_16x16x32_f16 v[16:19], v[92:95], v[112:115], v[16:19]\nv_mul_f32 v14, v10, v14\ns_waitcnt lgkmcnt(2)\n" \
  "v_mfma_f32_16x16x32_f16 v[16:19], v[84:87], v[28:31], v[16:19]\nv_mul_f32 v15, v10, v15\ns_waitcnt lgkmcnt(1)\n" \
  "v_mfma_f32_16x16x32_f16 v[16:19], v[76:79], v[104:107], v[16:19]\nv_cvt_pk_bf16_f32 v20, v12, v13\ns_waitcnt lgkmcnt(0)\n" \
  "v_mfma_f32_16x16x32_f16 v[16:19], v[68:71], v[32:35], v[16:19]\nv_cvt_pk_bf16_f32 v21, v14, v15\nds_write_b64 v4, v[20:21] offset:0\ns_nop 5\n" \
  "v_mul_f32 v16, v10, v16\nv_mul_f32 v17, v10, v17\nv_mul_f32 v18, v10, v18\nv_mul_f32 v19, v10, v19\nv_cvt_pk_bf16_f32 v20, v16, v17\n" \
  "v_cvt_pk_bf16_f32 v21, v18, v19\nds_write_b64 v4, v[20:21] offset:1024\ns_branch 8f\n3:\nds_read_b64 v[124:125], v3 offset:0\n" \
  "ds_read_b64 v[126:127], v3 offset:8\nds_read_b64 v[120:121], v3 offset:64\nds_read_b64 v[122:123], v3 offset:72\n" \
  "ds_read_b64 v[116:117], v3 offset:128\nds_read_b64 v[118:119], v3 offset:136\nds_read_b64 v[112:113], v3 offset:192\n" \
  "ds_read_b64 v[114:115], v3 offset:200\nds_read_b64 v[108:109], v3 offset:256\nds_read_b64 v[110:111], v3 offset:264\n" \
  "ds_read_b64 v[104:105], v3 offset:320\nds_read_b64 v[106:107], v3 offset:328\nds_read_b64 v[100:101], v3 offset:384\n" \
  "ds_read_b64 v[102:103], v3 offset:392\nds_read_b64 v[96:97], v3 offset:448\nds_read_b64 v[98:99], v3 offset:456\n" \
  "ds_read_b64 v[92:93], v3 offset:512\nds_read_b64 v[94:95], v3 offset:520\nds_read_b64 v[88:89], v3 offset:576\n" \
  "ds_read_b64 v[90:91], v3 offset:584\nds_read_b64 v[84:85], v3 offset:640\nds_read_b64 v[86:87], v3 offset:648\n" \
  "ds_read_b64 v[80:81], v3 offset:704\nds_read_b64 v[82:83], v3 offset:712\nds_read_b64 v[76:77], v3 offset:768\n" \
  "ds_read_b64 v[78:79], v3 offset:776\nds_read_b64 v[72:73], v3 offset:832\nds_read_b64 v[74:75], v3 offset:840\n" \
  "ds_read_b64 v[68:69], v3 offset:896\nds_read_b64 v[70:71], v3 offset:904\nds_read_b64 v[64:65], v3 offset:960\n" \
  "ds_read_b64 v[66:67], v3 offset:968\nds_read_b64 v[60:61], v3 offset:1024\nds_read_b64 v[62:63], v3 offset:1032\n" \
  "ds_read_b64 v[56:57], v6 offset:256\nds_read_b64 v[58:59], v6 offset:264\nds_read_b64 v[52:53], v6 offset:128\n" \
  "ds_read_b64 v[54:55], v6 offset:136\nds_read_b64 v[48:49], v6 offset:320\nds_read_b64 v[50:51], v6 offset:328\n" \
  "ds_read_b64 v[44:45], v6 offset:192\nds_read_b64 v[46:47], v6 offset:200\ns_waitcnt lgkmcnt(0)\ns_barrier\nds_read_b128 v[24:27], v2 offset:448\n" \
  "ds_read_b128 v[28:31], v2 offset:512\nds_read_b128 v[32:35], v2 offset:576\nds_read_b128 v[36:39], v2 offset:640\n" \
  "ds_read_b128 v[40:43], v2 offset:704\ns_waitcnt lgkmcnt(3)\nv_mfma_f32_16x16x32_f16 v[12:15], v[124:127], v[24:27], 0\ns_setprio 1\n" \
  "ds_read_b128 v[24:27], v2 offset:768\nv_mfma_f32_16x16x32_f16 v[12:15], v[120:123], v[28:31], v[12:15]\nds_read_b128 v[28:31], v2 offset:832\n" \
  "s_waitcnt lgkmcnt(3)\nv_mfma_f32_16x16x32_f16 v[12:15], v[116:119], v[32:35], v[12:15]\nds_read_b128 v[32:35], v2 offset:896\n" \
  "v_mfma_f32_16x16x32_f16 v[12:15], v[112:115], v[36:39], v[12:15]\nds_read_b128 v[36:39], v2 offset:960\ns_waitcnt lgkmcnt(3)\n" \
  "v_mfma_f32_16x16x32_f16 v[12:15], v[108:111], v[40:43], v[12:15]\nds_read_b128 v[40:43], v2 offset:1024\n" \
  "v_mfma_f32_16x16x32_f16 v[12:15], v[104:107], v[24:27], v[12:15]\nds_read_b128 v[24:27], v2 offset:1088\ns_waitcnt lgkmcnt(3)\n" \
  "v_mfma_f32_16x16x32_f16 v[12:15], v[100:103], v[28:31], v[12:15]\nds_read_b128 v[28:31], v2 offset:1152\n" \
  "v_mfma_f32_16x16x32_f16 v[12:15], v[96:99], v[32:35], v[12:15]\ns_waitcnt lgkmcnt(3)\n" \
  "v_mfma_f32_16x16x32_f16 v[12:15], v[92:95], v[36:39], v[12:15]\nds_read_b128 v[32:35], v2 offset:1216\n" \
  "v_mfma_f32_16x16x32_f16 v[16:19], v[124:127], v[36:39], 0\ns_waitcnt lgkmcnt(3)\nv_mfma_f32_16x16x32_f16 v[12:15], v[88:91], v[40:43], v[12:15]\n" \
  "ds_read_b128 v[124:127], v2 offset:1280\nv_mfma_f32_16x16x32_f16 v[16:19], v[120:123], v[40:43], v[16:19]\ns_waitcnt lgkmcnt(3)\n" \
  "v_mfma_f32_16x16x32_f16 v[12:15], v[84:87], v[24:27], v[12:15]\nds_read_b128 v[36:39], v2 offset:1344\n" \
  "v_mfma_f32_16x16x32_f16 v[16:19], v[116:119], v[24:27], v[16:19]\ns_waitcnt lgkmcnt(3)\n" \
  "v_mfma_f32_16x16x32_f16 v[12:15], v[80:83], v[28:31], v[12:15]\nds_read_b128 v[120:123], v2 offset:1408\n" \
  "v_mfma_f32_16x16x32_f16 v[16:19], v[112:115], v[28:31], v[16:19]\ns_waitcnt lgkmcnt(3)\n" \
  "v_mfma_f32_16x16x32_f16 v[12:15], v[76:79], v[32:35], v[12:15]\nds_read_b128 v[40:43], v2 offset:1472\n" \
  "v_mfma_f32_16x16x32_f16 v[16:19], v[108:111], v[32:35], v[16:19]\ns_waitcnt lgkmcnt(3)\n" \
  "v_mfma_f32_16x16x32_f16 v[12:15], v[72:75], v[124:127], v[12:15]\nds_read_b128 v[116:119], v2 offset:1536\n" \
  "v_mfma_f32_16x16x32_f16 v[16:19], v[104:107], v[124:127], v[16:19]\nds_read_b128 v[24:27], v2 offset:1600\ns_waitcnt lgkmcnt(3)\n" \
  "v_mfma_f32_16x16x32_f16 v[12:15], v[68:71], v[36:39], v[12:15]\nds_read_b128 v[112:115], v2 offset:1664\nds_read_b32 v23, v9 offset:32\n" \
  "v_mfma_f32_16x16x32_f16 v[16:19], v[100:103], v[36:39], v[16:19]\nds_read_b128 v[28:31], v2 offset:1728\n" \
  "v_mfma_f32_16x16x32_f16 v[12:15], v[64:67], v[120:123], v[12:15]\nds_read_b128 v[108:111], v2 offset:1792\n" \
  "v_mfma_f32_16x16x32_f16 v[16:19], v[96:99], v[120:123], v[16:19]\nds_read_b128 v[32:35], v2 offset:1856\ns_waitcnt lgkmcnt(6)\n" \
  "v_mfma_f32_16x16x32_f16 v[12:15], v[60:63], v[40:43], v[12:15]\nds_read_b128 v[104:107], v2 offset:1920\n" \
  "v_mfma_f32_16x16x32_f16 v[16:19], v[92:95], v[40:43], v[16:19]\nds_read_b128 v[124:127], v2 offset:1984\ns_waitcnt lgkmcnt(5)\n" \
  "v_readfirstlane_b32 s85, v23\ns_cmp_lt_u32 s85, 3\ns_cbranch_scc1 51f\n52:\nv_mfma_f32_16x16x32_f16 v[16:19], v[88:91], v[116:119], v[16:19]\n" \
  "ds_read_b64_tr_b16 v[100:101], v5 offset:0\nds_read_b64_tr_b16 v[102:103], v5 offset:32\n" \
  "v_mfma_f32_16x16x32_f16 v[16:19], v[84:87], v[24:27], v[16:19]\nds_read_b64_tr_b16 v[36:37], v5 offset:256\n" \
  "ds_read_b64_tr_b16 v[38:39], v5 offset:288\nv_mfma_f32_16x16x32_f16 v[16:19], v[80:83], v[112:115], v[16:19]\n" \
  "ds_read_b64_tr_b16 v[96:97], v5 offset:512\nds_read_b64_tr_b16 v[98:99], v5 offset:544\ns_waitcnt lgkmcnt(9)\n" \
  "v_mfma_f32_16x16x32_f16 v[16:19], v[76:79], v[28:31], v[16:19]\nds_read_b64_tr_b16 v[120:121], v5 offset:768\n" \
  "ds_read_b64_tr_b16 v[122:123], v5 offset:800\nv_mfma_f32_16x16x32_f16 v[16:19], v[72:75], v[108:111], v[16:19]\ns_waitcnt lgkmcnt(9)\n" \
  "v_mfma_f32_16x16x32_f16 v[16:19], v[68:71], v[32:35], v[16:19]\nv_mfma_f32_16x16x32_f16 v[16:19], v[64:67], v[104:107], v[16:19]\n" \
  "s_waitcnt lgkmcnt(6)\nv_mfma_f32_16x16x32_f16 v[16:19], v[60:63], v[124:127], v[16:19]\n" \
  "v_mfma_f32_16x16x32_f16 v[12:15], v[100:103], v[56:59], v[12:15]\nv_mfma_f32_16x16x32_f16 v[16:19], v[100:103], v[52:55], v[16:19]\n" \
  "s_waitcnt lgkmcnt(2)\nv_mfma_f32_16x16x32_f16 v[12:15], v[36:39], v[48:51], v[12:15]\n" \
  "v_mfma_f32_16x16x32_f16 v[16:19], v[36:39], v[44:47], v[16:19]\nv_mfma_f32_16x16x32_f16 v[16:19], v[96:99], v[56:59], v[16:19]\n" \
  "s_waitcnt lgkmcnt(0)\nv_mfma_f32_16x16x32_f16 v[16:19], v[120:123], v[48:51], v[16:19]\ns_nop 3\nv_mul_f32 v12, v10, v12\nv_mul_f32 v13, v10, v13\n" \
  "v_mul_f32 v14, v10, v14\nv_mul_f32 v15, v10, v15\nv_cvt_pk_bf16_f32 v20, v12, v13\nv_cvt_pk_bf16_f32 v21, v14, v15\n" \
  "ds_write_b64 v4, v[20:21] offset:2048\nv_mul_f32 v16, v10, v16\nv_mul_f32 v17, v10, v17\nv_mul_f32 v18, v10, v18\nv_mul_f32 v19, v10, v19\n" \
  "v_cvt_pk_bf16_f32 v20, v16, v17\nv_cvt_pk_bf16_f32 v21, v18, v19\nds_write_b64 v4, v[20:21] offset:3072\ns_branch 8f\n51:\ns_movk_i32 s84, 0x4000\n" \
  "53:\nds_read_b32 v23, v9 offset:32\ns_sub_u32 s84, s84, 1\ns_cbranch_scc1 52b\ns_waitcnt lgkmcnt(0)\nv_readfirstlane_b32 s85, v23\n" \
  "s_cmp_lt_u32 s85, 3\ns_cbranch_scc1 53b\ns_branch 52b\n4:\nds_read_b64 v[124:125], v3 offset:1088\nds_read_b64 v[126:127], v3 offset:1096\n" \
  "ds_read_b64 v[120:121], v3 offset:0\nds_read_b64 v[122:123], v3 offset:8\nds_read_b64 v[116:117], v3 offset:64\n" \
  "ds_read_b64 v[118:119], v3 offset:72\nds_read_b64 v[112:113], v3 offset:128\nds_read_b64 v[114:115], v3 offset:136\n" \
  "ds_read_b64 v[108:109], v3 offset:192\nds_read_b64 v[110:111], v3 offset:200\nds_read_b64 v[104:105], v3 offset:256\n" \
  "ds_read_b64 v[106:107], v3 offset:264\nds_read_b64 v[100:101], v3 offset:320\nds_read_b64 v[102:103], v3 offset:328\n" \
  "ds_read_b64 v[96:97], v3 offset:384\nds_read_b64 v[98:99], v3 offset:392\nds_read_b64 v[92:93], v3 offset:448\n" \
  "ds_read_b64 v[94:95], v3 offset:456\nds_read_b64 v[88:89], v3 offset:512\nds_read_b64 v[90:91], v3 offset:520\n" \
  "ds_read_b64 v[84:85], v3 offset:576\nds_read_b64 v[86:87], v3 offset:584\nds_read_b64 v[80:81], v3 offset:640\n" \
  "ds_read_b64 v[82:83], v3 offset:648\nds_read_b64 v[76:77], v3 offset:704\nds_read_b64 v[78:79], v3 offset:712\n" \
  "ds_read_b64 v[72:73], v3 offset:768\nds_read_b64 v[74:75], v3 offset:776\nds_read_b64 v[68:69], v3 offset:832\n" \
  "ds_read_b64 v[70:71], v3 offset:840\nds_read_b64 v[64:65], v3 offset:896\nds_read_b64 v[66:67], v3 offset:904\n" \
  "ds_read_b64 v[60:61], v3 offset:960\nds_read_b64 v[62:63], v3 offset:968\nds_read_b64 v[56:57], v3 offset:1024\n" \
  "ds_read_b64 v[58:59], v3 offset:1032\nds_read_b64 v[52:53], v6 offset:0\nds_read_b64 v[54:55], v6 offset:8\nds_read_b64 v[48:49], v6 offset:64\n" \
  "ds_read_b64 v[50:51], v6 offset:72\nds_read_b64 v[44:45], v6 offset:128\nds_read_b64 v[46:47], v6 offset:136\nds_read_b64 v[40:41], v6 offset:192\n" \
  "ds_read_b64 v[42:43], v6 offset:200\ns_waitcnt lgkmcnt(0)\ns_barrier\nds_read_b128 v[24:27], v2 offset:448\nds_read_b128 v[28:31], v2 offset:960\n" \
  "ds_read_b128 v[32:35], v2 offset:1472\nds_read_b128 v[36:39], v2 offset:1536\ns_waitcnt lgkmcnt(3)\n" \
  "v_mfma_f32_16x16x32_f16 v[12:15], v[124:127], v[24:27], 0\ns_setprio 1\nds_read_b128 v[24:27], v2 offset:1600\ns_waitcnt lgkmcnt(3)\n" \
  "v_mfma_f32_16x16x32_f16 v[16:19], v[124:127], v[28:31], 0\nds_read_b128 v[124:127], v2 offset:1664\ns_waitcnt lgkmcnt(3)\n" \
  "v_mfma_f32_16x16x32_f16 v[12:15], v[120:123], v[32:35], v[12:15]\nds_read_b128 v[28:31], v2 offset:1728\ns_waitcnt lgkmcnt(3)\n" \
  "v_mfma_f32_16x16x32_f16 v[12:15], v[116:119], v[36:39], v[12:15]\nds_read_b128 v[32:35], v2 offset:1792\ns_waitcnt lgkmcnt(3)\n" \
  "v_mfma_f32_16x16x32_f16 v[12:15], v[112:115], v[24:27], v[12:15]\nds_read_b128 v[36:39], v2 offset:1856\ns_waitcnt lgkmcnt(3)\n" \
  "v_mfma_f32_16x16x32_f16 v[12:15], v[108:111], v[124:127], v[12:15]\nds_read_b128 v[24:27], v2 offset:1920\ns_waitcnt lgkmcnt(3)\n" \
  "v_mfma_f32_16x16x32_f16 v[12:15], v[104:107], v[28:31], v[12:15]\nds_read_b128 v[124:127], v2 offset:1984\ns_waitcnt lgkmcnt(3)\n" \
  "v_mfma_f32_16x16x32_f16 v[12:15], v[100:103], v[32:35], v[12:15]\nds_read_b128 v[28:31], v2 offset:2048\ns_waitcnt lgkmcnt(3)\n" \
  "v_mfma_f32_16x16x32_f16 v[12:15], v[96:99], v[36:39], v[12:15]\nds_read_b128 v[32:35], v2 offset:2112\ns_waitcnt lgkmcnt(3)\n" \
  "v_mfma_f32_16x16x32_f16 v[12:15], v[92:95], v[24:27], v[12:15]\nds_read_b128 v[36:39], v2 offset:2176\ns_waitcnt lgkmcnt(3)\n" \
  "v_mfma_f32_16x16x32_f16 v[12:15], v[88:91], v[124:127], v[12:15]\nds_read_b128 v[24:27], v2 offset:2240\n" \
  "v_mfma_f32_16x16x32_f16 v[16:19], v[120:123], v[124:127], v[16:19]\nds_read_b64 v[120:121], v6 offset:256\nds_read_b64 v[122:123], v6 offset:264\n" \
  "s_waitcnt lgkmcnt(4)\nv_mfma_f32_16x16x32_f16 v[12:15], v[84:87], v[28:31], v[12:15]\nds_read_b128 v[124:127], v2 offset:2304\n" \
  "v_mfma_f32_16x16x32_f16 v[16:19], v[116:119], v[28:31], v[16:19]\nds_read_b64 v[116:117], v6 offset:320\nds_read_b64 v[118:119], v6 offset:328\n" \
  "v_mfma_f32_16x16x32_f16 v[12:15], v[80:83], v[32:35], v[12:15]\nds_read_b128 v[28:31], v2 offset:2368\n" \
  "v_mfma_f32_16x16x32_f16 v[16:19], v[112:115], v[32:35], v[16:19]\ns_waitcnt lgkmcnt(6)\n" \
  "v_mfma_f32_16x16x32_f16 v[12:15], v[76:79], v[36:39], v[12:15]\nds_read_b128 v[112:115], v2 offset:2432\n" \
  "v_mfma_f32_16x16x32_f16 v[16:19], v[108:111], v[36:39], v[16:19]\nds_read2_b32 v[32:33], v8 offset0:48 offset1:49\n" \
  "ds_read2_b32 v[34:35], v8 offset0:50 offset1:51\nv_mfma_f32_16x16x32_f16 v[12:15], v[72:75], v[24:27], v[12:15]\n" \
  "ds_read_b128 v[108:111], v2 offset:2496\nv_mfma_f32_16x16x32_f16 v[16:19], v[104:107], v[24:27], v[16:19]\n" \
  "ds_read2_b32 v[36:37], v8 offset0:32 offset1:33\nds_read2_b32 v[38:39], v8 offset0:34 offset1:35\ns_waitcnt lgkmcnt(6)\n" \
  "v_mfma_f32_16x16x32_f16 v[12:15], v[68:71], v[124:127], v[12:15]\nds_read_b128 v[104:107], v2 offset:2560\n" \
  "v_mfma_f32_16x16x32_f16 v[16:19], v[100:103], v[124:127], v[16:19]\nds_read_b128 v[24:27], v2 offset:2624\n" \
  "v_mfma_f32_16x16x32_f16 v[12:15], v[64:67], v[28:31], v[12:15]\nds_read_b128 v[100:103], v2 offset:2688\nds_read_b32 v23, v9 offset:32\n" \
  "v_mfma_f32_16x16x32_f16 v[16:19], v[96:99], v[28:31], v[16:19]\nds_read_b128 v[124:127], v2 offset:2752\ns_waitcnt lgkmcnt(7)\n" \
  "v_mfma_f32_16x16x32_f16 v[12:15], v[60:63], v[112:115], v[12:15]\nds_read_b128 v[96:99], v2 offset:2816\n" \
  "v_mfma_f32_16x16x32_f16 v[16:19], v[92:95], v[112:115], v[16:19]\nds_read_b128 v[28:31], v2 offset:2880\n" \
  "v_mfma_f32_16x16x32_f16 v[12:15], v[56:59], v[108:111], v[12:15]\nds_read_b128 v[92:95], v2 offset:2944\n" \
  "v_mfma_f32_16x16x32_f16 v[16:19], v[88:91], v[108:111], v[16:19]\nds_read_b128 v[112:115], v2 offset:3008\ns_waitcnt lgkmcnt(5)\n" \
  "v_readfirstlane_b32 s85, v23\ns_cmp_lt_u32 s85, 3\ns_cbranch_scc1 61f\n62:\nv_mfma_f32_16x16x32_f16 v[16:19], v[84:87], v[104:107], v[16:19]\n" \
  "ds_read_b64_tr_b16 v[88:89], v5 offset:0\nds_read_b64_tr_b16 v[90:91], v5 offset:32\n" \
  "v_mfma_f32_16x16x32_f16 v[16:19], v[80:83], v[24:27], v[16:19]\nds_read_b64_tr_b16 v[108:109], v5 offset:256\n" \
  "ds_read_b64_tr_b16 v[110:111], v5 offset:288\nv_mfma_f32_16x16x32_f16 v[16:19], v[76:79], v[100:103], v[16:19]\n" \
  "ds_read_b64_tr_b16 v[84:85], v5 offset:512\nds_read_b64_tr_b16 v[86:87], v5 offset:544\ns_waitcnt lgkmcnt(9)\n" \
  "v_mfma_f32_16x16x32_f16 v[16:19], v[72:75], v[124:127], v[16:19]\nds_read_b64_tr_b16 v[104:105], v5 offset:768\n" \
  "ds_read_b64_tr_b16 v[106:107], v5 offset:800\nv_mfma_f32_16x16x32_f16 v[16:19], v[68:71], v[96:99], v[16:19]\n" \
  "ds_read_b64_tr_b16 v[80:81], v5 offset:1024\nds_read_b64_tr_b16 v[82:83], v5 offset:1056\ns_waitcnt lgkmcnt(11)\n" \
  "v_mfma_f32_16x16x32_f16 v[16:19], v[64:67], v[28:31], v[16:19]\nds_read_b64_tr_b16 v[24:25], v5 offset:1280\n" \
  "ds_read_b64_tr_b16 v[26:27], v5 offset:1312\nv_mfma_f32_16x16x32_f16 v[16:19], v[60:63], v[92:95], v[16:19]\n" \
  "ds_read_b64_tr_b16 v[76:77], v5 offset:1536\nds_read_b64_tr_b16 v[78:79], v5 offset:1568\ns_waitcnt lgkmcnt(12)\n" \
  "v_mfma_f32_16x16x32_f16 v[16:19], v[56:59], v[112:115], v[16:19]\nds_read_b64_tr_b16 v[100:101], v5 offset:1792\n" \
  "ds_read_b64_tr_b16 v[102:103], v5 offset:1824\nv_mfma_f32_16x16x32_f16 v[12:15], v[88:91], v[52:55], v[12:15]\ns_waitcnt lgkmcnt(10)\n" \
  "v_mfma_f32_16x16x32_f16 v[12:15], v[108:111], v[48:51], v[12:15]\nds_read_b64_tr_b16 v[72:73], v7 offset:0\n" \
  "ds_read_b64_tr_b16 v[74:75], v7 offset:64\nv_mfma_f32_16x16x32_f16 v[12:15], v[84:87], v[44:47], v[12:15]\n" \
  "ds_read_b64_tr_b16 v[124:125], v7 offset:512\nds_read_b64_tr_b16 v[126:127], v7 offset:576\n" \
  "v_mfma_f32_16x16x32_f16 v[16:19], v[84:87], v[52:55], v[16:19]\ns_waitcnt lgkmcnt(10)\n" \
  "v_mfma_f32_16x16x32_f16 v[12:15], v[104:107], v[40:43], v[12:15]\nv_mfma_f32_16x16x32_f16 v[16:19], v[104:107], v[48:51], v[16:19]\n" \
  "v_mfma_f32_16x16x32_f16 v[12:15], v[80:83], v[120:123], v[12:15]\nv_mfma_f32_16x16x32_f16 v[16:19], v[80:83], v[44:47], v[16:19]\n" \
  "s_waitcnt lgkmcnt(6)\nv_mfma_f32_16x16x32_f16 v[12:15], v[24:27], v[116:119], v[12:15]\n" \
  "v_mfma_f32_16x16x32_f16 v[16:19], v[24:27], v[40:43], v[16:19]\nv_mfma_f32_16x16x32_f16 v[16:19], v[76:79], v[120:123], v[16:19]\n" \
  "s_waitcnt lgkmcnt(2)\nv_mfma_f32_16x16x32_f16 v[16:19], v[100:103], v[116:119], v[16:19]\n" \
  "v_mfma_f32_16x16x32_f16 v[12:15], v[72:75], v[32:35], v[12:15]\nv_mfma_f32_16x16x32_f16 v[16:19], v[72:75], v[36:39], v[16:19]\n" \
  "s_waitcnt lgkmcnt(0)\nv_mfma_f32_16x16x32_f16 v[16:19], v[124:127], v[32:35], v[16:19]\ns_nop 4\nv_mul_f32 v12, v10, v12\nv_mul_f32 v13, v10, v13\n" \
  "v_mul_f32 v14, v10, v14\nv_mul_f32 v15, v10, v15\nv_cvt_pk_bf16_f32 v20, v12, v13\nv_cvt_pk_bf16_f32 v21, v14, v15\n" \
  "ds_write_b64 v4, v[20:21] offset:4096\nv_mul_f32 v16, v10, v16\nv_mul_f32 v17, v10, v17\nv_mul_f32 v18, v10, v18\nv_mul_f32 v19, v10, v19\n" \
  "v_cvt_pk_bf16_f32 v20, v16, v17\nv_cvt_pk_bf16_f32 v21, v18, v19\nds_write_b64 v4, v[20:21] offset:5120\ns_branch 8f\n61:\ns_movk_i32 s84, 0x4000\n" \
  "63:\nds_read_b32 v23, v9 offset:32\ns_sub_u32 s84, s84, 1\ns_cbranch_scc1 62b\ns_waitcnt lgkmcnt(0)\nv_readfirstlane_b32 s85, v23\n" \
  "s_cmp_lt_u32 s85, 3\ns_cbranch_scc1 63b\ns_branch 62b\n5:\nds_read_b64 v[124:125], v3 offset:1088\nds_read_b64 v[126:127], v3 offset:1096\n" \
  "ds_read_b64 v[120:121], v3 offset:0\nds_read_b64 v[122:123], v3 offset:8\nds_read_b64 v[116:117], v3 offset:64\n" \
  "ds_read_b64 v[118:119], v3 offset:72\nds_read_b64 v[112:113], v3 offset:128\nds_read_b64 v[114:115], v3 offset:136\n" \
  "ds_read_b64 v[108:109], v3 offset:192\nds_read_b64 v[110:111], v3 offset:200\nds_read_b64 v[104:105], v3 offset:256\n" \
  "ds_read_b64 v[106:107], v3 offset:264\nds_read_b64 v[100:101], v3 offset:320\nds_read_b64 v[102:103], v3 offset:328\n" \
  "ds_read_b64 v[96:97], v3 offset:384\nds_read_b64 v[98:99], v3 offset:392\nds_read_b64 v[92:93], v3 offset:448\n" \
  "ds_read_b64 v[94:95], v3 offset:456\nds_read_b64 v[88:89], v3 offset:512\nds_read_b64 v[90:91], v3 offset:520\n" \
  "ds_read_b64 v[84:85], v3 offset:576\nds_read_b64 v[86:87], v3 offset:584\nds_read_b64 v[80:81], v3 offset:640\n" \
  "ds_read_b64 v[82:83], v3 offset:648\nds_read_b64 v[76:77], v3 offset:704\nds_read_b64 v[78:79], v3 offset:712\n" \
  "ds_read_b64 v[72:73], v3 offset:768\nds_read_b64 v[74:75], v3 offset:776\nds_read_b64 v[68:69], v3 offset:832\n" \
  "ds_read_b64 v[70:71], v3 offset:840\nds_read_b64 v[64:65], v3 offset:896\nds_read_b64 v[66:67], v3 offset:904\n" \
  "ds_read_b64 v[60:61], v3 offset:960\nds_read_b64 v[62:63], v3 offset:968\nds_read_b64 v[56:57], v3 offset:1024\n" \
  "ds_read_b64 v[58:59], v3 offset:1032\nds_read_b64 v[52:53], v6 offset:0\nds_read_b64 v[54:55], v6 offset:8\nds_read_b64 v[48:49], v6 offset:64\n" \
  "ds_read_b64 v[50:51], v6 offset:72\nds_read_b64 v[44:45], v6 offset:128\nds_read_b64 v[46:47], v6 offset:136\nds_read_b64 v[40:41], v6 offset:192\n" \
  "ds_read_b64 v[42:43], v6 offset:200\ns_waitcnt lgkmcnt(0)\ns_barrier\nds_read_b128 v[24:27], v2 offset:1472\n" \
  "ds_read_b128 v[28:31], v2 offset:1984\nds_read_b128 v[32:35], v2 offset:2496\nds_read_b128 v[36:39], v2 offset:2560\ns_waitcnt lgkmcnt(3)\n" \
  "v_mfma_f32_16x16x32_f16 v[12:15], v[124:127], v[24:27], 0\ns_setprio 1\nds_read_b128 v[24:27], v2 offset:2624\ns_waitcnt lgkmcnt(3)\n" \
  "v_mfma_f32_16x16x32_f16 v[16:19], v[124:127], v[28:31], 0\nds_read_b128 v[124:127], v2 offset:2688\ns_waitcnt lgkmcnt(3)\n" \
  "v_mfma_f32_16x16x32_f16 v[12:15], v[120:123], v[32:35], v[12:15]\nds_read_b128 v[28:31], v2 offset:2752\ns_waitcnt lgkmcnt(3)\n" \
  "v_mfma_f32_16x16x32_f16 v[12:15], v[116:119], v[36:39], v[12:15]\nds_read_b128 v[32:35], v2 offset:2816\ns_waitcnt lgkmcnt(3)\n" \
  "v_mfma_f32_16x16x32_f16 v[12:15], v[112:115], v[24:27], v[12:15]\nds_read_b128 v[36:39], v2 offset:2880\ns_waitcnt lgkmcnt(3)\n" \
  "v_mfma_f32_16x16x32_f16 v[12:15], v[108:111], v[124:127], v[12:15]\nds_read_b128 v[24:27], v2 offset:2944\ns_waitcnt lgkmcnt(3)\n" \
  "v_mfma_f32_16x16x32_f16 v[12:15], v[104:107], v[28:31], v[12:15]\nds_read_b128 v[124:127], v2 offset:3008\ns_waitcnt lgkmcnt(3)\n" \
  "v_mfma_f32_16x16x32_f16 v[12:15], v[100:103], v[32:35], v[12:15]\nds_read_b128 v[28:31], v2 offset:3072\ns_waitcnt lgkmcnt(3)\n" \
  "v_mfma_f32_16x16x32_f16 v[12:15], v[96:99], v[36:39], v[12:15]\nds_read_b128 v[32:35], v2 offset:3136\ns_waitcnt lgkmcnt(3)\n" \
  "v_mfma_f32_16x16x32_f16 v[12:15], v[92:95], v[24:27], v[12:15]\nds_read_b128 v[36:39], v2 offset:3200\ns_waitcnt lgkmcnt(3)\n" \
  "v_mfma_f32_16x16x32_f16 v[12:15], v[88:91], v[124:127], v[12:15]\nds_read_b128 v[24:27], v2 offset:3264\n" \
  "v_mfma_f32_16x16x32_f16 v[16:19], v[120:123], v[124:127], v[16:19]\nds_read_b64 v[120:121], v6 offset:256\nds_read_b64 v[122:123], v6 offset:264\n" \
  "s_waitcnt lgkmcnt(4)\nv_mfma_f32_16x16x32_f16 v[12:15], v[84:87], v[28:31], v[12:15]\nds_read_b128 v[124:127], v2 offset:3328\n" \
  "v_mfma_f32_16x16x32_f16 v[16:19], v[116:119], v[28:31], v[16:19]\nds_read_b64 v[116:117], v6 offset:320\nds_read_b64 v[118:119], v6 offset:328\n" \
  "v_mfma_f32_16x16x32_f16 v[12:15], v[80:83], v[32:35], v[12:15]\nds_read_b128 v[28:31], v2 offset:3392\n" \
  "v_mfma_f32_16x16x32_f16 v[16:19], v[112:115], v[32:35], v[16:19]\ns_waitcnt lgkmcnt(6)\n" \
  "v_mfma_f32_16x16x32_f16 v[12:15], v[76:79], v[36:39], v[12:15]\nds_read_b128 v[112:115], v2 offset:3456\n" \
  "v_mfma_f32_16x16x32_f16 v[16:19], v[108:111], v[36:39], v[16:19]\nds_read2_b32 v[32:33], v8 offset0:16 offset1:17\n" \
  "ds_read2_b32 v[34:35], v8 offset0:18 offset1:19\nv_mfma_f32_16x16x32_f16 v[12:15], v[72:75], v[24:27], v[12:15]\n" \
  "ds_read_b128 v[108:111], v2 offset:3520\nv_mfma_f32_16x16x32_f16 v[16:19], v[104:107], v[24:27], v[16:19]\n" \
  "ds_read2_b32 v[36:37], v8 offset0:0 offset1:1\nds_read2_b32 v[38:39], v8 offset0:2 offset1:3\ns_waitcnt lgkmcnt(6)\n" \
  "v_mfma_f32_16x16x32_f16 v[12:15], v[68:71], v[124:127], v[12:15]\nds_read_b128 v[104:107], v2 offset:3584\n" \
  "v_mfma_f32_16x16x32_f16 v[16:19], v[100:103], v[124:127], v[16:19]\nds_read_b128 v[24:27], v2 offset:3648\n" \
  "ds_read2_b32 v[100:101], v8 offset0:32 offset1:33\nds_read2_b32 v[102:103], v8 offset0:34 offset1:35\n" \
  "v_mfma_f32_16x16x32_f16 v[12:15], v[64:67], v[28:31], v[12:15]\nds_read_b128 v[124:127], v2 offset:3712\nds_read_b32 v23, v9 offset:32\n" \
  "v_mfma_f32_16x16x32_f16 v[16:19], v[96:99], v[28:31], v[16:19]\nds_read_b128 v[96:99], v2 offset:3776\ns_waitcnt lgkmcnt(9)\n" \
  "v_mfma_f32_16x16x32_f16 v[12:15], v[60:63], v[112:115], v[12:15]\nds_read_b128 v[28:31], v2 offset:3840\n" \
  "v_mfma_f32_16x16x32_f16 v[16:19], v[92:95], v[112:115], v[16:19]\nds_read_b128 v[92:95], v2 offset:3904\n" \
  "v_mfma_f32_16x16x32_f16 v[12:15], v[56:59], v[108:111], v[12:15]\nds_read_b128 v[112:115], v2 offset:3968\n" \
  "v_mfma_f32_16x16x32_f16 v[16:19], v[88:91], v[108:111], v[16:19]\nds_read_b128 v[88:91], v2 offset:4032\ns_waitcnt lgkmcnt(5)\n" \
  "v_readfirstlane_b32 s85, v23\ns_cmp_lt_u32 s85, 3\ns_cbranch_scc1 71f\n72:\nv_mfma_f32_16x16x32_f16 v[16:19], v[84:87], v[104:107], v[16:19]\n" \
  "ds_read_b64_tr_b16 v[108:109], v5 offset:1024\nds_read_b64_tr_b16 v[110:111], v5 offset:1056\n" \
  "v_mfma_f32_16x16x32_f16 v[16:19], v[80:83], v[24:27], v[16:19]\nds_read_b64_tr_b16 v[84:85], v5 offset:1280\n" \
  "ds_read_b64_tr_b16 v[86:87], v5 offset:1312\nv_mfma_f32_16x16x32_f16 v[16:19], v[76:79], v[124:127], v[16:19]\n" \
  "ds_read_b64_tr_b16 v[104:105], v5 offset:1536\nds_read_b64_tr_b16 v[106:107], v5 offset:1568\ns_waitcnt lgkmcnt(9)\n" \
  "v_mfma_f32_16x16x32_f16 v[16:19], v[72:75], v[96:99], v[16:19]\nds_read_b64_tr_b16 v[80:81], v5 offset:1792\n" \
  "ds_read_b64_tr_b16 v[82:83], v5 offset:1824\nv_mfma_f32_16x16x32_f16 v[16:19], v[68:71], v[28:31], v[16:19]\n" \
  "ds_read_b64_tr_b16 v[24:25], v5 offset:2048\nds_read_b64_tr_b16 v[26:27], v5 offset:2080\ns_waitcnt lgkmcnt(11)\n" \
  "v_mfma_f32_16x16x32_f16 v[16:19], v[64:67], v[92:95], v[16:19]\nds_read_b64_tr_b16 v[76:77], v5 offset:2304\n" \
  "ds_read_b64_tr_b16 v[78:79], v5 offset:2336\nv_mfma_f32_16x16x32_f16 v[16:19], v[60:63], v[112:115], v[16:19]\n" \
  "ds_read_b64_tr_b16 v[124:125], v5 offset:2560\nds_read_b64_tr_b16 v[126:127], v5 offset:2592\ns_waitcnt lgkmcnt(12)\n" \
  "v_mfma_f32_16x16x32_f16 v[16:19], v[56:59], v[88:91], v[16:19]\nds_read_b64_tr_b16 v[72:73], v5 offset:2816\n" \
  "ds_read_b64_tr_b16 v[74:75], v5 offset:2848\nv_mfma_f32_16x16x32_f16 v[12:15], v[108:111], v[52:55], v[12:15]\ns_waitcnt lgkmcnt(10)\n" \
  "v_mfma_f32_16x16x32_f16 v[12:15], v[84:87], v[48:51], v[12:15]\nds_read_b64_tr_b16 v[96:97], v7 offset:0\n" \
  "ds_read_b64_tr_b16 v[98:99], v7 offset:64\nv_mfma_f32_16x16x32_f16 v[12:15], v[104:107], v[44:47], v[12:15]\n" \
  "ds_read_b64_tr_b16 v[68:69], v7 offset:512\nds_read_b64_tr_b16 v[70:71], v7 offset:576\n" \
  "v_mfma_f32_16x16x32_f16 v[16:19], v[104:107], v[52:55], v[16:19]\ns_waitcnt lgkmcnt(10)\n" \
  "v_mfma_f32_16x16x32_f16 v[12:15], v[80:83], v[40:43], v[12:15]\nds_read_b64_tr_b16 v[28:29], v7 offset:1024\n" \
  "ds_read_b64_tr_b16 v[30:31], v7 offset:1088\nv_mfma_f32_16x16x32_f16 v[16:19], v[80:83], v[48:51], v[16:19]\n" \
  "ds_read2_b32 v[64:65], v8 offset0:48 offset1:49\nds_read2_b32 v[66:67], v8 offset0:50 offset1:51\n" \
  "v_mfma_f32_16x16x32_f16 v[12:15], v[24:27], v[120:123], v[12:15]\nv_mfma_f32_16x16x32_f16 v[16:19], v[24:27], v[44:47], v[16:19]\n" \
  "s_waitcnt lgkmcnt(10)\nv_mfma_f32_16x16x32_f16 v[12:15], v[76:79], v[116:119], v[12:15]\nds_read_b64_tr_b16 v[92:93], v7 offset:1536\n" \
  "ds_read_b64_tr_b16 v[94:95], v7 offset:1600\nv_mfma_f32_16x16x32_f16 v[16:19], v[76:79], v[40:43], v[16:19]\n" \
  "v_mfma_f32_16x16x32_f16 v[16:19], v[124:127], v[120:123], v[16:19]\ns_waitcnt lgkmcnt(8)\n" \
  "v_mfma_f32_16x16x32_f16 v[16:19], v[72:75], v[116:119], v[16:19]\nv_mfma_f32_16x16x32_f16 v[12:15], v[96:99], v[32:35], v[12:15]\n" \
  "v_mfma_f32_16x16x32_f16 v[16:19], v[96:99], v[36:39], v[16:19]\ns_waitcnt lgkmcnt(4)\n" \
  "v_mfma_f32_16x16x32_f16 v[12:15], v[68:71], v[100:103], v[12:15]\nv_mfma_f32_16x16x32_f16 v[16:19], v[68:71], v[32:35], v[16:19]\n" \
  "s_waitcnt lgkmcnt(2)\nv_mfma_f32_16x16x32_f16 v[12:15], v[28:31], v[64:67], v[12:15]\n" \
  "v_mfma_f32_16x16x32_f16 v[16:19], v[28:31], v[100:103], v[16:19]\ns_waitcnt lgkmcnt(0)\n" \
  "v_mfma_f32_16x16x32_f16 v[16:19], v[92:95], v[64:67], v[16:19]\ns_nop 4\nv_mul_f32 v12, v10, v12\nv_mul_f32 v13, v10, v13\n" \
  "v_mul_f32 v14, v10, v14\nv_mul_f32 v15, v10, v15\nv_cvt_pk_bf16_f32 v20, v12, v13\nv_cvt_pk_bf16_f32 v21, v14, v15\n" \
  "ds_write_b64 v4, v[20:21] offset:6144\nv_mul_f32 v16, v10, v16\nv_mul_f32 v17, v10, v17\nv_mul_f32 v18, v10, v18\nv_mul_f32 v19, v10, v19\n" \
  "v_cvt_pk_bf16_f32 v20, v16, v17\nv_cvt_pk_bf16_f32 v21, v18, v19\nds_write_b64 v4, v[20:21] offset:7168\ns_branch 8f\n71:\ns_movk_i32 s84, 0x4000\n" \
  "73:\nds_read_b32 v23, v9 offset:32\ns_sub_u32 s84, s84, 1\ns_cbranch_scc1 72b\ns_waitcnt lgkmcnt(0)\nv_readfirstlane_b32 s85, v23\n" \
  "s_cmp_lt_u32 s85, 3\ns_cbranch_scc1 73b\ns_branch 72b\n8:\nds_read_b128 v[16:19], v0\ns_waitcnt lgkmcnt(0)\nv_min_u32 v16, -2, v16\n" \
  "v_min_u32 v17, -2, v17\nv_min_u32 v18, -2, v18\nv_min_u32 v19, -2, v19\nglobal_store_dwordx4 v1, v[16:19], s[94:95] sc1\ns_setprio 0\n" \
  "s_setpc_b64 s[90:91]\n"
#define CONV_CLOB "memory", "scc", "s84", "s85", "s90", "s91", "v12", "v13", "v14", "v15", "v16", "v17", "v18", "v19", "v20", "v21", "v22", "v23", "v24", "v25", "v26", "v27", "v28", "v29", "v30", "v31", "v32", "v33", "v34", "v35", "v36", "v37", "v38", "v39", "v40", "v41", "v42", "v43", "v44", "v45", "v46", "v47", "v48", "v49", "v50", "v51", "v52", "v53", "v54", "v55", "v56", "v57", "v58", "v59", "v60", "v61", "v62", "v63", "v64", "v65", "v66", "v67", "v68", "v69", "v70", "v71", "v72", "v73", "v74", "v75", "v76", "v77", "v78", "v79", "v80", "v81", "v82", "v83", "v84", "v85", "v86", "v87", "v88", "v89", "v90", "v91", "v92", "v93", "v94", "v95", "v96", "v97", "v98", "v99", "v100", "v101", "v102", "v103", "v104", "v105", "v106", "v107", "v108", "v109", "v110", "v111", "v112", "v113", "v114", "v115", "v116", "v117", "v118", "v119", "v120", "v121", "v122", "v123", "v124", "v125", "v126", "v127"
constexpr int TDP = 1216, T_3 = 4 * TDP, T_4 = T_3 + 512, TB = T_4 + 320;
constexpr int C_U = 480, UB = C_U + 4096 + 96, C_G5 = 0, C_G3 = 3200, C_G9 = 6400, C_G7 = C_G9 + 2096, GB = C_G7 + 2080 + 112;
constexpr int O_FL = 2 * TB, O_UL = O_FL + 64, O_IM = O_UL + 2 * UB, O_ST = O_IM + 2 * GB;
constexpr int X_TB = 10240, X_UL = X_TB + TB, X_IM = X_UL + UB, X_ST = X_IM + GB;
constexpr int RP = 1216;
constexpr int LW = 36864 + 512, LA = LW, LM = LW + 36864;
constexpr int SMEM = O_IM + 256 * IP;
static_assert(LM + 2048 <= SMEM && O_ST + 8192 <= SMEM && X_IM + 128 * IP <= LM && X_ST + 8192 <= LM && UB % 128 == 64 && GB % 128 == 64 && SMEM <= 79872, "");
struct TOps { uint32_t xa, xb, ya, pd, pf, pg, pe; };
__device__ __forceinline__ void table_ops(uint32_t im, uint32_t tb, int ct, int lane, TOps& t) {
  const int j = lane & 15, kq = lane >> 4, q4 = (lane >> 2) & 3, p4 = lane & 3, jj = j + 16 * ct;
  const uint32_t sa = im + (uint32_t)(IP * (lane >> 2));
  t.xa = sa + (uint32_t)(I_X + 8 * p4); t.xb = sa + (uint32_t)(I_X + 32 + 8 * (p4 & 1)); t.ya = sa + (uint32_t)(32 * ct + 8 * p4);
  t.pd = tb + (uint32_t)(2 * (541 - (8 * jj + 4 * (kq & 1) + 256 * (kq >> 1))));
  t.pf = tb + (uint32_t)((kq & 1) ? T_4 + 2 * (63 - jj) : T_3 + 2 * (127 - 2 * jj));
  t.pg = t.pf - ((kq & 1) ? 64 : 2);
  t.pe = tb + (uint32_t)(p4 * (TDP + 2) + ((lane < 12) ? 2 * (32 - q4) : 2 * (572 + (q4 & 1))));
  asm volatile("" : "+v"(t.xa), "+v"(t.xb), "+v"(t.ya), "+v"(t.pd), "+v"(t.pf), "+v"(t.pg), "+v"(t.pe));
}
typedef __attribute__((address_space(3))) char* lptr;
#define LP(T, a) ((__attribute__((address_space(3))) T*)(uintptr_t)(a))
__device__ __forceinline__ float table_tile(const TOps& t, int ct, int lane, uint32_t evp, float dsk TSP) {
  u32x4 ev;
  f32x4 ad[2], af[2];
  asm volatile(T_ASM : "=&v"(ad[0]), "=&v"(ad[1]), "=&v"(af[0]), "=&v"(af[1]), "=&v"(ev) : "v"(t.xa), "v"(t.xb), "v"(t.ya), "v"(evp) : T_CLOB);
  const int e0 = (int)(ev[0] + ev[2]), e1 = (int)(ev[1] + ev[3]);
  const int em = min(max(max(max(e0, e1), (int)((__float_as_uint(dsk) >> 23) & 0xffu) - 133), -110), 115);
  float s0 = __builtin_amdgcn_ldexpf(1.f, e0 - 22 - em), s1 = __builtin_amdgcn_ldexpf(1.f, e1 - 22 - em);
  float d0 = (ct == 0 && lane == 0) ? __builtin_amdgcn_ldexpf(dsk, 7 - em) : 0.f;
  asm volatile("s_nop 1" : "+v"(ad[0]), "+v"(ad[1]), "+v"(af[0]), "+v"(af[1]), "+v"(s0), "+v"(s1), "+v"(d0));
  const f32x4 k = ad[0] * s0 + (f32x4){d0, 0.f, 0.f, 0.f} + ad[1] * s1, f = af[0] * s0 + af[1] * s1;
  const uint32_t p32 = packh(k[3], k[2]), p10 = packh(k[1], k[0]), p21 = packh(k[2], k[1]), p01 = packh(f[0], f[1]), p23 = packh(f[2], f[3]);
  *LP(u32x2, t.pd + 3 * TDP + 6) = (u32x2){p32, p10};
  *LP(uint32_t, t.pd + TDP + 2) = p32; *LP(uint32_t, t.pd + TDP + 6) = p10;
  *LP(uint32_t, t.pd + 2) = p21; *LP(uint32_t, t.pd + 2 * TDP + 6) = p21;
  *LP(u16, t.pd) = (u16)p32; *LP(u16, t.pd + 2 * TDP + 4) = (u16)p32;
  *LP(u16, t.pd + 6) = (u16)(p10 >> 16); *LP(u16, t.pd + 2 * TDP + 10) = (u16)(p10 >> 16);
  if (ct) {
    const uint32_t va = (uint32_t)__builtin_amdgcn_readlane((int)p32, 63), vb = (uint32_t)__builtin_amdgcn_readlane((int)p23, 15) >> 16;
    *LP(u16, t.pe) = (u16)((lane < 12) ? va : vb);
  }
  if (lane < 32) {
    *LP(u16, t.pf + 128) = (u16)p01; *LP(u16, t.pg + 128) = (u16)(p01 >> 16);
    *LP(u16, t.pf) = (u16)p23; *LP(u16, t.pg) = (u16)(p23 >> 16);
  }
  return __int_as_float((127 - 2 + em) << 23);
}
__device__ __forceinline__ uint32_t hadd(uint32_t a, uint32_t b) { return __builtin_bit_cast(uint32_t, __builtin_bit_cast(f16x2, a) + __builtin_bit_cast(f16x2, b)); }
template <bool FULL> __device__ __forceinline__ void sums(char* sm, const u32x4 p, const u32x4 c) {
  const uint32_t D[8] = {p[0], p[1], p[2], p[3], c[0], c[1], c[2], c[3]};
  uint32_t O[7];
#pragma unroll
  for (int j = 0; j < 7; ++j) O[j] = __builtin_amdgcn_alignbit(D[j + 1], D[j], 16);
  u32x4 a, b;
#pragma unroll
  for (int j = 0; j < 4; ++j) a[j] = hadd(D[j + 2], hadd(O[j + 1], O[j + 2]));
  *(u32x4*)(sm + C_G3 + 32) = a;
#pragma unroll
  for (int j = 0; j < 4; ++j) b[j] = hadd(a[j], hadd(D[j + 1], D[j + 3]));
  *(u32x4*)(sm + C_G5 + 32) = b;
  if (FULL) {
#pragma unroll
    for (int j = 0; j < 4; ++j) a[j] = hadd(b[j], hadd(O[j], O[j + 3]));
    *(u32x4*)(sm + C_G7 + 32) = a;
#pragma unroll
    for (int j = 0; j < 4; ++j) b[j] = hadd(a[j], hadd(D[j], D[j + 4]));
    *(u32x4*)(sm + C_G9 + 32) = b;
  }
}
__device__ __forceinline__ void count_up(uint32_t a) {
  uint32_t t;
  asm volatile("s_mov_b64 exec, 1\n\tv_mov_b32 %0, 1\n\tds_add_u32 %1, %0\n\ts_mov_b64 exec, -1" : "=&v"(t) : "v"(a) : "memory");
}
__device__ __forceinline__ int pad_at(int r) { return 16 * (r & 1) + 3200 * (r >> 1) - ((r >= 6) ? 3200 - 2096 : 0); }
struct COps { uint32_t b, a, o, g3, k3, g4, k4, f; };
__device__ __forceinline__ void conv_ops(uint32_t tb, uint32_t ub, uint32_t cb, int lane, COps& e) {
  const uint32_t j = lane & 15, kq = lane >> 4, q4 = (lane >> 2) & 3, p4 = lane & 3;
  e.b = ub + 32u * j + 16u * kq;
  e.a = tb + p4 * TDP + 32u - 8u * q4 + 16u * kq;
  e.g3 = cb + C_G5 + 8 + 64u * kq + 8u * (q4 + p4) + (q4 & 1u) * 3200u;
  e.g4 = cb + C_G9 + 8 + 128u * kq + 16u * q4 + 8u * p4 + (q4 & 1u) * 2096u;
  e.k3 = tb + T_3 + 120 - 8u * j + 16u * kq;
  e.k4 = tb + T_4 + 60 - 4u * j + 16u * kq;
  asm volatile("" : "+v"(e.b), "+v"(e.a), "+v"(e.g3), "+v"(e.g4), "+v"(e.k3), "+v"(e.k4));
}
#define CONV_RUN(prog, e, ys, ...) do { int prog_ = (prog); \
    asm volatile("s_call_b64 s[90:91], kf_conv" : "+{s89}"(prog_), "+{v2}"(e.b), "+{v3}"(e.a), "+{v4}"(e.o), "+{v5}"(e.g3), "+{v6}"(e.k3), "+{v7}"(e.g4), \
                 "+{v8}"(e.k4), "+{v9}"(e.f), "+{v10}"(ys) __VA_ARGS__ : : CONV_CLOB); } while (0)

template <class F>
__device__ __forceinline__ void ln_two(const uint32_t* v, int lane, float sc, uint32_t* c, f32x2* t, f32x4 z, F&& mid) {
  float s1a = z[0], s2a = z[1], s1b = z[2], s2b = z[3];
#pragma unroll
  for (int k = 0; k < 4; ++k) {
    s1a = dot2(v[k], 0x3f803f80u, s1a); s2a = dot2(v[k], v[k], s2a);
    s1b = dot2(v[4 + k], 0x3f803f80u, s1b); s2b = dot2(v[4 + k], v[4 + k], s2b);
  }
#pragma unroll
  for (int k = 0; k < 9; ++k) t[k] = (f32x2){bflo(v[k]), bfhi(v[k])};
  const float rs = row_sum(fold16(dot2(v[8], 0x3f803f80u, fold32(s1a, s1b)), dot2(v[8], v[8], fold32(s2a, s2b))));
  const f32x2 mean = (f32x2){rdl(rs, 15), rdl(rs, 47)} * (1.f / D_), m2 = mean * mean;
  f32x2 var = (f32x2){rdl(rs, 31), rdl(rs, 63)} * (1.f / D_) - m2;
  asm volatile("" : "+v"(t[0]), "+v"(t[1]), "+v"(t[2]), "+v"(t[3]), "+v"(t[4]), "+v"(t[5]), "+v"(t[6]), "+v"(t[7]), "+v"(t[8]));
  if (__builtin_expect(!(m2[0] <= 16.f * var[0] && m2[1] <= 16.f * var[1]), 0)) {
    float q[3] = {0.f, 0.f, 0.f};
#pragma unroll
    for (int k = 0; k < 9; ++k) {
      const f32x2 a = t[k] - ((k < 4 || (k == 8 && lane < 32)) ? mean[0] : mean[1]);
      q[k >> 2] += a[0] * a[0] + a[1] * a[1];
    }
    float qa = q[0] + ((lane < 32) ? q[2] : 0.f), qb = q[1] + ((lane < 32) ? 0.f : q[2]);
    wave_sum2(qa, qb);
    var = (f32x2){qa, qb} * (1.f / D_);
  }
  mid();
  const uint32_t mp = pack2(mean[0], mean[1]), vp = pack2(var[0], var[1]);
  const f32x2 ve = (f32x2){bflo(vp), bfhi(vp)} + 1e-5f;
  const uint32_t e2 = pack2(ve[0], ve[1]);
  const uint32_t sp = pack2(__builtin_amdgcn_sqrtf(bflo(e2)), __builtin_amdgcn_sqrtf(bfhi(e2)));
  const f32x2 rc = (f32x2){__builtin_amdgcn_rcpf(bflo(sp)), __builtin_amdgcn_rcpf(bfhi(sp))} * sc;
  const float nm[3] = {bflo(mp), bfhi(mp), (lane < 32) ? bflo(mp) : bfhi(mp)}, rr[3] = {rc[0], rc[1], (lane < 32) ? rc[0] : rc[1]};
#pragma unroll
  for (int k = 0; k < 9; ++k) { const f32x2 d = t[k] - nm[k >> 2]; c[k] = pack2(d[0], d[1]); }
#pragma unroll
  for (int k = 0; k < 9; ++k) { const f32x2 d = (f32x2){bflo(c[k]), bfhi(c[k])} * rr[k >> 2]; c[k] = pack2(d[0], d[1]); }
}
__device__ __forceinline__ uint32_t med3u(uint32_t a, uint32_t b, uint32_t c) { return max(min(a, b), min(max(a, b), c)); }
__device__ __forceinline__ uint32_t oct_maxu(uint32_t v) {
  v = max(v, (uint32_t)DPP_I((int)v, 0xb1, 0xf));
  v = max(v, (uint32_t)DPP_I((int)v, 0x4e, 0xf));
  v = max(v, (uint32_t)DPP_I((int)v, 0x141, 0xf));
  return v;
}

#define KARGS const u16* __restrict__ x, const u16* __restrict__ Are, const u16* __restrict__ Aim, const u16* __restrict__ Bre, \
    const u16* __restrict__ Bim, const u16* __restrict__ Cre, const u16* __restrict__ Cim, const u16* __restrict__ logdt, \
    const u16* __restrict__ Dskip, char* __restrict__ ws, const u16* __restrict__ wr, const u16* __restrict__ wg, \
    const u16* __restrict__ wu, const u16* __restrict__ wd, u16* __restrict__ out
#define KPASS x, Are, Aim, Bre, Bim, Cre, Cim, logdt, Dskip, ws, wr, wg, wu, wd, out
template <int ROLE>
__device__ __forceinline__ void role_body(char* smem, KARGS) {
  const int tid = threadIdx.x, lane = tid & 63;
  const int w = __builtin_amdgcn_readfirstlane(tid >> 6), wl = w & 3;
  const int b = blockIdx.x & 255, bx = b & 7, bj = b >> 3;
  constexpr bool isl = ROLE == 1 || ROLE == 2;
  const int tg = isl ? 32 * (bj >> 2) + 4 * bx + (bj & 3) : 32 * bx + bj;
  u32x4 xa, xc;
  uint32_t ep, r0;
#define P_WAIT(n, ...) asm volatile("s_waitcnt vmcnt(" #n ")\n\tv_lshlrev_b32 %0, 16, v100\n\tv_lshlrev_b32 %1, 16, v101\n\tv_lshlrev_b32 %2, 16, v102" \
    : "=v"(ldt), "=v"(are), "=v"(aim) : : "v100", "v101", "v102" __VA_ARGS__)
#define P_MORE() asm volatile("v_lshlrev_b32 %0, 16, v103\n\tv_lshlrev_b32 %1, 16, v104\n\tv_lshlrev_b32 %2, 16, v105\n\tv_lshlrev_b32 %3, 16, v106" \
    : "=v"(bre), "=v"(bim), "=v"(cre), "=v"(cim) : : "v103", "v104", "v105", "v106")
  TSD;
  TS(0, 0);
  const uint32_t ck0 = (ROLE == 0) ? 0u : (uint32_t)__builtin_amdgcn_s_memtime(), rt0 = (ROLE == 0) ? 0u : (uint32_t)__builtin_amdgcn_s_memrealtime();
  const uint32_t lb0 = (uint32_t)(uintptr_t)(__attribute__((address_space(3))) char*)smem;
  const int i = lane & 15, kq = lane >> 4;
  const u32x4 ones = {0xffffffffu, 0xffffffffu, 0xffffffffu, 0xffffffffu}, zero4 = {0u, 0u, 0u, 0u};
  auto u_again = [&](u32x4& uw, const u32x4& ut, uint32_t uo) {
#pragma unroll 1
    for (int spin = 0; spin < SPIN_CAP; ++spin) {
      const bool na = ((uw[0] | uw[1] | uw[2] | uw[3]) & UMASK) != 0u;
      if (__builtin_expect(!__any(na), 1)) break;
      TSC(19);
      SLEEP(USLEEP);
      if (na) { LD16W(uw, uo); uw ^= ut; }
    }
  };
  if constexpr (!isl) {
    const int ch = wl >> 1, tl = tid & 255;
    int* evp = (int*)(smem + O_FL + 16 * ch);
    float ldt, are, aim, dsk;
    if constexpr (ROLE == 0) {
      __builtin_amdgcn_s_setprio(KPRIO);
      COps e;
      conv_ops(lb0 + (uint32_t)(ch * TB), lb0 + (uint32_t)(O_UL + ch * UB), lb0 + (uint32_t)(O_IM + ch * GB), lane, e);
      e.o = lb0 + O_ST + (uint32_t)(16 * ((4 * i + 2 * (kq >> 1) + ch) ^ (i >> 1)) + 8 * (kq & 1));
      e.f = lb0 + O_FL;
      TOps t;
      table_ops(lb0 + (uint32_t)(O_IM + 128 * IP * ch), lb0 + (uint32_t)(TB * ch), wl & 1, lane, t);
      uint32_t pz = lb0 + O_IM + (uint32_t)(((lane >> 3) & 1) * GB + pad_at(lane & 7)), im = lb0 + (uint32_t)(O_IM + IP * tl);
      uint32_t sg = O_SV + 128u * tg + (uint32_t)(lane >> 3) * (320 * 128) + 16u * (lane & 7);
      asm volatile("" : "+v"(e.o), "+v"(e.f), "+v"(pz), "+v"(im), "+v"(sg));
      float bre, bim, cre, cim;
      if (w == 3) {
        SLEEP(RSL);
#pragma unroll
        for (int k = 0; k < 8; ++k) ST16(O_V + (uint32_t)tg * VP + 16u * lane + 1024u * k, ones);
        asm volatile("s_mov_b64 exec, 0xff\n\tglobal_store_dwordx4 %0, %1, %2 sc1\n\ts_mov_b64 exec, -1" : : "v"(O_KEY + 128u * b + 16u * lane), "v"(ones), "s"(ws) : "memory");
      }
      asm volatile("s_waitcnt vmcnt(9)\n\ts_cmp_eq_u32 %0, 3\n\ts_cbranch_scc1 1f\n\ts_waitcnt vmcnt(0)\n1:" : : "s"(w) : "scc");
      P_WAIT(9, , "v103", "v104", "v105", "v106", "v107", "v109");
      P_MORE();
      asm volatile("v_lshlrev_b32 %0, 16, v107" : "=v"(dsk) : : "v107");
      TS(0, 1);
      evp[wl & 1] = gen_y(ldt, are, aim, bre, bim, cre, cim, im, [&] {
        if (w == 3) {
          asm volatile("s_waitcnt vmcnt(0)" : "={v109}"(ep));
          const uint32_t T = ((uint32_t)__builtin_amdgcn_readfirstlane(ep) + 1u) * 0x9E3779B1u, t_ = (uint32_t)__builtin_amdgcn_s_memrealtime() + VD;
          ST16(sg, ((u32x4){T, t_, T, t_}));
        }
      } TSA);
      TS(0, 8);
      BAR();
      TS(0, 2);
      float ys = table_tile(t, wl & 1, lane, lb0 + O_FL + 16 * ch, dsk TSA);
      TS(0, 9);
      gsync0(lb0 + O_FL + 36, 4);
      TS(0, 3);
      *(u32x4*)(smem + O_ST + 16 * tl) = ones;
      *(u32x4*)(smem + O_ST + 4096 + 16 * tl) = ones;
      if (w == 0) *LP(u32x4, pz) = zero4;
      CONV_RUN(wl & 1, e, ys);
      TS(0, 5);
    } else {
      __builtin_amdgcn_s_setprio(UPRIO);
#pragma unroll
      for (int k = 0; k < 3; ++k) *(u32x4*)(smem + ((16 * (tl + 256 * k) < O_FL) ? 16 * (tl + 256 * k) : O_FL + 32)) = zero4;
      if (w == 7) *(u32x4*)(smem + O_UL + (lane >> 5) * UB + 16 * min(lane & 31, 29)) = zero4;
      uint32_t uo = u_at(tl >> 1, 2 * tg + (tid & 1)), go = O_SU + (uint32_t)(256 * bx + 32 * wl + (lane & 31) + 4 * (lane & 32)) * 128u;
      uint32_t ul = (uint32_t)(O_UL + (tid & 1) * UB + C_U - 16 + 16 * (tl >> 1)), ug = (uint32_t)(O_IM + (tid & 1) * GB + 16 * (tl >> 1)), so = lb0 + O_ST + 16u * (uint32_t)(lane ^ (lane >> 3));
      uint32_t vo = O_V + (uint32_t)tg * VP + 16u * lane;
      asm volatile("" : "+v"(uo), "+v"(go), "+v"(ul), "+v"(ug), "+v"(so), "+v"(vo));
      P_WAIT(1, , "v109");
      TS(0, 1);
      evp[2 + (wl & 1)] = gen_x(ldt, are, aim, smem + O_IM + IP * tl);
      asm volatile("s_waitcnt vmcnt(0)" : "={v109}"(ep));
      const uint32_t T = ((uint32_t)__builtin_amdgcn_readfirstlane(ep) + 1u) * 0x9E3779B1u;
      TS(0, 8);
      BAR();
      TS(0, 2);
      u32x4 ut = {utag(T, 0), utag(T, 1), utag(T, 2), utag(T, 3)};
      asm volatile("" : "+v"(ut));
      SLEEP(GSLEEP);
      u32x2 pv;
      pv[1] = gate(ws, go, T);
      TS(0, 3);
      until(latest(pv[1]) + UD);
      TS(0, 7);
      u32x4 uw, ux;
      asm volatile("global_load_dwordx4 %0, %2, %4 sc1\n\tglobal_load_dwordx4 %1, %3, %4 sc1"
                   : "=&v"(uw), "=&v"(ux) : "v"(uo), "v"(uo + 64 * UP * 32), "s"(ws) : "memory");
      __builtin_amdgcn_s_setprio(FPR);
      asm volatile("s_waitcnt vmcnt(1)" : "+v"(uw));
      uw ^= ut;
      *(u32x4*)(smem + ul + 16) = uw;
      asm volatile("s_waitcnt vmcnt(0)" : "+v"(ux));
      TS(0, 11);
      ux ^= ut;
      *(u32x4*)(smem + ul + 2048 + 16) = ux;
      if (__builtin_expect(__any(((uw[0] | uw[1] | uw[2] | uw[3] | ux[0] | ux[1] | ux[2] | ux[3]) & UMASK) != 0u), 0)) {
        u_again(uw, ut, uo);
        u_again(ux, ut, uo + 64 * UP * 32);
        *(u32x4*)(smem + ul + 16) = uw;
        *(u32x4*)(smem + ul + 2048 + 16) = ux;
      }
      BAR();
      TS(0, 4);
      const u32x4 pw = *(const u32x4*)(smem + ul);
      if (wl < 2) {
        const u32x4 px = *(const u32x4*)(smem + ul + 2048);
        sums<true>(smem + ug, pw, uw);
        sums<false>(smem + ug + 2048, px, ux);
      } else sums<true>(smem + ug, pw, uw);
      count_up(lb0 + O_FL + 32);
      __builtin_amdgcn_s_setprio(SPR);
      TS(0, 5);
#pragma unroll
      for (int k = 0; k < 2; ++k) {
        const uint32_t o = ((k ? 0x3247u : 0x1065u) >> (4 * wl) & 7u) * 1024u;
        u32x4 v;
#pragma unroll 1
        for (int spin = 0; spin < SPIN_CAP; ++spin) {
          asm volatile("ds_read_b128 %0, %1\n\ts_waitcnt lgkmcnt(0)" : "=&v"(v) : "v"(so + o) : "memory");
          if (!__any(max4(v) == 0xffffffffu)) break;
          __builtin_amdgcn_s_sleep(1);
          if (spin == SPIN_CAP - 1) v &= 0xfffffffeu;
        }
        TS(0, 12 + k);
        ST16(vo + o, v);
        TS(0, 10 - 4 * k);
      }
    }
    TSE();
  } else {
  uint32_t* cand = (uint32_t*)(smem + LM);
  uint32_t* sel = cand + 128;
  uint32_t* kl = sel + 48;
  uint32_t* cnt = kl + 8;
  const uint32_t ca = lb0 + LM + 4 * (128 + 48 + 8);
  u16* hm = (u16*)(cnt + 12);
  uint32_t T;
  const bool ffn = bj < 18, isx = bj >= 24;
  const int fb = 18 * bx + bj;
  u32x4 bw[18];
#pragma unroll
  for (int s = 0; s < 9; ++s) bw[s] = (u32x4){0u, 0u, 0u, 0u};
  int tid2 = threadIdx.x;
  auto gu_dma = [&](int q0, int q1, auto depth) {
    const char* gs = (const char*)((w < 2) ? wg : wu) + (size_t)((144 * (w & 1)) * (F_ * 2) + fb * 64);
    const uint32_t gv = (uint32_t)((lane >> 2) * (F_ * 2) + 16 * ((lane & 3) ^ (lane >> 5) ^ (w & 2)));
#pragma unroll 1
    for (int q = q0; q < q1; ++q) {
      const int k = (int)((((bj & 1) ? ORD1 : ((unsigned __int128)0x876543210u << 36 | 0x876543210ull)) >> (4 * (q - (q >= 9 ? 9 : 0)) + (q >= 9 ? 36 : 0))) & 15u), h = q >= 9;
      dma16(gs + (size_t)(288 * h + 16 * k) * (F_ * 2), gv, lb0 + LW * h + 9216 * w + 1024 * k);
      asm volatile("s_waitcnt vmcnt(%0)" : : "n"(depth.value) : "memory");
    }
  };
  auto down_dma = [&] {
#pragma unroll 1
    for (int k = 0; k < 4; ++k) {
      const int o = 9216 * wl + 2304 * k;
      const char* ds = (const char*)wd + (size_t)(fb * 36864 + o);
      const uint32_t dl = lb0 + o + 32 * ((4 * wl + k + 1) >> 1);
      dma16(ds, 16u * lane, dl);
      dma16(ds + 1024, 16u * lane, dl + 1024);
      uint32_t keep;
      asm volatile("s_mov_b32 %0, m0\n\ts_mov_b32 m0, %3\n\ts_mov_b64 exec, 0xffff\n\tglobal_load_lds_dwordx4 %1, %2" DPOL "\n\ts_mov_b64 exec, -1\n\ts_mov_b32 m0, %0\n\ts_waitcnt vmcnt(%4)"
                   : "=&s"(keep) : "v"(16u * lane), "s"(ds + 2048), "s"(dl + 2048), "n"(DDEPTH) : "memory");
    }
  };
  auto gu_read = [&](int h) {
    const uint32_t ra = lb0 + LW * h + (uint32_t)(((lane >> 1) & 1) * 18432 + (8 * kq + ((lane >> 2) & 3)) * 64 + ((w ^ (kq & 1) ^ (lane & 2)) * 16) + 8 * (lane & 1));
#pragma unroll
    for (int s = 0; s < 9; ++s) {
      const u32x2 h0 = trd(ra + 2048 * s), h1 = trd(ra + 2048 * s + 256);
      bw[9 * h + s] = (u32x4){h0[0], h0[1], h1[0], h1[1]};
    }
#pragma unroll
    for (int s = 0; s < 9; ++s) asm volatile("" : "+v"(bw[9 * h + s]));
  };
  uint32_t tv = rt0, tk = 0u;
  const uint32_t vearly = (uint32_t)((VEARLY >> (8 * (bj >> 2))) & 0xffull);
  auto w_line = [&](int n) {
    const int q = min(lane, 62) / 9, r = min(7 * (n >> 2) + q, 287), h = r >= 144;
    return (const char*)((n & 1) ? wu : wg) + (uint32_t)((r + 144 * (h + ((n >> 1) & 1))) * (F_ * 2) + bx * 1152 + (min(lane, 62) - 9 * q) * 128);
  };
#define TOUCH(pa) asm volatile("global_load_dword v126, %0, off" DPOL : : "v"(pa) : "memory", "v126")
  uint32_t wv[5], xv[9];
  auto rows_xw = [&](int lane) {
    u32x4 wa, xr, xs;
    const uint32_t xo = (uint32_t)((8 * tg + 2 * wl) * (D_ * 2));
    asm volatile("global_load_dwordx4 %0, %5, %9\n\tglobal_load_dword %1, %6, %9 offset:1024\n\t"
                 "global_load_dwordx4 %2, %7, %10\n\tglobal_load_dwordx4 %3, %7, %10 offset:1152\n\tglobal_load_dword %4, %8, %10 offset:1024\n\ts_waitcnt vmcnt(0)"
                 : "=&v"(wa), "=&v"(wv[4]), "=&v"(xr), "=&v"(xs), "=&v"(xv[8])
                 : "v"(16u * lane), "v"(4u * (lane & 31)), "v"(xo + 16u * lane), "v"(xo + 4u * lane + 32u * (lane & 32)), "s"(wr), "s"(x) : "memory");
#pragma unroll
    for (int k = 0; k < 4; ++k) { wv[k] = wa[k]; xv[k] = xr[k]; xv[4 + k] = xs[k]; }
  };
  const int dX = 512 - 24 + 8 * bx + bj;
  if constexpr (ROLE == 2) {
    const int tl = tid - 256;
    if (!isx && T0) {
      SLEEP(T0SLEEP);
      if (wl < T0) TOUCH(w_line(24 * wl + bj));
      if (T1) { asm volatile("s_waitcnt vmcnt(0)" ::: "memory"); SLEEP(T1SLEEP); if (wl < T1) TOUCH(w_line(24 * (T0 + wl) + bj)); }
    }
    if (isx) {
      if (w == 7) {
#pragma unroll
        for (int k = 0; k < 4; ++k) ST16(v_at(dX, lane + 64 * k), ones);
      }
      *(u32x4*)(smem + X_TB + 16 * tl) = zero4;
      *(u32x4*)(smem + X_TB + 16 * min(256 + tl, (TB + C_U) / 16 - 1)) = zero4;
      const int pr = (wl + XROT) & 3;
      COps e;
      conv_ops(lb0 + X_TB, lb0 + X_UL, lb0 + X_IM, lane, e);
      TOps t;
      table_ops(lb0 + X_IM, lb0 + X_TB, wl & 1, lane, t);
      e.o = lb0 + X_ST + (uint32_t)(16 * ((2 * i + (kq >> 1)) ^ ((i >> 2) & 1)) + 8 * (kq & 1));
      e.f = ca + 28 - 32;
      uint32_t rs = lb0 + X_ST + (uint32_t)(2048 * pr + 1024 * (lane >> 5) + 16 * ((lane & 31) ^ ((lane >> 3) & 1))), vo = v_at(dX, 64 * pr + lane);
      asm volatile("" : "+v"(e.o), "+v"(e.f), "+v"(rs), "+v"(vo));
      float ldt, are, aim, bre, bim, cre, cim, dsk;
      asm volatile("s_waitcnt vmcnt(4)\n\ts_cmp_eq_u32 %0, 7\n\ts_cbranch_scc1 1f\n\ts_waitcnt vmcnt(0)\n1:" : : "s"(w) : "scc");
      P_WAIT(4, , "v103", "v104", "v105", "v106", "v107", "v109");
      P_MORE();
      asm volatile("v_lshlrev_b32 %0, 16, v107" : "=v"(dsk) : : "v107");
      TS(0, 1);
      if (w == 7) {
        asm volatile("s_waitcnt vmcnt(0)" : "={v109}"(ep));
        T = ((uint32_t)__builtin_amdgcn_readfirstlane(ep) + 1u) * 0x9E3779B1u;
        SIGNAL(O_SV + 128u * (256 - 24 + 8 * bx + bj), 320 * 128, VDX);
      }
      __builtin_amdgcn_s_setprio(QPRIO);
      int* evp = (int*)(sel + 44);
      char* im = smem + X_IM + IP * (tl & 127);
      evp[wl] = (wl < 2) ? gen_y(ldt, are, aim, bre, bim, cre, cim, lb0 + X_IM + IP * tl, [] {} TSA) : gen_x(ldt, are, aim, im);
      TS(0, 2);
      __builtin_amdgcn_s_setprio(QPRIO2);
      gsync(ca + 8, 4);
      if (wl < 2) evp[-4 + wl] = __float_as_int(table_tile(t, wl & 1, lane, lb0 + LM + 4 * (128 + 44), dsk TSA));
      gsync0(ca + 8, 8);
      float ys = __int_as_float(evp[-4]);
      if (wl == 3) *(u32x4*)(smem + X_IM + pad_at(lane & 7)) = zero4;
      TS(0, 3);
      char* wsp = ws;
      CONV_RUN(2 + pr, e, ys, , "+{v0}"(rs), "+{v1}"(vo), "+{s[94:95]}"(wsp), "+{v11}"(tid2));
      TS(0, 6);
    }
    __builtin_amdgcn_s_setprio(PPRIO);
    asm volatile("" : "+v"(tid2));
    rows_xw(tid2 & 63);
    if (!isx) BAR();
    const uint32_t tp = (uint32_t)__builtin_amdgcn_readfirstlane(cnt[4]);
    T = (uint32_t)__builtin_amdgcn_readfirstlane(sel[43]);
    if (!isx && !ffn) {
      SLEEP(RSLEEP);
#pragma unroll
      for (int k = 0; k < 7; ++k) ST16(O_SL + (uint32_t)((6 * bx + bj - 18) * 1792 + 256 * k - 256 + tid2) * 16u, ones);
      DRAIN();
    }
    tv = tp + (uint32_t)__builtin_amdgcn_readfirstlane(cnt[8]) - vearly;
  } else {
    __builtin_amdgcn_s_setprio(3);
    if (tid < 12) cnt[tid] = 0u;
    BAR();
    uint32_t uo = u_at(tg, 64 * w + lane);
    uint32_t ta = lb0 + (uint32_t)(((lane >> 2) & 3) * RP + (64 * w + 16 * kq + 4 * (lane & 3)) * 2);
    if (tid < 20) ((uint32_t*)(hm + 320))[tid] = 0u;
    uint32_t la = (uint32_t)(2 * w * RP + 16 * lane), lt = (uint32_t)((2 * w + (lane >> 5)) * RP + 1024 + 4 * (lane & 31));
    uint32_t sg = O_SU + (uint32_t)(256 * (lane >> 3) + tg) * 128u + 16u * (lane & 7);
    asm volatile("" : "+v"(uo), "+v"(ta), "+v"(la), "+v"(lt), "+v"(sg));
    uint32_t sink = 0;
    f32x4 z4 = {0.f, 0.f, 0.f, 0.f};
    asm volatile("" : "+v"(z4));
    asm volatile("s_waitcnt vmcnt(1)" : "={v[100:103]}"(xa), "={v[104:107]}"(xc), "={v108}"(r0) : : "v109");
    TS(0, 1);
    {
      const uint32_t v[9] = {xa[0], xa[1], xa[2], xa[3], xc[0], xc[1], xc[2], xc[3], r0};
      uint32_t c[9];
      f32x2 tf[9];
      auto sig = [&] {
        asm volatile("s_waitcnt vmcnt(0)" : "={v109}"(ep));
        T = ((uint32_t)__builtin_amdgcn_readfirstlane(ep) + 1u) * 0x9E3779B1u;
        if (w == 3) { const uint32_t now = (uint32_t)__builtin_amdgcn_s_memrealtime() + SD; ST16(sg, ((u32x4){T, now, T, now})); }
      };
      if (SIGPOS == 0) sig();
      ln_two(v, lane, 0.03125f, c, tf, z4, [&] { if (SIGPOS) sig(); });
      const uint32_t tj = ((T >> (3 * w)) & 0x00070007u) | (((T >> (12 + w)) & 0x00010001u) << 14);
      uint32_t tw[3] = {(tj & 0xffffu) * 0x10001u, (tj >> 16) * 0x10001u, 0u};
      tw[2] = (lane < 32) ? tw[0] : tw[1];
      asm volatile("" : "+v"(tw[0]), "+v"(tw[1]), "+v"(tw[2]));
#pragma unroll
      for (int k = 0; k < 9; ++k) c[k] = (packh((f32x2){bflo(c[k]), bfhi(c[k])}) & ~UMASK) | tw[k >> 2];
      *(u32x4*)(smem + la) = (u32x4){c[0], c[1], c[2], c[3]};
      *(u32x4*)(smem + la + RP) = (u32x4){c[4], c[5], c[6], c[7]};
      *(uint32_t*)(smem + lt) = c[8];
    }
    gsync0(ca + 12, 4);
    {
      const u32x2 h0 = trd(ta), h1 = trd(ta + 4 * RP), h2 = trd(ta + 512), h3 = trd(ta + 512 + 4 * RP);
      ST16(uo, ((u32x4){h0[0], h0[1], h1[0], h1[1]}));
      ST16(uo + 64 * 128, ((u32x4){h2[0], h2[1], h3[0], h3[1]}));
    }
    if (w == 0) {
      const u32x2 h0 = trd(ta + 1024), h1 = trd(ta + 1024 + 4 * RP);
      ST16(uo + 128 * 128, ((u32x4){h0[0], h0[1], h1[0], h1[1]}));
    }
    TS(0, 2);
    if (!isx && 3 - w < TB1) TOUCH(w_line(24 * (3 - w) + 23 - bj));
    asm volatile("s_waitcnt vmcnt(0)" : "+v"(sink) : : "memory");
    TS(0, 13);
    if (w == 0) {
#pragma unroll
      for (int k = 0; k < 2; ++k) ST16(O_XC + (uint32_t)((511 - 2 * tg) * 144 + 64 * k + lane) * 16u, ones);
      const uint32_t oz = (lane < 16) ? O_XC + (uint32_t)((511 - 2 * tg) * 144 + 128 + lane) * 16u : O_FB + (uint32_t)(8 * b + lane - 16) * 16u;
      if (lane < ((b < 72) ? 24 : 16)) ST16(oz, ones);
    }
    SLEEP(ESLEEP);
    const uint32_t tu = latest(gate(ws, O_SU + (uint32_t)(256 * bx + 16 * i + 4 * w + (bj & 3)) * 128u, T));
    if (w == 0) { cnt[4] = tu; sel[43] = T; }
    {
      const uint32_t ts = gate2(ws, O_SV + (uint32_t)(320 * bx + tid) * 128u, 0u, O_SV + (uint32_t)(320 * bx + 256 + (tid >> 2)) * 128u, T);
      asm volatile("s_mov_b64 exec, 1\n\tds_max_i32 %0, %1\n\ts_mov_b64 exec, -1" : : "v"(ca + 32), "v"(max((int)(ts - tu), UD + VD2)) : "memory");
      TS(0, 9);
    }
    if (isx) {
      const uint32_t xo = u_at(tid, dX);
      char* sm = smem + X_UL + C_U - 16 + 16 * tid;
      const u32x4 ut = {utag(T, 0), utag(T, 1), utag(T, 2), utag(T, 3)};
      until(tu + UD + XD);
      u32x4 uw;
      asm volatile("global_load_dwordx4 %0, %1, %2 sc1" : "=&v"(uw) : "v"(xo), "s"(ws) : "memory");
      asm volatile("s_waitcnt vmcnt(0)" : "+v"(uw));
      uw ^= ut;
      u_again(uw, ut, xo);
      *(u32x4*)(sm + 16) = uw;
      BAR();
      if (w < 3) {
        const u32x4 pw = *(const u32x4*)sm;
        if (w < 2) sums<true>(smem + X_IM + 16 * tid, pw, uw); else sums<false>(smem + X_IM + 16 * tid, pw, uw);
        count_up(ca + 28);
      }
    } else {
      until(tu + UD + WD);
      BAR();
    }
    TS(0, 3);
    __builtin_amdgcn_s_setprio(DPRIO);
    auto behind_v = [&](int dt) {
      until(tu + (uint32_t)__builtin_amdgcn_readfirstlane(cnt[8]) + dt);
    };
    if (ffn) {
      gu_dma(0, NA, IC<WDEPTH>{});
      if (NA >= 9) {
        asm volatile("s_waitcnt vmcnt(%0)" : : "n"(NA - 9) : "memory");
        gsync(ca + 4, 4);
        gu_read(0);
      }
      DRAIN();
      TS(0, 4);
      behind_v(RD);
      TS(0, 8);
      gu_dma(NA, 15, IC<DDEPTH>{});
      if (NA < 9) {
        asm volatile("s_waitcnt vmcnt(6)" ::: "memory");
        gsync(ca + 4, 4);
        gu_read(0);
      }
      gsync(ca + 4, 8);
      down_dma();
      gwait(ca, 8);
      gu_dma(15, 18, IC<DDEPTH>{});
      DRAIN();
      TS(0, 7);
      gsync0(ca + 4, 12);
    } else {
      if (!isx && C3) {
        const int n = 6 * min(tid, 191) + bj - 18, r = n >> 2;
        const char* wb = (const char*)((n & 1) ? wu : wg) + bx * 1152;
        const int c0 = (3 - (int)((uintptr_t)wb >> 7)) & 7;
        wb += (uint32_t)((r + 144 * ((r >= 144) + ((n >> 1) & 1))) * (F_ * 2) + 128 * c0);
        TOUCH(wb);
        if (c0 == 0) TOUCH(wb + 1024);
      }
      auto d_line = [&](int k) {
        const int n = min((k * 14 + bj - 18) * 64 + lane, 5183), q = n / 1296, r = n - 1296 * q;
        return (const char*)wd + (uint32_t)(bx * 5184 + (r / 18) * 72 + 18 * q + r % 18) * 128u;
      };
      if (w < (isx ? HAX : HA)) TOUCH(d_line(w));
      DRAIN();
      TS(0, 4);
      behind_v(RD2);
      if (w < HW) {
#pragma unroll 1
        for (int k = (isx ? HAX : HA) + w; k < 6; k += HW) { TOUCH(d_line(k)); DRAIN(); }
      }
      TS(0, 7);
      gwait(ca, 8);
    }
    tk = tu + (uint32_t)__builtin_amdgcn_readfirstlane(cnt[8]) + KD;
  }
  {
  const int tid = tid2, lane = tid & 63, i = lane & 15, kq = lane >> 4;
  __builtin_amdgcn_s_setprio(3);
  constexpr int PL = LW + 9216 - 2560;
  f32x2 xf[9];
  f32x4 za = {0.f, 0.f, 0.f, 0.f}, zb = za;
  uint32_t pa = (uint32_t)(PL + 9216 * wl + 32 * lane), pb = (uint32_t)(PL + 9216 * wl + 2048 + 8 * (lane & 31)), ps = (lane < 32) ? 0x01000c0cu : 0x03020c0cu;
  uint32_t kid = (uint32_t)(((255 - tg) << 5) | (7 - 2 * wl - (lane >> 5)));
  uint32_t al = O_XC + 64u * (i >> 3) + 16u * kq + 128u * w;
  uint32_t ha = (ROLE == 1) ? (uint32_t)((4 * (kq & 1) + 2 * (kq >> 1) + (i >> 3)) * 80 + 16 * w + 2 * (i & 7)) : 0u;
  uint32_t h0a = (uint32_t)(min(i, 8) * 80 + 16 * kq), h1a = (uint32_t)(min(i ^ 8, 8) * 80 + 16 * kq);
  uint32_t so = O_SL + (uint32_t)fb * SP + ((ROLE == 2) ? 16u * tid - 4096u : 8192u + 16u * lane);
  uint32_t da = lb0 + (uint32_t)((8 * kq + ((lane >> 2) & 3)) * 1152 + 32 * ((8 * kq + ((lane >> 2) & 3) + 2) >> 2) + 8 * (lane & 3) + ((ROLE == 2) ? 128 * wl : 1024));
  if constexpr (ROLE == 2) {
    const int tl = tid - 256;
    uint32_t vo = v_at(tl, tg), vo2 = v_at(512 + 16 * wl + i, tg), px = (uint32_t)(PL + 4 * tl), py = (uint32_t)(PL + 2048 + 64 * wl + 4 * i);
#pragma unroll
    for (int k = 0; k < 9; ++k) xf[k] = (f32x2){bflo(xv[k]), bfhi(xv[k])};
    asm volatile("" : "+v"(xf[0]), "+v"(xf[1]), "+v"(xf[2]), "+v"(xf[3]), "+v"(xf[4]), "+v"(xf[5]), "+v"(xf[6]), "+v"(xf[7]), "+v"(xf[8]));
    asm volatile("" : "+v"(za), "+v"(zb), "+v"(pa), "+v"(pb), "+v"(ps), "+v"(kid), "+v"(h0a), "+v"(h1a), "+v"(so), "+v"(da), "+v"(vo), "+v"(vo2), "+v"(px), "+v"(py));
    TS(0, 7);
    until(tv);
    TS(0, 8);
    u32x4 va, vc, vb = {0u, 0u, 0u, 0u};
    asm volatile("global_load_dwordx4 %0, %3, %6 sc1\n\tglobal_load_dwordx4 %1, %4, %6 sc1\n\ts_mov_b64 exec, 0xffff\n\tglobal_load_dwordx4 %2, %5, %6 sc1\n\ts_mov_b64 exec, -1\n\t"
                 "s_waitcnt vmcnt(2)" : "=&v"(va), "=&v"(vc), "+v"(vb) : "v"(vo), "v"(vo + 128 * VP), "v"(vo2), "s"(ws) : "memory");
    auto turn = [&] {
#pragma unroll
      for (int e = 0; e < 4; ++e) *(uint32_t*)(smem + px + 1024 + 9216 * e) = vc[e];
      if (lane < 16) {
#pragma unroll
        for (int e = 0; e < 4; ++e) *(uint32_t*)(smem + py + 9216 * e) = vb[e];
      }
    };
#pragma unroll
    for (int e = 0; e < 4; ++e) *(uint32_t*)(smem + px + 9216 * e) = va[e];
    asm volatile("s_waitcnt vmcnt(0)" : "+v"(vc), "+v"(vb) : : "memory");
    turn();
    if (__builtin_expect(__any(max(max(max4(va), max4(vb)), max4(vc)) == 0xffffffffu), 0)) {
#pragma unroll 1
      for (int spin = 0; spin < SPIN_CAP; ++spin) {
        const bool na = max4(va) == 0xffffffffu, nb = max4(vb) == 0xffffffffu, nc = max4(vc) == 0xffffffffu;
        if (!__any(na || nb || nc)) break;
        TSC(16);
        SLEEP(PSLEEP);
        if (na) LD16W(va, vo);
        if (nb) LD16W(vb, vo2);
        if (nc) LD16W(vc, vo + 128 * VP);
      }
#pragma unroll
      for (int e = 0; e < 4; ++e) *(uint32_t*)(smem + px + 9216 * e) = va[e];
      turn();
    }
    gsync0(ca, 4);
    TS(0, 9);
  } else asm volatile("" : "+v"(ha), "+v"(al), "+v"(da));
  uint32_t hv[9], nv[9], keya = 0u, keyb = 0u;
  if constexpr (ROLE == 2) {
    const u32x4 a0 = *(const u32x4*)(smem + pa), a1 = *(const u32x4*)(smem + pa + 16);
    const u32x2 a2 = *(const u32x2*)(smem + pb);
    const f32x2 h[9] = {xf[0] + (f32x2){bflo(a0[0]), bflo(a0[1])}, xf[1] + (f32x2){bflo(a0[2]), bflo(a0[3])},
                        xf[2] + (f32x2){bflo(a1[0]), bflo(a1[1])}, xf[3] + (f32x2){bflo(a1[2]), bflo(a1[3])},
                        xf[4] + (f32x2){bfhi(a0[0]), bfhi(a0[1])}, xf[5] + (f32x2){bfhi(a0[2]), bfhi(a0[3])},
                        xf[6] + (f32x2){bfhi(a1[0]), bfhi(a1[1])}, xf[7] + (f32x2){bfhi(a1[2]), bfhi(a1[3])},
                        xf[8] + (f32x2){__uint_as_float(__builtin_amdgcn_perm(0u, a2[0], ps)), __uint_as_float(__builtin_amdgcn_perm(0u, a2[1], ps))}};
#pragma unroll
    for (int k = 0; k < 9; ++k) hv[k] = pack2(h[k][0], h[k][1]);
    ln_two(hv, lane, 1.f, nv, xf, za, [] {});
    const float da = dot2(nv[2], wv[2], dot2(nv[0], wv[0], zb[0])) + dot2(nv[3], wv[3], dot2(nv[1], wv[1], zb[1]));
    const float db = dot2(nv[6], wv[2], dot2(nv[4], wv[0], zb[2])) + dot2(nv[7], wv[3], dot2(nv[5], wv[1], zb[3]));
    float dt = row_sum(dot2(nv[8], wv[4], fold32(da, db)));
    asm("s_nop 1\n\tv_add_f32_dpp %0, %0, %0 row_bcast:15 row_mask:0xa bank_mask:0xf\n\ts_nop 0" : "+v"(dt));
    const uint32_t bb = pack2(dt, dt) & 0xffff0000u;
    const uint32_t kk = (bb ^ (((uint32_t)((int)bb >> 31) & 0x7fff0000u) | 0x80000000u)) | kid;
    if ((lane & 31) == 31) kl[2 * wl + (lane >> 5)] = kk;
    keya = (uint32_t)__builtin_amdgcn_readlane(kk, 31); keyb = (uint32_t)__builtin_amdgcn_readlane(kk, 63);
    gsync0(ca, 8);
  }
  if (ROLE == 2 && w == 4) {
    const u32x4 ka = *(const u32x4*)kl, kb = *(const u32x4*)(kl + 4);
    uint32_t m1 = max(max(ka[0], ka[1]), ka[2]), m2 = med3u(ka[0], ka[1], ka[2]), m3 = min(min(ka[0], ka[1]), ka[2]);
#pragma unroll
    for (int e = 0; e < 5; ++e) {
      const uint32_t kx = e ? kb[e - 1] : ka[3];
      m3 = med3u(m2, m3, kx); m2 = med3u(m1, m2, kx); m1 = max(m1, kx);
    }
    if (lane < 8) ST16(O_KEY + (uint32_t)(256 * lane + 8 * (tg & 31) + (tg >> 5)) * 16u, ((u32x4){m1 | 24u, m2 | 16u, m3 | 8u, T & ~1u}));
  }
  if (ROLE == 2 && w == 5) {
    const u32x4 kv = *(const u32x4*)&kl[4 * (lane & 1)];
    if (lane < 2) ST16(O_KF + (uint32_t)(2 * tg + lane) * 16u, ((u32x4){__builtin_amdgcn_perm(kv[1], kv[0], 0x07060302u), T, __builtin_amdgcn_perm(kv[3], kv[2], 0x07060302u), T}));
    if (b == 0 && lane == 0) ST4(O_EP, T * 0x0E8B2F51u);
  }
  if (ROLE == 2) TS(0, 10);
  auto put_row = [&](const uint32_t* r, int j, const void* base, uint32_t ro) {
    const u32x4 tl = {r[8], (uint32_t)DPP_I((int)r[8], 0x55, 0xf), (uint32_t)DPP_I((int)r[8], 0xaa, 0xf), (uint32_t)DPP_I((int)r[8], 0xff, 0xf)};
    asm volatile("global_store_dwordx4 %0, %1, %2 sc1\n\ts_nop 1" : : "v"(ro + 16u * lane), "v"((u32x4){r[4 * j], r[4 * j + 1], r[4 * j + 2], r[4 * j + 3]}), "s"(base) : "memory");
    if ((lane & 35) == 32 * j) asm volatile("global_store_dwordx4 %0, %1, %2 offset:1024 sc1\n\ts_nop 1" : : "v"(ro + 4u * (lane & 31)), "v"(tl), "s"(base) : "memory");
  };
  int rka = 0, rkb = 0;
  uint32_t k3rd = 0u;
  if constexpr (ROLE == 2) {
    __builtin_amdgcn_s_setprio(0);
    const uint32_t ko = kl[lane & 7];
    rka = FORCEFB + __popcll(__ballot(ko > keya) & 0xffull); rkb = FORCEFB + __popcll(__ballot(ko > keyb) & 0xffull);
#pragma unroll
    for (int k = 0; k < 9; ++k) {
      const f32x2 o = xf[k] + (f32x2){bflo(nv[k]), bfhi(nv[k])};
      hv[k] = pack2(o[0], o[1]);
      nv[k] = min(nv[k], 0xfffffffeu);
    }
    if (rka < 2) put_row(nv, 0, ws, O_XC + (uint32_t)(1023 - 4 * tg - rka) * 1152u);
    if (rkb < 2) put_row(nv, 1, ws, O_XC + (uint32_t)(1023 - 4 * tg - rkb) * 1152u);
    __builtin_amdgcn_s_setprio(3);
  } else {
    u32x4 key;
    const uint32_t kof = O_KEY + (uint32_t)(256 * bx + tid) * 16u;
    gu_read(1);
    TS(0, 10);
    until(tk);
    if (!ffn) SLEEP(NSLEEP);
    LD16W(key, kof);
    if (__builtin_expect(__any(key[3] != (T & ~1u)), 0)) {
#pragma unroll 1
      for (int spin = 0; spin < SPIN_CAP; ++spin) {
        const bool need = key[3] != (T & ~1u);
        if (!__any(need)) break;
        TSC(15);
        SLEEP(KBACK);
        if (need) LD16W(key, kof);
      }
    }
    TS(0, 11);
    const uint32_t m1 = oct_maxu(key[0]);
    const bool hit = key[0] == m1;
    const uint32_t k0 = hit ? key[1] : key[0], k1 = hit ? key[2] : key[1];
    const uint32_t m2 = oct_maxu(k0);
    if ((lane & 7) == 0) *(u32x2*)&cand[tid >> 2] = (u32x2){m1, m2};
    k3rd = (k0 == m2) ? k1 : k0;
    if (RPF && !ffn && w == (bj & 3)) {
      const uint32_t r1 = O_XC + ((m1 >> 3) & 0x3ffu) * 1152u + 128u * (lane & 7), r2 = O_XC + ((m2 >> 3) & 0x3ffu) * 1152u + 128u * (lane & 7);
      asm volatile("global_load_dword v126, %0, %3 sc1\n\tglobal_load_dword v126, %1, %3 sc1\n\tglobal_load_dword v126, %2, %3 offset:1024 sc1"
                   : : "v"(r1), "v"(r2), "v"(((lane & 1) ? r1 : r2) - 128u * (lane & 7)), "s"(ws) : "memory", "v126");
    }
  }
  auto pick = [&](const uint32_t* cd, uint32_t* sl) {
    BAR();
    const uint32_t c = cd[lane], own = cd[8 * w + (lane & 7)];
    const u32x4 la = *(const u32x4*)&cd[8 * w], lb = *(const u32x4*)&cd[8 * w + 4];
    uint32_t r = C_;
    asm("v_cmp_gt_u32 s[40:41], %1, %2\n\tv_cmp_gt_u32 s[42:43], %1, %3\n\tv_cmp_gt_u32 s[44:45], %1, %4\n\tv_cmp_gt_u32 s[46:47], %1, %5\n\t"
        "v_cmp_gt_u32 s[48:49], %1, %6\n\tv_cmp_gt_u32 s[50:51], %1, %7\n\tv_cmp_gt_u32 s[52:53], %1, %8\n\tv_cmp_gt_u32 s[54:55], %1, %9\n\t"
        "s_bcnt1_i32_b64 s40, s[40:41]\n\ts_bcnt1_i32_b64 s42, s[42:43]\n\tv_writelane_b32 %0, s40, 0\n\ts_bcnt1_i32_b64 s44, s[44:45]\n\t"
        "v_writelane_b32 %0, s42, 1\n\ts_bcnt1_i32_b64 s46, s[46:47]\n\tv_writelane_b32 %0, s44, 2\n\ts_bcnt1_i32_b64 s48, s[48:49]\n\t"
        "v_writelane_b32 %0, s46, 3\n\ts_bcnt1_i32_b64 s50, s[50:51]\n\tv_writelane_b32 %0, s48, 4\n\ts_bcnt1_i32_b64 s52, s[52:53]\n\t"
        "v_writelane_b32 %0, s50, 5\n\ts_bcnt1_i32_b64 s54, s[54:55]\n\tv_writelane_b32 %0, s52, 6\n\tv_writelane_b32 %0, s54, 7"
        : "+v"(r) : "v"(c), "v"(la[0]), "v"(la[1]), "v"(la[2]), "v"(la[3]), "v"(lb[0]), "v"(lb[1]), "v"(lb[2]), "v"(lb[3])
        : "scc", "s40", "s41", "s42", "s43", "s44", "s45", "s46", "s47", "s48", "s49", "s50", "s51", "s52", "s53", "s54", "s55");
    sl[min(r, (uint32_t)C_)] = own;
    BAR();
  };
  auto rows = [&](uint32_t ao, auto&& meanwhile) {
    u32x4 aa, ab, ac = __builtin_bit_cast(u32x4, zb);
    asm volatile("global_load_dwordx4 %0, %3, %4 sc1\n\tglobal_load_dwordx4 %1, %3, %4 offset:512 sc1\n\t"
                 "s_cmp_lg_u32 %5, 0\n\ts_cbranch_scc1 1f\n\tglobal_load_dwordx4 %2, %3, %4 offset:1024 sc1\n1:"
                 : "=&v"(aa), "=&v"(ab), "+v"(ac) : "v"(ao), "s"(ws), "s"(w) : "memory", "scc");
    __builtin_amdgcn_sched_barrier(0);
    meanwhile();
    asm volatile("s_waitcnt vmcnt(0)" : "+v"(aa), "+v"(ab), "+v"(ac) : : "memory");
    if (__builtin_expect(__any(max(max(max4(aa), max4(ab)), max4(ac)) == 0xffffffffu), 0)) {
#pragma unroll 1
      for (int spin = 0; spin < SPIN_CAP; ++spin) {
        const bool na = max4(aa) == 0xffffffffu, nb = max4(ab) == 0xffffffffu, nc = max4(ac) == 0xffffffffu;
        if (!__any(na || nb || nc)) break;
        TSC(14);
        SLEEP(PSLEEP);
        if (na) LD16W(aa, ao);
        if (nb) LD16W(ab, ao + 512u);
        if (nc) LD16W(ac, ao + 1024u);
      }
    }
    *(u32x4*)(smem + LA + 16 * tid) = aa;
    *(u32x4*)(smem + LA + 4096 + 16 * tid) = ab;
    if (w == 0) *(u32x4*)(smem + LA + 8192 + 16 * tid) = ac;
  };
  auto get_bd = [&](int t0, int t1) {
#pragma unroll
    for (int t = t0; t < t1; ++t) {
      const u32x2 h0 = trd(da + 512 * (t >> 2) + 32 * (t & 3)), h1 = trd(da + 512 * (t >> 2) + 32 * (t & 3) + 4608 + 32);
      bw[t] = (u32x4){h0[0], h0[1], h1[0], h1[1]};
    }
#pragma unroll
    for (int t = t0; t < t1; ++t) asm volatile("" : "+v"(bw[t]));
  };
  pick(cand, sel);
  uint32_t mykey = sel[lane & 7], ro = ((mykey >> 3) & 0x3ffu) * 1152u;
  TS(0, 12);
  auto check = [&] {
    cnt[(k3rd > (uint32_t)__builtin_amdgcn_readlane(mykey, 7)) ? 5 : 6] = 1u;
  };
  if constexpr (ROLE == 1) {
    if (ffn && !FORCEFB) rows(ro + al, check); else { check(); DRAIN(); }
  } else if (ffn) get_bd(0, 8);
  BAR();
  TS(0, 5);
  auto put_out = [&](int j, uint32_t k0, int rk) {
    const uint64_t mine = __ballot(((mykey ^ k0) & ~24u) == 0u) & 0xffull;
    if (mine == 0ull) put_row(hv, j, out, (uint32_t)((8 * tg + 2 * wl + j) * (D_ * 2)));
    else if (rk >= 2) put_row(nv, j, ws, O_FB + 1152u * (uint32_t)__builtin_ctzll(mine));
  };
  auto rest_out = [&] { put_out(0, keya, rka); put_out(1, keyb, rkb); };
  f32x4 lo, hi;
  auto gate_up = [&] {
    u32x4 av[5];
    const uint32_t aa = lb0 + LA + 16u * lane;
#pragma unroll
    for (int j = 0; j < 5; ++j) asm volatile("ds_read_b128 %0, %1 offset:%2" : "=v"(av[j]) : "v"(aa), "n"(1024 * j) : "memory");
    lo = (f32x4){0.f, 0.f, 0.f, 0.f}; hi = lo;
#pragma unroll
    for (int j = 0; j < 9; ++j) {
      if (j < 5) asm volatile("s_waitcnt lgkmcnt(4)" : "+v"(av[j % 5]));
      else if (j == 5) asm volatile("s_waitcnt lgkmcnt(2)" : "+v"(av[0]), "+v"(av[1]));
      else if (j == 7) asm volatile("s_waitcnt lgkmcnt(0)" : "+v"(av[2]), "+v"(av[3]));
      lo = MMB(av[j % 5], bw[2 * j], lo);
      hi = MMB(av[j % 5], bw[2 * j + 1], hi);
      __builtin_amdgcn_sched_barrier(0);
      if (j < 4) asm volatile("ds_read_b128 %0, %1 offset:%2" : "=v"(av[j]) : "v"(aa), "n"(1024 * (j + 5)) : "memory");
    }
  };
  uint32_t fl;
  asm volatile("ds_read_b32 %0, %1" : "=v"(fl) : "v"(ca + 20) : "memory");
  if (ROLE == 1) gate_up();
  asm volatile("s_waitcnt lgkmcnt(0)" : "+v"(fl));
  const bool slow = FORCESLOW || FORCEFB || __builtin_amdgcn_readfirstlane(fl) != 0;
  if (__builtin_expect(slow, 0)) {
    u32x4 key;
#pragma unroll 1
    for (int spin = 0; spin < SPIN_CAP; ++spin) {
      LD16W(key, O_KF + 16u * tid);
      if (!__any(key[1] != T || key[3] != T)) break;
      SLEEP(KBACK);
    }
    const uint32_t ib = (uint32_t)(((255 - (tid >> 1)) << 5) | (7 - 4 * (tid & 1)));
    uint32_t k0 = (key[0] << 16) | ib, k1 = (key[0] & 0xffff0000u) | (ib - 1), k2 = (key[2] << 16) | (ib - 2), k3 = (key[2] & 0xffff0000u) | (ib - 3);
    CEX(k0, k1) CEX(k2, k3) CEX(k0, k2) CEX(k1, k3) CEX(k1, k2)
#pragma unroll 1
    for (int c = 0; c < C_; ++c) {
      const uint32_t mx = wave_maxu(k0);
      const bool hit = (k0 == mx);
      k0 = hit ? k1 : k0; k1 = hit ? k2 : k1; k2 = hit ? k3 : k2; k3 = hit ? 0u : k3;
      if (lane == 0) cand[64 + 8 * w + c] = mx;
    }
    pick(cand + 64, sel + 16);
    mykey = sel[16 + (lane & 7)];
    const uint32_t okey = sel[16 + (lane >> 3)];
    const uint64_t same = __ballot(((mykey ^ okey) & 0x1fe0u) == 0u && okey > mykey);
    const int sl = FORCEFB + __popcll((same >> (lane & 7)) & 0x0101010101010101ull);
    ro = (sl < 2) ? (((mykey >> 3) & 0x3ffu) + 3 - sl) * 1152u : O_FB - O_XC + 1152u * (lane & 7);
    if (ROLE == 2) rest_out();
    if (ffn) {
      if (ROLE == 1) rows(ro + al, [] {});
      BAR();
      if (ROLE == 1) gate_up();
    }
  }
  if (OPOS == 0 && ROLE == 2 && ffn && !slow) { SLEEP(OD); rest_out(); }
  if (!ffn) {
    if (ROLE == 2 && !slow) { SLEEP(OD2); rest_out(); }
    TSE();
    return;
  }
  uint32_t fx = 0, fv = 0, fn = 0, fo = 0, sa[9];
  float rs, hx, xs;
  u32x4 se;
  if constexpr (ROLE == 1) {
    const f32x4 lh = (lane < 32) ? lo : hi;
    const float fa = fold32(lh[0], lh[2]), fb2 = fold32(lh[1], lh[3]);
    const float g = __int_as_float(__builtin_amdgcn_update_dpp(__float_as_int(fa), __float_as_int(fb2), 0x128, 0xf, 0xc, false));
    const float u = __int_as_float(__builtin_amdgcn_update_dpp(__float_as_int(fb2), __float_as_int(fa), 0x128, 0xf, 0x3, false));
    const uint32_t gu = pack2(g, u);
    const float gp = bflo(gu);
    const float sg = rbf(gp * __builtin_amdgcn_rcpf(1.f + __expf(-gp)));
    *(u16*)((char*)hm + ha) = f2bf(sg * bfhi(gu));
  }
  BAR();
  TS(0, 6);
  if (ROLE == 2 || w == 2) {
    const u32x4 a0 = *(const u32x4*)((char*)hm + h0a), a1 = *(const u32x4*)((char*)hm + h1a);
    if (ROLE == 1) get_bd(0, 4);
    const f32x4 z = {0.f, 0.f, 0.f, 0.f};
    f32x4 o[4];
#pragma unroll
    for (int t = 0; t < 2 * ROLE; ++t) o[t] = MMB(a1, bw[2 * t + 1], MMB(a0, bw[2 * t], z));
#pragma unroll
    for (int t = 0; t < ROLE; ++t) {
      u32x4 sv;
#pragma unroll
      for (int r = 0; r < 4; ++r) sv[r] = min(pack2(o[2 * t][r], o[2 * t + 1][r]), 0xfffffffeu);
      ST16(so + 4096u * t, sv);
    }
    TS(0, 13);
  }
  if (OPOS == 1 && ROLE == 2) {
    SLEEP(OSLEEP);
    if (!slow) rest_out();
  }
  if (ROLE == 1 && w == 1) {
    const int q = (fb & 15) >> 2, ts = 4 * (q & 1) + ((i & 7) >> 1);
    const int dd = 64 * (fb >> 4) + 16 * (2 * (i & 1) + (q >> 1)) + 4 * (fb & 3) + 2 * (kq & 1) + (kq >> 1);
    const uint32_t fkey = (uint32_t)__builtin_amdgcn_ds_bpermute(4 * ts, (int)mykey), fr = (uint32_t)__builtin_amdgcn_ds_bpermute(4 * ts, (int)ro);
    const int ft = 2047 - (int)(((fkey >> 2) & 0x7f8u) | (fkey & 7u));
    fo = (uint32_t)(ft * D_ + dd) * 2u;
    asm volatile("global_load_ushort %0, %3, %6\n\tglobal_load_ushort %1, %4, %7 sc1\n\tglobal_load_ushort %2, %5, %7 sc1"
                 : "=&v"(fx), "=&v"(fv), "=&v"(fn) : "v"(fo), "v"(v_at(dd, ft >> 3) + 2u * (ft & 7)), "v"(O_XC + fr + 2u * dd), "s"(x), "s"(ws) : "memory");
    const uint32_t o = fkey >> 16;
    const float rs0 = bf2f((u16)((o & 0x8000u) ? (o & 0x7fffu) : (~o & 0xffffu)));
    rs = rbf(__builtin_amdgcn_rcpf(1.f + __expf(-rs0)));
    const uint32_t one = (i < 8) ? (0x3f80u << (16 * (i & 1))) : 0u;
    se = (u32x4){(i >> 1) == 0 ? one : 0u, (i >> 1) == 1 ? one : 0u, (i >> 1) == 2 ? one : 0u, (i >> 1) == 3 ? one : 0u};
#pragma unroll
    for (int m = 0; m < 9; ++m) sa[m] = O_SL + (uint32_t)(16 * m + (lane >> 2)) * SP + (uint32_t)(4 * fb + (lane & 3)) * 16u;
    asm volatile("" : "+v"(rs), "+v"(se), "+v"(sa[0]), "+v"(sa[1]), "+v"(sa[2]), "+v"(sa[3]), "+v"(sa[4]), "+v"(sa[5]), "+v"(sa[6]), "+v"(sa[7]), "+v"(sa[8]));
    SLEEP(SSLEEP);
    TS(0, 1);
    float sum;
#pragma unroll 1
    for (int spin = 0; spin < SPIN_CAP; ++spin) {
      u32x4 sv[9];
#pragma unroll
      for (int m = 0; m < 9; ++m) LD16(sv[m], sa[m]);
      asm volatile("s_waitcnt vmcnt(9)" : "+v"(fx), "+v"(fv), "+v"(fn));
      hx = rbf(bf2f((u16)fx) + bf2f((u16)fv));
      xs = bf2f((u16)fn);
      asm volatile("" : "+v"(hx), "+v"(xs));
      f32x4 dv = {0.f, 0.f, 0.f, 0.f};
      static_for<0, 9>([&](auto mm) {
        constexpr int m = mm.value;
        asm volatile("s_waitcnt vmcnt(%1)" : "+v"(sv[m]) : "n"(8 - m));
        dv = MMB(sv[m], se, dv);
        __builtin_amdgcn_sched_barrier(0);
      });
      sum = fold16(fold32(dv[0], dv[1]), fold32(dv[2], dv[3]));
      if (__builtin_expect(!__any(sum != sum), 1)) break;
      uint32_t mx = 0u;
#pragma unroll
      for (int m = 0; m < 9; ++m) mx = max(mx, max4(sv[m]));
      if (!__any(mx == 0xffffffffu)) break;
      TSC(18);
      SLEEP(PSLEEP);
    }
    const float o1 = rbf(xs + rbf(rbf(sum) * rs));
    if (i < 8) asm volatile("global_store_short %0, %1, %2 sc1" : : "v"(fo), "v"((uint32_t)f2bf(hx + o1)), "s"(out) : "memory");
    TS(0, 2);
  }
  TSE();
  }
  }
}
extern "C" __global__ __launch_bounds__(512, 4) void kf_all(KARGS) {
  __shared__ __attribute__((aligned(16))) char smem[SMEM];
  asm volatile("v_readfirstlane_b32 s98, v0\n\ts_load_dwordx16 s[64:79], s[0:1], 0x0\n\ts_load_dwordx4 s[80:83], s[0:1], 0x40\n\t"
               "s_cmpk_lt_u32 s2, 0x100\n\ts_cbranch_scc0 1f\n\ts_cmpk_lt_u32 s98, 0x100\n\ts_cbranch_scc0 2f\n\t"
               "s_and_b32 s84, s2, 0xe0\n\ts_bfe_u32 s85, s2, 0x20003\n\ts_or_b32 s84, s84, s85\n\ts_and_b32 s85, s2, 7\n\ts_lshl2_add_u32 s84, s85, s84\n\t"
               "s_mulk_i32 s84, 0x2400\n\ts_mulk_i32 s98, 36\n\ts_add_u32 s84, s84, s98\n\tv_and_b32 v110, 63, v0\n\tv_and_b32 v111, 32, v0\n\t"
               "v_lshlrev_b32 v111, 5, v111\n\tv_lshl_add_u32 v111, v110, 2, v111\n\tv_lshl_add_u32 v110, v110, 4, s84\n\tv_add_u32 v111, s84, v111\n\t"
               "v_mov_b32 v109, 0\n\ts_waitcnt lgkmcnt(0)\n\t"
               "global_load_dwordx4 v[100:103], v110, s[64:65]\n\tglobal_load_dwordx4 v[104:107], v110, s[64:65] offset:1152\n\t"
               "global_load_dword v108, v111, s[64:65] offset:1024\n\tglobal_load_dword v109, v109, s[82:83] sc1\n\ts_branch kf_n\n"
               "1:\n\ts_bfe_u32 s84, s2, 0x50003\n\ts_and_b32 s85, s2, 7\n\ts_lshl_b32 s85, s85, 5\n\ts_or_b32 s84, s84, s85\n\t"
               "s_lshl_b32 s85, s84, 9\n\ts_lshl_b32 s84, s84, 2\n\tv_and_b32 v110, 0xff, v0\n\tv_bfe_u32 v107, v0, 7, 1\n\t"
               "v_lshl_add_u32 v110, v110, 1, s85\n\tv_lshl_add_u32 v107, v107, 1, s84\n\tv_mov_b32 v109, 0\n\ts_cmpk_lt_u32 s98, 0x100\n\ts_waitcnt lgkmcnt(0)\n\t"
               "global_load_ushort v100, v107, s[78:79]\n\tglobal_load_ushort v101, v110, s[66:67]\n\tglobal_load_ushort v102, v110, s[68:69]\n\t"
               "s_cbranch_scc0 3f\n"
               "4:\n\tglobal_load_ushort v103, v110, s[70:71]\n\tglobal_load_ushort v104, v110, s[72:73]\n\tglobal_load_ushort v105, v110, s[74:75]\n\t"
               "global_load_ushort v106, v110, s[76:77]\n\tglobal_load_ushort v107, v107, s[80:81]\n\tglobal_load_dword v109, v109, s[82:83] sc1\n\ts_cbranch_scc1 kf_c\n\ts_barrier\n\ts_branch kf_p\n"
               "3:\n\tglobal_load_dword v109, v109, s[82:83] sc1\n\ts_branch kf_u\n"
               "2:\n\ts_cmpk_lt_u32 s2, 0xc0\n\ts_cbranch_scc1 5f\n\t"
               "s_lshr_b32 s84, s2, 3\n\ts_and_b32 s85, s2, 7\n\ts_lshl3_add_u32 s84, s85, s84\n\ts_addk_i32 s84, 488\n\ts_lshl_b32 s85, s84, 8\n\ts_lshl_b32 s84, s84, 1\n\t"
               "v_and_b32 v110, 0x7f, v0\n\tv_lshl_add_u32 v110, v110, 1, s85\n\tv_mov_b32 v107, s84\n\tv_mov_b32 v109, 0\n\ts_cmp_eq_u32 s2, 0\n\ts_waitcnt lgkmcnt(0)\n\t"
               "global_load_ushort v100, v107, s[78:79]\n\tglobal_load_ushort v101, v110, s[66:67]\n\tglobal_load_ushort v102, v110, s[68:69]\n\ts_branch 4b\n"
               "5:\n\ts_and_b32 s99, s2, 0xf8\n\ts_lshl_b32 s99, s99, 5\n\ts_and_b32 s98, s98, 0xc0\n\ts_or_b32 s98, s98, s99\n\ts_lshl_b32 s98, s98, 1\n\t"
               "s_getpc_b64 s[96:97]\n\ts_add_u32 s96, s96, s98\n\ts_addc_u32 s97, s97, 0\n\t"
               "s_load_dword s99, s[96:97], 0x0\n\ts_load_dword s99, s[96:97], 0x40\n\ts_load_dword s99, s[96:97], 0x3000\n\ts_load_dword s99, s[96:97], 0x3040\n\t"
               "s_load_dword s99, s[96:97], 0x6000\n\ts_load_dword s99, s[96:97], 0x6040\n\t"
               "s_cmp_gt_i32 s98, kf_end-kf_all-0x9400\n\ts_cbranch_scc1 6f\n\t"
               "s_load_dword s99, s[96:97], 0x9000\n\ts_load_dword s99, s[96:97], 0x9040\n6:\n\ts_barrier\n\ts_branch kf_p"
               : : "s"(__builtin_amdgcn_kernarg_segment_ptr()), "s"(blockIdx.x), "v"(threadIdx.x),
                   "s"((uint32_t)(uintptr_t)(__attribute__((address_space(3))) char*)smem) : "scc", "s96", "s97", "s98", "s99", "v127");
}
extern "C" __global__ __launch_bounds__(512, 4) void kf_c(KARGS) {
  __shared__ __attribute__((aligned(16))) char smem[SMEM];
  role_body<0>(smem, KPASS);
}
extern "C" __global__ __launch_bounds__(512, 4) void kf_u(KARGS) {
  __shared__ __attribute__((aligned(16))) char smem[SMEM];
  role_body<3>(smem, KPASS);
}
extern "C" __global__ __launch_bounds__(512, 4) void kf_n(KARGS) {
  __shared__ __attribute__((aligned(16))) char smem[SMEM];
  role_body<1>(smem, KPASS);
}
extern "C" __global__ __launch_bounds__(512, 4) void kf_p(KARGS) {
  __shared__ __attribute__((aligned(16))) char smem[SMEM];
  role_body<2>(smem, KPASS);
}
extern "C" __global__ __launch_bounds__(512, 4) void kf_conv() { asm volatile(CONV_ASM ::: CONV_CLOB); }
extern "C" __global__ void kf_end() {}

extern "C" __global__ __launch_bounds__(1024) void k_probe(int* dummy) {
  __shared__ int sm[2];
  const uint32_t now = (uint32_t)__builtin_amdgcn_s_memrealtime();
  __shared__ int sn;
  if (threadIdx.x == 0) { sm[0] = 0; sm[1] = 2 == 1 ? 0x7fffffff : 0; sn = 0; }
  __syncthreads();
  for (int k = threadIdx.x; k < 4096; k += 1024) {
    const uint32_t a = (uint32_t)g_prb[k], b = (uint32_t)(g_prb[k] >> 32);
    if (a) atomicMax(&sm[0], (int)(now - a));
    if (b) { if (2 == 1) atomicMin(&sm[1], (int)(now - b)); else if (2 == 0) atomicMax(&sm[1], (int)(now - b)); else { atomicAdd(&sm[1], (int)(now - b)); atomicAdd(&sn, 1); } }
  }
  __syncthreads();
  const int q = min(max(sm[0] * 10 - (2 == 2 ? sm[1] * 10 / max(sn, 1) : sm[1] * 10), 0), 30000);
  if (threadIdx.x == 0) while ((int)((uint32_t)__builtin_amdgcn_s_memrealtime() - now) < q) __builtin_amdgcn_s_sleep(2);
}
extern "C" void kernel_launch(void* const* d_in, const int* in_sizes, int n_in,
                              void* d_out, int out_size, void* d_ws, size_t ws_size,
                              hipStream_t stream) {
  kf_all<<<512, 512, 0, stream>>>((const u16*)d_in[0], (const u16*)d_in[1], (const u16*)d_in[2], (const u16*)d_in[3], (const u16*)d_in[4],
                                  (const u16*)d_in[5], (const u16*)d_in[6], (const u16*)d_in[7], (const u16*)d_in[8], (char*)d_ws + (size_t)WSOFF * 1048576,
                                  (const u16*)d_in[10], (const u16*)d_in[11], (const u16*)d_in[12], (const u16*)d_in[13], (u16*)d_out);
  k_probe<<<1, 1024, 0, stream>>>((int*)d_ws);
}
```

```cpp
#include <hip/hip_runtime.h>
#include <hip/hip_bf16.h>
#include <stdint.h>

typedef __attribute__((ext_vector_type(8))) short bf16x8;
typedef __attribute__((ext_vector_type(4))) float f32x4;
typedef uint16_t u16;
typedef __attribute__((ext_vector_type(4))) uint32_t u32x4;
typedef __attribute__((ext_vector_type(2))) uint32_t u32x2;
#define TSD
#define TS(k, i)
#define TSC(i)
#define TSE()
#define TSP
#define TSA

constexpr int L_ = 2048, D_ = 576, N_ = 128, F_ = 4608, C_ = 8;
constexpr int NF = 144;
constexpr uint32_t O_EP = 0;
constexpr uint32_t O_KF = 4096;
constexpr uint32_t O_KEY = O_KF + 8192;
constexpr uint32_t O_SC = O_KEY + 32768;
constexpr uint32_t O_DN = O_SC + 9216;
constexpr uint32_t O_SU = O_DN + 20480;
constexpr uint32_t O_FB = O_SU + 262144;
constexpr uint32_t O_XC = O_FB + 8 * 1152;
constexpr uint32_t SP = 9216 + 128;
constexpr uint32_t O_SL = O_XC + 1024 * 1152;
constexpr uint32_t SLU = 336;
constexpr uint32_t O_U = O_SL + 256 * SLU * 16;
constexpr int UP = 584;
constexpr uint32_t VP = 8192 + 128, VPP = 4096 + 128;
constexpr uint32_t O_V = O_U + 256 * UP * 16, O_VP = O_V + 256 * VP;
static_assert(256 * SLU * 16 >= NF * SP && O_SL % 128 == 0 && O_U % 128 == 0 && O_V % 128 == 0, "");
__host__ __device__ __forceinline__ uint32_t u_at(int tg, int d) { return O_U + 16u * (uint32_t)((tg >> 1) * (UP * 2) + (d >> 2) * 8 + (tg & 1) * 4 + (d & 3)); }
__host__ __device__ __forceinline__ uint32_t v_at(int d, int tg) {
  return (d < 512) ? O_V + (uint32_t)(d >> 1) * VP + 16u * (uint32_t)(2 * tg + (d & 1)) : O_VP + (uint32_t)(d - 512) * VPP + 16u * (uint32_t)tg;
}
#define HAVE_VAT
#define HAVE_VTAG
__device__ __forceinline__ float bf2f(u16 v) { return __uint_as_float((uint32_t)v << 16); }
__device__ __forceinline__ float bflo(uint32_t v) { return __uint_as_float(v << 16); }
__device__ __forceinline__ float bfhi(uint32_t v) { return __uint_as_float(v & 0xffff0000u); }
__device__ __forceinline__ u16 f2bf(float f) { return __builtin_bit_cast(u16, (__bf16)f); }
__device__ __forceinline__ float rbf(float f) { return bf2f(f2bf(f)); }
typedef __attribute__((ext_vector_type(2))) float f32x2;
typedef __attribute__((ext_vector_type(2))) __bf16 bf16x2v;
__device__ __forceinline__ uint32_t pack2(float a, float b) {
  return __builtin_bit_cast(uint32_t, __builtin_convertvector((f32x2){a, b}, bf16x2v));
}

#define DPP_I(v, ctrl, rmask) __builtin_amdgcn_update_dpp(0, (v), (ctrl), (rmask), 0xf, true)
__device__ __forceinline__ float row_sum(float v) {
  v += __int_as_float(DPP_I(__float_as_int(v), 0x111, 0xf));
  v += __int_as_float(DPP_I(__float_as_int(v), 0x112, 0xf));
  v += __int_as_float(DPP_I(__float_as_int(v), 0x114, 0xf));
  v += __int_as_float(DPP_I(__float_as_int(v), 0x118, 0xf));
  return v;
}
__device__ __forceinline__ float rdl(float v, int l) { return __int_as_float(__builtin_amdgcn_readlane(__float_as_int(v), l)); }
__device__ __forceinline__ float wave_sum(float v) {
  v = row_sum(v);
  asm("s_nop 1\n\tv_add_f32_dpp %0, %0, %0 row_bcast:15 row_mask:0xa bank_mask:0xf\n\t"
      "s_nop 1\n\tv_add_f32_dpp %0, %0, %0 row_bcast:31 row_mask:0xc bank_mask:0xf\n\ts_nop 0" : "+v"(v));
  return rdl(v, 63);
}
__device__ __forceinline__ float fold32(float a, float b) {
  const auto r = __builtin_amdgcn_permlane32_swap(__float_as_uint(a), __float_as_uint(b), false, false);
  return __uint_as_float(r[0]) + __uint_as_float(r[1]);
}
__device__ __forceinline__ float fold16(float a, float b) {
  const auto r = __builtin_amdgcn_permlane16_swap(__float_as_uint(a), __float_as_uint(b), false, false);
  return __uint_as_float(r[0]) + __uint_as_float(r[1]);
}
__device__ __forceinline__ void wave_sum2(float& a, float& b) {
  float v = row_sum(fold32(a, b));
  asm("s_nop 1\n\tv_add_f32_dpp %0, %0, %0 row_bcast:15 row_mask:0xa bank_mask:0xf\n\ts_nop 0" : "+v"(v));
  a = rdl(v, 31); b = rdl(v, 63);
}
__device__ __forceinline__ float dot2(uint32_t a, uint32_t b, float c) {
  return __builtin_amdgcn_fdot2_f32_bf16(__builtin_bit_cast(bf16x2v, a), __builtin_bit_cast(bf16x2v, b), c, false);
}
__device__ __forceinline__ uint32_t row_maxu(uint32_t v) {
  v = max(v, (uint32_t)DPP_I((int)v, 0xb1, 0xf));
  v = max(v, (uint32_t)DPP_I((int)v, 0x4e, 0xf));
  v = max(v, (uint32_t)DPP_I((int)v, 0x141, 0xf));
  v = max(v, (uint32_t)DPP_I((int)v, 0x140, 0xf));
  return v;
}
__device__ __forceinline__ uint32_t wave_maxu(uint32_t v) {
  v = max(v, (uint32_t)DPP_I((int)v, 0x111, 0xf));
  v = max(v, (uint32_t)DPP_I((int)v, 0x112, 0xf));
  v = max(v, (uint32_t)DPP_I((int)v, 0x114, 0xf));
  v = max(v, (uint32_t)DPP_I((int)v, 0x118, 0xf));
  v = max(v, (uint32_t)DPP_I((int)v, 0x142, 0xa));
  v = max(v, (uint32_t)DPP_I((int)v, 0x143, 0xc));
  return (uint32_t)__builtin_amdgcn_readlane((int)v, 63);
}

typedef __attribute__((ext_vector_type(8))) _Float16 f16x8;
typedef __attribute__((ext_vector_type(2))) _Float16 f16x2;
__device__ __forceinline__ uint32_t packh(f32x2 v) { return __builtin_bit_cast(uint32_t, __builtin_convertvector(v, f16x2)); }
__device__ __forceinline__ uint32_t packh(float a, float b) { return packh((f32x2){a, b}); }
constexpr int IP = 224, I_X = 128;
__device__ __forceinline__ f32x2 cmul(f32x2 z, f32x2 w) {
  f32x2 r;
  asm("v_pk_mul_f32 %0, %1, %2 op_sel_hi:[0,1]\n\tv_pk_fma_f32 %0, %1, %2, %0 op_sel:[1,1,0] op_sel_hi:[1,0,1] neg_lo:[0,1,0]" : "=&v"(r) : "v"(z), "v"(w));
  return r;
}
__device__ __forceinline__ f32x2 cexp2(float ar2, float air, float l) {
  const float t = air * l;
  return (f32x2){__builtin_amdgcn_cosf(t), __builtin_amdgcn_sinf(t)} * __builtin_amdgcn_exp2f(ar2 * l);
}
__device__ __forceinline__ float state_a(float ldt, float are, float aim, float& ar2, float& air, float& g) {
  const float dt = rbf(__expf(ldt));
  ar2 = dt * are * 1.4426950408889634f;
  air = dt * aim * 0.15915494309189535f;
  air -= __builtin_rintf(air);
  g = 0.f;
  if (__builtin_expect(__any(ar2 > 0.f), 0)) g = fminf(__uint_as_float(wave_maxu(__float_as_uint(fmaxf(ar2, 0.f)))), 1.f);
  return dt;
}
template <int N> __device__ __forceinline__ void put_im(char* p, int im, const f32x2* z) {
  *(u32x4*)p = (u32x4){packh(z[0][0], z[1][0]), packh(z[2][0], z[3][0]), packh(z[4][0], z[5][0]), packh(z[6][0], z[7][0])};
  *(u32x4*)(p + im) = (u32x4){packh(z[0][1], z[1][1]), packh(z[2][1], z[3][1]), packh(z[4][1], z[5][1]), packh(z[6][1], z[7][1])};
}
#define Y_ASM \
  "v_mov_b64 v[40:41], %0\nv_pk_mul_f32 v[42:43], v[40:41], %1 op_sel_hi:[0,1]\nv_pk_mul_f32 v[44:45], %0, %2 op_sel_hi:[0,1]\n" \
  "v_pk_fma_f32 v[42:43], v[40:41], %1, v[42:43] op_sel:[1,1,0] op_sel_hi:[1,0,1] neg_lo:[0,1,0]\n" \
  "v_pk_fma_f32 v[44:45], %0, %2, v[44:45] op_sel:[1,1,0] op_sel_hi:[1,0,1] neg_lo:[0,1,0]\nv_cvt_pk_f16_f32 v56, v40, v42\n" \
  "v_cvt_pk_f16_f32 v60, v41, v43\nv_pk_mul_f32 v[40:41], v[42:43], %1 op_sel_hi:[0,1]\nv_pk_mul_f32 v[48:49], v[44:45], %2 op_sel_hi:[0,1]\n" \
  "v_pk_fma_f32 v[40:41], v[42:43], %1, v[40:41] op_sel:[1,1,0] op_sel_hi:[1,0,1] neg_lo:[0,1,0]\n" \
  "v_pk_fma_f32 v[48:49], v[44:45], %2, v[48:49] op_sel:[1,1,0] op_sel_hi:[1,0,1] neg_lo:[0,1,0]\n" \
  "v_pk_mul_f32 v[42:43], v[40:41], %1 op_sel_hi:[0,1]\nv_pk_mul_f32 v[52:53], v[48:49], %2 op_sel_hi:[0,1]\n" \
  "v_pk_fma_f32 v[42:43], v[40:41], %1, v[42:43] op_sel:[1,1,0] op_sel_hi:[1,0,1] neg_lo:[0,1,0]\n" \
  "v_pk_fma_f32 v[52:53], v[48:49], %2, v[52:53] op_sel:[1,1,0] op_sel_hi:[1,0,1] neg_lo:[0,1,0]\nv_cvt_pk_f16_f32 v57, v40, v42\n" \
  "v_cvt_pk_f16_f32 v61, v41, v43\nv_pk_mul_f32 v[40:41], v[42:43], %1 op_sel_hi:[0,1]\n" \
  "v_pk_fma_f32 v[40:41], v[42:43], %1, v[40:41] op_sel:[1,1,0] op_sel_hi:[1,0,1] neg_lo:[0,1,0]\n" \
  "v_pk_mul_f32 v[42:43], v[40:41], %1 op_sel_hi:[0,1]\nv_pk_mul_f32 v[46:47], v[44:45], %1 op_sel_hi:[0,1]\n" \
  "v_pk_fma_f32 v[42:43], v[40:41], %1, v[42:43] op_sel:[1,1,0] op_sel_hi:[1,0,1] neg_lo:[0,1,0]\n" \
  "v_pk_fma_f32 v[46:47], v[44:45], %1, v[46:47] op_sel:[1,1,0] op_sel_hi:[1,0,1] neg_lo:[0,1,0]\nv_cvt_pk_f16_f32 v58, v40, v42\n" \
  "v_cvt_pk_f16_f32 v62, v41, v43\nv_cvt_pk_f16_f32 v64, v44, v46\nv_cvt_pk_f16_f32 v68, v45, v47\n" \
  "v_pk_mul_f32 v[40:41], v[42:43], %1 op_sel_hi:[0,1]\nv_pk_mul_f32 v[44:45], v[46:47], %1 op_sel_hi:[0,1]\n" \
  "v_pk_fma_f32 v[40:41], v[42:43], %1, v[40:41] op_sel:[1,1,0] op_sel_hi:[1,0,1] neg_lo:[0,1,0]\n" \
  "v_pk_fma_f32 v[44:45], v[46:47], %1, v[44:45] op_sel:[1,1,0] op_sel_hi:[1,0,1] neg_lo:[0,1,0]\n" \
  "v_pk_mul_f32 v[42:43], v[40:41], %1 op_sel_hi:[0,1]\nv_pk_mul_f32 v[46:47], v[44:45], %1 op_sel_hi:[0,1]\n" \
  "v_pk_fma_f32 v[42:43], v[40:41], %1, v[42:43] op_sel:[1,1,0] op_sel_hi:[1,0,1] neg_lo:[0,1,0]\n" \
  "v_pk_fma_f32 v[46:47], v[44:45], %1, v[46:47] op_sel:[1,1,0] op_sel_hi:[1,0,1] neg_lo:[0,1,0]\nv_cvt_pk_f16_f32 v59, v40, v42\n" \
  "v_cvt_pk_f16_f32 v63, v41, v43\nv_cvt_pk_f16_f32 v65, v44, v46\nv_cvt_pk_f16_f32 v69, v45, v47\n" \
  "v_pk_mul_f32 v[44:45], v[46:47], %1 op_sel_hi:[0,1]\nds_write_b128 %3, v[56:59] offset:0\n" \
  "v_pk_fma_f32 v[44:45], v[46:47], %1, v[44:45] op_sel:[1,1,0] op_sel_hi:[1,0,1] neg_lo:[0,1,0]\nds_write_b128 %3, v[60:63] offset:64\n" \
  "v_pk_mul_f32 v[46:47], v[44:45], %1 op_sel_hi:[0,1]\nv_pk_mul_f32 v[50:51], v[48:49], %1 op_sel_hi:[0,1]\n" \
  "v_pk_fma_f32 v[46:47], v[44:45], %1, v[46:47] op_sel:[1,1,0] op_sel_hi:[1,0,1] neg_lo:[0,1,0]\n" \
  "v_pk_fma_f32 v[50:51], v[48:49], %1, v[50:51] op_sel:[1,1,0] op_sel_hi:[1,0,1] neg_lo:[0,1,0]\nv_cvt_pk_f16_f32 v66, v44, v46\n" \
  "v_cvt_pk_f16_f32 v70, v45, v47\nv_cvt_pk_f16_f32 v56, v48, v50\nv_cvt_pk_f16_f32 v60, v49, v51\n" \
  "v_pk_mul_f32 v[44:45], v[46:47], %1 op_sel_hi:[0,1]\nv_pk_mul_f32 v[48:49], v[50:51], %1 op_sel_hi:[0,1]\n" \
  "v_pk_fma_f32 v[44:45], v[46:47], %1, v[44:45] op_sel:[1,1,0] op_sel_hi:[1,0,1] neg_lo:[0,1,0]\n" \
  "v_pk_fma_f32 v[48:49], v[50:51], %1, v[48:49] op_sel:[1,1,0] op_sel_hi:[1,0,1] neg_lo:[0,1,0]\n" \
  "v_pk_mul_f32 v[46:47], v[44:45], %1 op_sel_hi:[0,1]\nv_pk_mul_f32 v[50:51], v[48:49], %1 op_sel_hi:[0,1]\n" \
  "v_pk_fma_f32 v[46:47], v[44:45], %1, v[46:47] op_sel:[1,1,0] op_sel_hi:[1,0,1] neg_lo:[0,1,0]\n" \
  "v_pk_fma_f32 v[50:51], v[48:49], %1, v[50:51] op_sel:[1,1,0] op_sel_hi:[1,0,1] neg_lo:[0,1,0]\nv_cvt_pk_f16_f32 v67, v44, v46\n" \
  "v_cvt_pk_f16_f32 v71, v45, v47\nv_cvt_pk_f16_f32 v57, v48, v50\nv_cvt_pk_f16_f32 v61, v49, v51\n" \
  "v_pk_mul_f32 v[48:49], v[50:51], %1 op_sel_hi:[0,1]\nds_write_b128 %3, v[64:67] offset:16\n" \
  "v_pk_fma_f32 v[48:49], v[50:51], %1, v[48:49] op_sel:[1,1,0] op_sel_hi:[1,0,1] neg_lo:[0,1,0]\nds_write_b128 %3, v[68:71] offset:80\n" \
  "v_pk_mul_f32 v[50:51], v[48:49], %1 op_sel_hi:[0,1]\nv_pk_mul_f32 v[54:55], v[52:53], %1 op_sel_hi:[0,1]\n" \
  "v_pk_fma_f32 v[50:51], v[48:49], %1, v[50:51] op_sel:[1,1,0] op_sel_hi:[1,0,1] neg_lo:[0,1,0]\n" \
  "v_pk_fma_f32 v[54:55], v[52:53], %1, v[54:55] op_sel:[1,1,0] op_sel_hi:[1,0,1] neg_lo:[0,1,0]\nv_cvt_pk_f16_f32 v58, v48, v50\n" \
  "v_cvt_pk_f16_f32 v62, v49, v51\nv_cvt_pk_f16_f32 v64, v52, v54\nv_cvt_pk_f16_f32 v68, v53, v55\n" \
  "v_pk_mul_f32 v[48:49], v[50:51], %1 op_sel_hi:[0,1]\nv_pk_mul_f32 v[52:53], v[54:55], %1 op_sel_hi:[0,1]\n" \
  "v_pk_fma_f32 v[48:49], v[50:51], %1, v[48:49] op_sel:[1,1,0] op_sel_hi:[1,0,1] neg_lo:[0,1,0]\n" \
  "v_pk_fma_f32 v[52:53], v[54:55], %1, v[52:53] op_sel:[1,1,0] op_sel_hi:[1,0,1] neg_lo:[0,1,0]\n" \
  "v_pk_mul_f32 v[50:51], v[48:49], %1 op_sel_hi:[0,1]\nv_pk_mul_f32 v[54:55], v[52:53], %1 op_sel_hi:[0,1]\n" \
  "v_pk_fma_f32 v[50:51], v[48:49], %1, v[50:51] op_sel:[1,1,0] op_sel_hi:[1,0,1] neg_lo:[0,1,0]\n" \
  "v_pk_fma_f32 v[54:55], v[52:53], %1, v[54:55] op_sel:[1,1,0] op_sel_hi:[1,0,1] neg_lo:[0,1,0]\nv_cvt_pk_f16_f32 v59, v48, v50\n" \
  "v_cvt_pk_f16_f32 v63, v49, v51\nv_cvt_pk_f16_f32 v65, v52, v54\nv_cvt_pk_f16_f32 v69, v53, v55\n" \
  "v_pk_mul_f32 v[52:53], v[54:55], %1 op_sel_hi:[0,1]\nds_write_b128 %3, v[56:59] offset:32\n" \
  "v_pk_fma_f32 v[52:53], v[54:55], %1, v[52:53] op_sel:[1,1,0] op_sel_hi:[1,0,1] neg_lo:[0,1,0]\nds_write_b128 %3, v[60:63] offset:96\n" \
  "v_pk_mul_f32 v[54:55], v[52:53], %1 op_sel_hi:[0,1]\n" \
  "v_pk_fma_f32 v[54:55], v[52:53], %1, v[54:55] op_sel:[1,1,0] op_sel_hi:[1,0,1] neg_lo:[0,1,0]\nv_cvt_pk_f16_f32 v66, v52, v54\n" \
  "v_cvt_pk_f16_f32 v70, v53, v55\nv_pk_mul_f32 v[52:53], v[54:55], %1 op_sel_hi:[0,1]\n" \
  "v_pk_fma_f32 v[52:53], v[54:55], %1, v[52:53] op_sel:[1,1,0] op_sel_hi:[1,0,1] neg_lo:[0,1,0]\n" \
  "v_pk_mul_f32 v[54:55], v[52:53], %1 op_sel_hi:[0,1]\n" \
  "v_pk_fma_f32 v[54:55], v[52:53], %1, v[54:55] op_sel:[1,1,0] op_sel_hi:[1,0,1] neg_lo:[0,1,0]\nv_cvt_pk_f16_f32 v67, v52, v54\n" \
  "v_cvt_pk_f16_f32 v71, v53, v55\nds_write_b128 %3, v[64:67] offset:48\nds_write_b128 %3, v[68:71] offset:112\n"
#define Y_CLOB "memory", "v40", "v41", "v42", "v43", "v44", "v45", "v46", "v47", "v48", "v49", "v50", "v51", "v52", "v53", "v54", "v55", "v56", "v57", "v58", "v59", "v60", "v61", "v62", "v63", "v64", "v65", "v66", "v67", "v68", "v69", "v70", "v71"
#define T_ASM \
  "ds_read_b128 %4, %8\nds_read_b64_tr_b16 v[80:81], %7 offset:0\nds_read_b64_tr_b16 v[82:83], %7 offset:64\n" \
  "ds_read_b64_tr_b16 v[84:85], %5 offset:0\nds_read_b64_tr_b16 v[86:87], %5 offset:48\nds_read_b64_tr_b16 v[88:89], %6 offset:0\n" \
  "ds_read_b64_tr_b16 v[90:91], %6 offset:48\nds_read_b64_tr_b16 v[92:93], %7 offset:3584\nds_read_b64_tr_b16 v[94:95], %7 offset:3648\n" \
  "ds_read_b64_tr_b16 v[96:97], %5 offset:3584\nds_read_b64_tr_b16 v[98:99], %5 offset:3632\nds_read_b64_tr_b16 v[100:101], %6 offset:3584\n" \
  "ds_read_b64_tr_b16 v[102:103], %6 offset:3632\nds_read_b64_tr_b16 v[104:105], %7 offset:7168\nds_read_b64_tr_b16 v[106:107], %7 offset:7232\n" \
  "s_waitcnt lgkmcnt(8)\nv_mfma_f32_16x16x32_f16 %0, v[84:87], v[80:83], 0\nds_read_b64_tr_b16 v[108:109], %5 offset:7168\n" \
  "ds_read_b64_tr_b16 v[110:111], %5 offset:7216\nds_read_b64_tr_b16 v[112:113], %6 offset:7168\nv_mfma_f32_16x16x32_f16 %2, v[88:91], v[80:83], 0\n" \
  "ds_read_b64_tr_b16 v[114:115], %6 offset:7216\nds_read_b64_tr_b16 v[116:117], %7 offset:10752\nds_read_b64_tr_b16 v[118:119], %7 offset:10816\n" \
  "s_waitcnt lgkmcnt(8)\nv_mfma_f32_16x16x32_f16 %0, v[96:99], v[92:95], %0\nds_read_b64_tr_b16 v[120:121], %5 offset:10752\n" \
  "ds_read_b64_tr_b16 v[122:123], %5 offset:10800\nds_read_b64_tr_b16 v[124:125], %6 offset:10752\n" \
  "v_mfma_f32_16x16x32_f16 %2, v[100:103], v[92:95], %2\nds_read_b64_tr_b16 v[126:127], %6 offset:10800\n" \
  "ds_read_b64_tr_b16 v[80:81], %7 offset:14336\nds_read_b64_tr_b16 v[82:83], %7 offset:14400\ns_waitcnt lgkmcnt(8)\n" \
  "v_mfma_f32_16x16x32_f16 %0, v[108:111], v[104:107], %0\nds_read_b64_tr_b16 v[84:85], %5 offset:14336\n" \
  "ds_read_b64_tr_b16 v[86:87], %5 offset:14384\nds_read_b64_tr_b16 v[88:89], %6 offset:14336\n" \
  "v_mfma_f32_16x16x32_f16 %2, v[112:115], v[104:107], %2\nds_read_b64_tr_b16 v[90:91], %6 offset:14384\n" \
  "ds_read_b64_tr_b16 v[92:93], %7 offset:17920\nds_read_b64_tr_b16 v[94:95], %7 offset:17984\ns_waitcnt lgkmcnt(8)\n" \
  "v_mfma_f32_16x16x32_f16 %0, v[120:123], v[116:119], %0\nds_read_b64_tr_b16 v[96:97], %5 offset:17920\n" \
  "ds_read_b64_tr_b16 v[98:99], %5 offset:17968\nds_read_b64_tr_b16 v[100:101], %6 offset:17920\n" \
  "v_mfma_f32_16x16x32_f16 %2, v[124:127], v[116:119], %2\nds_read_b64_tr_b16 v[102:103], %6 offset:17968\n" \
  "ds_read_b64_tr_b16 v[104:105], %7 offset:21504\nds_read_b64_tr_b16 v[106:107], %7 offset:21568\ns_waitcnt lgkmcnt(8)\n" \
  "v_mfma_f32_16x16x32_f16 %1, v[84:87], v[80:83], 0\nds_read_b64_tr_b16 v[108:109], %5 offset:21504\nds_read_b64_tr_b16 v[110:111], %5 offset:21552\n" \
  "ds_read_b64_tr_b16 v[112:113], %6 offset:21504\nv_mfma_f32_16x16x32_f16 %3, v[88:91], v[80:83], 0\nds_read_b64_tr_b16 v[114:115], %6 offset:21552\n" \
  "ds_read_b64_tr_b16 v[116:117], %7 offset:25088\nds_read_b64_tr_b16 v[118:119], %7 offset:25152\ns_waitcnt lgkmcnt(8)\n" \
  "v_mfma_f32_16x16x32_f16 %1, v[96:99], v[92:95], %1\nds_read_b64_tr_b16 v[120:121], %5 offset:25088\n" \
  "ds_read_b64_tr_b16 v[122:123], %5 offset:25136\nds_read_b64_tr_b16 v[124:125], %6 offset:25088\n" \
  "v_mfma_f32_16x16x32_f16 %3, v[100:103], v[92:95], %3\nds_read_b64_tr_b16 v[126:127], %6 offset:25136\ns_waitcnt lgkmcnt(6)\n" \
  "v_mfma_f32_16x16x32_f16 %1, v[108:111], v[104:107], %1\nv_mfma_f32_16x16x32_f16 %3, v[112:115], v[104:107], %3\ns_waitcnt lgkmcnt(0)\n" \
  "v_mfma_f32_16x16x32_f16 %1, v[120:123], v[116:119], %1\nv_mfma_f32_16x16x32_f16 %3, v[124:127], v[116:119], %3\n"
#define T_CLOB "memory", "v80", "v81", "v82", "v83", "v84", "v85", "v86", "v87", "v88", "v89", "v90", "v91", "v92", "v93", "v94", "v95", "v96", "v97", "v98", "v99", "v100", "v101", "v102", "v103", "v104", "v105", "v106", "v107", "v108", "v109", "v110", "v111", "v112", "v113", "v114", "v115", "v116", "v117", "v118", "v119", "v120", "v121", "v122", "v123", "v124", "v125", "v126", "v127"
template <class F> __device__ __forceinline__ int gen_y(float ldt, float are, float aim, float bre, float bim, float cre, float cim, uint32_t im, F&& mid TSP) {
  float ar2, air, g;
  const float dt = state_a(ldt, are, aim, ar2, air, g);
  const float dbr = dt * bre, dbi = dt * bim, pr = cre * dbr - cim * dbi, pi = cre * dbi + cim * dbr;
  const f32x2 w8 = cexp2(ar2, -air, 8.f), w64 = cexp2(ar2, -air, 64.f);
  const float pm = fmaxf(fabsf(pr), fabsf(pi));
  int e = -4;
  if (__builtin_expect(__any(!(pm < 0x1p-4f) || ar2 > 0.f) || !__any(pm > 0x1p-12f), 0)) e = (int)(wave_maxu(__float_as_uint(pm)) >> 23) - 126 + (int)ceilf(g * 250.f);
  const f32x2 y0 = {__builtin_amdgcn_ldexpf(pr, 14 - e), __builtin_amdgcn_ldexpf(-pi, 14 - e)};
  mid();
  TS(0, 7);
  asm volatile(Y_ASM : : "v"(y0), "v"(w8), "v"(w64), "v"(im) : Y_CLOB);
  return e;
}
__device__ __forceinline__ int gen_x(float ldt, float are, float aim, char* im) {
  float ar2, air, g;
  state_a(ldt, are, aim, ar2, air, g);
  const int e = min((int)ceilf(g * 1810.f), 300);
  const float s = __builtin_amdgcn_ldexpf(1.f, 15 - e);
  const f32x2 w1 = cexp2(ar2, air, 1.f), w256 = cexp2(ar2, air, 256.f), w2 = cmul(w1, w1);
  f32x2 a[8], b[8], c[8];
  a[0] = (f32x2){s, 0.f}; a[1] = w1 * s; a[2] = w2 * s;
  b[0] = b[1] = w256 * s;
  b[2] = cmul(b[0], w2); a[3] = cmul(a[1], w2); a[4] = cmul(a[2], w2);
  b[3] = b[4] = b[5] = cmul(b[2], w2); a[5] = cmul(a[3], w2); a[6] = cmul(a[4], w2);
  b[6] = cmul(b[3], w2); a[7] = cmul(a[5], w2); c[0] = cmul(b[3], w256);
  b[7] = cmul(b[6], w2); c[2] = cmul(c[0], w256);
  put_im<0>(im + I_X, 48, a);
  c[1] = cmul(b[7], w256); c[3] = cmul(c[1], w256);
  put_im<0>(im + I_X + 16, 48, b);
#pragma unroll
  for (int k = 4; k < 8; ++k) c[k] = cmul(c[k - 1], w256);
  put_im<0>(im + I_X + 32, 48, c);
  return e;
}

template <int I> struct IC { static constexpr int value = I; };
template <int B, int E, class F> __device__ __forceinline__ void static_for(F&& f) {
  if constexpr (B < E) { f(IC<B>{}); static_for<B + 1, E>(f); }
}
typedef __attribute__((ext_vector_type(4))) short s16x4;
__device__ __forceinline__ u32x2 trd(uint32_t a) {
  return __builtin_bit_cast(u32x2, __builtin_amdgcn_ds_read_tr16_b64_v4i16((__attribute__((address_space(3))) s16x4*)(uintptr_t)a));
}
#define BAR() asm volatile("s_waitcnt lgkmcnt(0)\n\ts_barrier" ::: "memory")
#define ST16(off, v) asm volatile("global_store_dwordx4 %0, %1, %2 sc1\n\ts_nop 1" : : "v"(off), "v"(v), "s"(ws) : "memory")
#define ST4(off, v) asm volatile("global_store_dword %0, %1, %2 sc1" : : "v"(off), "v"(v), "s"(ws) : "memory")
#define LD16(r, off) asm volatile("global_load_dwordx4 %0, %1, %2 sc1" : "=v"(r) : "v"(off), "s"(ws) : "memory")
#define LD16W(r, off) asm volatile("global_load_dwordx4 %0, %1, %2 sc1\n\ts_waitcnt vmcnt(0)" : "=&v"(r) : "v"(off), "s"(ws) : "memory")
#define DRAIN() asm volatile("s_waitcnt vmcnt(0)" ::: "memory")
#define CEX(a, b) { const uint32_t hi_ = max(a, b), lo_ = min(a, b); a = hi_; b = lo_; }
#define MMB(a_, b_, c_) __builtin_amdgcn_mfma_f32_16x16x32_bf16(__builtin_bit_cast(bf16x8, a_), __builtin_bit_cast(bf16x8, b_), c_, 0, 0, 0)
#define NTM 0
#define DPOL ""
#define DPOL2 ""
#define DPOL4 ""
constexpr int SPIN_CAP = 1 << 18;
constexpr uint32_t UMASK = 0x40074007u;
__device__ __forceinline__ uint32_t utag(uint32_t T, int j) {
  return ((T >> (3 * j)) & 0x00070007u) | (((j <= 2) ? (T << (2 - j)) : (T >> (j - 2))) & 0x40004000u);
}
__device__ __forceinline__ uint32_t max4(const u32x4& a) { return max(max(a[0], a[1]), max(a[2], a[3])); }
__device__ __forceinline__ void until(uint32_t t) {
#pragma unroll 1
  for (int spin = 0; spin < 600 && (int)((uint32_t)__builtin_amdgcn_s_memrealtime() - t) < 0; ++spin) __builtin_amdgcn_s_sleep(1);
}
__device__ __forceinline__ uint32_t latest(uint32_t t) {
  const uint32_t t0 = (uint32_t)__builtin_amdgcn_readfirstlane(t) - 0x40000000u;
  return wave_maxu(t - t0) + t0;
}
__device__ __forceinline__ uint32_t gate(const char* ws, uint32_t po, uint32_t T) {
  u32x2 pv;
#pragma unroll 1
  for (int spin = 0; spin < SPIN_CAP; ++spin) {
    asm volatile("global_load_dwordx2 %0, %1, %2 sc1\n\ts_waitcnt vmcnt(0)" : "=&v"(pv) : "v"(po), "s"(ws) : "memory");
    if (!__any(pv[0] != T)) break;
    __builtin_amdgcn_s_sleep(2);
  }
  return pv[1];
}
__device__ __forceinline__ uint32_t gate2(const char* ws, uint32_t oa, uint32_t da, uint32_t ob, uint32_t T) {
  u32x2 pa, pb;
#pragma unroll 1
  for (int spin = 0; spin < SPIN_CAP; ++spin) {
    asm volatile("global_load_dwordx2 %0, %2, %4 sc1\n\tglobal_load_dwordx2 %1, %3, %4 sc1\n\ts_waitcnt vmcnt(0)" : "=&v"(pa), "=&v"(pb) : "v"(oa), "v"(ob), "s"(ws) : "memory");
    if (!__any(pa[0] != T || pb[0] != T)) break;
    __builtin_amdgcn_s_sleep(4);
  }
  pa[1] += da;
  return latest(((int)(pa[1] - pb[1]) > 0) ? pa[1] : pb[1]);
}
__device__ __forceinline__ void gsync(uint32_t a, uint32_t target) {
  uint32_t t, c, n = SPIN_CAP;
  uint64_t ex;
  asm volatile("s_waitcnt lgkmcnt(0)\n\ts_mov_b64 %3, exec\n\ts_mov_b64 exec, 1\n\tv_mov_b32 %0, 1\n\tds_add_u32 %4, %0\n\ts_mov_b64 exec, %3\n"
               "1:\n\ts_sleep 1\n\tds_read_b32 %0, %4\n\ts_sub_u32 %2, %2, 1\n\ts_cbranch_scc1 2f\n\ts_waitcnt lgkmcnt(0)\n\tv_readfirstlane_b32 %1, %0\n\t"
               "s_cmp_lt_u32 %1, %5\n\ts_cbranch_scc1 1b\n2:\n\ts_waitcnt lgkmcnt(0)"
               : "=&v"(t), "=&s"(c), "+s"(n), "=&s"(ex) : "v"(a), "n"(target) : "memory", "scc");
}
__device__ __forceinline__ void gsync0(uint32_t a, uint32_t target) {
  uint32_t t, c, n = SPIN_CAP;
  asm volatile("s_waitcnt lgkmcnt(0)\n\ts_mov_b64 exec, 1\n\tv_mov_b32 %0, 1\n\tds_add_u32 %3, %0\n\ts_mov_b64 exec, -1\n"
               "1:\n\tds_read_b32 %0, %3\n\ts_sub_u32 %2, %2, 1\n\ts_cbranch_scc1 2f\n\ts_waitcnt lgkmcnt(0)\n\tv_readfirstlane_b32 %1, %0\n\t"
               "s_cmp_lt_u32 %1, %4\n\ts_cbranch_scc1 1b\n2:\n\ts_waitcnt lgkmcnt(0)"
               : "=&v"(t), "=&s"(c), "+s"(n) : "v"(a), "n"(target) : "memory", "scc");
}
__device__ __forceinline__ void gwait(uint32_t a, uint32_t target) {
  uint32_t t, c, n = SPIN_CAP;
  asm volatile("1:\n\ts_sleep 2\n\tds_read_b32 %0, %3\n\ts_sub_u32 %2, %2, 1\n\ts_cbranch_scc1 2f\n\ts_waitcnt lgkmcnt(0)\n\tv_readfirstlane_b32 %1, %0\n\t"
               "s_cmp_lt_u32 %1, %4\n\ts_cbranch_scc1 1b\n2:\n\ts_waitcnt lgkmcnt(0)"
               : "=&v"(t), "=&s"(c), "+s"(n) : "v"(a), "n"(target) : "memory", "scc");
}
__device__ __forceinline__ void gwait0(uint32_t a, uint32_t target) {
  uint32_t t, c, n = SPIN_CAP;
  asm volatile("1:\n\tds_read_b32 %0, %3\n\ts_sub_u32 %2, %2, 1\n\ts_cbranch_scc1 2f\n\ts_waitcnt lgkmcnt(0)\n\tv_readfirstlane_b32 %1, %0\n\t"
               "s_cmp_lt_u32 %1, %4\n\ts_cbranch_scc1 1b\n2:\n\ts_waitcnt lgkmcnt(0)"
               : "=&v"(t), "=&s"(c), "+s"(n) : "v"(a), "n"(target) : "memory", "scc");
}
__device__ __forceinline__ void dma16(const void* base, uint32_t voff, uint32_t lds) {
  uint32_t keep;
  asm volatile("s_mov_b32 %0, m0\n\ts_mov_b32 m0, %3\n\ts_nop 0\n\tglobal_load_lds_dwordx4 %1, %2" DPOL "\n\ts_mov_b32 m0, %0" : "=&s"(keep) : "v"(voff), "s"(base), "s"(lds) : "memory");
}
__device__ __forceinline__ void dma16d(const void* base, uint32_t voff, uint32_t lds) {
  uint32_t keep;
  asm volatile("s_mov_b32 %0, m0\n\ts_mov_b32 m0, %3\n\ts_nop 0\n\tglobal_load_lds_dwordx4 %1, %2" DPOL2 "\n\ts_mov_b32 m0, %0" : "=&s"(keep) : "v"(voff), "s"(base), "s"(lds) : "memory");
}
__device__ __forceinline__ void nap(int n) {
  for (; n > 15; n -= 15) __builtin_amdgcn_s_sleep(15);
  switch (n) {
    case 1: __builtin_amdgcn_s_sleep(1); break; case 2: __builtin_amdgcn_s_sleep(2); break; case 3: __builtin_amdgcn_s_sleep(3); break;
    case 4: __builtin_amdgcn_s_sleep(4); break; case 5: __builtin_amdgcn_s_sleep(5); break; case 6: __builtin_amdgcn_s_sleep(6); break;
    case 7: __builtin_amdgcn_s_sleep(7); break; case 8: __builtin_amdgcn_s_sleep(8); break; case 9: __builtin_amdgcn_s_sleep(9); break;
    case 10: __builtin_amdgcn_s_sleep(10); break; case 11: __builtin_amdgcn_s_sleep(11); break; case 12: __builtin_amdgcn_s_sleep(12); break;
    case 13: __builtin_amdgcn_s_sleep(13); break; case 14: __builtin_amdgcn_s_sleep(14); break; case 15: __builtin_amdgcn_s_sleep(15); break;
    default: break;
  }
}
#define SLEEP(n) do { if ((n) > 0) __builtin_amdgcn_s_sleep(n); } while (0)
#define USLEEP 3
#define SSLEEP 11
#define PSLEEP 2
#define PPRIO 0
#define LFIRST 1
#define WD 40
#define OSLEEP 12
#define QPRIO 1
#define KPRIO 2
#define GSLEEP 0
#define NSLEEP 0
#define UD 40
#define XD 0
#define RSLEEP 0
#define UPRIO 2
#define VD2 390
#define WDEPTH 4
#define ESLEEP 0
#define SD 58
#define KD 178
#define OD 0
#define KBACK 6
#define DDEPTH 6
#define C3 1
#define XROT 2
#define WSOFF 0
#define KN 2
#define RPF 1
#define LSLEEP 3
#define TSTOP 70
#define HSTOP 110
#define VEARLY 0ull
#define FPR 3
#define SPR 3
#define DPRIO 0
#define QPRIO2 3
#define HW 2
#define RD 62
#define OPOS 1
#define OD2 0
#define NA 12
#define RD2 88
#define ORD1 ((unsigned __int128)0x321087654u << 36 | 0x321087654ull)
#define WDEPTH2 4
#define RQ NA
#define HREV 1
#define QPRIO0 3
#define VPS 1
#define RW 1
#define NPRIO 0
#define XSL 10
#define V2D 22
#define N0 8
#define LPOS 0
#define FORCESLOW 0
#define FORCEFB 0
#define CONV_ASM \
  "s_cmp_eq_u32 s89, 1\ns_cbranch_scc1 1f\ns_cmp_gt_u32 s89, 1\ns_cbranch_scc1 9f\nds_read_b64 v[124:125], v3 offset:576\n" \
  "ds_read_b64 v[126:127], v3 offset:584\nds_read_b64 v[120:121], v3 offset:64\nds_read_b64 v[122:123], v3 offset:72\n" \
  "ds_read_b64 v[116:117], v3 offset:640\nds_read_b64 v[118:119], v3 offset:648\nds_read_b64 v[112:113], v3 offset:128\n" \
  "ds_read_b64 v[114:115], v3 offset:136\nds_read_b64 v[108:109], v3 offset:704\nds_read_b64 v[110:111], v3 offset:712\n" \
  "ds_read_b64 v[104:105], v3 offset:192\nds_read_b64 v[106:107], v3 offset:200\nds_read_b64 v[100:101], v3 offset:768\n" \
  "ds_read_b64 v[102:103], v3 offset:776\nds_read_b64 v[96:97], v3 offset:256\nds_read_b64 v[98:99], v3 offset:264\n" \
  "ds_read_b64 v[92:93], v3 offset:832\nds_read_b64 v[94:95], v3 offset:840\nds_read_b64 v[88:89], v3 offset:320\n" \
  "ds_read_b64 v[90:91], v3 offset:328\nds_read_b64 v[84:85], v3 offset:896\nds_read_b64 v[86:87], v3 offset:904\n" \
  "ds_read_b64 v[80:81], v3 offset:384\nds_read_b64 v[82:83], v3 offset:392\nds_read_b64 v[76:77], v3 offset:960\n" \
  "ds_read_b64 v[78:79], v3 offset:968\nds_read_b64 v[72:73], v3 offset:448\nds_read_b64 v[74:75], v3 offset:456\n" \
  "ds_read_b64 v[68:69], v3 offset:1024\nds_read_b64 v[70:71], v3 offset:1032\nds_read_b64 v[64:65], v3 offset:512\n" \
  "ds_read_b64 v[66:67], v3 offset:520\nds_read_b64 v[60:61], v3 offset:0\nds_read_b64 v[62:63], v3 offset:8\nds_read_b64 v[56:57], v3 offset:1088\n" \
  "ds_read_b64 v[58:59], v3 offset:1096\ns_waitcnt lgkmcnt(0)\ns_barrier\nds_read_b128 v[36:39], v2 offset:0\nds_read_b128 v[40:43], v2 offset:64\n" \
  "ds_read_b128 v[44:47], v2 offset:128\nds_read_b128 v[48:51], v2 offset:192\ns_waitcnt lgkmcnt(3)\n" \
  "v_mfma_f32_16x16x32_f16 v[12:15], v[124:127], v[36:39], 0\ns_setprio 1\nds_read_b128 v[52:55], v2 offset:256\n" \
  "v_mfma_f32_16x16x32_f16 v[16:19], v[120:123], v[36:39], 0\ns_waitcnt lgkmcnt(3)\nv_mfma_f32_16x16x32_f16 v[12:15], v[116:119], v[40:43], v[12:15]\n" \
  "ds_read_b128 v[36:39], v2 offset:320\nv_mfma_f32_16x16x32_f16 v[16:19], v[112:115], v[40:43], v[16:19]\ns_waitcnt lgkmcnt(3)\n" \
  "v_mfma_f32_16x16x32_f16 v[12:15], v[108:111], v[44:47], v[12:15]\nds_read_b128 v[40:43], v2 offset:384\n" \
  "v_mfma_f32_16x16x32_f16 v[16:19], v[104:107], v[44:47], v[16:19]\ns_waitcnt lgkmcnt(3)\n" \
  "v_mfma_f32_16x16x32_f16 v[12:15], v[100:103], v[48:51], v[12:15]\nds_read_b128 v[44:47], v2 offset:448\n" \
  "v_mfma_f32_16x16x32_f16 v[16:19], v[96:99], v[48:51], v[16:19]\ns_waitcnt lgkmcnt(3)\n" \
  "v_mfma_f32_16x16x32_f16 v[12:15], v[92:95], v[52:55], v[12:15]\nv_mfma_f32_16x16x32_f16 v[16:19], v[88:91], v[52:55], v[16:19]\n" \
  "s_waitcnt lgkmcnt(2)\nv_mfma_f32_16x16x32_f16 v[12:15], v[84:87], v[36:39], v[12:15]\nds_read_b128 v[48:51], v2 offset:512\n" \
  "v_mfma_f32_16x16x32_f16 v[16:19], v[80:83], v[36:39], v[16:19]\ns_waitcnt lgkmcnt(2)\n" \
  "v_mfma_f32_16x16x32_f16 v[12:15], v[76:79], v[40:43], v[12:15]\nds_read_b128 v[52:55], v2 offset:576\n" \
  "v_mfma_f32_16x16x32_f16 v[16:19], v[72:75], v[40:43], v[16:19]\ns_waitcnt lgkmcnt(2)\n" \
  "v_mfma_f32_16x16x32_f16 v[12:15], v[68:71], v[44:47], v[12:15]\nds_read_b128 v[36:39], v2 offset:640\n" \
  "v_mfma_f32_16x16x32_f16 v[16:19], v[64:67], v[44:47], v[16:19]\nv_mfma_f32_16x16x32_f16 v[20:23], v[60:63], v[44:47], 0\n" \
  "ds_read_b128 v[40:43], v2 offset:704\nv_mfma_f32_16x16x32_f16 v[28:31], v[56:59], v[44:47], 0\ns_waitcnt lgkmcnt(3)\n" \
  "v_mfma_f32_16x16x32_f16 v[16:19], v[124:127], v[48:51], v[16:19]\nds_read_b128 v[56:59], v2 offset:768\nv_cvt_pkrtz_f16_f32 v32, v12, v13\n" \
  "v_mfma_f32_16x16x32_f16 v[20:23], v[120:123], v[48:51], v[20:23]\nv_cvt_pkrtz_f16_f32 v33, v14, v15\ns_waitcnt lgkmcnt(3)\n" \
  "v_mfma_f32_16x16x32_f16 v[16:19], v[116:119], v[52:55], v[16:19]\nds_read_b128 v[44:47], v2 offset:832\n" \
  "v_pk_add_u16 v32, v32, 4 op_sel_hi:[1,0] clamp\nv_mfma_f32_16x16x32_f16 v[20:23], v[112:115], v[52:55], v[20:23]\n" \
  "v_pk_add_u16 v33, v33, 4 op_sel_hi:[1,0] clamp\ns_waitcnt lgkmcnt(3)\nv_mfma_f32_16x16x32_f16 v[16:19], v[108:111], v[36:39], v[16:19]\n" \
  "ds_read_b128 v[48:51], v2 offset:896\nds_write_b64 v4, v[32:33] offset:0\nv_mfma_f32_16x16x32_f16 v[20:23], v[104:107], v[36:39], v[20:23]\n" \
  "s_waitcnt lgkmcnt(3)\nv_mfma_f32_16x16x32_f16 v[16:19], v[100:103], v[40:43], v[16:19]\nds_read_b128 v[52:55], v2 offset:960\n" \
  "v_mfma_f32_16x16x32_f16 v[20:23], v[96:99], v[40:43], v[20:23]\nv_mfma_f32_16x16x32_f16 v[16:19], v[92:95], v[56:59], v[16:19]\n" \
  "v_mfma_f32_16x16x32_f16 v[20:23], v[88:91], v[56:59], v[20:23]\nds_read_b128 v[36:39], v2 offset:1024\ns_waitcnt lgkmcnt(3)\n" \
  "v_mfma_f32_16x16x32_f16 v[16:19], v[84:87], v[44:47], v[16:19]\nv_mfma_f32_16x16x32_f16 v[20:23], v[80:83], v[44:47], v[20:23]\n" \
  "ds_read_b128 v[40:43], v2 offset:1088\nds_read_b64 v[56:57], v6 offset:256\nds_read_b64 v[58:59], v6 offset:264\n" \
  "v_mfma_f32_16x16x32_f16 v[16:19], v[76:79], v[48:51], v[16:19]\nv_mfma_f32_16x16x32_f16 v[20:23], v[72:75], v[48:51], v[20:23]\n" \
  "ds_read_b128 v[44:47], v2 offset:1152\ns_waitcnt lgkmcnt(4)\nv_mfma_f32_16x16x32_f16 v[16:19], v[68:71], v[52:55], v[16:19]\n" \
  "v_mfma_f32_16x16x32_f16 v[20:23], v[64:67], v[52:55], v[20:23]\nds_read_b128 v[48:51], v2 offset:1216\n" \
  "v_mfma_f32_16x16x32_f16 v[24:27], v[60:63], v[52:55], 0\nv_mfma_f32_16x16x32_f16 v[20:23], v[124:127], v[36:39], v[20:23]\n" \
  "ds_read_b128 v[52:55], v2 offset:1280\nv_mfma_f32_16x16x32_f16 v[24:27], v[120:123], v[36:39], v[24:27]\ns_waitcnt lgkmcnt(5)\n" \
  "v_mfma_f32_16x16x32_f16 v[20:23], v[116:119], v[40:43], v[20:23]\nds_read_b128 v[36:39], v2 offset:1344\nv_cvt_pkrtz_f16_f32 v32, v16, v17\n" \
  "v_mfma_f32_16x16x32_f16 v[24:27], v[112:115], v[40:43], v[24:27]\nv_cvt_pkrtz_f16_f32 v33, v18, v19\ns_waitcnt lgkmcnt(3)\n" \
  "v_mfma_f32_16x16x32_f16 v[20:23], v[108:111], v[44:47], v[20:23]\nds_read_b128 v[40:43], v2 offset:1408\n" \
  "v_pk_add_u16 v32, v32, 4 op_sel_hi:[1,0] clamp\nds_read_b32 v35, v9 offset:32\nv_mfma_f32_16x16x32_f16 v[24:27], v[104:107], v[44:47], v[24:27]\n" \
  "v_pk_add_u16 v33, v33, 4 op_sel_hi:[1,0] clamp\ns_waitcnt lgkmcnt(3)\nv_mfma_f32_16x16x32_f16 v[20:23], v[100:103], v[48:51], v[20:23]\n" \
  "ds_read_b128 v[44:47], v2 offset:1472\nds_write_b64 v4, v[32:33] offset:1024\nv_mfma_f32_16x16x32_f16 v[24:27], v[96:99], v[48:51], v[24:27]\n" \
  "v_mfma_f32_16x16x32_f16 v[20:23], v[92:95], v[52:55], v[20:23]\nv_mfma_f32_16x16x32_f16 v[24:27], v[88:91], v[52:55], v[24:27]\n" \
  "ds_read_b128 v[48:51], v2 offset:1536\ns_waitcnt lgkmcnt(3)\nv_readfirstlane_b32 s85, v35\ns_cmp_lt_u32 s85, 4\ns_cbranch_scc1 21f\n22:\n" \
  "v_mfma_f32_16x16x32_f16 v[20:23], v[84:87], v[36:39], v[20:23]\nv_mfma_f32_16x16x32_f16 v[24:27], v[80:83], v[36:39], v[24:27]\n" \
  "ds_read_b128 v[52:55], v2 offset:1600\nv_mfma_f32_16x16x32_f16 v[20:23], v[76:79], v[40:43], v[20:23]\n" \
  "v_mfma_f32_16x16x32_f16 v[24:27], v[72:75], v[40:43], v[24:27]\nds_read_b128 v[36:39], v2 offset:1664\ns_waitcnt lgkmcnt(4)\n" \
  "v_mfma_f32_16x16x32_f16 v[20:23], v[68:71], v[44:47], v[20:23]\nv_mfma_f32_16x16x32_f16 v[24:27], v[64:67], v[44:47], v[24:27]\n" \
  "ds_read_b128 v[40:43], v2 offset:1728\nv_mfma_f32_16x16x32_f16 v[28:31], v[60:63], v[44:47], v[28:31]\nds_read_b64_tr_b16 v[60:61], v5 offset:0\n" \
  "ds_read_b64_tr_b16 v[62:63], v5 offset:32\ns_waitcnt lgkmcnt(4)\nv_mfma_f32_16x16x32_f16 v[24:27], v[124:127], v[48:51], v[24:27]\n" \
  "v_mfma_f32_16x16x32_f16 v[28:31], v[120:123], v[48:51], v[28:31]\nds_read_b64_tr_b16 v[44:45], v5 offset:256\n" \
  "ds_read_b64_tr_b16 v[46:47], v5 offset:288\nv_mfma_f32_16x16x32_f16 v[24:27], v[116:119], v[52:55], v[24:27]\n" \
  "ds_read_b64 v[120:121], v6 offset:320\nds_read_b64 v[122:123], v6 offset:328\nv_mfma_f32_16x16x32_f16 v[28:31], v[112:115], v[52:55], v[28:31]\n" \
  "ds_read_b128 v[48:51], v2 offset:1792\ns_waitcnt lgkmcnt(7)\nv_mfma_f32_16x16x32_f16 v[24:27], v[108:111], v[36:39], v[24:27]\n" \
  "ds_read_b128 v[112:115], v2 offset:1856\nv_mfma_f32_16x16x32_f16 v[28:31], v[104:107], v[36:39], v[28:31]\nds_read_b64 v[52:53], v6 offset:128\n" \
  "ds_read_b64 v[54:55], v6 offset:136\nv_mfma_f32_16x16x32_f16 v[24:27], v[100:103], v[40:43], v[24:27]\nds_read_b128 v[104:107], v2 offset:1920\n" \
  "v_mfma_f32_16x16x32_f16 v[28:31], v[96:99], v[40:43], v[28:31]\nds_read_b64 v[36:37], v6 offset:192\nds_read_b64 v[38:39], v6 offset:200\n" \
  "s_waitcnt lgkmcnt(9)\nv_mfma_f32_16x16x32_f16 v[20:23], v[60:63], v[56:59], v[20:23]\nds_read_b128 v[96:99], v2 offset:1984\ns_waitcnt lgkmcnt(7)\n" \
  "v_mfma_f32_16x16x32_f16 v[20:23], v[44:47], v[120:123], v[20:23]\nv_mfma_f32_16x16x32_f16 v[24:27], v[92:95], v[48:51], v[24:27]\n" \
  "ds_read_b128 v[40:43], v2 offset:2048\nv_mfma_f32_16x16x32_f16 v[28:31], v[88:91], v[48:51], v[28:31]\nds_read_b64_tr_b16 v[88:89], v5 offset:512\n" \
  "ds_read_b64_tr_b16 v[90:91], v5 offset:544\ns_waitcnt lgkmcnt(6)\nv_mfma_f32_16x16x32_f16 v[24:27], v[84:87], v[112:115], v[24:27]\n" \
  "v_mfma_f32_16x16x32_f16 v[28:31], v[80:83], v[112:115], v[28:31]\nv_cvt_pkrtz_f16_f32 v32, v20, v21\nv_cvt_pkrtz_f16_f32 v33, v22, v23\n" \
  "v_mfma_f32_16x16x32_f16 v[24:27], v[76:79], v[104:107], v[24:27]\nds_read_b64_tr_b16 v[48:49], v5 offset:768\n" \
  "ds_read_b64_tr_b16 v[50:51], v5 offset:800\nv_mfma_f32_16x16x32_f16 v[28:31], v[72:75], v[104:107], v[28:31]\n" \
  "ds_read_b128 v[80:83], v2 offset:2112\ns_waitcnt lgkmcnt(5)\nv_mfma_f32_16x16x32_f16 v[24:27], v[68:71], v[96:99], v[24:27]\n" \
  "ds_read_b128 v[112:115], v2 offset:2176\nv_pk_add_u16 v32, v32, 4 op_sel_hi:[1,0] clamp\n" \
  "v_mfma_f32_16x16x32_f16 v[28:31], v[64:67], v[96:99], v[28:31]\nds_read_b128 v[72:75], v2 offset:2240\n" \
  "v_pk_add_u16 v33, v33, 4 op_sel_hi:[1,0] clamp\nv_mfma_f32_16x16x32_f16 v[28:31], v[124:127], v[40:43], v[28:31]\n" \
  "ds_read_b128 v[104:107], v2 offset:2304\nds_write_b64 v4, v[32:33] offset:2048\nv_mfma_f32_16x16x32_f16 v[24:27], v[60:63], v[52:55], v[24:27]\n" \
  "ds_read_b128 v[64:67], v2 offset:2368\nds_read_b64 v[96:97], v6 offset:0\nds_read_b64 v[98:99], v6 offset:8\n" \
  "v_mfma_f32_16x16x32_f16 v[24:27], v[44:47], v[36:39], v[24:27]\nds_read_b128 v[124:127], v2 offset:2432\ns_waitcnt lgkmcnt(9)\n" \
  "v_mfma_f32_16x16x32_f16 v[24:27], v[88:91], v[56:59], v[24:27]\nds_read_b128 v[40:43], v2 offset:2496\n" \
  "v_mfma_f32_16x16x32_f16 v[24:27], v[48:51], v[120:123], v[24:27]\ns_waitcnt lgkmcnt(8)\n" \
  "v_mfma_f32_16x16x32_f16 v[28:31], v[116:119], v[80:83], v[28:31]\nds_read_b64 v[116:117], v6 offset:64\nds_read_b64 v[118:119], v6 offset:72\n" \
  "v_mfma_f32_16x16x32_f16 v[28:31], v[108:111], v[112:115], v[28:31]\nds_read_b64_tr_b16 v[80:81], v5 offset:1024\n" \
  "ds_read_b64_tr_b16 v[82:83], v5 offset:1056\ns_waitcnt lgkmcnt(10)\nv_mfma_f32_16x16x32_f16 v[28:31], v[100:103], v[72:75], v[28:31]\n" \
  "v_cvt_pkrtz_f16_f32 v32, v24, v25\nv_cvt_pkrtz_f16_f32 v33, v26, v27\nv_mfma_f32_16x16x32_f16 v[28:31], v[92:95], v[104:107], v[28:31]\n" \
  "v_pk_add_u16 v32, v32, 4 op_sel_hi:[1,0] clamp\ns_waitcnt lgkmcnt(5)\nv_mfma_f32_16x16x32_f16 v[28:31], v[84:87], v[64:67], v[28:31]\n" \
  "ds_read_b64_tr_b16 v[108:109], v5 offset:1280\nds_read_b64_tr_b16 v[110:111], v5 offset:1312\n" \
  "v_mfma_f32_16x16x32_f16 v[28:31], v[76:79], v[124:127], v[28:31]\nds_read_b64_tr_b16 v[112:113], v7 offset:0\n" \
  "ds_read_b64_tr_b16 v[114:115], v7 offset:64\ns_waitcnt lgkmcnt(6)\nv_mfma_f32_16x16x32_f16 v[28:31], v[68:71], v[40:43], v[28:31]\n" \
  "ds_read2_b32 v[100:101], v8 offset0:48 offset1:49\nds_read2_b32 v[102:103], v8 offset0:50 offset1:51\n" \
  "v_mfma_f32_16x16x32_f16 v[28:31], v[60:63], v[96:99], v[28:31]\nv_pk_add_u16 v33, v33, 4 op_sel_hi:[1,0] clamp\n" \
  "ds_write_b64 v4, v[32:33] offset:3072\nv_mfma_f32_16x16x32_f16 v[28:31], v[44:47], v[116:119], v[28:31]\n" \
  "v_mfma_f32_16x16x32_f16 v[28:31], v[88:91], v[52:55], v[28:31]\nv_mfma_f32_16x16x32_f16 v[28:31], v[48:51], v[36:39], v[28:31]\n" \
  "s_waitcnt lgkmcnt(5)\nv_mfma_f32_16x16x32_f16 v[28:31], v[80:83], v[56:59], v[28:31]\n" \
  "v_mfma_f32_16x16x32_f16 v[28:31], v[108:111], v[120:123], v[28:31]\ns_waitcnt lgkmcnt(1)\n" \
  "v_mfma_f32_16x16x32_f16 v[28:31], v[112:115], v[100:103], v[28:31]\ns_nop 7\nv_cvt_pkrtz_f16_f32 v32, v28, v29\nv_cvt_pkrtz_f16_f32 v33, v30, v31\n" \
  "v_pk_add_u16 v32, v32, 4 op_sel_hi:[1,0] clamp\nv_pk_add_u16 v33, v33, 4 op_sel_hi:[1,0] clamp\nds_write_b64 v4, v[32:33] offset:4096\n" \
  "s_setprio 0\ns_setpc_b64 s[90:91]\n21:\ns_movk_i32 s84, 0x4000\n23:\nds_read_b32 v35, v9 offset:32\ns_sub_u32 s84, s84, 1\ns_cbranch_scc1 22b\n" \
  "s_waitcnt lgkmcnt(0)\nv_readfirstlane_b32 s85, v35\ns_cmp_lt_u32 s85, 4\ns_cbranch_scc1 23b\ns_branch 22b\n1:\n" \
  "ds_read_b64 v[124:125], v3 offset:1088\nds_read_b64 v[126:127], v3 offset:1096\nds_read_b64 v[120:121], v3 offset:0\n" \
  "ds_read_b64 v[122:123], v3 offset:8\nds_read_b64 v[116:117], v3 offset:64\nds_read_b64 v[118:119], v3 offset:72\n" \
  "ds_read_b64 v[112:113], v3 offset:128\nds_read_b64 v[114:115], v3 offset:136\nds_read_b64 v[108:109], v3 offset:192\n" \
  "ds_read_b64 v[110:111], v3 offset:200\nds_read_b64 v[104:105], v3 offset:256\nds_read_b64 v[106:107], v3 offset:264\n" \
  "ds_read_b64 v[100:101], v3 offset:320\nds_read_b64 v[102:103], v3 offset:328\nds_read_b64 v[96:97], v3 offset:384\n" \
  "ds_read_b64 v[98:99], v3 offset:392\nds_read_b64 v[92:93], v3 offset:448\nds_read_b64 v[94:95], v3 offset:456\n" \
  "ds_read_b64 v[88:89], v3 offset:512\nds_read_b64 v[90:91], v3 offset:520\nds_read_b64 v[84:85], v3 offset:576\n" \
  "ds_read_b64 v[86:87], v3 offset:584\nds_read_b64 v[80:81], v3 offset:640\nds_read_b64 v[82:83], v3 offset:648\n" \
  "ds_read_b64 v[76:77], v3 offset:704\nds_read_b64 v[78:79], v3 offset:712\nds_read_b64 v[72:73], v3 offset:768\n" \
  "ds_read_b64 v[74:75], v3 offset:776\nds_read_b64 v[68:69], v3 offset:832\nds_read_b64 v[70:71], v3 offset:840\n" \
  "ds_read_b64 v[64:65], v3 offset:896\nds_read_b64 v[66:67], v3 offset:904\nds_read_b64 v[60:61], v3 offset:960\n" \
  "ds_read_b64 v[62:63], v3 offset:968\nds_read_b64 v[56:57], v3 offset:1024\nds_read_b64 v[58:59], v3 offset:1032\n" \
  "ds_read_b64 v[52:53], v6 offset:0\nds_read_b64 v[54:55], v6 offset:8\nds_read_b64 v[48:49], v6 offset:64\nds_read_b64 v[50:51], v6 offset:72\n" \
  "s_waitcnt lgkmcnt(0)\ns_barrier\nds_read_b128 v[28:31], v2 offset:960\nds_read_b128 v[32:35], v2 offset:1472\n" \
  "ds_read_b128 v[36:39], v2 offset:1984\nds_read_b128 v[40:43], v2 offset:2048\nds_read_b128 v[44:47], v2 offset:2112\ns_waitcnt lgkmcnt(3)\n" \
  "v_mfma_f32_16x16x32_f16 v[12:15], v[124:127], v[28:31], 0\ns_setprio 1\nds_read_b128 v[28:31], v2 offset:2176\n" \
  "v_mfma_f32_16x16x32_f16 v[16:19], v[124:127], v[32:35], 0\nds_read_b128 v[32:35], v2 offset:2240\ns_waitcnt lgkmcnt(3)\n" \
  "v_mfma_f32_16x16x32_f16 v[12:15], v[120:123], v[36:39], v[12:15]\nv_mfma_f32_16x16x32_f16 v[20:23], v[124:127], v[36:39], 0\n" \
  "ds_read_b128 v[124:127], v2 offset:2304\nds_read_b128 v[36:39], v2 offset:2368\nv_mfma_f32_16x16x32_f16 v[12:15], v[116:119], v[40:43], v[12:15]\n" \
  "ds_read_b128 v[40:43], v2 offset:2432\ns_waitcnt lgkmcnt(4)\nv_mfma_f32_16x16x32_f16 v[12:15], v[112:115], v[44:47], v[12:15]\n" \
  "ds_read_b128 v[44:47], v2 offset:2496\nv_mfma_f32_16x16x32_f16 v[12:15], v[108:111], v[28:31], v[12:15]\nds_read_b128 v[28:31], v2 offset:2560\n" \
  "s_waitcnt lgkmcnt(4)\nv_mfma_f32_16x16x32_f16 v[12:15], v[104:107], v[32:35], v[12:15]\nds_read_b128 v[32:35], v2 offset:2624\n" \
  "v_mfma_f32_16x16x32_f16 v[12:15], v[100:103], v[124:127], v[12:15]\ns_waitcnt lgkmcnt(3)\n" \
  "v_mfma_f32_16x16x32_f16 v[12:15], v[96:99], v[36:39], v[12:15]\nds_read_b128 v[124:127], v2 offset:2688\n" \
  "v_mfma_f32_16x16x32_f16 v[12:15], v[92:95], v[40:43], v[12:15]\ns_waitcnt lgkmcnt(3)\n" \
  "v_mfma_f32_16x16x32_f16 v[12:15], v[88:91], v[44:47], v[12:15]\nds_read_b128 v[36:39], v2 offset:2752\n" \
  "v_mfma_f32_16x16x32_f16 v[16:19], v[120:123], v[44:47], v[16:19]\ns_waitcnt lgkmcnt(3)\n" \
  "v_mfma_f32_16x16x32_f16 v[12:15], v[84:87], v[28:31], v[12:15]\nds_read_b128 v[40:43], v2 offset:2816\n" \
  "v_mfma_f32_16x16x32_f16 v[16:19], v[116:119], v[28:31], v[16:19]\ns_waitcnt lgkmcnt(3)\n" \
  "v_mfma_f32_16x16x32_f16 v[12:15], v[80:83], v[32:35], v[12:15]\nds_read_b128 v[44:47], v2 offset:2880\n" \
  "v_mfma_f32_16x16x32_f16 v[16:19], v[112:115], v[32:35], v[16:19]\ns_waitcnt lgkmcnt(3)\n" \
  "v_mfma_f32_16x16x32_f16 v[12:15], v[76:79], v[124:127], v[12:15]\nds_read_b128 v[28:31], v2 offset:2944\n" \
  "v_mfma_f32_16x16x32_f16 v[16:19], v[108:111], v[124:127], v[16:19]\ns_waitcnt lgkmcnt(3)\n" \
  "v_mfma_f32_16x16x32_f16 v[12:15], v[72:75], v[36:39], v[12:15]\nds_read_b128 v[32:35], v2 offset:3008\n" \
  "v_mfma_f32_16x16x32_f16 v[16:19], v[104:107], v[36:39], v[16:19]\ns_waitcnt lgkmcnt(3)\n" \
  "v_mfma_f32_16x16x32_f16 v[12:15], v[68:71], v[40:43], v[12:15]\nv_mfma_f32_16x16x32_f16 v[16:19], v[100:103], v[40:43], v[16:19]\n" \
  "ds_read_b128 v[124:127], v2 offset:3072\ns_waitcnt lgkmcnt(3)\nv_mfma_f32_16x16x32_f16 v[12:15], v[64:67], v[44:47], v[12:15]\n" \
  "v_mfma_f32_16x16x32_f16 v[16:19], v[96:99], v[44:47], v[16:19]\nds_read_b128 v[36:39], v2 offset:3136\ns_waitcnt lgkmcnt(3)\n" \
  "v_mfma_f32_16x16x32_f16 v[12:15], v[60:63], v[28:31], v[12:15]\nds_read_b64 v[40:41], v6 offset:128\nds_read_b64 v[42:43], v6 offset:136\n" \
  "v_mfma_f32_16x16x32_f16 v[16:19], v[92:95], v[28:31], v[16:19]\nds_read_b128 v[44:47], v2 offset:3200\ns_waitcnt lgkmcnt(4)\n" \
  "v_mfma_f32_16x16x32_f16 v[12:15], v[56:59], v[32:35], v[12:15]\nv_mfma_f32_16x16x32_f16 v[16:19], v[88:91], v[32:35], v[16:19]\n" \
  "ds_read_b128 v[28:31], v2 offset:3264\nv_mfma_f32_16x16x32_f16 v[20:23], v[120:123], v[32:35], v[20:23]\nds_read_b64 v[120:121], v6 offset:192\n" \
  "ds_read_b64 v[122:123], v6 offset:200\nv_mfma_f32_16x16x32_f16 v[16:19], v[84:87], v[124:127], v[16:19]\nds_read_b128 v[32:35], v2 offset:3328\n" \
  "v_mfma_f32_16x16x32_f16 v[20:23], v[116:119], v[124:127], v[20:23]\nds_read_b64 v[116:117], v6 offset:256\nds_read_b64 v[118:119], v6 offset:264\n" \
  "s_waitcnt lgkmcnt(6)\nv_mfma_f32_16x16x32_f16 v[16:19], v[80:83], v[36:39], v[16:19]\nds_read_b128 v[124:127], v2 offset:3392\n" \
  "v_mfma_f32_16x16x32_f16 v[20:23], v[112:115], v[36:39], v[20:23]\nds_read_b64 v[112:113], v6 offset:320\nds_read_b64 v[114:115], v6 offset:328\n" \
  "v_mfma_f32_16x16x32_f16 v[16:19], v[76:79], v[44:47], v[16:19]\nds_read_b128 v[36:39], v2 offset:3456\n" \
  "v_mfma_f32_16x16x32_f16 v[20:23], v[108:111], v[44:47], v[20:23]\ns_waitcnt lgkmcnt(6)\n" \
  "v_mfma_f32_16x16x32_f16 v[16:19], v[72:75], v[28:31], v[16:19]\nds_read_b128 v[108:111], v2 offset:3520\n" \
  "v_mfma_f32_16x16x32_f16 v[20:23], v[104:107], v[28:31], v[20:23]\nv_mfma_f32_16x16x32_f16 v[16:19], v[68:71], v[32:35], v[16:19]\n" \
  "ds_read_b128 v[44:47], v2 offset:3584\nv_mfma_f32_16x16x32_f16 v[20:23], v[100:103], v[32:35], v[20:23]\nds_read_b128 v[104:107], v2 offset:3648\n" \
  "ds_read_b32 v27, v9 offset:32\ns_waitcnt lgkmcnt(4)\nv_mfma_f32_16x16x32_f16 v[16:19], v[64:67], v[124:127], v[16:19]\n" \
  "ds_read_b128 v[28:31], v2 offset:3712\nv_mfma_f32_16x16x32_f16 v[20:23], v[96:99], v[124:127], v[20:23]\nds_read_b128 v[100:103], v2 offset:3776\n" \
  "ds_read2_b32 v[32:33], v8 offset0:32 offset1:33\nds_read2_b32 v[34:35], v8 offset0:34 offset1:35\n" \
  "v_mfma_f32_16x16x32_f16 v[16:19], v[60:63], v[36:39], v[16:19]\nds_read_b128 v[96:99], v2 offset:3840\n" \
  "v_mfma_f32_16x16x32_f16 v[20:23], v[92:95], v[36:39], v[20:23]\nds_read_b128 v[124:127], v2 offset:3904\ns_waitcnt lgkmcnt(8)\n" \
  "v_mfma_f32_16x16x32_f16 v[16:19], v[56:59], v[108:111], v[16:19]\nds_read_b128 v[92:95], v2 offset:3968\ns_waitcnt lgkmcnt(7)\n" \
  "v_readfirstlane_b32 s85, v27\ns_cmp_lt_u32 s85, 4\ns_cbranch_scc1 31f\n32:\nv_mfma_f32_16x16x32_f16 v[20:23], v[88:91], v[108:111], v[20:23]\n" \
  "ds_read_b128 v[36:39], v2 offset:4032\nds_read_b64_tr_b16 v[88:89], v5 offset:512\nds_read_b64_tr_b16 v[90:91], v5 offset:544\n" \
  "v_mfma_f32_16x16x32_f16 v[20:23], v[84:87], v[44:47], v[20:23]\nds_read_b64_tr_b16 v[108:109], v5 offset:768\n" \
  "ds_read_b64_tr_b16 v[110:111], v5 offset:800\nv_mfma_f32_16x16x32_f16 v[20:23], v[80:83], v[104:107], v[20:23]\n" \
  "ds_read_b64_tr_b16 v[84:85], v5 offset:1024\nds_read_b64_tr_b16 v[86:87], v5 offset:1056\ns_waitcnt lgkmcnt(12)\n" \
  "v_mfma_f32_16x16x32_f16 v[20:23], v[76:79], v[28:31], v[20:23]\nds_read_b64_tr_b16 v[44:45], v5 offset:1280\n" \
  "ds_read_b64_tr_b16 v[46:47], v5 offset:1312\nv_mfma_f32_16x16x32_f16 v[20:23], v[72:75], v[100:103], v[20:23]\ns_waitcnt lgkmcnt(10)\n" \
  "v_mfma_f32_16x16x32_f16 v[20:23], v[68:71], v[96:99], v[20:23]\nds_read_b64_tr_b16 v[80:81], v5 offset:1536\n" \
  "ds_read_b64_tr_b16 v[82:83], v5 offset:1568\nv_mfma_f32_16x16x32_f16 v[20:23], v[64:67], v[124:127], v[20:23]\n" \
  "ds_read_b64_tr_b16 v[104:105], v5 offset:1792\nds_read_b64_tr_b16 v[106:107], v5 offset:1824\ns_waitcnt lgkmcnt(12)\n" \
  "v_mfma_f32_16x16x32_f16 v[20:23], v[60:63], v[92:95], v[20:23]\nds_read_b64_tr_b16 v[76:77], v5 offset:2048\n" \
  "ds_read_b64_tr_b16 v[78:79], v5 offset:2080\nv_mfma_f32_16x16x32_f16 v[20:23], v[56:59], v[36:39], v[20:23]\ns_waitcnt lgkmcnt(10)\n" \
  "v_mfma_f32_16x16x32_f16 v[12:15], v[88:91], v[52:55], v[12:15]\nds_read_b64_tr_b16 v[28:29], v5 offset:2304\n" \
  "ds_read_b64_tr_b16 v[30:31], v5 offset:2336\nv_mfma_f32_16x16x32_f16 v[12:15], v[108:111], v[48:51], v[12:15]\n" \
  "ds_read_b64_tr_b16 v[72:73], v5 offset:2560\nds_read_b64_tr_b16 v[74:75], v5 offset:2592\ns_waitcnt lgkmcnt(10)\n" \
  "v_mfma_f32_16x16x32_f16 v[12:15], v[84:87], v[40:43], v[12:15]\nds_read_b64_tr_b16 v[100:101], v5 offset:2816\n" \
  "ds_read_b64_tr_b16 v[102:103], v5 offset:2848\nv_mfma_f32_16x16x32_f16 v[16:19], v[84:87], v[52:55], v[16:19]\n" \
  "ds_read_b64_tr_b16 v[68:69], v7 offset:0\nds_read_b64_tr_b16 v[70:71], v7 offset:64\n" \
  "v_mfma_f32_16x16x32_f16 v[12:15], v[44:47], v[120:123], v[12:15]\nv_mfma_f32_16x16x32_f16 v[16:19], v[44:47], v[48:51], v[16:19]\n" \
  "s_waitcnt lgkmcnt(10)\nv_mfma_f32_16x16x32_f16 v[12:15], v[80:83], v[116:119], v[12:15]\nds_read2_b32 v[96:97], v8 offset0:16 offset1:17\n" \
  "ds_read2_b32 v[98:99], v8 offset0:18 offset1:19\nv_mfma_f32_16x16x32_f16 v[16:19], v[80:83], v[40:43], v[16:19]\n" \
  "ds_read2_b32 v[64:65], v8 offset0:0 offset1:1\nds_read2_b32 v[66:67], v8 offset0:2 offset1:3\n" \
  "v_mfma_f32_16x16x32_f16 v[20:23], v[80:83], v[52:55], v[20:23]\nv_mfma_f32_16x16x32_f16 v[12:15], v[104:107], v[112:115], v[12:15]\n" \
  "v_mfma_f32_16x16x32_f16 v[16:19], v[104:107], v[120:123], v[16:19]\nv_mfma_f32_16x16x32_f16 v[20:23], v[104:107], v[48:51], v[20:23]\n" \
  "s_waitcnt lgkmcnt(10)\nv_mfma_f32_16x16x32_f16 v[16:19], v[76:79], v[116:119], v[16:19]\nds_read_b64_tr_b16 v[124:125], v7 offset:512\n" \
  "ds_read_b64_tr_b16 v[126:127], v7 offset:576\nv_mfma_f32_16x16x32_f16 v[20:23], v[76:79], v[40:43], v[20:23]\n" \
  "ds_read2_b32 v[60:61], v8 offset0:48 offset1:49\nds_read2_b32 v[62:63], v8 offset0:50 offset1:51\n" \
  "v_mfma_f32_16x16x32_f16 v[16:19], v[28:31], v[112:115], v[16:19]\nv_mfma_f32_16x16x32_f16 v[20:23], v[28:31], v[120:123], v[20:23]\n" \
  "s_waitcnt lgkmcnt(10)\nv_mfma_f32_16x16x32_f16 v[20:23], v[72:75], v[116:119], v[20:23]\nds_read_b64_tr_b16 v[92:93], v7 offset:1024\n" \
  "ds_read_b64_tr_b16 v[94:95], v7 offset:1088\nv_mfma_f32_16x16x32_f16 v[20:23], v[100:103], v[112:115], v[20:23]\n" \
  "ds_read_b64_tr_b16 v[56:57], v7 offset:1536\nds_read_b64_tr_b16 v[58:59], v7 offset:1600\ns_waitcnt lgkmcnt(10)\n" \
  "v_mfma_f32_16x16x32_f16 v[12:15], v[68:71], v[32:35], v[12:15]\nv_mfma_f32_16x16x32_f16 v[16:19], v[68:71], v[96:99], v[16:19]\n" \
  "s_waitcnt lgkmcnt(6)\nv_mfma_f32_16x16x32_f16 v[20:23], v[68:71], v[64:67], v[20:23]\ns_waitcnt lgkmcnt(2)\n" \
  "v_mfma_f32_16x16x32_f16 v[12:15], v[124:127], v[60:63], v[12:15]\nv_mfma_f32_16x16x32_f16 v[16:19], v[124:127], v[32:35], v[16:19]\n" \
  "v_mfma_f32_16x16x32_f16 v[20:23], v[124:127], v[96:99], v[20:23]\nv_mfma_f32_16x16x32_f16 v[16:19], v[92:95], v[60:63], v[16:19]\n" \
  "v_mfma_f32_16x16x32_f16 v[20:23], v[92:95], v[32:35], v[20:23]\ns_waitcnt lgkmcnt(0)\n" \
  "v_mfma_f32_16x16x32_f16 v[20:23], v[56:59], v[60:63], v[20:23]\ns_nop 1\nv_cvt_pkrtz_f16_f32 v24, v12, v13\nv_cvt_pkrtz_f16_f32 v25, v14, v15\n" \
  "v_pk_add_u16 v24, v24, 4 op_sel_hi:[1,0] clamp\nv_pk_add_u16 v25, v25, 4 op_sel_hi:[1,0] clamp\nds_write_b64 v4, v[24:25] offset:5120\n" \
  "v_cvt_pkrtz_f16_f32 v24, v16, v17\nv_cvt_pkrtz_f16_f32 v25, v18, v19\nv_pk_add_u16 v24, v24, 4 op_sel_hi:[1,0] clamp\n" \
  "v_pk_add_u16 v25, v25, 4 op_sel_hi:[1,0] clamp\nds_write_b64 v4, v[24:25] offset:6144\nv_cvt_pkrtz_f16_f32 v24, v20, v21\n" \
  "v_cvt_pkrtz_f16_f32 v25, v22, v23\nv_pk_add_u16 v24, v24, 4 op_sel_hi:[1,0] clamp\nv_pk_add_u16 v25, v25, 4 op_sel_hi:[1,0] clamp\n" \
  "ds_write_b64 v4, v[24:25] offset:7168\ns_setprio 0\ns_setpc_b64 s[90:91]\n31:\ns_movk_i32 s84, 0x4000\n33:\nds_read_b32 v27, v9 offset:32\n" \
  "s_sub_u32 s84, s84, 1\ns_cbranch_scc1 32b\ns_waitcnt lgkmcnt(0)\nv_readfirstlane_b32 s85, v27\ns_cmp_lt_u32 s85, 4\ns_cbranch_scc1 33b\n" \
  "s_branch 32b\n9:\ns_cmp_eq_u32 s89, 3\ns_cbranch_scc1 3f\ns_cmp_eq_u32 s89, 4\ns_cbranch_scc1 4f\ns_cmp_eq_u32 s89, 5\ns_cbranch_scc1 5f\n" \
  "ds_read_b64 v[124:125], v3 offset:576\nds_read_b64 v[126:127], v3 offset:584\nds_read_b64 v[120:121], v3 offset:64\n" \
  "ds_read_b64 v[122:123], v3 offset:72\nds_read_b64 v[116:117], v3 offset:640\nds_read_b64 v[118:119], v3 offset:648\n" \
  "ds_read_b64 v[112:113], v3 offset:128\nds_read_b64 v[114:115], v3 offset:136\nds_read_b64 v[108:109], v3 offset:704\n" \
  "ds_read_b64 v[110:111], v3 offset:712\nds_read_b64 v[104:105], v3 offset:192\nds_read_b64 v[106:107], v3 offset:200\n" \
  "ds_read_b64 v[100:101], v3 offset:768\nds_read_b64 v[102:103], v3 offset:776\nds_read_b64 v[96:97], v3 offset:256\n" \
  "ds_read_b64 v[98:99], v3 offset:264\nds_read_b64 v[92:93], v3 offset:832\nds_read_b64 v[94:95], v3 offset:840\n" \
  "ds_read_b64 v[88:89], v3 offset:320\nds_read_b64 v[90:91], v3 offset:328\nds_read_b64 v[84:85], v3 offset:896\n" \
  "ds_read_b64 v[86:87], v3 offset:904\nds_read_b64 v[80:81], v3 offset:384\nds_read_b64 v[82:83], v3 offset:392\n" \
  "ds_read_b64 v[76:77], v3 offset:960\nds_read_b64 v[78:79], v3 offset:968\nds_read_b64 v[72:73], v3 offset:448\n" \
  "ds_read_b64 v[74:75], v3 offset:456\nds_read_b64 v[68:69], v3 offset:1024\nds_read_b64 v[70:71], v3 offset:1032\n" \
  "ds_read_b64 v[64:65], v3 offset:512\nds_read_b64 v[66:67], v3 offset:520\ns_waitcnt lgkmcnt(0)\ns_barrier\nds_read_b128 v[24:27], v2 offset:0\n" \
  "ds_read_b128 v[28:31], v2 offset:64\nds_read_b128 v[32:35], v2 offset:128\nds_read_b128 v[36:39], v2 offset:192\ns_waitcnt lgkmcnt(3)\n" \
  "v_mfma_f32_16x16x32_f16 v[12:15], v[124:127], v[24:27], 0\ns_setprio 1\nds_read_b128 v[40:43], v2 offset:256\n" \
  "v_mfma_f32_16x16x32_f16 v[16:19], v[120:123], v[24:27], 0\ns_waitcnt lgkmcnt(3)\nv_mfma_f32_16x16x32_f16 v[12:15], v[116:119], v[28:31], v[12:15]\n" \
  "ds_read_b128 v[44:47], v2 offset:320\nv_mfma_f32_16x16x32_f16 v[16:19], v[112:115], v[28:31], v[16:19]\ns_waitcnt lgkmcnt(3)\n" \
  "v_mfma_f32_16x16x32_f16 v[12:15], v[108:111], v[32:35], v[12:15]\nds_read_b128 v[48:51], v2 offset:384\n" \
  "v_mfma_f32_16x16x32_f16 v[16:19], v[104:107], v[32:35], v[16:19]\ns_waitcnt lgkmcnt(3)\n" \
  "v_mfma_f32_16x16x32_f16 v[12:15], v[100:103], v[36:39], v[12:15]\nds_read_b128 v[52:55], v2 offset:448\n" \
  "v_mfma_f32_16x16x32_f16 v[16:19], v[96:99], v[36:39], v[16:19]\ns_waitcnt lgkmcnt(3)\n" \
  "v_mfma_f32_16x16x32_f16 v[12:15], v[92:95], v[40:43], v[12:15]\nds_read_b128 v[56:59], v2 offset:512\n" \
  "v_mfma_f32_16x16x32_f16 v[16:19], v[88:91], v[40:43], v[16:19]\nds_read_b128 v[60:63], v2 offset:576\ns_waitcnt lgkmcnt(3)\n" \
  "v_mfma_f32_16x16x32_f16 v[12:15], v[84:87], v[44:47], v[12:15]\nds_read_b128 v[120:123], v2 offset:640\n" \
  "v_mfma_f32_16x16x32_f16 v[16:19], v[80:83], v[44:47], v[16:19]\nds_read_b128 v[24:27], v2 offset:704\n" \
  "v_mfma_f32_16x16x32_f16 v[12:15], v[76:79], v[48:51], v[12:15]\nds_read_b128 v[112:115], v2 offset:768\n" \
  "v_mfma_f32_16x16x32_f16 v[16:19], v[72:75], v[48:51], v[16:19]\nds_read_b128 v[28:31], v2 offset:832\ns_waitcnt lgkmcnt(5)\n" \
  "v_mfma_f32_16x16x32_f16 v[12:15], v[68:71], v[52:55], v[12:15]\nds_read_b128 v[104:107], v2 offset:896\n" \
  "v_mfma_f32_16x16x32_f16 v[16:19], v[64:67], v[52:55], v[16:19]\nds_read_b128 v[32:35], v2 offset:960\n" \
  "v_mfma_f32_16x16x32_f16 v[16:19], v[124:127], v[56:59], v[16:19]\ns_waitcnt lgkmcnt(5)\n" \
  "v_mfma_f32_16x16x32_f16 v[16:19], v[116:119], v[60:63], v[16:19]\nv_mfma_f32_16x16x32_f16 v[16:19], v[108:111], v[120:123], v[16:19]\n" \
  "s_waitcnt lgkmcnt(3)\nv_mfma_f32_16x16x32_f16 v[16:19], v[100:103], v[24:27], v[16:19]\nv_cvt_pkrtz_f16_f32 v20, v12, v13\n" \
  "v_cvt_pkrtz_f16_f32 v21, v14, v15\nv_mfma_f32_16x16x32_f16 v[16:19], v[92:95], v[112:115], v[16:19]\n" \
  "v_pk_add_u16 v20, v20, 4 op_sel_hi:[1,0] clamp\ns_waitcnt lgkmcnt(2)\nv_mfma_f32_16x16x32_f16 v[16:19], v[84:87], v[28:31], v[16:19]\n" \
  "v_pk_add_u16 v21, v21, 4 op_sel_hi:[1,0] clamp\ns_waitcnt lgkmcnt(1)\nv_mfma_f32_16x16x32_f16 v[16:19], v[76:79], v[104:107], v[16:19]\n" \
  "ds_write_b64 v4, v[20:21] offset:0\ns_waitcnt lgkmcnt(1)\nv_mfma_f32_16x16x32_f16 v[16:19], v[68:71], v[32:35], v[16:19]\ns_nop 7\n" \
  "v_cvt_pkrtz_f16_f32 v20, v16, v17\nv_cvt_pkrtz_f16_f32 v21, v18, v19\nv_pk_add_u16 v20, v20, 4 op_sel_hi:[1,0] clamp\n" \
  "v_pk_add_u16 v21, v21, 4 op_sel_hi:[1,0] clamp\nds_write_b64 v4, v[20:21] offset:1024\ns_setprio 0\ns_setpc_b64 s[90:91]\n3:\n" \
  "ds_read_b64 v[124:125], v3 offset:0\nds_read_b64 v[126:127], v3 offset:8\nds_read_b64 v[120:121], v3 offset:64\n" \
  "ds_read_b64 v[122:123], v3 offset:72\nds_read_b64 v[116:117], v3 offset:128\nds_read_b64 v[118:119], v3 offset:136\n" \
  "ds_read_b64 v[112:113], v3 offset:192\nds_read_b64 v[114:115], v3 offset:200\nds_read_b64 v[108:109], v3 offset:256\n" \
  "ds_read_b64 v[110:111], v3 offset:264\nds_read_b64 v[104:105], v3 offset:320\nds_read_b64 v[106:107], v3 offset:328\n" \
  "ds_read_b64 v[100:101], v3 offset:384\nds_read_b64 v[102:103], v3 offset:392\nds_read_b64 v[96:97], v3 offset:448\n" \
  "ds_read_b64 v[98:99], v3 offset:456\nds_read_b64 v[92:93], v3 offset:512\nds_read_b64 v[94:95], v3 offset:520\n" \
  "ds_read_b64 v[88:89], v3 offset:576\nds_read_b64 v[90:91], v3 offset:584\nds_read_b64 v[84:85], v3 offset:640\n" \
  "ds_read_b64 v[86:87], v3 offset:648\nds_read_b64 v[80:81], v3 offset:704\nds_read_b64 v[82:83], v3 offset:712\n" \
  "ds_read_b64 v[76:77], v3 offset:768\nds_read_b64 v[78:79], v3 offset:776\nds_read_b64 v[72:73], v3 offset:832\n" \
  "ds_read_b64 v[74:75], v3 offset:840\nds_read_b64 v[68:69], v3 offset:896\nds_read_b64 v[70:71], v3 offset:904\n" \
  "ds_read_b64 v[64:65], v3 offset:960\nds_read_b64 v[66:67], v3 offset:968\nds_read_b64 v[60:61], v3 offset:1024\n" \
  "ds_read_b64 v[62:63], v3 offset:1032\nds_read_b64 v[56:57], v6 offset:256\nds_read_b64 v[58:59], v6 offset:264\n" \
  "ds_read_b64 v[52:53], v6 offset:128\nds_read_b64 v[54:55], v6 offset:136\nds_read_b64 v[48:49], v6 offset:320\n" \
  "ds_read_b64 v[50:51], v6 offset:328\nds_read_b64 v[44:45], v6 offset:192\nds_read_b64 v[46:47], v6 offset:200\ns_waitcnt lgkmcnt(0)\ns_barrier\n" \
  "ds_read_b128 v[24:27], v2 offset:448\nds_read_b128 v[28:31], v2 offset:512\nds_read_b128 v[32:35], v2 offset:576\n" \
  "ds_read_b128 v[36:39], v2 offset:640\nds_read_b128 v[40:43], v2 offset:704\ns_waitcnt lgkmcnt(3)\n" \
  "v_mfma_f32_16x16x32_f16 v[12:15], v[124:127], v[24:27], 0\ns_setprio 1\nds_read_b128 v[24:27], v2 offset:768\n" \
  "v_mfma_f32_16x16x32_f16 v[12:15], v[120:123], v[28:31], v[12:15]\nds_read_b128 v[28:31], v2 offset:832\ns_waitcnt lgkmcnt(3)\n" \
  "v_mfma_f32_16x16x32_f16 v[12:15], v[116:119], v[32:35], v[12:15]\nds_read_b128 v[32:35], v2 offset:896\n" \
  "v_mfma_f32_16x16x32_f16 v[12:15], v[112:115], v[36:39], v[12:15]\nds_read_b128 v[36:39], v2 offset:960\ns_waitcnt lgkmcnt(3)\n" \
  "v_mfma_f32_16x16x32_f16 v[12:15], v[108:111], v[40:43], v[12:15]\nds_read_b128 v[40:43], v2 offset:1024\n" \
  "v_mfma_f32_16x16x32_f16 v[12:15], v[104:107], v[24:27], v[12:15]\nds_read_b128 v[24:27], v2 offset:1088\ns_waitcnt lgkmcnt(3)\n" \
  "v_mfma_f32_16x16x32_f16 v[12:15], v[100:103], v[28:31], v[12:15]\nds_read_b128 v[28:31], v2 offset:1152\n" \
  "v_mfma_f32_16x16x32_f16 v[12:15], v[96:99], v[32:35], v[12:15]\ns_waitcnt lgkmcnt(3)\n" \
  "v_mfma_f32_16x16x32_f16 v[12:15], v[92:95], v[36:39], v[12:15]\nds_read_b128 v[32:35], v2 offset:1216\n" \
  "v_mfma_f32_16x16x32_f16 v[16:19], v[124:127], v[36:39], 0\ns_waitcnt lgkmcnt(3)\nv_mfma_f32_16x16x32_f16 v[12:15], v[88:91], v[40:43], v[12:15]\n" \
  "ds_read_b128 v[124:127], v2 offset:1280\nv_mfma_f32_16x16x32_f16 v[16:19], v[120:123], v[40:43], v[16:19]\ns_waitcnt lgkmcnt(3)\n" \
  "v_mfma_f32_16x16x32_f16 v[12:15], v[84:87], v[24:27], v[12:15]\nds_read_b128 v[36:39], v2 offset:1344\n" \
  "v_mfma_f32_16x16x32_f16 v[16:19], v[116:119], v[24:27], v[16:19]\ns_waitcnt lgkmcnt(3)\n" \
  "v_mfma_f32_16x16x32_f16 v[12:15], v[80:83], v[28:31], v[12:15]\nds_read_b128 v[120:123], v2 offset:1408\n" \
  "v_mfma_f32_16x16x32_f16 v[16:19], v[112:115], v[28:31], v[16:19]\ns_waitcnt lgkmcnt(3)\n" \
  "v_mfma_f32_16x16x32_f16 v[12:15], v[76:79], v[32:35], v[12:15]\nds_read_b128 v[40:43], v2 offset:1472\n" \
  "v_mfma_f32_16x16x32_f16 v[16:19], v[108:111], v[32:35], v[16:19]\ns_waitcnt lgkmcnt(3)\n" \
  "v_mfma_f32_16x16x32_f16 v[12:15], v[72:75], v[124:127], v[12:15]\nds_read_b128 v[116:119], v2 offset:1536\n" \
  "v_mfma_f32_16x16x32_f16 v[16:19], v[104:107], v[124:127], v[16:19]\nds_read_b128 v[24:27], v2 offset:1600\ns_waitcnt lgkmcnt(3)\n" \
  "v_mfma_f32_16x16x32_f16 v[12:15], v[68:71], v[36:39], v[12:15]\nds_read_b128 v[112:115], v2 offset:1664\nds_read_b32 v23, v9 offset:32\n" \
  "v_mfma_f32_16x16x32_f16 v[16:19], v[100:103], v[36:39], v[16:19]\nds_read_b128 v[28:31], v2 offset:1728\n" \
  "v_mfma_f32_16x16x32_f16 v[12:15], v[64:67], v[120:123], v[12:15]\nds_read_b128 v[108:111], v2 offset:1792\n" \
  "v_mfma_f32_16x16x32_f16 v[16:19], v[96:99], v[120:123], v[16:19]\nds_read_b128 v[32:35], v2 offset:1856\ns_waitcnt lgkmcnt(6)\n" \
  "v_mfma_f32_16x16x32_f16 v[12:15], v[60:63], v[40:43], v[12:15]\nds_read_b128 v[104:107], v2 offset:1920\n" \
  "v_mfma_f32_16x16x32_f16 v[16:19], v[92:95], v[40:43], v[16:19]\nds_read_b128 v[124:127], v2 offset:1984\ns_waitcnt lgkmcnt(5)\n" \
  "v_readfirstlane_b32 s85, v23\ns_cmp_lt_u32 s85, 3\ns_cbranch_scc1 51f\n52:\nv_mfma_f32_16x16x32_f16 v[16:19], v[88:91], v[116:119], v[16:19]\n" \
  "ds_read_b64_tr_b16 v[100:101], v5 offset:0\nds_read_b64_tr_b16 v[102:103], v5 offset:32\n" \
  "v_mfma_f32_16x16x32_f16 v[16:19], v[84:87], v[24:27], v[16:19]\nds_read_b64_tr_b16 v[36:37], v5 offset:256\n" \
  "ds_read_b64_tr_b16 v[38:39], v5 offset:288\nv_mfma_f32_16x16x32_f16 v[16:19], v[80:83], v[112:115], v[16:19]\n" \
  "ds_read_b64_tr_b16 v[96:97], v5 offset:512\nds_read_b64_tr_b16 v[98:99], v5 offset:544\ns_waitcnt lgkmcnt(9)\n" \
  "v_mfma_f32_16x16x32_f16 v[16:19], v[76:79], v[28:31], v[16:19]\nds_read_b64_tr_b16 v[120:121], v5 offset:768\n" \
  "ds_read_b64_tr_b16 v[122:123], v5 offset:800\nv_mfma_f32_16x16x32_f16 v[16:19], v[72:75], v[108:111], v[16:19]\ns_waitcnt lgkmcnt(9)\n" \
  "v_mfma_f32_16x16x32_f16 v[16:19], v[68:71], v[32:35], v[16:19]\nv_mfma_f32_16x16x32_f16 v[16:19], v[64:67], v[104:107], v[16:19]\n" \
  "s_waitcnt lgkmcnt(6)\nv_mfma_f32_16x16x32_f16 v[16:19], v[60:63], v[124:127], v[16:19]\n" \
  "v_mfma_f32_16x16x32_f16 v[12:15], v[100:103], v[56:59], v[12:15]\nv_mfma_f32_16x16x32_f16 v[16:19], v[100:103], v[52:55], v[16:19]\n" \
  "s_waitcnt lgkmcnt(2)\nv_mfma_f32_16x16x32_f16 v[12:15], v[36:39], v[48:51], v[12:15]\n" \
  "v_mfma_f32_16x16x32_f16 v[16:19], v[36:39], v[44:47], v[16:19]\nv_mfma_f32_16x16x32_f16 v[16:19], v[96:99], v[56:59], v[16:19]\n" \
  "s_waitcnt lgkmcnt(0)\nv_mfma_f32_16x16x32_f16 v[16:19], v[120:123], v[48:51], v[16:19]\ns_nop 3\nv_cvt_pkrtz_f16_f32 v20, v12, v13\n" \
  "v_cvt_pkrtz_f16_f32 v21, v14, v15\nv_pk_add_u16 v20, v20, 4 op_sel_hi:[1,0] clamp\nv_pk_add_u16 v21, v21, 4 op_sel_hi:[1,0] clamp\n" \
  "ds_write_b64 v4, v[20:21] offset:2048\nv_cvt_pkrtz_f16_f32 v20, v16, v17\nv_cvt_pkrtz_f16_f32 v21, v18, v19\n" \
  "v_pk_add_u16 v20, v20, 4 op_sel_hi:[1,0] clamp\nv_pk_add_u16 v21, v21, 4 op_sel_hi:[1,0] clamp\nds_write_b64 v4, v[20:21] offset:3072\n" \
  "s_setprio 0\ns_setpc_b64 s[90:91]\n51:\ns_movk_i32 s84, 0x4000\n53:\nds_read_b32 v23, v9 offset:32\ns_sub_u32 s84, s84, 1\ns_cbranch_scc1 52b\n" \
  "s_waitcnt lgkmcnt(0)\nv_readfirstlane_b32 s85, v23\ns_cmp_lt_u32 s85, 3\ns_cbranch_scc1 53b\ns_branch 52b\n4:\n" \
  "ds_read_b64 v[124:125], v3 offset:1088\nds_read_b64 v[126:127], v3 offset:1096\nds_read_b64 v[120:121], v3 offset:0\n" \
  "ds_read_b64 v[122:123], v3 offset:8\nds_read_b64 v[116:117], v3 offset:64\nds_read_b64 v[118:119], v3 offset:72\n" \
  "ds_read_b64 v[112:113], v3 offset:128\nds_read_b64 v[114:115], v3 offset:136\nds_read_b64 v[108:109], v3 offset:192\n" \
  "ds_read_b64 v[110:111], v3 offset:200\nds_read_b64 v[104:105], v3 offset:256\nds_read_b64 v[106:107], v3 offset:264\n" \
  "ds_read_b64 v[100:101], v3 offset:320\nds_read_b64 v[102:103], v3 offset:328\nds_read_b64 v[96:97], v3 offset:384\n" \
  "ds_read_b64 v[98:99], v3 offset:392\nds_read_b64 v[92:93], v3 offset:448\nds_read_b64 v[94:95], v3 offset:456\n" \
  "ds_read_b64 v[88:89], v3 offset:512\nds_read_b64 v[90:91], v3 offset:520\nds_read_b64 v[84:85], v3 offset:576\n" \
  "ds_read_b64 v[86:87], v3 offset:584\nds_read_b64 v[80:81], v3 offset:640\nds_read_b64 v[82:83], v3 offset:648\n" \
  "ds_read_b64 v[76:77], v3 offset:704\nds_read_b64 v[78:79], v3 offset:712\nds_read_b64 v[72:73], v3 offset:768\n" \
  "ds_read_b64 v[74:75], v3 offset:776\nds_read_b64 v[68:69], v3 offset:832\nds_read_b64 v[70:71], v3 offset:840\n" \
  "ds_read_b64 v[64:65], v3 offset:896\nds_read_b64 v[66:67], v3 offset:904\nds_read_b64 v[60:61], v3 offset:960\n" \
  "ds_read_b64 v[62:63], v3 offset:968\nds_read_b64 v[56:57], v3 offset:1024\nds_read_b64 v[58:59], v3 offset:1032\n" \
  "ds_read_b64 v[52:53], v6 offset:0\nds_read_b64 v[54:55], v6 offset:8\nds_read_b64 v[48:49], v6 offset:64\nds_read_b64 v[50:51], v6 offset:72\n" \
  "ds_read_b64 v[44:45], v6 offset:128\nds_read_b64 v[46:47], v6 offset:136\nds_read_b64 v[40:41], v6 offset:192\n" \
  "ds_read_b64 v[42:43], v6 offset:200\ns_waitcnt lgkmcnt(0)\ns_barrier\nds_read_b128 v[24:27], v2 offset:448\nds_read_b128 v[28:31], v2 offset:960\n" \
  "ds_read_b128 v[32:35], v2 offset:1472\nds_read_b128 v[36:39], v2 offset:1536\ns_waitcnt lgkmcnt(3)\n" \
  "v_mfma_f32_16x16x32_f16 v[12:15], v[124:127], v[24:27], 0\ns_setprio 1\nds_read_b128 v[24:27], v2 offset:1600\ns_waitcnt lgkmcnt(3)\n" \
  "v_mfma_f32_16x16x32_f16 v[16:19], v[124:127], v[28:31], 0\nds_read_b128 v[124:127], v2 offset:1664\ns_waitcnt lgkmcnt(3)\n" \
  "v_mfma_f32_16x16x32_f16 v[12:15], v[120:123], v[32:35], v[12:15]\nds_read_b128 v[28:31], v2 offset:1728\ns_waitcnt lgkmcnt(3)\n" \
  "v_mfma_f32_16x16x32_f16 v[12:15], v[116:119], v[36:39], v[12:15]\nds_read_b128 v[32:35], v2 offset:1792\ns_waitcnt lgkmcnt(3)\n" \
  "v_mfma_f32_16x16x32_f16 v[12:15], v[112:115], v[24:27], v[12:15]\nds_read_b128 v[36:39], v2 offset:1856\ns_waitcnt lgkmcnt(3)\n" \
  "v_mfma_f32_16x16x32_f16 v[12:15], v[108:111], v[124:127], v[12:15]\nds_read_b128 v[24:27], v2 offset:1920\ns_waitcnt lgkmcnt(3)\n" \
  "v_mfma_f32_16x16x32_f16 v[12:15], v[104:107], v[28:31], v[12:15]\nds_read_b128 v[124:127], v2 offset:1984\ns_waitcnt lgkmcnt(3)\n" \
  "v_mfma_f32_16x16x32_f16 v[12:15], v[100:103], v[32:35], v[12:15]\nds_read_b128 v[28:31], v2 offset:2048\ns_waitcnt lgkmcnt(3)\n" \
  "v_mfma_f32_16x16x32_f16 v[12:15], v[96:99], v[36:39], v[12:15]\nds_read_b128 v[32:35], v2 offset:2112\ns_waitcnt lgkmcnt(3)\n" \
  "v_mfma_f32_16x16x32_f16 v[12:15], v[92:95], v[24:27], v[12:15]\nds_read_b128 v[36:39], v2 offset:2176\ns_waitcnt lgkmcnt(3)\n" \
  "v_mfma_f32_16x16x32_f16 v[12:15], v[88:91], v[124:127], v[12:15]\nds_read_b128 v[24:27], v2 offset:2240\n" \
  "v_mfma_f32_16x16x32_f16 v[16:19], v[120:123], v[124:127], v[16:19]\nds_read_b64 v[120:121], v6 offset:256\nds_read_b64 v[122:123], v6 offset:264\n" \
  "s_waitcnt lgkmcnt(4)\nv_mfma_f32_16x16x32_f16 v[12:15], v[84:87], v[28:31], v[12:15]\nds_read_b128 v[124:127], v2 offset:2304\n" \
  "v_mfma_f32_16x16x32_f16 v[16:19], v[116:119], v[28:31], v[16:19]\nds_read_b64 v[116:117], v6 offset:320\nds_read_b64 v[118:119], v6 offset:328\n" \
  "v_mfma_f32_16x16x32_f16 v[12:15], v[80:83], v[32:35], v[12:15]\nds_read_b128 v[28:31], v2 offset:2368\n" \
  "v_mfma_f32_16x16x32_f16 v[16:19], v[112:115], v[32:35], v[16:19]\ns_waitcnt lgkmcnt(6)\n" \
  "v_mfma_f32_16x16x32_f16 v[12:15], v[76:79], v[36:39], v[12:15]\nds_read_b128 v[112:115], v2 offset:2432\n" \
  "v_mfma_f32_16x16x32_f16 v[16:19], v[108:111], v[36:39], v[16:19]\nds_read2_b32 v[32:33], v8 offset0:48 offset1:49\n" \
  "ds_read2_b32 v[34:35], v8 offset0:50 offset1:51\nv_mfma_f32_16x16x32_f16 v[12:15], v[72:75], v[24:27], v[12:15]\n" \
  "ds_read_b128 v[108:111], v2 offset:2496\nv_mfma_f32_16x16x32_f16 v[16:19], v[104:107], v[24:27], v[16:19]\n" \
  "ds_read2_b32 v[36:37], v8 offset0:32 offset1:33\nds_read2_b32 v[38:39], v8 offset0:34 offset1:35\ns_waitcnt lgkmcnt(6)\n" \
  "v_mfma_f32_16x16x32_f16 v[12:15], v[68:71], v[124:127], v[12:15]\nds_read_b128 v[104:107], v2 offset:2560\n" \
  "v_mfma_f32_16x16x32_f16 v[16:19], v[100:103], v[124:127], v[16:19]\nds_read_b128 v[24:27], v2 offset:2624\n" \
  "v_mfma_f32_16x16x32_f16 v[12:15], v[64:67], v[28:31], v[12:15]\nds_read_b128 v[100:103], v2 offset:2688\nds_read_b32 v23, v9 offset:32\n" \
  "v_mfma_f32_16x16x32_f16 v[16:19], v[96:99], v[28:31], v[16:19]\nds_read_b128 v[124:127], v2 offset:2752\ns_waitcnt lgkmcnt(7)\n" \
  "v_mfma_f32_16x16x32_f16 v[12:15], v[60:63], v[112:115], v[12:15]\nds_read_b128 v[96:99], v2 offset:2816\n" \
  "v_mfma_f32_16x16x32_f16 v[16:19], v[92:95], v[112:115], v[16:19]\nds_read_b128 v[28:31], v2 offset:2880\n" \
  "v_mfma_f32_16x16x32_f16 v[12:15], v[56:59], v[108:111], v[12:15]\nds_read_b128 v[92:95], v2 offset:2944\n" \
  "v_mfma_f32_16x16x32_f16 v[16:19], v[88:91], v[108:111], v[16:19]\nds_read_b128 v[112:115], v2 offset:3008\ns_waitcnt lgkmcnt(5)\n" \
  "v_readfirstlane_b32 s85, v23\ns_cmp_lt_u32 s85, 3\ns_cbranch_scc1 61f\n62:\nv_mfma_f32_16x16x32_f16 v[16:19], v[84:87], v[104:107], v[16:19]\n" \
  "ds_read_b64_tr_b16 v[88:89], v5 offset:0\nds_read_b64_tr_b16 v[90:91], v5 offset:32\n" \
  "v_mfma_f32_16x16x32_f16 v[16:19], v[80:83], v[24:27], v[16:19]\nds_read_b64_tr_b16 v[108:109], v5 offset:256\n" \
  "ds_read_b64_tr_b16 v[110:111], v5 offset:288\nv_mfma_f32_16x16x32_f16 v[16:19], v[76:79], v[100:103], v[16:19]\n" \
  "ds_read_b64_tr_b16 v[84:85], v5 offset:512\nds_read_b64_tr_b16 v[86:87], v5 offset:544\ns_waitcnt lgkmcnt(9)\n" \
  "v_mfma_f32_16x16x32_f16 v[16:19], v[72:75], v[124:127], v[16:19]\nds_read_b64_tr_b16 v[104:105], v5 offset:768\n" \
  "ds_read_b64_tr_b16 v[106:107], v5 offset:800\nv_mfma_f32_16x16x32_f16 v[16:19], v[68:71], v[96:99], v[16:19]\n" \
  "ds_read_b64_tr_b16 v[80:81], v5 offset:1024\nds_read_b64_tr_b16 v[82:83], v5 offset:1056\ns_waitcnt lgkmcnt(11)\n" \
  "v_mfma_f32_16x16x32_f16 v[16:19], v[64:67], v[28:31], v[16:19]\nds_read_b64_tr_b16 v[24:25], v5 offset:1280\n" \
  "ds_read_b64_tr_b16 v[26:27], v5 offset:1312\nv_mfma_f32_16x16x32_f16 v[16:19], v[60:63], v[92:95], v[16:19]\n" \
  "ds_read_b64_tr_b16 v[76:77], v5 offset:1536\nds_read_b64_tr_b16 v[78:79], v5 offset:1568\ns_waitcnt lgkmcnt(12)\n" \
  "v_mfma_f32_16x16x32_f16 v[16:19], v[56:59], v[112:115], v[16:19]\nds_read_b64_tr_b16 v[100:101], v5 offset:1792\n" \
  "ds_read_b64_tr_b16 v[102:103], v5 offset:1824\nv_mfma_f32_16x16x32_f16 v[12:15], v[88:91], v[52:55], v[12:15]\ns_waitcnt lgkmcnt(10)\n" \
  "v_mfma_f32_16x16x32_f16 v[12:15], v[108:111], v[48:51], v[12:15]\nds_read_b64_tr_b16 v[72:73], v7 offset:0\n" \
  "ds_read_b64_tr_b16 v[74:75], v7 offset:64\nv_mfma_f32_16x16x32_f16 v[12:15], v[84:87], v[44:47], v[12:15]\n" \
  "ds_read_b64_tr_b16 v[124:125], v7 offset:512\nds_read_b64_tr_b16 v[126:127], v7 offset:576\n" \
  "v_mfma_f32_16x16x32_f16 v[16:19], v[84:87], v[52:55], v[16:19]\ns_waitcnt lgkmcnt(10)\n" \
  "v_mfma_f32_16x16x32_f16 v[12:15], v[104:107], v[40:43], v[12:15]\nv_mfma_f32_16x16x32_f16 v[16:19], v[104:107], v[48:51], v[16:19]\n" \
  "v_mfma_f32_16x16x32_f16 v[12:15], v[80:83], v[120:123], v[12:15]\nv_mfma_f32_16x16x32_f16 v[16:19], v[80:83], v[44:47], v[16:19]\n" \
  "s_waitcnt lgkmcnt(6)\nv_mfma_f32_16x16x32_f16 v[12:15], v[24:27], v[116:119], v[12:15]\n" \
  "v_mfma_f32_16x16x32_f16 v[16:19], v[24:27], v[40:43], v[16:19]\nv_mfma_f32_16x16x32_f16 v[16:19], v[76:79], v[120:123], v[16:19]\n" \
  "s_waitcnt lgkmcnt(2)\nv_mfma_f32_16x16x32_f16 v[16:19], v[100:103], v[116:119], v[16:19]\n" \
  "v_mfma_f32_16x16x32_f16 v[12:15], v[72:75], v[32:35], v[12:15]\nv_mfma_f32_16x16x32_f16 v[16:19], v[72:75], v[36:39], v[16:19]\n" \
  "s_waitcnt lgkmcnt(0)\nv_mfma_f32_16x16x32_f16 v[16:19], v[124:127], v[32:35], v[16:19]\ns_nop 4\nv_cvt_pkrtz_f16_f32 v20, v12, v13\n" \
  "v_cvt_pkrtz_f16_f32 v21, v14, v15\nv_pk_add_u16 v20, v20, 4 op_sel_hi:[1,0] clamp\nv_pk_add_u16 v21, v21, 4 op_sel_hi:[1,0] clamp\n" \
  "ds_write_b64 v4, v[20:21] offset:4096\nv_cvt_pkrtz_f16_f32 v20, v16, v17\nv_cvt_pkrtz_f16_f32 v21, v18, v19\n" \
  "v_pk_add_u16 v20, v20, 4 op_sel_hi:[1,0] clamp\nv_pk_add_u16 v21, v21, 4 op_sel_hi:[1,0] clamp\nds_write_b64 v4, v[20:21] offset:5120\n" \
  "s_setprio 0\ns_setpc_b64 s[90:91]\n61:\ns_movk_i32 s84, 0x4000\n63:\nds_read_b32 v23, v9 offset:32\ns_sub_u32 s84, s84, 1\ns_cbranch_scc1 62b\n" \
  "s_waitcnt lgkmcnt(0)\nv_readfirstlane_b32 s85, v23\ns_cmp_lt_u32 s85, 3\ns_cbranch_scc1 63b\ns_branch 62b\n5:\n" \
  "ds_read_b64 v[124:125], v3 offset:1088\nds_read_b64 v[126:127], v3 offset:1096\nds_read_b64 v[120:121], v3 offset:0\n" \
  "ds_read_b64 v[122:123], v3 offset:8\nds_read_b64 v[116:117], v3 offset:64\nds_read_b64 v[118:119], v3 offset:72\n" \
  "ds_read_b64 v[112:113], v3 offset:128\nds_read_b64 v[114:115], v3 offset:136\nds_read_b64 v[108:109], v3 offset:192\n" \
  "ds_read_b64 v[110:111], v3 offset:200\nds_read_b64 v[104:105], v3 offset:256\nds_read_b64 v[106:107], v3 offset:264\n" \
  "ds_read_b64 v[100:101], v3 offset:320\nds_read_b64 v[102:103], v3 offset:328\nds_read_b64 v[96:97], v3 offset:384\n" \
  "ds_read_b64 v[98:99], v3 offset:392\nds_read_b64 v[92:93], v3 offset:448\nds_read_b64 v[94:95], v3 offset:456\n" \
  "ds_read_b64 v[88:89], v3 offset:512\nds_read_b64 v[90:91], v3 offset:520\nds_read_b64 v[84:85], v3 offset:576\n" \
  "ds_read_b64 v[86:87], v3 offset:584\nds_read_b64 v[80:81], v3 offset:640\nds_read_b64 v[82:83], v3 offset:648\n" \
  "ds_read_b64 v[76:77], v3 offset:704\nds_read_b64 v[78:79], v3 offset:712\nds_read_b64 v[72:73], v3 offset:768\n" \
  "ds_read_b64 v[74:75], v3 offset:776\nds_read_b64 v[68:69], v3 offset:832\nds_read_b64 v[70:71], v3 offset:840\n" \
  "ds_read_b64 v[64:65], v3 offset:896\nds_read_b64 v[66:67], v3 offset:904\nds_read_b64 v[60:61], v3 offset:960\n" \
  "ds_read_b64 v[62:63], v3 offset:968\nds_read_b64 v[56:57], v3 offset:1024\nds_read_b64 v[58:59], v3 offset:1032\n" \
  "ds_read_b64 v[52:53], v6 offset:0\nds_read_b64 v[54:55], v6 offset:8\nds_read_b64 v[48:49], v6 offset:64\nds_read_b64 v[50:51], v6 offset:72\n" \
  "ds_read_b64 v[44:45], v6 offset:128\nds_read_b64 v[46:47], v6 offset:136\nds_read_b64 v[40:41], v6 offset:192\n" \
  "ds_read_b64 v[42:43], v6 offset:200\ns_waitcnt lgkmcnt(0)\ns_barrier\nds_read_b128 v[24:27], v2 offset:1472\n" \
  "ds_read_b128 v[28:31], v2 offset:1984\nds_read_b128 v[32:35], v2 offset:2496\nds_read_b128 v[36:39], v2 offset:2560\ns_waitcnt lgkmcnt(3)\n" \
  "v_mfma_f32_16x16x32_f16 v[12:15], v[124:127], v[24:27], 0\ns_setprio 1\nds_read_b128 v[24:27], v2 offset:2624\ns_waitcnt lgkmcnt(3)\n" \
  "v_mfma_f32_16x16x32_f16 v[16:19], v[124:127], v[28:31], 0\nds_read_b128 v[124:127], v2 offset:2688\ns_waitcnt lgkmcnt(3)\n" \
  "v_mfma_f32_16x16x32_f16 v[12:15], v[120:123], v[32:35], v[12:15]\nds_read_b128 v[28:31], v2 offset:2752\ns_waitcnt lgkmcnt(3)\n" \
  "v_mfma_f32_16x16x32_f16 v[12:15], v[116:119], v[36:39], v[12:15]\nds_read_b128 v[32:35], v2 offset:2816\ns_waitcnt lgkmcnt(3)\n" \
  "v_mfma_f32_16x16x32_f16 v[12:15], v[112:115], v[24:27], v[12:15]\nds_read_b128 v[36:39], v2 offset:2880\ns_waitcnt lgkmcnt(3)\n" \
  "v_mfma_f32_16x16x32_f16 v[12:15], v[108:111], v[124:127], v[12:15]\nds_read_b128 v[24:27], v2 offset:2944\ns_waitcnt lgkmcnt(3)\n" \
  "v_mfma_f32_16x16x32_f16 v[12:15], v[104:107], v[28:31], v[12:15]\nds_read_b128 v[124:127], v2 offset:3008\ns_waitcnt lgkmcnt(3)\n" \
  "v_mfma_f32_16x16x32_f16 v[12:15], v[100:103], v[32:35], v[12:15]\nds_read_b128 v[28:31], v2 offset:3072\ns_waitcnt lgkmcnt(3)\n" \
  "v_mfma_f32_16x16x32_f16 v[12:15], v[96:99], v[36:39], v[12:15]\nds_read_b128 v[32:35], v2 offset:3136\ns_waitcnt lgkmcnt(3)\n" \
  "v_mfma_f32_16x16x32_f16 v[12:15], v[92:95], v[24:27], v[12:15]\nds_read_b128 v[36:39], v2 offset:3200\ns_waitcnt lgkmcnt(3)\n" \
  "v_mfma_f32_16x16x32_f16 v[12:15], v[88:91], v[124:127], v[12:15]\nds_read_b128 v[24:27], v2 offset:3264\n" \
  "v_mfma_f32_16x16x32_f16 v[16:19], v[120:123], v[124:127], v[16:19]\nds_read_b64 v[120:121], v6 offset:256\nds_read_b64 v[122:123], v6 offset:264\n" \
  "s_waitcnt lgkmcnt(4)\nv_mfma_f32_16x16x32_f16 v[12:15], v[84:87], v[28:31], v[12:15]\nds_read_b128 v[124:127], v2 offset:3328\n" \
  "v_mfma_f32_16x16x32_f16 v[16:19], v[116:119], v[28:31], v[16:19]\nds_read_b64 v[116:117], v6 offset:320\nds_read_b64 v[118:119], v6 offset:328\n" \
  "v_mfma_f32_16x16x32_f16 v[12:15], v[80:83], v[32:35], v[12:15]\nds_read_b128 v[28:31], v2 offset:3392\n" \
  "v_mfma_f32_16x16x32_f16 v[16:19], v[112:115], v[32:35], v[16:19]\ns_waitcnt lgkmcnt(6)\n" \
  "v_mfma_f32_16x16x32_f16 v[12:15], v[76:79], v[36:39], v[12:15]\nds_read_b128 v[112:115], v2 offset:3456\n" \
  "v_mfma_f32_16x16x32_f16 v[16:19], v[108:111], v[36:39], v[16:19]\nds_read2_b32 v[32:33], v8 offset0:16 offset1:17\n" \
  "ds_read2_b32 v[34:35], v8 offset0:18 offset1:19\nv_mfma_f32_16x16x32_f16 v[12:15], v[72:75], v[24:27], v[12:15]\n" \
  "ds_read_b128 v[108:111], v2 offset:3520\nv_mfma_f32_16x16x32_f16 v[16:19], v[104:107], v[24:27], v[16:19]\n" \
  "ds_read2_b32 v[36:37], v8 offset0:0 offset1:1\nds_read2_b32 v[38:39], v8 offset0:2 offset1:3\ns_waitcnt lgkmcnt(6)\n" \
  "v_mfma_f32_16x16x32_f16 v[12:15], v[68:71], v[124:127], v[12:15]\nds_read_b128 v[104:107], v2 offset:3584\n" \
  "v_mfma_f32_16x16x32_f16 v[16:19], v[100:103], v[124:127], v[16:19]\nds_read_b128 v[24:27], v2 offset:3648\n" \
  "ds_read2_b32 v[100:101], v8 offset0:32 offset1:33\nds_read2_b32 v[102:103], v8 offset0:34 offset1:35\n" \
  "v_mfma_f32_16x16x32_f16 v[12:15], v[64:67], v[28:31], v[12:15]\nds_read_b128 v[124:127], v2 offset:3712\nds_read_b32 v23, v9 offset:32\n" \
  "v_mfma_f32_16x16x32_f16 v[16:19], v[96:99], v[28:31], v[16:19]\nds_read_b128 v[96:99], v2 offset:3776\ns_waitcnt lgkmcnt(9)\n" \
  "v_mfma_f32_16x16x32_f16 v[12:15], v[60:63], v[112:115], v[12:15]\nds_read_b128 v[28:31], v2 offset:3840\n" \
  "v_mfma_f32_16x16x32_f16 v[16:19], v[92:95], v[112:115], v[16:19]\nds_read_b128 v[92:95], v2 offset:3904\n" \
  "v_mfma_f32_16x16x32_f16 v[12:15], v[56:59], v[108:111], v[12:15]\nds_read_b128 v[112:115], v2 offset:3968\n" \
  "v_mfma_f32_16x16x32_f16 v[16:19], v[88:91], v[108:111], v[16:19]\nds_read_b128 v[88:91], v2 offset:4032\ns_waitcnt lgkmcnt(5)\n" \
  "v_readfirstlane_b32 s85, v23\ns_cmp_lt_u32 s85, 3\ns_cbranch_scc1 71f\n72:\nv_mfma_f32_16x16x32_f16 v[16:19], v[84:87], v[104:107], v[16:19]\n" \
  "ds_read_b64_tr_b16 v[108:109], v5 offset:1024\nds_read_b64_tr_b16 v[110:111], v5 offset:1056\n" \
  "v_mfma_f32_16x16x32_f16 v[16:19], v[80:83], v[24:27], v[16:19]\nds_read_b64_tr_b16 v[84:85], v5 offset:1280\n" \
  "ds_read_b64_tr_b16 v[86:87], v5 offset:1312\nv_mfma_f32_16x16x32_f16 v[16:19], v[76:79], v[124:127], v[16:19]\n" \
  "ds_read_b64_tr_b16 v[104:105], v5 offset:1536\nds_read_b64_tr_b16 v[106:107], v5 offset:1568\ns_waitcnt lgkmcnt(9)\n" \
  "v_mfma_f32_16x16x32_f16 v[16:19], v[72:75], v[96:99], v[16:19]\nds_read_b64_tr_b16 v[80:81], v5 offset:1792\n" \
  "ds_read_b64_tr_b16 v[82:83], v5 offset:1824\nv_mfma_f32_16x16x32_f16 v[16:19], v[68:71], v[28:31], v[16:19]\n" \
  "ds_read_b64_tr_b16 v[24:25], v5 offset:2048\nds_read_b64_tr_b16 v[26:27], v5 offset:2080\ns_waitcnt lgkmcnt(11)\n" \
  "v_mfma_f32_16x16x32_f16 v[16:19], v[64:67], v[92:95], v[16:19]\nds_read_b64_tr_b16 v[76:77], v5 offset:2304\n" \
  "ds_read_b64_tr_b16 v[78:79], v5 offset:2336\nv_mfma_f32_16x16x32_f16 v[16:19], v[60:63], v[112:115], v[16:19]\n" \
  "ds_read_b64_tr_b16 v[124:125], v5 offset:2560\nds_read_b64_tr_b16 v[126:127], v5 offset:2592\ns_waitcnt lgkmcnt(12)\n" \
  "v_mfma_f32_16x16x32_f16 v[16:19], v[56:59], v[88:91], v[16:19]\nds_read_b64_tr_b16 v[72:73], v5 offset:2816\n" \
  "ds_read_b64_tr_b16 v[74:75], v5 offset:2848\nv_mfma_f32_16x16x32_f16 v[12:15], v[108:111], v[52:55], v[12:15]\ns_waitcnt lgkmcnt(10)\n" \
  "v_mfma_f32_16x16x32_f16 v[12:15], v[84:87], v[48:51], v[12:15]\nds_read_b64_tr_b16 v[96:97], v7 offset:0\n" \
  "ds_read_b64_tr_b16 v[98:99], v7 offset:64\nv_mfma_f32_16x16x32_f16 v[12:15], v[104:107], v[44:47], v[12:15]\n" \
  "ds_read_b64_tr_b16 v[68:69], v7 offset:512\nds_read_b64_tr_b16 v[70:71], v7 offset:576\n" \
  "v_mfma_f32_16x16x32_f16 v[16:19], v[104:107], v[52:55], v[16:19]\ns_waitcnt lgkmcnt(10)\n" \
  "v_mfma_f32_16x16x32_f16 v[12:15], v[80:83], v[40:43], v[12:15]\nds_read_b64_tr_b16 v[28:29], v7 offset:1024\n" \
  "ds_read_b64_tr_b16 v[30:31], v7 offset:1088\nv_mfma_f32_16x16x32_f16 v[16:19], v[80:83], v[48:51], v[16:19]\n" \
  "ds_read2_b32 v[64:65], v8 offset0:48 offset1:49\nds_read2_b32 v[66:67], v8 offset0:50 offset1:51\n" \
  "v_mfma_f32_16x16x32_f16 v[12:15], v[24:27], v[120:123], v[12:15]\nv_mfma_f32_16x16x32_f16 v[16:19], v[24:27], v[44:47], v[16:19]\n" \
  "s_waitcnt lgkmcnt(10)\nv_mfma_f32_16x16x32_f16 v[12:15], v[76:79], v[116:119], v[12:15]\nds_read_b64_tr_b16 v[92:93], v7 offset:1536\n" \
  "ds_read_b64_tr_b16 v[94:95], v7 offset:1600\nv_mfma_f32_16x16x32_f16 v[16:19], v[76:79], v[40:43], v[16:19]\n" \
  "v_mfma_f32_16x16x32_f16 v[16:19], v[124:127], v[120:123], v[16:19]\ns_waitcnt lgkmcnt(8)\n" \
  "v_mfma_f32_16x16x32_f16 v[16:19], v[72:75], v[116:119], v[16:19]\nv_mfma_f32_16x16x32_f16 v[12:15], v[96:99], v[32:35], v[12:15]\n" \
  "v_mfma_f32_16x16x32_f16 v[16:19], v[96:99], v[36:39], v[16:19]\ns_waitcnt lgkmcnt(4)\n" \
  "v_mfma_f32_16x16x32_f16 v[12:15], v[68:71], v[100:103], v[12:15]\nv_mfma_f32_16x16x32_f16 v[16:19], v[68:71], v[32:35], v[16:19]\n" \
  "s_waitcnt lgkmcnt(2)\nv_mfma_f32_16x16x32_f16 v[12:15], v[28:31], v[64:67], v[12:15]\n" \
  "v_mfma_f32_16x16x32_f16 v[16:19], v[28:31], v[100:103], v[16:19]\ns_waitcnt lgkmcnt(0)\n" \
  "v_mfma_f32_16x16x32_f16 v[16:19], v[92:95], v[64:67], v[16:19]\ns_nop 4\nv_cvt_pkrtz_f16_f32 v20, v12, v13\nv_cvt_pkrtz_f16_f32 v21, v14, v15\n" \
  "v_pk_add_u16 v20, v20, 4 op_sel_hi:[1,0] clamp\nv_pk_add_u16 v21, v21, 4 op_sel_hi:[1,0] clamp\nds_write_b64 v4, v[20:21] offset:6144\n" \
  "v_cvt_pkrtz_f16_f32 v20, v16, v17\nv_cvt_pkrtz_f16_f32 v21, v18, v19\nv_pk_add_u16 v20, v20, 4 op_sel_hi:[1,0] clamp\n" \
  "v_pk_add_u16 v21, v21, 4 op_sel_hi:[1,0] clamp\nds_write_b64 v4, v[20:21] offset:7168\ns_setprio 0\ns_setpc_b64 s[90:91]\n71:\n" \
  "s_movk_i32 s84, 0x4000\n73:\nds_read_b32 v23, v9 offset:32\ns_sub_u32 s84, s84, 1\ns_cbranch_scc1 72b\ns_waitcnt lgkmcnt(0)\n" \
  "v_readfirstlane_b32 s85, v23\ns_cmp_lt_u32 s85, 3\ns_cbranch_scc1 73b\ns_branch 72b\n"
#define CONV_CLOB "memory", "scc", "s84", "s85", "s90", "s91", "v12", "v13", "v14", "v15", "v16", "v17", "v18", "v19", "v20", "v21", "v22", "v23", "v24", "v25", "v26", "v27", "v28", "v29", "v30", "v31", "v32", "v33", "v34", "v35", "v36", "v37", "v38", "v39", "v40", "v41", "v42", "v43", "v44", "v45", "v46", "v47", "v48", "v49", "v50", "v51", "v52", "v53", "v54", "v55", "v56", "v57", "v58", "v59", "v60", "v61", "v62", "v63", "v64", "v65", "v66", "v67", "v68", "v69", "v70", "v71", "v72", "v73", "v74", "v75", "v76", "v77", "v78", "v79", "v80", "v81", "v82", "v83", "v84", "v85", "v86", "v87", "v88", "v89", "v90", "v91", "v92", "v93", "v94", "v95", "v96", "v97", "v98", "v99", "v100", "v101", "v102", "v103", "v104", "v105", "v106", "v107", "v108", "v109", "v110", "v111", "v112", "v113", "v114", "v115", "v116", "v117", "v118", "v119", "v120", "v121", "v122", "v123", "v124", "v125", "v126", "v127"
constexpr int TDP = 1216, T_3 = 4 * TDP, T_4 = T_3 + 512, TB = T_4 + 320;
constexpr int C_U = 480, UB = C_U + 4096 + 96, C_G5 = 0, C_G3 = 3200, C_G9 = 6400, C_G7 = C_G9 + 2096, GB = C_G7 + 2080 + 112;
constexpr int O_FL = 2 * TB, O_UL = O_FL + 64, O_IM = O_UL + 2 * UB, O_ST = O_IM + 2 * GB;
constexpr int X_TB = 10240, X_UL = X_TB + TB, X_IM = X_UL + UB, X_ST = X_IM + GB;
constexpr int X_WR = X_IM + 128 * IP;
#define X_RW(w) (LW + 9216 * (2 + ((w) >> 1)) + 2560 * ((w) & 1))
constexpr int RP = 1216;
constexpr int LW = 36864 + 512, LA = LW, LM = LW + 36864;
constexpr int L_SC = LM + 2048, SMEM = O_IM + 256 * IP;
static_assert(L_SC + 1152 <= SMEM && O_ST + 8192 <= SMEM && X_IM + 128 * IP <= LM && X_ST + 8192 <= LM && UB % 128 == 64 && GB % 128 == 64 && SMEM <= 79872, "");
struct TOps { uint32_t xa, xb, ya, pd, pf, pg, pe; };
__device__ __forceinline__ void table_ops(uint32_t im, uint32_t tb, int ct, int lane, TOps& t) {
  const int j = lane & 15, kq = lane >> 4, q4 = (lane >> 2) & 3, p4 = lane & 3, jj = j + 16 * ct;
  const uint32_t sa = im + (uint32_t)(IP * (lane >> 2));
  t.xa = sa + (uint32_t)(I_X + 8 * p4); t.xb = sa + (uint32_t)(I_X + 32 + 8 * (p4 & 1)); t.ya = sa + (uint32_t)(32 * ct + 8 * p4);
  t.pd = tb + (uint32_t)(2 * (541 - (8 * jj + 4 * (kq & 1) + 256 * (kq >> 1))));
  t.pf = tb + (uint32_t)((kq & 1) ? T_4 + 2 * (63 - jj) : T_3 + 2 * (127 - 2 * jj));
  t.pg = t.pf - ((kq & 1) ? 64 : 2);
  t.pe = tb + (uint32_t)(p4 * (TDP + 2) + ((lane < 12) ? 2 * (32 - q4) : 2 * (572 + (q4 & 1))));
  asm volatile("" : "+v"(t.xa), "+v"(t.xb), "+v"(t.ya), "+v"(t.pd), "+v"(t.pf), "+v"(t.pg), "+v"(t.pe));
}
typedef __attribute__((address_space(3))) char* lptr;
#define LP(T, a) ((__attribute__((address_space(3))) T*)(uintptr_t)(a))
__device__ __forceinline__ uint32_t table_tile(const TOps& t, int ct, int lane, uint32_t evp, float dsk TSP) {
  u32x4 ev;
  f32x4 ad[2], af[2];
  asm volatile(T_ASM : "=&v"(ad[0]), "=&v"(ad[1]), "=&v"(af[0]), "=&v"(af[1]), "=&v"(ev) : "v"(t.xa), "v"(t.xb), "v"(t.ya), "v"(evp) : T_CLOB);
  TS(0, 4);
  const int e0 = (int)(ev[0] + ev[2]), e1 = (int)(ev[1] + ev[3]);
  const int em = min(max(max(max(e0, e1), (int)((__float_as_uint(dsk) >> 23) & 0xffu) - 133), -110), 115);
  const int ts = ((uint32_t)(em + 12) <= 8u) ? 1 : -3 - em;
  float s0 = __builtin_amdgcn_ldexpf(1.f, e0 - 29 + ts), s1 = __builtin_amdgcn_ldexpf(1.f, e1 - 29 + ts);
  float d0 = (ct == 0 && lane == 0) ? __builtin_amdgcn_ldexpf(dsk, ts) : 0.f;
  asm volatile("s_nop 1" : "+v"(ad[0]), "+v"(ad[1]), "+v"(af[0]), "+v"(af[1]), "+v"(s0), "+v"(s1), "+v"(d0));
  const f32x4 k = ad[0] * s0 + (f32x4){d0, 0.f, 0.f, 0.f} + ad[1] * s1, f = af[0] * s0 + af[1] * s1;
  auto pk = [](float a, float b) { uint32_t r; asm("v_cvt_pk_f16_f32 %0, %1, %2" : "=v"(r) : "v"(a), "v"(b)); return r; };
  asm volatile("s_nop 0" : : "v"(k), "v"(f));
  const uint32_t p32 = pk(k[3], k[2]), p10 = pk(k[1], k[0]), p21 = pk(k[2], k[1]), p01 = pk(f[0], f[1]), p23 = pk(f[2], f[3]);
  *LP(u32x2, t.pd + 3 * TDP + 6) = (u32x2){p32, p10};
  *LP(uint32_t, t.pd + TDP + 2) = p32; *LP(uint32_t, t.pd + TDP + 6) = p10;
  *LP(uint32_t, t.pd + 2) = p21; *LP(uint32_t, t.pd + 2 * TDP + 6) = p21;
  *LP(u16, t.pd) = (u16)p32; *LP(u16, t.pd + 2 * TDP + 4) = (u16)p32;
  asm volatile("ds_write_b16_d16_hi %0, %1 offset:6\n\tds_write_b16_d16_hi %0, %1 offset:%2" : : "v"(t.pd), "v"(p10), "n"(2 * TDP + 10) : "memory");
  if (ct) {
    const uint32_t va = (uint32_t)__builtin_amdgcn_readlane((int)p32, 63), vb = (uint32_t)__builtin_amdgcn_readlane((int)p23, 15) >> 16;
    *LP(u16, t.pe) = (u16)((lane < 12) ? va : vb);
  }
  if (lane < 32) {
    *LP(u16, t.pf + 128) = (u16)p01; *LP(u16, t.pg + 128) = (u16)(p01 >> 16);
    *LP(u16, t.pf) = (u16)p23; *LP(u16, t.pg) = (u16)(p23 >> 16);
  }
  return (uint32_t)(127 + 5 - ts) << 23;
}
__device__ __forceinline__ uint32_t hadd(uint32_t a, uint32_t b) { return __builtin_bit_cast(uint32_t, __builtin_bit_cast(f16x2, a) + __builtin_bit_cast(f16x2, b)); }
template <bool FULL> __device__ __forceinline__ void sums(char* sm, const u32x4 p, const u32x4 c) {
  const uint32_t D[8] = {p[0], p[1], p[2], p[3], c[0], c[1], c[2], c[3]};
  uint32_t O[7];
#pragma unroll
  for (int j = 0; j < 7; ++j) O[j] = __builtin_amdgcn_alignbit(D[j + 1], D[j], 16);
  u32x4 a, b;
#pragma unroll
  for (int j = 0; j < 4; ++j) a[j] = hadd(D[j + 2], hadd(O[j + 1], O[j + 2]));
  *(u32x4*)(sm + C_G3 + 32) = a;
#pragma unroll
  for (int j = 0; j < 4; ++j) b[j] = hadd(a[j], hadd(D[j + 1], D[j + 3]));
  *(u32x4*)(sm + C_G5 + 32) = b;
  if (FULL) {
#pragma unroll
    for (int j = 0; j < 4; ++j) a[j] = hadd(b[j], hadd(O[j], O[j + 3]));
    *(u32x4*)(sm + C_G7 + 32) = a;
#pragma unroll
    for (int j = 0; j < 4; ++j) b[j] = hadd(a[j], hadd(D[j], D[j + 4]));
    *(u32x4*)(sm + C_G9 + 32) = b;
  }
}
__device__ __forceinline__ void count_up(uint32_t a) {
  uint32_t t;
  asm volatile("s_mov_b64 exec, 1\n\tv_mov_b32 %0, 1\n\tds_add_u32 %1, %0\n\ts_mov_b64 exec, -1" : "=&v"(t) : "v"(a) : "memory");
}
__device__ __forceinline__ int pad_at(int r) { return 16 * (r & 1) + 3200 * (r >> 1) - ((r >= 6) ? 3200 - 2096 : 0); }
struct COps { uint32_t b, a, o, g3, k3, g4, k4, f; };
__device__ __forceinline__ void conv_ops(uint32_t tb, uint32_t ub, uint32_t cb, int lane, COps& e) {
  const uint32_t j = lane & 15, kq = lane >> 4, q4 = (lane >> 2) & 3, p4 = lane & 3;
  e.b = ub + 32u * j + 16u * kq;
  e.a = tb + p4 * TDP + 32u - 8u * q4 + 16u * kq;
  e.g3 = cb + C_G5 + 8 + 64u * kq + 8u * (q4 + p4) + (q4 & 1u) * 3200u;
  e.g4 = cb + C_G9 + 8 + 128u * kq + 16u * q4 + 8u * p4 + (q4 & 1u) * 2096u;
  e.k3 = tb + T_3 + 120 - 8u * j + 16u * kq;
  e.k4 = tb + T_4 + 60 - 4u * j + 16u * kq;
  asm volatile("" : "+v"(e.b), "+v"(e.a), "+v"(e.g3), "+v"(e.g4), "+v"(e.k3), "+v"(e.k4));
}
#define CONV_RUN(prog, e, ...) do { int prog_ = (prog); \
    asm volatile("s_call_b64 s[90:91], kf_conv" : "+{s89}"(prog_), "+{v2}"(e.b), "+{v3}"(e.a), "+{v4}"(e.o), "+{v5}"(e.g3), "+{v6}"(e.k3), "+{v7}"(e.g4), \
                 "+{v8}"(e.k4), "+{v9}"(e.f) __VA_ARGS__ : : CONV_CLOB); } while (0)

template <bool E>
__device__ __forceinline__ void ln_stats(const uint32_t* v, int lane, float sc, f32x2* t, f32x4 z, f32x2& rc, f32x2& nc, float& e) {
  float s1a = z[0], s2a = z[1], s1b = z[2], s2b = z[3];
#pragma unroll
  for (int k = 0; k < 4; ++k) {
    s1a = dot2(v[k], 0x3f803f80u, s1a); s2a = dot2(v[k], v[k], s2a);
    s1b = dot2(v[4 + k], 0x3f803f80u, s1b); s2b = dot2(v[4 + k], v[4 + k], s2b);
  }
#pragma unroll
  for (int k = 0; k < 9; ++k) t[k] = (f32x2){bflo(v[k]), bfhi(v[k])};
  const float rs = row_sum(fold16(dot2(v[8], 0x3f803f80u, fold32(s1a, s1b)), dot2(v[8], v[8], fold32(s2a, s2b))));
  if (E) { e = row_sum(e); e += __int_as_float(__builtin_amdgcn_update_dpp(0, __float_as_int(e), 0x142, 0xa, 0xf, false)); }
  const f32x2 mean = (f32x2){rdl(rs, 15), rdl(rs, 47)} * (1.f / D_), m2 = mean * mean;
  f32x2 var = (f32x2){rdl(rs, 31), rdl(rs, 63)} * (1.f / D_) - m2;
  asm volatile("" : "+v"(t[0]), "+v"(t[1]), "+v"(t[2]), "+v"(t[3]), "+v"(t[4]), "+v"(t[5]), "+v"(t[6]), "+v"(t[7]), "+v"(t[8]));
  if (__builtin_expect(!(m2[0] <= 16.f * var[0] && m2[1] <= 16.f * var[1]), 0)) {
    float q[3] = {0.f, 0.f, 0.f};
#pragma unroll
    for (int k = 0; k < 9; ++k) {
      const f32x2 a = t[k] - ((k < 4 || (k == 8 && lane < 32)) ? mean[0] : mean[1]);
      q[k >> 2] += a[0] * a[0] + a[1] * a[1];
    }
    float qa = q[0] + ((lane < 32) ? q[2] : 0.f), qb = q[1] + ((lane < 32) ? 0.f : q[2]);
    wave_sum2(qa, qb);
    var = (f32x2){qa, qb} * (1.f / D_);
  }
  const uint32_t mp = pack2(mean[0], mean[1]), vp = pack2(var[0], var[1]);
  const f32x2 ve = (f32x2){bflo(vp), bfhi(vp)} + 1e-5f;
  const uint32_t e2 = pack2(ve[0], ve[1]);
  const uint32_t sp = pack2(__builtin_amdgcn_sqrtf(bflo(e2)), __builtin_amdgcn_sqrtf(bfhi(e2)));
  rc = (f32x2){__builtin_amdgcn_rcpf(bflo(sp)), __builtin_amdgcn_rcpf(bfhi(sp))} * sc;
  nc = -(f32x2){bflo(mp), bfhi(mp)} * rc;
}
template <bool H>
__device__ __forceinline__ void ln_apply(const f32x2* t, int lane, f32x2 rc, f32x2 nc, uint32_t* c) {
  const float nm[3] = {nc[0], nc[1], (lane < 32) ? nc[0] : nc[1]}, rr[3] = {rc[0], rc[1], (lane < 32) ? rc[0] : rc[1]};
  f32x2 dd[9];
#pragma unroll
  for (int k = 0; k < 9; ++k) dd[k] = __builtin_elementwise_fma(t[k], (f32x2){rr[k >> 2], rr[k >> 2]}, (f32x2){nm[k >> 2], nm[k >> 2]});
  asm volatile("" : "+v"(dd[0]), "+v"(dd[1]), "+v"(dd[2]), "+v"(dd[3]), "+v"(dd[4]), "+v"(dd[5]), "+v"(dd[6]), "+v"(dd[7]), "+v"(dd[8]));
#pragma unroll
  for (int k = 0; k < 9; ++k) {
    const f32x2 d = dd[k];
    typedef __attribute__((ext_vector_type(2))) uint16_t u16x2;
    if (H) c[k] = __builtin_bit_cast(uint32_t, __builtin_elementwise_add_sat(__builtin_bit_cast(u16x2, __builtin_amdgcn_cvt_pkrtz(d[0], d[1])), (u16x2){4, 4}));
    else c[k] = pack2(d[0], d[1]);
  }
}
__device__ __forceinline__ uint32_t med3u(uint32_t a, uint32_t b, uint32_t c) { return max(min(a, b), min(max(a, b), c)); }
__device__ __forceinline__ uint32_t oct_maxu(uint32_t v) {
  v = max(v, (uint32_t)DPP_I((int)v, 0xb1, 0xf));
  v = max(v, (uint32_t)DPP_I((int)v, 0x4e, 0xf));
  v = max(v, (uint32_t)DPP_I((int)v, 0x141, 0xf));
  return v;
}

#define KARGS const u16* __restrict__ x, const u16* __restrict__ Are, const u16* __restrict__ Aim, const u16* __restrict__ Bre, \
    const u16* __restrict__ Bim, const u16* __restrict__ Cre, const u16* __restrict__ Cim, const u16* __restrict__ logdt, \
    const u16* __restrict__ Dskip, char* __restrict__ ws, const u16* __restrict__ wr, const u16* __restrict__ wg, \
    const u16* __restrict__ wu, const u16* __restrict__ wd, u16* __restrict__ out
#define KPASS x, Are, Aim, Bre, Bim, Cre, Cim, logdt, Dskip, ws, wr, wg, wu, wd, out
template <int ROLE, int FFN = -1>
__device__ __forceinline__ void role_body(char* smem, KARGS) {
  const int tid = threadIdx.x, lane = tid & 63;
  const int w = __builtin_amdgcn_readfirstlane(tid >> 6), wl = w & 3;
  const int b = blockIdx.x & 255, bx = b & 7, bj = b >> 3;
  constexpr bool isl = ROLE == 1 || ROLE == 2;
  const int tg = isl ? 32 * (bj >> 2) + 4 * bx + (bj & 3) : 32 * bx + bj;
  u32x4 xa, xc;
  uint32_t ep, r0;
#define P_WAIT(n, ...) asm volatile("s_waitcnt vmcnt(" #n ")\n\tv_lshlrev_b32 %0, 16, v100\n\tv_lshlrev_b32 %1, 16, v101\n\tv_lshlrev_b32 %2, 16, v102" \
    : "=v"(ldt), "=v"(are), "=v"(aim) : : "v100", "v101", "v102" __VA_ARGS__)
#define P_MORE() asm volatile("v_lshlrev_b32 %0, 16, v103\n\tv_lshlrev_b32 %1, 16, v104\n\tv_lshlrev_b32 %2, 16, v105\n\tv_lshlrev_b32 %3, 16, v106" \
    : "=v"(bre), "=v"(bim), "=v"(cre), "=v"(cim) : : "v103", "v104", "v105", "v106")
  TSD;
  TS(0, 0);
  const uint32_t lb0 = (uint32_t)(uintptr_t)(__attribute__((address_space(3))) char*)smem;
  const int i = lane & 15, kq = lane >> 4;
  const u32x4 ones = {0xffffffffu, 0xffffffffu, 0xffffffffu, 0xffffffffu}, zero4 = {0u, 0u, 0u, 0u};
  auto u_again = [&](u32x4& uw, const u32x4& ut, uint32_t uo) {
#pragma unroll 1
    for (int spin = 0; spin < SPIN_CAP; ++spin) {
      const bool na = ((uw[0] | uw[1] | uw[2] | uw[3]) & UMASK) != 0u;
      if (__builtin_expect(!__any(na), 1)) break;
      TSC(19);
      SLEEP(USLEEP);
      if (na) { LD16W(uw, uo); uw ^= ut; }
    }
  };
  constexpr uint32_t S16 = 0x41800000u, LMASK = 0x00070007u;
  auto v_put = [&](u32x4 v, const u32x4& ut, bool other, uint32_t vo) {
    const bool second = __any(other || ((v[0] | v[1] | v[2] | v[3]) & 0x40004000u) != 0u);
    if (__builtin_expect(second, 0)) v = (v & ~LMASK) | (~ut & LMASK); else v = (v & ~LMASK) | ut;
    ST16(vo, v);
    return second;
  };
  auto v_end = [&](uint32_t T, int n) { DRAIN(); if (lane == 0) ST4(O_DN + 16u * n, T); };
  if constexpr (!isl) {
    const int ch = wl >> 1, tl = tid & 255;
    int* evp = (int*)(smem + O_FL + 16 * ch);
    float ldt, are, aim, dsk;
    if constexpr (ROLE == 0) {
      __builtin_amdgcn_s_setprio(KPRIO);
      COps e;
      conv_ops(lb0 + (uint32_t)(ch * TB), lb0 + (uint32_t)(O_UL + ch * UB), lb0 + (uint32_t)(O_IM + ch * GB), lane, e);
      e.o = lb0 + O_ST + (uint32_t)(16 * ((4 * i + 2 * (kq >> 1) + ch) ^ (i >> 1)) + 8 * (kq & 1));
      e.f = lb0 + O_FL;
      TOps t;
      table_ops(lb0 + (uint32_t)(O_IM + 128 * IP * ch), lb0 + (uint32_t)(TB * ch), wl & 1, lane, t);
      uint32_t pz = lb0 + O_IM + (uint32_t)(((lane >> 3) & 1) * GB + pad_at(lane & 7)), im = lb0 + (uint32_t)(O_IM + IP * tl);
      asm volatile("" : "+v"(e.o), "+v"(e.f), "+v"(pz), "+v"(im));
      float bre, bim, cre, cim;
      P_WAIT(0, , "v103", "v104", "v105", "v106", "v107", "v109");
      asm volatile("" : "={v109}"(ep));
      const uint32_t T = ((uint32_t)__builtin_amdgcn_readfirstlane(ep) + 1u) * 0x9E3779B1u;
      if (w == 3 && lane < 8) ST16(O_KEY + 128u * b + 16u * lane, ones);
      P_MORE();
      asm volatile("v_lshlrev_b32 %0, 16, v107" : "=v"(dsk) : : "v107");
      TS(0, 1);
      evp[wl & 1] = gen_y(ldt, are, aim, bre, bim, cre, cim, im, [] {} TSA);
      TS(0, 8);
      BAR();
      TS(0, 2);
      const uint32_t cs = table_tile(t, wl & 1, lane, lb0 + O_FL + 16 * ch, dsk TSA);
      *(uint32_t*)(smem + O_FL + 40 + 4 * ch) = cs;
      DRAIN();
      if ((wl & 1) && lane == 0) asm volatile("global_store_dwordx2 %0, %1, %2 sc1" : : "v"(O_SC + 16u * (2 * tg + ch)), "v"((u32x2){T, cs}), "s"(ws) : "memory");
      TS(0, 9);
      gsync0(lb0 + O_FL + 36, 4);
      TS(0, 3);
      *(u32x4*)(smem + O_ST + 16 * tl) = ones;
      *(u32x4*)(smem + O_ST + 4096 + 16 * tl) = ones;
      if (w == 0) *LP(u32x4, pz) = zero4;
      CONV_RUN(wl & 1, e);
      TS(0, 5);
    } else {
      __builtin_amdgcn_s_setprio(UPRIO);
#pragma unroll
      for (int k = 0; k < 3; ++k) *(u32x4*)(smem + ((16 * (tl + 256 * k) < O_FL) ? 16 * (tl + 256 * k) : O_FL + 32)) = zero4;
      if (w == 7) *(u32x4*)(smem + O_UL + (lane >> 5) * UB + 16 * min(lane & 31, 29)) = zero4;
      uint32_t uo = u_at(tl >> 1, 2 * tg + (tid & 1)), go = O_SU + (uint32_t)(256 * bx + 32 * wl + (lane & 31) + 4 * (lane & 32)) * 128u;
      uint32_t ul = (uint32_t)(O_UL + (tid & 1) * UB + C_U - 16 + 16 * (tl >> 1)), ug = (uint32_t)(O_IM + (tid & 1) * GB + 16 * (tl >> 1)), so = lb0 + O_ST + 16u * (uint32_t)(lane ^ (lane >> 3));
      uint32_t vo = O_V + (uint32_t)tg * VP + 16u * lane;
      asm volatile("" : "+v"(uo), "+v"(go), "+v"(ul), "+v"(ug), "+v"(so), "+v"(vo));
      P_WAIT(1, , "v109");
      TS(0, 1);
      evp[2 + (wl & 1)] = gen_x(ldt, are, aim, smem + O_IM + IP * tl);
      asm volatile("s_waitcnt vmcnt(0)" : "={v109}"(ep));
      const uint32_t T = ((uint32_t)__builtin_amdgcn_readfirstlane(ep) + 1u) * 0x9E3779B1u;
      TS(0, 8);
      BAR();
      TS(0, 2);
      u32x4 ut = {utag(T, 0), utag(T, 1), utag(T, 2), utag(T, 3)};
      asm volatile("" : "+v"(ut));
      SLEEP(GSLEEP);
      u32x2 pv;
      pv[1] = gate(ws, go, T);
      TS(0, 3);
      until(latest(pv[1]) + UD);
      TS(0, 7);
      u32x4 uw, ux;
      asm volatile("global_load_dwordx4 %0, %2, %4 sc1\n\tglobal_load_dwordx4 %1, %3, %4 sc1"
                   : "=&v"(uw), "=&v"(ux) : "v"(uo), "v"(uo + 64 * UP * 32), "s"(ws) : "memory");
      __builtin_amdgcn_s_setprio(FPR);
      asm volatile("s_waitcnt vmcnt(1)" : "+v"(uw));
      uw ^= ut;
      *(u32x4*)(smem + ul + 16) = uw;
      asm volatile("s_waitcnt vmcnt(0)" : "+v"(ux));
      TS(0, 11);
      ux ^= ut;
      *(u32x4*)(smem + ul + 2048 + 16) = ux;
      if (__builtin_expect(__any(((uw[0] | uw[1] | uw[2] | uw[3] | ux[0] | ux[1] | ux[2] | ux[3]) & UMASK) != 0u), 0)) {
        u_again(uw, ut, uo);
        u_again(ux, ut, uo + 64 * UP * 32);
        *(u32x4*)(smem + ul + 16) = uw;
        *(u32x4*)(smem + ul + 2048 + 16) = ux;
      }
      BAR();
      TS(0, 4);
      const u32x4 pw = *(const u32x4*)(smem + ul);
      if (wl < 2) {
        const u32x4 px = *(const u32x4*)(smem + ul + 2048);
        sums<true>(smem + ug, pw, uw);
        sums<false>(smem + ug + 2048, px, ux);
      } else sums<true>(smem + ug, pw, uw);
      count_up(lb0 + O_FL + 32);
      __builtin_amdgcn_s_setprio(SPR);
      TS(0, 5);
      const bool other = *(const uint32_t*)(smem + O_FL + 40 + 4 * (lane & 1)) != S16;
      bool second = false;
#pragma unroll
      for (int k = 0; k < 2; ++k) {
        const uint32_t o = ((k ? 0x3247u : 0x1065u) >> (4 * wl) & 7u) * 1024u;
        u32x4 v;
#pragma unroll 1
        for (int spin = 0; spin < SPIN_CAP; ++spin) {
          asm volatile("ds_read_b128 %0, %1\n\ts_waitcnt lgkmcnt(0)" : "=&v"(v) : "v"(so + o) : "memory");
          if (!__any(max4(v) == 0xffffffffu)) break;
          __builtin_amdgcn_s_sleep(VPS);
        }
        TS(0, 12 + k);
        second |= v_put(v, ut, other, vo + o);
        TS(0, 10 - 4 * k);
      }
      if (__builtin_expect(second, 0)) v_end(T, 4 * tg + wl);
    }
    TSE();
  } else {
  uint32_t* cand = (uint32_t*)(smem + LM);
  uint32_t* sel = cand + 128;
  uint32_t* kl = sel + 48;
  uint32_t* cnt = sel + 64;
  const uint32_t ca = lb0 + LM + 4 * (128 + 64);
  u16* hm = (u16*)(cnt + 12);
  uint32_t T;
  const bool ffn = (FFN < 0) ? bj < 18 : FFN, isx = FFN != 1 && bj >= 24;
  const int fb = 18 * bx + bj;
  constexpr bool ln2 = ROLE == 2;
  const int g0 = tg, g2 = tg;
  u32x4 bw[18];
#pragma unroll
  for (int s = 0; s < 9; ++s) bw[s] = (u32x4){0u, 0u, 0u, 0u};
  int tid2 = threadIdx.x;
  uint32_t ts1 = 0u;
  auto rest = [&] {
    if (ts1 && (int)((uint32_t)__builtin_amdgcn_s_memrealtime() - ts1) > 0) { TS(0, 8); until(ts1 + TSTOP + RD); if (RW) gwait(ca, 4); ts1 = 0u; }
  };
  auto gu_dma = [&](int q0, int q1, auto depth) {
    const char* gs = (const char*)((w < 2) ? wg : wu) + (size_t)((144 * (w & 1)) * (F_ * 2) + fb * 64);
    const uint32_t gv = (uint32_t)((lane >> 2) * (F_ * 2) + 16 * ((lane & 3) ^ (lane >> 5) ^ (w & 2)));
#pragma unroll 1
    for (int q = q0; q < q1; ++q) {
      const int k = (int)((((bj & 1) ? ORD1 : ((unsigned __int128)0x876543210u << 36 | 0x876543210ull)) >> (4 * (q - (q >= 9 ? 9 : 0)) + (q >= 9 ? 36 : 0))) & 15u), h = q >= 9;
      if (q >= RQ) rest();
      dma16(gs + (size_t)(288 * h + 16 * k) * (F_ * 2), gv, lb0 + LW * h + 9216 * w + 1024 * k);
      asm volatile("s_waitcnt vmcnt(%0)" : : "n"(depth.value) : "memory");
    }
  };
  auto down_dma = [&](int k0, int k1, int lane) {
#pragma unroll 1
    for (int k = k0; k < k1; ++k) {
      const int o = 9216 * wl + 2304 * k;
      const char* ds = (const char*)wd + (size_t)(fb * 36864 + o);
      const uint32_t dl = lb0 + o + 32 * ((4 * wl + k + 1) >> 1);
      if (ROLE == 1) rest();
      dma16d(ds, 16u * lane, dl);
      dma16d(ds + 1024, 16u * lane, dl + 1024);
      uint32_t keep;
      asm volatile("s_mov_b32 %0, m0\n\ts_mov_b32 m0, %3\n\ts_mov_b64 exec, 0xffff\n\tglobal_load_lds_dwordx4 %1, %2" DPOL2 "\n\ts_mov_b64 exec, -1\n\ts_mov_b32 m0, %0\n\ts_waitcnt vmcnt(%4)"
                   : "=&s"(keep) : "v"(16u * lane), "s"(ds + 2048), "s"(dl + 2048), "n"(ROLE == 1 ? DDEPTH : 40) : "memory");
    }
  };
  auto gu_read = [&](int h) {
    const uint32_t ra = lb0 + LW * h + (uint32_t)(((lane >> 1) & 1) * 18432 + (8 * kq + ((lane >> 2) & 3)) * 64 + ((w ^ (kq & 1) ^ (lane & 2)) * 16) + 8 * (lane & 1));
#pragma unroll
    for (int s = 0; s < 9; ++s) {
      const u32x2 h0 = trd(ra + 2048 * s), h1 = trd(ra + 2048 * s + 256);
      bw[9 * h + s] = (u32x4){h0[0], h0[1], h1[0], h1[1]};
    }
#pragma unroll
    for (int s = 0; s < 9; ++s) asm volatile("" : "+v"(bw[9 * h + s]));
  };
  uint32_t tv = 0u, tk = 0u;
  const uint32_t vearly = (uint32_t)((VEARLY >> (8 * (g0 >> 5))) & 0xffull);
#define TOUCH(pa) asm volatile("global_load_dword v126, %0, off" DPOL4 : : "v"(pa) : "memory", "v126")
  uint32_t wv[5], xv[9];
  auto rows_xw = [&](int lane) {
    u32x4 wa, xr, xs;
    const uint32_t xo = (uint32_t)((8 * g2 + 2 * wl) * (D_ * 2));
    asm volatile("global_load_dwordx4 %0, %5, %9\n\tglobal_load_dword %1, %6, %9 offset:1024\n\t"
                 "global_load_dwordx4 %2, %7, %10\n\tglobal_load_dwordx4 %3, %7, %10 offset:1152\n\tglobal_load_dword %4, %8, %10 offset:1024\n\ts_waitcnt vmcnt(0)"
                 : "=&v"(wa), "=&v"(wv[4]), "=&v"(xr), "=&v"(xs), "=&v"(xv[8])
                 : "v"(16u * lane), "v"(4u * (lane & 31)), "v"(xo + 16u * lane), "v"(xo + 4u * lane + 32u * (lane & 32)), "s"(wr), "s"(x) : "memory");
#pragma unroll
    for (int k = 0; k < 4; ++k) { wv[k] = wa[k]; xv[k] = xr[k]; xv[4 + k] = xs[k]; }
  };
  auto ln1 = [&](int g, int o, uint32_t c0) {
    uint32_t uo = u_at(g, 64 * wl + lane);
    uint32_t ta = lb0 + (uint32_t)(o + ((lane >> 2) & 3) * RP + (64 * wl + 16 * kq + 4 * (lane & 3)) * 2);
    uint32_t la = (uint32_t)(o + 2 * wl * RP + 16 * lane), lt = (uint32_t)(o + (2 * wl + (lane >> 5)) * RP + 1024 + 4 * (lane & 31));
    uint32_t sg = O_SU + (uint32_t)(256 * (lane >> 3) + g) * 128u + 16u * (lane & 7);
    asm volatile("" : "+v"(uo), "+v"(ta), "+v"(la), "+v"(lt), "+v"(sg));
    f32x4 z4 = {0.f, 0.f, 0.f, 0.f};
    asm volatile("" : "+v"(z4));
    asm volatile("s_waitcnt vmcnt(0)" : "={v[100:103]}"(xa), "={v[104:107]}"(xc), "={v108}"(r0), "={v109}"(ep));
    TS(0, 1);
    const uint32_t v[9] = {xa[0], xa[1], xa[2], xa[3], xc[0], xc[1], xc[2], xc[3], r0};
    uint32_t c[9];
    f32x2 tf[9];
    T = ((uint32_t)__builtin_amdgcn_readfirstlane(ep) + 1u) * 0x9E3779B1u;
    if (wl == 3) { const uint32_t now = (uint32_t)__builtin_amdgcn_s_memrealtime() + SD; ST16(sg, ((u32x4){T, now, T, now})); }
    const uint32_t tj = ((T >> (3 * wl)) & 0x00070007u) | (((T >> (12 + wl)) & 0x00010001u) << 14);
    uint32_t tw[3] = {(tj & 0xffffu) * 0x10001u, (tj >> 16) * 0x10001u, 0u};
    tw[2] = (lane < 32) ? tw[0] : tw[1];
    asm volatile("" : "+v"(tw[0]), "+v"(tw[1]), "+v"(tw[2]));
    f32x2 rc, nc;
    float e_;
    ln_stats<false>(v, lane, 0.03125f, tf, z4, rc, nc, e_);
    ln_apply<true>(tf, lane, rc, nc, c);
#pragma unroll
    for (int k = 0; k < 9; ++k) c[k] = (c[k] & ~UMASK) | tw[k >> 2];
    *(u32x4*)(smem + la) = (u32x4){c[0], c[1], c[2], c[3]};
    *(u32x4*)(smem + la + RP) = (u32x4){c[4], c[5], c[6], c[7]};
    *(uint32_t*)(smem + lt) = c[8];
    u32x4 ha, hb;
    {
      uint32_t t, cc, n = SPIN_CAP;
      asm volatile("s_mov_b64 exec, 1\n\tv_mov_b32 %2, 1\n\tds_add_u32 %5, %2\n\ts_mov_b64 exec, -1\n"
                   "1:\n\tds_read_b32 %2, %5\n\tds_read_b64_tr_b16 v[40:41], %6\n\tds_read_b64_tr_b16 v[42:43], %6 offset:%7\n\tds_read_b64_tr_b16 v[44:45], %6 offset:512\n\t"
                   "ds_read_b64_tr_b16 v[46:47], %6 offset:%8\n\ts_sub_u32 %4, %4, 1\n\ts_cbranch_scc1 2f\n\ts_waitcnt lgkmcnt(4)\n\tv_readfirstlane_b32 %3, %2\n\t"
                   "s_cmp_lt_u32 %3, 4\n\ts_cbranch_scc1 1b\n2:\n\ts_waitcnt lgkmcnt(0)"
                   : "=&{v[40:43]}"(ha), "=&{v[44:47]}"(hb), "=&v"(t), "=&s"(cc), "+s"(n) : "v"(c0), "v"(ta), "n"(4 * RP), "n"(512 + 4 * RP) : "memory", "scc");
    }
    ST16(uo, ha);
    ST16(uo + 64 * 128, hb);
    if (wl == 0) {
      const u32x2 h4 = trd(ta + 1024), h5 = trd(ta + 1024 + 4 * RP);
      ST16(uo + 128 * 128, ((u32x4){h4[0], h4[1], h5[0], h5[1]}));
    }
  };
  const int dX = 512 - 24 + 8 * bx + bj;
  if constexpr (ROLE == 2) {
    const int tl = tid - 256;
    if (isx) {
      __builtin_amdgcn_s_setprio(QPRIO0);
      *(u32x4*)(smem + X_TB + 16 * tl) = zero4;
      *(u32x4*)(smem + X_TB + 16 * min(256 + tl, (TB + C_U) / 16 - 1)) = zero4;
      const int pr = (wl + XROT) & 3;
      COps e;
      conv_ops(lb0 + X_TB, lb0 + X_UL, lb0 + X_IM, lane, e);
      TOps t;
      table_ops(lb0 + X_IM, lb0 + X_TB, wl & 1, lane, t);
      e.o = lb0 + X_ST + (uint32_t)(16 * ((2 * i + (kq >> 1)) ^ ((i >> 2) & 1)) + 8 * (kq & 1));
      e.f = ca + 28 - 32;
      asm volatile("" : "+v"(e.o), "+v"(e.f));
      float ldt, are, aim, bre, bim, cre, cim, dsk;
      P_WAIT(0, , "v103", "v104", "v105", "v106", "v107", "v109");
      asm volatile("" : "={v109}"(ep));
      T = ((uint32_t)__builtin_amdgcn_readfirstlane(ep) + 1u) * 0x9E3779B1u;
      P_MORE();
      asm volatile("v_lshlrev_b32 %0, 16, v107" : "=v"(dsk) : : "v107");
      TS(0, 1);
      __builtin_amdgcn_s_setprio(QPRIO);
      int* evp = (int*)(sel + 44);
      char* im = smem + X_IM + IP * (tl & 127);
      evp[wl] = (wl < 2) ? gen_y(ldt, are, aim, bre, bim, cre, cim, lb0 + X_IM + IP * tl, [] {} TSA) : gen_x(ldt, are, aim, im);
      TS(0, 2);
      __builtin_amdgcn_s_setprio(QPRIO2);
      gsync(ca + 8, 4);
      if (wl < 2) evp[-4 + wl] = (int)table_tile(t, wl & 1, lane, lb0 + LM + 4 * (128 + 44), dsk TSA);
      if (wl < 2) gsync0(ca + 8, 8); else { count_up(ca + 8); SLEEP(XSL); gwait(ca + 8, 8); }
      const uint32_t cs = (uint32_t)__builtin_amdgcn_readfirstlane(evp[-4]);
      if (wl == 2 && lane == 0) asm volatile("global_store_dwordx2 %0, %1, %2 sc1" : : "v"(O_SC + 16u * dX), "v"((u32x2){T, cs}), "s"(ws) : "memory");
      if (wl == 3) *(u32x4*)(smem + X_IM + pad_at(lane & 7)) = zero4;
      TS(0, 3);
      CONV_RUN(2 + pr, e, , "+{v11}"(tid2));
      {
        const int lane = tid2 & 63;
        const u32x4 v = *(const u32x4*)(smem + X_ST + 2048 * pr + 1024 * (lane >> 5) + 16 * ((lane & 31) ^ ((lane >> 3) & 1)));
        if (__builtin_expect(v_put(v, (u32x4){utag(T, 0), utag(T, 1), utag(T, 2), utag(T, 3)}, cs != S16, v_at(dX, 64 * pr + lane)), 0)) {
          DRAIN();
          if (lane == 0) ST4(O_DN + 16u * (4 * (dX - 256) + pr), T);
        }
      }
      TS(0, 6);
    }
    __builtin_amdgcn_s_setprio(PPRIO);
    asm volatile("" : "+v"(tid2));
    if (isx) {
      const int lane = tid2 & 63;
      const u32x4 ra = *(const u32x4*)(smem + X_RW(wl) + 16 * lane), rb = *(const u32x4*)(smem + X_RW(wl) + 1024 + 16 * lane), rw = *(const u32x4*)(smem + X_WR + 16 * lane);
      const u32x2 rc = *(const u32x2*)(smem + X_RW(wl) + 2048 + 8 * lane);
#pragma unroll
      for (int k = 0; k < 4; ++k) { xv[k] = ra[k]; xv[4 + k] = rb[k]; wv[k] = rw[k]; }
      xv[8] = rc[0]; wv[4] = rc[1];
    } else rows_xw(tid2 & 63);
    if (!isx && !ffn) {
      SLEEP(RSLEEP);
#pragma unroll
      for (int k = 0; k < 7; ++k) ST16(O_SL + (uint32_t)((6 * bx + bj - 18) * 1792 + 256 * k - 256 + tid2) * 16u, ones);
      DRAIN();
    }
    if (!isx) BAR();
    const uint32_t tp = (uint32_t)__builtin_amdgcn_readfirstlane(cnt[4]);
    if (!isx) T = (uint32_t)__builtin_amdgcn_readfirstlane(sel[43]);
    tv = tp + UD + VD2 - vearly;
  } else {
    __builtin_amdgcn_s_setprio(3);
    if (tid < 12) cnt[tid] = 0u;
    BAR();
    if (tid < 20) ((uint32_t*)(hm + 320))[tid] = 0u;
    if (tid >= 184) *(u32x4*)(smem + L_SC - 16 * 184 + 16 * tid) = (u32x4){0x41804180u, 0x41804180u, 0x41804180u, 0x41804180u};
    uint32_t sink = 0;
    ln1(tg, 0, ca + 12);
    TS(0, 2);
    __builtin_amdgcn_s_setprio(NPRIO);
    asm volatile("s_waitcnt vmcnt(0)" : "+v"(sink) : : "memory");
    TS(0, 13);
    if (w == 0) {
      const uint32_t xr = O_XC + (uint32_t)((511 - 2 * (g0 + w)) * 144 + lane) * 16u;
      ST16(xr, ones);
      ST16(xr + 1024u, ones);
      if (lane < 16) ST16(xr + 2048u, ones);
    }
    if (w == 2 && b < 72 && lane < 8) ST16(O_FB + (uint32_t)(8 * b + lane) * 16u, ones);
    if (isx) {
      rows_xw(lane);
      char* sp = smem + X_RW(w) + 16 * lane;
      *(u32x4*)sp = (u32x4){xv[0], xv[1], xv[2], xv[3]};
      *(u32x4*)(sp + 1024) = (u32x4){xv[4], xv[5], xv[6], xv[7]};
      *(u32x2*)(smem + X_RW(w) + 2048 + 8 * lane) = (u32x2){xv[8], wv[4]};
      if (w == 0) *(u32x4*)(smem + X_WR + 16 * lane) = (u32x4){wv[0], wv[1], wv[2], wv[3]};
    }
    SLEEP(ESLEEP);
    uint32_t tu = latest(gate(ws, O_SU + (uint32_t)(256 * bx + 64 * w + 4 * i + (bj & 3)) * 128u, T));
    asm volatile("s_mov_b64 exec, 1\n\tds_max_u32 %0, %1\n\ts_mov_b64 exec, -1" : : "v"(ca + 16), "v"(tu) : "memory");
    if (w == 0) sel[43] = T;
    if (isx) {
      const uint32_t xo = u_at(tid, dX);
      char* sm = smem + X_UL + C_U - 16 + 16 * tid;
      const u32x4 ut = {utag(T, 0), utag(T, 1), utag(T, 2), utag(T, 3)};
      __builtin_amdgcn_s_setprio(3);
      until(tu + UD + XD);
      u32x4 uw;
      asm volatile("global_load_dwordx4 %0, %1, %2 sc1" : "=&v"(uw) : "v"(xo), "s"(ws) : "memory");
      asm volatile("s_waitcnt vmcnt(0)" : "+v"(uw));
      uw ^= ut;
      u_again(uw, ut, xo);
      *(u32x4*)(sm + 16) = uw;
      BAR();
      if (w < 3) {
        const u32x4 pw = *(const u32x4*)sm;
        if (w < 2) sums<true>(smem + X_IM + 16 * tid, pw, uw); else sums<false>(smem + X_IM + 16 * tid, pw, uw);
        count_up(ca + 28);
      }
    } else {
      until(tu + UD + WD);
      BAR();
    }
    tu = (uint32_t)__builtin_amdgcn_readfirstlane(cnt[4]);
    TS(0, 3);
    __builtin_amdgcn_s_setprio(DPRIO);
    if (ffn) {
      ts1 = (tu + UD + VD2 - TSTOP) | 1u;
      gu_dma(0, N0, IC<40>{});
      gu_dma(N0, NA, IC<WDEPTH>{});
      asm volatile("s_waitcnt vmcnt(%0)" : : "n"(NA - 9) : "memory");
      gsync0(ca + 4, 4);
      gu_read(0);
      asm volatile("s_waitcnt lgkmcnt(0)" ::: "memory");
      count_up(ca + 36);
      TS(0, 4);
      gu_dma(NA, 18, IC<WDEPTH2>{});
      gwait(ca + 36, 4);
      down_dma(0, KN, lane);
      asm volatile("s_waitcnt vmcnt(%0)" : : "n"(3 * KN) : "memory");
      count_up(ca + 4);
      TS(0, 7);
    } else {
      if (!isx && C3) {
        const int n = 6 * min(tid, 191) + bj - 18, r = n >> 2;
        const char* wb = (const char*)((n & 1) ? wu : wg) + bx * 1152;
        const int c0 = (3 - (int)((uintptr_t)wb >> 7)) & 7;
        wb += (uint32_t)((r + 144 * ((r >= 144) + ((n >> 1) & 1))) * (F_ * 2) + 128 * c0);
        TOUCH(wb);
        if (c0 == 0) TOUCH(wb + 1024);
      }
      auto d_line = [&](int k) {
        const int n = min((k * 14 + bj - 18) * 64 + lane, 5183), q = n / 1296, r = n - 1296 * q;
        return (const char*)wd + (uint32_t)(bx * 5184 + (r / 18) * 72 + 18 * (HREV ? 3 - q : q) + r % 18) * 128u;
      };
      int k = w;
      if (w < HW) {
#pragma unroll 1
        for (; k < 6 && (int)((uint32_t)__builtin_amdgcn_s_memrealtime() - (tu + UD + VD2 - HSTOP)) < 0; k += HW) { TOUCH(d_line(k)); DRAIN(); }
      }
      TS(0, 4);
      until(tu + UD + VD2 + RD2);
      if (RW) gwait(ca, 4);
      if (w < HW) {
#pragma unroll 1
        for (; k < 6; k += HW) { TOUCH(d_line(k)); DRAIN(); }
      }
      TS(0, 7);
    }
    tv = tu + UD + VD2 - vearly;
    tk = tu + UD + VD2 + KD;
  }
  {
  const int tid = tid2, lane = tid & 63, i = lane & 15, kq = lane >> 4;
  __builtin_amdgcn_s_setprio(3);
  const int PL = ffn ? 6400 : LW + 6656;
  f32x2 xf[9];
  f32x4 za = {0.f, 0.f, 0.f, 0.f}, zb = za;
  uint32_t al = O_XC + 64u * (i >> 3) + 16u * kq + 128u * w;
  uint32_t ha = (ROLE == 1) ? (uint32_t)((4 * (kq & 1) + 2 * (kq >> 1) + (i >> 3)) * 80 + 16 * w + 2 * (i & 7)) : 0u;
  uint32_t h0a = (uint32_t)(min(i, 8) * 80 + 16 * kq), h1a = (uint32_t)(min(i ^ 8, 8) * 80 + 16 * kq);
  uint32_t so = O_SL + (uint32_t)fb * SP + ((ROLE == 2) ? 16u * tid - 4096u : 8192u + 16u * lane);
  uint32_t da = lb0 + (uint32_t)((8 * kq + ((lane >> 2) & 3)) * 1152 + 32 * ((8 * kq + ((lane >> 2) & 3) + 2) >> 2) + 8 * (lane & 3) + ((ROLE == 2) ? 128 * wl : 1024));
  if (ROLE == 2) asm volatile("" : "+v"(h0a), "+v"(h1a), "+v"(so), "+v"(da)); else asm volatile("" : "+v"(ha), "+v"(al), "+v"(da));
  uint32_t hv[9] = {}, nv[9] = {}, keya = 0u, keyb = 0u;
  int rka = 0, rkb = 0;
  auto put_row = [&](const uint32_t* r, int j, const void* base, uint32_t ro) {
    const u32x4 tl = {r[8], (uint32_t)DPP_I((int)r[8], 0x55, 0xf), (uint32_t)DPP_I((int)r[8], 0xaa, 0xf), (uint32_t)DPP_I((int)r[8], 0xff, 0xf)};
    asm volatile("global_store_dwordx4 %0, %1, %2 sc1\n\ts_nop 1" : : "v"(ro + 16u * lane), "v"((u32x4){r[4 * j], r[4 * j + 1], r[4 * j + 2], r[4 * j + 3]}), "s"(base) : "memory");
    if ((lane & 35) == 32 * j) asm volatile("global_store_dwordx4 %0, %1, %2 offset:1024 sc1\n\ts_nop 1" : : "v"(ro + 4u * (lane & 31)), "v"(tl), "s"(base) : "memory");
  };
  if (ln2) {
    const int po = PL - 2560 * (ROLE == 1);
    uint32_t pa = (uint32_t)(po + 9216 * wl + 32 * lane), pb = (uint32_t)(po + 9216 * wl + 2048 + 8 * (lane & 31)), ps = (lane < 32) ? 0u : 16u;
    uint32_t kid = (uint32_t)(((255 - g2) << 5) | (7 - 2 * wl - (lane >> 5)));
    const int tl = tid - 256, t2 = tl + 128, d1 = 64 * (tl / 48) + tl % 48, d3 = 64 * (t2 / 48) + t2 % 48;
    const int d2 = (tl < 128) ? 64 * (tl >> 4) + 48 + (tl & 15) : min(384 + tl, 575);
    uint32_t vo = v_at(d1, g0), vo2 = v_at(d2, g0), vo3 = v_at(d3, g0), px = (uint32_t)(PL + 4 * d1), py = (uint32_t)(PL + 4 * d2), pz = (uint32_t)(PL + 4 * d3);
#pragma unroll
    for (int k = 0; k < 9; ++k) xf[k] = (f32x2){bflo(xv[k]), bfhi(xv[k])};
    asm volatile("" : "+v"(xf[0]), "+v"(xf[1]), "+v"(xf[2]), "+v"(xf[3]), "+v"(xf[4]), "+v"(xf[5]), "+v"(xf[6]), "+v"(xf[7]), "+v"(xf[8]));
    asm volatile("" : "+v"(za), "+v"(zb), "+v"(pa), "+v"(pb), "+v"(ps), "+v"(kid), "+v"(vo), "+v"(vo2), "+v"(vo3), "+v"(px), "+v"(py), "+v"(pz));
    u32x4 ut = {utag(T, 0), utag(T, 1), utag(T, 2), utag(T, 3)};
    float sv[10], ws_ = dot2(wv[4], 0x3f803f80u, 0.f) * 0.5f;
#pragma unroll
    for (int k = 0; k < 4; ++k) ws_ = dot2(wv[k], 0x3f803f80u, ws_);
    ws_ = wave_sum(ws_);
#pragma unroll
    for (int k = 0; k < 10; ++k) sv[k] = 16.f;
    uint32_t ka_ = lb0 + LM + 4u * (128 + 48 + 2 * wl + (lane >> 5)), kb_ = lb0 + LM + 4u * (128 + 48);
    uint32_t ko_ = O_KEY + (uint32_t)(256 * (lane & 7) + 8 * (g2 & 31) + (g2 >> 5)) * 16u;
    asm volatile("" : "+v"(ut), "+v"(ws_), "+v"(sv[0]), "+v"(sv[1]), "+v"(sv[2]), "+v"(sv[3]), "+v"(sv[4]), "+v"(sv[5]), "+v"(sv[6]), "+v"(sv[7]), "+v"(sv[8]), "+v"(sv[9]),
                 "+v"(ka_), "+v"(kb_), "+v"(ko_));
    if (ffn) gwait(ca + 36, 4);
    TS(0, 7);
    until(tv);
    TS(0, 8);
    u32x4 va, vc = ut, vb = ut;
    asm volatile("global_load_dwordx4 %0, %2, %4 sc1\n\ts_cmp_lt_u32 %5, 2\n\ts_cbranch_scc1 1f\n\tglobal_load_dwordx4 %1, %3, %4 sc1\n1:"
                 : "=&v"(va), "+v"(vc) : "v"(vo), "v"(vo3), "s"(ws), "s"(wl) : "memory", "scc");
    until(tv + V2D);
    asm volatile("s_cmp_eq_u32 %5, 3\n\ts_cbranch_scc1 1f\n\tglobal_load_dwordx4 %2, %3, %4 sc1\n\ts_waitcnt vmcnt(1)\n\ts_branch 2f\n1:\n\ts_waitcnt vmcnt(0)\n2:"
                 : "+v"(va), "+v"(vc), "+v"(vb) : "v"(vo2), "s"(ws), "s"(wl) : "memory", "scc");
    auto turn1 = [&] {
#pragma unroll
      for (int e = 0; e < 4; ++e) *(uint32_t*)(smem + px + 9216 * e) = va[e];
      if (wl >= 2) {
#pragma unroll
        for (int e = 0; e < 4; ++e) *(uint32_t*)(smem + pz + 9216 * e) = vc[e];
      }
    };
    auto turn = [&] {
      if (wl < 3) {
#pragma unroll
        for (int e = 0; e < 4; ++e) *(uint32_t*)(smem + py + 9216 * e) = vb[e];
      }
    };
    va ^= ut; vc ^= ut;
    turn1();
    asm volatile("s_waitcnt vmcnt(0)" : "+v"(vb) : : "memory");
    vb ^= ut;
    turn();
    const u32x4 vs = va | vb | vc;
    if (__builtin_expect(__any(((vs[0] | vs[1] | vs[2] | vs[3]) & UMASK) != 0u), 0)) {
#pragma unroll 1
      for (int spin = 0; spin < 4; ++spin) {
        const bool na = ((va[0] | va[1] | va[2] | va[3]) & UMASK) != 0u, nb = ((vb[0] | vb[1] | vb[2] | vb[3]) & UMASK) != 0u, nc = ((vc[0] | vc[1] | vc[2] | vc[3]) & UMASK) != 0u;
        if (!__any(na || nb || nc)) break;
        TSC(16);
        if (na) LD16(va, vo);
        if (nb) LD16(vb, vo2);
        if (nc) LD16(vc, vo3);
        asm volatile("s_waitcnt vmcnt(0)" : "+v"(va), "+v"(vb), "+v"(vc) : : "memory");
        if (na) va ^= ut;
        if (nb) vb ^= ut;
        if (nc) vc ^= ut;
      }
      auto again = [&](u32x4& v, uint32_t o, int d) {
        const int J = g0 >> 5;
        const uint32_t dn = O_DN + 16u * (uint32_t)((d < 512) ? 4 * (d >> 1) + ((0x01013232 >> (4 * J)) & 3) : 4 * (d - 256) + (J >> 1));
        bool pend = ((v[0] | v[1] | v[2] | v[3]) & UMASK) != 0u;
#pragma unroll 1
        for (int spin = 0; spin < SPIN_CAP && __any(pend); ++spin) {
          TSC(16);
          SLEEP(PSLEEP);
          if (pend) {
            uint32_t fin = ~T;
            if ((v[0] & v[1] & v[2] & v[3] & LMASK) == LMASK)
              asm volatile("global_load_dword %0, %1, %2 sc1\n\ts_waitcnt vmcnt(0)" : "=&v"(fin) : "v"(dn), "s"(ws) : "memory");
            LD16W(v, o);
            v ^= ut;
            pend = ((v[0] | v[1] | v[2] | v[3]) & UMASK) != 0u;
            if (pend && fin == T) {
              u32x2 sc;
              for (int k = 0; k < SPIN_CAP; ++k) {
                asm volatile("global_load_dwordx2 %0, %1, %2 sc1\n\ts_waitcnt vmcnt(0)" : "=&v"(sc) : "v"(O_SC + 16u * d), "s"(ws) : "memory");
                if (sc[0] == T) break;
              }
              *(u16*)(smem + L_SC + 2 * d) = (u16)(sc[1] >> 16);
              cnt[10] = 1u;
              v = (v ^ ut) & ~LMASK;
              pend = false;
            }
          }
        }
      };
      again(va, vo, d1); again(vc, vo3, d3); again(vb, vo2, d2);
      turn1();
      turn();
    }
    u32x4 a0, a1;
    u32x2 a2, cf;
    {
      uint32_t cc, n = SPIN_CAP;
      asm volatile("s_mov_b64 exec, 1\n\tv_mov_b32 v90, 1\n\tds_add_u32 %6, v90\n\ts_mov_b64 exec, -1\n"
                   "1:\n\tds_read2_b32 %3, %6 offset1:10\n\tds_read_b128 %0, %7\n\tds_read_b128 %1, %7 offset:16\n\tds_read_b64 %2, %8\n\t"
                   "s_sub_u32 %5, %5, 1\n\ts_cbranch_scc1 2f\n\ts_waitcnt lgkmcnt(3)\n\tv_readfirstlane_b32 %4, v90\n\t"
                   "s_cmp_lt_u32 %4, 4\n\ts_cbranch_scc1 1b\n2:\n\ts_waitcnt lgkmcnt(0)"
                   : "=&v"(a0), "=&v"(a1), "=&v"(a2), "=&{v[90:91]}"(cf), "=&s"(cc), "+s"(n) : "v"(ca), "v"(lb0 + pa), "v"(lb0 + pb) : "memory", "scc");
    }
    TS(0, 9);
    if (__builtin_expect(__builtin_amdgcn_readfirstlane(cf[1]) != 0, 0)) {
      const u32x4 q = *(const u32x4*)(smem + L_SC + 16 * lane);
      const uint32_t r = *(const uint32_t*)(smem + L_SC + 1024 + 4 * (lane & 31));
#pragma unroll
      for (int k = 0; k < 4; ++k) { sv[2 * k] = bflo(q[k]); sv[2 * k + 1] = bfhi(q[k]); }
      sv[8] = bflo(r); sv[9] = bfhi(r);
    }
    const uint32_t av[10] = {a0[0], a0[1], a0[2], a0[3], a1[0], a1[1], a1[2], a1[3], a2[0] >> ps, a2[1] >> ps};
    f32x2 h[9];
#pragma unroll
    for (int k = 0; k < 10; ++k) {
      asm("v_fma_mix_f32 %0, %1, %2, %3 op_sel_hi:[1,0,0]" : "=v"(h[(k < 8) ? k >> 1 : 8][k & 1]) : "v"(av[k]), "v"(sv[k]), "v"(xf[(k < 8) ? k >> 1 : 8][k & 1]));
      if (k < 8) asm("v_fma_mix_f32 %0, %1, %2, %3 op_sel:[1,0,0] op_sel_hi:[1,0,0]" : "=v"(h[4 + (k >> 1)][k & 1]) : "v"(av[k]), "v"(sv[k]), "v"(xf[4 + (k >> 1)][k & 1]));
    }
#pragma unroll
    for (int k = 0; k < 9; ++k) hv[k] = pack2(h[k][0], h[k][1]);
    const float da = dot2(hv[2], wv[2], dot2(hv[0], wv[0], zb[0])) + dot2(hv[3], wv[3], dot2(hv[1], wv[1], zb[1]));
    const float db = dot2(hv[6], wv[2], dot2(hv[4], wv[0], zb[2])) + dot2(hv[7], wv[3], dot2(hv[5], wv[1], zb[3]));
    float dt = dot2(hv[8], wv[4], fold32(da, db));
    f32x2 rc, nc;
    ln_stats<true>(hv, lane, 1.f, xf, za, rc, nc, dt);
    dt = (lane < 32) ? dt * rc[0] + ws_ * nc[0] : dt * rc[1] + ws_ * nc[1];
    const uint32_t bb = pack2(dt, dt) & 0xffff0000u;
    const uint32_t kk = (bb ^ (((uint32_t)((int)bb >> 31) & 0x7fff0000u) | 0x80000000u)) | kid;
    u32x4 ka, kb;
    uint32_t old;
    asm volatile("s_mov_b64 exec, %5\n\tds_write_b32 %3, %4\n\ts_mov_b64 exec, 1\n\tv_mov_b32 %2, 1\n\tds_add_rtn_u32 %2, %7, %2\n\ts_mov_b64 exec, -1\n\t"
                 "ds_read_b128 %0, %6\n\tds_read_b128 %1, %6 offset:16\n\ts_waitcnt lgkmcnt(0)"
                 : "=&v"(ka), "=&v"(kb), "=&v"(old) : "v"(ka_), "v"(kk), "s"(0x8000000080000000ull), "v"(kb_), "v"(ca) : "memory");
    if (__builtin_amdgcn_readfirstlane(old) == 7) {
      uint32_t m1 = max(max(ka[0], ka[1]), ka[2]), m2 = med3u(ka[0], ka[1], ka[2]), m3 = min(min(ka[0], ka[1]), ka[2]);
#pragma unroll
      for (int e = 0; e < 5; ++e) {
        const uint32_t kx = e ? kb[e - 1] : ka[3];
        m3 = med3u(m2, m3, kx); m2 = med3u(m1, m2, kx); m1 = max(m1, kx);
      }
      if (lane < 8) ST16(ko_, ((u32x4){m1 | 24u, m2 | 16u, m3 | 8u, T & ~1u}));
    }
    TS(0, 10);
    keya = (uint32_t)__builtin_amdgcn_readlane(kk, 31); keyb = (uint32_t)__builtin_amdgcn_readlane(kk, 63);
    ln_apply<false>(xf, lane, rc, nc, nv);
    gwait(ca, 8);
    if (wl == 1) {
      const u32x4 kv = *(const u32x4*)&kl[4 * (lane & 1)];
      if (lane < 2) ST16(O_KF + (uint32_t)(2 * g2 + lane) * 16u, ((u32x4){__builtin_amdgcn_perm(kv[1], kv[0], 0x07060302u), T, __builtin_amdgcn_perm(kv[3], kv[2], 0x07060302u), T}));
      if (ROLE == 2 && b == 0 && lane == 0) ST4(O_EP, T * 0x0E8B2F51u);
    }
    const uint32_t ko = kl[lane & 7];
    rka = FORCEFB + __popcll(__ballot(ko > keya) & 0xffull); rkb = FORCEFB + __popcll(__ballot(ko > keyb) & 0xffull);
#pragma unroll
    for (int k = 0; k < 9; ++k) {
      const f32x2 o = xf[k] + (f32x2){bflo(nv[k]), bfhi(nv[k])};
      hv[k] = pack2(o[0], o[1]);
      nv[k] = min(nv[k], 0xfffffffeu);
    }
    if (rka < 2) put_row(nv, 0, ws, O_XC + (uint32_t)(1023 - 4 * g2 - rka) * 1152u);
    if (rkb < 2) put_row(nv, 1, ws, O_XC + (uint32_t)(1023 - 4 * g2 - rkb) * 1152u);
    if (!LPOS && ffn) down_dma(KN, 4, lane);
  }
  uint32_t k3rd = 0u, k4th = 0u;
  if constexpr (ROLE == 1) {
    u32x4 key;
    const uint32_t kof = O_KEY + (uint32_t)(256 * bx + tid) * 16u;
    until(tk);
    if (!ffn) SLEEP(NSLEEP);
    LD16(key, kof);
    if (ffn) {
      gwait0(ca + 4, 8);
      gu_read(1);
    }
    asm volatile("s_waitcnt vmcnt(0)" : "+v"(key) : : "memory");
    if (__builtin_expect(__any(key[3] != (T & ~1u)), 0)) {
#pragma unroll 1
      for (int spin = 0; spin < SPIN_CAP; ++spin) {
        const bool need = key[3] != (T & ~1u);
        if (!__any(need)) break;
        TSC(15);
        SLEEP(KBACK);
        if (need) LD16W(key, kof);
      }
    }
    TS(0, 11);
    const uint32_t m1 = oct_maxu(key[0]);
    const bool hit = key[0] == m1;
    const uint32_t k0 = hit ? key[1] : key[0], k1 = hit ? key[2] : key[1];
    const uint32_t m2 = oct_maxu(k0);
    if ((lane & 7) == 0) *(u32x2*)&cand[tid >> 2] = (u32x2){m1, m2};
    k3rd = (k0 == m2) ? k1 : k0;
    k4th = (k0 == m2) ? (hit ? 0u : key[2]) : k1;
    if (RPF == 1 && !ffn && w == (bj & 3)) {
      const uint32_t r1 = O_XC + ((m1 >> 3) & 0x3ffu) * 1152u + 128u * (lane & 7), r2 = O_XC + ((m2 >> 3) & 0x3ffu) * 1152u + 128u * (lane & 7);
      asm volatile("global_load_dword v126, %0, %3 sc1\n\tglobal_load_dword v126, %1, %3 sc1\n\tglobal_load_dword v126, %2, %3 offset:1024 sc1"
                   : : "v"(r1), "v"(r2), "v"(((lane & 1) ? r1 : r2) - 128u * (lane & 7)), "s"(ws) : "memory", "v126");
    }
  }
  auto pick = [&](const uint32_t* cd, uint32_t* sl) {
    BAR();
    const uint32_t c = cd[lane], own = cd[8 * w + (lane & 7)];
    const u32x4 la = *(const u32x4*)&cd[8 * w], lb = *(const u32x4*)&cd[8 * w + 4];
    uint32_t r = C_;
    asm("v_cmp_gt_u32 s[40:41], %1, %2\n\tv_cmp_gt_u32 s[42:43], %1, %3\n\tv_cmp_gt_u32 s[44:45], %1, %4\n\tv_cmp_gt_u32 s[46:47], %1, %5\n\t"
        "v_cmp_gt_u32 s[48:49], %1, %6\n\tv_cmp_gt_u32 s[50:51], %1, %7\n\tv_cmp_gt_u32 s[52:53], %1, %8\n\tv_cmp_gt_u32 s[54:55], %1, %9\n\t"
        "s_bcnt1_i32_b64 s40, s[40:41]\n\ts_bcnt1_i32_b64 s42, s[42:43]\n\tv_writelane_b32 %0, s40, 0\n\ts_bcnt1_i32_b64 s44, s[44:45]\n\t"
        "v_writelane_b32 %0, s42, 1\n\ts_bcnt1_i32_b64 s46, s[46:47]\n\tv_writelane_b32 %0, s44, 2\n\ts_bcnt1_i32_b64 s48, s[48:49]\n\t"
        "v_writelane_b32 %0, s46, 3\n\ts_bcnt1_i32_b64 s50, s[50:51]\n\tv_writelane_b32 %0, s48, 4\n\ts_bcnt1_i32_b64 s52, s[52:53]\n\t"
        "v_writelane_b32 %0, s50, 5\n\ts_bcnt1_i32_b64 s54, s[54:55]\n\tv_writelane_b32 %0, s52, 6\n\tv_writelane_b32 %0, s54, 7"
        : "+v"(r) : "v"(c), "v"(la[0]), "v"(la[1]), "v"(la[2]), "v"(la[3]), "v"(lb[0]), "v"(lb[1]), "v"(lb[2]), "v"(lb[3])
        : "scc", "s40", "s41", "s42", "s43", "s44", "s45", "s46", "s47", "s48", "s49", "s50", "s51", "s52", "s53", "s54", "s55");
    sl[min(r, (uint32_t)C_)] = own;
    BAR();
  };
  auto rows = [&](uint32_t ao, auto&& meanwhile) {
    u32x4 aa, ab, ac = __builtin_bit_cast(u32x4, zb);
    asm volatile("global_load_dwordx4 %0, %3, %4 sc1\n\tglobal_load_dwordx4 %1, %3, %4 offset:512 sc1\n\t"
                 "s_cmp_lg_u32 %5, 0\n\ts_cbranch_scc1 1f\n\tglobal_load_dwordx4 %2, %3, %4 offset:1024 sc1\n1:"
                 : "=&v"(aa), "=&v"(ab), "+v"(ac) : "v"(ao), "s"(ws), "s"(w) : "memory", "scc");
    __builtin_amdgcn_sched_barrier(0);
    meanwhile();
    asm volatile("s_waitcnt vmcnt(0)" : "+v"(aa), "+v"(ab), "+v"(ac) : : "memory");
    if (__builtin_expect(__any(max(max(max4(aa), max4(ab)), max4(ac)) == 0xffffffffu), 0)) {
#pragma unroll 1
      for (int spin = 0; spin < SPIN_CAP; ++spin) {
        const bool na = max4(aa) == 0xffffffffu, nb = max4(ab) == 0xffffffffu, nc = max4(ac) == 0xffffffffu;
        if (!__any(na || nb || nc)) break;
        TSC(14);
        SLEEP(PSLEEP);
        if (na) LD16W(aa, ao);
        if (nb) LD16W(ab, ao + 512u);
        if (nc) LD16W(ac, ao + 1024u);
      }
    }
    *(u32x4*)(smem + LA + 16 * tid) = aa;
    *(u32x4*)(smem + LA + 4096 + 16 * tid) = ab;
    if (w == 0) *(u32x4*)(smem + LA + 8192 + 16 * tid) = ac;
  };
  auto rows_dma = [&](uint32_t ao) {
    uint32_t dl = lb0 + LA + 1024u * w, keep;
    asm volatile("s_mov_b32 %0, m0\n\ts_mov_b32 m0, %1\n\ts_add_u32 %1, %1, 4096 - 512\n\tglobal_load_lds_dwordx4 %2, %3 sc1\n\ts_mov_b32 m0, %1\n\ts_add_u32 %1, %1, 4096 - 512\n\t"
                 "global_load_lds_dwordx4 %2, %3 offset:512 sc1\n\ts_cmp_lg_u32 %4, 0\n\ts_cbranch_scc1 1f\n\ts_mov_b32 m0, %1\n\ts_nop 0\n\t"
                 "global_load_lds_dwordx4 %2, %3 offset:1024 sc1\n1:\n\ts_mov_b32 m0, %0" : "=&s"(keep), "+s"(dl) : "v"(ao), "s"(ws), "s"(w) : "memory", "scc");
  };
  auto get_bd = [&](int t0, int t1) {
#pragma unroll
    for (int t = t0; t < t1; ++t) {
      const u32x2 h0 = trd(da + 512 * (t >> 2) + 32 * (t & 3)), h1 = trd(da + 512 * (t >> 2) + 32 * (t & 3) + 4608 + 32);
      bw[t] = (u32x4){h0[0], h0[1], h1[0], h1[1]};
    }
#pragma unroll
    for (int t = t0; t < t1; ++t) asm volatile("" : "+v"(bw[t]));
  };
  pick(cand, sel);
  uint32_t mykey = sel[lane & 7], ro = ((mykey >> 3) & 0x3ffu) * 1152u;
  TS(0, 12);
  auto check = [&] {
    cnt[(k3rd > (uint32_t)__builtin_amdgcn_readlane(mykey, 7)) ? 5 : 6] = 1u;
    const uint32_t m3 = oct_maxu(k3rd);
    if (k3rd == m3) k3rd = k4th;
    if ((lane & 7) == 0) cand[72 + (tid >> 3)] = m3;
    if (w == 0 && lane < 32) cand[(lane < 8) ? 64 + lane : 96 + lane] = (lane < 8) ? mykey : 0u;
  };
  if constexpr (ROLE == 1) {
    if (ffn && !FORCEFB) rows_dma(ro + al);
    check();
    DRAIN();
  } else if (ffn) {
    if (LPOS) { SLEEP(LSLEEP); down_dma(KN, 4, lane); } else { DRAIN(); gsync0(ca, 12); get_bd(0, 8); }
  }
  BAR();
  TS(0, 5);
  auto put_out = [&](int j, uint32_t k0, int rk) {
    const uint64_t mine = __ballot(((mykey ^ k0) & ~24u) == 0u) & 0xffull;
    if (mine == 0ull) put_row(hv, j, out, (uint32_t)((8 * g2 + 2 * wl + j) * (D_ * 2)));
    else if (rk >= 2) put_row(nv, j, ws, O_FB + 1152u * (uint32_t)__builtin_ctzll(mine));
  };
  auto rest_out = [&] { put_out(0, keya, rka); put_out(1, keyb, rkb); };
  f32x4 lo, hi;
  auto gate_up = [&] {
    u32x4 av[5];
    const uint32_t aa = lb0 + LA + 16u * lane;
#pragma unroll
    for (int j = 0; j < 5; ++j) asm volatile("ds_read_b128 %0, %1 offset:%2" : "=v"(av[j]) : "v"(aa), "n"(1024 * j) : "memory");
    lo = (f32x4){0.f, 0.f, 0.f, 0.f}; hi = lo;
#pragma unroll
    for (int j = 0; j < 9; ++j) {
      if (j < 5) asm volatile("s_waitcnt lgkmcnt(4)" : "+v"(av[j % 5]));
      else if (j == 5) asm volatile("s_waitcnt lgkmcnt(2)" : "+v"(av[0]), "+v"(av[1]));
      else if (j == 7) asm volatile("s_waitcnt lgkmcnt(0)" : "+v"(av[2]), "+v"(av[3]));
      lo = MMB(av[j % 5], bw[2 * j], lo);
      hi = MMB(av[j % 5], bw[2 * j + 1], hi);
      __builtin_amdgcn_sched_barrier(0);
      if (j < 4) asm volatile("ds_read_b128 %0, %1 offset:%2" : "=v"(av[j]) : "v"(aa), "n"(1024 * (j + 5)) : "memory");
    }
  };
  uint32_t fl;
  asm volatile("ds_read_b32 %0, %1" : "=v"(fl) : "v"(ca + 20) : "memory");
  if (ROLE == 1) gate_up();
  asm volatile("s_waitcnt lgkmcnt(0)" : "+v"(fl));
  const bool slow = FORCESLOW || FORCEFB || __builtin_amdgcn_readfirstlane(fl) != 0;
  if (__builtin_expect(slow, 0)) {
    TSC(17);
    pick(cand + 64, sel + 16);
    mykey = sel[16 + (lane & 7)];
    ro = ((mykey >> 3) & 0x3ffu) * 1152u;
    if (ROLE == 1 && (k3rd > (uint32_t)__builtin_amdgcn_readlane(mykey, 7) || (mykey & 24u) == 8u)) cnt[11] = 1u;
    BAR();
    if (FORCESLOW > 1 || FORCEFB || __builtin_amdgcn_readfirstlane(cnt[11]) != 0) {
    u32x4 key;
#pragma unroll 1
    for (int spin = 0; spin < SPIN_CAP; ++spin) {
      LD16W(key, O_KF + 16u * tid);
      if (!__any(key[1] != T || key[3] != T)) break;
      SLEEP(KBACK);
    }
    const uint32_t ib = (uint32_t)(((255 - (tid >> 1)) << 5) | (7 - 4 * (tid & 1)));
    uint32_t k0 = (key[0] << 16) | ib, k1 = (key[0] & 0xffff0000u) | (ib - 1), k2 = (key[2] << 16) | (ib - 2), k3 = (key[2] & 0xffff0000u) | (ib - 3);
    CEX(k0, k1) CEX(k2, k3) CEX(k0, k2) CEX(k1, k3) CEX(k1, k2)
#pragma unroll 1
    for (int c = 0; c < C_; ++c) {
      const uint32_t mx = wave_maxu(k0);
      const bool hit = (k0 == mx);
      k0 = hit ? k1 : k0; k1 = hit ? k2 : k1; k2 = hit ? k3 : k2; k3 = hit ? 0u : k3;
      if (lane == 0) cand[64 + 8 * w + c] = mx;
    }
    pick(cand + 64, sel + 32);
    mykey = sel[32 + (lane & 7)];
    const uint32_t okey = sel[32 + (lane >> 3)];
    const uint64_t same = __ballot(((mykey ^ okey) & 0x1fe0u) == 0u && okey > mykey);
    const int sl = FORCEFB + __popcll((same >> (lane & 7)) & 0x0101010101010101ull);
    ro = (sl < 2) ? (((mykey >> 3) & 0x3ffu) + 3 - sl) * 1152u : O_FB - O_XC + 1152u * (lane & 7);
    }
    if (ln2) rest_out();
    if (ffn) {
      if (ROLE == 1) rows(ro + al, [] {});
      BAR();
      if (ROLE == 1) gate_up();
    }
  }
  if (ROLE == 1 && ffn && !slow && __builtin_expect(__any(lo[0] != lo[0] || lo[1] != lo[1] || lo[2] != lo[2] || lo[3] != lo[3] ||
                                                           hi[0] != hi[0] || hi[1] != hi[1] || hi[2] != hi[2] || hi[3] != hi[3]), 0)) {
    gsync0(ca + 4, 12);
    rows(ro + al, [] {});
    gsync0(ca + 4, 16);
    gate_up();
  }
  if (OPOS == 0 && ROLE == 2 && ffn && !slow) { SLEEP(OD); rest_out(); }
  if (!ffn) {
    if (ln2 && !slow) { SLEEP(OD2); rest_out(); }
    TSE();
    return;
  }
  uint32_t fx = 0, fv = 0, fn = 0, fo = 0, sa[9];
  float rs, hx, xs;
  u32x4 se;
  if constexpr (ROLE == 1) {
    const f32x4 lh = (lane < 32) ? lo : hi;
    const float fa = fold32(lh[0], lh[2]), fb2 = fold32(lh[1], lh[3]);
    const float g = __int_as_float(__builtin_amdgcn_update_dpp(__float_as_int(fa), __float_as_int(fb2), 0x128, 0xf, 0xc, false));
    const float u = __int_as_float(__builtin_amdgcn_update_dpp(__float_as_int(fb2), __float_as_int(fa), 0x128, 0xf, 0x3, false));
    const uint32_t gu = pack2(g, u);
    const float gp = bflo(gu);
    const float sg = rbf(gp * __builtin_amdgcn_rcpf(1.f + __expf(-gp)));
    *(u16*)((char*)hm + ha) = f2bf(sg * bfhi(gu));
  } else if (LPOS) {
    DRAIN();
    gsync0(ca, 12);
    get_bd(0, 8);
  }
  BAR();
  TS(0, 6);
  if (ROLE == 2 || w == 2) {
    const u32x4 a0 = *(const u32x4*)((char*)hm + h0a), a1 = *(const u32x4*)((char*)hm + h1a);
    if (ROLE == 1) get_bd(0, 4);
    const f32x4 z = {0.f, 0.f, 0.f, 0.f};
    f32x4 o[4];
#pragma unroll
    for (int t = 0; t < 2 * ROLE; ++t) o[t] = MMB(a1, bw[2 * t + 1], MMB(a0, bw[2 * t], z));
#pragma unroll
    for (int t = 0; t < ROLE; ++t) {
      u32x4 sv;
#pragma unroll
      for (int r = 0; r < 4; ++r) sv[r] = min(pack2(o[2 * t][r], o[2 * t + 1][r]), 0xfffffffeu);
      ST16(so + 4096u * t, sv);
    }
    TS(0, 13);
  }
  if (OPOS == 1 && ROLE == 2) {
    SLEEP(OSLEEP);
    if (!slow) rest_out();
  }
  if (ROLE == 1 && w == 1) {
    const int q = (fb & 15) >> 2, ts = 4 * (q & 1) + ((i & 7) >> 1);
    const int dd = 64 * (fb >> 4) + 16 * (2 * (i & 1) + (q >> 1)) + 4 * (fb & 3) + 2 * (kq & 1) + (kq >> 1);
    const uint32_t fkey = (uint32_t)__builtin_amdgcn_ds_bpermute(4 * ts, (int)mykey), fr = (uint32_t)__builtin_amdgcn_ds_bpermute(4 * ts, (int)ro);
    const int ft = 2047 - (int)(((fkey >> 2) & 0x7f8u) | (fkey & 7u));
    fo = (uint32_t)(ft * D_ + dd) * 2u;
    u32x2 fs;
    asm volatile("global_load_ushort %0, %4, %8\n\tglobal_load_ushort %1, %5, %9 sc1\n\tglobal_load_ushort %2, %6, %9 sc1\n\tglobal_load_dwordx2 %3, %7, %9 sc1"
                 : "=&v"(fx), "=&v"(fv), "=&v"(fn), "=&v"(fs) : "v"(fo), "v"(v_at(dd, ft >> 3) + 2u * (ft & 7)), "v"(O_XC + fr + 2u * dd), "v"(O_SC + 16u * dd), "s"(x), "s"(ws) : "memory");
    uint32_t t16 = (((T >> (3 * ((ft >> 1) & 3))) & LMASK) | (((T >> (12 + ((ft >> 1) & 3))) & 0x00010001u) << 14)) >> (16 * (ft & 1)) & 0xffffu;
    const uint32_t o = fkey >> 16;
    const float rs0 = bf2f((u16)((o & 0x8000u) ? (o & 0x7fffu) : (~o & 0xffffu)));
    rs = rbf(__builtin_amdgcn_rcpf(1.f + __expf(-rs0)));
    const uint32_t one = (i < 8) ? (0x3f80u << (16 * (i & 1))) : 0u;
    se = (u32x4){(i >> 1) == 0 ? one : 0u, (i >> 1) == 1 ? one : 0u, (i >> 1) == 2 ? one : 0u, (i >> 1) == 3 ? one : 0u};
#pragma unroll
    for (int m = 0; m < 9; ++m) sa[m] = O_SL + (uint32_t)(16 * m + (lane >> 2)) * SP + (uint32_t)(4 * fb + (lane & 3)) * 16u;
    asm volatile("" : "+v"(t16), "+v"(rs), "+v"(se), "+v"(sa[0]), "+v"(sa[1]), "+v"(sa[2]), "+v"(sa[3]), "+v"(sa[4]), "+v"(sa[5]), "+v"(sa[6]), "+v"(sa[7]), "+v"(sa[8]));
    SLEEP(SSLEEP);
    TS(0, 1);
    float sum;
#pragma unroll 1
    for (int spin = 0; spin < SPIN_CAP; ++spin) {
      u32x4 sv[9];
#pragma unroll
      for (int m = 0; m < 9; ++m) LD16(sv[m], sa[m]);
      asm volatile("s_waitcnt vmcnt(9)" : "+v"(fx), "+v"(fv), "+v"(fn), "+v"(fs));
      if (__builtin_expect(__any(fs[0] != T), 0)) {
        for (int k = 0; k < SPIN_CAP && fs[0] != T; ++k)
          asm volatile("global_load_dwordx2 %0, %1, %2 sc1\n\ts_waitcnt vmcnt(0)" : "=&v"(fs) : "v"(O_SC + 16u * dd), "s"(ws) : "memory");
      }
      const uint32_t f1 = fv ^ t16, f2 = (f1 & 7u) ? fv & ~7u : f1;
      hx = rbf(bf2f((u16)fx) + (float)__builtin_bit_cast(_Float16, (u16)f2) * __uint_as_float(fs[1]));
      xs = bf2f((u16)fn);
      asm volatile("" : "+v"(hx), "+v"(xs));
      f32x4 dv = {0.f, 0.f, 0.f, 0.f};
      static_for<0, 9>([&](auto mm) {
        constexpr int m = mm.value;
        asm volatile("s_waitcnt vmcnt(%1)" : "+v"(sv[m]) : "n"(8 - m));
        dv = MMB(sv[m], se, dv);
        __builtin_amdgcn_sched_barrier(0);
      });
      sum = fold16(fold32(dv[0], dv[1]), fold32(dv[2], dv[3]));
      if (__builtin_expect(!__any(sum != sum), 1)) break;
      uint32_t mx = 0u;
#pragma unroll
      for (int m = 0; m < 9; ++m) mx = max(mx, max4(sv[m]));
      if (!__any(mx == 0xffffffffu)) break;
      TSC(18);
      SLEEP(PSLEEP);
    }
    const float o1 = rbf(xs + rbf(rbf(sum) * rs));
    if (i < 8) asm volatile("global_store_short %0, %1, %2 sc1" : : "v"(fo), "v"((uint32_t)f2bf(hx + o1)), "s"(out) : "memory");
    TS(0, 2);
  }
  TSE();
  }
  }
}
#define BROT "192"
#define CPF_A "s_getpc_b64 s[96:97]\n\ts_lshl_b32 s99, s2, 4\n\ts_lshl_b32 s84, s98, 7\n\ts_add_u32 s99, s99, s84\n\tv_and_b32 v112, 1, v0\n\t" \
              "v_lshl_add_u32 v112, v112, 12, s99\n\tv_min_u32 v112, kf_end-kf_all-0x80, v112\n\ts_mov_b64 exec, 3\n\tglobal_load_dword v112, v112, s[96:97]\n\ts_mov_b64 exec, -1\n\t"
extern "C" __global__ __launch_bounds__(512, 4) void kf_all(KARGS) {
  __shared__ __attribute__((aligned(16))) char smem[SMEM];
  asm volatile("s_load_dwordx16 s[64:79], s[0:1], 0x0\n\ts_load_dwordx4 s[80:83], s[0:1], 0x40\n\tv_readfirstlane_b32 s98, v0\n\t"
               "s_add_u32 s84, s2, " BROT "\n\ts_xor_b32 s84, s84, s2\n\ts_bitset0_b32 s84, 8\n\ts_bitset0_b32 s84, 9\n\ts_xor_b32 s2, s2, s84\n\t"
               "v_mov_b32 v109, 0\n\ts_and_b32 s85, s2, 7\n\ts_bitcmp1_b32 s2, 8\n\ts_cbranch_scc1 1f\n\t" CPF_A "s_bitcmp1_b32 s98, 8\n\ts_cbranch_scc1 2f\n\t"
               "s_andn2_b32 s84, s2, 31\n\ts_lshl2_add_u32 s84, s85, s84\n\ts_bfe_u32 s85, s2, 0x20003\n\ts_or_b32 s84, s84, s85\n\t"
               "s_mulk_i32 s84, 0x2400\n\ts_mulk_i32 s98, 36\n\ts_add_u32 s84, s84, s98\n\tv_and_b32 v110, 63, v0\n\tv_and_b32 v111, 32, v0\n\t"
               "v_lshlrev_b32 v111, 5, v111\n\tv_lshl_add_u32 v111, v110, 2, v111\n\tv_lshl_add_u32 v110, v110, 4, s84\n\tv_add_u32 v111, s84, v111\n\t"
               "s_cmpk_lt_u32 s2, 0x90\n\ts_waitcnt lgkmcnt(0)\n\t"
               "global_load_dwordx4 v[100:103], v110, s[64:65]\n\tglobal_load_dwordx4 v[104:107], v110, s[64:65] offset:1152\n\t"
               "global_load_dword v108, v111, s[64:65] offset:1024\n\tglobal_load_dword v109, v109, s[82:83] sc1\n\t"
               "s_cbranch_scc1 kf_n\n\ts_branch kf_h\n"
               "1:\n\ts_bfe_u32 s84, s2, 0x50003\n\ts_lshl_b32 s85, s85, 5\n\ts_or_b32 s84, s84, s85\n\ts_lshl_b32 s84, s84, 2\n\t"
               "v_and_b32 v110, 0xff, v0\n\tv_bfe_u32 v107, v0, 7, 1\n"
               "4:\n\ts_lshl_b32 s85, s84, 7\n\tv_lshl_add_u32 v110, v110, 1, s85\n\tv_lshl_add_u32 v107, v107, 1, s84\n\ts_and_b32 s85, s98, s2\n\ts_waitcnt lgkmcnt(0)\n\t"
               "global_load_ushort v100, v107, s[78:79]\n\tglobal_load_ushort v101, v110, s[66:67]\n\tglobal_load_ushort v102, v110, s[68:69]\n\t"
               "global_load_dword v109, v109, s[82:83] sc1\n\ts_bitcmp1_b32 s85, 8\n\ts_cbranch_scc1 kf_u\n\t"
               "global_load_ushort v103, v110, s[70:71]\n\tglobal_load_ushort v104, v110, s[72:73]\n\tglobal_load_ushort v105, v110, s[74:75]\n\t"
               "global_load_ushort v106, v110, s[76:77]\n\tglobal_load_ushort v107, v107, s[80:81]\n\ts_bitcmp1_b32 s2, 8\n\ts_cbranch_scc1 kf_c\n"
               "6:\n\ts_barrier\n\ts_branch kf_p\n"
               "2:\n\ts_cmpk_lt_u32 s2, 0xc0\n\ts_cbranch_scc1 6b\n\t"
               "s_lshr_b32 s84, s2, 3\n\ts_lshl3_add_u32 s84, s85, s84\n\ts_lshl1_add_u32 s84, s84, 976\n\t"
               "v_and_b32 v110, 0x7f, v0\n\tv_mov_b32 v107, 0\n\ts_branch 4b"
               : : "s"(__builtin_amdgcn_kernarg_segment_ptr()), "s"(blockIdx.x), "v"(threadIdx.x),
                   "s"((uint32_t)(uintptr_t)(__attribute__((address_space(3))) char*)smem) : "scc", "s96", "s97", "s98", "s99", "v127");
}
extern "C" __global__ __launch_bounds__(512, 4) void kf_c(KARGS) {
  __shared__ __attribute__((aligned(16))) char smem[SMEM];
  role_body<0>(smem, KPASS);
}
extern "C" __global__ __launch_bounds__(512, 4) void kf_u(KARGS) {
  __shared__ __attribute__((aligned(16))) char smem[SMEM];
  role_body<3>(smem, KPASS);
}
extern "C" __global__ __launch_bounds__(512, 4) void kf_n(KARGS) {
  __shared__ __attribute__((aligned(16))) char smem[SMEM];
  role_body<1, 1>(smem, KPASS);
}
extern "C" __global__ __launch_bounds__(512, 4) void kf_h(KARGS) {
  __shared__ __attribute__((aligned(16))) char smem[SMEM];
  role_body<1, 0>(smem, KPASS);
}
extern "C" __global__ __launch_bounds__(512, 4) void kf_p(KARGS) {
  __shared__ __attribute__((aligned(16))) char smem[SMEM];
  role_body<2>(smem, KPASS);
}
extern "C" __global__ __launch_bounds__(512, 4) void kf_conv() { asm volatile(CONV_ASM ::: CONV_CLOB); }
extern "C" __global__ void kf_end() {}

extern "C" void kernel_launch(void* const* d_in, const int* in_sizes, int n_in,
                              void* d_out, int out_size, void* d_ws, size_t ws_size,
                              hipStream_t stream) {
  kf_all<<<512, 512, 0, stream>>>((const u16*)d_in[0], (const u16*)d_in[1], (const u16*)d_in[2], (const u16*)d_in[3], (const u16*)d_in[4],
                                  (const u16*)d_in[5], (const u16*)d_in[6], (const u16*)d_in[7], (const u16*)d_in[8], (char*)d_ws + (size_t)WSOFF * 1048576,
                                  (const u16*)d_in[10], (const u16*)d_in[11], (const u16*)d_in[12], (const u16*)d_in[13], (u16*)d_out);
}
```

```cpp
#include <hip/hip_runtime.h>
#include <hip/hip_bf16.h>
#include <stdint.h>

typedef __attribute__((ext_vector_type(8))) short bf16x8;
typedef __attribute__((ext_vector_type(4))) float f32x4;
typedef uint16_t u16;
typedef __attribute__((ext_vector_type(4))) uint32_t u32x4;
typedef __attribute__((ext_vector_type(2))) uint32_t u32x2;
#define TSD
#define TS(k, i)
#define TSC(i)
#define TSE()
#define TSP
#define TSA

constexpr int L_ = 2048, D_ = 576, N_ = 128, F_ = 4608, C_ = 8;
constexpr int NF = 144;
constexpr uint32_t O_EP = 0;
constexpr uint32_t O_KF = 4096;
constexpr uint32_t O_KEY = O_KF + 8192;
constexpr uint32_t O_SC = O_KEY + 32768;
constexpr uint32_t O_DN = O_SC + 9216;
constexpr uint32_t O_SU = O_DN + 20480;
constexpr uint32_t O_FB = O_SU + 262144;
constexpr uint32_t O_XC = O_FB + 8 * 1152;
constexpr uint32_t SP = 9216 + 128;
constexpr uint32_t O_SL = O_XC + 1024 * 1152;
constexpr uint32_t SLU = 336;
constexpr uint32_t O_U = O_SL + 256 * SLU * 16;
constexpr int UP = 584;
constexpr uint32_t VP = 8192 + 128, VPP = 4096 + 128;
constexpr uint32_t O_V = O_U + 256 * UP * 16, O_VP = O_V + 256 * VP;
static_assert(256 * SLU * 16 >= NF * SP && O_SL % 128 == 0 && O_U % 128 == 0 && O_V % 128 == 0, "");
__host__ __device__ __forceinline__ uint32_t u_at(int tg, int d) { return O_U + 16u * (uint32_t)((tg >> 1) * (UP * 2) + (d >> 2) * 8 + (tg & 1) * 4 + (d & 3)); }
__host__ __device__ __forceinline__ uint32_t v_at(int d, int tg) {
  return (d < 512) ? O_V + (uint32_t)(d >> 1) * VP + 16u * (uint32_t)(2 * tg + (d & 1)) : O_VP + (uint32_t)(d - 512) * VPP + 16u * (uint32_t)tg;
}
#define HAVE_VAT
#define HAVE_VTAG
__device__ __forceinline__ float bf2f(u16 v) { return __uint_as_float((uint32_t)v << 16); }
__device__ __forceinline__ float bflo(uint32_t v) { return __uint_as_float(v << 16); }
__device__ __forceinline__ float bfhi(uint32_t v) { return __uint_as_float(v & 0xffff0000u); }
__device__ __forceinline__ u16 f2bf(float f) { return __builtin_bit_cast(u16, (__bf16)f); }
__device__ __forceinline__ float rbf(float f) { return bf2f(f2bf(f)); }
typedef __attribute__((ext_vector_type(2))) float f32x2;
typedef __attribute__((ext_vector_type(2))) __bf16 bf16x2v;
__device__ __forceinline__ uint32_t pack2(float a, float b) {
  return __builtin_bit_cast(uint32_t, __builtin_convertvector((f32x2){a, b}, bf16x2v));
}

#define DPP_I(v, ctrl, rmask) __builtin_amdgcn_update_dpp(0, (v), (ctrl), (rmask), 0xf, true)
__device__ __forceinline__ float row_sum(float v) {
  v += __int_as_float(DPP_I(__float_as_int(v), 0x111, 0xf));
  v += __int_as_float(DPP_I(__float_as_int(v), 0x112, 0xf));
  v += __int_as_float(DPP_I(__float_as_int(v), 0x114, 0xf));
  v += __int_as_float(DPP_I(__float_as_int(v), 0x118, 0xf));
  return v;
}
__device__ __forceinline__ float rdl(float v, int l) { return __int_as_float(__builtin_amdgcn_readlane(__float_as_int(v), l)); }
__device__ __forceinline__ float wave_sum(float v) {
  v = row_sum(v);
  asm("s_nop 1\n\tv_add_f32_dpp %0, %0, %0 row_bcast:15 row_mask:0xa bank_mask:0xf\n\t"
      "s_nop 1\n\tv_add_f32_dpp %0, %0, %0 row_bcast:31 row_mask:0xc bank_mask:0xf\n\ts_nop 0" : "+v"(v));
  return rdl(v, 63);
}
__device__ __forceinline__ float fold32(float a, float b) {
  const auto r = __builtin_amdgcn_permlane32_swap(__float_as_uint(a), __float_as_uint(b), false, false);
  return __uint_as_float(r[0]) + __uint_as_float(r[1]);
}
__device__ __forceinline__ float fold16(float a, float b) {
  const auto r = __builtin_amdgcn_permlane16_swap(__float_as_uint(a), __float_as_uint(b), false, false);
  return __uint_as_float(r[0]) + __uint_as_float(r[1]);
}
__device__ __forceinline__ void wave_sum2(float& a, float& b) {
  float v = row_sum(fold32(a, b));
  asm("s_nop 1\n\tv_add_f32_dpp %0, %0, %0 row_bcast:15 row_mask:0xa bank_mask:0xf\n\ts_nop 0" : "+v"(v));
  a = rdl(v, 31); b = rdl(v, 63);
}
__device__ __forceinline__ float dot2(uint32_t a, uint32_t b, float c) {
  return __builtin_amdgcn_fdot2_f32_bf16(__builtin_bit_cast(bf16x2v, a), __builtin_bit_cast(bf16x2v, b), c, false);
}
__device__ __forceinline__ uint32_t row_maxu(uint32_t v) {
  v = max(v, (uint32_t)DPP_I((int)v, 0xb1, 0xf));
  v = max(v, (uint32_t)DPP_I((int)v, 0x4e, 0xf));
  v = max(v, (uint32_t)DPP_I((int)v, 0x141, 0xf));
  v = max(v, (uint32_t)DPP_I((int)v, 0x140, 0xf));
  return v;
}
__device__ __forceinline__ uint32_t wave_maxu(uint32_t v) {
  v = max(v, (uint32_t)DPP_I((int)v, 0x111, 0xf));
  v = max(v, (uint32_t)DPP_I((int)v, 0x112, 0xf));
  v = max(v, (uint32_t)DPP_I((int)v, 0x114, 0xf));
  v = max(v, (uint32_t)DPP_I((int)v, 0x118, 0xf));
  v = max(v, (uint32_t)DPP_I((int)v, 0x142, 0xa));
  v = max(v, (uint32_t)DPP_I((int)v, 0x143, 0xc));
  return (uint32_t)__builtin_amdgcn_readlane((int)v, 63);
}

typedef __attribute__((ext_vector_type(8))) _Float16 f16x8;
typedef __attribute__((ext_vector_type(2))) _Float16 f16x2;
__device__ __forceinline__ uint32_t packh(f32x2 v) { return __builtin_bit_cast(uint32_t, __builtin_convertvector(v, f16x2)); }
__device__ __forceinline__ uint32_t packh(float a, float b) { return packh((f32x2){a, b}); }
constexpr int IP = 224, I_X = 128;
__device__ __forceinline__ f32x2 cmul(f32x2 z, f32x2 w) {
  f32x2 r;
  asm("v_pk_mul_f32 %0, %1, %2 op_sel_hi:[0,1]\n\tv_pk_fma_f32 %0, %1, %2, %0 op_sel:[1,1,0] op_sel_hi:[1,0,1] neg_lo:[0,1,0]" : "=&v"(r) : "v"(z), "v"(w));
  return r;
}
__device__ __forceinline__ f32x2 cexp2(float ar2, float air, float l) {
  const float t = air * l;
  return (f32x2){__builtin_amdgcn_cosf(t), __builtin_amdgcn_sinf(t)} * __builtin_amdgcn_exp2f(ar2 * l);
}
__device__ __forceinline__ float state_a(float ldt, float are, float aim, float& ar2, float& air, float& g) {
  const float dt = rbf(__expf(ldt));
  ar2 = dt * are * 1.4426950408889634f;
  air = dt * aim * 0.15915494309189535f;
  air -= __builtin_rintf(air);
  g = 0.f;
  if (__builtin_expect(__any(ar2 > 0.f), 0)) g = fminf(__uint_as_float(wave_maxu(__float_as_uint(fmaxf(ar2, 0.f)))), 1.f);
  return dt;
}
template <int N> __device__ __forceinline__ void put_im(char* p, int im, const f32x2* z) {
  *(u32x4*)p = (u32x4){packh(z[0][0], z[1][0]), packh(z[2][0], z[3][0]), packh(z[4][0], z[5][0]), packh(z[6][0], z[7][0])};
  *(u32x4*)(p + im) = (u32x4){packh(z[0][1], z[1][1]), packh(z[2][1], z[3][1]), packh(z[4][1], z[5][1]), packh(z[6][1], z[7][1])};
}
#define Y_ASM \
  "v_mov_b64 v[40:41], %0\nv_pk_mul_f32 v[42:43], v[40:41], %1 op_sel_hi:[0,1]\nv_pk_mul_f32 v[44:45], %0, %2 op_sel_hi:[0,1]\n" \
  "v_pk_fma_f32 v[42:43], v[40:41], %1, v[42:43] op_sel:[1,1,0] op_sel_hi:[1,0,1] neg_lo:[0,1,0]\n" \
  "v_pk_fma_f32 v[44:45], %0, %2, v[44:45] op_sel:[1,1,0] op_sel_hi:[1,0,1] neg_lo:[0,1,0]\nv_cvt_pk_f16_f32 v56, v40, v42\n" \
  "v_cvt_pk_f16_f32 v60, v41, v43\nv_pk_mul_f32 v[40:41], v[42:43], %1 op_sel_hi:[0,1]\nv_pk_mul_f32 v[48:49], v[44:45], %2 op_sel_hi:[0,1]\n" \
  "v_pk_fma_f32 v[40:41], v[42:43], %1, v[40:41] op_sel:[1,1,0] op_sel_hi:[1,0,1] neg_lo:[0,1,0]\n" \
  "v_pk_fma_f32 v[48:49], v[44:45], %2, v[48:49] op_sel:[1,1,0] op_sel_hi:[1,0,1] neg_lo:[0,1,0]\n" \
  "v_pk_mul_f32 v[42:43], v[40:41], %1 op_sel_hi:[0,1]\nv_pk_mul_f32 v[52:53], v[48:49], %2 op_sel_hi:[0,1]\n" \
  "v_pk_fma_f32 v[42:43], v[40:41], %1, v[42:43] op_sel:[1,1,0] op_sel_hi:[1,0,1] neg_lo:[0,1,0]\n" \
  "v_pk_fma_f32 v[52:53], v[48:49], %2, v[52:53] op_sel:[1,1,0] op_sel_hi:[1,0,1] neg_lo:[0,1,0]\nv_cvt_pk_f16_f32 v57, v40, v42\n" \
  "v_cvt_pk_f16_f32 v61, v41, v43\nv_pk_mul_f32 v[40:41], v[42:43], %1 op_sel_hi:[0,1]\n" \
  "v_pk_fma_f32 v[40:41], v[42:43], %1, v[40:41] op_sel:[1,1,0] op_sel_hi:[1,0,1] neg_lo:[0,1,0]\n" \
  "v_pk_mul_f32 v[42:43], v[40:41], %1 op_sel_hi:[0,1]\nv_pk_mul_f32 v[46:47], v[44:45], %1 op_sel_hi:[0,1]\n" \
  "v_pk_fma_f32 v[42:43], v[40:41], %1, v[42:43] op_sel:[1,1,0] op_sel_hi:[1,0,1] neg_lo:[0,1,0]\n" \
  "v_pk_fma_f32 v[46:47], v[44:45], %1, v[46:47] op_sel:[1,1,0] op_sel_hi:[1,0,1] neg_lo:[0,1,0]\nv_cvt_pk_f16_f32 v58, v40, v42\n" \
  "v_cvt_pk_f16_f32 v62, v41, v43\nv_cvt_pk_f16_f32 v64, v44, v46\nv_cvt_pk_f16_f32 v68, v45, v47\n" \
  "v_pk_mul_f32 v[40:41], v[42:43], %1 op_sel_hi:[0,1]\nv_pk_mul_f32 v[44:45], v[46:47], %1 op_sel_hi:[0,1]\n" \
  "v_pk_fma_f32 v[40:41], v[42:43], %1, v[40:41] op_sel:[1,1,0] op_sel_hi:[1,0,1] neg_lo:[0,1,0]\n" \
  "v_pk_fma_f32 v[44:45], v[46:47], %1, v[44:45] op_sel:[1,1,0] op_sel_hi:[1,0,1] neg_lo:[0,1,0]\n" \
  "v_pk_mul_f32 v[42:43], v[40:41], %1 op_sel_hi:[0,1]\nv_pk_mul_f32 v[46:47], v[44:45], %1 op_sel_hi:[0,1]\n" \
  "v_pk_fma_f32 v[42:43], v[40:41], %1, v[42:43] op_sel:[1,1,0] op_sel_hi:[1,0,1] neg_lo:[0,1,0]\n" \
  "v_pk_fma_f32 v[46:47], v[44:45], %1, v[46:47] op_sel:[1,1,0] op_sel_hi:[1,0,1] neg_lo:[0,1,0]\nv_cvt_pk_f16_f32 v59, v40, v42\n" \
  "v_cvt_pk_f16_f32 v63, v41, v43\nv_cvt_pk_f16_f32 v65, v44, v46\nv_cvt_pk_f16_f32 v69, v45, v47\n" \
  "v_pk_mul_f32 v[44:45], v[46:47], %1 op_sel_hi:[0,1]\nds_write_b128 %3, v[56:59] offset:0\n" \
  "v_pk_fma_f32 v[44:45], v[46:47], %1, v[44:45] op_sel:[1,1,0] op_sel_hi:[1,0,1] neg_lo:[0,1,0]\nds_write_b128 %3, v[60:63] offset:64\n" \
  "v_pk_mul_f32 v[46:47], v[44:45], %1 op_sel_hi:[0,1]\nv_pk_mul_f32 v[50:51], v[48:49], %1 op_sel_hi:[0,1]\n" \
  "v_pk_fma_f32 v[46:47], v[44:45], %1, v[46:47] op_sel:[1,1,0] op_sel_hi:[1,0,1] neg_lo:[0,1,0]\n" \
  "v_pk_fma_f32 v[50:51], v[48:49], %1, v[50:51] op_sel:[1,1,0] op_sel_hi:[1,0,1] neg_lo:[0,1,0]\nv_cvt_pk_f16_f32 v66, v44, v46\n" \
  "v_cvt_pk_f16_f32 v70, v45, v47\nv_cvt_pk_f16_f32 v56, v48, v50\nv_cvt_pk_f16_f32 v60, v49, v51\n" \
  "v_pk_mul_f32 v[44:45], v[46:47], %1 op_sel_hi:[0,1]\nv_pk_mul_f32 v[48:49], v[50:51], %1 op_sel_hi:[0,1]\n" \
  "v_pk_fma_f32 v[44:45], v[46:47], %1, v[44:45] op_sel:[1,1,0] op_sel_hi:[1,0,1] neg_lo:[0,1,0]\n" \
  "v_pk_fma_f32 v[48:49], v[50:51], %1, v[48:49] op_sel:[1,1,0] op_sel_hi:[1,0,1] neg_lo:[0,1,0]\n" \
  "v_pk_mul_f32 v[46:47], v[44:45], %1 op_sel_hi:[0,1]\nv_pk_mul_f32 v[50:51], v[48:49], %1 op_sel_hi:[0,1]\n" \
  "v_pk_fma_f32 v[46:47], v[44:45], %1, v[46:47] op_sel:[1,1,0] op_sel_hi:[1,0,1] neg_lo:[0,1,0]\n" \
  "v_pk_fma_f32 v[50:51], v[48:49], %1, v[50:51] op_sel:[1,1,0] op_sel_hi:[1,0,1] neg_lo:[0,1,0]\nv_cvt_pk_f16_f32 v67, v44, v46\n" \
  "v_cvt_pk_f16_f32 v71, v45, v47\nv_cvt_pk_f16_f32 v57, v48, v50\nv_cvt_pk_f16_f32 v61, v49, v51\n" \
  "v_pk_mul_f32 v[48:49], v[50:51], %1 op_sel_hi:[0,1]\nds_write_b128 %3, v[64:67] offset:16\n" \
  "v_pk_fma_f32 v[48:49], v[50:51], %1, v[48:49] op_sel:[1,1,0] op_sel_hi:[1,0,1] neg_lo:[0,1,0]\nds_write_b128 %3, v[68:71] offset:80\n" \
  "v_pk_mul_f32 v[50:51], v[48:49], %1 op_sel_hi:[0,1]\nv_pk_mul_f32 v[54:55], v[52:53], %1 op_sel_hi:[0,1]\n" \
  "v_pk_fma_f32 v[50:51], v[48:49], %1, v[50:51] op_sel:[1,1,0] op_sel_hi:[1,0,1] neg_lo:[0,1,0]\n" \
  "v_pk_fma_f32 v[54:55], v[52:53], %1, v[54:55] op_sel:[1,1,0] op_sel_hi:[1,0,1] neg_lo:[0,1,0]\nv_cvt_pk_f16_f32 v58, v48, v50\n" \
  "v_cvt_pk_f16_f32 v62, v49, v51\nv_cvt_pk_f16_f32 v64, v52, v54\nv_cvt_pk_f16_f32 v68, v53, v55\n" \
  "v_pk_mul_f32 v[48:49], v[50:51], %1 op_sel_hi:[0,1]\nv_pk_mul_f32 v[52:53], v[54:55], %1 op_sel_hi:[0,1]\n" \
  "v_pk_fma_f32 v[48:49], v[50:51], %1, v[48:49] op_sel:[1,1,0] op_sel_hi:[1,0,1] neg_lo:[0,1,0]\n" \
  "v_pk_fma_f32 v[52:53], v[54:55], %1, v[52:53] op_sel:[1,1,0] op_sel_hi:[1,0,1] neg_lo:[0,1,0]\n" \
  "v_pk_mul_f32 v[50:51], v[48:49], %1 op_sel_hi:[0,1]\nv_pk_mul_f32 v[54:55], v[52:53], %1 op_sel_hi:[0,1]\n" \
  "v_pk_fma_f32 v[50:51], v[48:49], %1, v[50:51] op_sel:[1,1,0] op_sel_hi:[1,0,1] neg_lo:[0,1,0]\n" \
  "v_pk_fma_f32 v[54:55], v[52:53], %1, v[54:55] op_sel:[1,1,0] op_sel_hi:[1,0,1] neg_lo:[0,1,0]\nv_cvt_pk_f16_f32 v59, v48, v50\n" \
  "v_cvt_pk_f16_f32 v63, v49, v51\nv_cvt_pk_f16_f32 v65, v52, v54\nv_cvt_pk_f16_f32 v69, v53, v55\n" \
  "v_pk_mul_f32 v[52:53], v[54:55], %1 op_sel_hi:[0,1]\nds_write_b128 %3, v[56:59] offset:32\n" \
  "v_pk_fma_f32 v[52:53], v[54:55], %1, v[52:53] op_sel:[1,1,0] op_sel_hi:[1,0,1] neg_lo:[0,1,0]\nds_write_b128 %3, v[60:63] offset:96\n" \
  "v_pk_mul_f32 v[54:55], v[52:53], %1 op_sel_hi:[0,1]\n" \
  "v_pk_fma_f32 v[54:55], v[52:53], %1, v[54:55] op_sel:[1,1,0] op_sel_hi:[1,0,1] neg_lo:[0,1,0]\nv_cvt_pk_f16_f32 v66, v52, v54\n" \
  "v_cvt_pk_f16_f32 v70, v53, v55\nv_pk_mul_f32 v[52:53], v[54:55], %1 op_sel_hi:[0,1]\n" \
  "v_pk_fma_f32 v[52:53], v[54:55], %1, v[52:53] op_sel:[1,1,0] op_sel_hi:[1,0,1] neg_lo:[0,1,0]\n" \
  "v_pk_mul_f32 v[54:55], v[52:53], %1 op_sel_hi:[0,1]\n" \
  "v_pk_fma_f32 v[54:55], v[52:53], %1, v[54:55] op_sel:[1,1,0] op_sel_hi:[1,0,1] neg_lo:[0,1,0]\nv_cvt_pk_f16_f32 v67, v52, v54\n" \
  "v_cvt_pk_f16_f32 v71, v53, v55\nds_write_b128 %3, v[64:67] offset:48\nds_write_b128 %3, v[68:71] offset:112\n"
#define Y_CLOB "memory", "v40", "v41", "v42", "v43", "v44", "v45", "v46", "v47", "v48", "v49", "v50", "v51", "v52", "v53", "v54", "v55", "v56", "v57", "v58", "v59", "v60", "v61", "v62", "v63", "v64", "v65", "v66", "v67", "v68", "v69", "v70", "v71"
#define T_ASM \
  "ds_read_b128 %4, %8\nds_read_b64_tr_b16 v[80:81], %7 offset:0\nds_read_b64_tr_b16 v[82:83], %7 offset:64\n" \
  "ds_read_b64_tr_b16 v[84:85], %5 offset:0\nds_read_b64_tr_b16 v[86:87], %5 offset:48\nds_read_b64_tr_b16 v[88:89], %6 offset:0\n" \
  "ds_read_b64_tr_b16 v[90:91], %6 offset:48\nds_read_b64_tr_b16 v[92:93], %7 offset:3584\nds_read_b64_tr_b16 v[94:95], %7 offset:3648\n" \
  "ds_read_b64_tr_b16 v[96:97], %5 offset:3584\nds_read_b64_tr_b16 v[98:99], %5 offset:3632\nds_read_b64_tr_b16 v[100:101], %6 offset:3584\n" \
  "ds_read_b64_tr_b16 v[102:103], %6 offset:3632\nds_read_b64_tr_b16 v[104:105], %7 offset:7168\nds_read_b64_tr_b16 v[106:107], %7 offset:7232\n" \
  "s_waitcnt lgkmcnt(8)\nv_mfma_f32_16x16x32_f16 %0, v[84:87], v[80:83], 0\nds_read_b64_tr_b16 v[108:109], %5 offset:7168\n" \
  "ds_read_b64_tr_b16 v[110:111], %5 offset:7216\nds_read_b64_tr_b16 v[112:113], %6 offset:7168\nv_mfma_f32_16x16x32_f16 %2, v[88:91], v[80:83], 0\n" \
  "ds_read_b64_tr_b16 v[114:115], %6 offset:7216\nds_read_b64_tr_b16 v[116:117], %7 offset:10752\nds_read_b64_tr_b16 v[118:119], %7 offset:10816\n" \
  "s_waitcnt lgkmcnt(8)\nv_mfma_f32_16x16x32_f16 %0, v[96:99], v[92:95], %0\nds_read_b64_tr_b16 v[120:121], %5 offset:10752\n" \
  "ds_read_b64_tr_b16 v[122:123], %5 offset:10800\nds_read_b64_tr_b16 v[124:125], %6 offset:10752\n" \
  "v_mfma_f32_16x16x32_f16 %2, v[100:103], v[92:95], %2\nds_read_b64_tr_b16 v[126:127], %6 offset:10800\n" \
  "ds_read_b64_tr_b16 v[80:81], %7 offset:14336\nds_read_b64_tr_b16 v[82:83], %7 offset:14400\ns_waitcnt lgkmcnt(8)\n" \
  "v_mfma_f32_16x16x32_f16 %0, v[108:111], v[104:107], %0\nds_read_b64_tr_b16 v[84:85], %5 offset:14336\n" \
  "ds_read_b64_tr_b16 v[86:87], %5 offset:14384\nds_read_b64_tr_b16 v[88:89], %6 offset:14336\n" \
  "v_mfma_f32_16x16x32_f16 %2, v[112:115], v[104:107], %2\nds_read_b64_tr_b16 v[90:91], %6 offset:14384\n" \
  "ds_read_b64_tr_b16 v[92:93], %7 offset:17920\nds_read_b64_tr_b16 v[94:95], %7 offset:17984\ns_waitcnt lgkmcnt(8)\n" \
  "v_mfma_f32_16x16x32_f16 %0, v[120:123], v[116:119], %0\nds_read_b64_tr_b16 v[96:97], %5 offset:17920\n" \
  "ds_read_b64_tr_b16 v[98:99], %5 offset:17968\nds_read_b64_tr_b16 v[100:101], %6 offset:17920\n" \
  "v_mfma_f32_16x16x32_f16 %2, v[124:127], v[116:119], %2\nds_read_b64_tr_b16 v[102:103], %6 offset:17968\n" \
  "ds_read_b64_tr_b16 v[104:105], %7 offset:21504\nds_read_b64_tr_b16 v[106:107], %7 offset:21568\ns_waitcnt lgkmcnt(8)\n" \
  "v_mfma_f32_16x16x32_f16 %1, v[84:87], v[80:83], 0\nds_read_b64_tr_b16 v[108:109], %5 offset:21504\nds_read_b64_tr_b16 v[110:111], %5 offset:21552\n" \
  "ds_read_b64_tr_b16 v[112:113], %6 offset:21504\nv_mfma_f32_16x16x32_f16 %3, v[88:91], v[80:83], 0\nds_read_b64_tr_b16 v[114:115], %6 offset:21552\n" \
  "ds_read_b64_tr_b16 v[116:117], %7 offset:25088\nds_read_b64_tr_b16 v[118:119], %7 offset:25152\ns_waitcnt lgkmcnt(8)\n" \
  "v_mfma_f32_16x16x32_f16 %1, v[96:99], v[92:95], %1\nds_read_b64_tr_b16 v[120:121], %5 offset:25088\n" \
  "ds_read_b64_tr_b16 v[122:123], %5 offset:25136\nds_read_b64_tr_b16 v[124:125], %6 offset:25088\n" \
  "v_mfma_f32_16x16x32_f16 %3, v[100:103], v[92:95], %3\nds_read_b64_tr_b16 v[126:127], %6 offset:25136\ns_waitcnt lgkmcnt(6)\n" \
  "v_mfma_f32_16x16x32_f16 %1, v[108:111], v[104:107], %1\nv_mfma_f32_16x16x32_f16 %3, v[112:115], v[104:107], %3\ns_waitcnt lgkmcnt(0)\n" \
  "v_mfma_f32_16x16x32_f16 %1, v[120:123], v[116:119], %1\nv_mfma_f32_16x16x32_f16 %3, v[124:127], v[116:119], %3\n"
#define T_CLOB "memory", "v80", "v81", "v82", "v83", "v84", "v85", "v86", "v87", "v88", "v89", "v90", "v91", "v92", "v93", "v94", "v95", "v96", "v97", "v98", "v99", "v100", "v101", "v102", "v103", "v104", "v105", "v106", "v107", "v108", "v109", "v110", "v111", "v112", "v113", "v114", "v115", "v116", "v117", "v118", "v119", "v120", "v121", "v122", "v123", "v124", "v125", "v126", "v127"
template <class F> __device__ __forceinline__ int gen_y(float ldt, float are, float aim, float bre, float bim, float cre, float cim, uint32_t im, F&& mid TSP) {
  float ar2, air, g;
  const float dt = state_a(ldt, are, aim, ar2, air, g);
  const float dbr = dt * bre, dbi = dt * bim, pr = cre * dbr - cim * dbi, pi = cre * dbi + cim * dbr;
  const f32x2 w8 = cexp2(ar2, -air, 8.f), w64 = cexp2(ar2, -air, 64.f);
  const float pm = fmaxf(fabsf(pr), fabsf(pi));
  int e = -4;
  if (__builtin_expect(__any(!(pm < 0x1p-4f) || ar2 > 0.f) || !__any(pm > 0x1p-12f), 0)) e = (int)(wave_maxu(__float_as_uint(pm)) >> 23) - 126 + (int)ceilf(g * 250.f);
  const f32x2 y0 = {__builtin_amdgcn_ldexpf(pr, 14 - e), __builtin_amdgcn_ldexpf(-pi, 14 - e)};
  mid();
  TS(0, 7);
  asm volatile(Y_ASM : : "v"(y0), "v"(w8), "v"(w64), "v"(im) : Y_CLOB);
  return e;
}
__device__ __forceinline__ int gen_x(float ldt, float are, float aim, char* im) {
  float ar2, air, g;
  state_a(ldt, are, aim, ar2, air, g);
  const int e = min((int)ceilf(g * 1810.f), 300);
  const float s = __builtin_amdgcn_ldexpf(1.f, 15 - e);
  const f32x2 w1 = cexp2(ar2, air, 1.f), w256 = cexp2(ar2, air, 256.f), w2 = cmul(w1, w1);
  f32x2 a[8], b[8], c[8];
  a[0] = (f32x2){s, 0.f}; a[1] = w1 * s; a[2] = w2 * s;
  b[0] = b[1] = w256 * s;
  b[2] = cmul(b[0], w2); a[3] = cmul(a[1], w2); a[4] = cmul(a[2], w2);
  b[3] = b[4] = b[5] = cmul(b[2], w2); a[5] = cmul(a[3], w2); a[6] = cmul(a[4], w2);
  b[6] = cmul(b[3], w2); a[7] = cmul(a[5], w2); c[0] = cmul(b[3], w256);
  b[7] = cmul(b[6], w2); c[2] = cmul(c[0], w256);
  put_im<0>(im + I_X, 48, a);
  c[1] = cmul(b[7], w256); c[3] = cmul(c[1], w256);
  put_im<0>(im + I_X + 16, 48, b);
#pragma unroll
  for (int k = 4; k < 8; ++k) c[k] = cmul(c[k - 1], w256);
  put_im<0>(im + I_X + 32, 48, c);
  return e;
}

template <int I> struct IC { static constexpr int value = I; };
template <int B, int E, class F> __device__ __forceinline__ void static_for(F&& f) {
  if constexpr (B < E) { f(IC<B>{}); static_for<B + 1, E>(f); }
}
typedef __attribute__((ext_vector_type(4))) short s16x4;
__device__ __forceinline__ u32x2 trd(uint32_t a) {
  return __builtin_bit_cast(u32x2, __builtin_amdgcn_ds_read_tr16_b64_v4i16((__attribute__((address_space(3))) s16x4*)(uintptr_t)a));
}
#define BAR() asm volatile("s_waitcnt lgkmcnt(0)\n\ts_barrier" ::: "memory")
#define ST16(off, v) asm volatile("global_store_dwordx4 %0, %1, %2 sc1\n\ts_nop 1" : : "v"(off), "v"(v), "s"(ws) : "memory")
#define ST4(off, v) asm volatile("global_store_dword %0, %1, %2 sc1" : : "v"(off), "v"(v), "s"(ws) : "memory")
#define LD16(r, off) asm volatile("global_load_dwordx4 %0, %1, %2 sc1" : "=v"(r) : "v"(off), "s"(ws) : "memory")
#define LD16W(r, off) asm volatile("global_load_dwordx4 %0, %1, %2 sc1\n\ts_waitcnt vmcnt(0)" : "=&v"(r) : "v"(off), "s"(ws) : "memory")
#define DRAIN() asm volatile("s_waitcnt vmcnt(0)" ::: "memory")
#define CEX(a, b) { const uint32_t hi_ = max(a, b), lo_ = min(a, b); a = hi_; b = lo_; }
#define MMB(a_, b_, c_) __builtin_amdgcn_mfma_f32_16x16x32_bf16(__builtin_bit_cast(bf16x8, a_), __builtin_bit_cast(bf16x8, b_), c_, 0, 0, 0)
#define NTM 0
#define DPOL ""
#define DPOL2 ""
#define DPOL4 ""
constexpr int SPIN_CAP = 1 << 18;
constexpr uint32_t UMASK = 0x40074007u;
__device__ __forceinline__ uint32_t utag(uint32_t T, int j) {
  return ((T >> (3 * j)) & 0x00070007u) | (((j <= 2) ? (T << (2 - j)) : (T >> (j - 2))) & 0x40004000u);
}
__device__ __forceinline__ uint32_t max4(const u32x4& a) { return max(max(a[0], a[1]), max(a[2], a[3])); }
__device__ __forceinline__ void until(uint32_t t) {
#pragma unroll 1
  for (int spin = 0; spin < 600 && (int)((uint32_t)__builtin_amdgcn_s_memrealtime() - t) < 0; ++spin) __builtin_amdgcn_s_sleep(1);
}
__device__ __forceinline__ uint32_t latest(uint32_t t) {
  const uint32_t t0 = (uint32_t)__builtin_amdgcn_readfirstlane(t) - 0x40000000u;
  return wave_maxu(t - t0) + t0;
}
__device__ __forceinline__ uint32_t gate(const char* ws, uint32_t po, uint32_t T) {
  u32x2 pv;
#pragma unroll 1
  for (int spin = 0; spin < SPIN_CAP; ++spin) {
    asm volatile("global_load_dwordx2 %0, %1, %2 sc1\n\ts_waitcnt vmcnt(0)" : "=&v"(pv) : "v"(po), "s"(ws) : "memory");
    if (!__any(pv[0] != T)) break;
    __builtin_amdgcn_s_sleep(2);
  }
  return pv[1];
}
__device__ __forceinline__ uint32_t gate2(const char* ws, uint32_t oa, uint32_t da, uint32_t ob, uint32_t T) {
  u32x2 pa, pb;
#pragma unroll 1
  for (int spin = 0; spin < SPIN_CAP; ++spin) {
    asm volatile("global_load_dwordx2 %0, %2, %4 sc1\n\tglobal_load_dwordx2 %1, %3, %4 sc1\n\ts_waitcnt vmcnt(0)" : "=&v"(pa), "=&v"(pb) : "v"(oa), "v"(ob), "s"(ws) : "memory");
    if (!__any(pa[0] != T || pb[0] != T)) break;
    __builtin_amdgcn_s_sleep(4);
  }
  pa[1] += da;
  return latest(((int)(pa[1] - pb[1]) > 0) ? pa[1] : pb[1]);
}
__device__ __forceinline__ void gsync(uint32_t a, uint32_t target) {
  uint32_t t, c, n = SPIN_CAP;
  uint64_t ex;
  asm volatile("s_waitcnt lgkmcnt(0)\n\ts_mov_b64 %3, exec\n\ts_mov_b64 exec, 1\n\tv_mov_b32 %0, 1\n\tds_add_u32 %4, %0\n\ts_mov_b64 exec, %3\n"
               "1:\n\ts_sleep 1\n\tds_read_b32 %0, %4\n\ts_sub_u32 %2, %2, 1\n\ts_cbranch_scc1 2f\n\ts_waitcnt lgkmcnt(0)\n\tv_readfirstlane_b32 %1, %0\n\t"
               "s_cmp_lt_u32 %1, %5\n\ts_cbranch_scc1 1b\n2:\n\ts_waitcnt lgkmcnt(0)"
               : "=&v"(t), "=&s"(c), "+s"(n), "=&s"(ex) : "v"(a), "n"(target) : "memory", "scc");
}
__device__ __forceinline__ void gsync0(uint32_t a, uint32_t target) {
  uint32_t t, c, n = SPIN_CAP;
  asm volatile("s_waitcnt lgkmcnt(0)\n\ts_mov_b64 exec, 1\n\tv_mov_b32 %0, 1\n\tds_add_u32 %3, %0\n\ts_mov_b64 exec, -1\n"
               "1:\n\tds_read_b32 %0, %3\n\ts_sub_u32 %2, %2, 1\n\ts_cbranch_scc1 2f\n\ts_waitcnt lgkmcnt(0)\n\tv_readfirstlane_b32 %1, %0\n\t"
               "s_cmp_lt_u32 %1, %4\n\ts_cbranch_scc1 1b\n2:\n\ts_waitcnt lgkmcnt(0)"
               : "=&v"(t), "=&s"(c), "+s"(n) : "v"(a), "n"(target) : "memory", "scc");
}
__device__ __forceinline__ void gwait(uint32_t a, uint32_t target) {
  uint32_t t, c, n = SPIN_CAP;
  asm volatile("1:\n\ts_sleep 2\n\tds_read_b32 %0, %3\n\ts_sub_u32 %2, %2, 1\n\ts_cbranch_scc1 2f\n\ts_waitcnt lgkmcnt(0)\n\tv_readfirstlane_b32 %1, %0\n\t"
               "s_cmp_lt_u32 %1, %4\n\ts_cbranch_scc1 1b\n2:\n\ts_waitcnt lgkmcnt(0)"
               : "=&v"(t), "=&s"(c), "+s"(n) : "v"(a), "n"(target) : "memory", "scc");
}
__device__ __forceinline__ void gwait0(uint32_t a, uint32_t target) {
  uint32_t t, c, n = SPIN_CAP;
  asm volatile("1:\n\tds_read_b32 %0, %3\n\ts_sub_u32 %2, %2, 1\n\ts_cbranch_scc1 2f\n\ts_waitcnt lgkmcnt(0)\n\tv_readfirstlane_b32 %1, %0\n\t"
               "s_cmp_lt_u32 %1, %4\n\ts_cbranch_scc1 1b\n2:\n\ts_waitcnt lgkmcnt(0)"
               : "=&v"(t), "=&s"(c), "+s"(n) : "v"(a), "n"(target) : "memory", "scc");
}
__device__ __forceinline__ void dma16(const void* base, uint32_t voff, uint32_t lds) {
  uint32_t keep;
  asm volatile("s_mov_b32 %0, m0\n\ts_mov_b32 m0, %3\n\ts_nop 0\n\tglobal_load_lds_dwordx4 %1, %2" DPOL "\n\ts_mov_b32 m0, %0" : "=&s"(keep) : "v"(voff), "s"(base), "s"(lds) : "memory");
}
__device__ __forceinline__ void dma16d(const void* base, uint32_t voff, uint32_t lds) {
  uint32_t keep;
  asm volatile("s_mov_b32 %0, m0\n\ts_mov_b32 m0, %3\n\ts_nop 0\n\tglobal_load_lds_dwordx4 %1, %2" DPOL2 "\n\ts_mov_b32 m0, %0" : "=&s"(keep) : "v"(voff), "s"(base), "s"(lds) : "memory");
}
__device__ __forceinline__ void nap(int n) {
  for (; n > 15; n -= 15) __builtin_amdgcn_s_sleep(15);
  switch (n) {
    case 1: __builtin_amdgcn_s_sleep(1); break; case 2: __builtin_amdgcn_s_sleep(2); break; case 3: __builtin_amdgcn_s_sleep(3); break;
    case 4: __builtin_amdgcn_s_sleep(4); break; case 5: __builtin_amdgcn_s_sleep(5); break; case 6: __builtin_amdgcn_s_sleep(6); break;
    case 7: __builtin_amdgcn_s_sleep(7); break; case 8: __builtin_amdgcn_s_sleep(8); break; case 9: __builtin_amdgcn_s_sleep(9); break;
    case 10: __builtin_amdgcn_s_sleep(10); break; case 11: __builtin_amdgcn_s_sleep(11); break; case 12: __builtin_amdgcn_s_sleep(12); break;
    case 13: __builtin_amdgcn_s_sleep(13); break; case 14: __builtin_amdgcn_s_sleep(14); break; case 15: __builtin_amdgcn_s_sleep(15); break;
    default: break;
  }
}
#define SLEEP(n) do { if ((n) > 0) __builtin_amdgcn_s_sleep(n); } while (0)
#define USLEEP 3
#define SSLEEP 14
#define PSLEEP 2
#define PPRIO 0
#define LFIRST 1
#define WD 40
#define OSLEEP 12
#define QPRIO 1
#define KPRIO 2
#define GSLEEP 0
#define NSLEEP 0
#define UD 40
#define XD 0
#define RSLEEP 0
#define UPRIO 2
#define VD2 390
#define WDEPTH 4
#define ESLEEP 0
#define SD 58
#define KD 175
#define OD 0
#define KBACK 6
#define DDEPTH 6
#define C3 1
#define XROT 2
#define WSOFF 0
#define KN 2
#define RPF 1
#define LSLEEP 3
#define TSTOP 70
#define HSTOP 110
#define VEARLY 0ull
#define FPR 3
#define SPR 3
#define DPRIO 0
#define QPRIO2 3
#define HW 2
#define RD 55
#define OPOS 1
#define OD2 0
#define NA 12
#define RD2 88
#define ORD1 ((unsigned __int128)0x321087654u << 36 | 0x321087654ull)
#define WDEPTH2 4
#define RQ NA
#define HREV 1
#define QPRIO0 3
#define VPS 1
#define RW 0
#define NPRIO 0
#define XSL 10
#define V2D 22
#define N0 8
#define LPOS 0
#define FORCESLOW 0
#define FORCEFB 0
#define CONV_ASM \
  "s_cmp_eq_u32 s89, 1\ns_cbranch_scc1 1f\ns_cmp_gt_u32 s89, 1\ns_cbranch_scc1 9f\nds_read_b64 v[124:125], v3 offset:576\n" \
  "ds_read_b64 v[126:127], v3 offset:584\nds_read_b64 v[120:121], v3 offset:64\nds_read_b64 v[122:123], v3 offset:72\n" \
  "ds_read_b64 v[116:117], v3 offset:640\nds_read_b64 v[118:119], v3 offset:648\nds_read_b64 v[112:113], v3 offset:128\n" \
  "ds_read_b64 v[114:115], v3 offset:136\nds_read_b64 v[108:109], v3 offset:704\nds_read_b64 v[110:111], v3 offset:712\n" \
  "ds_read_b64 v[104:105], v3 offset:192\nds_read_b64 v[106:107], v3 offset:200\nds_read_b64 v[100:101], v3 offset:768\n" \
  "ds_read_b64 v[102:103], v3 offset:776\nds_read_b64 v[96:97], v3 offset:256\nds_read_b64 v[98:99], v3 offset:264\n" \
  "ds_read_b64 v[92:93], v3 offset:832\nds_read_b64 v[94:95], v3 offset:840\nds_read_b64 v[88:89], v3 offset:320\n" \
  "ds_read_b64 v[90:91], v3 offset:328\nds_read_b64 v[84:85], v3 offset:896\nds_read_b64 v[86:87], v3 offset:904\n" \
  "ds_read_b64 v[80:81], v3 offset:384\nds_read_b64 v[82:83], v3 offset:392\nds_read_b64 v[76:77], v3 offset:960\n" \
  "ds_read_b64 v[78:79], v3 offset:968\nds_read_b64 v[72:73], v3 offset:448\nds_read_b64 v[74:75], v3 offset:456\n" \
  "ds_read_b64 v[68:69], v3 offset:1024\nds_read_b64 v[70:71], v3 offset:1032\nds_read_b64 v[64:65], v3 offset:512\n" \
  "ds_read_b64 v[66:67], v3 offset:520\nds_read_b64 v[60:61], v3 offset:0\nds_read_b64 v[62:63], v3 offset:8\nds_read_b64 v[56:57], v3 offset:1088\n" \
  "ds_read_b64 v[58:59], v3 offset:1096\ns_waitcnt lgkmcnt(0)\ns_barrier\nds_read_b128 v[36:39], v2 offset:0\nds_read_b128 v[40:43], v2 offset:64\n" \
  "ds_read_b128 v[44:47], v2 offset:128\nds_read_b128 v[48:51], v2 offset:192\ns_waitcnt lgkmcnt(3)\n" \
  "v_mfma_f32_16x16x32_f16 v[12:15], v[124:127], v[36:39], 0\ns_setprio 1\nds_read_b128 v[52:55], v2 offset:256\n" \
  "v_mfma_f32_16x16x32_f16 v[16:19], v[120:123], v[36:39], 0\ns_waitcnt lgkmcnt(3)\nv_mfma_f32_16x16x32_f16 v[12:15], v[116:119], v[40:43], v[12:15]\n" \
  "ds_read_b128 v[36:39], v2 offset:320\nv_mfma_f32_16x16x32_f16 v[16:19], v[112:115], v[40:43], v[16:19]\ns_waitcnt lgkmcnt(3)\n" \
  "v_mfma_f32_16x16x32_f16 v[12:15], v[108:111], v[44:47], v[12:15]\nds_read_b128 v[40:43], v2 offset:384\n" \
  "v_mfma_f32_16x16x32_f16 v[16:19], v[104:107], v[44:47], v[16:19]\ns_waitcnt lgkmcnt(3)\n" \
  "v_mfma_f32_16x16x32_f16 v[12:15], v[100:103], v[48:51], v[12:15]\nds_read_b128 v[44:47], v2 offset:448\n" \
  "v_mfma_f32_16x16x32_f16 v[16:19], v[96:99], v[48:51], v[16:19]\ns_waitcnt lgkmcnt(3)\n" \
  "v_mfma_f32_16x16x32_f16 v[12:15], v[92:95], v[52:55], v[12:15]\nv_mfma_f32_16x16x32_f16 v[16:19], v[88:91], v[52:55], v[16:19]\n" \
  "s_waitcnt lgkmcnt(2)\nv_mfma_f32_16x16x32_f16 v[12:15], v[84:87], v[36:39], v[12:15]\nds_read_b128 v[48:51], v2 offset:512\n" \
  "v_mfma_f32_16x16x32_f16 v[16:19], v[80:83], v[36:39], v[16:19]\ns_waitcnt lgkmcnt(2)\n" \
  "v_mfma_f32_16x16x32_f16 v[12:15], v[76:79], v[40:43], v[12:15]\nds_read_b128 v[52:55], v2 offset:576\n" \
  "v_mfma_f32_16x16x32_f16 v[16:19], v[72:75], v[40:43], v[16:19]\ns_waitcnt lgkmcnt(2)\n" \
  "v_mfma_f32_16x16x32_f16 v[12:15], v[68:71], v[44:47], v[12:15]\nds_read_b128 v[36:39], v2 offset:640\n" \
  "v_mfma_f32_16x16x32_f16 v[16:19], v[64:67], v[44:47], v[16:19]\nv_mfma_f32_16x16x32_f16 v[20:23], v[60:63], v[44:47], 0\n" \
  "ds_read_b128 v[40:43], v2 offset:704\nv_mfma_f32_16x16x32_f16 v[28:31], v[56:59], v[44:47], 0\ns_waitcnt lgkmcnt(3)\n" \
  "v_mfma_f32_16x16x32_f16 v[16:19], v[124:127], v[48:51], v[16:19]\nds_read_b128 v[56:59], v2 offset:768\nv_cvt_pkrtz_f16_f32 v32, v12, v13\n" \
  "v_mfma_f32_16x16x32_f16 v[20:23], v[120:123], v[48:51], v[20:23]\nv_cvt_pkrtz_f16_f32 v33, v14, v15\ns_waitcnt lgkmcnt(3)\n" \
  "v_mfma_f32_16x16x32_f16 v[16:19], v[116:119], v[52:55], v[16:19]\nds_read_b128 v[44:47], v2 offset:832\n" \
  "v_pk_add_u16 v32, v32, 4 op_sel_hi:[1,0] clamp\nv_mfma_f32_16x16x32_f16 v[20:23], v[112:115], v[52:55], v[20:23]\n" \
  "v_pk_add_u16 v33, v33, 4 op_sel_hi:[1,0] clamp\ns_waitcnt lgkmcnt(3)\nv_mfma_f32_16x16x32_f16 v[16:19], v[108:111], v[36:39], v[16:19]\n" \
  "ds_read_b128 v[48:51], v2 offset:896\nds_write_b64 v4, v[32:33] offset:0\nv_mfma_f32_16x16x32_f16 v[20:23], v[104:107], v[36:39], v[20:23]\n" \
  "s_waitcnt lgkmcnt(3)\nv_mfma_f32_16x16x32_f16 v[16:19], v[100:103], v[40:43], v[16:19]\nds_read_b128 v[52:55], v2 offset:960\n" \
  "v_mfma_f32_16x16x32_f16 v[20:23], v[96:99], v[40:43], v[20:23]\nv_mfma_f32_16x16x32_f16 v[16:19], v[92:95], v[56:59], v[16:19]\n" \
  "v_mfma_f32_16x16x32_f16 v[20:23], v[88:91], v[56:59], v[20:23]\nds_read_b128 v[36:39], v2 offset:1024\ns_waitcnt lgkmcnt(3)\n" \
  "v_mfma_f32_16x16x32_f16 v[16:19], v[84:87], v[44:47], v[16:19]\nv_mfma_f32_16x16x32_f16 v[20:23], v[80:83], v[44:47], v[20:23]\n" \
  "ds_read_b128 v[40:43], v2 offset:1088\nds_read_b64 v[56:57], v6 offset:256\nds_read_b64 v[58:59], v6 offset:264\n" \
  "v_mfma_f32_16x16x32_f16 v[16:19], v[76:79], v[48:51], v[16:19]\nv_mfma_f32_16x16x32_f16 v[20:23], v[72:75], v[48:51], v[20:23]\n" \
  "ds_read_b128 v[44:47], v2 offset:1152\ns_waitcnt lgkmcnt(4)\nv_mfma_f32_16x16x32_f16 v[16:19], v[68:71], v[52:55], v[16:19]\n" \
  "v_mfma_f32_16x16x32_f16 v[20:23], v[64:67], v[52:55], v[20:23]\nds_read_b128 v[48:51], v2 offset:1216\n" \
  "v_mfma_f32_16x16x32_f16 v[24:27], v[60:63], v[52:55], 0\nv_mfma_f32_16x16x32_f16 v[20:23], v[124:127], v[36:39], v[20:23]\n" \
  "ds_read_b128 v[52:55], v2 offset:1280\nv_mfma_f32_16x16x32_f16 v[24:27], v[120:123], v[36:39], v[24:27]\ns_waitcnt lgkmcnt(5)\n" \
  "v_mfma_f32_16x16x32_f16 v[20:23], v[116:119], v[40:43], v[20:23]\nds_read_b128 v[36:39], v2 offset:1344\nv_cvt_pkrtz_f16_f32 v32, v16, v17\n" \
  "v_mfma_f32_16x16x32_f16 v[24:27], v[112:115], v[40:43], v[24:27]\nv_cvt_pkrtz_f16_f32 v33, v18, v19\ns_waitcnt lgkmcnt(3)\n" \
  "v_mfma_f32_16x16x32_f16 v[20:23], v[108:111], v[44:47], v[20:23]\nds_read_b128 v[40:43], v2 offset:1408\n" \
  "v_pk_add_u16 v32, v32, 4 op_sel_hi:[1,0] clamp\nds_read_b32 v35, v9 offset:32\nv_mfma_f32_16x16x32_f16 v[24:27], v[104:107], v[44:47], v[24:27]\n" \
  "v_pk_add_u16 v33, v33, 4 op_sel_hi:[1,0] clamp\ns_waitcnt lgkmcnt(3)\nv_mfma_f32_16x16x32_f16 v[20:23], v[100:103], v[48:51], v[20:23]\n" \
  "ds_read_b128 v[44:47], v2 offset:1472\nds_write_b64 v4, v[32:33] offset:1024\nv_mfma_f32_16x16x32_f16 v[24:27], v[96:99], v[48:51], v[24:27]\n" \
  "v_mfma_f32_16x16x32_f16 v[20:23], v[92:95], v[52:55], v[20:23]\nv_mfma_f32_16x16x32_f16 v[24:27], v[88:91], v[52:55], v[24:27]\n" \
  "ds_read_b128 v[48:51], v2 offset:1536\ns_waitcnt lgkmcnt(3)\nv_readfirstlane_b32 s85, v35\ns_cmp_lt_u32 s85, 4\ns_cbranch_scc1 21f\n22:\n" \
  "v_mfma_f32_16x16x32_f16 v[20:23], v[84:87], v[36:39], v[20:23]\nv_mfma_f32_16x16x32_f16 v[24:27], v[80:83], v[36:39], v[24:27]\n" \
  "ds_read_b128 v[52:55], v2 offset:1600\nv_mfma_f32_16x16x32_f16 v[20:23], v[76:79], v[40:43], v[20:23]\n" \
  "v_mfma_f32_16x16x32_f16 v[24:27], v[72:75], v[40:43], v[24:27]\nds_read_b128 v[36:39], v2 offset:1664\ns_waitcnt lgkmcnt(4)\n" \
  "v_mfma_f32_16x16x32_f16 v[20:23], v[68:71], v[44:47], v[20:23]\nv_mfma_f32_16x16x32_f16 v[24:27], v[64:67], v[44:47], v[24:27]\n" \
  "ds_read_b128 v[40:43], v2 offset:1728\nv_mfma_f32_16x16x32_f16 v[28:31], v[60:63], v[44:47], v[28:31]\nds_read_b64_tr_b16 v[60:61], v5 offset:0\n" \
  "ds_read_b64_tr_b16 v[62:63], v5 offset:32\ns_waitcnt lgkmcnt(4)\nv_mfma_f32_16x16x32_f16 v[24:27], v[124:127], v[48:51], v[24:27]\n" \
  "v_mfma_f32_16x16x32_f16 v[28:31], v[120:123], v[48:51], v[28:31]\nds_read_b64_tr_b16 v[44:45], v5 offset:256\n" \
  "ds_read_b64_tr_b16 v[46:47], v5 offset:288\nv_mfma_f32_16x16x32_f16 v[24:27], v[116:119], v[52:55], v[24:27]\n" \
  "ds_read_b64 v[120:121], v6 offset:320\nds_read_b64 v[122:123], v6 offset:328\nv_mfma_f32_16x16x32_f16 v[28:31], v[112:115], v[52:55], v[28:31]\n" \
  "ds_read_b128 v[48:51], v2 offset:1792\ns_waitcnt lgkmcnt(7)\nv_mfma_f32_16x16x32_f16 v[24:27], v[108:111], v[36:39], v[24:27]\n" \
  "ds_read_b128 v[112:115], v2 offset:1856\nv_mfma_f32_16x16x32_f16 v[28:31], v[104:107], v[36:39], v[28:31]\nds_read_b64 v[52:53], v6 offset:128\n" \
  "ds_read_b64 v[54:55], v6 offset:136\nv_mfma_f32_16x16x32_f16 v[24:27], v[100:103], v[40:43], v[24:27]\nds_read_b128 v[104:107], v2 offset:1920\n" \
  "v_mfma_f32_16x16x32_f16 v[28:31], v[96:99], v[40:43], v[28:31]\nds_read_b64 v[36:37], v6 offset:192\nds_read_b64 v[38:39], v6 offset:200\n" \
  "s_waitcnt lgkmcnt(9)\nv_mfma_f32_16x16x32_f16 v[20:23], v[60:63], v[56:59], v[20:23]\nds_read_b128 v[96:99], v2 offset:1984\ns_waitcnt lgkmcnt(7)\n" \
  "v_mfma_f32_16x16x32_f16 v[20:23], v[44:47], v[120:123], v[20:23]\nv_mfma_f32_16x16x32_f16 v[24:27], v[92:95], v[48:51], v[24:27]\n" \
  "ds_read_b128 v[40:43], v2 offset:2048\nv_mfma_f32_16x16x32_f16 v[28:31], v[88:91], v[48:51], v[28:31]\nds_read_b64_tr_b16 v[88:89], v5 offset:512\n" \
  "ds_read_b64_tr_b16 v[90:91], v5 offset:544\ns_waitcnt lgkmcnt(6)\nv_mfma_f32_16x16x32_f16 v[24:27], v[84:87], v[112:115], v[24:27]\n" \
  "v_mfma_f32_16x16x32_f16 v[28:31], v[80:83], v[112:115], v[28:31]\nv_cvt_pkrtz_f16_f32 v32, v20, v21\nv_cvt_pkrtz_f16_f32 v33, v22, v23\n" \
  "v_mfma_f32_16x16x32_f16 v[24:27], v[76:79], v[104:107], v[24:27]\nds_read_b64_tr_b16 v[48:49], v5 offset:768\n" \
  "ds_read_b64_tr_b16 v[50:51], v5 offset:800\nv_mfma_f32_16x16x32_f16 v[28:31], v[72:75], v[104:107], v[28:31]\n" \
  "ds_read_b128 v[80:83], v2 offset:2112\ns_waitcnt lgkmcnt(5)\nv_mfma_f32_16x16x32_f16 v[24:27], v[68:71], v[96:99], v[24:27]\n" \
  "ds_read_b128 v[112:115], v2 offset:2176\nv_pk_add_u16 v32, v32, 4 op_sel_hi:[1,0] clamp\n" \
  "v_mfma_f32_16x16x32_f16 v[28:31], v[64:67], v[96:99], v[28:31]\nds_read_b128 v[72:75], v2 offset:2240\n" \
  "v_pk_add_u16 v33, v33, 4 op_sel_hi:[1,0] clamp\nv_mfma_f32_16x16x32_f16 v[28:31], v[124:127], v[40:43], v[28:31]\n" \
  "ds_read_b128 v[104:107], v2 offset:2304\nds_write_b64 v4, v[32:33] offset:2048\nv_mfma_f32_16x16x32_f16 v[24:27], v[60:63], v[52:55], v[24:27]\n" \
  "ds_read_b128 v[64:67], v2 offset:2368\nds_read_b64 v[96:97], v6 offset:0\nds_read_b64 v[98:99], v6 offset:8\n" \
  "v_mfma_f32_16x16x32_f16 v[24:27], v[44:47], v[36:39], v[24:27]\nds_read_b128 v[124:127], v2 offset:2432\ns_waitcnt lgkmcnt(9)\n" \
  "v_mfma_f32_16x16x32_f16 v[24:27], v[88:91], v[56:59], v[24:27]\nds_read_b128 v[40:43], v2 offset:2496\n" \
  "v_mfma_f32_16x16x32_f16 v[24:27], v[48:51], v[120:123], v[24:27]\ns_waitcnt lgkmcnt(8)\n" \
  "v_mfma_f32_16x16x32_f16 v[28:31], v[116:119], v[80:83], v[28:31]\nds_read_b64 v[116:117], v6 offset:64\nds_read_b64 v[118:119], v6 offset:72\n" \
  "v_mfma_f32_16x16x32_f16 v[28:31], v[108:111], v[112:115], v[28:31]\nds_read_b64_tr_b16 v[80:81], v5 offset:1024\n" \
  "ds_read_b64_tr_b16 v[82:83], v5 offset:1056\ns_waitcnt lgkmcnt(10)\nv_mfma_f32_16x16x32_f16 v[28:31], v[100:103], v[72:75], v[28:31]\n" \
  "v_cvt_pkrtz_f16_f32 v32, v24, v25\nv_cvt_pkrtz_f16_f32 v33, v26, v27\nv_mfma_f32_16x16x32_f16 v[28:31], v[92:95], v[104:107], v[28:31]\n" \
  "v_pk_add_u16 v32, v32, 4 op_sel_hi:[1,0] clamp\ns_waitcnt lgkmcnt(5)\nv_mfma_f32_16x16x32_f16 v[28:31], v[84:87], v[64:67], v[28:31]\n" \
  "ds_read_b64_tr_b16 v[108:109], v5 offset:1280\nds_read_b64_tr_b16 v[110:111], v5 offset:1312\n" \
  "v_mfma_f32_16x16x32_f16 v[28:31], v[76:79], v[124:127], v[28:31]\nds_read_b64_tr_b16 v[112:113], v7 offset:0\n" \
  "ds_read_b64_tr_b16 v[114:115], v7 offset:64\ns_waitcnt lgkmcnt(6)\nv_mfma_f32_16x16x32_f16 v[28:31], v[68:71], v[40:43], v[28:31]\n" \
  "ds_read2_b32 v[100:101], v8 offset0:48 offset1:49\nds_read2_b32 v[102:103], v8 offset0:50 offset1:51\n" \
  "v_mfma_f32_16x16x32_f16 v[28:31], v[60:63], v[96:99], v[28:31]\nv_pk_add_u16 v33, v33, 4 op_sel_hi:[1,0] clamp\n" \
  "ds_write_b64 v4, v[32:33] offset:3072\nv_mfma_f32_16x16x32_f16 v[28:31], v[44:47], v[116:119], v[28:31]\n" \
  "v_mfma_f32_16x16x32_f16 v[28:31], v[88:91], v[52:55], v[28:31]\nv_mfma_f32_16x16x32_f16 v[28:31], v[48:51], v[36:39], v[28:31]\n" \
  "s_waitcnt lgkmcnt(5)\nv_mfma_f32_16x16x32_f16 v[28:31], v[80:83], v[56:59], v[28:31]\n" \
  "v_mfma_f32_16x16x32_f16 v[28:31], v[108:111], v[120:123], v[28:31]\ns_waitcnt lgkmcnt(1)\n" \
  "v_mfma_f32_16x16x32_f16 v[28:31], v[112:115], v[100:103], v[28:31]\ns_nop 7\nv_cvt_pkrtz_f16_f32 v32, v28, v29\nv_cvt_pkrtz_f16_f32 v33, v30, v31\n" \
  "v_pk_add_u16 v32, v32, 4 op_sel_hi:[1,0] clamp\nv_pk_add_u16 v33, v33, 4 op_sel_hi:[1,0] clamp\nds_write_b64 v4, v[32:33] offset:4096\n" \
  "s_setprio 0\ns_setpc_b64 s[90:91]\n21:\ns_movk_i32 s84, 0x4000\n23:\nds_read_b32 v35, v9 offset:32\ns_sub_u32 s84, s84, 1\ns_cbranch_scc1 22b\n" \
  "s_waitcnt lgkmcnt(0)\nv_readfirstlane_b32 s85, v35\ns_cmp_lt_u32 s85, 4\ns_cbranch_scc1 23b\ns_branch 22b\n1:\n" \
  "ds_read_b64 v[124:125], v3 offset:1088\nds_read_b64 v[126:127], v3 offset:1096\nds_read_b64 v[120:121], v3 offset:0\n" \
  "ds_read_b64 v[122:123], v3 offset:8\nds_read_b64 v[116:117], v3 offset:64\nds_read_b64 v[118:119], v3 offset:72\n" \
  "ds_read_b64 v[112:113], v3 offset:128\nds_read_b64 v[114:115], v3 offset:136\nds_read_b64 v[108:109], v3 offset:192\n" \
  "ds_read_b64 v[110:111], v3 offset:200\nds_read_b64 v[104:105], v3 offset:256\nds_read_b64 v[106:107], v3 offset:264\n" \
  "ds_read_b64 v[100:101], v3 offset:320\nds_read_b64 v[102:103], v3 offset:328\nds_read_b64 v[96:97], v3 offset:384\n" \
  "ds_read_b64 v[98:99], v3 offset:392\nds_read_b64 v[92:93], v3 offset:448\nds_read_b64 v[94:95], v3 offset:456\n" \
  "ds_read_b64 v[88:89], v3 offset:512\nds_read_b64 v[90:91], v3 offset:520\nds_read_b64 v[84:85], v3 offset:576\n" \
  "ds_read_b64 v[86:87], v3 offset:584\nds_read_b64 v[80:81], v3 offset:640\nds_read_b64 v[82:83], v3 offset:648\n" \
  "ds_read_b64 v[76:77], v3 offset:704\nds_read_b64 v[78:79], v3 offset:712\nds_read_b64 v[72:73], v3 offset:768\n" \
  "ds_read_b64 v[74:75], v3 offset:776\nds_read_b64 v[68:69], v3 offset:832\nds_read_b64 v[70:71], v3 offset:840\n" \
  "ds_read_b64 v[64:65], v3 offset:896\nds_read_b64 v[66:67], v3 offset:904\nds_read_b64 v[60:61], v3 offset:960\n" \
  "ds_read_b64 v[62:63], v3 offset:968\nds_read_b64 v[56:57], v3 offset:1024\nds_read_b64 v[58:59], v3 offset:1032\n" \
  "ds_read_b64 v[52:53], v6 offset:0\nds_read_b64 v[54:55], v6 offset:8\nds_read_b64 v[48:49], v6 offset:64\nds_read_b64 v[50:51], v6 offset:72\n" \
  "s_waitcnt lgkmcnt(0)\ns_barrier\nds_read_b128 v[28:31], v2 offset:960\nds_read_b128 v[32:35], v2 offset:1472\n" \
  "ds_read_b128 v[36:39], v2 offset:1984\nds_read_b128 v[40:43], v2 offset:2048\nds_read_b128 v[44:47], v2 offset:2112\ns_waitcnt lgkmcnt(3)\n" \
  "v_mfma_f32_16x16x32_f16 v[12:15], v[124:127], v[28:31], 0\ns_setprio 1\nds_read_b128 v[28:31], v2 offset:2176\n" \
  "v_mfma_f32_16x16x32_f16 v[16:19], v[124:127], v[32:35], 0\nds_read_b128 v[32:35], v2 offset:2240\ns_waitcnt lgkmcnt(3)\n" \
  "v_mfma_f32_16x16x32_f16 v[12:15], v[120:123], v[36:39], v[12:15]\nv_mfma_f32_16x16x32_f16 v[20:23], v[124:127], v[36:39], 0\n" \
  "ds_read_b128 v[124:127], v2 offset:2304\nds_read_b128 v[36:39], v2 offset:2368\nv_mfma_f32_16x16x32_f16 v[12:15], v[116:119], v[40:43], v[12:15]\n" \
  "ds_read_b128 v[40:43], v2 offset:2432\ns_waitcnt lgkmcnt(4)\nv_mfma_f32_16x16x32_f16 v[12:15], v[112:115], v[44:47], v[12:15]\n" \
  "ds_read_b128 v[44:47], v2 offset:2496\nv_mfma_f32_16x16x32_f16 v[12:15], v[108:111], v[28:31], v[12:15]\nds_read_b128 v[28:31], v2 offset:2560\n" \
  "s_waitcnt lgkmcnt(4)\nv_mfma_f32_16x16x32_f16 v[12:15], v[104:107], v[32:35], v[12:15]\nds_read_b128 v[32:35], v2 offset:2624\n" \
  "v_mfma_f32_16x16x32_f16 v[12:15], v[100:103], v[124:127], v[12:15]\ns_waitcnt lgkmcnt(3)\n" \
  "v_mfma_f32_16x16x32_f16 v[12:15], v[96:99], v[36:39], v[12:15]\nds_read_b128 v[124:127], v2 offset:2688\n" \
  "v_mfma_f32_16x16x32_f16 v[12:15], v[92:95], v[40:43], v[12:15]\ns_waitcnt lgkmcnt(3)\n" \
  "v_mfma_f32_16x16x32_f16 v[12:15], v[88:91], v[44:47], v[12:15]\nds_read_b128 v[36:39], v2 offset:2752\n" \
  "v_mfma_f32_16x16x32_f16 v[16:19], v[120:123], v[44:47], v[16:19]\ns_waitcnt lgkmcnt(3)\n" \
  "v_mfma_f32_16x16x32_f16 v[12:15], v[84:87], v[28:31], v[12:15]\nds_read_b128 v[40:43], v2 offset:2816\n" \
  "v_mfma_f32_16x16x32_f16 v[16:19], v[116:119], v[28:31], v[16:19]\ns_waitcnt lgkmcnt(3)\n" \
  "v_mfma_f32_16x16x32_f16 v[12:15], v[80:83], v[32:35], v[12:15]\nds_read_b128 v[44:47], v2 offset:2880\n" \
  "v_mfma_f32_16x16x32_f16 v[16:19], v[112:115], v[32:35], v[16:19]\ns_waitcnt lgkmcnt(3)\n" \
  "v_mfma_f32_16x16x32_f16 v[12:15], v[76:79], v[124:127], v[12:15]\nds_read_b128 v[28:31], v2 offset:2944\n" \
  "v_mfma_f32_16x16x32_f16 v[16:19], v[108:111], v[124:127], v[16:19]\ns_waitcnt lgkmcnt(3)\n" \
  "v_mfma_f32_16x16x32_f16 v[12:15], v[72:75], v[36:39], v[12:15]\nds_read_b128 v[32:35], v2 offset:3008\n" \
  "v_mfma_f32_16x16x32_f16 v[16:19], v[104:107], v[36:39], v[16:19]\ns_waitcnt lgkmcnt(3)\n" \
  "v_mfma_f32_16x16x32_f16 v[12:15], v[68:71], v[40:43], v[12:15]\nv_mfma_f32_16x16x32_f16 v[16:19], v[100:103], v[40:43], v[16:19]\n" \
  "ds_read_b128 v[124:127], v2 offset:3072\ns_waitcnt lgkmcnt(3)\nv_mfma_f32_16x16x32_f16 v[12:15], v[64:67], v[44:47], v[12:15]\n" \
  "v_mfma_f32_16x16x32_f16 v[16:19], v[96:99], v[44:47], v[16:19]\nds_read_b128 v[36:39], v2 offset:3136\ns_waitcnt lgkmcnt(3)\n" \
  "v_mfma_f32_16x16x32_f16 v[12:15], v[60:63], v[28:31], v[12:15]\nds_read_b64 v[40:41], v6 offset:128\nds_read_b64 v[42:43], v6 offset:136\n" \
  "v_mfma_f32_16x16x32_f16 v[16:19], v[92:95], v[28:31], v[16:19]\nds_read_b128 v[44:47], v2 offset:3200\ns_waitcnt lgkmcnt(4)\n" \
  "v_mfma_f32_16x16x32_f16 v[12:15], v[56:59], v[32:35], v[12:15]\nv_mfma_f32_16x16x32_f16 v[16:19], v[88:91], v[32:35], v[16:19]\n" \
  "ds_read_b128 v[28:31], v2 offset:3264\nv_mfma_f32_16x16x32_f16 v[20:23], v[120:123], v[32:35], v[20:23]\nds_read_b64 v[120:121], v6 offset:192\n" \
  "ds_read_b64 v[122:123], v6 offset:200\nv_mfma_f32_16x16x32_f16 v[16:19], v[84:87], v[124:127], v[16:19]\nds_read_b128 v[32:35], v2 offset:3328\n" \
  "v_mfma_f32_16x16x32_f16 v[20:23], v[116:119], v[124:127], v[20:23]\nds_read_b64 v[116:117], v6 offset:256\nds_read_b64 v[118:119], v6 offset:264\n" \
  "s_waitcnt lgkmcnt(6)\nv_mfma_f32_16x16x32_f16 v[16:19], v[80:83], v[36:39], v[16:19]\nds_read_b128 v[124:127], v2 offset:3392\n" \
  "v_mfma_f32_16x16x32_f16 v[20:23], v[112:115], v[36:39], v[20:23]\nds_read_b64 v[112:113], v6 offset:320\nds_read_b64 v[114:115], v6 offset:328\n" \
  "v_mfma_f32_16x16x32_f16 v[16:19], v[76:79], v[44:47], v[16:19]\nds_read_b128 v[36:39], v2 offset:3456\n" \
  "v_mfma_f32_16x16x32_f16 v[20:23], v[108:111], v[44:47], v[20:23]\ns_waitcnt lgkmcnt(6)\n" \
  "v_mfma_f32_16x16x32_f16 v[16:19], v[72:75], v[28:31], v[16:19]\nds_read_b128 v[108:111], v2 offset:3520\n" \
  "v_mfma_f32_16x16x32_f16 v[20:23], v[104:107], v[28:31], v[20:23]\nv_mfma_f32_16x16x32_f16 v[16:19], v[68:71], v[32:35], v[16:19]\n" \
  "ds_read_b128 v[44:47], v2 offset:3584\nv_mfma_f32_16x16x32_f16 v[20:23], v[100:103], v[32:35], v[20:23]\nds_read_b128 v[104:107], v2 offset:3648\n" \
  "ds_read_b32 v27, v9 offset:32\ns_waitcnt lgkmcnt(4)\nv_mfma_f32_16x16x32_f16 v[16:19], v[64:67], v[124:127], v[16:19]\n" \
  "ds_read_b128 v[28:31], v2 offset:3712\nv_mfma_f32_16x16x32_f16 v[20:23], v[96:99], v[124:127], v[20:23]\nds_read_b128 v[100:103], v2 offset:3776\n" \
  "ds_read2_b32 v[32:33], v8 offset0:32 offset1:33\nds_read2_b32 v[34:35], v8 offset0:34 offset1:35\n" \
  "v_mfma_f32_16x16x32_f16 v[16:19], v[60:63], v[36:39], v[16:19]\nds_read_b128 v[96:99], v2 offset:3840\n" \
  "v_mfma_f32_16x16x32_f16 v[20:23], v[92:95], v[36:39], v[20:23]\nds_read_b128 v[124:127], v2 offset:3904\ns_waitcnt lgkmcnt(8)\n" \
  "v_mfma_f32_16x16x32_f16 v[16:19], v[56:59], v[108:111], v[16:19]\nds_read_b128 v[92:95], v2 offset:3968\ns_waitcnt lgkmcnt(7)\n" \
  "v_readfirstlane_b32 s85, v27\ns_cmp_lt_u32 s85, 4\ns_cbranch_scc1 31f\n32:\nv_mfma_f32_16x16x32_f16 v[20:23], v[88:91], v[108:111], v[20:23]\n" \
  "ds_read_b128 v[36:39], v2 offset:4032\nds_read_b64_tr_b16 v[88:89], v5 offset:512\nds_read_b64_tr_b16 v[90:91], v5 offset:544\n" \
  "v_mfma_f32_16x16x32_f16 v[20:23], v[84:87], v[44:47], v[20:23]\nds_read_b64_tr_b16 v[108:109], v5 offset:768\n" \
  "ds_read_b64_tr_b16 v[110:111], v5 offset:800\nv_mfma_f32_16x16x32_f16 v[20:23], v[80:83], v[104:107], v[20:23]\n" \
  "ds_read_b64_tr_b16 v[84:85], v5 offset:1024\nds_read_b64_tr_b16 v[86:87], v5 offset:1056\ns_waitcnt lgkmcnt(12)\n" \
  "v_mfma_f32_16x16x32_f16 v[20:23], v[76:79], v[28:31], v[20:23]\nds_read_b64_tr_b16 v[44:45], v5 offset:1280\n" \
  "ds_read_b64_tr_b16 v[46:47], v5 offset:1312\nv_mfma_f32_16x16x32_f16 v[20:23], v[72:75], v[100:103], v[20:23]\ns_waitcnt lgkmcnt(10)\n" \
  "v_mfma_f32_16x16x32_f16 v[20:23], v[68:71], v[96:99], v[20:23]\nds_read_b64_tr_b16 v[80:81], v5 offset:1536\n" \
  "ds_read_b64_tr_b16 v[82:83], v5 offset:1568\nv_mfma_f32_16x16x32_f16 v[20:23], v[64:67], v[124:127], v[20:23]\n" \
  "ds_read_b64_tr_b16 v[104:105], v5 offset:1792\nds_read_b64_tr_b16 v[106:107], v5 offset:1824\ns_waitcnt lgkmcnt(12)\n" \
  "v_mfma_f32_16x16x32_f16 v[20:23], v[60:63], v[92:95], v[20:23]\nds_read_b64_tr_b16 v[76:77], v5 offset:2048\n" \
  "ds_read_b64_tr_b16 v[78:79], v5 offset:2080\nv_mfma_f32_16x16x32_f16 v[20:23], v[56:59], v[36:39], v[20:23]\ns_waitcnt lgkmcnt(10)\n" \
  "v_mfma_f32_16x16x32_f16 v[12:15], v[88:91], v[52:55], v[12:15]\nds_read_b64_tr_b16 v[28:29], v5 offset:2304\n" \
  "ds_read_b64_tr_b16 v[30:31], v5 offset:2336\nv_mfma_f32_16x16x32_f16 v[12:15], v[108:111], v[48:51], v[12:15]\n" \
  "ds_read_b64_tr_b16 v[72:73], v5 offset:2560\nds_read_b64_tr_b16 v[74:75], v5 offset:2592\ns_waitcnt lgkmcnt(10)\n" \
  "v_mfma_f32_16x16x32_f16 v[12:15], v[84:87], v[40:43], v[12:15]\nds_read_b64_tr_b16 v[100:101], v5 offset:2816\n" \
  "ds_read_b64_tr_b16 v[102:103], v5 offset:2848\nv_mfma_f32_16x16x32_f16 v[16:19], v[84:87], v[52:55], v[16:19]\n" \
  "ds_read_b64_tr_b16 v[68:69], v7 offset:0\nds_read_b64_tr_b16 v[70:71], v7 offset:64\n" \
  "v_mfma_f32_16x16x32_f16 v[12:15], v[44:47], v[120:123], v[12:15]\nv_mfma_f32_16x16x32_f16 v[16:19], v[44:47], v[48:51], v[16:19]\n" \
  "s_waitcnt lgkmcnt(10)\nv_mfma_f32_16x16x32_f16 v[12:15], v[80:83], v[116:119], v[12:15]\nds_read2_b32 v[96:97], v8 offset0:16 offset1:17\n" \
  "ds_read2_b32 v[98:99], v8 offset0:18 offset1:19\nv_mfma_f32_16x16x32_f16 v[16:19], v[80:83], v[40:43], v[16:19]\n" \
  "ds_read2_b32 v[64:65], v8 offset0:0 offset1:1\nds_read2_b32 v[66:67], v8 offset0:2 offset1:3\n" \
  "v_mfma_f32_16x16x32_f16 v[20:23], v[80:83], v[52:55], v[20:23]\nv_mfma_f32_16x16x32_f16 v[12:15], v[104:107], v[112:115], v[12:15]\n" \
  "v_mfma_f32_16x16x32_f16 v[16:19], v[104:107], v[120:123], v[16:19]\nv_mfma_f32_16x16x32_f16 v[20:23], v[104:107], v[48:51], v[20:23]\n" \
  "s_waitcnt lgkmcnt(10)\nv_mfma_f32_16x16x32_f16 v[16:19], v[76:79], v[116:119], v[16:19]\nds_read_b64_tr_b16 v[124:125], v7 offset:512\n" \
  "ds_read_b64_tr_b16 v[126:127], v7 offset:576\nv_mfma_f32_16x16x32_f16 v[20:23], v[76:79], v[40:43], v[20:23]\n" \
  "ds_read2_b32 v[60:61], v8 offset0:48 offset1:49\nds_read2_b32 v[62:63], v8 offset0:50 offset1:51\n" \
  "v_mfma_f32_16x16x32_f16 v[16:19], v[28:31], v[112:115], v[16:19]\nv_mfma_f32_16x16x32_f16 v[20:23], v[28:31], v[120:123], v[20:23]\n" \
  "s_waitcnt lgkmcnt(10)\nv_mfma_f32_16x16x32_f16 v[20:23], v[72:75], v[116:119], v[20:23]\nds_read_b64_tr_b16 v[92:93], v7 offset:1024\n" \
  "ds_read_b64_tr_b16 v[94:95], v7 offset:1088\nv_mfma_f32_16x16x32_f16 v[20:23], v[100:103], v[112:115], v[20:23]\n" \
  "ds_read_b64_tr_b16 v[56:57], v7 offset:1536\nds_read_b64_tr_b16 v[58:59], v7 offset:1600\ns_waitcnt lgkmcnt(10)\n" \
  "v_mfma_f32_16x16x32_f16 v[12:15], v[68:71], v[32:35], v[12:15]\nv_mfma_f32_16x16x32_f16 v[16:19], v[68:71], v[96:99], v[16:19]\n" \
  "s_waitcnt lgkmcnt(6)\nv_mfma_f32_16x16x32_f16 v[20:23], v[68:71], v[64:67], v[20:23]\ns_waitcnt lgkmcnt(2)\n" \
  "v_mfma_f32_16x16x32_f16 v[12:15], v[124:127], v[60:63], v[12:15]\nv_mfma_f32_16x16x32_f16 v[16:19], v[124:127], v[32:35], v[16:19]\n" \
  "v_mfma_f32_16x16x32_f16 v[20:23], v[124:127], v[96:99], v[20:23]\nv_mfma_f32_16x16x32_f16 v[16:19], v[92:95], v[60:63], v[16:19]\n" \
  "v_mfma_f32_16x16x32_f16 v[20:23], v[92:95], v[32:35], v[20:23]\ns_waitcnt lgkmcnt(0)\n" \
  "v_mfma_f32_16x16x32_f16 v[20:23], v[56:59], v[60:63], v[20:23]\ns_nop 1\nv_cvt_pkrtz_f16_f32 v24, v12, v13\nv_cvt_pkrtz_f16_f32 v25, v14, v15\n" \
  "v_pk_add_u16 v24, v24, 4 op_sel_hi:[1,0] clamp\nv_pk_add_u16 v25, v25, 4 op_sel_hi:[1,0] clamp\nds_write_b64 v4, v[24:25] offset:5120\n" \
  "v_cvt_pkrtz_f16_f32 v24, v16, v17\nv_cvt_pkrtz_f16_f32 v25, v18, v19\nv_pk_add_u16 v24, v24, 4 op_sel_hi:[1,0] clamp\n" \
  "v_pk_add_u16 v25, v25, 4 op_sel_hi:[1,0] clamp\nds_write_b64 v4, v[24:25] offset:6144\nv_cvt_pkrtz_f16_f32 v24, v20, v21\n" \
  "v_cvt_pkrtz_f16_f32 v25, v22, v23\nv_pk_add_u16 v24, v24, 4 op_sel_hi:[1,0] clamp\nv_pk_add_u16 v25, v25, 4 op_sel_hi:[1,0] clamp\n" \
  "ds_write_b64 v4, v[24:25] offset:7168\ns_setprio 0\ns_setpc_b64 s[90:91]\n31:\ns_movk_i32 s84, 0x4000\n33:\nds_read_b32 v27, v9 offset:32\n" \
  "s_sub_u32 s84, s84, 1\ns_cbranch_scc1 32b\ns_waitcnt lgkmcnt(0)\nv_readfirstlane_b32 s85, v27\ns_cmp_lt_u32 s85, 4\ns_cbranch_scc1 33b\n" \
  "s_branch 32b\n9:\ns_cmp_eq_u32 s89, 3\ns_cbranch_scc1 3f\ns_cmp_eq_u32 s89, 4\ns_cbranch_scc1 4f\ns_cmp_eq_u32 s89, 5\ns_cbranch_scc1 5f\n" \
  "ds_read_b64 v[124:125], v3 offset:576\nds_read_b64 v[126:127], v3 offset:584\nds_read_b64 v[120:121], v3 offset:64\n" \
  "ds_read_b64 v[122:123], v3 offset:72\nds_read_b64 v[116:117], v3 offset:640\nds_read_b64 v[118:119], v3 offset:648\n" \
  "ds_read_b64 v[112:113], v3 offset:128\nds_read_b64 v[114:115], v3 offset:136\nds_read_b64 v[108:109], v3 offset:704\n" \
  "ds_read_b64 v[110:111], v3 offset:712\nds_read_b64 v[104:105], v3 offset:192\nds_read_b64 v[106:107], v3 offset:200\n" \
  "ds_read_b64 v[100:101], v3 offset:768\nds_read_b64 v[102:103], v3 offset:776\nds_read_b64 v[96:97], v3 offset:256\n" \
  "ds_read_b64 v[98:99], v3 offset:264\nds_read_b64 v[92:93], v3 offset:832\nds_read_b64 v[94:95], v3 offset:840\n" \
  "ds_read_b64 v[88:89], v3 offset:320\nds_read_b64 v[90:91], v3 offset:328\nds_read_b64 v[84:85], v3 offset:896\n" \
  "ds_read_b64 v[86:87], v3 offset:904\nds_read_b64 v[80:81], v3 offset:384\nds_read_b64 v[82:83], v3 offset:392\n" \
  "ds_read_b64 v[76:77], v3 offset:960\nds_read_b64 v[78:79], v3 offset:968\nds_read_b64 v[72:73], v3 offset:448\n" \
  "ds_read_b64 v[74:75], v3 offset:456\nds_read_b64 v[68:69], v3 offset:1024\nds_read_b64 v[70:71], v3 offset:1032\n" \
  "ds_read_b64 v[64:65], v3 offset:512\nds_read_b64 v[66:67], v3 offset:520\ns_waitcnt lgkmcnt(0)\ns_barrier\nds_read_b128 v[24:27], v2 offset:0\n" \
  "ds_read_b128 v[28:31], v2 offset:64\nds_read_b128 v[32:35], v2 offset:128\nds_read_b128 v[36:39], v2 offset:192\ns_waitcnt lgkmcnt(3)\n" \
  "v_mfma_f32_16x16x32_f16 v[12:15], v[124:127], v[24:27], 0\ns_setprio 1\nds_read_b128 v[40:43], v2 offset:256\n" \
  "v_mfma_f32_16x16x32_f16 v[16:19], v[120:123], v[24:27], 0\ns_waitcnt lgkmcnt(3)\nv_mfma_f32_16x16x32_f16 v[12:15], v[116:119], v[28:31], v[12:15]\n" \
  "ds_read_b128 v[44:47], v2 offset:320\nv_mfma_f32_16x16x32_f16 v[16:19], v[112:115], v[28:31], v[16:19]\ns_waitcnt lgkmcnt(3)\n" \
  "v_mfma_f32_16x16x32_f16 v[12:15], v[108:111], v[32:35], v[12:15]\nds_read_b128 v[48:51], v2 offset:384\n" \
  "v_mfma_f32_16x16x32_f16 v[16:19], v[104:107], v[32:35], v[16:19]\ns_waitcnt lgkmcnt(3)\n" \
  "v_mfma_f32_16x16x32_f16 v[12:15], v[100:103], v[36:39], v[12:15]\nds_read_b128 v[52:55], v2 offset:448\n" \
  "v_mfma_f32_16x16x32_f16 v[16:19], v[96:99], v[36:39], v[16:19]\ns_waitcnt lgkmcnt(3)\n" \
  "v_mfma_f32_16x16x32_f16 v[12:15], v[92:95], v[40:43], v[12:15]\nds_read_b128 v[56:59], v2 offset:512\n" \
  "v_mfma_f32_16x16x32_f16 v[16:19], v[88:91], v[40:43], v[16:19]\nds_read_b128 v[60:63], v2 offset:576\ns_waitcnt lgkmcnt(3)\n" \
  "v_mfma_f32_16x16x32_f16 v[12:15], v[84:87], v[44:47], v[12:15]\nds_read_b128 v[120:123], v2 offset:640\n" \
  "v_mfma_f32_16x16x32_f16 v[16:19], v[80:83], v[44:47], v[16:19]\nds_read_b128 v[24:27], v2 offset:704\n" \
  "v_mfma_f32_16x16x32_f16 v[12:15], v[76:79], v[48:51], v[12:15]\nds_read_b128 v[112:115], v2 offset:768\n" \
  "v_mfma_f32_16x16x32_f16 v[16:19], v[72:75], v[48:51], v[16:19]\nds_read_b128 v[28:31], v2 offset:832\ns_waitcnt lgkmcnt(5)\n" \
  "v_mfma_f32_16x16x32_f16 v[12:15], v[68:71], v[52:55], v[12:15]\nds_read_b128 v[104:107], v2 offset:896\n" \
  "v_mfma_f32_16x16x32_f16 v[16:19], v[64:67], v[52:55], v[16:19]\nds_read_b128 v[32:35], v2 offset:960\n" \
  "v_mfma_f32_16x16x32_f16 v[16:19], v[124:127], v[56:59], v[16:19]\ns_waitcnt lgkmcnt(5)\n" \
  "v_mfma_f32_16x16x32_f16 v[16:19], v[116:119], v[60:63], v[16:19]\nv_mfma_f32_16x16x32_f16 v[16:19], v[108:111], v[120:123], v[16:19]\n" \
  "s_waitcnt lgkmcnt(3)\nv_mfma_f32_16x16x32_f16 v[16:19], v[100:103], v[24:27], v[16:19]\nv_cvt_pkrtz_f16_f32 v20, v12, v13\n" \
  "v_cvt_pkrtz_f16_f32 v21, v14, v15\nv_mfma_f32_16x16x32_f16 v[16:19], v[92:95], v[112:115], v[16:19]\n" \
  "v_pk_add_u16 v20, v20, 4 op_sel_hi:[1,0] clamp\ns_waitcnt lgkmcnt(2)\nv_mfma_f32_16x16x32_f16 v[16:19], v[84:87], v[28:31], v[16:19]\n" \
  "v_pk_add_u16 v21, v21, 4 op_sel_hi:[1,0] clamp\ns_waitcnt lgkmcnt(1)\nv_mfma_f32_16x16x32_f16 v[16:19], v[76:79], v[104:107], v[16:19]\n" \
  "ds_write_b64 v4, v[20:21] offset:0\ns_waitcnt lgkmcnt(1)\nv_mfma_f32_16x16x32_f16 v[16:19], v[68:71], v[32:35], v[16:19]\ns_nop 7\n" \
  "v_cvt_pkrtz_f16_f32 v20, v16, v17\nv_cvt_pkrtz_f16_f32 v21, v18, v19\nv_pk_add_u16 v20, v20, 4 op_sel_hi:[1,0] clamp\n" \
  "v_pk_add_u16 v21, v21, 4 op_sel_hi:[1,0] clamp\nds_write_b64 v4, v[20:21] offset:1024\ns_setprio 0\ns_setpc_b64 s[90:91]\n3:\n" \
  "ds_read_b64 v[124:125], v3 offset:0\nds_read_b64 v[126:127], v3 offset:8\nds_read_b64 v[120:121], v3 offset:64\n" \
  "ds_read_b64 v[122:123], v3 offset:72\nds_read_b64 v[116:117], v3 offset:128\nds_read_b64 v[118:119], v3 offset:136\n" \
  "ds_read_b64 v[112:113], v3 offset:192\nds_read_b64 v[114:115], v3 offset:200\nds_read_b64 v[108:109], v3 offset:256\n" \
  "ds_read_b64 v[110:111], v3 offset:264\nds_read_b64 v[104:105], v3 offset:320\nds_read_b64 v[106:107], v3 offset:328\n" \
  "ds_read_b64 v[100:101], v3 offset:384\nds_read_b64 v[102:103], v3 offset:392\nds_read_b64 v[96:97], v3 offset:448\n" \
  "ds_read_b64 v[98:99], v3 offset:456\nds_read_b64 v[92:93], v3 offset:512\nds_read_b64 v[94:95], v3 offset:520\n" \
  "ds_read_b64 v[88:89], v3 offset:576\nds_read_b64 v[90:91], v3 offset:584\nds_read_b64 v[84:85], v3 offset:640\n" \
  "ds_read_b64 v[86:87], v3 offset:648\nds_read_b64 v[80:81], v3 offset:704\nds_read_b64 v[82:83], v3 offset:712\n" \
  "ds_read_b64 v[76:77], v3 offset:768\nds_read_b64 v[78:79], v3 offset:776\nds_read_b64 v[72:73], v3 offset:832\n" \
  "ds_read_b64 v[74:75], v3 offset:840\nds_read_b64 v[68:69], v3 offset:896\nds_read_b64 v[70:71], v3 offset:904\n" \
  "ds_read_b64 v[64:65], v3 offset:960\nds_read_b64 v[66:67], v3 offset:968\nds_read_b64 v[60:61], v3 offset:1024\n" \
  "ds_read_b64 v[62:63], v3 offset:1032\nds_read_b64 v[56:57], v6 offset:256\nds_read_b64 v[58:59], v6 offset:264\n" \
  "ds_read_b64 v[52:53], v6 offset:128\nds_read_b64 v[54:55], v6 offset:136\nds_read_b64 v[48:49], v6 offset:320\n" \
  "ds_read_b64 v[50:51], v6 offset:328\nds_read_b64 v[44:45], v6 offset:192\nds_read_b64 v[46:47], v6 offset:200\ns_waitcnt lgkmcnt(0)\ns_barrier\n" \
  "ds_read_b128 v[24:27], v2 offset:448\nds_read_b128 v[28:31], v2 offset:512\nds_read_b128 v[32:35], v2 offset:576\n" \
  "ds_read_b128 v[36:39], v2 offset:640\nds_read_b128 v[40:43], v2 offset:704\ns_waitcnt lgkmcnt(3)\n" \
  "v_mfma_f32_16x16x32_f16 v[12:15], v[124:127], v[24:27], 0\ns_setprio 1\nds_read_b128 v[24:27], v2 offset:768\n" \
  "v_mfma_f32_16x16x32_f16 v[12:15], v[120:123], v[28:31], v[12:15]\nds_read_b128 v[28:31], v2 offset:832\ns_waitcnt lgkmcnt(3)\n" \
  "v_mfma_f32_16x16x32_f16 v[12:15], v[116:119], v[32:35], v[12:15]\nds_read_b128 v[32:35], v2 offset:896\n" \
  "v_mfma_f32_16x16x32_f16 v[12:15], v[112:115], v[36:39], v[12:15]\nds_read_b128 v[36:39], v2 offset:960\ns_waitcnt lgkmcnt(3)\n" \
  "v_mfma_f32_16x16x32_f16 v[12:15], v[108:111], v[40:43], v[12:15]\nds_read_b128 v[40:43], v2 offset:1024\n" \
  "v_mfma_f32_16x16x32_f16 v[12:15], v[104:107], v[24:27], v[12:15]\nds_read_b128 v[24:27], v2 offset:1088\ns_waitcnt lgkmcnt(3)\n" \
  "v_mfma_f32_16x16x32_f16 v[12:15], v[100:103], v[28:31], v[12:15]\nds_read_b128 v[28:31], v2 offset:1152\n" \
  "v_mfma_f32_16x16x32_f16 v[12:15], v[96:99], v[32:35], v[12:15]\ns_waitcnt lgkmcnt(3)\n" \
  "v_mfma_f32_16x16x32_f16 v[12:15], v[92:95], v[36:39], v[12:15]\nds_read_b128 v[32:35], v2 offset:1216\n" \
  "v_mfma_f32_16x16x32_f16 v[16:19], v[124:127], v[36:39], 0\ns_waitcnt lgkmcnt(3)\nv_mfma_f32_16x16x32_f16 v[12:15], v[88:91], v[40:43], v[12:15]\n" \
  "ds_read_b128 v[124:127], v2 offset:1280\nv_mfma_f32_16x16x32_f16 v[16:19], v[120:123], v[40:43], v[16:19]\ns_waitcnt lgkmcnt(3)\n" \
  "v_mfma_f32_16x16x32_f16 v[12:15], v[84:87], v[24:27], v[12:15]\nds_read_b128 v[36:39], v2 offset:1344\n" \
  "v_mfma_f32_16x16x32_f16 v[16:19], v[116:119], v[24:27], v[16:19]\ns_waitcnt lgkmcnt(3)\n" \
  "v_mfma_f32_16x16x32_f16 v[12:15], v[80:83], v[28:31], v[12:15]\nds_read_b128 v[120:123], v2 offset:1408\n" \
  "v_mfma_f32_16x16x32_f16 v[16:19], v[112:115], v[28:31], v[16:19]\ns_waitcnt lgkmcnt(3)\n" \
  "v_mfma_f32_16x16x32_f16 v[12:15], v[76:79], v[32:35], v[12:15]\nds_read_b128 v[40:43], v2 offset:1472\n" \
  "v_mfma_f32_16x16x32_f16 v[16:19], v[108:111], v[32:35], v[16:19]\ns_waitcnt lgkmcnt(3)\n" \
  "v_mfma_f32_16x16x32_f16 v[12:15], v[72:75], v[124:127], v[12:15]\nds_read_b128 v[116:119], v2 offset:1536\n" \
  "v_mfma_f32_16x16x32_f16 v[16:19], v[104:107], v[124:127], v[16:19]\nds_read_b128 v[24:27], v2 offset:1600\ns_waitcnt lgkmcnt(3)\n" \
  "v_mfma_f32_16x16x32_f16 v[12:15], v[68:71], v[36:39], v[12:15]\nds_read_b128 v[112:115], v2 offset:1664\nds_read_b32 v23, v9 offset:32\n" \
  "v_mfma_f32_16x16x32_f16 v[16:19], v[100:103], v[36:39], v[16:19]\nds_read_b128 v[28:31], v2 offset:1728\n" \
  "v_mfma_f32_16x16x32_f16 v[12:15], v[64:67], v[120:123], v[12:15]\nds_read_b128 v[108:111], v2 offset:1792\n" \
  "v_mfma_f32_16x16x32_f16 v[16:19], v[96:99], v[120:123], v[16:19]\nds_read_b128 v[32:35], v2 offset:1856\ns_waitcnt lgkmcnt(6)\n" \
  "v_mfma_f32_16x16x32_f16 v[12:15], v[60:63], v[40:43], v[12:15]\nds_read_b128 v[104:107], v2 offset:1920\n" \
  "v_mfma_f32_16x16x32_f16 v[16:19], v[92:95], v[40:43], v[16:19]\nds_read_b128 v[124:127], v2 offset:1984\ns_waitcnt lgkmcnt(5)\n" \
  "v_readfirstlane_b32 s85, v23\ns_cmp_lt_u32 s85, 3\ns_cbranch_scc1 51f\n52:\nv_mfma_f32_16x16x32_f16 v[16:19], v[88:91], v[116:119], v[16:19]\n" \
  "ds_read_b64_tr_b16 v[100:101], v5 offset:0\nds_read_b64_tr_b16 v[102:103], v5 offset:32\n" \
  "v_mfma_f32_16x16x32_f16 v[16:19], v[84:87], v[24:27], v[16:19]\nds_read_b64_tr_b16 v[36:37], v5 offset:256\n" \
  "ds_read_b64_tr_b16 v[38:39], v5 offset:288\nv_mfma_f32_16x16x32_f16 v[16:19], v[80:83], v[112:115], v[16:19]\n" \
  "ds_read_b64_tr_b16 v[96:97], v5 offset:512\nds_read_b64_tr_b16 v[98:99], v5 offset:544\ns_waitcnt lgkmcnt(9)\n" \
  "v_mfma_f32_16x16x32_f16 v[16:19], v[76:79], v[28:31], v[16:19]\nds_read_b64_tr_b16 v[120:121], v5 offset:768\n" \
  "ds_read_b64_tr_b16 v[122:123], v5 offset:800\nv_mfma_f32_16x16x32_f16 v[16:19], v[72:75], v[108:111], v[16:19]\ns_waitcnt lgkmcnt(9)\n" \
  "v_mfma_f32_16x16x32_f16 v[16:19], v[68:71], v[32:35], v[16:19]\nv_mfma_f32_16x16x32_f16 v[16:19], v[64:67], v[104:107], v[16:19]\n" \
  "s_waitcnt lgkmcnt(6)\nv_mfma_f32_16x16x32_f16 v[16:19], v[60:63], v[124:127], v[16:19]\n" \
  "v_mfma_f32_16x16x32_f16 v[12:15], v[100:103], v[56:59], v[12:15]\nv_mfma_f32_16x16x32_f16 v[16:19], v[100:103], v[52:55], v[16:19]\n" \
  "s_waitcnt lgkmcnt(2)\nv_mfma_f32_16x16x32_f16 v[12:15], v[36:39], v[48:51], v[12:15]\n" \
  "v_mfma_f32_16x16x32_f16 v[16:19], v[36:39], v[44:47], v[16:19]\nv_mfma_f32_16x16x32_f16 v[16:19], v[96:99], v[56:59], v[16:19]\n" \
  "s_waitcnt lgkmcnt(0)\nv_mfma_f32_16x16x32_f16 v[16:19], v[120:123], v[48:51], v[16:19]\ns_nop 3\nv_cvt_pkrtz_f16_f32 v20, v12, v13\n" \
  "v_cvt_pkrtz_f16_f32 v21, v14, v15\nv_pk_add_u16 v20, v20, 4 op_sel_hi:[1,0] clamp\nv_pk_add_u16 v21, v21, 4 op_sel_hi:[1,0] clamp\n" \
  "ds_write_b64 v4, v[20:21] offset:2048\nv_cvt_pkrtz_f16_f32 v20, v16, v17\nv_cvt_pkrtz_f16_f32 v21, v18, v19\n" \
  "v_pk_add_u16 v20, v20, 4 op_sel_hi:[1,0] clamp\nv_pk_add_u16 v21, v21, 4 op_sel_hi:[1,0] clamp\nds_write_b64 v4, v[20:21] offset:3072\n" \
  "s_setprio 0\ns_setpc_b64 s[90:91]\n51:\ns_movk_i32 s84, 0x4000\n53:\nds_read_b32 v23, v9 offset:32\ns_sub_u32 s84, s84, 1\ns_cbranch_scc1 52b\n" \
  "s_waitcnt lgkmcnt(0)\nv_readfirstlane_b32 s85, v23\ns_cmp_lt_u32 s85, 3\ns_cbranch_scc1 53b\ns_branch 52b\n4:\n" \
  "ds_read_b64 v[124:125], v3 offset:1088\nds_read_b64 v[126:127], v3 offset:1096\nds_read_b64 v[120:121], v3 offset:0\n" \
  "ds_read_b64 v[122:123], v3 offset:8\nds_read_b64 v[116:117], v3 offset:64\nds_read_b64 v[118:119], v3 offset:72\n" \
  "ds_read_b64 v[112:113], v3 offset:128\nds_read_b64 v[114:115], v3 offset:136\nds_read_b64 v[108:109], v3 offset:192\n" \
  "ds_read_b64 v[110:111], v3 offset:200\nds_read_b64 v[104:105], v3 offset:256\nds_read_b64 v[106:107], v3 offset:264\n" \
  "ds_read_b64 v[100:101], v3 offset:320\nds_read_b64 v[102:103], v3 offset:328\nds_read_b64 v[96:97], v3 offset:384\n" \
  "ds_read_b64 v[98:99], v3 offset:392\nds_read_b64 v[92:93], v3 offset:448\nds_read_b64 v[94:95], v3 offset:456\n" \
  "ds_read_b64 v[88:89], v3 offset:512\nds_read_b64 v[90:91], v3 offset:520\nds_read_b64 v[84:85], v3 offset:576\n" \
  "ds_read_b64 v[86:87], v3 offset:584\nds_read_b64 v[80:81], v3 offset:640\nds_read_b64 v[82:83], v3 offset:648\n" \
  "ds_read_b64 v[76:77], v3 offset:704\nds_read_b64 v[78:79], v3 offset:712\nds_read_b64 v[72:73], v3 offset:768\n" \
  "ds_read_b64 v[74:75], v3 offset:776\nds_read_b64 v[68:69], v3 offset:832\nds_read_b64 v[70:71], v3 offset:840\n" \
  "ds_read_b64 v[64:65], v3 offset:896\nds_read_b64 v[66:67], v3 offset:904\nds_read_b64 v[60:61], v3 offset:960\n" \
  "ds_read_b64 v[62:63], v3 offset:968\nds_read_b64 v[56:57], v3 offset:1024\nds_read_b64 v[58:59], v3 offset:1032\n" \
  "ds_read_b64 v[52:53], v6 offset:0\nds_read_b64 v[54:55], v6 offset:8\nds_read_b64 v[48:49], v6 offset:64\nds_read_b64 v[50:51], v6 offset:72\n" \
  "ds_read_b64 v[44:45], v6 offset:128\nds_read_b64 v[46:47], v6 offset:136\nds_read_b64 v[40:41], v6 offset:192\n" \
  "ds_read_b64 v[42:43], v6 offset:200\ns_waitcnt lgkmcnt(0)\ns_barrier\nds_read_b128 v[24:27], v2 offset:448\nds_read_b128 v[28:31], v2 offset:960\n" \
  "ds_read_b128 v[32:35], v2 offset:1472\nds_read_b128 v[36:39], v2 offset:1536\ns_waitcnt lgkmcnt(3)\n" \
  "v_mfma_f32_16x16x32_f16 v[12:15], v[124:127], v[24:27], 0\ns_setprio 1\nds_read_b128 v[24:27], v2 offset:1600\ns_waitcnt lgkmcnt(3)\n" \
  "v_mfma_f32_16x16x32_f16 v[16:19], v[124:127], v[28:31], 0\nds_read_b128 v[124:127], v2 offset:1664\ns_waitcnt lgkmcnt(3)\n" \
  "v_mfma_f32_16x16x32_f16 v[12:15], v[120:123], v[32:35], v[12:15]\nds_read_b128 v[28:31], v2 offset:1728\ns_waitcnt lgkmcnt(3)\n" \
  "v_mfma_f32_16x16x32_f16 v[12:15], v[116:119], v[36:39], v[12:15]\nds_read_b128 v[32:35], v2 offset:1792\ns_waitcnt lgkmcnt(3)\n" \
  "v_mfma_f32_16x16x32_f16 v[12:15], v[112:115], v[24:27], v[12:15]\nds_read_b128 v[36:39], v2 offset:1856\ns_waitcnt lgkmcnt(3)\n" \
  "v_mfma_f32_16x16x32_f16 v[12:15], v[108:111], v[124:127], v[12:15]\nds_read_b128 v[24:27], v2 offset:1920\ns_waitcnt lgkmcnt(3)\n" \
  "v_mfma_f32_16x16x32_f16 v[12:15], v[104:107], v[28:31], v[12:15]\nds_read_b128 v[124:127], v2 offset:1984\ns_waitcnt lgkmcnt(3)\n" \
  "v_mfma_f32_16x16x32_f16 v[12:15], v[100:103], v[32:35], v[12:15]\nds_read_b128 v[28:31], v2 offset:2048\ns_waitcnt lgkmcnt(3)\n" \
  "v_mfma_f32_16x16x32_f16 v[12:15], v[96:99], v[36:39], v[12:15]\nds_read_b128 v[32:35], v2 offset:2112\ns_waitcnt lgkmcnt(3)\n" \
  "v_mfma_f32_16x16x32_f16 v[12:15], v[92:95], v[24:27], v[12:15]\nds_read_b128 v[36:39], v2 offset:2176\ns_waitcnt lgkmcnt(3)\n" \
  "v_mfma_f32_16x16x32_f16 v[12:15], v[88:91], v[124:127], v[12:15]\nds_read_b128 v[24:27], v2 offset:2240\n" \
  "v_mfma_f32_16x16x32_f16 v[16:19], v[120:123], v[124:127], v[16:19]\nds_read_b64 v[120:121], v6 offset:256\nds_read_b64 v[122:123], v6 offset:264\n" \
  "s_waitcnt lgkmcnt(4)\nv_mfma_f32_16x16x32_f16 v[12:15], v[84:87], v[28:31], v[12:15]\nds_read_b128 v[124:127], v2 offset:2304\n" \
  "v_mfma_f32_16x16x32_f16 v[16:19], v[116:119], v[28:31], v[16:19]\nds_read_b64 v[116:117], v6 offset:320\nds_read_b64 v[118:119], v6 offset:328\n" \
  "v_mfma_f32_16x16x32_f16 v[12:15], v[80:83], v[32:35], v[12:15]\nds_read_b128 v[28:31], v2 offset:2368\n" \
  "v_mfma_f32_16x16x32_f16 v[16:19], v[112:115], v[32:35], v[16:19]\ns_waitcnt lgkmcnt(6)\n" \
  "v_mfma_f32_16x16x32_f16 v[12:15], v[76:79], v[36:39], v[12:15]\nds_read_b128 v[112:115], v2 offset:2432\n" \
  "v_mfma_f32_16x16x32_f16 v[16:19], v[108:111], v[36:39], v[16:19]\nds_read2_b32 v[32:33], v8 offset0:48 offset1:49\n" \
  "ds_read2_b32 v[34:35], v8 offset0:50 offset1:51\nv_mfma_f32_16x16x32_f16 v[12:15], v[72:75], v[24:27], v[12:15]\n" \
  "ds_read_b128 v[108:111], v2 offset:2496\nv_mfma_f32_16x16x32_f16 v[16:19], v[104:107], v[24:27], v[16:19]\n" \
  "ds_read2_b32 v[36:37], v8 offset0:32 offset1:33\nds_read2_b32 v[38:39], v8 offset0:34 offset1:35\ns_waitcnt lgkmcnt(6)\n" \
  "v_mfma_f32_16x16x32_f16 v[12:15], v[68:71], v[124:127], v[12:15]\nds_read_b128 v[104:107], v2 offset:2560\n" \
  "v_mfma_f32_16x16x32_f16 v[16:19], v[100:103], v[124:127], v[16:19]\nds_read_b128 v[24:27], v2 offset:2624\n" \
  "v_mfma_f32_16x16x32_f16 v[12:15], v[64:67], v[28:31], v[12:15]\nds_read_b128 v[100:103], v2 offset:2688\nds_read_b32 v23, v9 offset:32\n" \
  "v_mfma_f32_16x16x32_f16 v[16:19], v[96:99], v[28:31], v[16:19]\nds_read_b128 v[124:127], v2 offset:2752\ns_waitcnt lgkmcnt(7)\n" \
  "v_mfma_f32_16x16x32_f16 v[12:15], v[60:63], v[112:115], v[12:15]\nds_read_b128 v[96:99], v2 offset:2816\n" \
  "v_mfma_f32_16x16x32_f16 v[16:19], v[92:95], v[112:115], v[16:19]\nds_read_b128 v[28:31], v2 offset:2880\n" \
  "v_mfma_f32_16x16x32_f16 v[12:15], v[56:59], v[108:111], v[12:15]\nds_read_b128 v[92:95], v2 offset:2944\n" \
  "v_mfma_f32_16x16x32_f16 v[16:19], v[88:91], v[108:111], v[16:19]\nds_read_b128 v[112:115], v2 offset:3008\ns_waitcnt lgkmcnt(5)\n" \
  "v_readfirstlane_b32 s85, v23\ns_cmp_lt_u32 s85, 3\ns_cbranch_scc1 61f\n62:\nv_mfma_f32_16x16x32_f16 v[16:19], v[84:87], v[104:107], v[16:19]\n" \
  "ds_read_b64_tr_b16 v[88:89], v5 offset:0\nds_read_b64_tr_b16 v[90:91], v5 offset:32\n" \
  "v_mfma_f32_16x16x32_f16 v[16:19], v[80:83], v[24:27], v[16:19]\nds_read_b64_tr_b16 v[108:109], v5 offset:256\n" \
  "ds_read_b64_tr_b16 v[110:111], v5 offset:288\nv_mfma_f32_16x16x32_f16 v[16:19], v[76:79], v[100:103], v[16:19]\n" \
  "ds_read_b64_tr_b16 v[84:85], v5 offset:512\nds_read_b64_tr_b16 v[86:87], v5 offset:544\ns_waitcnt lgkmcnt(9)\n" \
  "v_mfma_f32_16x16x32_f16 v[16:19], v[72:75], v[124:127], v[16:19]\nds_read_b64_tr_b16 v[104:105], v5 offset:768\n" \
  "ds_read_b64_tr_b16 v[106:107], v5 offset:800\nv_mfma_f32_16x16x32_f16 v[16:19], v[68:71], v[96:99], v[16:19]\n" \
  "ds_read_b64_tr_b16 v[80:81], v5 offset:1024\nds_read_b64_tr_b16 v[82:83], v5 offset:1056\ns_waitcnt lgkmcnt(11)\n" \
  "v_mfma_f32_16x16x32_f16 v[16:19], v[64:67], v[28:31], v[16:19]\nds_read_b64_tr_b16 v[24:25], v5 offset:1280\n" \
  "ds_read_b64_tr_b16 v[26:27], v5 offset:1312\nv_mfma_f32_16x16x32_f16 v[16:19], v[60:63], v[92:95], v[16:19]\n" \
  "ds_read_b64_tr_b16 v[76:77], v5 offset:1536\nds_read_b64_tr_b16 v[78:79], v5 offset:1568\ns_waitcnt lgkmcnt(12)\n" \
  "v_mfma_f32_16x16x32_f16 v[16:19], v[56:59], v[112:115], v[16:19]\nds_read_b64_tr_b16 v[100:101], v5 offset:1792\n" \
  "ds_read_b64_tr_b16 v[102:103], v5 offset:1824\nv_mfma_f32_16x16x32_f16 v[12:15], v[88:91], v[52:55], v[12:15]\ns_waitcnt lgkmcnt(10)\n" \
  "v_mfma_f32_16x16x32_f16 v[12:15], v[108:111], v[48:51], v[12:15]\nds_read_b64_tr_b16 v[72:73], v7 offset:0\n" \
  "ds_read_b64_tr_b16 v[74:75], v7 offset:64\nv_mfma_f32_16x16x32_f16 v[12:15], v[84:87], v[44:47], v[12:15]\n" \
  "ds_read_b64_tr_b16 v[124:125], v7 offset:512\nds_read_b64_tr_b16 v[126:127], v7 offset:576\n" \
  "v_mfma_f32_16x16x32_f16 v[16:19], v[84:87], v[52:55], v[16:19]\ns_waitcnt lgkmcnt(10)\n" \
  "v_mfma_f32_16x16x32_f16 v[12:15], v[104:107], v[40:43], v[12:15]\nv_mfma_f32_16x16x32_f16 v[16:19], v[104:107], v[48:51], v[16:19]\n" \
  "v_mfma_f32_16x16x32_f16 v[12:15], v[80:83], v[120:123], v[12:15]\nv_mfma_f32_16x16x32_f16 v[16:19], v[80:83], v[44:47], v[16:19]\n" \
  "s_waitcnt lgkmcnt(6)\nv_mfma_f32_16x16x32_f16 v[12:15], v[24:27], v[116:119], v[12:15]\n" \
  "v_mfma_f32_16x16x32_f16 v[16:19], v[24:27], v[40:43], v[16:19]\nv_mfma_f32_16x16x32_f16 v[16:19], v[76:79], v[120:123], v[16:19]\n" \
  "s_waitcnt lgkmcnt(2)\nv_mfma_f32_16x16x32_f16 v[16:19], v[100:103], v[116:119], v[16:19]\n" \
  "v_mfma_f32_16x16x32_f16 v[12:15], v[72:75], v[32:35], v[12:15]\nv_mfma_f32_16x16x32_f16 v[16:19], v[72:75], v[36:39], v[16:19]\n" \
  "s_waitcnt lgkmcnt(0)\nv_mfma_f32_16x16x32_f16 v[16:19], v[124:127], v[32:35], v[16:19]\ns_nop 4\nv_cvt_pkrtz_f16_f32 v20, v12, v13\n" \
  "v_cvt_pkrtz_f16_f32 v21, v14, v15\nv_pk_add_u16 v20, v20, 4 op_sel_hi:[1,0] clamp\nv_pk_add_u16 v21, v21, 4 op_sel_hi:[1,0] clamp\n" \
  "ds_write_b64 v4, v[20:21] offset:4096\nv_cvt_pkrtz_f16_f32 v20, v16, v17\nv_cvt_pkrtz_f16_f32 v21, v18, v19\n" \
  "v_pk_add_u16 v20, v20, 4 op_sel_hi:[1,0] clamp\nv_pk_add_u16 v21, v21, 4 op_sel_hi:[1,0] clamp\nds_write_b64 v4, v[20:21] offset:5120\n" \
  "s_setprio 0\ns_setpc_b64 s[90:91]\n61:\ns_movk_i32 s84, 0x4000\n63:\nds_read_b32 v23, v9 offset:32\ns_sub_u32 s84, s84, 1\ns_cbranch_scc1 62b\n" \
  "s_waitcnt lgkmcnt(0)\nv_readfirstlane_b32 s85, v23\ns_cmp_lt_u32 s85, 3\ns_cbranch_scc1 63b\ns_branch 62b\n5:\n" \
  "ds_read_b64 v[124:125], v3 offset:1088\nds_read_b64 v[126:127], v3 offset:1096\nds_read_b64 v[120:121], v3 offset:0\n" \
  "ds_read_b64 v[122:123], v3 offset:8\nds_read_b64 v[116:117], v3 offset:64\nds_read_b64 v[118:119], v3 offset:72\n" \
  "ds_read_b64 v[112:113], v3 offset:128\nds_read_b64 v[114:115], v3 offset:136\nds_read_b64 v[108:109], v3 offset:192\n" \
  "ds_read_b64 v[110:111], v3 offset:200\nds_read_b64 v[104:105], v3 offset:256\nds_read_b64 v[106:107], v3 offset:264\n" \
  "ds_read_b64 v[100:101], v3 offset:320\nds_read_b64 v[102:103], v3 offset:328\nds_read_b64 v[96:97], v3 offset:384\n" \
  "ds_read_b64 v[98:99], v3 offset:392\nds_read_b64 v[92:93], v3 offset:448\nds_read_b64 v[94:95], v3 offset:456\n" \
  "ds_read_b64 v[88:89], v3 offset:512\nds_read_b64 v[90:91], v3 offset:520\nds_read_b64 v[84:85], v3 offset:576\n" \
  "ds_read_b64 v[86:87], v3 offset:584\nds_read_b64 v[80:81], v3 offset:640\nds_read_b64 v[82:83], v3 offset:648\n" \
  "ds_read_b64 v[76:77], v3 offset:704\nds_read_b64 v[78:79], v3 offset:712\nds_read_b64 v[72:73], v3 offset:768\n" \
  "ds_read_b64 v[74:75], v3 offset:776\nds_read_b64 v[68:69], v3 offset:832\nds_read_b64 v[70:71], v3 offset:840\n" \
  "ds_read_b64 v[64:65], v3 offset:896\nds_read_b64 v[66:67], v3 offset:904\nds_read_b64 v[60:61], v3 offset:960\n" \
  "ds_read_b64 v[62:63], v3 offset:968\nds_read_b64 v[56:57], v3 offset:1024\nds_read_b64 v[58:59], v3 offset:1032\n" \
  "ds_read_b64 v[52:53], v6 offset:0\nds_read_b64 v[54:55], v6 offset:8\nds_read_b64 v[48:49], v6 offset:64\nds_read_b64 v[50:51], v6 offset:72\n" \
  "ds_read_b64 v[44:45], v6 offset:128\nds_read_b64 v[46:47], v6 offset:136\nds_read_b64 v[40:41], v6 offset:192\n" \
  "ds_read_b64 v[42:43], v6 offset:200\ns_waitcnt lgkmcnt(0)\ns_barrier\nds_read_b128 v[24:27], v2 offset:1472\n" \
  "ds_read_b128 v[28:31], v2 offset:1984\nds_read_b128 v[32:35], v2 offset:2496\nds_read_b128 v[36:39], v2 offset:2560\ns_waitcnt lgkmcnt(3)\n" \
  "v_mfma_f32_16x16x32_f16 v[12:15], v[124:127], v[24:27], 0\ns_setprio 1\nds_read_b128 v[24:27], v2 offset:2624\ns_waitcnt lgkmcnt(3)\n" \
  "v_mfma_f32_16x16x32_f16 v[16:19], v[124:127], v[28:31], 0\nds_read_b128 v[124:127], v2 offset:2688\ns_waitcnt lgkmcnt(3)\n" \
  "v_mfma_f32_16x16x32_f16 v[12:15], v[120:123], v[32:35], v[12:15]\nds_read_b128 v[28:31], v2 offset:2752\ns_waitcnt lgkmcnt(3)\n" \
  "v_mfma_f32_16x16x32_f16 v[12:15], v[116:119], v[36:39], v[12:15]\nds_read_b128 v[32:35], v2 offset:2816\ns_waitcnt lgkmcnt(3)\n" \
  "v_mfma_f32_16x16x32_f16 v[12:15], v[112:115], v[24:27], v[12:15]\nds_read_b128 v[36:39], v2 offset:2880\ns_waitcnt lgkmcnt(3)\n" \
  "v_mfma_f32_16x16x32_f16 v[12:15], v[108:111], v[124:127], v[12:15]\nds_read_b128 v[24:27], v2 offset:2944\ns_waitcnt lgkmcnt(3)\n" \
  "v_mfma_f32_16x16x32_f16 v[12:15], v[104:107], v[28:31], v[12:15]\nds_read_b128 v[124:127], v2 offset:3008\ns_waitcnt lgkmcnt(3)\n" \
  "v_mfma_f32_16x16x32_f16 v[12:15], v[100:103], v[32:35], v[12:15]\nds_read_b128 v[28:31], v2 offset:3072\ns_waitcnt lgkmcnt(3)\n" \
  "v_mfma_f32_16x16x32_f16 v[12:15], v[96:99], v[36:39], v[12:15]\nds_read_b128 v[32:35], v2 offset:3136\ns_waitcnt lgkmcnt(3)\n" \
  "v_mfma_f32_16x16x32_f16 v[12:15], v[92:95], v[24:27], v[12:15]\nds_read_b128 v[36:39], v2 offset:3200\ns_waitcnt lgkmcnt(3)\n" \
  "v_mfma_f32_16x16x32_f16 v[12:15], v[88:91], v[124:127], v[12:15]\nds_read_b128 v[24:27], v2 offset:3264\n" \
  "v_mfma_f32_16x16x32_f16 v[16:19], v[120:123], v[124:127], v[16:19]\nds_read_b64 v[120:121], v6 offset:256\nds_read_b64 v[122:123], v6 offset:264\n" \
  "s_waitcnt lgkmcnt(4)\nv_mfma_f32_16x16x32_f16 v[12:15], v[84:87], v[28:31], v[12:15]\nds_read_b128 v[124:127], v2 offset:3328\n" \
  "v_mfma_f32_16x16x32_f16 v[16:19], v[116:119], v[28:31], v[16:19]\nds_read_b64 v[116:117], v6 offset:320\nds_read_b64 v[118:119], v6 offset:328\n" \
  "v_mfma_f32_16x16x32_f16 v[12:15], v[80:83], v[32:35], v[12:15]\nds_read_b128 v[28:31], v2 offset:3392\n" \
  "v_mfma_f32_16x16x32_f16 v[16:19], v[112:115], v[32:35], v[16:19]\ns_waitcnt lgkmcnt(6)\n" \
  "v_mfma_f32_16x16x32_f16 v[12:15], v[76:79], v[36:39], v[12:15]\nds_read_b128 v[112:115], v2 offset:3456\n" \
  "v_mfma_f32_16x16x32_f16 v[16:19], v[108:111], v[36:39], v[16:19]\nds_read2_b32 v[32:33], v8 offset0:16 offset1:17\n" \
  "ds_read2_b32 v[34:35], v8 offset0:18 offset1:19\nv_mfma_f32_16x16x32_f16 v[12:15], v[72:75], v[24:27], v[12:15]\n" \
  "ds_read_b128 v[108:111], v2 offset:3520\nv_mfma_f32_16x16x32_f16 v[16:19], v[104:107], v[24:27], v[16:19]\n" \
  "ds_read2_b32 v[36:37], v8 offset0:0 offset1:1\nds_read2_b32 v[38:39], v8 offset0:2 offset1:3\ns_waitcnt lgkmcnt(6)\n" \
  "v_mfma_f32_16x16x32_f16 v[12:15], v[68:71], v[124:127], v[12:15]\nds_read_b128 v[104:107], v2 offset:3584\n" \
  "v_mfma_f32_16x16x32_f16 v[16:19], v[100:103], v[124:127], v[16:19]\nds_read_b128 v[24:27], v2 offset:3648\n" \
  "ds_read2_b32 v[100:101], v8 offset0:32 offset1:33\nds_read2_b32 v[102:103], v8 offset0:34 offset1:35\n" \
  "v_mfma_f32_16x16x32_f16 v[12:15], v[64:67], v[28:31], v[12:15]\nds_read_b128 v[124:127], v2 offset:3712\nds_read_b32 v23, v9 offset:32\n" \
  "v_mfma_f32_16x16x32_f16 v[16:19], v[96:99], v[28:31], v[16:19]\nds_read_b128 v[96:99], v2 offset:3776\ns_waitcnt lgkmcnt(9)\n" \
  "v_mfma_f32_16x16x32_f16 v[12:15], v[60:63], v[112:115], v[12:15]\nds_read_b128 v[28:31], v2 offset:3840\n" \
  "v_mfma_f32_16x16x32_f16 v[16:19], v[92:95], v[112:115], v[16:19]\nds_read_b128 v[92:95], v2 offset:3904\n" \
  "v_mfma_f32_16x16x32_f16 v[12:15], v[56:59], v[108:111], v[12:15]\nds_read_b128 v[112:115], v2 offset:3968\n" \
  "v_mfma_f32_16x16x32_f16 v[16:19], v[88:91], v[108:111], v[16:19]\nds_read_b128 v[88:91], v2 offset:4032\ns_waitcnt lgkmcnt(5)\n" \
  "v_readfirstlane_b32 s85, v23\ns_cmp_lt_u32 s85, 3\ns_cbranch_scc1 71f\n72:\nv_mfma_f32_16x16x32_f16 v[16:19], v[84:87], v[104:107], v[16:19]\n" \
  "ds_read_b64_tr_b16 v[108:109], v5 offset:1024\nds_read_b64_tr_b16 v[110:111], v5 offset:1056\n" \
  "v_mfma_f32_16x16x32_f16 v[16:19], v[80:83], v[24:27], v[16:19]\nds_read_b64_tr_b16 v[84:85], v5 offset:1280\n" \
  "ds_read_b64_tr_b16 v[86:87], v5 offset:1312\nv_mfma_f32_16x16x32_f16 v[16:19], v[76:79], v[124:127], v[16:19]\n" \
  "ds_read_b64_tr_b16 v[104:105], v5 offset:1536\nds_read_b64_tr_b16 v[106:107], v5 offset:1568\ns_waitcnt lgkmcnt(9)\n" \
  "v_mfma_f32_16x16x32_f16 v[16:19], v[72:75], v[96:99], v[16:19]\nds_read_b64_tr_b16 v[80:81], v5 offset:1792\n" \
  "ds_read_b64_tr_b16 v[82:83], v5 offset:1824\nv_mfma_f32_16x16x32_f16 v[16:19], v[68:71], v[28:31], v[16:19]\n" \
  "ds_read_b64_tr_b16 v[24:25], v5 offset:2048\nds_read_b64_tr_b16 v[26:27], v5 offset:2080\ns_waitcnt lgkmcnt(11)\n" \
  "v_mfma_f32_16x16x32_f16 v[16:19], v[64:67], v[92:95], v[16:19]\nds_read_b64_tr_b16 v[76:77], v5 offset:2304\n" \
  "ds_read_b64_tr_b16 v[78:79], v5 offset:2336\nv_mfma_f32_16x16x32_f16 v[16:19], v[60:63], v[112:115], v[16:19]\n" \
  "ds_read_b64_tr_b16 v[124:125], v5 offset:2560\nds_read_b64_tr_b16 v[126:127], v5 offset:2592\ns_waitcnt lgkmcnt(12)\n" \
  "v_mfma_f32_16x16x32_f16 v[16:19], v[56:59], v[88:91], v[16:19]\nds_read_b64_tr_b16 v[72:73], v5 offset:2816\n" \
  "ds_read_b64_tr_b16 v[74:75], v5 offset:2848\nv_mfma_f32_16x16x32_f16 v[12:15], v[108:111], v[52:55], v[12:15]\ns_waitcnt lgkmcnt(10)\n" \
  "v_mfma_f32_16x16x32_f16 v[12:15], v[84:87], v[48:51], v[12:15]\nds_read_b64_tr_b16 v[96:97], v7 offset:0\n" \
  "ds_read_b64_tr_b16 v[98:99], v7 offset:64\nv_mfma_f32_16x16x32_f16 v[12:15], v[104:107], v[44:47], v[12:15]\n" \
  "ds_read_b64_tr_b16 v[68:69], v7 offset:512\nds_read_b64_tr_b16 v[70:71], v7 offset:576\n" \
  "v_mfma_f32_16x16x32_f16 v[16:19], v[104:107], v[52:55], v[16:19]\ns_waitcnt lgkmcnt(10)\n" \
  "v_mfma_f32_16x16x32_f16 v[12:15], v[80:83], v[40:43], v[12:15]\nds_read_b64_tr_b16 v[28:29], v7 offset:1024\n" \
  "ds_read_b64_tr_b16 v[30:31], v7 offset:1088\nv_mfma_f32_16x16x32_f16 v[16:19], v[80:83], v[48:51], v[16:19]\n" \
  "ds_read2_b32 v[64:65], v8 offset0:48 offset1:49\nds_read2_b32 v[66:67], v8 offset0:50 offset1:51\n" \
  "v_mfma_f32_16x16x32_f16 v[12:15], v[24:27], v[120:123], v[12:15]\nv_mfma_f32_16x16x32_f16 v[16:19], v[24:27], v[44:47], v[16:19]\n" \
  "s_waitcnt lgkmcnt(10)\nv_mfma_f32_16x16x32_f16 v[12:15], v[76:79], v[116:119], v[12:15]\nds_read_b64_tr_b16 v[92:93], v7 offset:1536\n" \
  "ds_read_b64_tr_b16 v[94:95], v7 offset:1600\nv_mfma_f32_16x16x32_f16 v[16:19], v[76:79], v[40:43], v[16:19]\n" \
  "v_mfma_f32_16x16x32_f16 v[16:19], v[124:127], v[120:123], v[16:19]\ns_waitcnt lgkmcnt(8)\n" \
  "v_mfma_f32_16x16x32_f16 v[16:19], v[72:75], v[116:119], v[16:19]\nv_mfma_f32_16x16x32_f16 v[12:15], v[96:99], v[32:35], v[12:15]\n" \
  "v_mfma_f32_16x16x32_f16 v[16:19], v[96:99], v[36:39], v[16:19]\ns_waitcnt lgkmcnt(4)\n" \
  "v_mfma_f32_16x16x32_f16 v[12:15], v[68:71], v[100:103], v[12:15]\nv_mfma_f32_16x16x32_f16 v[16:19], v[68:71], v[32:35], v[16:19]\n" \
  "s_waitcnt lgkmcnt(2)\nv_mfma_f32_16x16x32_f16 v[12:15], v[28:31], v[64:67], v[12:15]\n" \
  "v_mfma_f32_16x16x32_f16 v[16:19], v[28:31], v[100:103], v[16:19]\ns_waitcnt lgkmcnt(0)\n" \
  "v_mfma_f32_16x16x32_f16 v[16:19], v[92:95], v[64:67], v[16:19]\ns_nop 4\nv_cvt_pkrtz_f16_f32 v20, v12, v13\nv_cvt_pkrtz_f16_f32 v21, v14, v15\n" \
  "v_pk_add_u16 v20, v20, 4 op_sel_hi:[1,0] clamp\nv_pk_add_u16 v21, v21, 4 op_sel_hi:[1,0] clamp\nds_write_b64 v4, v[20:21] offset:6144\n" \
  "v_cvt_pkrtz_f16_f32 v20, v16, v17\nv_cvt_pkrtz_f16_f32 v21, v18, v19\nv_pk_add_u16 v20, v20, 4 op_sel_hi:[1,0] clamp\n" \
  "v_pk_add_u16 v21, v21, 4 op_sel_hi:[1,0] clamp\nds_write_b64 v4, v[20:21] offset:7168\ns_setprio 0\ns_setpc_b64 s[90:91]\n71:\n" \
  "s_movk_i32 s84, 0x4000\n73:\nds_read_b32 v23, v9 offset:32\ns_sub_u32 s84, s84, 1\ns_cbranch_scc1 72b\ns_waitcnt lgkmcnt(0)\n" \
  "v_readfirstlane_b32 s85, v23\ns_cmp_lt_u32 s85, 3\ns_cbranch_scc1 73b\ns_branch 72b\n"
#define CONV_CLOB "memory", "scc", "s84", "s85", "s90", "s91", "v12", "v13", "v14", "v15", "v16", "v17", "v18", "v19", "v20", "v21", "v22", "v23", "v24", "v25", "v26", "v27", "v28", "v29", "v30", "v31", "v32", "v33", "v34", "v35", "v36", "v37", "v38", "v39", "v40", "v41", "v42", "v43", "v44", "v45", "v46", "v47", "v48", "v49", "v50", "v51", "v52", "v53", "v54", "v55", "v56", "v57", "v58", "v59", "v60", "v61", "v62", "v63", "v64", "v65", "v66", "v67", "v68", "v69", "v70", "v71", "v72", "v73", "v74", "v75", "v76", "v77", "v78", "v79", "v80", "v81", "v82", "v83", "v84", "v85", "v86", "v87", "v88", "v89", "v90", "v91", "v92", "v93", "v94", "v95", "v96", "v97", "v98", "v99", "v100", "v101", "v102", "v103", "v104", "v105", "v106", "v107", "v108", "v109", "v110", "v111", "v112", "v113", "v114", "v115", "v116", "v117", "v118", "v119", "v120", "v121", "v122", "v123", "v124", "v125", "v126", "v127"
constexpr int TDP = 1216, T_3 = 4 * TDP, T_4 = T_3 + 512, TB = T_4 + 320;
constexpr int C_U = 480, UB = C_U + 4096 + 96, C_G5 = 0, C_G3 = 3200, C_G9 = 6400, C_G7 = C_G9 + 2096, GB = C_G7 + 2080 + 112;
constexpr int O_FL = 2 * TB, O_UL = O_FL + 64, O_IM = O_UL + 2 * UB, O_ST = O_IM + 2 * GB;
constexpr int X_TB = 10240, X_UL = X_TB + TB, X_IM = X_UL + UB, X_ST = X_IM + GB;
constexpr int X_WR = X_IM + 128 * IP;
#define X_RW(w) (LW + 9216 * (2 + ((w) >> 1)) + 2560 * ((w) & 1))
constexpr int RP = 1216;
constexpr int LW = 36864 + 512, LA = LW, LM = LW + 36864;
constexpr int L_SC = LM + 2048, SMEM = O_IM + 256 * IP;
static_assert(L_SC + 1152 <= SMEM && O_ST + 8192 <= SMEM && X_IM + 128 * IP <= LM && X_ST + 8192 <= LM && UB % 128 == 64 && GB % 128 == 64 && SMEM <= 79872, "");
struct TOps { uint32_t xa, xb, ya, pd, pf, pg, pe; };
__device__ __forceinline__ void table_ops(uint32_t im, uint32_t tb, int ct, int lane, TOps& t) {
  const int j = lane & 15, kq = lane >> 4, q4 = (lane >> 2) & 3, p4 = lane & 3, jj = j + 16 * ct;
  const uint32_t sa = im + (uint32_t)(IP * (lane >> 2));
  t.xa = sa + (uint32_t)(I_X + 8 * p4); t.xb = sa + (uint32_t)(I_X + 32 + 8 * (p4 & 1)); t.ya = sa + (uint32_t)(32 * ct + 8 * p4);
  t.pd = tb + (uint32_t)(2 * (541 - (8 * jj + 4 * (kq & 1) + 256 * (kq >> 1))));
  t.pf = tb + (uint32_t)((kq & 1) ? T_4 + 2 * (63 - jj) : T_3 + 2 * (127 - 2 * jj));
  t.pg = t.pf - ((kq & 1) ? 64 : 2);
  t.pe = tb + (uint32_t)(p4 * (TDP + 2) + ((lane < 12) ? 2 * (32 - q4) : 2 * (572 + (q4 & 1))));
  asm volatile("" : "+v"(t.xa), "+v"(t.xb), "+v"(t.ya), "+v"(t.pd), "+v"(t.pf), "+v"(t.pg), "+v"(t.pe));
}
typedef __attribute__((address_space(3))) char* lptr;
#define LP(T, a) ((__attribute__((address_space(3))) T*)(uintptr_t)(a))
__device__ __forceinline__ uint32_t table_tile(const TOps& t, int ct, int lane, uint32_t evp, float dsk TSP) {
  u32x4 ev;
  f32x4 ad[2], af[2];
  asm volatile(T_ASM : "=&v"(ad[0]), "=&v"(ad[1]), "=&v"(af[0]), "=&v"(af[1]), "=&v"(ev) : "v"(t.xa), "v"(t.xb), "v"(t.ya), "v"(evp) : T_CLOB);
  TS(0, 4);
  const int e0 = (int)(ev[0] + ev[2]), e1 = (int)(ev[1] + ev[3]);
  const int em = min(max(max(max(e0, e1), (int)((__float_as_uint(dsk) >> 23) & 0xffu) - 133), -110), 115);
  const int ts = ((uint32_t)(em + 12) <= 8u) ? 1 : -3 - em;
  float s0 = __builtin_amdgcn_ldexpf(1.f, e0 - 29 + ts), s1 = __builtin_amdgcn_ldexpf(1.f, e1 - 29 + ts);
  float d0 = (ct == 0 && lane == 0) ? __builtin_amdgcn_ldexpf(dsk, ts) : 0.f;
  asm volatile("s_nop 1" : "+v"(ad[0]), "+v"(ad[1]), "+v"(af[0]), "+v"(af[1]), "+v"(s0), "+v"(s1), "+v"(d0));
  const f32x4 k = ad[0] * s0 + (f32x4){d0, 0.f, 0.f, 0.f} + ad[1] * s1, f = af[0] * s0 + af[1] * s1;
  auto pk = [](float a, float b) { uint32_t r; asm("v_cvt_pk_f16_f32 %0, %1, %2" : "=v"(r) : "v"(a), "v"(b)); return r; };
  asm volatile("s_nop 0" : : "v"(k), "v"(f));
  const uint32_t p32 = pk(k[3], k[2]), p10 = pk(k[1], k[0]), p21 = pk(k[2], k[1]), p01 = pk(f[0], f[1]), p23 = pk(f[2], f[3]);
  *LP(u32x2, t.pd + 3 * TDP + 6) = (u32x2){p32, p10};
  *LP(uint32_t, t.pd + TDP + 2) = p32; *LP(uint32_t, t.pd + TDP + 6) = p10;
  *LP(uint32_t, t.pd + 2) = p21; *LP(uint32_t, t.pd + 2 * TDP + 6) = p21;
  *LP(u16, t.pd) = (u16)p32; *LP(u16, t.pd + 2 * TDP + 4) = (u16)p32;
  asm volatile("ds_write_b16_d16_hi %0, %1 offset:6\n\tds_write_b16_d16_hi %0, %1 offset:%2" : : "v"(t.pd), "v"(p10), "n"(2 * TDP + 10) : "memory");
  if (ct) {
    const uint32_t va = (uint32_t)__builtin_amdgcn_readlane((int)p32, 63), vb = (uint32_t)__builtin_amdgcn_readlane((int)p23, 15) >> 16;
    *LP(u16, t.pe) = (u16)((lane < 12) ? va : vb);
  }
  if (lane < 32) {
    *LP(u16, t.pf + 128) = (u16)p01; *LP(u16, t.pg + 128) = (u16)(p01 >> 16);
    *LP(u16, t.pf) = (u16)p23; *LP(u16, t.pg) = (u16)(p23 >> 16);
  }
  return (uint32_t)(127 + 5 - ts) << 23;
}
__device__ __forceinline__ uint32_t hadd(uint32_t a, uint32_t b) { return __builtin_bit_cast(uint32_t, __builtin_bit_cast(f16x2, a) + __builtin_bit_cast(f16x2, b)); }
template <bool FULL> __device__ __forceinline__ void sums(char* sm, const u32x4 p, const u32x4 c) {
  const uint32_t D[8] = {p[0], p[1], p[2], p[3], c[0], c[1], c[2], c[3]};
  uint32_t O[7];
#pragma unroll
  for (int j = 0; j < 7; ++j) O[j] = __builtin_amdgcn_alignbit(D[j + 1], D[j], 16);
  u32x4 a, b;
#pragma unroll
  for (int j = 0; j < 4; ++j) a[j] = hadd(D[j + 2], hadd(O[j + 1], O[j + 2]));
  *(u32x4*)(sm + C_G3 + 32) = a;
#pragma unroll
  for (int j = 0; j < 4; ++j) b[j] = hadd(a[j], hadd(D[j + 1], D[j + 3]));
  *(u32x4*)(sm + C_G5 + 32) = b;
  if (FULL) {
#pragma unroll
    for (int j = 0; j < 4; ++j) a[j] = hadd(b[j], hadd(O[j], O[j + 3]));
    *(u32x4*)(sm + C_G7 + 32) = a;
#pragma unroll
    for (int j = 0; j < 4; ++j) b[j] = hadd(a[j], hadd(D[j], D[j + 4]));
    *(u32x4*)(sm + C_G9 + 32) = b;
  }
}
__device__ __forceinline__ void count_up(uint32_t a) {
  uint32_t t;
  asm volatile("s_mov_b64 exec, 1\n\tv_mov_b32 %0, 1\n\tds_add_u32 %1, %0\n\ts_mov_b64 exec, -1" : "=&v"(t) : "v"(a) : "memory");
}
__device__ __forceinline__ int pad_at(int r) { return 16 * (r & 1) + 3200 * (r >> 1) - ((r >= 6) ? 3200 - 2096 : 0); }
struct COps { uint32_t b, a, o, g3, k3, g4, k4, f; };
__device__ __forceinline__ void conv_ops(uint32_t tb, uint32_t ub, uint32_t cb, int lane, COps& e) {
  const uint32_t j = lane & 15, kq = lane >> 4, q4 = (lane >> 2) & 3, p4 = lane & 3;
  e.b = ub + 32u * j + 16u * kq;
  e.a = tb + p4 * TDP + 32u - 8u * q4 + 16u * kq;
  e.g3 = cb + C_G5 + 8 + 64u * kq + 8u * (q4 + p4) + (q4 & 1u) * 3200u;
  e.g4 = cb + C_G9 + 8 + 128u * kq + 16u * q4 + 8u * p4 + (q4 & 1u) * 2096u;
  e.k3 = tb + T_3 + 120 - 8u * j + 16u * kq;
  e.k4 = tb + T_4 + 60 - 4u * j + 16u * kq;
  asm volatile("" : "+v"(e.b), "+v"(e.a), "+v"(e.g3), "+v"(e.g4), "+v"(e.k3), "+v"(e.k4));
}
#define CONV_RUN(prog, e, ...) do { int prog_ = (prog); \
    asm volatile("s_call_b64 s[90:91], kf_conv" : "+{s89}"(prog_), "+{v2}"(e.b), "+{v3}"(e.a), "+{v4}"(e.o), "+{v5}"(e.g3), "+{v6}"(e.k3), "+{v7}"(e.g4), \
                 "+{v8}"(e.k4), "+{v9}"(e.f) __VA_ARGS__ : : CONV_CLOB); } while (0)

template <bool E>
__device__ __forceinline__ void ln_stats(const uint32_t* v, int lane, float sc, f32x2* t, f32x4 z, f32x2& rc, f32x2& nc, float& e) {
  float s1a = z[0], s2a = z[1], s1b = z[2], s2b = z[3];
#pragma unroll
  for (int k = 0; k < 4; ++k) {
    s1a = dot2(v[k], 0x3f803f80u, s1a); s2a = dot2(v[k], v[k], s2a);
    s1b = dot2(v[4 + k], 0x3f803f80u, s1b); s2b = dot2(v[4 + k], v[4 + k], s2b);
  }
#pragma unroll
  for (int k = 0; k < 9; ++k) t[k] = (f32x2){bflo(v[k]), bfhi(v[k])};
  const float rs = row_sum(fold16(dot2(v[8], 0x3f803f80u, fold32(s1a, s1b)), dot2(v[8], v[8], fold32(s2a, s2b))));
  if (E) { e = row_sum(e); e += __int_as_float(__builtin_amdgcn_update_dpp(0, __float_as_int(e), 0x142, 0xa, 0xf, false)); }
  const f32x2 mean = (f32x2){rdl(rs, 15), rdl(rs, 47)} * (1.f / D_), m2 = mean * mean;
  f32x2 var = (f32x2){rdl(rs, 31), rdl(rs, 63)} * (1.f / D_) - m2;
  asm volatile("" : "+v"(t[0]), "+v"(t[1]), "+v"(t[2]), "+v"(t[3]), "+v"(t[4]), "+v"(t[5]), "+v"(t[6]), "+v"(t[7]), "+v"(t[8]));
  if (__builtin_expect(!(m2[0] <= 16.f * var[0] && m2[1] <= 16.f * var[1]), 0)) {
    float q[3] = {0.f, 0.f, 0.f};
#pragma unroll
    for (int k = 0; k < 9; ++k) {
      const f32x2 a = t[k] - ((k < 4 || (k == 8 && lane < 32)) ? mean[0] : mean[1]);
      q[k >> 2] += a[0] * a[0] + a[1] * a[1];
    }
    float qa = q[0] + ((lane < 32) ? q[2] : 0.f), qb = q[1] + ((lane < 32) ? 0.f : q[2]);
    wave_sum2(qa, qb);
    var = (f32x2){qa, qb} * (1.f / D_);
  }
  const uint32_t mp = pack2(mean[0], mean[1]), vp = pack2(var[0], var[1]);
  const f32x2 ve = (f32x2){bflo(vp), bfhi(vp)} + 1e-5f;
  const uint32_t e2 = pack2(ve[0], ve[1]);
  const uint32_t sp = pack2(__builtin_amdgcn_sqrtf(bflo(e2)), __builtin_amdgcn_sqrtf(bfhi(e2)));
  rc = (f32x2){__builtin_amdgcn_rcpf(bflo(sp)), __builtin_amdgcn_rcpf(bfhi(sp))} * sc;
  nc = -(f32x2){bflo(mp), bfhi(mp)} * rc;
}
template <bool H>
__device__ __forceinline__ void ln_apply(const f32x2* t, int lane, f32x2 rc, f32x2 nc, uint32_t* c) {
  const float nm[3] = {nc[0], nc[1], (lane < 32) ? nc[0] : nc[1]}, rr[3] = {rc[0], rc[1], (lane < 32) ? rc[0] : rc[1]};
  f32x2 dd[9];
#pragma unroll
  for (int k = 0; k < 9; ++k) dd[k] = __builtin_elementwise_fma(t[k], (f32x2){rr[k >> 2], rr[k >> 2]}, (f32x2){nm[k >> 2], nm[k >> 2]});
  asm volatile("" : "+v"(dd[0]), "+v"(dd[1]), "+v"(dd[2]), "+v"(dd[3]), "+v"(dd[4]), "+v"(dd[5]), "+v"(dd[6]), "+v"(dd[7]), "+v"(dd[8]));
#pragma unroll
  for (int k = 0; k < 9; ++k) {
    const f32x2 d = dd[k];
    typedef __attribute__((ext_vector_type(2))) uint16_t u16x2;
    if (H) c[k] = __builtin_bit_cast(uint32_t, __builtin_elementwise_add_sat(__builtin_bit_cast(u16x2, __builtin_amdgcn_cvt_pkrtz(d[0], d[1])), (u16x2){4, 4}));
    else c[k] = pack2(d[0], d[1]);
  }
}
__device__ __forceinline__ uint32_t med3u(uint32_t a, uint32_t b, uint32_t c) { return max(min(a, b), min(max(a, b), c)); }
__device__ __forceinline__ uint32_t oct_maxu(uint32_t v) {
  v = max(v, (uint32_t)DPP_I((int)v, 0xb1, 0xf));
  v = max(v, (uint32_t)DPP_I((int)v, 0x4e, 0xf));
  v = max(v, (uint32_t)DPP_I((int)v, 0x141, 0xf));
  return v;
}

#define KARGS const u16* __restrict__ x, const u16* __restrict__ Are, const u16* __restrict__ Aim, const u16* __restrict__ Bre, \
    const u16* __restrict__ Bim, const u16* __restrict__ Cre, const u16* __restrict__ Cim, const u16* __restrict__ logdt, \
    const u16* __restrict__ Dskip, char* __restrict__ ws, const u16* __restrict__ wr, const u16* __restrict__ wg, \
    const u16* __restrict__ wu, const u16* __restrict__ wd, u16* __restrict__ out
#define KPASS x, Are, Aim, Bre, Bim, Cre, Cim, logdt, Dskip, ws, wr, wg, wu, wd, out
template <int ROLE, int FFN = -1>
__device__ __forceinline__ void role_body(char* smem, KARGS) {
  const int tid = threadIdx.x, lane = tid & 63;
  const int w = __builtin_amdgcn_readfirstlane(tid >> 6), wl = w & 3;
  const int b = blockIdx.x & 255, bx = b & 7, bj = b >> 3;
  constexpr bool isl = ROLE == 1 || ROLE == 2;
  const int tg = isl ? 32 * (bj >> 2) + 4 * bx + (bj & 3) : 32 * bx + bj;
  u32x4 xa, xc;
  uint32_t ep, r0;
#define P_WAIT(n, ...) asm volatile("s_waitcnt vmcnt(" #n ")\n\tv_lshlrev_b32 %0, 16, v100\n\tv_lshlrev_b32 %1, 16, v101\n\tv_lshlrev_b32 %2, 16, v102" \
    : "=v"(ldt), "=v"(are), "=v"(aim) : : "v100", "v101", "v102" __VA_ARGS__)
#define P_MORE() asm volatile("v_lshlrev_b32 %0, 16, v103\n\tv_lshlrev_b32 %1, 16, v104\n\tv_lshlrev_b32 %2, 16, v105\n\tv_lshlrev_b32 %3, 16, v106" \
    : "=v"(bre), "=v"(bim), "=v"(cre), "=v"(cim) : : "v103", "v104", "v105", "v106")
  TSD;
  TS(0, 0);
  const uint32_t lb0 = (uint32_t)(uintptr_t)(__attribute__((address_space(3))) char*)smem;
  const int i = lane & 15, kq = lane >> 4;
  const u32x4 ones = {0xffffffffu, 0xffffffffu, 0xffffffffu, 0xffffffffu}, zero4 = {0u, 0u, 0u, 0u};
  auto u_again = [&](u32x4& uw, const u32x4& ut, uint32_t uo) {
#pragma unroll 1
    for (int spin = 0; spin < SPIN_CAP; ++spin) {
      const bool na = ((uw[0] | uw[1] | uw[2] | uw[3]) & UMASK) != 0u;
      if (__builtin_expect(!__any(na), 1)) break;
      TSC(19);
      SLEEP(USLEEP);
      if (na) { LD16W(uw, uo); uw ^= ut; }
    }
  };
  constexpr uint32_t S16 = 0x41800000u, LMASK = 0x00070007u;
  auto v_put = [&](u32x4 v, const u32x4& ut, bool other, uint32_t vo) {
    const bool second = __any(other || ((v[0] | v[1] | v[2] | v[3]) & 0x40004000u) != 0u);
    if (__builtin_expect(second, 0)) v = (v & ~LMASK) | (~ut & LMASK); else v = (v & ~LMASK) | ut;
    ST16(vo, v);
    return second;
  };
  auto v_end = [&](uint32_t T, int n) { DRAIN(); if (lane == 0) ST4(O_DN + 16u * n, T); };
  if constexpr (!isl) {
    const int ch = wl >> 1, tl = tid & 255;
    int* evp = (int*)(smem + O_FL + 16 * ch);
    float ldt, are, aim, dsk;
    if constexpr (ROLE == 0) {
      __builtin_amdgcn_s_setprio(KPRIO);
      COps e;
      conv_ops(lb0 + (uint32_t)(ch * TB), lb0 + (uint32_t)(O_UL + ch * UB), lb0 + (uint32_t)(O_IM + ch * GB), lane, e);
      e.o = lb0 + O_ST + (uint32_t)(16 * ((4 * i + 2 * (kq >> 1) + ch) ^ (i >> 1)) + 8 * (kq & 1));
      e.f = lb0 + O_FL;
      TOps t;
      table_ops(lb0 + (uint32_t)(O_IM + 128 * IP * ch), lb0 + (uint32_t)(TB * ch), wl & 1, lane, t);
      uint32_t pz = lb0 + O_IM + (uint32_t)(((lane >> 3) & 1) * GB + pad_at(lane & 7)), im = lb0 + (uint32_t)(O_IM + IP * tl);
      asm volatile("" : "+v"(e.o), "+v"(e.f), "+v"(pz), "+v"(im));
      float bre, bim, cre, cim;
      P_WAIT(0, , "v103", "v104", "v105", "v106", "v107", "v109");
      asm volatile("" : "={v109}"(ep));
      const uint32_t T = ((uint32_t)__builtin_amdgcn_readfirstlane(ep) + 1u) * 0x9E3779B1u;
      if (w == 3 && lane < 8) ST16(O_KEY + 128u * b + 16u * lane, ones);
      P_MORE();
      asm volatile("v_lshlrev_b32 %0, 16, v107" : "=v"(dsk) : : "v107");
      TS(0, 1);
      evp[wl & 1] = gen_y(ldt, are, aim, bre, bim, cre, cim, im, [] {} TSA);
      TS(0, 8);
      BAR();
      TS(0, 2);
      const uint32_t cs = table_tile(t, wl & 1, lane, lb0 + O_FL + 16 * ch, dsk TSA);
      *(uint32_t*)(smem + O_FL + 40 + 4 * ch) = cs;
      DRAIN();
      if ((wl & 1) && lane == 0) asm volatile("global_store_dwordx2 %0, %1, %2 sc1" : : "v"(O_SC + 16u * (2 * tg + ch)), "v"((u32x2){T, cs}), "s"(ws) : "memory");
      TS(0, 9);
      gsync0(lb0 + O_FL + 36, 4);
      TS(0, 3);
      *(u32x4*)(smem + O_ST + 16 * tl) = ones;
      *(u32x4*)(smem + O_ST + 4096 + 16 * tl) = ones;
      if (w == 0) *LP(u32x4, pz) = zero4;
      CONV_RUN(wl & 1, e);
      TS(0, 5);
    } else {
      __builtin_amdgcn_s_setprio(UPRIO);
#pragma unroll
      for (int k = 0; k < 3; ++k) *(u32x4*)(smem + ((16 * (tl + 256 * k) < O_FL) ? 16 * (tl + 256 * k) : O_FL + 32)) = zero4;
      if (w == 7) *(u32x4*)(smem + O_UL + (lane >> 5) * UB + 16 * min(lane & 31, 29)) = zero4;
      uint32_t uo = u_at(tl >> 1, 2 * tg + (tid & 1)), go = O_SU + (uint32_t)(256 * bx + 32 * wl + (lane & 31) + 4 * (lane & 32)) * 128u;
      uint32_t ul = (uint32_t)(O_UL + (tid & 1) * UB + C_U - 16 + 16 * (tl >> 1)), ug = (uint32_t)(O_IM + (tid & 1) * GB + 16 * (tl >> 1)), so = lb0 + O_ST + 16u * (uint32_t)(lane ^ (lane >> 3));
      uint32_t vo = O_V + (uint32_t)tg * VP + 16u * lane;
      asm volatile("" : "+v"(uo), "+v"(go), "+v"(ul), "+v"(ug), "+v"(so), "+v"(vo));
      P_WAIT(1, , "v109");
      TS(0, 1);
      evp[2 + (wl & 1)] = gen_x(ldt, are, aim, smem + O_IM + IP * tl);
      asm volatile("s_waitcnt vmcnt(0)" : "={v109}"(ep));
      const uint32_t T = ((uint32_t)__builtin_amdgcn_readfirstlane(ep) + 1u) * 0x9E3779B1u;
      TS(0, 8);
      BAR();
      TS(0, 2);
      u32x4 ut = {utag(T, 0), utag(T, 1), utag(T, 2), utag(T, 3)};
      asm volatile("" : "+v"(ut));
      SLEEP(GSLEEP);
      u32x2 pv;
      pv[1] = gate(ws, go, T);
      TS(0, 3);
      until(latest(pv[1]) + UD);
      TS(0, 7);
      u32x4 uw, ux;
      asm volatile("global_load_dwordx4 %0, %2, %4 sc1\n\tglobal_load_dwordx4 %1, %3, %4 sc1"
                   : "=&v"(uw), "=&v"(ux) : "v"(uo), "v"(uo + 64 * UP * 32), "s"(ws) : "memory");
      __builtin_amdgcn_s_setprio(FPR);
      asm volatile("s_waitcnt vmcnt(1)" : "+v"(uw));
      uw ^= ut;
      *(u32x4*)(smem + ul + 16) = uw;
      asm volatile("s_waitcnt vmcnt(0)" : "+v"(ux));
      TS(0, 11);
      ux ^= ut;
      *(u32x4*)(smem + ul + 2048 + 16) = ux;
      if (__builtin_expect(__any(((uw[0] | uw[1] | uw[2] | uw[3] | ux[0] | ux[1] | ux[2] | ux[3]) & UMASK) != 0u), 0)) {
        u_again(uw, ut, uo);
        u_again(ux, ut, uo + 64 * UP * 32);
        *(u32x4*)(smem + ul + 16) = uw;
        *(u32x4*)(smem + ul + 2048 + 16) = ux;
      }
      BAR();
      TS(0, 4);
      const u32x4 pw = *(const u32x4*)(smem + ul);
      if (wl < 2) {
        const u32x4 px = *(const u32x4*)(smem + ul + 2048);
        sums<true>(smem + ug, pw, uw);
        sums<false>(smem + ug + 2048, px, ux);
      } else sums<true>(smem + ug, pw, uw);
      count_up(lb0 + O_FL + 32);
      __builtin_amdgcn_s_setprio(SPR);
      TS(0, 5);
      const bool other = *(const uint32_t*)(smem + O_FL + 40 + 4 * (lane & 1)) != S16;
      bool second = false;
#pragma unroll
      for (int k = 0; k < 2; ++k) {
        const uint32_t o = ((k ? 0x3247u : 0x1065u) >> (4 * wl) & 7u) * 1024u;
        u32x4 v;
#pragma unroll 1
        for (int spin = 0; spin < SPIN_CAP; ++spin) {
          asm volatile("ds_read_b128 %0, %1\n\ts_waitcnt lgkmcnt(0)" : "=&v"(v) : "v"(so + o) : "memory");
          if (!__any(max4(v) == 0xffffffffu)) break;
          __builtin_amdgcn_s_sleep(VPS);
        }
        TS(0, 12 + k);
        second |= v_put(v, ut, other, vo + o);
        TS(0, 10 - 4 * k);
      }
      if (__builtin_expect(second, 0)) v_end(T, 4 * tg + wl);
    }
    TSE();
  } else {
  uint32_t* cand = (uint32_t*)(smem + LM);
  uint32_t* sel = cand + 128;
  uint32_t* kl = sel + 48;
  uint32_t* cnt = sel + 64;
  const uint32_t ca = lb0 + LM + 4 * (128 + 64);
  u16* hm = (u16*)(cnt + 12);
  uint32_t T;
  const bool ffn = (FFN < 0) ? bj < 18 : FFN, isx = FFN != 1 && bj >= 24;
  const int fb = 18 * bx + bj;
  constexpr bool ln2 = ROLE == 2;
  const int g0 = tg, g2 = tg;
  u32x4 bw[18];
#pragma unroll
  for (int s = 0; s < 9; ++s) bw[s] = (u32x4){0u, 0u, 0u, 0u};
  int tid2 = threadIdx.x;
  uint32_t ts1 = 0u;
  auto rest = [&] {
    if (ts1 && (int)((uint32_t)__builtin_amdgcn_s_memrealtime() - ts1) > 0) { TS(0, 8); until(ts1 + TSTOP + RD); if (RW) gwait(ca, 4); ts1 = 0u; }
  };
  auto gu_dma = [&](int q0, int q1, auto depth) {
    const char* gs = (const char*)((w < 2) ? wg : wu) + (size_t)((144 * (w & 1)) * (F_ * 2) + fb * 64);
    const uint32_t gv = (uint32_t)((lane >> 2) * (F_ * 2) + 16 * ((lane & 3) ^ (lane >> 5) ^ (w & 2)));
#pragma unroll 1
    for (int q = q0; q < q1; ++q) {
      const int k = (int)((((bj & 1) ? ORD1 : ((unsigned __int128)0x876543210u << 36 | 0x876543210ull)) >> (4 * (q - (q >= 9 ? 9 : 0)) + (q >= 9 ? 36 : 0))) & 15u), h = q >= 9;
      if (q >= RQ) rest();
      dma16(gs + (size_t)(288 * h + 16 * k) * (F_ * 2), gv, lb0 + LW * h + 9216 * w + 1024 * k);
      asm volatile("s_waitcnt vmcnt(%0)" : : "n"(depth.value) : "memory");
    }
  };
  auto down_dma = [&](int k0, int k1, int lane) {
#pragma unroll 1
    for (int k = k0; k < k1; ++k) {
      const int o = 9216 * wl + 2304 * k;
      const char* ds = (const char*)wd + (size_t)(fb * 36864 + o);
      const uint32_t dl = lb0 + o + 32 * ((4 * wl + k + 1) >> 1);
      if (ROLE == 1) rest();
      dma16d(ds, 16u * lane, dl);
      dma16d(ds + 1024, 16u * lane, dl + 1024);
      uint32_t keep;
      asm volatile("s_mov_b32 %0, m0\n\ts_mov_b32 m0, %3\n\ts_mov_b64 exec, 0xffff\n\tglobal_load_lds_dwordx4 %1, %2" DPOL2 "\n\ts_mov_b64 exec, -1\n\ts_mov_b32 m0, %0\n\ts_waitcnt vmcnt(%4)"
                   : "=&s"(keep) : "v"(16u * lane), "s"(ds + 2048), "s"(dl + 2048), "n"(ROLE == 1 ? DDEPTH : 40) : "memory");
    }
  };
  auto gu_read = [&](int h) {
    const uint32_t ra = lb0 + LW * h + (uint32_t)(((lane >> 1) & 1) * 18432 + (8 * kq + ((lane >> 2) & 3)) * 64 + ((w ^ (kq & 1) ^ (lane & 2)) * 16) + 8 * (lane & 1));
#pragma unroll
    for (int s = 0; s < 9; ++s) {
      const u32x2 h0 = trd(ra + 2048 * s), h1 = trd(ra + 2048 * s + 256);
      bw[9 * h + s] = (u32x4){h0[0], h0[1], h1[0], h1[1]};
    }
#pragma unroll
    for (int s = 0; s < 9; ++s) asm volatile("" : "+v"(bw[9 * h + s]));
  };
  uint32_t tv = 0u, tk = 0u;
  const uint32_t vearly = (uint32_t)((VEARLY >> (8 * (g0 >> 5))) & 0xffull);
#define TOUCH(pa) asm volatile("global_load_dword v126, %0, off" DPOL4 : : "v"(pa) : "memory", "v126")
  uint32_t wv[5], xv[9];
  auto rows_xw = [&](int lane) {
    u32x4 wa, xr, xs;
    const uint32_t xo = (uint32_t)((8 * g2 + 2 * wl) * (D_ * 2));
    asm volatile("global_load_dwordx4 %0, %5, %9\n\tglobal_load_dword %1, %6, %9 offset:1024\n\t"
                 "global_load_dwordx4 %2, %7, %10\n\tglobal_load_dwordx4 %3, %7, %10 offset:1152\n\tglobal_load_dword %4, %8, %10 offset:1024\n\ts_waitcnt vmcnt(0)"
                 : "=&v"(wa), "=&v"(wv[4]), "=&v"(xr), "=&v"(xs), "=&v"(xv[8])
                 : "v"(16u * lane), "v"(4u * (lane & 31)), "v"(xo + 16u * lane), "v"(xo + 4u * lane + 32u * (lane & 32)), "s"(wr), "s"(x) : "memory");
#pragma unroll
    for (int k = 0; k < 4; ++k) { wv[k] = wa[k]; xv[k] = xr[k]; xv[4 + k] = xs[k]; }
  };
  auto ln1 = [&](int g, int o, uint32_t c0) {
    uint32_t uo = u_at(g, 64 * wl + lane);
    uint32_t ta = lb0 + (uint32_t)(o + ((lane >> 2) & 3) * RP + (64 * wl + 16 * kq + 4 * (lane & 3)) * 2);
    uint32_t la = (uint32_t)(o + 2 * wl * RP + 16 * lane), lt = (uint32_t)(o + (2 * wl + (lane >> 5)) * RP + 1024 + 4 * (lane & 31));
    uint32_t sg = O_SU + (uint32_t)(256 * (lane >> 3) + g) * 128u + 16u * (lane & 7);
    asm volatile("" : "+v"(uo), "+v"(ta), "+v"(la), "+v"(lt), "+v"(sg));
    f32x4 z4 = {0.f, 0.f, 0.f, 0.f};
    asm volatile("" : "+v"(z4));
    asm volatile("s_waitcnt vmcnt(0)" : "={v[100:103]}"(xa), "={v[104:107]}"(xc), "={v108}"(r0), "={v109}"(ep));
    TS(0, 1);
    const uint32_t v[9] = {xa[0], xa[1], xa[2], xa[3], xc[0], xc[1], xc[2], xc[3], r0};
    uint32_t c[9];
    f32x2 tf[9];
    T = ((uint32_t)__builtin_amdgcn_readfirstlane(ep) + 1u) * 0x9E3779B1u;
    if (wl == 3) { const uint32_t now = (uint32_t)__builtin_amdgcn_s_memrealtime() + SD; ST16(sg, ((u32x4){T, now, T, now})); }
    const uint32_t tj = ((T >> (3 * wl)) & 0x00070007u) | (((T >> (12 + wl)) & 0x00010001u) << 14);
    uint32_t tw[3] = {(tj & 0xffffu) * 0x10001u, (tj >> 16) * 0x10001u, 0u};
    tw[2] = (lane < 32) ? tw[0] : tw[1];
    asm volatile("" : "+v"(tw[0]), "+v"(tw[1]), "+v"(tw[2]));
    f32x2 rc, nc;
    float e_;
    ln_stats<false>(v, lane, 0.03125f, tf, z4, rc, nc, e_);
    ln_apply<true>(tf, lane, rc, nc, c);
#pragma unroll
    for (int k = 0; k < 9; ++k) c[k] = (c[k] & ~UMASK) | tw[k >> 2];
    *(u32x4*)(smem + la) = (u32x4){c[0], c[1], c[2], c[3]};
    *(u32x4*)(smem + la + RP) = (u32x4){c[4], c[5], c[6], c[7]};
    *(uint32_t*)(smem + lt) = c[8];
    u32x4 ha, hb;
    {
      uint32_t t, cc, n = SPIN_CAP;
      asm volatile("s_mov_b64 exec, 1\n\tv_mov_b32 %2, 1\n\tds_add_u32 %5, %2\n\ts_mov_b64 exec, -1\n"
                   "1:\n\tds_read_b32 %2, %5\n\tds_read_b64_tr_b16 v[40:41], %6\n\tds_read_b64_tr_b16 v[42:43], %6 offset:%7\n\tds_read_b64_tr_b16 v[44:45], %6 offset:512\n\t"
                   "ds_read_b64_tr_b16 v[46:47], %6 offset:%8\n\ts_sub_u32 %4, %4, 1\n\ts_cbranch_scc1 2f\n\ts_waitcnt lgkmcnt(4)\n\tv_readfirstlane_b32 %3, %2\n\t"
                   "s_cmp_lt_u32 %3, 4\n\ts_cbranch_scc1 1b\n2:\n\ts_waitcnt lgkmcnt(0)"
                   : "=&{v[40:43]}"(ha), "=&{v[44:47]}"(hb), "=&v"(t), "=&s"(cc), "+s"(n) : "v"(c0), "v"(ta), "n"(4 * RP), "n"(512 + 4 * RP) : "memory", "scc");
    }
    ST16(uo, ha);
    ST16(uo + 64 * 128, hb);
    if (wl == 0) {
      const u32x2 h4 = trd(ta + 1024), h5 = trd(ta + 1024 + 4 * RP);
      ST16(uo + 128 * 128, ((u32x4){h4[0], h4[1], h5[0], h5[1]}));
    }
  };
  const int dX = 512 - 24 + 8 * bx + bj;
  if constexpr (ROLE == 2) {
    const int tl = tid - 256;
    if (isx) {
      __builtin_amdgcn_s_setprio(QPRIO0);
      *(u32x4*)(smem + X_TB + 16 * tl) = zero4;
      *(u32x4*)(smem + X_TB + 16 * min(256 + tl, (TB + C_U) / 16 - 1)) = zero4;
      const int pr = (wl + XROT) & 3;
      COps e;
      conv_ops(lb0 + X_TB, lb0 + X_UL, lb0 + X_IM, lane, e);
      TOps t;
      table_ops(lb0 + X_IM, lb0 + X_TB, wl & 1, lane, t);
      e.o = lb0 + X_ST + (uint32_t)(16 * ((2 * i + (kq >> 1)) ^ ((i >> 2) & 1)) + 8 * (kq & 1));
      e.f = ca + 28 - 32;
      asm volatile("" : "+v"(e.o), "+v"(e.f));
      float ldt, are, aim, bre, bim, cre, cim, dsk;
      P_WAIT(0, , "v103", "v104", "v105", "v106", "v107", "v109");
      asm volatile("" : "={v109}"(ep));
      T = ((uint32_t)__builtin_amdgcn_readfirstlane(ep) + 1u) * 0x9E3779B1u;
      P_MORE();
      asm volatile("v_lshlrev_b32 %0, 16, v107" : "=v"(dsk) : : "v107");
      TS(0, 1);
      __builtin_amdgcn_s_setprio(QPRIO);
      int* evp = (int*)(sel + 44);
      char* im = smem + X_IM + IP * (tl & 127);
      evp[wl] = (wl < 2) ? gen_y(ldt, are, aim, bre, bim, cre, cim, lb0 + X_IM + IP * tl, [] {} TSA) : gen_x(ldt, are, aim, im);
      TS(0, 2);
      __builtin_amdgcn_s_setprio(QPRIO2);
      gsync(ca + 8, 4);
      if (wl < 2) evp[-4 + wl] = (int)table_tile(t, wl & 1, lane, lb0 + LM + 4 * (128 + 44), dsk TSA);
      if (wl < 2) gsync0(ca + 8, 8); else { count_up(ca + 8); SLEEP(XSL); gwait(ca + 8, 8); }
      const uint32_t cs = (uint32_t)__builtin_amdgcn_readfirstlane(evp[-4]);
      if (wl == 2 && lane == 0) asm volatile("global_store_dwordx2 %0, %1, %2 sc1" : : "v"(O_SC + 16u * dX), "v"((u32x2){T, cs}), "s"(ws) : "memory");
      if (wl == 3) *(u32x4*)(smem + X_IM + pad_at(lane & 7)) = zero4;
      TS(0, 3);
      CONV_RUN(2 + pr, e, , "+{v11}"(tid2));
      {
        const int lane = tid2 & 63;
        const u32x4 v = *(const u32x4*)(smem + X_ST + 2048 * pr + 1024 * (lane >> 5) + 16 * ((lane & 31) ^ ((lane >> 3) & 1)));
        if (__builtin_expect(v_put(v, (u32x4){utag(T, 0), utag(T, 1), utag(T, 2), utag(T, 3)}, cs != S16, v_at(dX, 64 * pr + lane)), 0)) {
          DRAIN();
          if (lane == 0) ST4(O_DN + 16u * (4 * (dX - 256) + pr), T);
        }
      }
      TS(0, 6);
    }
    __builtin_amdgcn_s_setprio(PPRIO);
    asm volatile("" : "+v"(tid2));
    if (isx) {
      const int lane = tid2 & 63;
      const u32x4 ra = *(const u32x4*)(smem + X_RW(wl) + 16 * lane), rb = *(const u32x4*)(smem + X_RW(wl) + 1024 + 16 * lane), rw = *(const u32x4*)(smem + X_WR + 16 * lane);
      const u32x2 rc = *(const u32x2*)(smem + X_RW(wl) + 2048 + 8 * lane);
#pragma unroll
      for (int k = 0; k < 4; ++k) { xv[k] = ra[k]; xv[4 + k] = rb[k]; wv[k] = rw[k]; }
      xv[8] = rc[0]; wv[4] = rc[1];
    } else rows_xw(tid2 & 63);
    if (!isx && !ffn) {
      SLEEP(RSLEEP);
#pragma unroll
      for (int k = 0; k < 7; ++k) ST16(O_SL + (uint32_t)((6 * bx + bj - 18) * 1792 + 256 * k - 256 + tid2) * 16u, ones);
      DRAIN();
    }
    if (!isx) BAR();
    const uint32_t tp = (uint32_t)__builtin_amdgcn_readfirstlane(cnt[4]);
    if (!isx) T = (uint32_t)__builtin_amdgcn_readfirstlane(sel[43]);
    tv = tp + UD + VD2 - vearly;
  } else {
    __builtin_amdgcn_s_setprio(3);
    if (tid < 12) cnt[tid] = 0u;
    BAR();
    if (tid < 20) ((uint32_t*)(hm + 320))[tid] = 0u;
    if (tid >= 184) *(u32x4*)(smem + L_SC - 16 * 184 + 16 * tid) = (u32x4){0x41804180u, 0x41804180u, 0x41804180u, 0x41804180u};
    uint32_t sink = 0;
    ln1(tg, 0, ca + 12);
    TS(0, 2);
    __builtin_amdgcn_s_setprio(NPRIO);
    asm volatile("s_waitcnt vmcnt(0)" : "+v"(sink) : : "memory");
    TS(0, 13);
    if (w == 0) {
      const uint32_t xr = O_XC + (uint32_t)((511 - 2 * (g0 + w)) * 144 + lane) * 16u;
      ST16(xr, ones);
      ST16(xr + 1024u, ones);
      if (lane < 16) ST16(xr + 2048u, ones);
    }
    if (w == 2 && b < 72 && lane < 8) ST16(O_FB + (uint32_t)(8 * b + lane) * 16u, ones);
    if (isx) {
      rows_xw(lane);
      char* sp = smem + X_RW(w) + 16 * lane;
      *(u32x4*)sp = (u32x4){xv[0], xv[1], xv[2], xv[3]};
      *(u32x4*)(sp + 1024) = (u32x4){xv[4], xv[5], xv[6], xv[7]};
      *(u32x2*)(smem + X_RW(w) + 2048 + 8 * lane) = (u32x2){xv[8], wv[4]};
      if (w == 0) *(u32x4*)(smem + X_WR + 16 * lane) = (u32x4){wv[0], wv[1], wv[2], wv[3]};
    }
    SLEEP(ESLEEP);
    uint32_t tu = latest(gate(ws, O_SU + (uint32_t)(256 * bx + 64 * w + 4 * i + (bj & 3)) * 128u, T));
    asm volatile("s_mov_b64 exec, 1\n\tds_max_u32 %0, %1\n\ts_mov_b64 exec, -1" : : "v"(ca + 16), "v"(tu) : "memory");
    if (w == 0) sel[43] = T;
    if (isx) {
      const uint32_t xo = u_at(tid, dX);
      char* sm = smem + X_UL + C_U - 16 + 16 * tid;
      const u32x4 ut = {utag(T, 0), utag(T, 1), utag(T, 2), utag(T, 3)};
      __builtin_amdgcn_s_setprio(3);
      until(tu + UD + XD);
      u32x4 uw;
      asm volatile("global_load_dwordx4 %0, %1, %2 sc1" : "=&v"(uw) : "v"(xo), "s"(ws) : "memory");
      asm volatile("s_waitcnt vmcnt(0)" : "+v"(uw));
      uw ^= ut;
      u_again(uw, ut, xo);
      *(u32x4*)(sm + 16) = uw;
      BAR();
      if (w < 3) {
        const u32x4 pw = *(const u32x4*)sm;
        if (w < 2) sums<true>(smem + X_IM + 16 * tid, pw, uw); else sums<false>(smem + X_IM + 16 * tid, pw, uw);
        count_up(ca + 28);
      }
    } else {
      until(tu + UD + WD);
      BAR();
    }
    tu = (uint32_t)__builtin_amdgcn_readfirstlane(cnt[4]);
    TS(0, 3);
    __builtin_amdgcn_s_setprio(DPRIO);
    if (ffn) {
      ts1 = (tu + UD + VD2 - TSTOP) | 1u;
      gu_dma(0, N0, IC<40>{});
      gu_dma(N0, NA, IC<WDEPTH>{});
      asm volatile("s_waitcnt vmcnt(%0)" : : "n"(NA - 9) : "memory");
      gsync0(ca + 4, 4);
      gu_read(0);
      asm volatile("s_waitcnt lgkmcnt(0)" ::: "memory");
      count_up(ca + 36);
      TS(0, 4);
      gu_dma(NA, 18, IC<WDEPTH2>{});
      gwait(ca + 36, 4);
      down_dma(0, KN, lane);
      asm volatile("s_waitcnt vmcnt(%0)" : : "n"(3 * KN) : "memory");
      count_up(ca + 4);
      TS(0, 7);
    } else {
      if (!isx && C3) {
        const int n = 6 * min(tid, 191) + bj - 18, r = n >> 2;
        const char* wb = (const char*)((n & 1) ? wu : wg) + bx * 1152;
        const int c0 = (3 - (int)((uintptr_t)wb >> 7)) & 7;
        wb += (uint32_t)((r + 144 * ((r >= 144) + ((n >> 1) & 1))) * (F_ * 2) + 128 * c0);
        TOUCH(wb);
        if (c0 == 0) TOUCH(wb + 1024);
      }
      auto d_line = [&](int k) {
        const int n = min((k * 14 + bj - 18) * 64 + lane, 5183), q = n / 1296, r = n - 1296 * q;
        return (const char*)wd + (uint32_t)(bx * 5184 + (r / 18) * 72 + 18 * (HREV ? 3 - q : q) + r % 18) * 128u;
      };
      int k = w;
      if (w < HW) {
#pragma unroll 1
        for (; k < 6 && (int)((uint32_t)__builtin_amdgcn_s_memrealtime() - (tu + UD + VD2 - HSTOP)) < 0; k += HW) { TOUCH(d_line(k)); DRAIN(); }
      }
      TS(0, 4);
      until(tu + UD + VD2 + RD2);
      if (RW) gwait(ca, 4);
      if (w < HW) {
#pragma unroll 1
        for (; k < 6; k += HW) { TOUCH(d_line(k)); DRAIN(); }
      }
      TS(0, 7);
    }
    tv = tu + UD + VD2 - vearly;
    tk = tu + UD + VD2 + KD;
  }
  {
  const int tid = tid2, lane = tid & 63, i = lane & 15, kq = lane >> 4;
  __builtin_amdgcn_s_setprio(3);
  const int PL = ffn ? 6400 : LW + 6656;
  f32x2 xf[9];
  f32x4 za = {0.f, 0.f, 0.f, 0.f}, zb = za;
  uint32_t al = O_XC + 64u * (i >> 3) + 16u * kq + 128u * w;
  uint32_t ha = (ROLE == 1) ? (uint32_t)((4 * (kq & 1) + 2 * (kq >> 1) + (i >> 3)) * 80 + 16 * w + 2 * (i & 7)) : 0u;
  uint32_t h0a = (uint32_t)(min(i, 8) * 80 + 16 * kq), h1a = (uint32_t)(min(i ^ 8, 8) * 80 + 16 * kq);
  uint32_t so = O_SL + (uint32_t)fb * SP + ((ROLE == 2) ? 16u * tid - 4096u : 8192u + 16u * lane);
  uint32_t da = lb0 + (uint32_t)((8 * kq + ((lane >> 2) & 3)) * 1152 + 32 * ((8 * kq + ((lane >> 2) & 3) + 2) >> 2) + 8 * (lane & 3) + ((ROLE == 2) ? 128 * wl : 1024));
  if (ROLE == 2) asm volatile("" : "+v"(h0a), "+v"(h1a), "+v"(so), "+v"(da)); else asm volatile("" : "+v"(ha), "+v"(al), "+v"(da));
  uint32_t hv[9] = {}, nv[9] = {}, keya = 0u, keyb = 0u;
  int rka = 0, rkb = 0;
  auto put_row = [&](const uint32_t* r, int j, const void* base, uint32_t ro) {
    const u32x4 tl = {r[8], (uint32_t)DPP_I((int)r[8], 0x55, 0xf), (uint32_t)DPP_I((int)r[8], 0xaa, 0xf), (uint32_t)DPP_I((int)r[8], 0xff, 0xf)};
    asm volatile("global_store_dwordx4 %0, %1, %2 sc1\n\ts_nop 1" : : "v"(ro + 16u * lane), "v"((u32x4){r[4 * j], r[4 * j + 1], r[4 * j + 2], r[4 * j + 3]}), "s"(base) : "memory");
    if ((lane & 35) == 32 * j) asm volatile("global_store_dwordx4 %0, %1, %2 offset:1024 sc1\n\ts_nop 1" : : "v"(ro + 4u * (lane & 31)), "v"(tl), "s"(base) : "memory");
  };
  if (ln2) {
    const int po = PL - 2560 * (ROLE == 1);
    uint32_t pa = (uint32_t)(po + 9216 * wl + 32 * lane), pb = (uint32_t)(po + 9216 * wl + 2048 + 8 * (lane & 31)), ps = (lane < 32) ? 0u : 16u;
    uint32_t kid = (uint32_t)(((255 - g2) << 5) | (7 - 2 * wl - (lane >> 5)));
    const int tl = tid - 256, t2 = tl + 128, d1 = 64 * (tl / 48) + tl % 48, d3 = 64 * (t2 / 48) + t2 % 48;
    const int d2 = (tl < 128) ? 64 * (tl >> 4) + 48 + (tl & 15) : min(384 + tl, 575);
    uint32_t vo = v_at(d1, g0), vo2 = v_at(d2, g0), vo3 = v_at(d3, g0), px = (uint32_t)(PL + 4 * d1), py = (uint32_t)(PL + 4 * d2), pz = (uint32_t)(PL + 4 * d3);
#pragma unroll
    for (int k = 0; k < 9; ++k) xf[k] = (f32x2){bflo(xv[k]), bfhi(xv[k])};
    asm volatile("" : "+v"(xf[0]), "+v"(xf[1]), "+v"(xf[2]), "+v"(xf[3]), "+v"(xf[4]), "+v"(xf[5]), "+v"(xf[6]), "+v"(xf[7]), "+v"(xf[8]));
    asm volatile("" : "+v"(za), "+v"(zb), "+v"(pa), "+v"(pb), "+v"(ps), "+v"(kid), "+v"(vo), "+v"(vo2), "+v"(vo3), "+v"(px), "+v"(py), "+v"(pz));
    u32x4 ut = {utag(T, 0), utag(T, 1), utag(T, 2), utag(T, 3)};
    float sv[10], ws_ = dot2(wv[4], 0x3f803f80u, 0.f) * 0.5f;
#pragma unroll
    for (int k = 0; k < 4; ++k) ws_ = dot2(wv[k], 0x3f803f80u, ws_);
    ws_ = wave_sum(ws_);
#pragma unroll
    for (int k = 0; k < 10; ++k) sv[k] = 16.f;
    uint32_t ka_ = lb0 + LM + 4u * (128 + 48 + 2 * wl + (lane >> 5)), kb_ = lb0 + LM + 4u * (128 + 48);
    uint32_t ko_ = O_KEY + (uint32_t)(256 * (lane & 7) + 8 * (g2 & 31) + (g2 >> 5)) * 16u;
    asm volatile("" : "+v"(ut), "+v"(ws_), "+v"(sv[0]), "+v"(sv[1]), "+v"(sv[2]), "+v"(sv[3]), "+v"(sv[4]), "+v"(sv[5]), "+v"(sv[6]), "+v"(sv[7]), "+v"(sv[8]), "+v"(sv[9]),
                 "+v"(ka_), "+v"(kb_), "+v"(ko_));
    if (ffn) gwait(ca + 36, 4);
    TS(0, 7);
    until(tv);
    TS(0, 8);
    u32x4 va, vc = ut, vb = ut;
    asm volatile("global_load_dwordx4 %0, %2, %4 sc1\n\ts_cmp_lt_u32 %5, 2\n\ts_cbranch_scc1 1f\n\tglobal_load_dwordx4 %1, %3, %4 sc1\n1:"
                 : "=&v"(va), "+v"(vc) : "v"(vo), "v"(vo3), "s"(ws), "s"(wl) : "memory", "scc");
    until(tv + V2D);
    asm volatile("s_cmp_eq_u32 %5, 3\n\ts_cbranch_scc1 1f\n\tglobal_load_dwordx4 %2, %3, %4 sc1\n\ts_waitcnt vmcnt(1)\n\ts_branch 2f\n1:\n\ts_waitcnt vmcnt(0)\n2:"
                 : "+v"(va), "+v"(vc), "+v"(vb) : "v"(vo2), "s"(ws), "s"(wl) : "memory", "scc");
    auto turn1 = [&] {
#pragma unroll
      for (int e = 0; e < 4; ++e) *(uint32_t*)(smem + px + 9216 * e) = va[e];
      if (wl >= 2) {
#pragma unroll
        for (int e = 0; e < 4; ++e) *(uint32_t*)(smem + pz + 9216 * e) = vc[e];
      }
    };
    auto turn = [&] {
      if (wl < 3) {
#pragma unroll
        for (int e = 0; e < 4; ++e) *(uint32_t*)(smem + py + 9216 * e) = vb[e];
      }
    };
    va ^= ut; vc ^= ut;
    turn1();
    asm volatile("s_waitcnt vmcnt(0)" : "+v"(vb) : : "memory");
    vb ^= ut;
    turn();
    const u32x4 vs = va | vb | vc;
    if (__builtin_expect(__any(((vs[0] | vs[1] | vs[2] | vs[3]) & UMASK) != 0u), 0)) {
#pragma unroll 1
      for (int spin = 0; spin < 4; ++spin) {
        const bool na = ((va[0] | va[1] | va[2] | va[3]) & UMASK) != 0u, nb = ((vb[0] | vb[1] | vb[2] | vb[3]) & UMASK) != 0u, nc = ((vc[0] | vc[1] | vc[2] | vc[3]) & UMASK) != 0u;
        if (!__any(na || nb || nc)) break;
        TSC(16);
        if (na) LD16(va, vo);
        if (nb) LD16(vb, vo2);
        if (nc) LD16(vc, vo3);
        asm volatile("s_waitcnt vmcnt(0)" : "+v"(va), "+v"(vb), "+v"(vc) : : "memory");
        if (na) va ^= ut;
        if (nb) vb ^= ut;
        if (nc) vc ^= ut;
      }
      auto again = [&](u32x4& v, uint32_t o, int d) {
        const int J = g0 >> 5;
        const uint32_t dn = O_DN + 16u * (uint32_t)((d < 512) ? 4 * (d >> 1) + ((0x01013232 >> (4 * J)) & 3) : 4 * (d - 256) + (J >> 1));
        bool pend = ((v[0] | v[1] | v[2] | v[3]) & UMASK) != 0u;
#pragma unroll 1
        for (int spin = 0; spin < SPIN_CAP && __any(pend); ++spin) {
          TSC(16);
          SLEEP(PSLEEP);
          if (pend) {
            uint32_t fin = ~T;
            if ((v[0] & v[1] & v[2] & v[3] & LMASK) == LMASK)
              asm volatile("global_load_dword %0, %1, %2 sc1\n\ts_waitcnt vmcnt(0)" : "=&v"(fin) : "v"(dn), "s"(ws) : "memory");
            LD16W(v, o);
            v ^= ut;
            pend = ((v[0] | v[1] | v[2] | v[3]) & UMASK) != 0u;
            if (pend && fin == T) {
              u32x2 sc;
              for (int k = 0; k < SPIN_CAP; ++k) {
                asm volatile("global_load_dwordx2 %0, %1, %2 sc1\n\ts_waitcnt vmcnt(0)" : "=&v"(sc) : "v"(O_SC + 16u * d), "s"(ws) : "memory");
                if (sc[0] == T) break;
              }
              *(u16*)(smem + L_SC + 2 * d) = (u16)(sc[1] >> 16);
              cnt[10] = 1u;
              v = (v ^ ut) & ~LMASK;
              pend = false;
            }
          }
        }
      };
      again(va, vo, d1); again(vc, vo3, d3); again(vb, vo2, d2);
      turn1();
      turn();
    }
    u32x4 a0, a1;
    u32x2 a2, cf;
    {
      uint32_t cc, n = SPIN_CAP;
      asm volatile("s_mov_b64 exec, 1\n\tv_mov_b32 v90, 1\n\tds_add_u32 %6, v90\n\ts_mov_b64 exec, -1\n"
                   "1:\n\tds_read2_b32 %3, %6 offset1:10\n\tds_read_b128 %0, %7\n\tds_read_b128 %1, %7 offset:16\n\tds_read_b64 %2, %8\n\t"
                   "s_sub_u32 %5, %5, 1\n\ts_cbranch_scc1 2f\n\ts_waitcnt lgkmcnt(3)\n\tv_readfirstlane_b32 %4, v90\n\t"
                   "s_cmp_lt_u32 %4, 4\n\ts_cbranch_scc1 1b\n2:\n\ts_waitcnt lgkmcnt(0)"
                   : "=&v"(a0), "=&v"(a1), "=&v"(a2), "=&{v[90:91]}"(cf), "=&s"(cc), "+s"(n) : "v"(ca), "v"(lb0 + pa), "v"(lb0 + pb) : "memory", "scc");
    }
    TS(0, 9);
    if (__builtin_expect(__builtin_amdgcn_readfirstlane(cf[1]) != 0, 0)) {
      const u32x4 q = *(const u32x4*)(smem + L_SC + 16 * lane);
      const uint32_t r = *(const uint32_t*)(smem + L_SC + 1024 + 4 * (lane & 31));
#pragma unroll
      for (int k = 0; k < 4; ++k) { sv[2 * k] = bflo(q[k]); sv[2 * k + 1] = bfhi(q[k]); }
      sv[8] = bflo(r); sv[9] = bfhi(r);
    }
    const uint32_t av[10] = {a0[0], a0[1], a0[2], a0[3], a1[0], a1[1], a1[2], a1[3], a2[0] >> ps, a2[1] >> ps};
    f32x2 h[9];
#pragma unroll
    for (int k = 0; k < 10; ++k) {
      asm("v_fma_mix_f32 %0, %1, %2, %3 op_sel_hi:[1,0,0]" : "=v"(h[(k < 8) ? k >> 1 : 8][k & 1]) : "v"(av[k]), "v"(sv[k]), "v"(xf[(k < 8) ? k >> 1 : 8][k & 1]));
      if (k < 8) asm("v_fma_mix_f32 %0, %1, %2, %3 op_sel:[1,0,0] op_sel_hi:[1,0,0]" : "=v"(h[4 + (k >> 1)][k & 1]) : "v"(av[k]), "v"(sv[k]), "v"(xf[4 + (k >> 1)][k & 1]));
    }
#pragma unroll
    for (int k = 0; k < 9; ++k) hv[k] = pack2(h[k][0], h[k][1]);
    const float da = dot2(hv[2], wv[2], dot2(hv[0], wv[0], zb[0])) + dot2(hv[3], wv[3], dot2(hv[1], wv[1], zb[1]));
    const float db = dot2(hv[6], wv[2], dot2(hv[4], wv[0], zb[2])) + dot2(hv[7], wv[3], dot2(hv[5], wv[1], zb[3]));
    float dt = dot2(hv[8], wv[4], fold32(da, db));
    f32x2 rc, nc;
    ln_stats<true>(hv, lane, 1.f, xf, za, rc, nc, dt);
    dt = (lane < 32) ? dt * rc[0] + ws_ * nc[0] : dt * rc[1] + ws_ * nc[1];
    const uint32_t bb = pack2(dt, dt) & 0xffff0000u;
    const uint32_t kk = (bb ^ (((uint32_t)((int)bb >> 31) & 0x7fff0000u) | 0x80000000u)) | kid;
    u32x4 ka, kb;
    uint32_t old;
    asm volatile("s_mov_b64 exec, %5\n\tds_write_b32 %3, %4\n\ts_mov_b64 exec, 1\n\tv_mov_b32 %2, 1\n\tds_add_rtn_u32 %2, %7, %2\n\ts_mov_b64 exec, -1\n\t"
                 "ds_read_b128 %0, %6\n\tds_read_b128 %1, %6 offset:16\n\ts_waitcnt lgkmcnt(0)"
                 : "=&v"(ka), "=&v"(kb), "=&v"(old) : "v"(ka_), "v"(kk), "s"(0x8000000080000000ull), "v"(kb_), "v"(ca) : "memory");
    if (__builtin_amdgcn_readfirstlane(old) == 7) {
      uint32_t m1 = max(max(ka[0], ka[1]), ka[2]), m2 = med3u(ka[0], ka[1], ka[2]), m3 = min(min(ka[0], ka[1]), ka[2]);
#pragma unroll
      for (int e = 0; e < 5; ++e) {
        const uint32_t kx = e ? kb[e - 1] : ka[3];
        m3 = med3u(m2, m3, kx); m2 = med3u(m1, m2, kx); m1 = max(m1, kx);
      }
      if (lane < 8) ST16(ko_, ((u32x4){m1 | 24u, m2 | 16u, m3 | 8u, T & ~1u}));
    }
    TS(0, 10);
    keya = (uint32_t)__builtin_amdgcn_readlane(kk, 31); keyb = (uint32_t)__builtin_amdgcn_readlane(kk, 63);
    ln_apply<false>(xf, lane, rc, nc, nv);
    gwait(ca, 8);
    if (wl == 1) {
      const u32x4 kv = *(const u32x4*)&kl[4 * (lane & 1)];
      if (lane < 2) ST16(O_KF + (uint32_t)(2 * g2 + lane) * 16u, ((u32x4){__builtin_amdgcn_perm(kv[1], kv[0], 0x07060302u), T, __builtin_amdgcn_perm(kv[3], kv[2], 0x07060302u), T}));
      if (ROLE == 2 && b == 0 && lane == 0) ST4(O_EP, T * 0x0E8B2F51u);
    }
    const uint32_t ko = kl[lane & 7];
    rka = FORCEFB + __popcll(__ballot(ko > keya) & 0xffull); rkb = FORCEFB + __popcll(__ballot(ko > keyb) & 0xffull);
#pragma unroll
    for (int k = 0; k < 9; ++k) {
      const f32x2 o = xf[k] + (f32x2){bflo(nv[k]), bfhi(nv[k])};
      hv[k] = pack2(o[0], o[1]);
      nv[k] = min(nv[k], 0xfffffffeu);
    }
    if (rka < 2) put_row(nv, 0, ws, O_XC + (uint32_t)(1023 - 4 * g2 - rka) * 1152u);
    if (rkb < 2) put_row(nv, 1, ws, O_XC + (uint32_t)(1023 - 4 * g2 - rkb) * 1152u);
    if (!LPOS && ffn) down_dma(KN, 4, lane);
  }
  uint32_t k3rd = 0u, k4th = 0u;
  uint32_t q0 = lb0 + LM + 4u * lane, q1 = lb0 + LM + 32u * w, q2 = q1 + 4u * (lane & 7), q3 = lb0 + LM + 512u + 4u * (lane & 7), q4 = lb0 + LM + (uint32_t)(tid & ~7);
  uint32_t r8 = C_, sb = lb0 + LM + 512u;
  asm volatile("" : "+v"(q0), "+v"(q1), "+v"(q2), "+v"(q3), "+v"(q4), "+v"(r8), "+v"(sb));
  if constexpr (ROLE == 1) {
    u32x4 key;
    const uint32_t kof = O_KEY + (uint32_t)(256 * bx + tid) * 16u;
    until(tk);
    if (!ffn) SLEEP(NSLEEP);
    LD16(key, kof);
    if (ffn) {
      gwait0(ca + 4, 8);
      gu_read(1);
    }
    asm volatile("s_waitcnt vmcnt(0)" : "+v"(key) : : "memory");
    if (__builtin_expect(__any(key[3] != (T & ~1u)), 0)) {
#pragma unroll 1
      for (int spin = 0; spin < SPIN_CAP; ++spin) {
        const bool need = key[3] != (T & ~1u);
        if (!__any(need)) break;
        TSC(15);
        SLEEP(KBACK);
        if (need) LD16W(key, kof);
      }
    }
    TS(0, 11);
    const uint32_t m1 = oct_maxu(key[0]);
    const bool hit = key[0] == m1;
    const uint32_t k0 = hit ? key[1] : key[0], k1 = hit ? key[2] : key[1];
    const uint32_t m2 = oct_maxu(k0);
    *LP(u32x2, q4) = (u32x2){m1, m2};
    k3rd = (k0 == m2) ? k1 : k0;
    k4th = (k0 == m2) ? (hit ? 0u : key[2]) : k1;
    if (RPF == 1 && !ffn && w == (bj & 3)) {
      const uint32_t r1 = O_XC + ((m1 >> 3) & 0x3ffu) * 1152u + 128u * (lane & 7), r2 = O_XC + ((m2 >> 3) & 0x3ffu) * 1152u + 128u * (lane & 7);
      asm volatile("global_load_dword v126, %0, %3 sc1\n\tglobal_load_dword v126, %1, %3 sc1\n\tglobal_load_dword v126, %2, %3 offset:1024 sc1"
                   : : "v"(r1), "v"(r2), "v"(((lane & 1) ? r1 : r2) - 128u * (lane & 7)), "s"(ws) : "memory", "v126");
    }
  }
  auto pick = [&](auto co, auto so_) {
    BAR();
    const uint32_t c = *LP(uint32_t, q0 + co.value), own = *LP(uint32_t, q2 + co.value);
    const u32x4 la = *LP(u32x4, q1 + co.value), lb = *LP(u32x4, q1 + co.value + 16);
    uint32_t r = r8;
    asm("v_cmp_gt_u32 s[40:41], %1, %2\n\tv_cmp_gt_u32 s[42:43], %1, %3\n\tv_cmp_gt_u32 s[44:45], %1, %4\n\tv_cmp_gt_u32 s[46:47], %1, %5\n\t"
        "v_cmp_gt_u32 s[48:49], %1, %6\n\tv_cmp_gt_u32 s[50:51], %1, %7\n\tv_cmp_gt_u32 s[52:53], %1, %8\n\tv_cmp_gt_u32 s[54:55], %1, %9\n\t"
        "s_bcnt1_i32_b64 s40, s[40:41]\n\ts_bcnt1_i32_b64 s42, s[42:43]\n\tv_writelane_b32 %0, s40, 0\n\ts_bcnt1_i32_b64 s44, s[44:45]\n\t"
        "v_writelane_b32 %0, s42, 1\n\ts_bcnt1_i32_b64 s46, s[46:47]\n\tv_writelane_b32 %0, s44, 2\n\ts_bcnt1_i32_b64 s48, s[48:49]\n\t"
        "v_writelane_b32 %0, s46, 3\n\ts_bcnt1_i32_b64 s50, s[50:51]\n\tv_writelane_b32 %0, s48, 4\n\ts_bcnt1_i32_b64 s52, s[52:53]\n\t"
        "v_writelane_b32 %0, s50, 5\n\ts_bcnt1_i32_b64 s54, s[54:55]\n\tv_writelane_b32 %0, s52, 6\n\tv_writelane_b32 %0, s54, 7"
        : "+v"(r) : "v"(c), "v"(la[0]), "v"(la[1]), "v"(la[2]), "v"(la[3]), "v"(lb[0]), "v"(lb[1]), "v"(lb[2]), "v"(lb[3])
        : "scc", "s40", "s41", "s42", "s43", "s44", "s45", "s46", "s47", "s48", "s49", "s50", "s51", "s52", "s53", "s54", "s55");
    *LP(uint32_t, sb + 4u * min(r, (uint32_t)C_) + so_.value) = own;
    BAR();
  };
  auto rows = [&](uint32_t ao, auto&& meanwhile) {
    u32x4 aa, ab, ac = __builtin_bit_cast(u32x4, zb);
    asm volatile("global_load_dwordx4 %0, %3, %4 sc1\n\tglobal_load_dwordx4 %1, %3, %4 offset:512 sc1\n\t"
                 "s_cmp_lg_u32 %5, 0\n\ts_cbranch_scc1 1f\n\tglobal_load_dwordx4 %2, %3, %4 offset:1024 sc1\n1:"
                 : "=&v"(aa), "=&v"(ab), "+v"(ac) : "v"(ao), "s"(ws), "s"(w) : "memory", "scc");
    __builtin_amdgcn_sched_barrier(0);
    meanwhile();
    asm volatile("s_waitcnt vmcnt(0)" : "+v"(aa), "+v"(ab), "+v"(ac) : : "memory");
    if (__builtin_expect(__any(max(max(max4(aa), max4(ab)), max4(ac)) == 0xffffffffu), 0)) {
#pragma unroll 1
      for (int spin = 0; spin < SPIN_CAP; ++spin) {
        const bool na = max4(aa) == 0xffffffffu, nb = max4(ab) == 0xffffffffu, nc = max4(ac) == 0xffffffffu;
        if (!__any(na || nb || nc)) break;
        TSC(14);
        SLEEP(PSLEEP);
        if (na) LD16W(aa, ao);
        if (nb) LD16W(ab, ao + 512u);
        if (nc) LD16W(ac, ao + 1024u);
      }
    }
    *(u32x4*)(smem + LA + 16 * tid) = aa;
    *(u32x4*)(smem + LA + 4096 + 16 * tid) = ab;
    if (w == 0) *(u32x4*)(smem + LA + 8192 + 16 * tid) = ac;
  };
  auto rows_dma = [&](uint32_t ao) {
    uint32_t dl = lb0 + LA + 1024u * w, keep;
    asm volatile("s_mov_b32 %0, m0\n\ts_mov_b32 m0, %1\n\ts_add_u32 %1, %1, 4096 - 512\n\tglobal_load_lds_dwordx4 %2, %3 sc1\n\ts_mov_b32 m0, %1\n\ts_add_u32 %1, %1, 4096 - 512\n\t"
                 "global_load_lds_dwordx4 %2, %3 offset:512 sc1\n\ts_cmp_lg_u32 %4, 0\n\ts_cbranch_scc1 1f\n\ts_mov_b32 m0, %1\n\ts_nop 0\n\t"
                 "global_load_lds_dwordx4 %2, %3 offset:1024 sc1\n1:\n\ts_mov_b32 m0, %0" : "=&s"(keep), "+s"(dl) : "v"(ao), "s"(ws), "s"(w) : "memory", "scc");
  };
  auto get_bd = [&](int t0, int t1) {
#pragma unroll
    for (int t = t0; t < t1; ++t) {
      const u32x2 h0 = trd(da + 512 * (t >> 2) + 32 * (t & 3)), h1 = trd(da + 512 * (t >> 2) + 32 * (t & 3) + 4608 + 32);
      bw[t] = (u32x4){h0[0], h0[1], h1[0], h1[1]};
    }
#pragma unroll
    for (int t = t0; t < t1; ++t) asm volatile("" : "+v"(bw[t]));
  };
  pick(IC<0>{}, IC<0>{});
  uint32_t mykey = *LP(uint32_t, q3), ro = ((mykey >> 3) & 0x3ffu) * 1152u;
  TS(0, 12);
  auto check = [&] {
    cnt[(k3rd > (uint32_t)__builtin_amdgcn_readlane(mykey, 7)) ? 5 : 6] = 1u;
    const uint32_t m3 = oct_maxu(k3rd);
    if (k3rd == m3) k3rd = k4th;
    if ((lane & 7) == 0) cand[72 + (tid >> 3)] = m3;
    if (w == 0 && lane < 32) cand[(lane < 8) ? 64 + lane : 96 + lane] = (lane < 8) ? mykey : 0u;
  };
  if constexpr (ROLE == 1) {
    if (ffn && !FORCEFB) rows_dma(ro + al);
    check();
    DRAIN();
  } else if (ffn) {
    if (LPOS) { SLEEP(LSLEEP); down_dma(KN, 4, lane); } else { DRAIN(); gsync0(ca, 12); get_bd(0, 8); }
  }
  BAR();
  TS(0, 5);
  auto put_out = [&](int j, uint32_t k0, int rk) {
    const uint64_t mine = __ballot(((mykey ^ k0) & ~24u) == 0u) & 0xffull;
    if (mine == 0ull) put_row(hv, j, out, (uint32_t)((8 * g2 + 2 * wl + j) * (D_ * 2)));
    else if (rk >= 2) put_row(nv, j, ws, O_FB + 1152u * (uint32_t)__builtin_ctzll(mine));
  };
  auto rest_out = [&] { put_out(0, keya, rka); put_out(1, keyb, rkb); };
  f32x4 lo, hi;
  auto gate_up = [&] {
    u32x4 av[5];
    const uint32_t aa = lb0 + LA + 16u * lane;
#pragma unroll
    for (int j = 0; j < 5; ++j) asm volatile("ds_read_b128 %0, %1 offset:%2" : "=v"(av[j]) : "v"(aa), "n"(1024 * j) : "memory");
    lo = (f32x4){0.f, 0.f, 0.f, 0.f}; hi = lo;
#pragma unroll
    for (int j = 0; j < 9; ++j) {
      if (j < 5) asm volatile("s_waitcnt lgkmcnt(4)" : "+v"(av[j % 5]));
      else if (j == 5) asm volatile("s_waitcnt lgkmcnt(2)" : "+v"(av[0]), "+v"(av[1]));
      else if (j == 7) asm volatile("s_waitcnt lgkmcnt(0)" : "+v"(av[2]), "+v"(av[3]));
      lo = MMB(av[j % 5], bw[2 * j], lo);
      hi = MMB(av[j % 5], bw[2 * j + 1], hi);
      __builtin_amdgcn_sched_barrier(0);
      if (j < 4) asm volatile("ds_read_b128 %0, %1 offset:%2" : "=v"(av[j]) : "v"(aa), "n"(1024 * (j + 5)) : "memory");
    }
  };
  uint32_t fl;
  asm volatile("ds_read_b32 %0, %1" : "=v"(fl) : "v"(ca + 20) : "memory");
  if (ROLE == 1) gate_up();
  asm volatile("s_waitcnt lgkmcnt(0)" : "+v"(fl));
  const bool slow = FORCESLOW || FORCEFB || __builtin_amdgcn_readfirstlane(fl) != 0;
  if (__builtin_expect(slow, 0)) {
    TSC(17);
    pick(IC<256>{}, IC<64>{});
    mykey = sel[16 + (lane & 7)];
    ro = ((mykey >> 3) & 0x3ffu) * 1152u;
    if (ROLE == 1 && (k3rd > (uint32_t)__builtin_amdgcn_readlane(mykey, 7) || (mykey & 24u) == 8u)) cnt[11] = 1u;
    BAR();
    if (FORCESLOW > 1 || FORCEFB || __builtin_amdgcn_readfirstlane(cnt[11]) != 0) {
    u32x4 key;
#pragma unroll 1
    for (int spin = 0; spin < SPIN_CAP; ++spin) {
      LD16W(key, O_KF + 16u * tid);
      if (!__any(key[1] != T || key[3] != T)) break;
      SLEEP(KBACK);
    }
    const uint32_t ib = (uint32_t)(((255 - (tid >> 1)) << 5) | (7 - 4 * (tid & 1)));
    uint32_t k0 = (key[0] << 16) | ib, k1 = (key[0] & 0xffff0000u) | (ib - 1), k2 = (key[2] << 16) | (ib - 2), k3 = (key[2] & 0xffff0000u) | (ib - 3);
    CEX(k0, k1) CEX(k2, k3) CEX(k0, k2) CEX(k1, k3) CEX(k1, k2)
#pragma unroll 1
    for (int c = 0; c < C_; ++c) {
      const uint32_t mx = wave_maxu(k0);
      const bool hit = (k0 == mx);
      k0 = hit ? k1 : k0; k1 = hit ? k2 : k1; k2 = hit ? k3 : k2; k3 = hit ? 0u : k3;
      if (lane == 0) cand[64 + 8 * w + c] = mx;
    }
    pick(IC<256>{}, IC<128>{});
    mykey = sel[32 + (lane & 7)];
    const uint32_t okey = sel[32 + (lane >> 3)];
    const uint64_t same = __ballot(((mykey ^ okey) & 0x1fe0u) == 0u && okey > mykey);
    const int sl = FORCEFB + __popcll((same >> (lane & 7)) & 0x0101010101010101ull);
    ro = (sl < 2) ? (((mykey >> 3) & 0x3ffu) + 3 - sl) * 1152u : O_FB - O_XC + 1152u * (lane & 7);
    }
    if (ln2) rest_out();
    if (ffn) {
      if (ROLE == 1) rows(ro + al, [] {});
      BAR();
      if (ROLE == 1) gate_up();
    }
  }
  if (OPOS == 0 && ROLE == 2 && ffn && !slow) { SLEEP(OD); rest_out(); }
  if (!ffn) {
    if (ln2 && !slow) { SLEEP(OD2); rest_out(); }
    TSE();
    return;
  }
  uint32_t fx = 0, fv = 0, fn = 0, fo = 0, sa[9];
  float rs, hx, xs;
  u32x4 se;
  if constexpr (ROLE == 1) {
    f32x4 lh = (lane < 32) ? lo : hi;
    float fa = fold32(lh[0], lh[2]), fb2 = fold32(lh[1], lh[3]);
    if (__builtin_expect(__any(__builtin_isunordered(fa, fb2)) && !slow, 0)) {
      gsync0(ca + 4, 12);
      rows(ro + al, [] {});
      gsync0(ca + 4, 16);
      gate_up();
      lh = (lane < 32) ? lo : hi;
      fa = fold32(lh[0], lh[2]); fb2 = fold32(lh[1], lh[3]);
    }
    const float g = __int_as_float(__builtin_amdgcn_update_dpp(__float_as_int(fa), __float_as_int(fb2), 0x128, 0xf, 0xc, false));
    const float u = __int_as_float(__builtin_amdgcn_update_dpp(__float_as_int(fb2), __float_as_int(fa), 0x128, 0xf, 0x3, false));
    const uint32_t gu = pack2(g, u);
    const float gp = bflo(gu);
    const float sg = rbf(gp * __builtin_amdgcn_rcpf(1.f + __expf(-gp)));
    *(u16*)((char*)hm + ha) = f2bf(sg * bfhi(gu));
  } else if (LPOS) {
    DRAIN();
    gsync0(ca, 12);
    get_bd(0, 8);
  }
  BAR();
  TS(0, 6);
  if (ROLE == 2 || w == 2) {
    const u32x4 a0 = *(const u32x4*)((char*)hm + h0a), a1 = *(const u32x4*)((char*)hm + h1a);
    if (ROLE == 1) get_bd(0, 4);
    const f32x4 z = {0.f, 0.f, 0.f, 0.f};
    f32x4 o[4];
#pragma unroll
    for (int t = 0; t < 2 * ROLE; ++t) o[t] = MMB(a1, bw[2 * t + 1], MMB(a0, bw[2 * t], z));
#pragma unroll
    for (int t = 0; t < ROLE; ++t) {
      u32x4 sv;
#pragma unroll
      for (int r = 0; r < 4; ++r) sv[r] = min(pack2(o[2 * t][r], o[2 * t + 1][r]), 0xfffffffeu);
      ST16(so + 4096u * t, sv);
    }
    TS(0, 13);
  }
  if (OPOS == 1 && ROLE == 2) {
    SLEEP(OSLEEP);
    if (!slow) rest_out();
  }
  if (ROLE == 1 && w == 1) {
    const int q = (fb & 15) >> 2, ts = 4 * (q & 1) + ((i & 7) >> 1);
    const int dd = 64 * (fb >> 4) + 16 * (2 * (i & 1) + (q >> 1)) + 4 * (fb & 3) + 2 * (kq & 1) + (kq >> 1);
    const uint32_t fkey = (uint32_t)__builtin_amdgcn_ds_bpermute(4 * ts, (int)mykey), fr = (uint32_t)__builtin_amdgcn_ds_bpermute(4 * ts, (int)ro);
    const int ft = 2047 - (int)(((fkey >> 2) & 0x7f8u) | (fkey & 7u));
    fo = (uint32_t)(ft * D_ + dd) * 2u;
    u32x2 fs;
    asm volatile("global_load_ushort %0, %4, %8\n\tglobal_load_ushort %1, %5, %9 sc1\n\tglobal_load_ushort %2, %6, %9 sc1\n\tglobal_load_dwordx2 %3, %7, %9 sc1"
                 : "=&v"(fx), "=&v"(fv), "=&v"(fn), "=&v"(fs) : "v"(fo), "v"(v_at(dd, ft >> 3) + 2u * (ft & 7)), "v"(O_XC + fr + 2u * dd), "v"(O_SC + 16u * dd), "s"(x), "s"(ws) : "memory");
    uint32_t t16 = (((T >> (3 * ((ft >> 1) & 3))) & LMASK) | (((T >> (12 + ((ft >> 1) & 3))) & 0x00010001u) << 14)) >> (16 * (ft & 1)) & 0xffffu;
    const uint32_t o = fkey >> 16;
    const float rs0 = bf2f((u16)((o & 0x8000u) ? (o & 0x7fffu) : (~o & 0xffffu)));
    rs = rbf(__builtin_amdgcn_rcpf(1.f + __expf(-rs0)));
    const uint32_t one = (i < 8) ? (0x3f80u << (16 * (i & 1))) : 0u;
    se = (u32x4){(i >> 1) == 0 ? one : 0u, (i >> 1) == 1 ? one : 0u, (i >> 1) == 2 ? one : 0u, (i >> 1) == 3 ? one : 0u};
#pragma unroll
    for (int m = 0; m < 9; ++m) sa[m] = O_SL + (uint32_t)(16 * m + (lane >> 2)) * SP + (uint32_t)(4 * fb + (lane & 3)) * 16u;
    asm volatile("" : "+v"(t16), "+v"(rs), "+v"(se), "+v"(sa[0]), "+v"(sa[1]), "+v"(sa[2]), "+v"(sa[3]), "+v"(sa[4]), "+v"(sa[5]), "+v"(sa[6]), "+v"(sa[7]), "+v"(sa[8]));
    SLEEP(SSLEEP);
    TS(0, 1);
    float sum;
#pragma unroll 1
    for (int spin = 0; spin < SPIN_CAP; ++spin) {
      u32x4 sv[9];
#pragma unroll
      for (int m = 0; m < 9; ++m) LD16(sv[m], sa[m]);
      asm volatile("s_waitcnt vmcnt(9)" : "+v"(fx), "+v"(fv), "+v"(fn), "+v"(fs));
      if (__builtin_expect(__any(fs[0] != T), 0)) {
        for (int k = 0; k < SPIN_CAP && fs[0] != T; ++k)
          asm volatile("global_load_dwordx2 %0, %1, %2 sc1\n\ts_waitcnt vmcnt(0)" : "=&v"(fs) : "v"(O_SC + 16u * dd), "s"(ws) : "memory");
      }
      const uint32_t f1 = fv ^ t16, f2 = (f1 & 7u) ? fv & ~7u : f1;
      hx = rbf(bf2f((u16)fx) + (float)__builtin_bit_cast(_Float16, (u16)f2) * __uint_as_float(fs[1]));
      xs = bf2f((u16)fn);
      asm volatile("" : "+v"(hx), "+v"(xs));
      f32x4 dv = {0.f, 0.f, 0.f, 0.f};
      static_for<0, 9>([&](auto mm) {
        constexpr int m = mm.value;
        asm volatile("s_waitcnt vmcnt(%1)" : "+v"(sv[m]) : "n"(8 - m));
        dv = MMB(sv[m], se, dv);
        __builtin_amdgcn_sched_barrier(0);
      });
      sum = fold16(fold32(dv[0], dv[1]), fold32(dv[2], dv[3]));
      if (__builtin_expect(!__any(sum != sum), 1)) break;
      uint32_t mx = 0u;
#pragma unroll
      for (int m = 0; m < 9; ++m) mx = max(mx, max4(sv[m]));
      if (!__any(mx == 0xffffffffu)) break;
      TSC(18);
      SLEEP(PSLEEP);
    }
    const float o1 = rbf(xs + rbf(rbf(sum) * rs));
    if (i < 8) asm volatile("global_store_short %0, %1, %2 sc1" : : "v"(fo), "v"((uint32_t)f2bf(hx + o1)), "s"(out) : "memory");
    TS(0, 2);
  }
  TSE();
  }
  }
}
#define BROT "192"
#define CPF_A "s_getpc_b64 s[96:97]\n\ts_lshl_b32 s99, s2, 4\n\ts_lshl_b32 s84, s98, 7\n\ts_add_u32 s99, s99, s84\n\tv_and_b32 v112, 1, v0\n\t" \
              "v_lshl_add_u32 v112, v112, 12, s99\n\tv_min_u32 v112, kf_end-kf_all-0x80, v112\n\ts_mov_b64 exec, 3\n\tglobal_load_dword v112, v112, s[96:97]\n\ts_mov_b64 exec, -1\n\t"
extern "C" __global__ __launch_bounds__(512, 4) void kf_all(KARGS) {
  __shared__ __attribute__((aligned(16))) char smem[SMEM];
  asm volatile("s_load_dwordx16 s[64:79], s[0:1], 0x0\n\ts_load_dwordx4 s[80:83], s[0:1], 0x40\n\tv_readfirstlane_b32 s98, v0\n\t"
               "s_add_u32 s84, s2, " BROT "\n\ts_xor_b32 s84, s84, s2\n\ts_bitset0_b32 s84, 8\n\ts_bitset0_b32 s84, 9\n\ts_xor_b32 s2, s2, s84\n\t"
               "v_mov_b32 v109, 0\n\ts_and_b32 s85, s2, 7\n\ts_bitcmp1_b32 s2, 8\n\ts_cbranch_scc1 1f\n\t" CPF_A "s_bitcmp1_b32 s98, 8\n\ts_cbranch_scc1 2f\n\t"
               "s_andn2_b32 s84, s2, 31\n\ts_lshl2_add_u32 s84, s85, s84\n\ts_bfe_u32 s85, s2, 0x20003\n\ts_or_b32 s84, s84, s85\n\t"
               "s_mulk_i32 s84, 0x2400\n\ts_mulk_i32 s98, 36\n\ts_add_u32 s84, s84, s98\n\tv_and_b32 v110, 63, v0\n\tv_and_b32 v111, 32, v0\n\t"
               "v_lshlrev_b32 v111, 5, v111\n\tv_lshl_add_u32 v111, v110, 2, v111\n\tv_lshl_add_u32 v110, v110, 4, s84\n\tv_add_u32 v111, s84, v111\n\t"
               "s_cmpk_lt_u32 s2, 0x90\n\ts_waitcnt lgkmcnt(0)\n\t"
               "global_load_dwordx4 v[100:103], v110, s[64:65]\n\tglobal_load_dwordx4 v[104:107], v110, s[64:65] offset:1152\n\t"
               "global_load_dword v108, v111, s[64:65] offset:1024\n\tglobal_load_dword v109, v109, s[82:83] sc1\n\t"
               "s_cbranch_scc1 kf_n\n\ts_branch kf_h\n"
               "1:\n\ts_bfe_u32 s84, s2, 0x50003\n\ts_lshl_b32 s85, s85, 5\n\ts_or_b32 s84, s84, s85\n\ts_lshl_b32 s84, s84, 2\n\t"
               "v_and_b32 v110, 0xff, v0\n\tv_bfe_u32 v107, v0, 7, 1\n"
               "4:\n\ts_lshl_b32 s85, s84, 7\n\tv_lshl_add_u32 v110, v110, 1, s85\n\tv_lshl_add_u32 v107, v107, 1, s84\n\ts_and_b32 s85, s98, s2\n\ts_waitcnt lgkmcnt(0)\n\t"
               "global_load_ushort v100, v107, s[78:79]\n\tglobal_load_ushort v101, v110, s[66:67]\n\tglobal_load_ushort v102, v110, s[68:69]\n\t"
               "global_load_dword v109, v109, s[82:83] sc1\n\ts_bitcmp1_b32 s85, 8\n\ts_cbranch_scc1 kf_u\n\t"
               "global_load_ushort v103, v110, s[70:71]\n\tglobal_load_ushort v104, v110, s[72:73]\n\tglobal_load_ushort v105, v110, s[74:75]\n\t"
               "global_load_ushort v106, v110, s[76:77]\n\tglobal_load_ushort v107, v107, s[80:81]\n\ts_bitcmp1_b32 s2, 8\n\ts_cbranch_scc1 kf_c\n"
               "6:\n\ts_barrier\n\ts_branch kf_p\n"
               "2:\n\ts_cmpk_lt_u32 s2, 0xc0\n\ts_cbranch_scc1 6b\n\t"
               "s_lshr_b32 s84, s2, 3\n\ts_lshl3_add_u32 s84, s85, s84\n\ts_lshl1_add_u32 s84, s84, 976\n\t"
               "v_and_b32 v110, 0x7f, v0\n\tv_mov_b32 v107, 0\n\ts_branch 4b"
               : : "s"(__builtin_amdgcn_kernarg_segment_ptr()), "s"(blockIdx.x), "v"(threadIdx.x),
                   "s"((uint32_t)(uintptr_t)(__attribute__((address_space(3))) char*)smem) : "scc", "s96", "s97", "s98", "s99", "v127");
}
extern "C" __global__ __launch_bounds__(512, 4) void kf_c(KARGS) {
  __shared__ __attribute__((aligned(16))) char smem[SMEM];
  role_body<0>(smem, KPASS);
}
extern "C" __global__ __launch_bounds__(512, 4) void kf_u(KARGS) {
  __shared__ __attribute__((aligned(16))) char smem[SMEM];
  role_body<3>(smem, KPASS);
}
extern "C" __global__ __launch_bounds__(512, 4) void kf_n(KARGS) {
  __shared__ __attribute__((aligned(16))) char smem[SMEM];
  role_body<1, 1>(smem, KPASS);
}
extern "C" __global__ __launch_bounds__(512, 4) void kf_h(KARGS) {
  __shared__ __attribute__((aligned(16))) char smem[SMEM];
  role_body<1, 0>(smem, KPASS);
}
extern "C" __global__ __launch_bounds__(512, 4) void kf_p(KARGS) {
  __shared__ __attribute__((aligned(16))) char smem[SMEM];
  role_body<2>(smem, KPASS);
}
extern "C" __global__ __launch_bounds__(512, 4) void kf_conv() { asm volatile(CONV_ASM ::: CONV_CLOB); }
extern "C" __global__ void kf_end() {}

extern "C" void kernel_launch(void* const* d_in, const int* in_sizes, int n_in,
                              void* d_out, int out_size, void* d_ws, size_t ws_size,
                              hipStream_t stream) {
  kf_all<<<512, 512, 0, stream>>>((const u16*)d_in[0], (const u16*)d_in[1], (const u16*)d_in[2], (const u16*)d_in[3], (const u16*)d_in[4],
                                  (const u16*)d_in[5], (const u16*)d_in[6], (const u16*)d_in[7], (const u16*)d_in[8], (char*)d_ws + (size_t)WSOFF * 1048576,
                                  (const u16*)d_in[10], (const u16*)d_in[11], (const u16*)d_in[12], (const u16*)d_in[13], (u16*)d_out);
}
```

```cpp
#include <hip/hip_runtime.h>
#include <hip/hip_bf16.h>
#include <stdint.h>

typedef __attribute__((ext_vector_type(8))) short bf16x8;
typedef __attribute__((ext_vector_type(4))) float f32x4;
typedef uint16_t u16;
typedef __attribute__((ext_vector_type(4))) uint32_t u32x4;
typedef __attribute__((ext_vector_type(2))) uint32_t u32x2;
#define TSD
#define TS(k, i)
#define TSC(i)
#define TSE()
#define TSP
#define TSA

constexpr int L_ = 2048, D_ = 576, N_ = 128, F_ = 4608, C_ = 8;
constexpr int NF = 144;
constexpr uint32_t O_EP = 0;
constexpr uint32_t O_KF = 4096;
constexpr uint32_t O_KEY = O_KF + 8192;
constexpr uint32_t O_SC = O_KEY + 32768;
constexpr uint32_t O_DN = O_SC + 9216;
constexpr uint32_t O_SU = O_DN + 20480;
constexpr uint32_t O_FB = O_SU + 262144;
constexpr uint32_t O_XC = O_FB + 8 * 1152;
constexpr uint32_t SP = 9216 + 128;
constexpr uint32_t O_SL = O_XC + 1024 * 1152;
constexpr uint32_t SLU = 336;
constexpr uint32_t O_U = O_SL + 256 * SLU * 16;
constexpr int UP = 584;
constexpr uint32_t VP = 8192 + 128, VPP = 4096 + 128;
constexpr uint32_t O_V = O_U + 256 * UP * 16, O_VP = O_V + 256 * VP;
static_assert(256 * SLU * 16 >= NF * SP && O_SL % 128 == 0 && O_U % 128 == 0 && O_V % 128 == 0, "");
__host__ __device__ __forceinline__ uint32_t u_at(int tg, int d) { return O_U + 16u * (uint32_t)((tg >> 1) * (UP * 2) + (d >> 2) * 8 + (tg & 1) * 4 + (d & 3)); }
__host__ __device__ __forceinline__ uint32_t v_at(int d, int tg) {
  return (d < 512) ? O_V + (uint32_t)(d >> 1) * VP + 16u * (uint32_t)(2 * tg + (d & 1)) : O_VP + (uint32_t)(d - 512) * VPP + 16u * (uint32_t)tg;
}
#define HAVE_VAT
#define HAVE_VTAG
__device__ __forceinline__ float bf2f(u16 v) { return __uint_as_float((uint32_t)v << 16); }
__device__ __forceinline__ float bflo(uint32_t v) { return __uint_as_float(v << 16); }
__device__ __forceinline__ float bfhi(uint32_t v) { return __uint_as_float(v & 0xffff0000u); }
__device__ __forceinline__ u16 f2bf(float f) { return __builtin_bit_cast(u16, (__bf16)f); }
__device__ __forceinline__ float rbf(float f) { return bf2f(f2bf(f)); }
typedef __attribute__((ext_vector_type(2))) float f32x2;
typedef __attribute__((ext_vector_type(2))) __bf16 bf16x2v;
__device__ __forceinline__ uint32_t pack2(float a, float b) {
  return __builtin_bit_cast(uint32_t, __builtin_convertvector((f32x2){a, b}, bf16x2v));
}

#define DPP_I(v, ctrl, rmask) __builtin_amdgcn_update_dpp(0, (v), (ctrl), (rmask), 0xf, true)
__device__ __forceinline__ float row_sum(float v) {
  v += __int_as_float(DPP_I(__float_as_int(v), 0x111, 0xf));
  v += __int_as_float(DPP_I(__float_as_int(v), 0x112, 0xf));
  v += __int_as_float(DPP_I(__float_as_int(v), 0x114, 0xf));
  v += __int_as_float(DPP_I(__float_as_int(v), 0x118, 0xf));
  return v;
}
__device__ __forceinline__ float rdl(float v, int l) { return __int_as_float(__builtin_amdgcn_readlane(__float_as_int(v), l)); }
__device__ __forceinline__ float wave_sum(float v) {
  v = row_sum(v);
  asm("s_nop 1\n\tv_add_f32_dpp %0, %0, %0 row_bcast:15 row_mask:0xa bank_mask:0xf\n\t"
      "s_nop 1\n\tv_add_f32_dpp %0, %0, %0 row_bcast:31 row_mask:0xc bank_mask:0xf\n\ts_nop 0" : "+v"(v));
  return rdl(v, 63);
}
__device__ __forceinline__ float fold32(float a, float b) {
  const auto r = __builtin_amdgcn_permlane32_swap(__float_as_uint(a), __float_as_uint(b), false, false);
  return __uint_as_float(r[0]) + __uint_as_float(r[1]);
}
__device__ __forceinline__ float fold16(float a, float b) {
  const auto r = __builtin_amdgcn_permlane16_swap(__float_as_uint(a), __float_as_uint(b), false, false);
  return __uint_as_float(r[0]) + __uint_as_float(r[1]);
}
__device__ __forceinline__ void wave_sum2(float& a, float& b) {
  float v = row_sum(fold32(a, b));
  asm("s_nop 1\n\tv_add_f32_dpp %0, %0, %0 row_bcast:15 row_mask:0xa bank_mask:0xf\n\ts_nop 0" : "+v"(v));
  a = rdl(v, 31); b = rdl(v, 63);
}
__device__ __forceinline__ float dot2(uint32_t a, uint32_t b, float c) {
  return __builtin_amdgcn_fdot2_f32_bf16(__builtin_bit_cast(bf16x2v, a), __builtin_bit_cast(bf16x2v, b), c, false);
}
__device__ __forceinline__ uint32_t row_maxu(uint32_t v) {
  v = max(v, (uint32_t)DPP_I((int)v, 0xb1, 0xf));
  v = max(v, (uint32_t)DPP_I((int)v, 0x4e, 0xf));
  v = max(v, (uint32_t)DPP_I((int)v, 0x141, 0xf));
  v = max(v, (uint32_t)DPP_I((int)v, 0x140, 0xf));
  return v;
}
__device__ __forceinline__ uint32_t wave_maxu(uint32_t v) {
  v = max(v, (uint32_t)DPP_I((int)v, 0x111, 0xf));
  v = max(v, (uint32_t)DPP_I((int)v, 0x112, 0xf));
  v = max(v, (uint32_t)DPP_I((int)v, 0x114, 0xf));
  v = max(v, (uint32_t)DPP_I((int)v, 0x118, 0xf));
  v = max(v, (uint32_t)DPP_I((int)v, 0x142, 0xa));
  v = max(v, (uint32_t)DPP_I((int)v, 0x143, 0xc));
  return (uint32_t)__builtin_amdgcn_readlane((int)v, 63);
}

typedef __attribute__((ext_vector_type(8))) _Float16 f16x8;
typedef __attribute__((ext_vector_type(2))) _Float16 f16x2;
__device__ __forceinline__ uint32_t packh(f32x2 v) { return __builtin_bit_cast(uint32_t, __builtin_convertvector(v, f16x2)); }
__device__ __forceinline__ uint32_t packh(float a, float b) { return packh((f32x2){a, b}); }
constexpr int IP = 224, I_X = 128;
__device__ __forceinline__ f32x2 cmul(f32x2 z, f32x2 w) {
  f32x2 r;
  asm("v_pk_mul_f32 %0, %1, %2 op_sel_hi:[0,1]\n\tv_pk_fma_f32 %0, %1, %2, %0 op_sel:[1,1,0] op_sel_hi:[1,0,1] neg_lo:[0,1,0]" : "=&v"(r) : "v"(z), "v"(w));
  return r;
}
__device__ __forceinline__ f32x2 cexp2(float ar2, float air, float l) {
  const float t = air * l;
  return (f32x2){__builtin_amdgcn_cosf(t), __builtin_amdgcn_sinf(t)} * __builtin_amdgcn_exp2f(ar2 * l);
}
__device__ __forceinline__ float state_a(float ldt, float are, float aim, float& ar2, float& air, float& g) {
  const float dt = rbf(__expf(ldt));
  ar2 = dt * are * 1.4426950408889634f;
  air = dt * aim * 0.15915494309189535f;
  air -= __builtin_rintf(air);
  g = 0.f;
  if (__builtin_expect(__any(ar2 > 0.f), 0)) g = fminf(__uint_as_float(wave_maxu(__float_as_uint(fmaxf(ar2, 0.f)))), 1.f);
  return dt;
}
template <int N> __device__ __forceinline__ void put_im(char* p, int im, const f32x2* z) {
  *(u32x4*)p = (u32x4){packh(z[0][0], z[1][0]), packh(z[2][0], z[3][0]), packh(z[4][0], z[5][0]), packh(z[6][0], z[7][0])};
  *(u32x4*)(p + im) = (u32x4){packh(z[0][1], z[1][1]), packh(z[2][1], z[3][1]), packh(z[4][1], z[5][1]), packh(z[6][1], z[7][1])};
}
#define Y_ASM \
  "v_mov_b64 v[40:41], %0\nv_pk_mul_f32 v[42:43], v[40:41], %1 op_sel_hi:[0,1]\nv_pk_mul_f32 v[44:45], %0, %2 op_sel_hi:[0,1]\n" \
  "v_pk_fma_f32 v[42:43], v[40:41], %1, v[42:43] op_sel:[1,1,0] op_sel_hi:[1,0,1] neg_lo:[0,1,0]\n" \
  "v_pk_fma_f32 v[44:45], %0, %2, v[44:45] op_sel:[1,1,0] op_sel_hi:[1,0,1] neg_lo:[0,1,0]\nv_cvt_pk_f16_f32 v56, v40, v42\n" \
  "v_cvt_pk_f16_f32 v60, v41, v43\nv_pk_mul_f32 v[40:41], v[42:43], %1 op_sel_hi:[0,1]\nv_pk_mul_f32 v[48:49], v[44:45], %2 op_sel_hi:[0,1]\n" \
  "v_pk_fma_f32 v[40:41], v[42:43], %1, v[40:41] op_sel:[1,1,0] op_sel_hi:[1,0,1] neg_lo:[0,1,0]\n" \
  "v_pk_fma_f32 v[48:49], v[44:45], %2, v[48:49] op_sel:[1,1,0] op_sel_hi:[1,0,1] neg_lo:[0,1,0]\n" \
  "v_pk_mul_f32 v[42:43], v[40:41], %1 op_sel_hi:[0,1]\nv_pk_mul_f32 v[52:53], v[48:49], %2 op_sel_hi:[0,1]\n" \
  "v_pk_fma_f32 v[42:43], v[40:41], %1, v[42:43] op_sel:[1,1,0] op_sel_hi:[1,0,1] neg_lo:[0,1,0]\n" \
  "v_pk_fma_f32 v[52:53], v[48:49], %2, v[52:53] op_sel:[1,1,0] op_sel_hi:[1,0,1] neg_lo:[0,1,0]\nv_cvt_pk_f16_f32 v57, v40, v42\n" \
  "v_cvt_pk_f16_f32 v61, v41, v43\nv_pk_mul_f32 v[40:41], v[42:43], %1 op_sel_hi:[0,1]\n" \
  "v_pk_fma_f32 v[40:41], v[42:43], %1, v[40:41] op_sel:[1,1,0] op_sel_hi:[1,0,1] neg_lo:[0,1,0]\n" \
  "v_pk_mul_f32 v[42:43], v[40:41], %1 op_sel_hi:[0,1]\nv_pk_mul_f32 v[46:47], v[44:45], %1 op_sel_hi:[0,1]\n" \
  "v_pk_fma_f32 v[42:43], v[40:41], %1, v[42:43] op_sel:[1,1,0] op_sel_hi:[1,0,1] neg_lo:[0,1,0]\n" \
  "v_pk_fma_f32 v[46:47], v[44:45], %1, v[46:47] op_sel:[1,1,0] op_sel_hi:[1,0,1] neg_lo:[0,1,0]\nv_cvt_pk_f16_f32 v58, v40, v42\n" \
  "v_cvt_pk_f16_f32 v62, v41, v43\nv_cvt_pk_f16_f32 v64, v44, v46\nv_cvt_pk_f16_f32 v68, v45, v47\n" \
  "v_pk_mul_f32 v[40:41], v[42:43], %1 op_sel_hi:[0,1]\nv_pk_mul_f32 v[44:45], v[46:47], %1 op_sel_hi:[0,1]\n" \
  "v_pk_fma_f32 v[40:41], v[42:43], %1, v[40:41] op_sel:[1,1,0] op_sel_hi:[1,0,1] neg_lo:[0,1,0]\n" \
  "v_pk_fma_f32 v[44:45], v[46:47], %1, v[44:45] op_sel:[1,1,0] op_sel_hi:[1,0,1] neg_lo:[0,1,0]\n" \
  "v_pk_mul_f32 v[42:43], v[40:41], %1 op_sel_hi:[0,1]\nv_pk_mul_f32 v[46:47], v[44:45], %1 op_sel_hi:[0,1]\n" \
  "v_pk_fma_f32 v[42:43], v[40:41], %1, v[42:43] op_sel:[1,1,0] op_sel_hi:[1,0,1] neg_lo:[0,1,0]\n" \
  "v_pk_fma_f32 v[46:47], v[44:45], %1, v[46:47] op_sel:[1,1,0] op_sel_hi:[1,0,1] neg_lo:[0,1,0]\nv_cvt_pk_f16_f32 v59, v40, v42\n" \
  "v_cvt_pk_f16_f32 v63, v41, v43\nv_cvt_pk_f16_f32 v65, v44, v46\nv_cvt_pk_f16_f32 v69, v45, v47\n" \
  "v_pk_mul_f32 v[44:45], v[46:47], %1 op_sel_hi:[0,1]\nds_write_b128 %3, v[56:59] offset:0\n" \
  "v_pk_fma_f32 v[44:45], v[46:47], %1, v[44:45] op_sel:[1,1,0] op_sel_hi:[1,0,1] neg_lo:[0,1,0]\nds_write_b128 %3, v[60:63] offset:64\n" \
  "v_pk_mul_f32 v[46:47], v[44:45], %1 op_sel_hi:[0,1]\nv_pk_mul_f32 v[50:51], v[48:49], %1 op_sel_hi:[0,1]\n" \
  "v_pk_fma_f32 v[46:47], v[44:45], %1, v[46:47] op_sel:[1,1,0] op_sel_hi:[1,0,1] neg_lo:[0,1,0]\n" \
  "v_pk_fma_f32 v[50:51], v[48:49], %1, v[50:51] op_sel:[1,1,0] op_sel_hi:[1,0,1] neg_lo:[0,1,0]\nv_cvt_pk_f16_f32 v66, v44, v46\n" \
  "v_cvt_pk_f16_f32 v70, v45, v47\nv_cvt_pk_f16_f32 v56, v48, v50\nv_cvt_pk_f16_f32 v60, v49, v51\n" \
  "v_pk_mul_f32 v[44:45], v[46:47], %1 op_sel_hi:[0,1]\nv_pk_mul_f32 v[48:49], v[50:51], %1 op_sel_hi:[0,1]\n" \
  "v_pk_fma_f32 v[44:45], v[46:47], %1, v[44:45] op_sel:[1,1,0] op_sel_hi:[1,0,1] neg_lo:[0,1,0]\n" \
  "v_pk_fma_f32 v[48:49], v[50:51], %1, v[48:49] op_sel:[1,1,0] op_sel_hi:[1,0,1] neg_lo:[0,1,0]\n" \
  "v_pk_mul_f32 v[46:47], v[44:45], %1 op_sel_hi:[0,1]\nv_pk_mul_f32 v[50:51], v[48:49], %1 op_sel_hi:[0,1]\n" \
  "v_pk_fma_f32 v[46:47], v[44:45], %1, v[46:47] op_sel:[1,1,0] op_sel_hi:[1,0,1] neg_lo:[0,1,0]\n" \
  "v_pk_fma_f32 v[50:51], v[48:49], %1, v[50:51] op_sel:[1,1,0] op_sel_hi:[1,0,1] neg_lo:[0,1,0]\nv_cvt_pk_f16_f32 v67, v44, v46\n" \
  "v_cvt_pk_f16_f32 v71, v45, v47\nv_cvt_pk_f16_f32 v57, v48, v50\nv_cvt_pk_f16_f32 v61, v49, v51\n" \
  "v_pk_mul_f32 v[48:49], v[50:51], %1 op_sel_hi:[0,1]\nds_write_b128 %3, v[64:67] offset:16\n" \
  "v_pk_fma_f32 v[48:49], v[50:51], %1, v[48:49] op_sel:[1,1,0] op_sel_hi:[1,0,1] neg_lo:[0,1,0]\nds_write_b128 %3, v[68:71] offset:80\n" \
  "v_pk_mul_f32 v[50:51], v[48:49], %1 op_sel_hi:[0,1]\nv_pk_mul_f32 v[54:55], v[52:53], %1 op_sel_hi:[0,1]\n" \
  "v_pk_fma_f32 v[50:51], v[48:49], %1, v[50:51] op_sel:[1,1,0] op_sel_hi:[1,0,1] neg_lo:[0,1,0]\n" \
  "v_pk_fma_f32 v[54:55], v[52:53], %1, v[54:55] op_sel:[1,1,0] op_sel_hi:[1,0,1] neg_lo:[0,1,0]\nv_cvt_pk_f16_f32 v58, v48, v50\n" \
  "v_cvt_pk_f16_f32 v62, v49, v51\nv_cvt_pk_f16_f32 v64, v52, v54\nv_cvt_pk_f16_f32 v68, v53, v55\n" \
  "v_pk_mul_f32 v[48:49], v[50:51], %1 op_sel_hi:[0,1]\nv_pk_mul_f32 v[52:53], v[54:55], %1 op_sel_hi:[0,1]\n" \
  "v_pk_fma_f32 v[48:49], v[50:51], %1, v[48:49] op_sel:[1,1,0] op_sel_hi:[1,0,1] neg_lo:[0,1,0]\n" \
  "v_pk_fma_f32 v[52:53], v[54:55], %1, v[52:53] op_sel:[1,1,0] op_sel_hi:[1,0,1] neg_lo:[0,1,0]\n" \
  "v_pk_mul_f32 v[50:51], v[48:49], %1 op_sel_hi:[0,1]\nv_pk_mul_f32 v[54:55], v[52:53], %1 op_sel_hi:[0,1]\n" \
  "v_pk_fma_f32 v[50:51], v[48:49], %1, v[50:51] op_sel:[1,1,0] op_sel_hi:[1,0,1] neg_lo:[0,1,0]\n" \
  "v_pk_fma_f32 v[54:55], v[52:53], %1, v[54:55] op_sel:[1,1,0] op_sel_hi:[1,0,1] neg_lo:[0,1,0]\nv_cvt_pk_f16_f32 v59, v48, v50\n" \
  "v_cvt_pk_f16_f32 v63, v49, v51\nv_cvt_pk_f16_f32 v65, v52, v54\nv_cvt_pk_f16_f32 v69, v53, v55\n" \
  "v_pk_mul_f32 v[52:53], v[54:55], %1 op_sel_hi:[0,1]\nds_write_b128 %3, v[56:59] offset:32\n" \
  "v_pk_fma_f32 v[52:53], v[54:55], %1, v[52:53] op_sel:[1,1,0] op_sel_hi:[1,0,1] neg_lo:[0,1,0]\nds_write_b128 %3, v[60:63] offset:96\n" \
  "v_pk_mul_f32 v[54:55], v[52:53], %1 op_sel_hi:[0,1]\n" \
  "v_pk_fma_f32 v[54:55], v[52:53], %1, v[54:55] op_sel:[1,1,0] op_sel_hi:[1,0,1] neg_lo:[0,1,0]\nv_cvt_pk_f16_f32 v66, v52, v54\n" \
  "v_cvt_pk_f16_f32 v70, v53, v55\nv_pk_mul_f32 v[52:53], v[54:55], %1 op_sel_hi:[0,1]\n" \
  "v_pk_fma_f32 v[52:53], v[54:55], %1, v[52:53] op_sel:[1,1,0] op_sel_hi:[1,0,1] neg_lo:[0,1,0]\n" \
  "v_pk_mul_f32 v[54:55], v[52:53], %1 op_sel_hi:[0,1]\n" \
  "v_pk_fma_f32 v[54:55], v[52:53], %1, v[54:55] op_sel:[1,1,0] op_sel_hi:[1,0,1] neg_lo:[0,1,0]\nv_cvt_pk_f16_f32 v67, v52, v54\n" \
  "v_cvt_pk_f16_f32 v71, v53, v55\nds_write_b128 %3, v[64:67] offset:48\nds_write_b128 %3, v[68:71] offset:112\n"
#define Y_CLOB "memory", "v40", "v41", "v42", "v43", "v44", "v45", "v46", "v47", "v48", "v49", "v50", "v51", "v52", "v53", "v54", "v55", "v56", "v57", "v58", "v59", "v60", "v61", "v62", "v63", "v64", "v65", "v66", "v67", "v68", "v69", "v70", "v71"
#define T_ASM \
  "ds_read_b128 %4, %8\nds_read_b64_tr_b16 v[80:81], %7 offset:0\nds_read_b64_tr_b16 v[82:83], %7 offset:64\n" \
  "ds_read_b64_tr_b16 v[84:85], %5 offset:0\nds_read_b64_tr_b16 v[86:87], %5 offset:48\nds_read_b64_tr_b16 v[88:89], %6 offset:0\n" \
  "ds_read_b64_tr_b16 v[90:91], %6 offset:48\nds_read_b64_tr_b16 v[92:93], %7 offset:3584\nds_read_b64_tr_b16 v[94:95], %7 offset:3648\n" \
  "ds_read_b64_tr_b16 v[96:97], %5 offset:3584\nds_read_b64_tr_b16 v[98:99], %5 offset:3632\nds_read_b64_tr_b16 v[100:101], %6 offset:3584\n" \
  "ds_read_b64_tr_b16 v[102:103], %6 offset:3632\nds_read_b64_tr_b16 v[104:105], %7 offset:7168\nds_read_b64_tr_b16 v[106:107], %7 offset:7232\n" \
  "s_waitcnt lgkmcnt(8)\nv_mfma_f32_16x16x32_f16 %0, v[84:87], v[80:83], 0\nds_read_b64_tr_b16 v[108:109], %5 offset:7168\n" \
  "ds_read_b64_tr_b16 v[110:111], %5 offset:7216\nds_read_b64_tr_b16 v[112:113], %6 offset:7168\nv_mfma_f32_16x16x32_f16 %2, v[88:91], v[80:83], 0\n" \
  "ds_read_b64_tr_b16 v[114:115], %6 offset:7216\nds_read_b64_tr_b16 v[116:117], %7 offset:10752\nds_read_b64_tr_b16 v[118:119], %7 offset:10816\n" \
  "s_waitcnt lgkmcnt(8)\nv_mfma_f32_16x16x32_f16 %0, v[96:99], v[92:95], %0\nds_read_b64_tr_b16 v[120:121], %5 offset:10752\n" \
  "ds_read_b64_tr_b16 v[122:123], %5 offset:10800\nds_read_b64_tr_b16 v[124:125], %6 offset:10752\n" \
  "v_mfma_f32_16x16x32_f16 %2, v[100:103], v[92:95], %2\nds_read_b64_tr_b16 v[126:127], %6 offset:10800\n" \
  "ds_read_b64_tr_b16 v[80:81], %7 offset:14336\nds_read_b64_tr_b16 v[82:83], %7 offset:14400\ns_waitcnt lgkmcnt(8)\n" \
  "v_mfma_f32_16x16x32_f16 %0, v[108:111], v[104:107], %0\nds_read_b64_tr_b16 v[84:85], %5 offset:14336\n" \
  "ds_read_b64_tr_b16 v[86:87], %5 offset:14384\nds_read_b64_tr_b16 v[88:89], %6 offset:14336\n" \
  "v_mfma_f32_16x16x32_f16 %2, v[112:115], v[104:107], %2\nds_read_b64_tr_b16 v[90:91], %6 offset:14384\n" \
  "ds_read_b64_tr_b16 v[92:93], %7 offset:17920\nds_read_b64_tr_b16 v[94:95], %7 offset:17984\ns_waitcnt lgkmcnt(8)\n" \
  "v_mfma_f32_16x16x32_f16 %0, v[120:123], v[116:119], %0\nds_read_b64_tr_b16 v[96:97], %5 offset:17920\n" \
  "ds_read_b64_tr_b16 v[98:99], %5 offset:17968\nds_read_b64_tr_b16 v[100:101], %6 offset:17920\n" \
  "v_mfma_f32_16x16x32_f16 %2, v[124:127], v[116:119], %2\nds_read_b64_tr_b16 v[102:103], %6 offset:17968\n" \
  "ds_read_b64_tr_b16 v[104:105], %7 offset:21504\nds_read_b64_tr_b16 v[106:107], %7 offset:21568\ns_waitcnt lgkmcnt(8)\n" \
  "v_mfma_f32_16x16x32_f16 %1, v[84:87], v[80:83], 0\nds_read_b64_tr_b16 v[108:109], %5 offset:21504\nds_read_b64_tr_b16 v[110:111], %5 offset:21552\n" \
  "ds_read_b64_tr_b16 v[112:113], %6 offset:21504\nv_mfma_f32_16x16x32_f16 %3, v[88:91], v[80:83], 0\nds_read_b64_tr_b16 v[114:115], %6 offset:21552\n" \
  "ds_read_b64_tr_b16 v[116:117], %7 offset:25088\nds_read_b64_tr_b16 v[118:119], %7 offset:25152\ns_waitcnt lgkmcnt(8)\n" \
  "v_mfma_f32_16x16x32_f16 %1, v[96:99], v[92:95], %1\nds_read_b64_tr_b16 v[120:121], %5 offset:25088\n" \
  "ds_read_b64_tr_b16 v[122:123], %5 offset:25136\nds_read_b64_tr_b16 v[124:125], %6 offset:25088\n" \
  "v_mfma_f32_16x16x32_f16 %3, v[100:103], v[92:95], %3\nds_read_b64_tr_b16 v[126:127], %6 offset:25136\ns_waitcnt lgkmcnt(6)\n" \
  "v_mfma_f32_16x16x32_f16 %1, v[108:111], v[104:107], %1\nv_mfma_f32_16x16x32_f16 %3, v[112:115], v[104:107], %3\ns_waitcnt lgkmcnt(0)\n" \
  "v_mfma_f32_16x16x32_f16 %1, v[120:123], v[116:119], %1\nv_mfma_f32_16x16x32_f16 %3, v[124:127], v[116:119], %3\n"
#define T_CLOB "memory", "v80", "v81", "v82", "v83", "v84", "v85", "v86", "v87", "v88", "v89", "v90", "v91", "v92", "v93", "v94", "v95", "v96", "v97", "v98", "v99", "v100", "v101", "v102", "v103", "v104", "v105", "v106", "v107", "v108", "v109", "v110", "v111", "v112", "v113", "v114", "v115", "v116", "v117", "v118", "v119", "v120", "v121", "v122", "v123", "v124", "v125", "v126", "v127"
template <class F> __device__ __forceinline__ int gen_y(float ldt, float are, float aim, float bre, float bim, float cre, float cim, uint32_t im, F&& mid TSP) {
  float ar2, air, g;
  const float dt = state_a(ldt, are, aim, ar2, air, g);
  const float dbr = dt * bre, dbi = dt * bim, pr = cre * dbr - cim * dbi, pi = cre * dbi + cim * dbr;
  const f32x2 w8 = cexp2(ar2, -air, 8.f), w64 = cexp2(ar2, -air, 64.f);
  const float pm = fmaxf(fabsf(pr), fabsf(pi));
  int e = -4;
  if (__builtin_expect(__any(!(pm < 0x1p-4f) || ar2 > 0.f) || !__any(pm > 0x1p-12f), 0)) e = (int)(wave_maxu(__float_as_uint(pm)) >> 23) - 126 + (int)ceilf(g * 250.f);
  const f32x2 y0 = {__builtin_amdgcn_ldexpf(pr, 14 - e), __builtin_amdgcn_ldexpf(-pi, 14 - e)};
  mid();
  TS(0, 7);
  asm volatile(Y_ASM : : "v"(y0), "v"(w8), "v"(w64), "v"(im) : Y_CLOB);
  return e;
}
__device__ __forceinline__ int gen_x(float ldt, float are, float aim, char* im) {
  float ar2, air, g;
  state_a(ldt, are, aim, ar2, air, g);
  const int e = min((int)ceilf(g * 1810.f), 300);
  const float s = __builtin_amdgcn_ldexpf(1.f, 15 - e);
  const f32x2 w1 = cexp2(ar2, air, 1.f), w256 = cexp2(ar2, air, 256.f), w2 = cmul(w1, w1);
  f32x2 a[8], b[8], c[8];
  a[0] = (f32x2){s, 0.f}; a[1] = w1 * s; a[2] = w2 * s;
  b[0] = b[1] = w256 * s;
  b[2] = cmul(b[0], w2); a[3] = cmul(a[1], w2); a[4] = cmul(a[2], w2);
  b[3] = b[4] = b[5] = cmul(b[2], w2); a[5] = cmul(a[3], w2); a[6] = cmul(a[4], w2);
  b[6] = cmul(b[3], w2); a[7] = cmul(a[5], w2); c[0] = cmul(b[3], w256);
  b[7] = cmul(b[6], w2); c[2] = cmul(c[0], w256);
  put_im<0>(im + I_X, 48, a);
  c[1] = cmul(b[7], w256); c[3] = cmul(c[1], w256);
  put_im<0>(im + I_X + 16, 48, b);
#pragma unroll
  for (int k = 4; k < 8; ++k) c[k] = cmul(c[k - 1], w256);
  put_im<0>(im + I_X + 32, 48, c);
  return e;
}

template <int I> struct IC { static constexpr int value = I; };
template <int B, int E, class F> __device__ __forceinline__ void static_for(F&& f) {
  if constexpr (B < E) { f(IC<B>{}); static_for<B + 1, E>(f); }
}
typedef __attribute__((ext_vector_type(4))) short s16x4;
__device__ __forceinline__ u32x2 trd(uint32_t a) {
  return __builtin_bit_cast(u32x2, __builtin_amdgcn_ds_read_tr16_b64_v4i16((__attribute__((address_space(3))) s16x4*)(uintptr_t)a));
}
#define BAR() asm volatile("s_waitcnt lgkmcnt(0)\n\ts_barrier" ::: "memory")
#define ST16(off, v) asm volatile("global_store_dwordx4 %0, %1, %2 sc1\n\ts_nop 1" : : "v"(off), "v"(v), "s"(ws) : "memory")
#define ST4(off, v) asm volatile("global_store_dword %0, %1, %2 sc1" : : "v"(off), "v"(v), "s"(ws) : "memory")
#define LD16(r, off) asm volatile("global_load_dwordx4 %0, %1, %2 sc1" : "=v"(r) : "v"(off), "s"(ws) : "memory")
#define LD16W(r, off) asm volatile("global_load_dwordx4 %0, %1, %2 sc1\n\ts_waitcnt vmcnt(0)" : "=&v"(r) : "v"(off), "s"(ws) : "memory")
#define DRAIN() asm volatile("s_waitcnt vmcnt(0)" ::: "memory")
#define CEX(a, b) { const uint32_t hi_ = max(a, b), lo_ = min(a, b); a = hi_; b = lo_; }
#define MMB(a_, b_, c_) __builtin_amdgcn_mfma_f32_16x16x32_bf16(__builtin_bit_cast(bf16x8, a_), __builtin_bit_cast(bf16x8, b_), c_, 0, 0, 0)
#define NTM 0
#define DPOL ""
#define DPOL2 ""
#define DPOL4 ""
constexpr int SPIN_CAP = 1 << 18;
constexpr uint32_t UMASK = 0x40074007u;
__device__ __forceinline__ uint32_t utag(uint32_t T, int j) {
  return ((T >> (3 * j)) & 0x00070007u) | (((j <= 2) ? (T << (2 - j)) : (T >> (j - 2))) & 0x40004000u);
}
__device__ __forceinline__ uint32_t max4(const u32x4& a) { return max(max(a[0], a[1]), max(a[2], a[3])); }
__device__ __forceinline__ void until(uint32_t t) {
#pragma unroll 1
  for (int spin = 0; spin < 600 && (int)((uint32_t)__builtin_amdgcn_s_memrealtime() - t) < 0; ++spin) __builtin_amdgcn_s_sleep(1);
}
__device__ __forceinline__ uint32_t latest(uint32_t t) {
  const uint32_t t0 = (uint32_t)__builtin_amdgcn_readfirstlane(t) - 0x40000000u;
  return wave_maxu(t - t0) + t0;
}
__device__ __forceinline__ uint32_t gate(const char* ws, uint32_t po, uint32_t T) {
  u32x2 pv;
#pragma unroll 1
  for (int spin = 0; spin < SPIN_CAP; ++spin) {
    asm volatile("global_load_dwordx2 %0, %1, %2 sc1\n\ts_waitcnt vmcnt(0)" : "=&v"(pv) : "v"(po), "s"(ws) : "memory");
    if (!__any(pv[0] != T)) break;
    __builtin_amdgcn_s_sleep(2);
  }
  return pv[1];
}
__device__ __forceinline__ uint32_t gate2(const char* ws, uint32_t oa, uint32_t da, uint32_t ob, uint32_t T) {
  u32x2 pa, pb;
#pragma unroll 1
  for (int spin = 0; spin < SPIN_CAP; ++spin) {
    asm volatile("global_load_dwordx2 %0, %2, %4 sc1\n\tglobal_load_dwordx2 %1, %3, %4 sc1\n\ts_waitcnt vmcnt(0)" : "=&v"(pa), "=&v"(pb) : "v"(oa), "v"(ob), "s"(ws) : "memory");
    if (!__any(pa[0] != T || pb[0] != T)) break;
    __builtin_amdgcn_s_sleep(4);
  }
  pa[1] += da;
  return latest(((int)(pa[1] - pb[1]) > 0) ? pa[1] : pb[1]);
}
__device__ __forceinline__ void gsync(uint32_t a, uint32_t target) {
  uint32_t t, c, n = SPIN_CAP;
  uint64_t ex;
  asm volatile("s_waitcnt lgkmcnt(0)\n\ts_mov_b64 %3, exec\n\ts_mov_b64 exec, 1\n\tv_mov_b32 %0, 1\n\tds_add_u32 %4, %0\n\ts_mov_b64 exec, %3\n"
               "1:\n\ts_sleep 1\n\tds_read_b32 %0, %4\n\ts_sub_u32 %2, %2, 1\n\ts_cbranch_scc1 2f\n\ts_waitcnt lgkmcnt(0)\n\tv_readfirstlane_b32 %1, %0\n\t"
               "s_cmp_lt_u32 %1, %5\n\ts_cbranch_scc1 1b\n2:\n\ts_waitcnt lgkmcnt(0)"
               : "=&v"(t), "=&s"(c), "+s"(n), "=&s"(ex) : "v"(a), "n"(target) : "memory", "scc");
}
__device__ __forceinline__ void gsync0(uint32_t a, uint32_t target) {
  uint32_t t, c, n = SPIN_CAP;
  asm volatile("s_waitcnt lgkmcnt(0)\n\ts_mov_b64 exec, 1\n\tv_mov_b32 %0, 1\n\tds_add_u32 %3, %0\n\ts_mov_b64 exec, -1\n"
               "1:\n\tds_read_b32 %0, %3\n\ts_sub_u32 %2, %2, 1\n\ts_cbranch_scc1 2f\n\ts_waitcnt lgkmcnt(0)\n\tv_readfirstlane_b32 %1, %0\n\t"
               "s_cmp_lt_u32 %1, %4\n\ts_cbranch_scc1 1b\n2:\n\ts_waitcnt lgkmcnt(0)"
               : "=&v"(t), "=&s"(c), "+s"(n) : "v"(a), "n"(target) : "memory", "scc");
}
__device__ __forceinline__ void gwait(uint32_t a, uint32_t target) {
  uint32_t t, c, n = SPIN_CAP;
  asm volatile("1:\n\ts_sleep 2\n\tds_read_b32 %0, %3\n\ts_sub_u32 %2, %2, 1\n\ts_cbranch_scc1 2f\n\ts_waitcnt lgkmcnt(0)\n\tv_readfirstlane_b32 %1, %0\n\t"
               "s_cmp_lt_u32 %1, %4\n\ts_cbranch_scc1 1b\n2:\n\ts_waitcnt lgkmcnt(0)"
               : "=&v"(t), "=&s"(c), "+s"(n) : "v"(a), "n"(target) : "memory", "scc");
}
__device__ __forceinline__ void gwait0(uint32_t a, uint32_t target) {
  uint32_t t, c, n = SPIN_CAP;
  asm volatile("1:\n\tds_read_b32 %0, %3\n\ts_sub_u32 %2, %2, 1\n\ts_cbranch_scc1 2f\n\ts_waitcnt lgkmcnt(0)\n\tv_readfirstlane_b32 %1, %0\n\t"
               "s_cmp_lt_u32 %1, %4\n\ts_cbranch_scc1 1b\n2:\n\ts_waitcnt lgkmcnt(0)"
               : "=&v"(t), "=&s"(c), "+s"(n) : "v"(a), "n"(target) : "memory", "scc");
}
__device__ __forceinline__ void dma16(const void* base, uint32_t voff, uint32_t lds) {
  uint32_t keep;
  asm volatile("s_mov_b32 %0, m0\n\ts_mov_b32 m0, %3\n\ts_nop 0\n\tglobal_load_lds_dwordx4 %1, %2" DPOL "\n\ts_mov_b32 m0, %0" : "=&s"(keep) : "v"(voff), "s"(base), "s"(lds) : "memory");
}
__device__ __forceinline__ void dma16d(const void* base, uint32_t voff, uint32_t lds) {
  uint32_t keep;
  asm volatile("s_mov_b32 %0, m0\n\ts_mov_b32 m0, %3\n\ts_nop 0\n\tglobal_load_lds_dwordx4 %1, %2" DPOL2 "\n\ts_mov_b32 m0, %0" : "=&s"(keep) : "v"(voff), "s"(base), "s"(lds) : "memory");
}
__device__ __forceinline__ void nap(int n) {
  for (; n > 15; n -= 15) __builtin_amdgcn_s_sleep(15);
  switch (n) {
    case 1: __builtin_amdgcn_s_sleep(1); break; case 2: __builtin_amdgcn_s_sleep(2); break; case 3: __builtin_amdgcn_s_sleep(3); break;
    case 4: __builtin_amdgcn_s_sleep(4); break; case 5: __builtin_amdgcn_s_sleep(5); break; case 6: __builtin_amdgcn_s_sleep(6); break;
    case 7: __builtin_amdgcn_s_sleep(7); break; case 8: __builtin_amdgcn_s_sleep(8); break; case 9: __builtin_amdgcn_s_sleep(9); break;
    case 10: __builtin_amdgcn_s_sleep(10); break; case 11: __builtin_amdgcn_s_sleep(11); break; case 12: __builtin_amdgcn_s_sleep(12); break;
    case 13: __builtin_amdgcn_s_sleep(13); break; case 14: __builtin_amdgcn_s_sleep(14); break; case 15: __builtin_amdgcn_s_sleep(15); break;
    default: break;
  }
}
#define SLEEP(n) do { if ((n) > 0) __builtin_amdgcn_s_sleep(n); } while (0)
#define USLEEP 3
#define SSLEEP 14
#define PSLEEP 2
#define PPRIO 0
#define LFIRST 1
#define WD 40
#define OSLEEP 12
#define QPRIO 1
#define KPRIO 2
#define GSLEEP 0
#define NSLEEP 0
#define UD 40
#define XD 0
#define RSLEEP 0
#define UPRIO 2
#define VD2 387
#define WDEPTH 4
#define ESLEEP 0
#define SD 58
#define KD 175
#define OD 0
#define KBACK 6
#define DDEPTH 6
#define C3 1
#define XROT 2
#define WSOFF 0
#define KN 2
#define RPF 1
#define LSLEEP 3
#define TSTOP 70
#define HSTOP 110
#define VEARLY 0ull
#define FPR 3
#define SPR 3
#define DPRIO 0
#define QPRIO2 3
#define HW 2
#define RD 55
#define OPOS 1
#define OD2 0
#define NA 12
#define RD2 88
#define ORD1 ((unsigned __int128)0x321087654u << 36 | 0x321087654ull)
#define WDEPTH2 4
#define RQ NA
#define HREV 1
#define QPRIO0 3
#define VPS 1
#define RW 0
#define NPRIO 0
#define XSL 10
#define V2D 22
#define N0 8
#define LPOS 0
#define FORCESLOW 0
#define FORCEFB 0
#define CONV_ASM \
  "s_cmp_eq_u32 s89, 1\ns_cbranch_scc1 1f\ns_cmp_gt_u32 s89, 1\ns_cbranch_scc1 9f\nds_read_b64 v[124:125], v3 offset:576\n" \
  "ds_read_b64 v[126:127], v3 offset:584\nds_read_b64 v[120:121], v3 offset:64\nds_read_b64 v[122:123], v3 offset:72\n" \
  "ds_read_b64 v[116:117], v3 offset:640\nds_read_b64 v[118:119], v3 offset:648\nds_read_b64 v[112:113], v3 offset:128\n" \
  "ds_read_b64 v[114:115], v3 offset:136\nds_read_b64 v[108:109], v3 offset:704\nds_read_b64 v[110:111], v3 offset:712\n" \
  "ds_read_b64 v[104:105], v3 offset:192\nds_read_b64 v[106:107], v3 offset:200\nds_read_b64 v[100:101], v3 offset:768\n" \
  "ds_read_b64 v[102:103], v3 offset:776\nds_read_b64 v[96:97], v3 offset:256\nds_read_b64 v[98:99], v3 offset:264\n" \
  "ds_read_b64 v[92:93], v3 offset:832\nds_read_b64 v[94:95], v3 offset:840\nds_read_b64 v[88:89], v3 offset:320\n" \
  "ds_read_b64 v[90:91], v3 offset:328\nds_read_b64 v[84:85], v3 offset:896\nds_read_b64 v[86:87], v3 offset:904\n" \
  "ds_read_b64 v[80:81], v3 offset:384\nds_read_b64 v[82:83], v3 offset:392\nds_read_b64 v[76:77], v3 offset:960\n" \
  "ds_read_b64 v[78:79], v3 offset:968\nds_read_b64 v[72:73], v3 offset:448\nds_read_b64 v[74:75], v3 offset:456\n" \
  "ds_read_b64 v[68:69], v3 offset:1024\nds_read_b64 v[70:71], v3 offset:1032\nds_read_b64 v[64:65], v3 offset:512\n" \
  "ds_read_b64 v[66:67], v3 offset:520\nds_read_b64 v[60:61], v3 offset:0\nds_read_b64 v[62:63], v3 offset:8\nds_read_b64 v[56:57], v3 offset:1088\n" \
  "ds_read_b64 v[58:59], v3 offset:1096\ns_waitcnt lgkmcnt(0)\ns_barrier\nds_read_b128 v[36:39], v2 offset:0\nds_read_b128 v[40:43], v2 offset:64\n" \
  "ds_read_b128 v[44:47], v2 offset:128\nds_read_b128 v[48:51], v2 offset:192\ns_waitcnt lgkmcnt(3)\n" \
  "v_mfma_f32_16x16x32_f16 v[12:15], v[124:127], v[36:39], 0\ns_setprio 1\nds_read_b128 v[52:55], v2 offset:256\n" \
  "v_mfma_f32_16x16x32_f16 v[16:19], v[120:123], v[36:39], 0\ns_waitcnt lgkmcnt(3)\nv_mfma_f32_16x16x32_f16 v[12:15], v[116:119], v[40:43], v[12:15]\n" \
  "ds_read_b128 v[36:39], v2 offset:320\nv_mfma_f32_16x16x32_f16 v[16:19], v[112:115], v[40:43], v[16:19]\ns_waitcnt lgkmcnt(3)\n" \
  "v_mfma_f32_16x16x32_f16 v[12:15], v[108:111], v[44:47], v[12:15]\nds_read_b128 v[40:43], v2 offset:384\n" \
  "v_mfma_f32_16x16x32_f16 v[16:19], v[104:107], v[44:47], v[16:19]\ns_waitcnt lgkmcnt(3)\n" \
  "v_mfma_f32_16x16x32_f16 v[12:15], v[100:103], v[48:51], v[12:15]\nds_read_b128 v[44:47], v2 offset:448\n" \
  "v_mfma_f32_16x16x32_f16 v[16:19], v[96:99], v[48:51], v[16:19]\ns_waitcnt lgkmcnt(3)\n" \
  "v_mfma_f32_16x16x32_f16 v[12:15], v[92:95], v[52:55], v[12:15]\nv_mfma_f32_16x16x32_f16 v[16:19], v[88:91], v[52:55], v[16:19]\n" \
  "s_waitcnt lgkmcnt(2)\nv_mfma_f32_16x16x32_f16 v[12:15], v[84:87], v[36:39], v[12:15]\nds_read_b128 v[48:51], v2 offset:512\n" \
  "v_mfma_f32_16x16x32_f16 v[16:19], v[80:83], v[36:39], v[16:19]\ns_waitcnt lgkmcnt(2)\n" \
  "v_mfma_f32_16x16x32_f16 v[12:15], v[76:79], v[40:43], v[12:15]\nds_read_b128 v[52:55], v2 offset:576\n" \
  "v_mfma_f32_16x16x32_f16 v[16:19], v[72:75], v[40:43], v[16:19]\ns_waitcnt lgkmcnt(2)\n" \
  "v_mfma_f32_16x16x32_f16 v[12:15], v[68:71], v[44:47], v[12:15]\nds_read_b128 v[36:39], v2 offset:640\n" \
  "v_mfma_f32_16x16x32_f16 v[16:19], v[64:67], v[44:47], v[16:19]\nv_mfma_f32_16x16x32_f16 v[20:23], v[60:63], v[44:47], 0\n" \
  "ds_read_b128 v[40:43], v2 offset:704\nv_mfma_f32_16x16x32_f16 v[28:31], v[56:59], v[44:47], 0\ns_waitcnt lgkmcnt(3)\n" \
  "v_mfma_f32_16x16x32_f16 v[16:19], v[124:127], v[48:51], v[16:19]\nds_read_b128 v[56:59], v2 offset:768\nv_cvt_pkrtz_f16_f32 v32, v12, v13\n" \
  "v_mfma_f32_16x16x32_f16 v[20:23], v[120:123], v[48:51], v[20:23]\nv_cvt_pkrtz_f16_f32 v33, v14, v15\ns_waitcnt lgkmcnt(3)\n" \
  "v_mfma_f32_16x16x32_f16 v[16:19], v[116:119], v[52:55], v[16:19]\nds_read_b128 v[44:47], v2 offset:832\n" \
  "v_pk_add_u16 v32, v32, 4 op_sel_hi:[1,0] clamp\nv_mfma_f32_16x16x32_f16 v[20:23], v[112:115], v[52:55], v[20:23]\n" \
  "v_pk_add_u16 v33, v33, 4 op_sel_hi:[1,0] clamp\ns_waitcnt lgkmcnt(3)\nv_mfma_f32_16x16x32_f16 v[16:19], v[108:111], v[36:39], v[16:19]\n" \
  "ds_read_b128 v[48:51], v2 offset:896\nds_write_b64 v4, v[32:33] offset:0\nv_mfma_f32_16x16x32_f16 v[20:23], v[104:107], v[36:39], v[20:23]\n" \
  "s_waitcnt lgkmcnt(3)\nv_mfma_f32_16x16x32_f16 v[16:19], v[100:103], v[40:43], v[16:19]\nds_read_b128 v[52:55], v2 offset:960\n" \
  "v_mfma_f32_16x16x32_f16 v[20:23], v[96:99], v[40:43], v[20:23]\nv_mfma_f32_16x16x32_f16 v[16:19], v[92:95], v[56:59], v[16:19]\n" \
  "v_mfma_f32_16x16x32_f16 v[20:23], v[88:91], v[56:59], v[20:23]\nds_read_b128 v[36:39], v2 offset:1024\ns_waitcnt lgkmcnt(3)\n" \
  "v_mfma_f32_16x16x32_f16 v[16:19], v[84:87], v[44:47], v[16:19]\nv_mfma_f32_16x16x32_f16 v[20:23], v[80:83], v[44:47], v[20:23]\n" \
  "ds_read_b128 v[40:43], v2 offset:1088\nds_read_b64 v[56:57], v6 offset:256\nds_read_b64 v[58:59], v6 offset:264\n" \
  "v_mfma_f32_16x16x32_f16 v[16:19], v[76:79], v[48:51], v[16:19]\nv_mfma_f32_16x16x32_f16 v[20:23], v[72:75], v[48:51], v[20:23]\n" \
  "ds_read_b128 v[44:47], v2 offset:1152\ns_waitcnt lgkmcnt(4)\nv_mfma_f32_16x16x32_f16 v[16:19], v[68:71], v[52:55], v[16:19]\n" \
  "v_mfma_f32_16x16x32_f16 v[20:23], v[64:67], v[52:55], v[20:23]\nds_read_b128 v[48:51], v2 offset:1216\n" \
  "v_mfma_f32_16x16x32_f16 v[24:27], v[60:63], v[52:55], 0\nv_mfma_f32_16x16x32_f16 v[20:23], v[124:127], v[36:39], v[20:23]\n" \
  "ds_read_b128 v[52:55], v2 offset:1280\nv_mfma_f32_16x16x32_f16 v[24:27], v[120:123], v[36:39], v[24:27]\ns_waitcnt lgkmcnt(5)\n" \
  "v_mfma_f32_16x16x32_f16 v[20:23], v[116:119], v[40:43], v[20:23]\nds_read_b128 v[36:39], v2 offset:1344\nv_cvt_pkrtz_f16_f32 v32, v16, v17\n" \
  "v_mfma_f32_16x16x32_f16 v[24:27], v[112:115], v[40:43], v[24:27]\nv_cvt_pkrtz_f16_f32 v33, v18, v19\ns_waitcnt lgkmcnt(3)\n" \
  "v_mfma_f32_16x16x32_f16 v[20:23], v[108:111], v[44:47], v[20:23]\nds_read_b128 v[40:43], v2 offset:1408\n" \
  "v_pk_add_u16 v32, v32, 4 op_sel_hi:[1,0] clamp\nds_read_b32 v35, v9 offset:32\nv_mfma_f32_16x16x32_f16 v[24:27], v[104:107], v[44:47], v[24:27]\n" \
  "v_pk_add_u16 v33, v33, 4 op_sel_hi:[1,0] clamp\ns_waitcnt lgkmcnt(3)\nv_mfma_f32_16x16x32_f16 v[20:23], v[100:103], v[48:51], v[20:23]\n" \
  "ds_read_b128 v[44:47], v2 offset:1472\nds_write_b64 v4, v[32:33] offset:1024\nv_mfma_f32_16x16x32_f16 v[24:27], v[96:99], v[48:51], v[24:27]\n" \
  "v_mfma_f32_16x16x32_f16 v[20:23], v[92:95], v[52:55], v[20:23]\nv_mfma_f32_16x16x32_f16 v[24:27], v[88:91], v[52:55], v[24:27]\n" \
  "ds_read_b128 v[48:51], v2 offset:1536\ns_waitcnt lgkmcnt(3)\nv_readfirstlane_b32 s85, v35\ns_cmp_lt_u32 s85, 4\ns_cbranch_scc1 21f\n22:\n" \
  "v_mfma_f32_16x16x32_f16 v[20:23], v[84:87], v[36:39], v[20:23]\nv_mfma_f32_16x16x32_f16 v[24:27], v[80:83], v[36:39], v[24:27]\n" \
  "ds_read_b128 v[52:55], v2 offset:1600\nv_mfma_f32_16x16x32_f16 v[20:23], v[76:79], v[40:43], v[20:23]\n" \
  "v_mfma_f32_16x16x32_f16 v[24:27], v[72:75], v[40:43], v[24:27]\nds_read_b128 v[36:39], v2 offset:1664\ns_waitcnt lgkmcnt(4)\n" \
  "v_mfma_f32_16x16x32_f16 v[20:23], v[68:71], v[44:47], v[20:23]\nv_mfma_f32_16x16x32_f16 v[24:27], v[64:67], v[44:47], v[24:27]\n" \
  "ds_read_b128 v[40:43], v2 offset:1728\nv_mfma_f32_16x16x32_f16 v[28:31], v[60:63], v[44:47], v[28:31]\nds_read_b64_tr_b16 v[60:61], v5 offset:0\n" \
  "ds_read_b64_tr_b16 v[62:63], v5 offset:32\ns_waitcnt lgkmcnt(4)\nv_mfma_f32_16x16x32_f16 v[24:27], v[124:127], v[48:51], v[24:27]\n" \
  "v_mfma_f32_16x16x32_f16 v[28:31], v[120:123], v[48:51], v[28:31]\nds_read_b64_tr_b16 v[44:45], v5 offset:256\n" \
  "ds_read_b64_tr_b16 v[46:47], v5 offset:288\nv_mfma_f32_16x16x32_f16 v[24:27], v[116:119], v[52:55], v[24:27]\n" \
  "ds_read_b64 v[120:121], v6 offset:320\nds_read_b64 v[122:123], v6 offset:328\nv_mfma_f32_16x16x32_f16 v[28:31], v[112:115], v[52:55], v[28:31]\n" \
  "ds_read_b128 v[48:51], v2 offset:1792\ns_waitcnt lgkmcnt(7)\nv_mfma_f32_16x16x32_f16 v[24:27], v[108:111], v[36:39], v[24:27]\n" \
  "ds_read_b128 v[112:115], v2 offset:1856\nv_mfma_f32_16x16x32_f16 v[28:31], v[104:107], v[36:39], v[28:31]\nds_read_b64 v[52:53], v6 offset:128\n" \
  "ds_read_b64 v[54:55], v6 offset:136\nv_mfma_f32_16x16x32_f16 v[24:27], v[100:103], v[40:43], v[24:27]\nds_read_b128 v[104:107], v2 offset:1920\n" \
  "v_mfma_f32_16x16x32_f16 v[28:31], v[96:99], v[40:43], v[28:31]\nds_read_b64 v[36:37], v6 offset:192\nds_read_b64 v[38:39], v6 offset:200\n" \
  "s_waitcnt lgkmcnt(9)\nv_mfma_f32_16x16x32_f16 v[20:23], v[60:63], v[56:59], v[20:23]\nds_read_b128 v[96:99], v2 offset:1984\ns_waitcnt lgkmcnt(7)\n" \
  "v_mfma_f32_16x16x32_f16 v[20:23], v[44:47], v[120:123], v[20:23]\nv_mfma_f32_16x16x32_f16 v[24:27], v[92:95], v[48:51], v[24:27]\n" \
  "ds_read_b128 v[40:43], v2 offset:2048\nv_mfma_f32_16x16x32_f16 v[28:31], v[88:91], v[48:51], v[28:31]\nds_read_b64_tr_b16 v[88:89], v5 offset:512\n" \
  "ds_read_b64_tr_b16 v[90:91], v5 offset:544\ns_waitcnt lgkmcnt(6)\nv_mfma_f32_16x16x32_f16 v[24:27], v[84:87], v[112:115], v[24:27]\n" \
  "v_mfma_f32_16x16x32_f16 v[28:31], v[80:83], v[112:115], v[28:31]\nv_cvt_pkrtz_f16_f32 v32, v20, v21\nv_cvt_pkrtz_f16_f32 v33, v22, v23\n" \
  "v_mfma_f32_16x16x32_f16 v[24:27], v[76:79], v[104:107], v[24:27]\nds_read_b64_tr_b16 v[48:49], v5 offset:768\n" \
  "ds_read_b64_tr_b16 v[50:51], v5 offset:800\nv_mfma_f32_16x16x32_f16 v[28:31], v[72:75], v[104:107], v[28:31]\n" \
  "ds_read_b128 v[80:83], v2 offset:2112\ns_waitcnt lgkmcnt(5)\nv_mfma_f32_16x16x32_f16 v[24:27], v[68:71], v[96:99], v[24:27]\n" \
  "ds_read_b128 v[112:115], v2 offset:2176\nv_pk_add_u16 v32, v32, 4 op_sel_hi:[1,0] clamp\n" \
  "v_mfma_f32_16x16x32_f16 v[28:31], v[64:67], v[96:99], v[28:31]\nds_read_b128 v[72:75], v2 offset:2240\n" \
  "v_pk_add_u16 v33, v33, 4 op_sel_hi:[1,0] clamp\nv_mfma_f32_16x16x32_f16 v[28:31], v[124:127], v[40:43], v[28:31]\n" \
  "ds_read_b128 v[104:107], v2 offset:2304\nds_write_b64 v4, v[32:33] offset:2048\nv_mfma_f32_16x16x32_f16 v[24:27], v[60:63], v[52:55], v[24:27]\n" \
  "ds_read_b128 v[64:67], v2 offset:2368\nds_read_b64 v[96:97], v6 offset:0\nds_read_b64 v[98:99], v6 offset:8\n" \
  "v_mfma_f32_16x16x32_f16 v[24:27], v[44:47], v[36:39], v[24:27]\nds_read_b128 v[124:127], v2 offset:2432\ns_waitcnt lgkmcnt(9)\n" \
  "v_mfma_f32_16x16x32_f16 v[24:27], v[88:91], v[56:59], v[24:27]\nds_read_b128 v[40:43], v2 offset:2496\n" \
  "v_mfma_f32_16x16x32_f16 v[24:27], v[48:51], v[120:123], v[24:27]\ns_waitcnt lgkmcnt(8)\n" \
  "v_mfma_f32_16x16x32_f16 v[28:31], v[116:119], v[80:83], v[28:31]\nds_read_b64 v[116:117], v6 offset:64\nds_read_b64 v[118:119], v6 offset:72\n" \
  "v_mfma_f32_16x16x32_f16 v[28:31], v[108:111], v[112:115], v[28:31]\nds_read_b64_tr_b16 v[80:81], v5 offset:1024\n" \
  "ds_read_b64_tr_b16 v[82:83], v5 offset:1056\ns_waitcnt lgkmcnt(10)\nv_mfma_f32_16x16x32_f16 v[28:31], v[100:103], v[72:75], v[28:31]\n" \
  "v_cvt_pkrtz_f16_f32 v32, v24, v25\nv_cvt_pkrtz_f16_f32 v33, v26, v27\nv_mfma_f32_16x16x32_f16 v[28:31], v[92:95], v[104:107], v[28:31]\n" \
  "v_pk_add_u16 v32, v32, 4 op_sel_hi:[1,0] clamp\ns_waitcnt lgkmcnt(5)\nv_mfma_f32_16x16x32_f16 v[28:31], v[84:87], v[64:67], v[28:31]\n" \
  "ds_read_b64_tr_b16 v[108:109], v5 offset:1280\nds_read_b64_tr_b16 v[110:111], v5 offset:1312\n" \
  "v_mfma_f32_16x16x32_f16 v[28:31], v[76:79], v[124:127], v[28:31]\nds_read_b64_tr_b16 v[112:113], v7 offset:0\n" \
  "ds_read_b64_tr_b16 v[114:115], v7 offset:64\ns_waitcnt lgkmcnt(6)\nv_mfma_f32_16x16x32_f16 v[28:31], v[68:71], v[40:43], v[28:31]\n" \
  "ds_read2_b32 v[100:101], v8 offset0:48 offset1:49\nds_read2_b32 v[102:103], v8 offset0:50 offset1:51\n" \
  "v_mfma_f32_16x16x32_f16 v[28:31], v[60:63], v[96:99], v[28:31]\nv_pk_add_u16 v33, v33, 4 op_sel_hi:[1,0] clamp\n" \
  "ds_write_b64 v4, v[32:33] offset:3072\nv_mfma_f32_16x16x32_f16 v[28:31], v[44:47], v[116:119], v[28:31]\n" \
  "v_mfma_f32_16x16x32_f16 v[28:31], v[88:91], v[52:55], v[28:31]\nv_mfma_f32_16x16x32_f16 v[28:31], v[48:51], v[36:39], v[28:31]\n" \
  "s_waitcnt lgkmcnt(5)\nv_mfma_f32_16x16x32_f16 v[28:31], v[80:83], v[56:59], v[28:31]\n" \
  "v_mfma_f32_16x16x32_f16 v[28:31], v[108:111], v[120:123], v[28:31]\ns_waitcnt lgkmcnt(1)\n" \
  "v_mfma_f32_16x16x32_f16 v[28:31], v[112:115], v[100:103], v[28:31]\ns_nop 7\nv_cvt_pkrtz_f16_f32 v32, v28, v29\nv_cvt_pkrtz_f16_f32 v33, v30, v31\n" \
  "v_pk_add_u16 v32, v32, 4 op_sel_hi:[1,0] clamp\nv_pk_add_u16 v33, v33, 4 op_sel_hi:[1,0] clamp\nds_write_b64 v4, v[32:33] offset:4096\n" \
  "s_setprio 0\ns_setpc_b64 s[90:91]\n21:\ns_movk_i32 s84, 0x4000\n23:\nds_read_b32 v35, v9 offset:32\ns_sub_u32 s84, s84, 1\ns_cbranch_scc1 22b\n" \
  "s_waitcnt lgkmcnt(0)\nv_readfirstlane_b32 s85, v35\ns_cmp_lt_u32 s85, 4\ns_cbranch_scc1 23b\ns_branch 22b\n1:\n" \
  "ds_read_b64 v[124:125], v3 offset:1088\nds_read_b64 v[126:127], v3 offset:1096\nds_read_b64 v[120:121], v3 offset:0\n" \
  "ds_read_b64 v[122:123], v3 offset:8\nds_read_b64 v[116:117], v3 offset:64\nds_read_b64 v[118:119], v3 offset:72\n" \
  "ds_read_b64 v[112:113], v3 offset:128\nds_read_b64 v[114:115], v3 offset:136\nds_read_b64 v[108:109], v3 offset:192\n" \
  "ds_read_b64 v[110:111], v3 offset:200\nds_read_b64 v[104:105], v3 offset:256\nds_read_b64 v[106:107], v3 offset:264\n" \
  "ds_read_b64 v[100:101], v3 offset:320\nds_read_b64 v[102:103], v3 offset:328\nds_read_b64 v[96:97], v3 offset:384\n" \
  "ds_read_b64 v[98:99], v3 offset:392\nds_read_b64 v[92:93], v3 offset:448\nds_read_b64 v[94:95], v3 offset:456\n" \
  "ds_read_b64 v[88:89], v3 offset:512\nds_read_b64 v[90:91], v3 offset:520\nds_read_b64 v[84:85], v3 offset:576\n" \
  "ds_read_b64 v[86:87], v3 offset:584\nds_read_b64 v[80:81], v3 offset:640\nds_read_b64 v[82:83], v3 offset:648\n" \
  "ds_read_b64 v[76:77], v3 offset:704\nds_read_b64 v[78:79], v3 offset:712\nds_read_b64 v[72:73], v3 offset:768\n" \
  "ds_read_b64 v[74:75], v3 offset:776\nds_read_b64 v[68:69], v3 offset:832\nds_read_b64 v[70:71], v3 offset:840\n" \
  "ds_read_b64 v[64:65], v3 offset:896\nds_read_b64 v[66:67], v3 offset:904\nds_read_b64 v[60:61], v3 offset:960\n" \
  "ds_read_b64 v[62:63], v3 offset:968\nds_read_b64 v[56:57], v3 offset:1024\nds_read_b64 v[58:59], v3 offset:1032\n" \
  "ds_read_b64 v[52:53], v6 offset:0\nds_read_b64 v[54:55], v6 offset:8\nds_read_b64 v[48:49], v6 offset:64\nds_read_b64 v[50:51], v6 offset:72\n" \
  "s_waitcnt lgkmcnt(0)\ns_barrier\nds_read_b128 v[28:31], v2 offset:960\nds_read_b128 v[32:35], v2 offset:1472\n" \
  "ds_read_b128 v[36:39], v2 offset:1984\nds_read_b128 v[40:43], v2 offset:2048\nds_read_b128 v[44:47], v2 offset:2112\ns_waitcnt lgkmcnt(3)\n" \
  "v_mfma_f32_16x16x32_f16 v[12:15], v[124:127], v[28:31], 0\ns_setprio 1\nds_read_b128 v[28:31], v2 offset:2176\n" \
  "v_mfma_f32_16x16x32_f16 v[16:19], v[124:127], v[32:35], 0\nds_read_b128 v[32:35], v2 offset:2240\ns_waitcnt lgkmcnt(3)\n" \
  "v_mfma_f32_16x16x32_f16 v[12:15], v[120:123], v[36:39], v[12:15]\nv_mfma_f32_16x16x32_f16 v[20:23], v[124:127], v[36:39], 0\n" \
  "ds_read_b128 v[124:127], v2 offset:2304\nds_read_b128 v[36:39], v2 offset:2368\nv_mfma_f32_16x16x32_f16 v[12:15], v[116:119], v[40:43], v[12:15]\n" \
  "ds_read_b128 v[40:43], v2 offset:2432\ns_waitcnt lgkmcnt(4)\nv_mfma_f32_16x16x32_f16 v[12:15], v[112:115], v[44:47], v[12:15]\n" \
  "ds_read_b128 v[44:47], v2 offset:2496\nv_mfma_f32_16x16x32_f16 v[12:15], v[108:111], v[28:31], v[12:15]\nds_read_b128 v[28:31], v2 offset:2560\n" \
  "s_waitcnt lgkmcnt(4)\nv_mfma_f32_16x16x32_f16 v[12:15], v[104:107], v[32:35], v[12:15]\nds_read_b128 v[32:35], v2 offset:2624\n" \
  "v_mfma_f32_16x16x32_f16 v[12:15], v[100:103], v[124:127], v[12:15]\ns_waitcnt lgkmcnt(3)\n" \
  "v_mfma_f32_16x16x32_f16 v[12:15], v[96:99], v[36:39], v[12:15]\nds_read_b128 v[124:127], v2 offset:2688\n" \
  "v_mfma_f32_16x16x32_f16 v[12:15], v[92:95], v[40:43], v[12:15]\ns_waitcnt lgkmcnt(3)\n" \
  "v_mfma_f32_16x16x32_f16 v[12:15], v[88:91], v[44:47], v[12:15]\nds_read_b128 v[36:39], v2 offset:2752\n" \
  "v_mfma_f32_16x16x32_f16 v[16:19], v[120:123], v[44:47], v[16:19]\ns_waitcnt lgkmcnt(3)\n" \
  "v_mfma_f32_16x16x32_f16 v[12:15], v[84:87], v[28:31], v[12:15]\nds_read_b128 v[40:43], v2 offset:2816\n" \
  "v_mfma_f32_16x16x32_f16 v[16:19], v[116:119], v[28:31], v[16:19]\ns_waitcnt lgkmcnt(3)\n" \
  "v_mfma_f32_16x16x32_f16 v[12:15], v[80:83], v[32:35], v[12:15]\nds_read_b128 v[44:47], v2 offset:2880\n" \
  "v_mfma_f32_16x16x32_f16 v[16:19], v[112:115], v[32:35], v[16:19]\ns_waitcnt lgkmcnt(3)\n" \
  "v_mfma_f32_16x16x32_f16 v[12:15], v[76:79], v[124:127], v[12:15]\nds_read_b128 v[28:31], v2 offset:2944\n" \
  "v_mfma_f32_16x16x32_f16 v[16:19], v[108:111], v[124:127], v[16:19]\ns_waitcnt lgkmcnt(3)\n" \
  "v_mfma_f32_16x16x32_f16 v[12:15], v[72:75], v[36:39], v[12:15]\nds_read_b128 v[32:35], v2 offset:3008\n" \
  "v_mfma_f32_16x16x32_f16 v[16:19], v[104:107], v[36:39], v[16:19]\ns_waitcnt lgkmcnt(3)\n" \
  "v_mfma_f32_16x16x32_f16 v[12:15], v[68:71], v[40:43], v[12:15]\nv_mfma_f32_16x16x32_f16 v[16:19], v[100:103], v[40:43], v[16:19]\n" \
  "ds_read_b128 v[124:127], v2 offset:3072\ns_waitcnt lgkmcnt(3)\nv_mfma_f32_16x16x32_f16 v[12:15], v[64:67], v[44:47], v[12:15]\n" \
  "v_mfma_f32_16x16x32_f16 v[16:19], v[96:99], v[44:47], v[16:19]\nds_read_b128 v[36:39], v2 offset:3136\ns_waitcnt lgkmcnt(3)\n" \
  "v_mfma_f32_16x16x32_f16 v[12:15], v[60:63], v[28:31], v[12:15]\nds_read_b64 v[40:41], v6 offset:128\nds_read_b64 v[42:43], v6 offset:136\n" \
  "v_mfma_f32_16x16x32_f16 v[16:19], v[92:95], v[28:31], v[16:19]\nds_read_b128 v[44:47], v2 offset:3200\ns_waitcnt lgkmcnt(4)\n" \
  "v_mfma_f32_16x16x32_f16 v[12:15], v[56:59], v[32:35], v[12:15]\nv_mfma_f32_16x16x32_f16 v[16:19], v[88:91], v[32:35], v[16:19]\n" \
  "ds_read_b128 v[28:31], v2 offset:3264\nv_mfma_f32_16x16x32_f16 v[20:23], v[120:123], v[32:35], v[20:23]\nds_read_b64 v[120:121], v6 offset:192\n" \
  "ds_read_b64 v[122:123], v6 offset:200\nv_mfma_f32_16x16x32_f16 v[16:19], v[84:87], v[124:127], v[16:19]\nds_read_b128 v[32:35], v2 offset:3328\n" \
  "v_mfma_f32_16x16x32_f16 v[20:23], v[116:119], v[124:127], v[20:23]\nds_read_b64 v[116:117], v6 offset:256\nds_read_b64 v[118:119], v6 offset:264\n" \
  "s_waitcnt lgkmcnt(6)\nv_mfma_f32_16x16x32_f16 v[16:19], v[80:83], v[36:39], v[16:19]\nds_read_b128 v[124:127], v2 offset:3392\n" \
  "v_mfma_f32_16x16x32_f16 v[20:23], v[112:115], v[36:39], v[20:23]\nds_read_b64 v[112:113], v6 offset:320\nds_read_b64 v[114:115], v6 offset:328\n" \
  "v_mfma_f32_16x16x32_f16 v[16:19], v[76:79], v[44:47], v[16:19]\nds_read_b128 v[36:39], v2 offset:3456\n" \
  "v_mfma_f32_16x16x32_f16 v[20:23], v[108:111], v[44:47], v[20:23]\ns_waitcnt lgkmcnt(6)\n" \
  "v_mfma_f32_16x16x32_f16 v[16:19], v[72:75], v[28:31], v[16:19]\nds_read_b128 v[108:111], v2 offset:3520\n" \
  "v_mfma_f32_16x16x32_f16 v[20:23], v[104:107], v[28:31], v[20:23]\nv_mfma_f32_16x16x32_f16 v[16:19], v[68:71], v[32:35], v[16:19]\n" \
  "ds_read_b128 v[44:47], v2 offset:3584\nv_mfma_f32_16x16x32_f16 v[20:23], v[100:103], v[32:35], v[20:23]\nds_read_b128 v[104:107], v2 offset:3648\n" \
  "ds_read_b32 v27, v9 offset:32\ns_waitcnt lgkmcnt(4)\nv_mfma_f32_16x16x32_f16 v[16:19], v[64:67], v[124:127], v[16:19]\n" \
  "ds_read_b128 v[28:31], v2 offset:3712\nv_mfma_f32_16x16x32_f16 v[20:23], v[96:99], v[124:127], v[20:23]\nds_read_b128 v[100:103], v2 offset:3776\n" \
  "ds_read2_b32 v[32:33], v8 offset0:32 offset1:33\nds_read2_b32 v[34:35], v8 offset0:34 offset1:35\n" \
  "v_mfma_f32_16x16x32_f16 v[16:19], v[60:63], v[36:39], v[16:19]\nds_read_b128 v[96:99], v2 offset:3840\n" \
  "v_mfma_f32_16x16x32_f16 v[20:23], v[92:95], v[36:39], v[20:23]\nds_read_b128 v[124:127], v2 offset:3904\ns_waitcnt lgkmcnt(8)\n" \
  "v_mfma_f32_16x16x32_f16 v[16:19], v[56:59], v[108:111], v[16:19]\nds_read_b128 v[92:95], v2 offset:3968\ns_waitcnt lgkmcnt(7)\n" \
  "v_readfirstlane_b32 s85, v27\ns_cmp_lt_u32 s85, 4\ns_cbranch_scc1 31f\n32:\nv_mfma_f32_16x16x32_f16 v[20:23], v[88:91], v[108:111], v[20:23]\n" \
  "ds_read_b128 v[36:39], v2 offset:4032\nds_read_b64_tr_b16 v[88:89], v5 offset:512\nds_read_b64_tr_b16 v[90:91], v5 offset:544\n" \
  "v_mfma_f32_16x16x32_f16 v[20:23], v[84:87], v[44:47], v[20:23]\nds_read_b64_tr_b16 v[108:109], v5 offset:768\n" \
  "ds_read_b64_tr_b16 v[110:111], v5 offset:800\nv_mfma_f32_16x16x32_f16 v[20:23], v[80:83], v[104:107], v[20:23]\n" \
  "ds_read_b64_tr_b16 v[84:85], v5 offset:1024\nds_read_b64_tr_b16 v[86:87], v5 offset:1056\ns_waitcnt lgkmcnt(12)\n" \
  "v_mfma_f32_16x16x32_f16 v[20:23], v[76:79], v[28:31], v[20:23]\nds_read_b64_tr_b16 v[44:45], v5 offset:1280\n" \
  "ds_read_b64_tr_b16 v[46:47], v5 offset:1312\nv_mfma_f32_16x16x32_f16 v[20:23], v[72:75], v[100:103], v[20:23]\ns_waitcnt lgkmcnt(10)\n" \
  "v_mfma_f32_16x16x32_f16 v[20:23], v[68:71], v[96:99], v[20:23]\nds_read_b64_tr_b16 v[80:81], v5 offset:1536\n" \
  "ds_read_b64_tr_b16 v[82:83], v5 offset:1568\nv_mfma_f32_16x16x32_f16 v[20:23], v[64:67], v[124:127], v[20:23]\n" \
  "ds_read_b64_tr_b16 v[104:105], v5 offset:1792\nds_read_b64_tr_b16 v[106:107], v5 offset:1824\ns_waitcnt lgkmcnt(12)\n" \
  "v_mfma_f32_16x16x32_f16 v[20:23], v[60:63], v[92:95], v[20:23]\nds_read_b64_tr_b16 v[76:77], v5 offset:2048\n" \
  "ds_read_b64_tr_b16 v[78:79], v5 offset:2080\nv_mfma_f32_16x16x32_f16 v[20:23], v[56:59], v[36:39], v[20:23]\ns_waitcnt lgkmcnt(10)\n" \
  "v_mfma_f32_16x16x32_f16 v[12:15], v[88:91], v[52:55], v[12:15]\nds_read_b64_tr_b16 v[28:29], v5 offset:2304\n" \
  "ds_read_b64_tr_b16 v[30:31], v5 offset:2336\nv_mfma_f32_16x16x32_f16 v[12:15], v[108:111], v[48:51], v[12:15]\n" \
  "ds_read_b64_tr_b16 v[72:73], v5 offset:2560\nds_read_b64_tr_b16 v[74:75], v5 offset:2592\ns_waitcnt lgkmcnt(10)\n" \
  "v_mfma_f32_16x16x32_f16 v[12:15], v[84:87], v[40:43], v[12:15]\nds_read_b64_tr_b16 v[100:101], v5 offset:2816\n" \
  "ds_read_b64_tr_b16 v[102:103], v5 offset:2848\nv_mfma_f32_16x16x32_f16 v[16:19], v[84:87], v[52:55], v[16:19]\n" \
  "ds_read_b64_tr_b16 v[68:69], v7 offset:0\nds_read_b64_tr_b16 v[70:71], v7 offset:64\n" \
  "v_mfma_f32_16x16x32_f16 v[12:15], v[44:47], v[120:123], v[12:15]\nv_mfma_f32_16x16x32_f16 v[16:19], v[44:47], v[48:51], v[16:19]\n" \
  "s_waitcnt lgkmcnt(10)\nv_mfma_f32_16x16x32_f16 v[12:15], v[80:83], v[116:119], v[12:15]\nds_read2_b32 v[96:97], v8 offset0:16 offset1:17\n" \
  "ds_read2_b32 v[98:99], v8 offset0:18 offset1:19\nv_mfma_f32_16x16x32_f16 v[16:19], v[80:83], v[40:43], v[16:19]\n" \
  "ds_read2_b32 v[64:65], v8 offset0:0 offset1:1\nds_read2_b32 v[66:67], v8 offset0:2 offset1:3\n" \
  "v_mfma_f32_16x16x32_f16 v[20:23], v[80:83], v[52:55], v[20:23]\nv_mfma_f32_16x16x32_f16 v[12:15], v[104:107], v[112:115], v[12:15]\n" \
  "v_mfma_f32_16x16x32_f16 v[16:19], v[104:107], v[120:123], v[16:19]\nv_mfma_f32_16x16x32_f16 v[20:23], v[104:107], v[48:51], v[20:23]\n" \
  "s_waitcnt lgkmcnt(10)\nv_mfma_f32_16x16x32_f16 v[16:19], v[76:79], v[116:119], v[16:19]\nds_read_b64_tr_b16 v[124:125], v7 offset:512\n" \
  "ds_read_b64_tr_b16 v[126:127], v7 offset:576\nv_mfma_f32_16x16x32_f16 v[20:23], v[76:79], v[40:43], v[20:23]\n" \
  "ds_read2_b32 v[60:61], v8 offset0:48 offset1:49\nds_read2_b32 v[62:63], v8 offset0:50 offset1:51\n" \
  "v_mfma_f32_16x16x32_f16 v[16:19], v[28:31], v[112:115], v[16:19]\nv_mfma_f32_16x16x32_f16 v[20:23], v[28:31], v[120:123], v[20:23]\n" \
  "s_waitcnt lgkmcnt(10)\nv_mfma_f32_16x16x32_f16 v[20:23], v[72:75], v[116:119], v[20:23]\nds_read_b64_tr_b16 v[92:93], v7 offset:1024\n" \
  "ds_read_b64_tr_b16 v[94:95], v7 offset:1088\nv_mfma_f32_16x16x32_f16 v[20:23], v[100:103], v[112:115], v[20:23]\n" \
  "ds_read_b64_tr_b16 v[56:57], v7 offset:1536\nds_read_b64_tr_b16 v[58:59], v7 offset:1600\ns_waitcnt lgkmcnt(10)\n" \
  "v_mfma_f32_16x16x32_f16 v[12:15], v[68:71], v[32:35], v[12:15]\nv_mfma_f32_16x16x32_f16 v[16:19], v[68:71], v[96:99], v[16:19]\n" \
  "s_waitcnt lgkmcnt(6)\nv_mfma_f32_16x16x32_f16 v[20:23], v[68:71], v[64:67], v[20:23]\ns_waitcnt lgkmcnt(2)\n" \
  "v_mfma_f32_16x16x32_f16 v[12:15], v[124:127], v[60:63], v[12:15]\nv_mfma_f32_16x16x32_f16 v[16:19], v[124:127], v[32:35], v[16:19]\n" \
  "v_mfma_f32_16x16x32_f16 v[20:23], v[124:127], v[96:99], v[20:23]\nv_mfma_f32_16x16x32_f16 v[16:19], v[92:95], v[60:63], v[16:19]\n" \
  "v_mfma_f32_16x16x32_f16 v[20:23], v[92:95], v[32:35], v[20:23]\ns_waitcnt lgkmcnt(0)\n" \
  "v_mfma_f32_16x16x32_f16 v[20:23], v[56:59], v[60:63], v[20:23]\ns_nop 1\nv_cvt_pkrtz_f16_f32 v24, v12, v13\nv_cvt_pkrtz_f16_f32 v25, v14, v15\n" \
  "v_pk_add_u16 v24, v24, 4 op_sel_hi:[1,0] clamp\nv_pk_add_u16 v25, v25, 4 op_sel_hi:[1,0] clamp\nds_write_b64 v4, v[24:25] offset:5120\n" \
  "v_cvt_pkrtz_f16_f32 v24, v16, v17\nv_cvt_pkrtz_f16_f32 v25, v18, v19\nv_pk_add_u16 v24, v24, 4 op_sel_hi:[1,0] clamp\n" \
  "v_pk_add_u16 v25, v25, 4 op_sel_hi:[1,0] clamp\nds_write_b64 v4, v[24:25] offset:6144\nv_cvt_pkrtz_f16_f32 v24, v20, v21\n" \
  "v_cvt_pkrtz_f16_f32 v25, v22, v23\nv_pk_add_u16 v24, v24, 4 op_sel_hi:[1,0] clamp\nv_pk_add_u16 v25, v25, 4 op_sel_hi:[1,0] clamp\n" \
  "ds_write_b64 v4, v[24:25] offset:7168\ns_setprio 0\ns_setpc_b64 s[90:91]\n31:\ns_movk_i32 s84, 0x4000\n33:\nds_read_b32 v27, v9 offset:32\n" \
  "s_sub_u32 s84, s84, 1\ns_cbranch_scc1 32b\ns_waitcnt lgkmcnt(0)\nv_readfirstlane_b32 s85, v27\ns_cmp_lt_u32 s85, 4\ns_cbranch_scc1 33b\n" \
  "s_branch 32b\n9:\ns_cmp_eq_u32 s89, 3\ns_cbranch_scc1 3f\ns_cmp_eq_u32 s89, 4\ns_cbranch_scc1 4f\ns_cmp_eq_u32 s89, 5\ns_cbranch_scc1 5f\n" \
  "ds_read_b64 v[124:125], v3 offset:576\nds_read_b64 v[126:127], v3 offset:584\nds_read_b64 v[120:121], v3 offset:64\n" \
  "ds_read_b64 v[122:123], v3 offset:72\nds_read_b64 v[116:117], v3 offset:640\nds_read_b64 v[118:119], v3 offset:648\n" \
  "ds_read_b64 v[112:113], v3 offset:128\nds_read_b64 v[114:115], v3 offset:136\nds_read_b64 v[108:109], v3 offset:704\n" \
  "ds_read_b64 v[110:111], v3 offset:712\nds_read_b64 v[104:105], v3 offset:192\nds_read_b64 v[106:107], v3 offset:200\n" \
  "ds_read_b64 v[100:101], v3 offset:768\nds_read_b64 v[102:103], v3 offset:776\nds_read_b64 v[96:97], v3 offset:256\n" \
  "ds_read_b64 v[98:99], v3 offset:264\nds_read_b64 v[92:93], v3 offset:832\nds_read_b64 v[94:95], v3 offset:840\n" \
  "ds_read_b64 v[88:89], v3 offset:320\nds_read_b64 v[90:91], v3 offset:328\nds_read_b64 v[84:85], v3 offset:896\n" \
  "ds_read_b64 v[86:87], v3 offset:904\nds_read_b64 v[80:81], v3 offset:384\nds_read_b64 v[82:83], v3 offset:392\n" \
  "ds_read_b64 v[76:77], v3 offset:960\nds_read_b64 v[78:79], v3 offset:968\nds_read_b64 v[72:73], v3 offset:448\n" \
  "ds_read_b64 v[74:75], v3 offset:456\nds_read_b64 v[68:69], v3 offset:1024\nds_read_b64 v[70:71], v3 offset:1032\n" \
  "ds_read_b64 v[64:65], v3 offset:512\nds_read_b64 v[66:67], v3 offset:520\ns_waitcnt lgkmcnt(0)\ns_barrier\nds_read_b128 v[24:27], v2 offset:0\n" \
  "ds_read_b128 v[28:31], v2 offset:64\nds_read_b128 v[32:35], v2 offset:128\nds_read_b128 v[36:39], v2 offset:192\ns_waitcnt lgkmcnt(3)\n" \
  "v_mfma_f32_16x16x32_f16 v[12:15], v[124:127], v[24:27], 0\ns_setprio 1\nds_read_b128 v[40:43], v2 offset:256\n" \
  "v_mfma_f32_16x16x32_f16 v[16:19], v[120:123], v[24:27], 0\ns_waitcnt lgkmcnt(3)\nv_mfma_f32_16x16x32_f16 v[12:15], v[116:119], v[28:31], v[12:15]\n" \
  "ds_read_b128 v[44:47], v2 offset:320\nv_mfma_f32_16x16x32_f16 v[16:19], v[112:115], v[28:31], v[16:19]\ns_waitcnt lgkmcnt(3)\n" \
  "v_mfma_f32_16x16x32_f16 v[12:15], v[108:111], v[32:35], v[12:15]\nds_read_b128 v[48:51], v2 offset:384\n" \
  "v_mfma_f32_16x16x32_f16 v[16:19], v[104:107], v[32:35], v[16:19]\ns_waitcnt lgkmcnt(3)\n" \
  "v_mfma_f32_16x16x32_f16 v[12:15], v[100:103], v[36:39], v[12:15]\nds_read_b128 v[52:55], v2 offset:448\n" \
  "v_mfma_f32_16x16x32_f16 v[16:19], v[96:99], v[36:39], v[16:19]\ns_waitcnt lgkmcnt(3)\n" \
  "v_mfma_f32_16x16x32_f16 v[12:15], v[92:95], v[40:43], v[12:15]\nds_read_b128 v[56:59], v2 offset:512\n" \
  "v_mfma_f32_16x16x32_f16 v[16:19], v[88:91], v[40:43], v[16:19]\nds_read_b128 v[60:63], v2 offset:576\ns_waitcnt lgkmcnt(3)\n" \
  "v_mfma_f32_16x16x32_f16 v[12:15], v[84:87], v[44:47], v[12:15]\nds_read_b128 v[120:123], v2 offset:640\n" \
  "v_mfma_f32_16x16x32_f16 v[16:19], v[80:83], v[44:47], v[16:19]\nds_read_b128 v[24:27], v2 offset:704\n" \
  "v_mfma_f32_16x16x32_f16 v[12:15], v[76:79], v[48:51], v[12:15]\nds_read_b128 v[112:115], v2 offset:768\n" \
  "v_mfma_f32_16x16x32_f16 v[16:19], v[72:75], v[48:51], v[16:19]\nds_read_b128 v[28:31], v2 offset:832\ns_waitcnt lgkmcnt(5)\n" \
  "v_mfma_f32_16x16x32_f16 v[12:15], v[68:71], v[52:55], v[12:15]\nds_read_b128 v[104:107], v2 offset:896\n" \
  "v_mfma_f32_16x16x32_f16 v[16:19], v[64:67], v[52:55], v[16:19]\nds_read_b128 v[32:35], v2 offset:960\n" \
  "v_mfma_f32_16x16x32_f16 v[16:19], v[124:127], v[56:59], v[16:19]\ns_waitcnt lgkmcnt(5)\n" \
  "v_mfma_f32_16x16x32_f16 v[16:19], v[116:119], v[60:63], v[16:19]\nv_mfma_f32_16x16x32_f16 v[16:19], v[108:111], v[120:123], v[16:19]\n" \
  "s_waitcnt lgkmcnt(3)\nv_mfma_f32_16x16x32_f16 v[16:19], v[100:103], v[24:27], v[16:19]\nv_cvt_pkrtz_f16_f32 v20, v12, v13\n" \
  "v_cvt_pkrtz_f16_f32 v21, v14, v15\nv_mfma_f32_16x16x32_f16 v[16:19], v[92:95], v[112:115], v[16:19]\n" \
  "v_pk_add_u16 v20, v20, 4 op_sel_hi:[1,0] clamp\ns_waitcnt lgkmcnt(2)\nv_mfma_f32_16x16x32_f16 v[16:19], v[84:87], v[28:31], v[16:19]\n" \
  "v_pk_add_u16 v21, v21, 4 op_sel_hi:[1,0] clamp\ns_waitcnt lgkmcnt(1)\nv_mfma_f32_16x16x32_f16 v[16:19], v[76:79], v[104:107], v[16:19]\n" \
  "ds_write_b64 v4, v[20:21] offset:0\ns_waitcnt lgkmcnt(1)\nv_mfma_f32_16x16x32_f16 v[16:19], v[68:71], v[32:35], v[16:19]\ns_nop 7\n" \
  "v_cvt_pkrtz_f16_f32 v20, v16, v17\nv_cvt_pkrtz_f16_f32 v21, v18, v19\nv_pk_add_u16 v20, v20, 4 op_sel_hi:[1,0] clamp\n" \
  "v_pk_add_u16 v21, v21, 4 op_sel_hi:[1,0] clamp\nds_write_b64 v4, v[20:21] offset:1024\ns_setprio 0\ns_setpc_b64 s[90:91]\n3:\n" \
  "ds_read_b64 v[124:125], v3 offset:0\nds_read_b64 v[126:127], v3 offset:8\nds_read_b64 v[120:121], v3 offset:64\n" \
  "ds_read_b64 v[122:123], v3 offset:72\nds_read_b64 v[116:117], v3 offset:128\nds_read_b64 v[118:119], v3 offset:136\n" \
  "ds_read_b64 v[112:113], v3 offset:192\nds_read_b64 v[114:115], v3 offset:200\nds_read_b64 v[108:109], v3 offset:256\n" \
  "ds_read_b64 v[110:111], v3 offset:264\nds_read_b64 v[104:105], v3 offset:320\nds_read_b64 v[106:107], v3 offset:328\n" \
  "ds_read_b64 v[100:101], v3 offset:384\nds_read_b64 v[102:103], v3 offset:392\nds_read_b64 v[96:97], v3 offset:448\n" \
  "ds_read_b64 v[98:99], v3 offset:456\nds_read_b64 v[92:93], v3 offset:512\nds_read_b64 v[94:95], v3 offset:520\n" \
  "ds_read_b64 v[88:89], v3 offset:576\nds_read_b64 v[90:91], v3 offset:584\nds_read_b64 v[84:85], v3 offset:640\n" \
  "ds_read_b64 v[86:87], v3 offset:648\nds_read_b64 v[80:81], v3 offset:704\nds_read_b64 v[82:83], v3 offset:712\n" \
  "ds_read_b64 v[76:77], v3 offset:768\nds_read_b64 v[78:79], v3 offset:776\nds_read_b64 v[72:73], v3 offset:832\n" \
  "ds_read_b64 v[74:75], v3 offset:840\nds_read_b64 v[68:69], v3 offset:896\nds_read_b64 v[70:71], v3 offset:904\n" \
  "ds_read_b64 v[64:65], v3 offset:960\nds_read_b64 v[66:67], v3 offset:968\nds_read_b64 v[60:61], v3 offset:1024\n" \
  "ds_read_b64 v[62:63], v3 offset:1032\nds_read_b64 v[56:57], v6 offset:256\nds_read_b64 v[58:59], v6 offset:264\n" \
  "ds_read_b64 v[52:53], v6 offset:128\nds_read_b64 v[54:55], v6 offset:136\nds_read_b64 v[48:49], v6 offset:320\n" \
  "ds_read_b64 v[50:51], v6 offset:328\nds_read_b64 v[44:45], v6 offset:192\nds_read_b64 v[46:47], v6 offset:200\ns_waitcnt lgkmcnt(0)\ns_barrier\n" \
  "ds_read_b128 v[24:27], v2 offset:448\nds_read_b128 v[28:31], v2 offset:512\nds_read_b128 v[32:35], v2 offset:576\n" \
  "ds_read_b128 v[36:39], v2 offset:640\nds_read_b128 v[40:43], v2 offset:704\ns_waitcnt lgkmcnt(3)\n" \
  "v_mfma_f32_16x16x32_f16 v[12:15], v[124:127], v[24:27], 0\ns_setprio 1\nds_read_b128 v[24:27], v2 offset:768\n" \
  "v_mfma_f32_16x16x32_f16 v[12:15], v[120:123], v[28:31], v[12:15]\nds_read_b128 v[28:31], v2 offset:832\ns_waitcnt lgkmcnt(3)\n" \
  "v_mfma_f32_16x16x32_f16 v[12:15], v[116:119], v[32:35], v[12:15]\nds_read_b128 v[32:35], v2 offset:896\n" \
  "v_mfma_f32_16x16x32_f16 v[12:15], v[112:115], v[36:39], v[12:15]\nds_read_b128 v[36:39], v2 offset:960\ns_waitcnt lgkmcnt(3)\n" \
  "v_mfma_f32_16x16x32_f16 v[12:15], v[108:111], v[40:43], v[12:15]\nds_read_b128 v[40:43], v2 offset:1024\n" \
  "v_mfma_f32_16x16x32_f16 v[12:15], v[104:107], v[24:27], v[12:15]\nds_read_b128 v[24:27], v2 offset:1088\ns_waitcnt lgkmcnt(3)\n" \
  "v_mfma_f32_16x16x32_f16 v[12:15], v[100:103], v[28:31], v[12:15]\nds_read_b128 v[28:31], v2 offset:1152\n" \
  "v_mfma_f32_16x16x32_f16 v[12:15], v[96:99], v[32:35], v[12:15]\ns_waitcnt lgkmcnt(3)\n" \
  "v_mfma_f32_16x16x32_f16 v[12:15], v[92:95], v[36:39], v[12:15]\nds_read_b128 v[32:35], v2 offset:1216\n" \
  "v_mfma_f32_16x16x32_f16 v[16:19], v[124:127], v[36:39], 0\ns_waitcnt lgkmcnt(3)\nv_mfma_f32_16x16x32_f16 v[12:15], v[88:91], v[40:43], v[12:15]\n" \
  "ds_read_b128 v[124:127], v2 offset:1280\nv_mfma_f32_16x16x32_f16 v[16:19], v[120:123], v[40:43], v[16:19]\ns_waitcnt lgkmcnt(3)\n" \
  "v_mfma_f32_16x16x32_f16 v[12:15], v[84:87], v[24:27], v[12:15]\nds_read_b128 v[36:39], v2 offset:1344\n" \
  "v_mfma_f32_16x16x32_f16 v[16:19], v[116:119], v[24:27], v[16:19]\ns_waitcnt lgkmcnt(3)\n" \
  "v_mfma_f32_16x16x32_f16 v[12:15], v[80:83], v[28:31], v[12:15]\nds_read_b128 v[120:123], v2 offset:1408\n" \
  "v_mfma_f32_16x16x32_f16 v[16:19], v[112:115], v[28:31], v[16:19]\ns_waitcnt lgkmcnt(3)\n" \
  "v_mfma_f32_16x16x32_f16 v[12:15], v[76:79], v[32:35], v[12:15]\nds_read_b128 v[40:43], v2 offset:1472\n" \
  "v_mfma_f32_16x16x32_f16 v[16:19], v[108:111], v[32:35], v[16:19]\ns_waitcnt lgkmcnt(3)\n" \
  "v_mfma_f32_16x16x32_f16 v[12:15], v[72:75], v[124:127], v[12:15]\nds_read_b128 v[116:119], v2 offset:1536\n" \
  "v_mfma_f32_16x16x32_f16 v[16:19], v[104:107], v[124:127], v[16:19]\nds_read_b128 v[24:27], v2 offset:1600\ns_waitcnt lgkmcnt(3)\n" \
  "v_mfma_f32_16x16x32_f16 v[12:15], v[68:71], v[36:39], v[12:15]\nds_read_b128 v[112:115], v2 offset:1664\nds_read_b32 v23, v9 offset:32\n" \
  "v_mfma_f32_16x16x32_f16 v[16:19], v[100:103], v[36:39], v[16:19]\nds_read_b128 v[28:31], v2 offset:1728\n" \
  "v_mfma_f32_16x16x32_f16 v[12:15], v[64:67], v[120:123], v[12:15]\nds_read_b128 v[108:111], v2 offset:1792\n" \
  "v_mfma_f32_16x16x32_f16 v[16:19], v[96:99], v[120:123], v[16:19]\nds_read_b128 v[32:35], v2 offset:1856\ns_waitcnt lgkmcnt(6)\n" \
  "v_mfma_f32_16x16x32_f16 v[12:15], v[60:63], v[40:43], v[12:15]\nds_read_b128 v[104:107], v2 offset:1920\n" \
  "v_mfma_f32_16x16x32_f16 v[16:19], v[92:95], v[40:43], v[16:19]\nds_read_b128 v[124:127], v2 offset:1984\ns_waitcnt lgkmcnt(5)\n" \
  "v_readfirstlane_b32 s85, v23\ns_cmp_lt_u32 s85, 3\ns_cbranch_scc1 51f\n52:\nv_mfma_f32_16x16x32_f16 v[16:19], v[88:91], v[116:119], v[16:19]\n" \
  "ds_read_b64_tr_b16 v[100:101], v5 offset:0\nds_read_b64_tr_b16 v[102:103], v5 offset:32\n" \
  "v_mfma_f32_16x16x32_f16 v[16:19], v[84:87], v[24:27], v[16:19]\nds_read_b64_tr_b16 v[36:37], v5 offset:256\n" \
  "ds_read_b64_tr_b16 v[38:39], v5 offset:288\nv_mfma_f32_16x16x32_f16 v[16:19], v[80:83], v[112:115], v[16:19]\n" \
  "ds_read_b64_tr_b16 v[96:97], v5 offset:512\nds_read_b64_tr_b16 v[98:99], v5 offset:544\ns_waitcnt lgkmcnt(9)\n" \
  "v_mfma_f32_16x16x32_f16 v[16:19], v[76:79], v[28:31], v[16:19]\nds_read_b64_tr_b16 v[120:121], v5 offset:768\n" \
  "ds_read_b64_tr_b16 v[122:123], v5 offset:800\nv_mfma_f32_16x16x32_f16 v[16:19], v[72:75], v[108:111], v[16:19]\ns_waitcnt lgkmcnt(9)\n" \
  "v_mfma_f32_16x16x32_f16 v[16:19], v[68:71], v[32:35], v[16:19]\nv_mfma_f32_16x16x32_f16 v[16:19], v[64:67], v[104:107], v[16:19]\n" \
  "s_waitcnt lgkmcnt(6)\nv_mfma_f32_16x16x32_f16 v[16:19], v[60:63], v[124:127], v[16:19]\n" \
  "v_mfma_f32_16x16x32_f16 v[12:15], v[100:103], v[56:59], v[12:15]\nv_mfma_f32_16x16x32_f16 v[16:19], v[100:103], v[52:55], v[16:19]\n" \
  "s_waitcnt lgkmcnt(2)\nv_mfma_f32_16x16x32_f16 v[12:15], v[36:39], v[48:51], v[12:15]\n" \
  "v_mfma_f32_16x16x32_f16 v[16:19], v[36:39], v[44:47], v[16:19]\nv_mfma_f32_16x16x32_f16 v[16:19], v[96:99], v[56:59], v[16:19]\n" \
  "s_waitcnt lgkmcnt(0)\nv_mfma_f32_16x16x32_f16 v[16:19], v[120:123], v[48:51], v[16:19]\ns_nop 3\nv_cvt_pkrtz_f16_f32 v20, v12, v13\n" \
  "v_cvt_pkrtz_f16_f32 v21, v14, v15\nv_pk_add_u16 v20, v20, 4 op_sel_hi:[1,0] clamp\nv_pk_add_u16 v21, v21, 4 op_sel_hi:[1,0] clamp\n" \
  "ds_write_b64 v4, v[20:21] offset:2048\nv_cvt_pkrtz_f16_f32 v20, v16, v17\nv_cvt_pkrtz_f16_f32 v21, v18, v19\n" \
  "v_pk_add_u16 v20, v20, 4 op_sel_hi:[1,0] clamp\nv_pk_add_u16 v21, v21, 4 op_sel_hi:[1,0] clamp\nds_write_b64 v4, v[20:21] offset:3072\n" \
  "s_setprio 0\ns_setpc_b64 s[90:91]\n51:\ns_movk_i32 s84, 0x4000\n53:\nds_read_b32 v23, v9 offset:32\ns_sub_u32 s84, s84, 1\ns_cbranch_scc1 52b\n" \
  "s_waitcnt lgkmcnt(0)\nv_readfirstlane_b32 s85, v23\ns_cmp_lt_u32 s85, 3\ns_cbranch_scc1 53b\ns_branch 52b\n4:\n" \
  "ds_read_b64 v[124:125], v3 offset:1088\nds_read_b64 v[126:127], v3 offset:1096\nds_read_b64 v[120:121], v3 offset:0\n" \
  "ds_read_b64 v[122:123], v3 offset:8\nds_read_b64 v[116:117], v3 offset:64\nds_read_b64 v[118:119], v3 offset:72\n" \
  "ds_read_b64 v[112:113], v3 offset:128\nds_read_b64 v[114:115], v3 offset:136\nds_read_b64 v[108:109], v3 offset:192\n" \
  "ds_read_b64 v[110:111], v3 offset:200\nds_read_b64 v[104:105], v3 offset:256\nds_read_b64 v[106:107], v3 offset:264\n" \
  "ds_read_b64 v[100:101], v3 offset:320\nds_read_b64 v[102:103], v3 offset:328\nds_read_b64 v[96:97], v3 offset:384\n" \
  "ds_read_b64 v[98:99], v3 offset:392\nds_read_b64 v[92:93], v3 offset:448\nds_read_b64 v[94:95], v3 offset:456\n" \
  "ds_read_b64 v[88:89], v3 offset:512\nds_read_b64 v[90:91], v3 offset:520\nds_read_b64 v[84:85], v3 offset:576\n" \
  "ds_read_b64 v[86:87], v3 offset:584\nds_read_b64 v[80:81], v3 offset:640\nds_read_b64 v[82:83], v3 offset:648\n" \
  "ds_read_b64 v[76:77], v3 offset:704\nds_read_b64 v[78:79], v3 offset:712\nds_read_b64 v[72:73], v3 offset:768\n" \
  "ds_read_b64 v[74:75], v3 offset:776\nds_read_b64 v[68:69], v3 offset:832\nds_read_b64 v[70:71], v3 offset:840\n" \
  "ds_read_b64 v[64:65], v3 offset:896\nds_read_b64 v[66:67], v3 offset:904\nds_read_b64 v[60:61], v3 offset:960\n" \
  "ds_read_b64 v[62:63], v3 offset:968\nds_read_b64 v[56:57], v3 offset:1024\nds_read_b64 v[58:59], v3 offset:1032\n" \
  "ds_read_b64 v[52:53], v6 offset:0\nds_read_b64 v[54:55], v6 offset:8\nds_read_b64 v[48:49], v6 offset:64\nds_read_b64 v[50:51], v6 offset:72\n" \
  "ds_read_b64 v[44:45], v6 offset:128\nds_read_b64 v[46:47], v6 offset:136\nds_read_b64 v[40:41], v6 offset:192\n" \
  "ds_read_b64 v[42:43], v6 offset:200\ns_waitcnt lgkmcnt(0)\ns_barrier\nds_read_b128 v[24:27], v2 offset:448\nds_read_b128 v[28:31], v2 offset:960\n" \
  "ds_read_b128 v[32:35], v2 offset:1472\nds_read_b128 v[36:39], v2 offset:1536\ns_waitcnt lgkmcnt(3)\n" \
  "v_mfma_f32_16x16x32_f16 v[12:15], v[124:127], v[24:27], 0\ns_setprio 1\nds_read_b128 v[24:27], v2 offset:1600\ns_waitcnt lgkmcnt(3)\n" \
  "v_mfma_f32_16x16x32_f16 v[16:19], v[124:127], v[28:31], 0\nds_read_b128 v[124:127], v2 offset:1664\ns_waitcnt lgkmcnt(3)\n" \
  "v_mfma_f32_16x16x32_f16 v[12:15], v[120:123], v[32:35], v[12:15]\nds_read_b128 v[28:31], v2 offset:1728\ns_waitcnt lgkmcnt(3)\n" \
  "v_mfma_f32_16x16x32_f16 v[12:15], v[116:119], v[36:39], v[12:15]\nds_read_b128 v[32:35], v2 offset:1792\ns_waitcnt lgkmcnt(3)\n" \
  "v_mfma_f32_16x16x32_f16 v[12:15], v[112:115], v[24:27], v[12:15]\nds_read_b128 v[36:39], v2 offset:1856\ns_waitcnt lgkmcnt(3)\n" \
  "v_mfma_f32_16x16x32_f16 v[12:15], v[108:111], v[124:127], v[12:15]\nds_read_b128 v[24:27], v2 offset:1920\ns_waitcnt lgkmcnt(3)\n" \
  "v_mfma_f32_16x16x32_f16 v[12:15], v[104:107], v[28:31], v[12:15]\nds_read_b128 v[124:127], v2 offset:1984\ns_waitcnt lgkmcnt(3)\n" \
  "v_mfma_f32_16x16x32_f16 v[12:15], v[100:103], v[32:35], v[12:15]\nds_read_b128 v[28:31], v2 offset:2048\ns_waitcnt lgkmcnt(3)\n" \
  "v_mfma_f32_16x16x32_f16 v[12:15], v[96:99], v[36:39], v[12:15]\nds_read_b128 v[32:35], v2 offset:2112\ns_waitcnt lgkmcnt(3)\n" \
  "v_mfma_f32_16x16x32_f16 v[12:15], v[92:95], v[24:27], v[12:15]\nds_read_b128 v[36:39], v2 offset:2176\ns_waitcnt lgkmcnt(3)\n" \
  "v_mfma_f32_16x16x32_f16 v[12:15], v[88:91], v[124:127], v[12:15]\nds_read_b128 v[24:27], v2 offset:2240\n" \
  "v_mfma_f32_16x16x32_f16 v[16:19], v[120:123], v[124:127], v[16:19]\nds_read_b64 v[120:121], v6 offset:256\nds_read_b64 v[122:123], v6 offset:264\n" \
  "s_waitcnt lgkmcnt(4)\nv_mfma_f32_16x16x32_f16 v[12:15], v[84:87], v[28:31], v[12:15]\nds_read_b128 v[124:127], v2 offset:2304\n" \
  "v_mfma_f32_16x16x32_f16 v[16:19], v[116:119], v[28:31], v[16:19]\nds_read_b64 v[116:117], v6 offset:320\nds_read_b64 v[118:119], v6 offset:328\n" \
  "v_mfma_f32_16x16x32_f16 v[12:15], v[80:83], v[32:35], v[12:15]\nds_read_b128 v[28:31], v2 offset:2368\n" \
  "v_mfma_f32_16x16x32_f16 v[16:19], v[112:115], v[32:35], v[16:19]\ns_waitcnt lgkmcnt(6)\n" \
  "v_mfma_f32_16x16x32_f16 v[12:15], v[76:79], v[36:39], v[12:15]\nds_read_b128 v[112:115], v2 offset:2432\n" \
  "v_mfma_f32_16x16x32_f16 v[16:19], v[108:111], v[36:39], v[16:19]\nds_read2_b32 v[32:33], v8 offset0:48 offset1:49\n" \
  "ds_read2_b32 v[34:35], v8 offset0:50 offset1:51\nv_mfma_f32_16x16x32_f16 v[12:15], v[72:75], v[24:27], v[12:15]\n" \
  "ds_read_b128 v[108:111], v2 offset:2496\nv_mfma_f32_16x16x32_f16 v[16:19], v[104:107], v[24:27], v[16:19]\n" \
  "ds_read2_b32 v[36:37], v8 offset0:32 offset1:33\nds_read2_b32 v[38:39], v8 offset0:34 offset1:35\ns_waitcnt lgkmcnt(6)\n" \
  "v_mfma_f32_16x16x32_f16 v[12:15], v[68:71], v[124:127], v[12:15]\nds_read_b128 v[104:107], v2 offset:2560\n" \
  "v_mfma_f32_16x16x32_f16 v[16:19], v[100:103], v[124:127], v[16:19]\nds_read_b128 v[24:27], v2 offset:2624\n" \
  "v_mfma_f32_16x16x32_f16 v[12:15], v[64:67], v[28:31], v[12:15]\nds_read_b128 v[100:103], v2 offset:2688\nds_read_b32 v23, v9 offset:32\n" \
  "v_mfma_f32_16x16x32_f16 v[16:19], v[96:99], v[28:31], v[16:19]\nds_read_b128 v[124:127], v2 offset:2752\ns_waitcnt lgkmcnt(7)\n" \
  "v_mfma_f32_16x16x32_f16 v[12:15], v[60:63], v[112:115], v[12:15]\nds_read_b128 v[96:99], v2 offset:2816\n" \
  "v_mfma_f32_16x16x32_f16 v[16:19], v[92:95], v[112:115], v[16:19]\nds_read_b128 v[28:31], v2 offset:2880\n" \
  "v_mfma_f32_16x16x32_f16 v[12:15], v[56:59], v[108:111], v[12:15]\nds_read_b128 v[92:95], v2 offset:2944\n" \
  "v_mfma_f32_16x16x32_f16 v[16:19], v[88:91], v[108:111], v[16:19]\nds_read_b128 v[112:115], v2 offset:3008\ns_waitcnt lgkmcnt(5)\n" \
  "v_readfirstlane_b32 s85, v23\ns_cmp_lt_u32 s85, 3\ns_cbranch_scc1 61f\n62:\nv_mfma_f32_16x16x32_f16 v[16:19], v[84:87], v[104:107], v[16:19]\n" \
  "ds_read_b64_tr_b16 v[88:89], v5 offset:0\nds_read_b64_tr_b16 v[90:91], v5 offset:32\n" \
  "v_mfma_f32_16x16x32_f16 v[16:19], v[80:83], v[24:27], v[16:19]\nds_read_b64_tr_b16 v[108:109], v5 offset:256\n" \
  "ds_read_b64_tr_b16 v[110:111], v5 offset:288\nv_mfma_f32_16x16x32_f16 v[16:19], v[76:79], v[100:103], v[16:19]\n" \
  "ds_read_b64_tr_b16 v[84:85], v5 offset:512\nds_read_b64_tr_b16 v[86:87], v5 offset:544\ns_waitcnt lgkmcnt(9)\n" \
  "v_mfma_f32_16x16x32_f16 v[16:19], v[72:75], v[124:127], v[16:19]\nds_read_b64_tr_b16 v[104:105], v5 offset:768\n" \
  "ds_read_b64_tr_b16 v[106:107], v5 offset:800\nv_mfma_f32_16x16x32_f16 v[16:19], v[68:71], v[96:99], v[16:19]\n" \
  "ds_read_b64_tr_b16 v[80:81], v5 offset:1024\nds_read_b64_tr_b16 v[82:83], v5 offset:1056\ns_waitcnt lgkmcnt(11)\n" \
  "v_mfma_f32_16x16x32_f16 v[16:19], v[64:67], v[28:31], v[16:19]\nds_read_b64_tr_b16 v[24:25], v5 offset:1280\n" \
  "ds_read_b64_tr_b16 v[26:27], v5 offset:1312\nv_mfma_f32_16x16x32_f16 v[16:19], v[60:63], v[92:95], v[16:19]\n" \
  "ds_read_b64_tr_b16 v[76:77], v5 offset:1536\nds_read_b64_tr_b16 v[78:79], v5 offset:1568\ns_waitcnt lgkmcnt(12)\n" \
  "v_mfma_f32_16x16x32_f16 v[16:19], v[56:59], v[112:115], v[16:19]\nds_read_b64_tr_b16 v[100:101], v5 offset:1792\n" \
  "ds_read_b64_tr_b16 v[102:103], v5 offset:1824\nv_mfma_f32_16x16x32_f16 v[12:15], v[88:91], v[52:55], v[12:15]\ns_waitcnt lgkmcnt(10)\n" \
  "v_mfma_f32_16x16x32_f16 v[12:15], v[108:111], v[48:51], v[12:15]\nds_read_b64_tr_b16 v[72:73], v7 offset:0\n" \
  "ds_read_b64_tr_b16 v[74:75], v7 offset:64\nv_mfma_f32_16x16x32_f16 v[12:15], v[84:87], v[44:47], v[12:15]\n" \
  "ds_read_b64_tr_b16 v[124:125], v7 offset:512\nds_read_b64_tr_b16 v[126:127], v7 offset:576\n" \
  "v_mfma_f32_16x16x32_f16 v[16:19], v[84:87], v[52:55], v[16:19]\ns_waitcnt lgkmcnt(10)\n" \
  "v_mfma_f32_16x16x32_f16 v[12:15], v[104:107], v[40:43], v[12:15]\nv_mfma_f32_16x16x32_f16 v[16:19], v[104:107], v[48:51], v[16:19]\n" \
  "v_mfma_f32_16x16x32_f16 v[12:15], v[80:83], v[120:123], v[12:15]\nv_mfma_f32_16x16x32_f16 v[16:19], v[80:83], v[44:47], v[16:19]\n" \
  "s_waitcnt lgkmcnt(6)\nv_mfma_f32_16x16x32_f16 v[12:15], v[24:27], v[116:119], v[12:15]\n" \
  "v_mfma_f32_16x16x32_f16 v[16:19], v[24:27], v[40:43], v[16:19]\nv_mfma_f32_16x16x32_f16 v[16:19], v[76:79], v[120:123], v[16:19]\n" \
  "s_waitcnt lgkmcnt(2)\nv_mfma_f32_16x16x32_f16 v[16:19], v[100:103], v[116:119], v[16:19]\n" \
  "v_mfma_f32_16x16x32_f16 v[12:15], v[72:75], v[32:35], v[12:15]\nv_mfma_f32_16x16x32_f16 v[16:19], v[72:75], v[36:39], v[16:19]\n" \
  "s_waitcnt lgkmcnt(0)\nv_mfma_f32_16x16x32_f16 v[16:19], v[124:127], v[32:35], v[16:19]\ns_nop 4\nv_cvt_pkrtz_f16_f32 v20, v12, v13\n" \
  "v_cvt_pkrtz_f16_f32 v21, v14, v15\nv_pk_add_u16 v20, v20, 4 op_sel_hi:[1,0] clamp\nv_pk_add_u16 v21, v21, 4 op_sel_hi:[1,0] clamp\n" \
  "ds_write_b64 v4, v[20:21] offset:4096\nv_cvt_pkrtz_f16_f32 v20, v16, v17\nv_cvt_pkrtz_f16_f32 v21, v18, v19\n" \
  "v_pk_add_u16 v20, v20, 4 op_sel_hi:[1,0] clamp\nv_pk_add_u16 v21, v21, 4 op_sel_hi:[1,0] clamp\nds_write_b64 v4, v[20:21] offset:5120\n" \
  "s_setprio 0\ns_setpc_b64 s[90:91]\n61:\ns_movk_i32 s84, 0x4000\n63:\nds_read_b32 v23, v9 offset:32\ns_sub_u32 s84, s84, 1\ns_cbranch_scc1 62b\n" \
  "s_waitcnt lgkmcnt(0)\nv_readfirstlane_b32 s85, v23\ns_cmp_lt_u32 s85, 3\ns_cbranch_scc1 63b\ns_branch 62b\n5:\n" \
  "ds_read_b64 v[124:125], v3 offset:1088\nds_read_b64 v[126:127], v3 offset:1096\nds_read_b64 v[120:121], v3 offset:0\n" \
  "ds_read_b64 v[122:123], v3 offset:8\nds_read_b64 v[116:117], v3 offset:64\nds_read_b64 v[118:119], v3 offset:72\n" \
  "ds_read_b64 v[112:113], v3 offset:128\nds_read_b64 v[114:115], v3 offset:136\nds_read_b64 v[108:109], v3 offset:192\n" \
  "ds_read_b64 v[110:111], v3 offset:200\nds_read_b64 v[104:105], v3 offset:256\nds_read_b64 v[106:107], v3 offset:264\n" \
  "ds_read_b64 v[100:101], v3 offset:320\nds_read_b64 v[102:103], v3 offset:328\nds_read_b64 v[96:97], v3 offset:384\n" \
  "ds_read_b64 v[98:99], v3 offset:392\nds_read_b64 v[92:93], v3 offset:448\nds_read_b64 v[94:95], v3 offset:456\n" \
  "ds_read_b64 v[88:89], v3 offset:512\nds_read_b64 v[90:91], v3 offset:520\nds_read_b64 v[84:85], v3 offset:576\n" \
  "ds_read_b64 v[86:87], v3 offset:584\nds_read_b64 v[80:81], v3 offset:640\nds_read_b64 v[82:83], v3 offset:648\n" \
  "ds_read_b64 v[76:77], v3 offset:704\nds_read_b64 v[78:79], v3 offset:712\nds_read_b64 v[72:73], v3 offset:768\n" \
  "ds_read_b64 v[74:75], v3 offset:776\nds_read_b64 v[68:69], v3 offset:832\nds_read_b64 v[70:71], v3 offset:840\n" \
  "ds_read_b64 v[64:65], v3 offset:896\nds_read_b64 v[66:67], v3 offset:904\nds_read_b64 v[60:61], v3 offset:960\n" \
  "ds_read_b64 v[62:63], v3 offset:968\nds_read_b64 v[56:57], v3 offset:1024\nds_read_b64 v[58:59], v3 offset:1032\n" \
  "ds_read_b64 v[52:53], v6 offset:0\nds_read_b64 v[54:55], v6 offset:8\nds_read_b64 v[48:49], v6 offset:64\nds_read_b64 v[50:51], v6 offset:72\n" \
  "ds_read_b64 v[44:45], v6 offset:128\nds_read_b64 v[46:47], v6 offset:136\nds_read_b64 v[40:41], v6 offset:192\n" \
  "ds_read_b64 v[42:43], v6 offset:200\ns_waitcnt lgkmcnt(0)\ns_barrier\nds_read_b128 v[24:27], v2 offset:1472\n" \
  "ds_read_b128 v[28:31], v2 offset:1984\nds_read_b128 v[32:35], v2 offset:2496\nds_read_b128 v[36:39], v2 offset:2560\ns_waitcnt lgkmcnt(3)\n" \
  "v_mfma_f32_16x16x32_f16 v[12:15], v[124:127], v[24:27], 0\ns_setprio 1\nds_read_b128 v[24:27], v2 offset:2624\ns_waitcnt lgkmcnt(3)\n" \
  "v_mfma_f32_16x16x32_f16 v[16:19], v[124:127], v[28:31], 0\nds_read_b128 v[124:127], v2 offset:2688\ns_waitcnt lgkmcnt(3)\n" \
  "v_mfma_f32_16x16x32_f16 v[12:15], v[120:123], v[32:35], v[12:15]\nds_read_b128 v[28:31], v2 offset:2752\ns_waitcnt lgkmcnt(3)\n" \
  "v_mfma_f32_16x16x32_f16 v[12:15], v[116:119], v[36:39], v[12:15]\nds_read_b128 v[32:35], v2 offset:2816\ns_waitcnt lgkmcnt(3)\n" \
  "v_mfma_f32_16x16x32_f16 v[12:15], v[112:115], v[24:27], v[12:15]\nds_read_b128 v[36:39], v2 offset:2880\ns_waitcnt lgkmcnt(3)\n" \
  "v_mfma_f32_16x16x32_f16 v[12:15], v[108:111], v[124:127], v[12:15]\nds_read_b128 v[24:27], v2 offset:2944\ns_waitcnt lgkmcnt(3)\n" \
  "v_mfma_f32_16x16x32_f16 v[12:15], v[104:107], v[28:31], v[12:15]\nds_read_b128 v[124:127], v2 offset:3008\ns_waitcnt lgkmcnt(3)\n" \
  "v_mfma_f32_16x16x32_f16 v[12:15], v[100:103], v[32:35], v[12:15]\nds_read_b128 v[28:31], v2 offset:3072\ns_waitcnt lgkmcnt(3)\n" \
  "v_mfma_f32_16x16x32_f16 v[12:15], v[96:99], v[36:39], v[12:15]\nds_read_b128 v[32:35], v2 offset:3136\ns_waitcnt lgkmcnt(3)\n" \
  "v_mfma_f32_16x16x32_f16 v[12:15], v[92:95], v[24:27], v[12:15]\nds_read_b128 v[36:39], v2 offset:3200\ns_waitcnt lgkmcnt(3)\n" \
  "v_mfma_f32_16x16x32_f16 v[12:15], v[88:91], v[124:127], v[12:15]\nds_read_b128 v[24:27], v2 offset:3264\n" \
  "v_mfma_f32_16x16x32_f16 v[16:19], v[120:123], v[124:127], v[16:19]\nds_read_b64 v[120:121], v6 offset:256\nds_read_b64 v[122:123], v6 offset:264\n" \
  "s_waitcnt lgkmcnt(4)\nv_mfma_f32_16x16x32_f16 v[12:15], v[84:87], v[28:31], v[12:15]\nds_read_b128 v[124:127], v2 offset:3328\n" \
  "v_mfma_f32_16x16x32_f16 v[16:19], v[116:119], v[28:31], v[16:19]\nds_read_b64 v[116:117], v6 offset:320\nds_read_b64 v[118:119], v6 offset:328\n" \
  "v_mfma_f32_16x16x32_f16 v[12:15], v[80:83], v[32:35], v[12:15]\nds_read_b128 v[28:31], v2 offset:3392\n" \
  "v_mfma_f32_16x16x32_f16 v[16:19], v[112:115], v[32:35], v[16:19]\ns_waitcnt lgkmcnt(6)\n" \
  "v_mfma_f32_16x16x32_f16 v[12:15], v[76:79], v[36:39], v[12:15]\nds_read_b128 v[112:115], v2 offset:3456\n" \
  "v_mfma_f32_16x16x32_f16 v[16:19], v[108:111], v[36:39], v[16:19]\nds_read2_b32 v[32:33], v8 offset0:16 offset1:17\n" \
  "ds_read2_b32 v[34:35], v8 offset0:18 offset1:19\nv_mfma_f32_16x16x32_f16 v[12:15], v[72:75], v[24:27], v[12:15]\n" \
  "ds_read_b128 v[108:111], v2 offset:3520\nv_mfma_f32_16x16x32_f16 v[16:19], v[104:107], v[24:27], v[16:19]\n" \
  "ds_read2_b32 v[36:37], v8 offset0:0 offset1:1\nds_read2_b32 v[38:39], v8 offset0:2 offset1:3\ns_waitcnt lgkmcnt(6)\n" \
  "v_mfma_f32_16x16x32_f16 v[12:15], v[68:71], v[124:127], v[12:15]\nds_read_b128 v[104:107], v2 offset:3584\n" \
  "v_mfma_f32_16x16x32_f16 v[16:19], v[100:103], v[124:127], v[16:19]\nds_read_b128 v[24:27], v2 offset:3648\n" \
  "ds_read2_b32 v[100:101], v8 offset0:32 offset1:33\nds_read2_b32 v[102:103], v8 offset0:34 offset1:35\n" \
  "v_mfma_f32_16x16x32_f16 v[12:15], v[64:67], v[28:31], v[12:15]\nds_read_b128 v[124:127], v2 offset:3712\nds_read_b32 v23, v9 offset:32\n" \
  "v_mfma_f32_16x16x32_f16 v[16:19], v[96:99], v[28:31], v[16:19]\nds_read_b128 v[96:99], v2 offset:3776\ns_waitcnt lgkmcnt(9)\n" \
  "v_mfma_f32_16x16x32_f16 v[12:15], v[60:63], v[112:115], v[12:15]\nds_read_b128 v[28:31], v2 offset:3840\n" \
  "v_mfma_f32_16x16x32_f16 v[16:19], v[92:95], v[112:115], v[16:19]\nds_read_b128 v[92:95], v2 offset:3904\n" \
  "v_mfma_f32_16x16x32_f16 v[12:15], v[56:59], v[108:111], v[12:15]\nds_read_b128 v[112:115], v2 offset:3968\n" \
  "v_mfma_f32_16x16x32_f16 v[16:19], v[88:91], v[108:111], v[16:19]\nds_read_b128 v[88:91], v2 offset:4032\ns_waitcnt lgkmcnt(5)\n" \
  "v_readfirstlane_b32 s85, v23\ns_cmp_lt_u32 s85, 3\ns_cbranch_scc1 71f\n72:\nv_mfma_f32_16x16x32_f16 v[16:19], v[84:87], v[104:107], v[16:19]\n" \
  "ds_read_b64_tr_b16 v[108:109], v5 offset:1024\nds_read_b64_tr_b16 v[110:111], v5 offset:1056\n" \
  "v_mfma_f32_16x16x32_f16 v[16:19], v[80:83], v[24:27], v[16:19]\nds_read_b64_tr_b16 v[84:85], v5 offset:1280\n" \
  "ds_read_b64_tr_b16 v[86:87], v5 offset:1312\nv_mfma_f32_16x16x32_f16 v[16:19], v[76:79], v[124:127], v[16:19]\n" \
  "ds_read_b64_tr_b16 v[104:105], v5 offset:1536\nds_read_b64_tr_b16 v[106:107], v5 offset:1568\ns_waitcnt lgkmcnt(9)\n" \
  "v_mfma_f32_16x16x32_f16 v[16:19], v[72:75], v[96:99], v[16:19]\nds_read_b64_tr_b16 v[80:81], v5 offset:1792\n" \
  "ds_read_b64_tr_b16 v[82:83], v5 offset:1824\nv_mfma_f32_16x16x32_f16 v[16:19], v[68:71], v[28:31], v[16:19]\n" \
  "ds_read_b64_tr_b16 v[24:25], v5 offset:2048\nds_read_b64_tr_b16 v[26:27], v5 offset:2080\ns_waitcnt lgkmcnt(11)\n" \
  "v_mfma_f32_16x16x32_f16 v[16:19], v[64:67], v[92:95], v[16:19]\nds_read_b64_tr_b16 v[76:77], v5 offset:2304\n" \
  "ds_read_b64_tr_b16 v[78:79], v5 offset:2336\nv_mfma_f32_16x16x32_f16 v[16:19], v[60:63], v[112:115], v[16:19]\n" \
  "ds_read_b64_tr_b16 v[124:125], v5 offset:2560\nds_read_b64_tr_b16 v[126:127], v5 offset:2592\ns_waitcnt lgkmcnt(12)\n" \
  "v_mfma_f32_16x16x32_f16 v[16:19], v[56:59], v[88:91], v[16:19]\nds_read_b64_tr_b16 v[72:73], v5 offset:2816\n" \
  "ds_read_b64_tr_b16 v[74:75], v5 offset:2848\nv_mfma_f32_16x16x32_f16 v[12:15], v[108:111], v[52:55], v[12:15]\ns_waitcnt lgkmcnt(10)\n" \
  "v_mfma_f32_16x16x32_f16 v[12:15], v[84:87], v[48:51], v[12:15]\nds_read_b64_tr_b16 v[96:97], v7 offset:0\n" \
  "ds_read_b64_tr_b16 v[98:99], v7 offset:64\nv_mfma_f32_16x16x32_f16 v[12:15], v[104:107], v[44:47], v[12:15]\n" \
  "ds_read_b64_tr_b16 v[68:69], v7 offset:512\nds_read_b64_tr_b16 v[70:71], v7 offset:576\n" \
  "v_mfma_f32_16x16x32_f16 v[16:19], v[104:107], v[52:55], v[16:19]\ns_waitcnt lgkmcnt(10)\n" \
  "v_mfma_f32_16x16x32_f16 v[12:15], v[80:83], v[40:43], v[12:15]\nds_read_b64_tr_b16 v[28:29], v7 offset:1024\n" \
  "ds_read_b64_tr_b16 v[30:31], v7 offset:1088\nv_mfma_f32_16x16x32_f16 v[16:19], v[80:83], v[48:51], v[16:19]\n" \
  "ds_read2_b32 v[64:65], v8 offset0:48 offset1:49\nds_read2_b32 v[66:67], v8 offset0:50 offset1:51\n" \
  "v_mfma_f32_16x16x32_f16 v[12:15], v[24:27], v[120:123], v[12:15]\nv_mfma_f32_16x16x32_f16 v[16:19], v[24:27], v[44:47], v[16:19]\n" \
  "s_waitcnt lgkmcnt(10)\nv_mfma_f32_16x16x32_f16 v[12:15], v[76:79], v[116:119], v[12:15]\nds_read_b64_tr_b16 v[92:93], v7 offset:1536\n" \
  "ds_read_b64_tr_b16 v[94:95], v7 offset:1600\nv_mfma_f32_16x16x32_f16 v[16:19], v[76:79], v[40:43], v[16:19]\n" \
  "v_mfma_f32_16x16x32_f16 v[16:19], v[124:127], v[120:123], v[16:19]\ns_waitcnt lgkmcnt(8)\n" \
  "v_mfma_f32_16x16x32_f16 v[16:19], v[72:75], v[116:119], v[16:19]\nv_mfma_f32_16x16x32_f16 v[12:15], v[96:99], v[32:35], v[12:15]\n" \
  "v_mfma_f32_16x16x32_f16 v[16:19], v[96:99], v[36:39], v[16:19]\ns_waitcnt lgkmcnt(4)\n" \
  "v_mfma_f32_16x16x32_f16 v[12:15], v[68:71], v[100:103], v[12:15]\nv_mfma_f32_16x16x32_f16 v[16:19], v[68:71], v[32:35], v[16:19]\n" \
  "s_waitcnt lgkmcnt(2)\nv_mfma_f32_16x16x32_f16 v[12:15], v[28:31], v[64:67], v[12:15]\n" \
  "v_mfma_f32_16x16x32_f16 v[16:19], v[28:31], v[100:103], v[16:19]\ns_waitcnt lgkmcnt(0)\n" \
  "v_mfma_f32_16x16x32_f16 v[16:19], v[92:95], v[64:67], v[16:19]\ns_nop 4\nv_cvt_pkrtz_f16_f32 v20, v12, v13\nv_cvt_pkrtz_f16_f32 v21, v14, v15\n" \
  "v_pk_add_u16 v20, v20, 4 op_sel_hi:[1,0] clamp\nv_pk_add_u16 v21, v21, 4 op_sel_hi:[1,0] clamp\nds_write_b64 v4, v[20:21] offset:6144\n" \
  "v_cvt_pkrtz_f16_f32 v20, v16, v17\nv_cvt_pkrtz_f16_f32 v21, v18, v19\nv_pk_add_u16 v20, v20, 4 op_sel_hi:[1,0] clamp\n" \
  "v_pk_add_u16 v21, v21, 4 op_sel_hi:[1,0] clamp\nds_write_b64 v4, v[20:21] offset:7168\ns_setprio 0\ns_setpc_b64 s[90:91]\n71:\n" \
  "s_movk_i32 s84, 0x4000\n73:\nds_read_b32 v23, v9 offset:32\ns_sub_u32 s84, s84, 1\ns_cbranch_scc1 72b\ns_waitcnt lgkmcnt(0)\n" \
  "v_readfirstlane_b32 s85, v23\ns_cmp_lt_u32 s85, 3\ns_cbranch_scc1 73b\ns_branch 72b\n"
#define CONV_CLOB "memory", "scc", "s84", "s85", "s90", "s91", "v12", "v13", "v14", "v15", "v16", "v17", "v18", "v19", "v20", "v21", "v22", "v23", "v24", "v25", "v26", "v27", "v28", "v29", "v30", "v31", "v32", "v33", "v34", "v35", "v36", "v37", "v38", "v39", "v40", "v41", "v42", "v43", "v44", "v45", "v46", "v47", "v48", "v49", "v50", "v51", "v52", "v53", "v54", "v55", "v56", "v57", "v58", "v59", "v60", "v61", "v62", "v63", "v64", "v65", "v66", "v67", "v68", "v69", "v70", "v71", "v72", "v73", "v74", "v75", "v76", "v77", "v78", "v79", "v80", "v81", "v82", "v83", "v84", "v85", "v86", "v87", "v88", "v89", "v90", "v91", "v92", "v93", "v94", "v95", "v96", "v97", "v98", "v99", "v100", "v101", "v102", "v103", "v104", "v105", "v106", "v107", "v108", "v109", "v110", "v111", "v112", "v113", "v114", "v115", "v116", "v117", "v118", "v119", "v120", "v121", "v122", "v123", "v124", "v125", "v126", "v127"
constexpr int TDP = 1216, T_3 = 4 * TDP, T_4 = T_3 + 512, TB = T_4 + 320;
constexpr int C_U = 480, UB = C_U + 4096 + 96, C_G5 = 0, C_G3 = 3200, C_G9 = 6400, C_G7 = C_G9 + 2096, GB = C_G7 + 2080 + 112;
constexpr int O_FL = 2 * TB, O_UL = O_FL + 64, O_IM = O_UL + 2 * UB, O_ST = O_IM + 2 * GB;
constexpr int X_TB = 10240, X_UL = X_TB + TB, X_IM = X_UL + UB, X_ST = X_IM + GB;
constexpr int X_WR = X_IM + 128 * IP;
#define X_RW(w) (LW + 9216 * (2 + ((w) >> 1)) + 2560 * ((w) & 1))
constexpr int RP = 1216;
constexpr int LW = 36864 + 512, LA = LW, LM = LW + 36864;
constexpr int L_SC = LM + 2048, SMEM = O_IM + 256 * IP;
static_assert(L_SC + 1152 <= SMEM && O_ST + 8192 <= SMEM && X_IM + 128 * IP <= LM && X_ST + 8192 <= LM && UB % 128 == 64 && GB % 128 == 64 && SMEM <= 79872, "");
struct TOps { uint32_t xa, xb, ya, pd, pf, pg, pe; };
__device__ __forceinline__ void table_ops(uint32_t im, uint32_t tb, int ct, int lane, TOps& t) {
  const int j = lane & 15, kq = lane >> 4, q4 = (lane >> 2) & 3, p4 = lane & 3, jj = j + 16 * ct;
  const uint32_t sa = im + (uint32_t)(IP * (lane >> 2));
  t.xa = sa + (uint32_t)(I_X + 8 * p4); t.xb = sa + (uint32_t)(I_X + 32 + 8 * (p4 & 1)); t.ya = sa + (uint32_t)(32 * ct + 8 * p4);
  t.pd = tb + (uint32_t)(2 * (541 - (8 * jj + 4 * (kq & 1) + 256 * (kq >> 1))));
  t.pf = tb + (uint32_t)((kq & 1) ? T_4 + 2 * (63 - jj) : T_3 + 2 * (127 - 2 * jj));
  t.pg = t.pf - ((kq & 1) ? 64 : 2);
  t.pe = tb + (uint32_t)(p4 * (TDP + 2) + ((lane < 12) ? 2 * (32 - q4) : 2 * (572 + (q4 & 1))));
  asm volatile("" : "+v"(t.xa), "+v"(t.xb), "+v"(t.ya), "+v"(t.pd), "+v"(t.pf), "+v"(t.pg), "+v"(t.pe));
}
typedef __attribute__((address_space(3))) char* lptr;
#define LP(T, a) ((__attribute__((address_space(3))) T*)(uintptr_t)(a))
__device__ __forceinline__ uint32_t table_tile(const TOps& t, int ct, int lane, uint32_t evp, float dsk TSP) {
  u32x4 ev;
  f32x4 ad[2], af[2];
  asm volatile(T_ASM : "=&v"(ad[0]), "=&v"(ad[1]), "=&v"(af[0]), "=&v"(af[1]), "=&v"(ev) : "v"(t.xa), "v"(t.xb), "v"(t.ya), "v"(evp) : T_CLOB);
  TS(0, 4);
  const int e0 = (int)(ev[0] + ev[2]), e1 = (int)(ev[1] + ev[3]);
  const int em = min(max(max(max(e0, e1), (int)((__float_as_uint(dsk) >> 23) & 0xffu) - 133), -110), 115);
  const int ts = ((uint32_t)(em + 12) <= 8u) ? 1 : -3 - em;
  float s0 = __builtin_amdgcn_ldexpf(1.f, e0 - 29 + ts), s1 = __builtin_amdgcn_ldexpf(1.f, e1 - 29 + ts);
  float d0 = (ct == 0 && lane == 0) ? __builtin_amdgcn_ldexpf(dsk, ts) : 0.f;
  asm volatile("s_nop 1" : "+v"(ad[0]), "+v"(ad[1]), "+v"(af[0]), "+v"(af[1]), "+v"(s0), "+v"(s1), "+v"(d0));
  const f32x4 k = ad[0] * s0 + (f32x4){d0, 0.f, 0.f, 0.f} + ad[1] * s1, f = af[0] * s0 + af[1] * s1;
  auto pk = [](float a, float b) { uint32_t r; asm("v_cvt_pk_f16_f32 %0, %1, %2" : "=v"(r) : "v"(a), "v"(b)); return r; };
  asm volatile("s_nop 0" : : "v"(k), "v"(f));
  const uint32_t p32 = pk(k[3], k[2]), p10 = pk(k[1], k[0]), p21 = pk(k[2], k[1]), p01 = pk(f[0], f[1]), p23 = pk(f[2], f[3]);
  *LP(u32x2, t.pd + 3 * TDP + 6) = (u32x2){p32, p10};
  *LP(uint32_t, t.pd + TDP + 2) = p32; *LP(uint32_t, t.pd + TDP + 6) = p10;
  *LP(uint32_t, t.pd + 2) = p21; *LP(uint32_t, t.pd + 2 * TDP + 6) = p21;
  *LP(u16, t.pd) = (u16)p32; *LP(u16, t.pd + 2 * TDP + 4) = (u16)p32;
  asm volatile("ds_write_b16_d16_hi %0, %1 offset:6\n\tds_write_b16_d16_hi %0, %1 offset:%2" : : "v"(t.pd), "v"(p10), "n"(2 * TDP + 10) : "memory");
  if (ct) {
    const uint32_t va = (uint32_t)__builtin_amdgcn_readlane((int)p32, 63), vb = (uint32_t)__builtin_amdgcn_readlane((int)p23, 15) >> 16;
    *LP(u16, t.pe) = (u16)((lane < 12) ? va : vb);
  }
  if (lane < 32) {
    *LP(u16, t.pf + 128) = (u16)p01; *LP(u16, t.pg + 128) = (u16)(p01 >> 16);
    *LP(u16, t.pf) = (u16)p23; *LP(u16, t.pg) = (u16)(p23 >> 16);
  }
  return (uint32_t)(127 + 5 - ts) << 23;
}
__device__ __forceinline__ uint32_t hadd(uint32_t a, uint32_t b) { return __builtin_bit_cast(uint32_t, __builtin_bit_cast(f16x2, a) + __builtin_bit_cast(f16x2, b)); }
template <bool FULL> __device__ __forceinline__ void sums(char* sm, const u32x4 p, const u32x4 c) {
  const uint32_t D[8] = {p[0], p[1], p[2], p[3], c[0], c[1], c[2], c[3]};
  uint32_t O[7];
#pragma unroll
  for (int j = 0; j < 7; ++j) O[j] = __builtin_amdgcn_alignbit(D[j + 1], D[j], 16);
  u32x4 a, b;
#pragma unroll
  for (int j = 0; j < 4; ++j) a[j] = hadd(D[j + 2], hadd(O[j + 1], O[j + 2]));
  *(u32x4*)(sm + C_G3 + 32) = a;
#pragma unroll
  for (int j = 0; j < 4; ++j) b[j] = hadd(a[j], hadd(D[j + 1], D[j + 3]));
  *(u32x4*)(sm + C_G5 + 32) = b;
  if (FULL) {
#pragma unroll
    for (int j = 0; j < 4; ++j) a[j] = hadd(b[j], hadd(O[j], O[j + 3]));
    *(u32x4*)(sm + C_G7 + 32) = a;
#pragma unroll
    for (int j = 0; j < 4; ++j) b[j] = hadd(a[j], hadd(D[j], D[j + 4]));
    *(u32x4*)(sm + C_G9 + 32) = b;
  }
}
__device__ __forceinline__ void count_up(uint32_t a) {
  uint32_t t;
  asm volatile("s_mov_b64 exec, 1\n\tv_mov_b32 %0, 1\n\tds_add_u32 %1, %0\n\ts_mov_b64 exec, -1" : "=&v"(t) : "v"(a) : "memory");
}
__device__ __forceinline__ int pad_at(int r) { return 16 * (r & 1) + 3200 * (r >> 1) - ((r >= 6) ? 3200 - 2096 : 0); }
struct COps { uint32_t b, a, o, g3, k3, g4, k4, f; };
__device__ __forceinline__ void conv_ops(uint32_t tb, uint32_t ub, uint32_t cb, int lane, COps& e) {
  const uint32_t j = lane & 15, kq = lane >> 4, q4 = (lane >> 2) & 3, p4 = lane & 3;
  e.b = ub + 32u * j + 16u * kq;
  e.a = tb + p4 * TDP + 32u - 8u * q4 + 16u * kq;
  e.g3 = cb + C_G5 + 8 + 64u * kq + 8u * (q4 + p4) + (q4 & 1u) * 3200u;
  e.g4 = cb + C_G9 + 8 + 128u * kq + 16u * q4 + 8u * p4 + (q4 & 1u) * 2096u;
  e.k3 = tb + T_3 + 120 - 8u * j + 16u * kq;
  e.k4 = tb + T_4 + 60 - 4u * j + 16u * kq;
  asm volatile("" : "+v"(e.b), "+v"(e.a), "+v"(e.g3), "+v"(e.g4), "+v"(e.k3), "+v"(e.k4));
}
#define CONV_RUN(prog, e, ...) do { int prog_ = (prog); \
    asm volatile("s_call_b64 s[90:91], kf_conv" : "+{s89}"(prog_), "+{v2}"(e.b), "+{v3}"(e.a), "+{v4}"(e.o), "+{v5}"(e.g3), "+{v6}"(e.k3), "+{v7}"(e.g4), \
                 "+{v8}"(e.k4), "+{v9}"(e.f) __VA_ARGS__ : : CONV_CLOB); } while (0)

template <bool E>
__device__ __forceinline__ void ln_stats(const uint32_t* v, int lane, float sc, f32x2* t, f32x4 z, f32x2& rc, f32x2& nc, float& e) {
  float s1a = z[0], s2a = z[1], s1b = z[2], s2b = z[3];
#pragma unroll
  for (int k = 0; k < 4; ++k) {
    s1a = dot2(v[k], 0x3f803f80u, s1a); s2a = dot2(v[k], v[k], s2a);
    s1b = dot2(v[4 + k], 0x3f803f80u, s1b); s2b = dot2(v[4 + k], v[4 + k], s2b);
  }
#pragma unroll
  for (int k = 0; k < 9; ++k) t[k] = (f32x2){bflo(v[k]), bfhi(v[k])};
  const float rs = row_sum(fold16(dot2(v[8], 0x3f803f80u, fold32(s1a, s1b)), dot2(v[8], v[8], fold32(s2a, s2b))));
  if (E) { e = row_sum(e); e += __int_as_float(__builtin_amdgcn_update_dpp(0, __float_as_int(e), 0x142, 0xa, 0xf, false)); }
  const f32x2 mean = (f32x2){rdl(rs, 15), rdl(rs, 47)} * (1.f / D_), m2 = mean * mean;
  f32x2 var = (f32x2){rdl(rs, 31), rdl(rs, 63)} * (1.f / D_) - m2;
  asm volatile("" : "+v"(t[0]), "+v"(t[1]), "+v"(t[2]), "+v"(t[3]), "+v"(t[4]), "+v"(t[5]), "+v"(t[6]), "+v"(t[7]), "+v"(t[8]));
  if (__builtin_expect(!(m2[0] <= 16.f * var[0] && m2[1] <= 16.f * var[1]), 0)) {
    float q[3] = {0.f, 0.f, 0.f};
#pragma unroll
    for (int k = 0; k < 9; ++k) {
      const f32x2 a = t[k] - ((k < 4 || (k == 8 && lane < 32)) ? mean[0] : mean[1]);
      q[k >> 2] += a[0] * a[0] + a[1] * a[1];
    }
    float qa = q[0] + ((lane < 32) ? q[2] : 0.f), qb = q[1] + ((lane < 32) ? 0.f : q[2]);
    wave_sum2(qa, qb);
    var = (f32x2){qa, qb} * (1.f / D_);
  }
  const uint32_t mp = pack2(mean[0], mean[1]), vp = pack2(var[0], var[1]);
  const f32x2 ve = (f32x2){bflo(vp), bfhi(vp)} + 1e-5f;
  const uint32_t e2 = pack2(ve[0], ve[1]);
  const uint32_t sp = pack2(__builtin_amdgcn_sqrtf(bflo(e2)), __builtin_amdgcn_sqrtf(bfhi(e2)));
  rc = (f32x2){__builtin_amdgcn_rcpf(bflo(sp)), __builtin_amdgcn_rcpf(bfhi(sp))} * sc;
  nc = -(f32x2){bflo(mp), bfhi(mp)} * rc;
}
template <bool H>
__device__ __forceinline__ void ln_apply(const f32x2* t, int lane, f32x2 rc, f32x2 nc, uint32_t* c) {
  const float nm[3] = {nc[0], nc[1], (lane < 32) ? nc[0] : nc[1]}, rr[3] = {rc[0], rc[1], (lane < 32) ? rc[0] : rc[1]};
  f32x2 dd[9];
#pragma unroll
  for (int k = 0; k < 9; ++k) dd[k] = __builtin_elementwise_fma(t[k], (f32x2){rr[k >> 2], rr[k >> 2]}, (f32x2){nm[k >> 2], nm[k >> 2]});
  asm volatile("" : "+v"(dd[0]), "+v"(dd[1]), "+v"(dd[2]), "+v"(dd[3]), "+v"(dd[4]), "+v"(dd[5]), "+v"(dd[6]), "+v"(dd[7]), "+v"(dd[8]));
#pragma unroll
  for (int k = 0; k < 9; ++k) {
    const f32x2 d = dd[k];
    typedef __attribute__((ext_vector_type(2))) uint16_t u16x2;
    if (H) c[k] = __builtin_bit_cast(uint32_t, __builtin_elementwise_add_sat(__builtin_bit_cast(u16x2, __builtin_amdgcn_cvt_pkrtz(d[0], d[1])), (u16x2){4, 4}));
    else c[k] = pack2(d[0], d[1]);
  }
}
__device__ __forceinline__ uint32_t med3u(uint32_t a, uint32_t b, uint32_t c) { return max(min(a, b), min(max(a, b), c)); }
__device__ __forceinline__ uint32_t oct_maxu(uint32_t v) {
  v = max(v, (uint32_t)DPP_I((int)v, 0xb1, 0xf));
  v = max(v, (uint32_t)DPP_I((int)v, 0x4e, 0xf));
  v = max(v, (uint32_t)DPP_I((int)v, 0x141, 0xf));
  return v;
}

#define KARGS const u16* __restrict__ x, const u16* __restrict__ Are, const u16* __restrict__ Aim, const u16* __restrict__ Bre, \
    const u16* __restrict__ Bim, const u16* __restrict__ Cre, const u16* __restrict__ Cim, const u16* __restrict__ logdt, \
    const u16* __restrict__ Dskip, char* __restrict__ ws, const u16* __restrict__ wr, const u16* __restrict__ wg, \
    const u16* __restrict__ wu, const u16* __restrict__ wd, u16* __restrict__ out
#define KPASS x, Are, Aim, Bre, Bim, Cre, Cim, logdt, Dskip, ws, wr, wg, wu, wd, out
template <int ROLE, int FFN = -1>
__device__ __forceinline__ void role_body(char* smem, KARGS) {
  const int tid = threadIdx.x, lane = tid & 63;
  const int w = __builtin_amdgcn_readfirstlane(tid >> 6), wl = w & 3;
  const int b = blockIdx.x & 255, bx = b & 7, bj = b >> 3;
  constexpr bool isl = ROLE == 1 || ROLE == 2;
  const int tg = isl ? 32 * (bj >> 2) + 4 * bx + (bj & 3) : 32 * bx + bj;
  u32x4 xa, xc;
  uint32_t ep, r0;
#define P_WAIT(n, ...) asm volatile("s_waitcnt vmcnt(" #n ")\n\tv_lshlrev_b32 %0, 16, v100\n\tv_lshlrev_b32 %1, 16, v101\n\tv_lshlrev_b32 %2, 16, v102" \
    : "=v"(ldt), "=v"(are), "=v"(aim) : : "v100", "v101", "v102" __VA_ARGS__)
#define P_MORE() asm volatile("v_lshlrev_b32 %0, 16, v103\n\tv_lshlrev_b32 %1, 16, v104\n\tv_lshlrev_b32 %2, 16, v105\n\tv_lshlrev_b32 %3, 16, v106" \
    : "=v"(bre), "=v"(bim), "=v"(cre), "=v"(cim) : : "v103", "v104", "v105", "v106")
  TSD;
  TS(0, 0);
  const uint32_t lb0 = (uint32_t)(uintptr_t)(__attribute__((address_space(3))) char*)smem;
  const int i = lane & 15, kq = lane >> 4;
  const u32x4 ones = {0xffffffffu, 0xffffffffu, 0xffffffffu, 0xffffffffu}, zero4 = {0u, 0u, 0u, 0u};
  auto u_again = [&](u32x4& uw, const u32x4& ut, uint32_t uo) {
#pragma unroll 1
    for (int spin = 0; spin < SPIN_CAP; ++spin) {
      const bool na = ((uw[0] | uw[1] | uw[2] | uw[3]) & UMASK) != 0u;
      if (__builtin_expect(!__any(na), 1)) break;
      TSC(19);
      SLEEP(USLEEP);
      if (na) { LD16W(uw, uo); uw ^= ut; }
    }
  };
  constexpr uint32_t S16 = 0x41800000u, LMASK = 0x00070007u;
  auto v_put = [&](u32x4 v, const u32x4& ut, bool other, uint32_t vo) {
    const bool second = __any(other || ((v[0] | v[1] | v[2] | v[3]) & 0x40004000u) != 0u);
    if (__builtin_expect(second, 0)) v = (v & ~LMASK) | (~ut & LMASK); else v = (v & ~LMASK) | ut;
    ST16(vo, v);
    return second;
  };
  auto v_end = [&](uint32_t T, int n) { DRAIN(); if (lane == 0) ST4(O_DN + 16u * n, T); };
  if constexpr (!isl) {
    const int ch = wl >> 1, tl = tid & 255;
    int* evp = (int*)(smem + O_FL + 16 * ch);
    float ldt, are, aim, dsk;
    if constexpr (ROLE == 0) {
      __builtin_amdgcn_s_setprio(KPRIO);
      COps e;
      conv_ops(lb0 + (uint32_t)(ch * TB), lb0 + (uint32_t)(O_UL + ch * UB), lb0 + (uint32_t)(O_IM + ch * GB), lane, e);
      e.o = lb0 + O_ST + (uint32_t)(16 * ((4 * i + 2 * (kq >> 1) + ch) ^ (i >> 1)) + 8 * (kq & 1));
      e.f = lb0 + O_FL;
      TOps t;
      table_ops(lb0 + (uint32_t)(O_IM + 128 * IP * ch), lb0 + (uint32_t)(TB * ch), wl & 1, lane, t);
      uint32_t pz = lb0 + O_IM + (uint32_t)(((lane >> 3) & 1) * GB + pad_at(lane & 7)), im = lb0 + (uint32_t)(O_IM + IP * tl);
      asm volatile("" : "+v"(e.o), "+v"(e.f), "+v"(pz), "+v"(im));
      float bre, bim, cre, cim;
      P_WAIT(0, , "v103", "v104", "v105", "v106", "v107", "v109");
      asm volatile("" : "={v109}"(ep));
      const uint32_t T = ((uint32_t)__builtin_amdgcn_readfirstlane(ep) + 1u) * 0x9E3779B1u;
      if (w == 3 && lane < 8) ST16(O_KEY + 128u * b + 16u * lane, ones);
      P_MORE();
      asm volatile("v_lshlrev_b32 %0, 16, v107" : "=v"(dsk) : : "v107");
      TS(0, 1);
      evp[wl & 1] = gen_y(ldt, are, aim, bre, bim, cre, cim, im, [] {} TSA);
      TS(0, 8);
      BAR();
      TS(0, 2);
      const uint32_t cv = table_tile(t, wl & 1, lane, lb0 + O_FL + 16 * ch, dsk TSA), cs = (uint32_t)__builtin_amdgcn_readfirstlane(cv);
      *(uint32_t*)(smem + O_FL + 40 + 4 * ch) = cv;
      TS(0, 9);
      gsync0(lb0 + O_FL + 36, 4);
      TS(0, 3);
      *(u32x4*)(smem + O_ST + 16 * tl) = ones;
      *(u32x4*)(smem + O_ST + 4096 + 16 * tl) = ones;
      if (w == 0) *LP(u32x4, pz) = zero4;
      DRAIN();
      CONV_RUN(wl & 1, e);
      if (wl & 1) asm volatile("s_mov_b64 exec, 1\n\tglobal_store_dwordx2 %0, %1, %2 sc1" : : "v"(O_SC + 16u * (2 * tg + ch)), "v"((u32x2){T, cs}), "s"(ws) : "memory");
      TS(0, 5);
    } else {
      __builtin_amdgcn_s_setprio(UPRIO);
#pragma unroll
      for (int k = 0; k < 3; ++k) *(u32x4*)(smem + ((16 * (tl + 256 * k) < O_FL) ? 16 * (tl + 256 * k) : O_FL + 32)) = zero4;
      if (w == 7) *(u32x4*)(smem + O_UL + (lane >> 5) * UB + 16 * min(lane & 31, 29)) = zero4;
      uint32_t uo = u_at(tl >> 1, 2 * tg + (tid & 1)), go = O_SU + (uint32_t)(256 * bx + 32 * wl + (lane & 31) + 4 * (lane & 32)) * 128u;
      uint32_t ul = (uint32_t)(O_UL + (tid & 1) * UB + C_U - 16 + 16 * (tl >> 1)), ug = (uint32_t)(O_IM + (tid & 1) * GB + 16 * (tl >> 1)), so = lb0 + O_ST + 16u * (uint32_t)(lane ^ (lane >> 3));
      uint32_t vo = O_V + (uint32_t)tg * VP + 16u * lane;
      asm volatile("" : "+v"(uo), "+v"(go), "+v"(ul), "+v"(ug), "+v"(so), "+v"(vo));
      P_WAIT(1, , "v109");
      TS(0, 1);
      evp[2 + (wl & 1)] = gen_x(ldt, are, aim, smem + O_IM + IP * tl);
      asm volatile("s_waitcnt vmcnt(0)" : "={v109}"(ep));
      const uint32_t T = ((uint32_t)__builtin_amdgcn_readfirstlane(ep) + 1u) * 0x9E3779B1u;
      TS(0, 8);
      BAR();
      TS(0, 2);
      u32x4 ut = {utag(T, 0), utag(T, 1), utag(T, 2), utag(T, 3)};
      asm volatile("" : "+v"(ut));
      SLEEP(GSLEEP);
      u32x2 pv;
      pv[1] = gate(ws, go, T);
      TS(0, 3);
      until(latest(pv[1]) + UD);
      TS(0, 7);
      u32x4 uw, ux;
      asm volatile("global_load_dwordx4 %0, %2, %4 sc1\n\tglobal_load_dwordx4 %1, %3, %4 sc1"
                   : "=&v"(uw), "=&v"(ux) : "v"(uo), "v"(uo + 64 * UP * 32), "s"(ws) : "memory");
      __builtin_amdgcn_s_setprio(FPR);
      asm volatile("s_waitcnt vmcnt(1)" : "+v"(uw));
      uw ^= ut;
      *(u32x4*)(smem + ul + 16) = uw;
      asm volatile("s_waitcnt vmcnt(0)" : "+v"(ux));
      TS(0, 11);
      ux ^= ut;
      *(u32x4*)(smem + ul + 2048 + 16) = ux;
      if (__builtin_expect(__any(((uw[0] | uw[1] | uw[2] | uw[3] | ux[0] | ux[1] | ux[2] | ux[3]) & UMASK) != 0u), 0)) {
        u_again(uw, ut, uo);
        u_again(ux, ut, uo + 64 * UP * 32);
        *(u32x4*)(smem + ul + 16) = uw;
        *(u32x4*)(smem + ul + 2048 + 16) = ux;
      }
      BAR();
      TS(0, 4);
      const u32x4 pw = *(const u32x4*)(smem + ul);
      if (wl < 2) {
        const u32x4 px = *(const u32x4*)(smem + ul + 2048);
        sums<true>(smem + ug, pw, uw);
        sums<false>(smem + ug + 2048, px, ux);
      } else sums<true>(smem + ug, pw, uw);
      count_up(lb0 + O_FL + 32);
      __builtin_amdgcn_s_setprio(SPR);
      TS(0, 5);
      const bool other = *(const uint32_t*)(smem + O_FL + 40 + 4 * (lane & 1)) != S16;
      bool second = false;
#pragma unroll
      for (int k = 0; k < 2; ++k) {
        const uint32_t o = ((k ? 0x3247u : 0x1065u) >> (4 * wl) & 7u) * 1024u;
        u32x4 v;
#pragma unroll 1
        for (int spin = 0; spin < SPIN_CAP; ++spin) {
          asm volatile("ds_read_b128 %0, %1\n\ts_waitcnt lgkmcnt(0)" : "=&v"(v) : "v"(so + o) : "memory");
          if (!__any(max4(v) == 0xffffffffu)) break;
          __builtin_amdgcn_s_sleep(VPS);
        }
        TS(0, 12 + k);
        second |= v_put(v, ut, other, vo + o);
        TS(0, 10 - 4 * k);
      }
      if (__builtin_expect(second, 0)) v_end(T, 4 * tg + wl);
    }
    TSE();
  } else {
  uint32_t* cand = (uint32_t*)(smem + LM);
  uint32_t* sel = cand + 128;
  uint32_t* kl = sel + 48;
  uint32_t* cnt = sel + 64;
  const uint32_t ca = lb0 + LM + 4 * (128 + 64);
  u16* hm = (u16*)(cnt + 12);
  uint32_t T;
  const bool ffn = (FFN < 0) ? bj < 18 : FFN, isx = FFN != 1 && bj >= 24;
  const int fb = 18 * bx + bj;
  constexpr bool ln2 = ROLE == 2;
  const int g0 = tg, g2 = tg;
  u32x4 bw[18];
#pragma unroll
  for (int s = 0; s < 9; ++s) bw[s] = (u32x4){0u, 0u, 0u, 0u};
  int tid2 = threadIdx.x;
  uint32_t ts1 = 0u;
  auto rest = [&] {
    if (ts1 && (int)((uint32_t)__builtin_amdgcn_s_memrealtime() - ts1) > 0) { TS(0, 8); until(ts1 + TSTOP + RD); if (RW) gwait(ca, 4); ts1 = 0u; }
  };
  auto gu_dma = [&](int q0, int q1, auto depth) {
    const char* gs = (const char*)((w < 2) ? wg : wu) + (size_t)((144 * (w & 1)) * (F_ * 2) + fb * 64);
    const uint32_t gv = (uint32_t)((lane >> 2) * (F_ * 2) + 16 * ((lane & 3) ^ (lane >> 5) ^ (w & 2)));
#pragma unroll 1
    for (int q = q0; q < q1; ++q) {
      const int k = (int)((((bj & 1) ? ORD1 : ((unsigned __int128)0x876543210u << 36 | 0x876543210ull)) >> (4 * (q - (q >= 9 ? 9 : 0)) + (q >= 9 ? 36 : 0))) & 15u), h = q >= 9;
      if (q >= RQ) rest();
      dma16(gs + (size_t)(288 * h + 16 * k) * (F_ * 2), gv, lb0 + LW * h + 9216 * w + 1024 * k);
      asm volatile("s_waitcnt vmcnt(%0)" : : "n"(depth.value) : "memory");
    }
  };
  auto down_dma = [&](int k0, int k1, int lane) {
#pragma unroll 1
    for (int k = k0; k < k1; ++k) {
      const int o = 9216 * wl + 2304 * k;
      const char* ds = (const char*)wd + (size_t)(fb * 36864 + o);
      const uint32_t dl = lb0 + o + 32 * ((4 * wl + k + 1) >> 1);
      if (ROLE == 1) rest();
      dma16d(ds, 16u * lane, dl);
      dma16d(ds + 1024, 16u * lane, dl + 1024);
      uint32_t keep;
      asm volatile("s_mov_b32 %0, m0\n\ts_mov_b32 m0, %3\n\ts_mov_b64 exec, 0xffff\n\tglobal_load_lds_dwordx4 %1, %2" DPOL2 "\n\ts_mov_b64 exec, -1\n\ts_mov_b32 m0, %0\n\ts_waitcnt vmcnt(%4)"
                   : "=&s"(keep) : "v"(16u * lane), "s"(ds + 2048), "s"(dl + 2048), "n"(ROLE == 1 ? DDEPTH : 40) : "memory");
    }
  };
  auto gu_read = [&](int h) {
    const uint32_t ra = lb0 + LW * h + (uint32_t)(((lane >> 1) & 1) * 18432 + (8 * kq + ((lane >> 2) & 3)) * 64 + ((w ^ (kq & 1) ^ (lane & 2)) * 16) + 8 * (lane & 1));
#pragma unroll
    for (int s = 0; s < 9; ++s) {
      const u32x2 h0 = trd(ra + 2048 * s), h1 = trd(ra + 2048 * s + 256);
      bw[9 * h + s] = (u32x4){h0[0], h0[1], h1[0], h1[1]};
    }
#pragma unroll
    for (int s = 0; s < 9; ++s) asm volatile("" : "+v"(bw[9 * h + s]));
  };
  uint32_t tv = 0u, tk = 0u;
  const uint32_t vearly = (uint32_t)((VEARLY >> (8 * (g0 >> 5))) & 0xffull);
#define TOUCH(pa) asm volatile("global_load_dword v126, %0, off" DPOL4 : : "v"(pa) : "memory", "v126")
  uint32_t wv[5], xv[9];
  auto rows_xw = [&](int lane) {
    u32x4 wa, xr, xs;
    const uint32_t xo = (uint32_t)((8 * g2 + 2 * wl) * (D_ * 2));
    asm volatile("global_load_dwordx4 %0, %5, %9\n\tglobal_load_dword %1, %6, %9 offset:1024\n\t"
                 "global_load_dwordx4 %2, %7, %10\n\tglobal_load_dwordx4 %3, %7, %10 offset:1152\n\tglobal_load_dword %4, %8, %10 offset:1024\n\ts_waitcnt vmcnt(0)"
                 : "=&v"(wa), "=&v"(wv[4]), "=&v"(xr), "=&v"(xs), "=&v"(xv[8])
                 : "v"(16u * lane), "v"(4u * (lane & 31)), "v"(xo + 16u * lane), "v"(xo + 4u * lane + 32u * (lane & 32)), "s"(wr), "s"(x) : "memory");
#pragma unroll
    for (int k = 0; k < 4; ++k) { wv[k] = wa[k]; xv[k] = xr[k]; xv[4 + k] = xs[k]; }
  };
  auto ln1 = [&](int g, int o, uint32_t c0) {
    uint32_t uo = u_at(g, 64 * wl + lane);
    uint32_t ta = lb0 + (uint32_t)(o + ((lane >> 2) & 3) * RP + (64 * wl + 16 * kq + 4 * (lane & 3)) * 2);
    uint32_t la = (uint32_t)(o + 2 * wl * RP + 16 * lane), lt = (uint32_t)(o + (2 * wl + (lane >> 5)) * RP + 1024 + 4 * (lane & 31));
    uint32_t sg = O_SU + (uint32_t)(256 * (lane >> 3) + g) * 128u + 16u * (lane & 7);
    asm volatile("" : "+v"(uo), "+v"(ta), "+v"(la), "+v"(lt), "+v"(sg));
    f32x4 z4 = {0.f, 0.f, 0.f, 0.f};
    asm volatile("" : "+v"(z4));
    asm volatile("s_waitcnt vmcnt(0)" : "={v[100:103]}"(xa), "={v[104:107]}"(xc), "={v108}"(r0), "={v109}"(ep));
    TS(0, 1);
    const uint32_t v[9] = {xa[0], xa[1], xa[2], xa[3], xc[0], xc[1], xc[2], xc[3], r0};
    uint32_t c[9];
    f32x2 tf[9];
    T = ((uint32_t)__builtin_amdgcn_readfirstlane(ep) + 1u) * 0x9E3779B1u;
    if (wl == 3) { const uint32_t now = (uint32_t)__builtin_amdgcn_s_memrealtime() + SD; ST16(sg, ((u32x4){T, now, T, now})); }
    const uint32_t tj = ((T >> (3 * wl)) & 0x00070007u) | (((T >> (12 + wl)) & 0x00010001u) << 14);
    uint32_t tw[3] = {(tj & 0xffffu) * 0x10001u, (tj >> 16) * 0x10001u, 0u};
    tw[2] = (lane < 32) ? tw[0] : tw[1];
    asm volatile("" : "+v"(tw[0]), "+v"(tw[1]), "+v"(tw[2]));
    f32x2 rc, nc;
    float e_;
    ln_stats<false>(v, lane, 0.03125f, tf, z4, rc, nc, e_);
    ln_apply<true>(tf, lane, rc, nc, c);
#pragma unroll
    for (int k = 0; k < 9; ++k) c[k] = (c[k] & ~UMASK) | tw[k >> 2];
    *(u32x4*)(smem + la) = (u32x4){c[0], c[1], c[2], c[3]};
    *(u32x4*)(smem + la + RP) = (u32x4){c[4], c[5], c[6], c[7]};
    *(uint32_t*)(smem + lt) = c[8];
    u32x4 ha, hb;
    {
      uint32_t t, cc, n = SPIN_CAP;
      asm volatile("s_mov_b64 exec, 1\n\tv_mov_b32 %2, 1\n\tds_add_u32 %5, %2\n\ts_mov_b64 exec, -1\n"
                   "1:\n\tds_read_b32 %2, %5\n\tds_read_b64_tr_b16 v[40:41], %6\n\tds_read_b64_tr_b16 v[42:43], %6 offset:%7\n\tds_read_b64_tr_b16 v[44:45], %6 offset:512\n\t"
                   "ds_read_b64_tr_b16 v[46:47], %6 offset:%8\n\ts_sub_u32 %4, %4, 1\n\ts_cbranch_scc1 2f\n\ts_waitcnt lgkmcnt(4)\n\tv_readfirstlane_b32 %3, %2\n\t"
                   "s_cmp_lt_u32 %3, 4\n\ts_cbranch_scc1 1b\n2:\n\ts_waitcnt lgkmcnt(0)"
                   : "=&{v[40:43]}"(ha), "=&{v[44:47]}"(hb), "=&v"(t), "=&s"(cc), "+s"(n) : "v"(c0), "v"(ta), "n"(4 * RP), "n"(512 + 4 * RP) : "memory", "scc");
    }
    ST16(uo, ha);
    ST16(uo + 64 * 128, hb);
    if (wl == 0) {
      const u32x2 h4 = trd(ta + 1024), h5 = trd(ta + 1024 + 4 * RP);
      ST16(uo + 128 * 128, ((u32x4){h4[0], h4[1], h5[0], h5[1]}));
    }
  };
  const int dX = 512 - 24 + 8 * bx + bj;
  if constexpr (ROLE == 2) {
    const int tl = tid - 256;
    if (isx) {
      __builtin_amdgcn_s_setprio(QPRIO0);
      *(u32x4*)(smem + X_TB + 16 * tl) = zero4;
      *(u32x4*)(smem + X_TB + 16 * min(256 + tl, (TB + C_U) / 16 - 1)) = zero4;
      const int pr = (wl + XROT) & 3;
      COps e;
      conv_ops(lb0 + X_TB, lb0 + X_UL, lb0 + X_IM, lane, e);
      TOps t;
      table_ops(lb0 + X_IM, lb0 + X_TB, wl & 1, lane, t);
      e.o = lb0 + X_ST + (uint32_t)(16 * ((2 * i + (kq >> 1)) ^ ((i >> 2) & 1)) + 8 * (kq & 1));
      e.f = ca + 28 - 32;
      asm volatile("" : "+v"(e.o), "+v"(e.f));
      float ldt, are, aim, bre, bim, cre, cim, dsk;
      P_WAIT(0, , "v103", "v104", "v105", "v106", "v107", "v109");
      asm volatile("" : "={v109}"(ep));
      T = ((uint32_t)__builtin_amdgcn_readfirstlane(ep) + 1u) * 0x9E3779B1u;
      P_MORE();
      asm volatile("v_lshlrev_b32 %0, 16, v107" : "=v"(dsk) : : "v107");
      TS(0, 1);
      __builtin_amdgcn_s_setprio(QPRIO);
      int* evp = (int*)(sel + 44);
      char* im = smem + X_IM + IP * (tl & 127);
      evp[wl] = (wl < 2) ? gen_y(ldt, are, aim, bre, bim, cre, cim, lb0 + X_IM + IP * tl, [] {} TSA) : gen_x(ldt, are, aim, im);
      TS(0, 2);
      __builtin_amdgcn_s_setprio(QPRIO2);
      gsync(ca + 8, 4);
      if (wl < 2) evp[-4 + wl] = (int)table_tile(t, wl & 1, lane, lb0 + LM + 4 * (128 + 44), dsk TSA);
      if (wl < 2) gsync0(ca + 8, 8); else { count_up(ca + 8); SLEEP(XSL); gwait(ca + 8, 8); }
      const uint32_t cs = (uint32_t)__builtin_amdgcn_readfirstlane(evp[-4]);
      if (wl == 3) *(u32x4*)(smem + X_IM + pad_at(lane & 7)) = zero4;
      TS(0, 3);
      CONV_RUN(2 + pr, e, , "+{v11}"(tid2));
      __builtin_amdgcn_s_setprio(3);
      {
        const int lane = tid2 & 63;
        const u32x4 v = *(const u32x4*)(smem + X_ST + 2048 * pr + 1024 * (lane >> 5) + 16 * ((lane & 31) ^ ((lane >> 3) & 1)));
        if (__builtin_expect(v_put(v, (u32x4){utag(T, 0), utag(T, 1), utag(T, 2), utag(T, 3)}, cs != S16, v_at(dX, 64 * pr + lane)), 0)) {
          DRAIN();
          if (lane == 0) ST4(O_DN + 16u * (4 * (dX - 256) + pr), T);
        }
        if (pr == 0 && lane == 0) asm volatile("global_store_dwordx2 %0, %1, %2 sc1" : : "v"(O_SC + 16u * dX), "v"((u32x2){T, cs}), "s"(ws) : "memory");
      }
      TS(0, 6);
    }
    if (!isx) __builtin_amdgcn_s_setprio(PPRIO);
    asm volatile("" : "+v"(tid2));
    if (isx) {
      const int lane = tid2 & 63;
      const u32x4 ra = *(const u32x4*)(smem + X_RW(wl) + 16 * lane), rb = *(const u32x4*)(smem + X_RW(wl) + 1024 + 16 * lane), rw = *(const u32x4*)(smem + X_WR + 16 * lane);
      const u32x2 rc = *(const u32x2*)(smem + X_RW(wl) + 2048 + 8 * lane);
#pragma unroll
      for (int k = 0; k < 4; ++k) { xv[k] = ra[k]; xv[4 + k] = rb[k]; wv[k] = rw[k]; }
      xv[8] = rc[0]; wv[4] = rc[1];
    } else rows_xw(tid2 & 63);
    if (!isx && !ffn) {
      SLEEP(RSLEEP);
#pragma unroll
      for (int k = 0; k < 7; ++k) ST16(O_SL + (uint32_t)((6 * bx + bj - 18) * 1792 + 256 * k - 256 + tid2) * 16u, ones);
      DRAIN();
    }
    if (!isx) BAR();
    const uint32_t tp = (uint32_t)__builtin_amdgcn_readfirstlane(cnt[4]);
    if (!isx) T = (uint32_t)__builtin_amdgcn_readfirstlane(sel[43]);
    tv = tp + UD + VD2 - vearly;
  } else {
    __builtin_amdgcn_s_setprio(3);
    if (tid < 12) cnt[tid] = 0u;
    BAR();
    if (tid < 20) ((uint32_t*)(hm + 320))[tid] = 0u;
    if (tid >= 184) *(u32x4*)(smem + L_SC - 16 * 184 + 16 * tid) = (u32x4){0x41804180u, 0x41804180u, 0x41804180u, 0x41804180u};
    uint32_t sink = 0;
    ln1(tg, 0, ca + 12);
    TS(0, 2);
    __builtin_amdgcn_s_setprio(NPRIO);
    asm volatile("s_waitcnt vmcnt(0)" : "+v"(sink) : : "memory");
    TS(0, 13);
    if (w == 0) {
      const uint32_t xr = O_XC + (uint32_t)((511 - 2 * (g0 + w)) * 144 + lane) * 16u;
      ST16(xr, ones);
      ST16(xr + 1024u, ones);
      if (lane < 16) ST16(xr + 2048u, ones);
    }
    if (w == 2 && b < 72 && lane < 8) ST16(O_FB + (uint32_t)(8 * b + lane) * 16u, ones);
    if (isx) {
      rows_xw(lane);
      char* sp = smem + X_RW(w) + 16 * lane;
      *(u32x4*)sp = (u32x4){xv[0], xv[1], xv[2], xv[3]};
      *(u32x4*)(sp + 1024) = (u32x4){xv[4], xv[5], xv[6], xv[7]};
      *(u32x2*)(smem + X_RW(w) + 2048 + 8 * lane) = (u32x2){xv[8], wv[4]};
      if (w == 0) *(u32x4*)(smem + X_WR + 16 * lane) = (u32x4){wv[0], wv[1], wv[2], wv[3]};
    }
    SLEEP(ESLEEP);
    uint32_t tu = latest(gate(ws, O_SU + (uint32_t)(256 * bx + 64 * w + 4 * i + (bj & 3)) * 128u, T));
    asm volatile("s_mov_b64 exec, 1\n\tds_max_u32 %0, %1\n\ts_mov_b64 exec, -1" : : "v"(ca + 16), "v"(tu) : "memory");
    if (w == 0) sel[43] = T;
    if (isx) {
      const uint32_t xo = u_at(tid, dX);
      char* sm = smem + X_UL + C_U - 16 + 16 * tid;
      const u32x4 ut = {utag(T, 0), utag(T, 1), utag(T, 2), utag(T, 3)};
      __builtin_amdgcn_s_setprio(3);
      until(tu + UD + XD);
      u32x4 uw;
      asm volatile("global_load_dwordx4 %0, %1, %2 sc1" : "=&v"(uw) : "v"(xo), "s"(ws) : "memory");
      asm volatile("s_waitcnt vmcnt(0)" : "+v"(uw));
      uw ^= ut;
      u_again(uw, ut, xo);
      *(u32x4*)(sm + 16) = uw;
      BAR();
      if (w < 3) {
        const u32x4 pw = *(const u32x4*)sm;
        if (w < 2) sums<true>(smem + X_IM + 16 * tid, pw, uw); else sums<false>(smem + X_IM + 16 * tid, pw, uw);
        count_up(ca + 28);
      }
    } else {
      until(tu + UD + WD);
      BAR();
    }
    tu = (uint32_t)__builtin_amdgcn_readfirstlane(cnt[4]);
    TS(0, 3);
    __builtin_amdgcn_s_setprio(DPRIO);
    if (ffn) {
      ts1 = (tu + UD + VD2 - TSTOP) | 1u;
      gu_dma(0, N0, IC<40>{});
      gu_dma(N0, NA, IC<WDEPTH>{});
      asm volatile("s_waitcnt vmcnt(%0)" : : "n"(NA - 9) : "memory");
      gsync0(ca + 4, 4);
      gu_read(0);
      asm volatile("s_waitcnt lgkmcnt(0)" ::: "memory");
      count_up(ca + 36);
      TS(0, 4);
      gu_dma(NA, 18, IC<WDEPTH2>{});
      gwait(ca + 36, 4);
      down_dma(0, KN, lane);
      asm volatile("s_waitcnt vmcnt(%0)" : : "n"(3 * KN) : "memory");
      count_up(ca + 4);
      TS(0, 7);
    } else {
      if (!isx && C3) {
        const int n = 6 * min(tid, 191) + bj - 18, r = n >> 2;
        const char* wb = (const char*)((n & 1) ? wu : wg) + bx * 1152;
        const int c0 = (3 - (int)((uintptr_t)wb >> 7)) & 7;
        wb += (uint32_t)((r + 144 * ((r >= 144) + ((n >> 1) & 1))) * (F_ * 2) + 128 * c0);
        TOUCH(wb);
        if (c0 == 0) TOUCH(wb + 1024);
      }
      auto d_line = [&](int k) {
        const int n = min((k * 14 + bj - 18) * 64 + lane, 5183), q = n / 1296, r = n - 1296 * q;
        return (const char*)wd + (uint32_t)(bx * 5184 + (r / 18) * 72 + 18 * (HREV ? 3 - q : q) + r % 18) * 128u;
      };
      int k = w;
      if (w < HW) {
#pragma unroll 1
        for (; k < 6 && (int)((uint32_t)__builtin_amdgcn_s_memrealtime() - (tu + UD + VD2 - HSTOP)) < 0; k += HW) { TOUCH(d_line(k)); DRAIN(); }
      }
      TS(0, 4);
      until(tu + UD + VD2 + RD2);
      if (RW) gwait(ca, 4);
      if (w < HW) {
#pragma unroll 1
        for (; k < 6; k += HW) { TOUCH(d_line(k)); DRAIN(); }
      }
      TS(0, 7);
    }
    tv = tu + UD + VD2 - vearly;
    tk = tu + UD + VD2 + KD;
  }
  {
  const int tid = tid2, lane = tid & 63, i = lane & 15, kq = lane >> 4;
  __builtin_amdgcn_s_setprio(3);
  const int PL = ffn ? 6400 : LW + 6656;
  f32x2 xf[9];
  f32x4 za = {0.f, 0.f, 0.f, 0.f}, zb = za;
  uint32_t al = O_XC + 64u * (i >> 3) + 16u * kq + 128u * w;
  uint32_t ha = (ROLE == 1) ? (uint32_t)((4 * (kq & 1) + 2 * (kq >> 1) + (i >> 3)) * 80 + 16 * w + 2 * (i & 7)) : 0u;
  uint32_t h0a = (uint32_t)(min(i, 8) * 80 + 16 * kq), h1a = (uint32_t)(min(i ^ 8, 8) * 80 + 16 * kq);
  uint32_t so = O_SL + (uint32_t)fb * SP + ((ROLE == 2) ? 16u * tid - 4096u : 8192u + 16u * lane);
  uint32_t da = lb0 + (uint32_t)((8 * kq + ((lane >> 2) & 3)) * 1152 + 32 * ((8 * kq + ((lane >> 2) & 3) + 2) >> 2) + 8 * (lane & 3) + ((ROLE == 2) ? 128 * wl : 1024));
  if (ROLE == 2) asm volatile("" : "+v"(h0a), "+v"(h1a), "+v"(so), "+v"(da)); else asm volatile("" : "+v"(ha), "+v"(al), "+v"(da));
  uint32_t hv[9] = {}, nv[9] = {}, keya = 0u, keyb = 0u;
  int rka = 0, rkb = 0;
  auto put_row = [&](const uint32_t* r, int j, const void* base, uint32_t ro) {
    const u32x4 tl = {r[8], (uint32_t)DPP_I((int)r[8], 0x55, 0xf), (uint32_t)DPP_I((int)r[8], 0xaa, 0xf), (uint32_t)DPP_I((int)r[8], 0xff, 0xf)};
    asm volatile("global_store_dwordx4 %0, %1, %2 sc1\n\ts_nop 1" : : "v"(ro + 16u * lane), "v"((u32x4){r[4 * j], r[4 * j + 1], r[4 * j + 2], r[4 * j + 3]}), "s"(base) : "memory");
    if ((lane & 35) == 32 * j) asm volatile("global_store_dwordx4 %0, %1, %2 offset:1024 sc1\n\ts_nop 1" : : "v"(ro + 4u * (lane & 31)), "v"(tl), "s"(base) : "memory");
  };
  if (ln2) {
    const int po = PL - 2560 * (ROLE == 1);
    uint32_t pa = (uint32_t)(po + 9216 * wl + 32 * lane), pb = (uint32_t)(po + 9216 * wl + 2048 + 8 * (lane & 31)), ps = (lane < 32) ? 0u : 16u;
    uint32_t kid = (uint32_t)(((255 - g2) << 5) | (7 - 2 * wl - (lane >> 5)));
    const int tl = tid - 256, t2 = tl + 128, d1 = 64 * (tl / 48) + tl % 48, d3 = 64 * (t2 / 48) + t2 % 48;
    const int d2 = (tl < 128) ? 64 * (tl >> 4) + 48 + (tl & 15) : min(384 + tl, 575);
    uint32_t vo = v_at(d1, g0), vo2 = v_at(d2, g0), vo3 = v_at(d3, g0), px = (uint32_t)(PL + 4 * d1), py = (uint32_t)(PL + 4 * d2), pz = (uint32_t)(PL + 4 * d3);
#pragma unroll
    for (int k = 0; k < 9; ++k) xf[k] = (f32x2){bflo(xv[k]), bfhi(xv[k])};
    asm volatile("" : "+v"(xf[0]), "+v"(xf[1]), "+v"(xf[2]), "+v"(xf[3]), "+v"(xf[4]), "+v"(xf[5]), "+v"(xf[6]), "+v"(xf[7]), "+v"(xf[8]));
    asm volatile("" : "+v"(za), "+v"(zb), "+v"(pa), "+v"(pb), "+v"(ps), "+v"(kid), "+v"(vo), "+v"(vo2), "+v"(vo3), "+v"(px), "+v"(py), "+v"(pz));
    u32x4 ut = {utag(T, 0), utag(T, 1), utag(T, 2), utag(T, 3)};
    float sv[10], ws_ = dot2(wv[4], 0x3f803f80u, 0.f) * 0.5f;
#pragma unroll
    for (int k = 0; k < 4; ++k) ws_ = dot2(wv[k], 0x3f803f80u, ws_);
    ws_ = wave_sum(ws_);
#pragma unroll
    for (int k = 0; k < 10; ++k) sv[k] = 16.f;
    uint32_t ka_ = lb0 + LM + 4u * (128 + 48 + 2 * wl + (lane >> 5)), kb_ = lb0 + LM + 4u * (128 + 48);
    uint32_t ko_ = O_KEY + (uint32_t)(256 * (lane & 7) + 8 * (g2 & 31) + (g2 >> 5)) * 16u;
    asm volatile("" : "+v"(ut), "+v"(ws_), "+v"(sv[0]), "+v"(sv[1]), "+v"(sv[2]), "+v"(sv[3]), "+v"(sv[4]), "+v"(sv[5]), "+v"(sv[6]), "+v"(sv[7]), "+v"(sv[8]), "+v"(sv[9]),
                 "+v"(ka_), "+v"(kb_), "+v"(ko_));
    if (ffn) gwait(ca + 36, 4);
    TS(0, 7);
    until(tv);
    TS(0, 8);
    u32x4 va, vc = ut, vb = ut;
    asm volatile("global_load_dwordx4 %0, %2, %4 sc1\n\ts_cmp_lt_u32 %5, 2\n\ts_cbranch_scc1 1f\n\tglobal_load_dwordx4 %1, %3, %4 sc1\n1:"
                 : "=&v"(va), "+v"(vc) : "v"(vo), "v"(vo3), "s"(ws), "s"(wl) : "memory", "scc");
    until(tv + V2D);
    asm volatile("s_cmp_eq_u32 %5, 3\n\ts_cbranch_scc1 1f\n\tglobal_load_dwordx4 %2, %3, %4 sc1\n\ts_waitcnt vmcnt(1)\n\ts_branch 2f\n1:\n\ts_waitcnt vmcnt(0)\n2:"
                 : "+v"(va), "+v"(vc), "+v"(vb) : "v"(vo2), "s"(ws), "s"(wl) : "memory", "scc");
    auto turn1 = [&] {
#pragma unroll
      for (int e = 0; e < 4; ++e) *(uint32_t*)(smem + px + 9216 * e) = va[e];
      if (wl >= 2) {
#pragma unroll
        for (int e = 0; e < 4; ++e) *(uint32_t*)(smem + pz + 9216 * e) = vc[e];
      }
    };
    auto turn = [&] {
      if (wl < 3) {
#pragma unroll
        for (int e = 0; e < 4; ++e) *(uint32_t*)(smem + py + 9216 * e) = vb[e];
      }
    };
    va ^= ut; vc ^= ut;
    turn1();
    asm volatile("s_waitcnt vmcnt(0)" : "+v"(vb) : : "memory");
    vb ^= ut;
    turn();
    const u32x4 vs = va | vb | vc;
    if (__builtin_expect(__any(((vs[0] | vs[1] | vs[2] | vs[3]) & UMASK) != 0u), 0)) {
#pragma unroll 1
      for (int spin = 0; spin < 4; ++spin) {
        const bool na = ((va[0] | va[1] | va[2] | va[3]) & UMASK) != 0u, nb = ((vb[0] | vb[1] | vb[2] | vb[3]) & UMASK) != 0u, nc = ((vc[0] | vc[1] | vc[2] | vc[3]) & UMASK) != 0u;
        if (!__any(na || nb || nc)) break;
        TSC(16);
        if (na) LD16(va, vo);
        if (nb) LD16(vb, vo2);
        if (nc) LD16(vc, vo3);
        asm volatile("s_waitcnt vmcnt(0)" : "+v"(va), "+v"(vb), "+v"(vc) : : "memory");
        if (na) va ^= ut;
        if (nb) vb ^= ut;
        if (nc) vc ^= ut;
      }
      auto again = [&](u32x4& v, uint32_t o, int d) {
        const int J = g0 >> 5;
        const uint32_t dn = O_DN + 16u * (uint32_t)((d < 512) ? 4 * (d >> 1) + ((0x01013232 >> (4 * J)) & 3) : 4 * (d - 256) + (J >> 1));
        bool pend = ((v[0] | v[1] | v[2] | v[3]) & UMASK) != 0u;
#pragma unroll 1
        for (int spin = 0; spin < SPIN_CAP && __any(pend); ++spin) {
          TSC(16);
          SLEEP(PSLEEP);
          if (pend) {
            uint32_t fin = ~T;
            if ((v[0] & v[1] & v[2] & v[3] & LMASK) == LMASK)
              asm volatile("global_load_dword %0, %1, %2 sc1\n\ts_waitcnt vmcnt(0)" : "=&v"(fin) : "v"(dn), "s"(ws) : "memory");
            LD16W(v, o);
            v ^= ut;
            pend = ((v[0] | v[1] | v[2] | v[3]) & UMASK) != 0u;
            if (pend && fin == T) {
              u32x2 sc;
              for (int k = 0; k < SPIN_CAP; ++k) {
                asm volatile("global_load_dwordx2 %0, %1, %2 sc1\n\ts_waitcnt vmcnt(0)" : "=&v"(sc) : "v"(O_SC + 16u * d), "s"(ws) : "memory");
                if (sc[0] == T) break;
              }
              *(u16*)(smem + L_SC + 2 * d) = (u16)(sc[1] >> 16);
              cnt[10] = 1u;
              v = (v ^ ut) & ~LMASK;
              pend = false;
            }
          }
        }
      };
      again(va, vo, d1); again(vc, vo3, d3); again(vb, vo2, d2);
      turn1();
      turn();
    }
    u32x4 a0, a1;
    u32x2 a2, cf;
    {
      uint32_t cc, n = SPIN_CAP;
      asm volatile("s_mov_b64 exec, 1\n\tv_mov_b32 v90, 1\n\tds_add_u32 %6, v90\n\ts_mov_b64 exec, -1\n"
                   "1:\n\tds_read2_b32 %3, %6 offset1:10\n\tds_read_b128 %0, %7\n\tds_read_b128 %1, %7 offset:16\n\tds_read_b64 %2, %8\n\t"
                   "s_sub_u32 %5, %5, 1\n\ts_cbranch_scc1 2f\n\ts_waitcnt lgkmcnt(3)\n\tv_readfirstlane_b32 %4, v90\n\t"
                   "s_cmp_lt_u32 %4, 4\n\ts_cbranch_scc1 1b\n2:\n\ts_waitcnt lgkmcnt(0)"
                   : "=&v"(a0), "=&v"(a1), "=&v"(a2), "=&{v[90:91]}"(cf), "=&s"(cc), "+s"(n) : "v"(ca), "v"(lb0 + pa), "v"(lb0 + pb) : "memory", "scc");
    }
    TS(0, 9);
    if (__builtin_expect(__builtin_amdgcn_readfirstlane(cf[1]) != 0, 0)) {
      const u32x4 q = *(const u32x4*)(smem + L_SC + 16 * lane);
      const uint32_t r = *(const uint32_t*)(smem + L_SC + 1024 + 4 * (lane & 31));
#pragma unroll
      for (int k = 0; k < 4; ++k) { sv[2 * k] = bflo(q[k]); sv[2 * k + 1] = bfhi(q[k]); }
      sv[8] = bflo(r); sv[9] = bfhi(r);
    }
    const uint32_t av[10] = {a0[0], a0[1], a0[2], a0[3], a1[0], a1[1], a1[2], a1[3], a2[0] >> ps, a2[1] >> ps};
    f32x2 h[9];
#pragma unroll
    for (int k = 0; k < 10; ++k) {
      asm("v_fma_mix_f32 %0, %1, %2, %3 op_sel_hi:[1,0,0]" : "=v"(h[(k < 8) ? k >> 1 : 8][k & 1]) : "v"(av[k]), "v"(sv[k]), "v"(xf[(k < 8) ? k >> 1 : 8][k & 1]));
      if (k < 8) asm("v_fma_mix_f32 %0, %1, %2, %3 op_sel:[1,0,0] op_sel_hi:[1,0,0]" : "=v"(h[4 + (k >> 1)][k & 1]) : "v"(av[k]), "v"(sv[k]), "v"(xf[4 + (k >> 1)][k & 1]));
    }
#pragma unroll
    for (int k = 0; k < 9; ++k) hv[k] = pack2(h[k][0], h[k][1]);
    const float da = dot2(hv[2], wv[2], dot2(hv[0], wv[0], zb[0])) + dot2(hv[3], wv[3], dot2(hv[1], wv[1], zb[1]));
    const float db = dot2(hv[6], wv[2], dot2(hv[4], wv[0], zb[2])) + dot2(hv[7], wv[3], dot2(hv[5], wv[1], zb[3]));
    float dt = dot2(hv[8], wv[4], fold32(da, db));
    f32x2 rc, nc;
    ln_stats<true>(hv, lane, 1.f, xf, za, rc, nc, dt);
    dt = (lane < 32) ? dt * rc[0] + ws_ * nc[0] : dt * rc[1] + ws_ * nc[1];
    const uint32_t bb = pack2(dt, dt) & 0xffff0000u;
    const uint32_t kk = (bb ^ (((uint32_t)((int)bb >> 31) & 0x7fff0000u) | 0x80000000u)) | kid;
    u32x4 ka, kb;
    uint32_t old;
    asm volatile("s_mov_b64 exec, %5\n\tds_write_b32 %3, %4\n\ts_mov_b64 exec, 1\n\tv_mov_b32 %2, 1\n\tds_add_rtn_u32 %2, %7, %2\n\ts_mov_b64 exec, -1\n\t"
                 "ds_read_b128 %0, %6\n\tds_read_b128 %1, %6 offset:16\n\ts_waitcnt lgkmcnt(0)"
                 : "=&v"(ka), "=&v"(kb), "=&v"(old) : "v"(ka_), "v"(kk), "s"(0x8000000080000000ull), "v"(kb_), "v"(ca) : "memory");
    if (__builtin_amdgcn_readfirstlane(old) == 7) {
      uint32_t m1 = max(max(ka[0], ka[1]), ka[2]), m2 = med3u(ka[0], ka[1], ka[2]), m3 = min(min(ka[0], ka[1]), ka[2]);
#pragma unroll
      for (int e = 0; e < 5; ++e) {
        const uint32_t kx = e ? kb[e - 1] : ka[3];
        m3 = med3u(m2, m3, kx); m2 = med3u(m1, m2, kx); m1 = max(m1, kx);
      }
      if (lane < 8) ST16(ko_, ((u32x4){m1 | 24u, m2 | 16u, m3 | 8u, T & ~1u}));
    }
    TS(0, 10);
    keya = (uint32_t)__builtin_amdgcn_readlane(kk, 31); keyb = (uint32_t)__builtin_amdgcn_readlane(kk, 63);
    ln_apply<false>(xf, lane, rc, nc, nv);
    gwait(ca, 8);
    if (wl == 1) {
      const u32x4 kv = *(const u32x4*)&kl[4 * (lane & 1)];
      if (lane < 2) ST16(O_KF + (uint32_t)(2 * g2 + lane) * 16u, ((u32x4){__builtin_amdgcn_perm(kv[1], kv[0], 0x07060302u), T, __builtin_amdgcn_perm(kv[3], kv[2], 0x07060302u), T}));
      if (ROLE == 2 && b == 0 && lane == 0) ST4(O_EP, T * 0x0E8B2F51u);
    }
    const uint32_t ko = kl[lane & 7];
    rka = FORCEFB + __popcll(__ballot(ko > keya) & 0xffull); rkb = FORCEFB + __popcll(__ballot(ko > keyb) & 0xffull);
#pragma unroll
    for (int k = 0; k < 9; ++k) {
      const f32x2 o = xf[k] + (f32x2){bflo(nv[k]), bfhi(nv[k])};
      hv[k] = pack2(o[0], o[1]);
      nv[k] = min(nv[k], 0xfffffffeu);
    }
    if (rka < 2) put_row(nv, 0, ws, O_XC + (uint32_t)(1023 - 4 * g2 - rka) * 1152u);
    if (rkb < 2) put_row(nv, 1, ws, O_XC + (uint32_t)(1023 - 4 * g2 - rkb) * 1152u);
    if (!LPOS && ffn) down_dma(KN, 4, lane);
  }
  uint32_t k3rd = 0u, k4th = 0u;
  uint32_t q0 = lb0 + LM + 4u * lane, q1 = lb0 + LM + 32u * w, q2 = q1 + 4u * (lane & 7), q3 = lb0 + LM + 512u + 4u * (lane & 7), q4 = lb0 + LM + (uint32_t)(tid & ~7);
  uint32_t r8 = C_, sb = lb0 + LM + 512u;
  asm volatile("" : "+v"(q0), "+v"(q1), "+v"(q2), "+v"(q3), "+v"(q4), "+v"(r8), "+v"(sb));
  if constexpr (ROLE == 1) {
    u32x4 key;
    const uint32_t kof = O_KEY + (uint32_t)(256 * bx + tid) * 16u;
    until(tk);
    if (!ffn) SLEEP(NSLEEP);
    LD16(key, kof);
    if (ffn) {
      gwait0(ca + 4, 8);
      gu_read(1);
    }
    asm volatile("s_waitcnt vmcnt(0)" : "+v"(key) : : "memory");
    if (__builtin_expect(__any(key[3] != (T & ~1u)), 0)) {
#pragma unroll 1
      for (int spin = 0; spin < SPIN_CAP; ++spin) {
        const bool need = key[3] != (T & ~1u);
        if (!__any(need)) break;
        TSC(15);
        SLEEP(KBACK);
        if (need) LD16W(key, kof);
      }
    }
    TS(0, 11);
    const uint32_t m1 = oct_maxu(key[0]);
    const bool hit = key[0] == m1;
    const uint32_t k0 = hit ? key[1] : key[0], k1 = hit ? key[2] : key[1];
    const uint32_t m2 = oct_maxu(k0);
    *LP(u32x2, q4) = (u32x2){m1, m2};
    k3rd = (k0 == m2) ? k1 : k0;
    k4th = (k0 == m2) ? (hit ? 0u : key[2]) : k1;
    if (RPF == 1 && !ffn && w == (bj & 3)) {
      const uint32_t r1 = O_XC + ((m1 >> 3) & 0x3ffu) * 1152u + 128u * (lane & 7), r2 = O_XC + ((m2 >> 3) & 0x3ffu) * 1152u + 128u * (lane & 7);
      asm volatile("global_load_dword v126, %0, %3 sc1\n\tglobal_load_dword v126, %1, %3 sc1\n\tglobal_load_dword v126, %2, %3 offset:1024 sc1"
                   : : "v"(r1), "v"(r2), "v"(((lane & 1) ? r1 : r2) - 128u * (lane & 7)), "s"(ws) : "memory", "v126");
    }
  }
  auto pick = [&](auto co, auto so_) {
    BAR();
    const uint32_t c = *LP(uint32_t, q0 + co.value), own = *LP(uint32_t, q2 + co.value);
    const u32x4 la = *LP(u32x4, q1 + co.value), lb = *LP(u32x4, q1 + co.value + 16);
    uint32_t r = r8;
    asm("v_cmp_gt_u32 s[40:41], %1, %2\n\tv_cmp_gt_u32 s[42:43], %1, %3\n\tv_cmp_gt_u32 s[44:45], %1, %4\n\tv_cmp_gt_u32 s[46:47], %1, %5\n\t"
        "v_cmp_gt_u32 s[48:49], %1, %6\n\tv_cmp_gt_u32 s[50:51], %1, %7\n\tv_cmp_gt_u32 s[52:53], %1, %8\n\tv_cmp_gt_u32 s[54:55], %1, %9\n\t"
        "s_bcnt1_i32_b64 s40, s[40:41]\n\ts_bcnt1_i32_b64 s42, s[42:43]\n\tv_writelane_b32 %0, s40, 0\n\ts_bcnt1_i32_b64 s44, s[44:45]\n\t"
        "v_writelane_b32 %0, s42, 1\n\ts_bcnt1_i32_b64 s46, s[46:47]\n\tv_writelane_b32 %0, s44, 2\n\ts_bcnt1_i32_b64 s48, s[48:49]\n\t"
        "v_writelane_b32 %0, s46, 3\n\ts_bcnt1_i32_b64 s50, s[50:51]\n\tv_writelane_b32 %0, s48, 4\n\ts_bcnt1_i32_b64 s52, s[52:53]\n\t"
        "v_writelane_b32 %0, s50, 5\n\ts_bcnt1_i32_b64 s54, s[54:55]\n\tv_writelane_b32 %0, s52, 6\n\tv_writelane_b32 %0, s54, 7"
        : "+v"(r) : "v"(c), "v"(la[0]), "v"(la[1]), "v"(la[2]), "v"(la[3]), "v"(lb[0]), "v"(lb[1]), "v"(lb[2]), "v"(lb[3])
        : "scc", "s40", "s41", "s42", "s43", "s44", "s45", "s46", "s47", "s48", "s49", "s50", "s51", "s52", "s53", "s54", "s55");
    *LP(uint32_t, sb + 4u * min(r, (uint32_t)C_) + so_.value) = own;
    BAR();
  };
  auto rows = [&](uint32_t ao, auto&& meanwhile) {
    u32x4 aa, ab, ac = __builtin_bit_cast(u32x4, zb);
    asm volatile("global_load_dwordx4 %0, %3, %4 sc1\n\tglobal_load_dwordx4 %1, %3, %4 offset:512 sc1\n\t"
                 "s_cmp_lg_u32 %5, 0\n\ts_cbranch_scc1 1f\n\tglobal_load_dwordx4 %2, %3, %4 offset:1024 sc1\n1:"
                 : "=&v"(aa), "=&v"(ab), "+v"(ac) : "v"(ao), "s"(ws), "s"(w) : "memory", "scc");
    __builtin_amdgcn_sched_barrier(0);
    meanwhile();
    asm volatile("s_waitcnt vmcnt(0)" : "+v"(aa), "+v"(ab), "+v"(ac) : : "memory");
    if (__builtin_expect(__any(max(max(max4(aa), max4(ab)), max4(ac)) == 0xffffffffu), 0)) {
#pragma unroll 1
      for (int spin = 0; spin < SPIN_CAP; ++spin) {
        const bool na = max4(aa) == 0xffffffffu, nb = max4(ab) == 0xffffffffu, nc = max4(ac) == 0xffffffffu;
        if (!__any(na || nb || nc)) break;
        TSC(14);
        SLEEP(PSLEEP);
        if (na) LD16W(aa, ao);
        if (nb) LD16W(ab, ao + 512u);
        if (nc) LD16W(ac, ao + 1024u);
      }
    }
    *(u32x4*)(smem + LA + 16 * tid) = aa;
    *(u32x4*)(smem + LA + 4096 + 16 * tid) = ab;
    if (w == 0) *(u32x4*)(smem + LA + 8192 + 16 * tid) = ac;
  };
  auto rows_dma = [&](uint32_t ao) {
    uint32_t dl = lb0 + LA + 1024u * w, keep;
    asm volatile("s_mov_b32 %0, m0\n\ts_mov_b32 m0, %1\n\ts_add_u32 %1, %1, 4096 - 512\n\tglobal_load_lds_dwordx4 %2, %3 sc1\n\ts_mov_b32 m0, %1\n\ts_add_u32 %1, %1, 4096 - 512\n\t"
                 "global_load_lds_dwordx4 %2, %3 offset:512 sc1\n\ts_cmp_lg_u32 %4, 0\n\ts_cbranch_scc1 1f\n\ts_mov_b32 m0, %1\n\ts_nop 0\n\t"
                 "global_load_lds_dwordx4 %2, %3 offset:1024 sc1\n1:\n\ts_mov_b32 m0, %0" : "=&s"(keep), "+s"(dl) : "v"(ao), "s"(ws), "s"(w) : "memory", "scc");
  };
  auto get_bd = [&](int t0, int t1) {
#pragma unroll
    for (int t = t0; t < t1; ++t) {
      const u32x2 h0 = trd(da + 512 * (t >> 2) + 32 * (t & 3)), h1 = trd(da + 512 * (t >> 2) + 32 * (t & 3) + 4608 + 32);
      bw[t] = (u32x4){h0[0], h0[1], h1[0], h1[1]};
    }
#pragma unroll
    for (int t = t0; t < t1; ++t) asm volatile("" : "+v"(bw[t]));
  };
  pick(IC<0>{}, IC<0>{});
  uint32_t mykey = *LP(uint32_t, q3), ro = ((mykey >> 3) & 0x3ffu) * 1152u;
  TS(0, 12);
  auto check = [&] {
    cnt[(k3rd > (uint32_t)__builtin_amdgcn_readlane(mykey, 7)) ? 5 : 6] = 1u;
    const uint32_t m3 = oct_maxu(k3rd);
    if (k3rd == m3) k3rd = k4th;
    if ((lane & 7) == 0) cand[72 + (tid >> 3)] = m3;
    if (w == 0 && lane < 32) cand[(lane < 8) ? 64 + lane : 96 + lane] = (lane < 8) ? mykey : 0u;
  };
  if constexpr (ROLE == 1) {
    if (ffn && !FORCEFB) rows_dma(ro + al);
    check();
    DRAIN();
  } else if (ffn) {
    if (LPOS) { SLEEP(LSLEEP); down_dma(KN, 4, lane); } else { DRAIN(); gsync0(ca, 12); get_bd(0, 8); }
  }
  BAR();
  TS(0, 5);
  auto put_out = [&](int j, uint32_t k0, int rk) {
    const uint64_t mine = __ballot(((mykey ^ k0) & ~24u) == 0u) & 0xffull;
    if (mine == 0ull) put_row(hv, j, out, (uint32_t)((8 * g2 + 2 * wl + j) * (D_ * 2)));
    else if (rk >= 2) put_row(nv, j, ws, O_FB + 1152u * (uint32_t)__builtin_ctzll(mine));
  };
  auto rest_out = [&] { put_out(0, keya, rka); put_out(1, keyb, rkb); };
  f32x4 lo, hi;
  auto gate_up = [&] {
    u32x4 av[5];
    const uint32_t aa = lb0 + LA + 16u * lane;
#pragma unroll
    for (int j = 0; j < 5; ++j) asm volatile("ds_read_b128 %0, %1 offset:%2" : "=v"(av[j]) : "v"(aa), "n"(1024 * j) : "memory");
    lo = (f32x4){0.f, 0.f, 0.f, 0.f}; hi = lo;
#pragma unroll
    for (int j = 0; j < 9; ++j) {
      if (j < 5) asm volatile("s_waitcnt lgkmcnt(4)" : "+v"(av[j % 5]));
      else if (j == 5) asm volatile("s_waitcnt lgkmcnt(2)" : "+v"(av[0]), "+v"(av[1]));
      else if (j == 7) asm volatile("s_waitcnt lgkmcnt(0)" : "+v"(av[2]), "+v"(av[3]));
      lo = MMB(av[j % 5], bw[2 * j], lo);
      hi = MMB(av[j % 5], bw[2 * j + 1], hi);
      __builtin_amdgcn_sched_barrier(0);
      if (j < 4) asm volatile("ds_read_b128 %0, %1 offset:%2" : "=v"(av[j]) : "v"(aa), "n"(1024 * (j + 5)) : "memory");
    }
  };
  uint32_t fl;
  asm volatile("ds_read_b32 %0, %1" : "=v"(fl) : "v"(ca + 20) : "memory");
  if (ROLE == 1) gate_up();
  asm volatile("s_waitcnt lgkmcnt(0)" : "+v"(fl));
  const bool slow = FORCESLOW || FORCEFB || __builtin_amdgcn_readfirstlane(fl) != 0;
  if (__builtin_expect(slow, 0)) {
    TSC(17);
    pick(IC<256>{}, IC<64>{});
    mykey = sel[16 + (lane & 7)];
    ro = ((mykey >> 3) & 0x3ffu) * 1152u;
    if (ROLE == 1 && (k3rd > (uint32_t)__builtin_amdgcn_readlane(mykey, 7) || (mykey & 24u) == 8u)) cnt[11] = 1u;
    BAR();
    if (FORCESLOW > 1 || FORCEFB || __builtin_amdgcn_readfirstlane(cnt[11]) != 0) {
    u32x4 key;
#pragma unroll 1
    for (int spin = 0; spin < SPIN_CAP; ++spin) {
      LD16W(key, O_KF + 16u * tid);
      if (!__any(key[1] != T || key[3] != T)) break;
      SLEEP(KBACK);
    }
    const uint32_t ib = (uint32_t)(((255 - (tid >> 1)) << 5) | (7 - 4 * (tid & 1)));
    uint32_t k0 = (key[0] << 16) | ib, k1 = (key[0] & 0xffff0000u) | (ib - 1), k2 = (key[2] << 16) | (ib - 2), k3 = (key[2] & 0xffff0000u) | (ib - 3);
    CEX(k0, k1) CEX(k2, k3) CEX(k0, k2) CEX(k1, k3) CEX(k1, k2)
#pragma unroll 1
    for (int c = 0; c < C_; ++c) {
      const uint32_t mx = wave_maxu(k0);
      const bool hit = (k0 == mx);
      k0 = hit ? k1 : k0; k1 = hit ? k2 : k1; k2 = hit ? k3 : k2; k3 = hit ? 0u : k3;
      if (lane == 0) cand[64 + 8 * w + c] = mx;
    }
    pick(IC<256>{}, IC<128>{});
    mykey = sel[32 + (lane & 7)];
    const uint32_t okey = sel[32 + (lane >> 3)];
    const uint64_t same = __ballot(((mykey ^ okey) & 0x1fe0u) == 0u && okey > mykey);
    const int sl = FORCEFB + __popcll((same >> (lane & 7)) & 0x0101010101010101ull);
    ro = (sl < 2) ? (((mykey >> 3) & 0x3ffu) + 3 - sl) * 1152u : O_FB - O_XC + 1152u * (lane & 7);
    }
    if (ln2) rest_out();
    if (ffn) {
      if (ROLE == 1) rows(ro + al, [] {});
      BAR();
      if (ROLE == 1) gate_up();
    }
  }
  if (OPOS == 0 && ROLE == 2 && ffn && !slow) { SLEEP(OD); rest_out(); }
  if (!ffn) {
    if (ln2 && !slow) { SLEEP(OD2); rest_out(); }
    TSE();
    return;
  }
  uint32_t fx = 0, fv = 0, fn = 0, fo = 0, sa[9];
  float rs, hx, xs;
  u32x4 se;
  if constexpr (ROLE == 1) {
    f32x4 lh = (lane < 32) ? lo : hi;
    float fa = fold32(lh[0], lh[2]), fb2 = fold32(lh[1], lh[3]);
    if (__builtin_expect(__any(__builtin_isunordered(fa, fb2)) && !slow, 0)) {
      gsync0(ca + 4, 12);
      rows(ro + al, [] {});
      gsync0(ca + 4, 16);
      gate_up();
      lh = (lane < 32) ? lo : hi;
      fa = fold32(lh[0], lh[2]); fb2 = fold32(lh[1], lh[3]);
    }
    const float g = __int_as_float(__builtin_amdgcn_update_dpp(__float_as_int(fa), __float_as_int(fb2), 0x128, 0xf, 0xc, false));
    const float u = __int_as_float(__builtin_amdgcn_update_dpp(__float_as_int(fb2), __float_as_int(fa), 0x128, 0xf, 0x3, false));
    const uint32_t gu = pack2(g, u);
    const float gp = bflo(gu);
    const float sg = rbf(gp * __builtin_amdgcn_rcpf(1.f + __expf(-gp)));
    *(u16*)((char*)hm + ha) = f2bf(sg * bfhi(gu));
  } else if (LPOS) {
    DRAIN();
    gsync0(ca, 12);
    get_bd(0, 8);
  }
  BAR();
  TS(0, 6);
  if (ROLE == 2 || w == 2) {
    const u32x4 a0 = *(const u32x4*)((char*)hm + h0a), a1 = *(const u32x4*)((char*)hm + h1a);
    if (ROLE == 1) get_bd(0, 4);
    const f32x4 z = {0.f, 0.f, 0.f, 0.f};
    f32x4 o[4];
#pragma unroll
    for (int t = 0; t < 2 * ROLE; ++t) o[t] = MMB(a1, bw[2 * t + 1], MMB(a0, bw[2 * t], z));
#pragma unroll
    for (int t = 0; t < ROLE; ++t) {
      u32x4 sv;
#pragma unroll
      for (int r = 0; r < 4; ++r) sv[r] = min(pack2(o[2 * t][r], o[2 * t + 1][r]), 0xfffffffeu);
      ST16(so + 4096u * t, sv);
    }
    TS(0, 13);
  }
  if (OPOS == 1 && ROLE == 2) {
    SLEEP(OSLEEP);
    if (!slow) rest_out();
  }
  if (ROLE == 1 && w == 1) {
    const int q = (fb & 15) >> 2, ts = 4 * (q & 1) + ((i & 7) >> 1);
    const int dd = 64 * (fb >> 4) + 16 * (2 * (i & 1) + (q >> 1)) + 4 * (fb & 3) + 2 * (kq & 1) + (kq >> 1);
    const uint32_t fkey = (uint32_t)__builtin_amdgcn_ds_bpermute(4 * ts, (int)mykey), fr = (uint32_t)__builtin_amdgcn_ds_bpermute(4 * ts, (int)ro);
    const int ft = 2047 - (int)(((fkey >> 2) & 0x7f8u) | (fkey & 7u));
    fo = (uint32_t)(ft * D_ + dd) * 2u;
    u32x2 fs;
    asm volatile("global_load_ushort %0, %4, %8\n\tglobal_load_ushort %1, %5, %9 sc1\n\tglobal_load_ushort %2, %6, %9 sc1\n\tglobal_load_dwordx2 %3, %7, %9 sc1"
                 : "=&v"(fx), "=&v"(fv), "=&v"(fn), "=&v"(fs) : "v"(fo), "v"(v_at(dd, ft >> 3) + 2u * (ft & 7)), "v"(O_XC + fr + 2u * dd), "v"(O_SC + 16u * dd), "s"(x), "s"(ws) : "memory");
    uint32_t t16 = (((T >> (3 * ((ft >> 1) & 3))) & LMASK) | (((T >> (12 + ((ft >> 1) & 3))) & 0x00010001u) << 14)) >> (16 * (ft & 1)) & 0xffffu;
    const uint32_t o = fkey >> 16;
    const float rs0 = bf2f((u16)((o & 0x8000u) ? (o & 0x7fffu) : (~o & 0xffffu)));
    rs = rbf(__builtin_amdgcn_rcpf(1.f + __expf(-rs0)));
    const uint32_t one = (i < 8) ? (0x3f80u << (16 * (i & 1))) : 0u;
    se = (u32x4){(i >> 1) == 0 ? one : 0u, (i >> 1) == 1 ? one : 0u, (i >> 1) == 2 ? one : 0u, (i >> 1) == 3 ? one : 0u};
#pragma unroll
    for (int m = 0; m < 9; ++m) sa[m] = O_SL + (uint32_t)(16 * m + (lane >> 2)) * SP + (uint32_t)(4 * fb + (lane & 3)) * 16u;
    asm volatile("" : "+v"(t16), "+v"(rs), "+v"(se), "+v"(sa[0]), "+v"(sa[1]), "+v"(sa[2]), "+v"(sa[3]), "+v"(sa[4]), "+v"(sa[5]), "+v"(sa[6]), "+v"(sa[7]), "+v"(sa[8]));
    SLEEP(SSLEEP);
    TS(0, 1);
    float sum;
#pragma unroll 1
    for (int spin = 0; spin < SPIN_CAP; ++spin) {
      u32x4 sv[9];
#pragma unroll
      for (int m = 0; m < 9; ++m) LD16(sv[m], sa[m]);
      asm volatile("s_waitcnt vmcnt(9)" : "+v"(fx), "+v"(fv), "+v"(fn), "+v"(fs));
      if (__builtin_expect(__any(fs[0] != T), 0)) {
        for (int k = 0; k < SPIN_CAP && fs[0] != T; ++k)
          asm volatile("global_load_dwordx2 %0, %1, %2 sc1\n\ts_waitcnt vmcnt(0)" : "=&v"(fs) : "v"(O_SC + 16u * dd), "s"(ws) : "memory");
      }
      const uint32_t f1 = fv ^ t16, f2 = (f1 & 7u) ? fv & ~7u : f1;
      hx = rbf(bf2f((u16)fx) + (float)__builtin_bit_cast(_Float16, (u16)f2) * __uint_as_float(fs[1]));
      xs = bf2f((u16)fn);
      asm volatile("" : "+v"(hx), "+v"(xs));
      f32x4 dv = {0.f, 0.f, 0.f, 0.f};
      static_for<0, 9>([&](auto mm) {
        constexpr int m = mm.value;
        asm volatile("s_waitcnt vmcnt(%1)" : "+v"(sv[m]) : "n"(8 - m));
        dv = MMB(sv[m], se, dv);
        __builtin_amdgcn_sched_barrier(0);
      });
      sum = fold16(fold32(dv[0], dv[1]), fold32(dv[2], dv[3]));
      if (__builtin_expect(!__any(sum != sum), 1)) break;
      uint32_t mx = 0u;
#pragma unroll
      for (int m = 0; m < 9; ++m) mx = max(mx, max4(sv[m]));
      if (!__any(mx == 0xffffffffu)) break;
      TSC(18);
      SLEEP(PSLEEP);
    }
    const float o1 = rbf(xs + rbf(rbf(sum) * rs));
    if (i < 8) asm volatile("global_store_short %0, %1, %2 sc1" : : "v"(fo), "v"((uint32_t)f2bf(hx + o1)), "s"(out) : "memory");
    TS(0, 2);
  }
  TSE();
  }
  }
}
#define BROT "192"
#define CPF_A "s_getpc_b64 s[96:97]\n\ts_lshl_b32 s99, s2, 4\n\ts_lshl_b32 s84, s98, 7\n\ts_add_u32 s99, s99, s84\n\tv_and_b32 v112, 1, v0\n\t" \
              "v_lshl_add_u32 v112, v112, 12, s99\n\tv_min_u32 v112, kf_end-kf_all-0x80, v112\n\ts_mov_b64 exec, 3\n\tglobal_load_dword v112, v112, s[96:97]\n\ts_mov_b64 exec, -1\n\t"
extern "C" __global__ __launch_bounds__(512, 4) void kf_all(KARGS) {
  __shared__ __attribute__((aligned(16))) char smem[SMEM];
  asm volatile("s_load_dwordx16 s[64:79], s[0:1], 0x0\n\ts_load_dwordx4 s[80:83], s[0:1], 0x40\n\tv_readfirstlane_b32 s98, v0\n\t"
               "s_add_u32 s84, s2, " BROT "\n\ts_xor_b32 s84, s84, s2\n\ts_bitset0_b32 s84, 8\n\ts_bitset0_b32 s84, 9\n\ts_xor_b32 s2, s2, s84\n\t"
               "v_mov_b32 v109, 0\n\ts_and_b32 s85, s2, 7\n\ts_bitcmp1_b32 s2, 8\n\ts_cbranch_scc1 1f\n\t" CPF_A "s_bitcmp1_b32 s98, 8\n\ts_cbranch_scc1 2f\n\t"
               "s_andn2_b32 s84, s2, 31\n\ts_lshl2_add_u32 s84, s85, s84\n\ts_bfe_u32 s85, s2, 0x20003\n\ts_or_b32 s84, s84, s85\n\t"
               "s_mulk_i32 s84, 0x2400\n\ts_mulk_i32 s98, 36\n\ts_add_u32 s84, s84, s98\n\tv_and_b32 v110, 63, v0\n\tv_and_b32 v111, 32, v0\n\t"
               "v_lshlrev_b32 v111, 5, v111\n\tv_lshl_add_u32 v111, v110, 2, v111\n\tv_lshl_add_u32 v110, v110, 4, s84\n\tv_add_u32 v111, s84, v111\n\t"
               "s_cmpk_lt_u32 s2, 0x90\n\ts_waitcnt lgkmcnt(0)\n\t"
               "global_load_dwordx4 v[100:103], v110, s[64:65]\n\tglobal_load_dwordx4 v[104:107], v110, s[64:65] offset:1152\n\t"
               "global_load_dword v108, v111, s[64:65] offset:1024\n\tglobal_load_dword v109, v109, s[82:83] sc1\n\t"
               "s_cbranch_scc1 kf_n\n\ts_branch kf_h\n"
               "1:\n\ts_bfe_u32 s84, s2, 0x50003\n\ts_lshl_b32 s85, s85, 5\n\ts_or_b32 s84, s84, s85\n\ts_lshl_b32 s84, s84, 2\n\t"
               "v_and_b32 v110, 0xff, v0\n\tv_bfe_u32 v107, v0, 7, 1\n"
               "4:\n\ts_lshl_b32 s85, s84, 7\n\tv_lshl_add_u32 v110, v110, 1, s85\n\tv_lshl_add_u32 v107, v107, 1, s84\n\ts_and_b32 s85, s98, s2\n\ts_waitcnt lgkmcnt(0)\n\t"
               "global_load_ushort v100, v107, s[78:79]\n\tglobal_load_ushort v101, v110, s[66:67]\n\tglobal_load_ushort v102, v110, s[68:69]\n\t"
               "global_load_dword v109, v109, s[82:83] sc1\n\ts_bitcmp1_b32 s85, 8\n\ts_cbranch_scc1 kf_u\n\t"
               "global_load_ushort v103, v110, s[70:71]\n\tglobal_load_ushort v104, v110, s[72:73]\n\tglobal_load_ushort v105, v110, s[74:75]\n\t"
               "global_load_ushort v106, v110, s[76:77]\n\tglobal_load_ushort v107, v107, s[80:81]\n\ts_bitcmp1_b32 s2, 8\n\ts_cbranch_scc1 kf_c\n"
               "6:\n\ts_barrier\n\ts_branch kf_p\n"
               "2:\n\ts_cmpk_lt_u32 s2, 0xc0\n\ts_cbranch_scc1 6b\n\t"
               "s_lshr_b32 s84, s2, 3\n\ts_lshl3_add_u32 s84, s85, s84\n\ts_lshl1_add_u32 s84, s84, 976\n\t"
               "v_and_b32 v110, 0x7f, v0\n\tv_mov_b32 v107, 0\n\ts_branch 4b"
               : : "s"(__builtin_amdgcn_kernarg_segment_ptr()), "s"(blockIdx.x), "v"(threadIdx.x),
                   "s"((uint32_t)(uintptr_t)(__attribute__((address_space(3))) char*)smem) : "scc", "s96", "s97", "s98", "s99", "v127");
}
extern "C" __global__ __launch_bounds__(512, 4) void kf_c(KARGS) {
  __shared__ __attribute__((aligned(16))) char smem[SMEM];
  role_body<0>(smem, KPASS);
}
extern "C" __global__ __launch_bounds__(512, 4) void kf_u(KARGS) {
  __shared__ __attribute__((aligned(16))) char smem[SMEM];
  role_body<3>(smem, KPASS);
}
extern "C" __global__ __launch_bounds__(512, 4) void kf_n(KARGS) {
  __shared__ __attribute__((aligned(16))) char smem[SMEM];
  role_body<1, 1>(smem, KPASS);
}
extern "C" __global__ __launch_bounds__(512, 4) void kf_h(KARGS) {
  __shared__ __attribute__((aligned(16))) char smem[SMEM];
  role_body<1, 0>(smem, KPASS);
}
extern "C" __global__ __launch_bounds__(512, 4) void kf_p(KARGS) {
  __shared__ __attribute__((aligned(16))) char smem[SMEM];
  role_body<2>(smem, KPASS);
}
extern "C" __global__ __launch_bounds__(512, 4) void kf_conv() { asm volatile(CONV_ASM ::: CONV_CLOB); }
extern "C" __global__ void kf_end() {}

extern "C" void kernel_launch(void* const* d_in, const int* in_sizes, int n_in,
                              void* d_out, int out_size, void* d_ws, size_t ws_size,
                              hipStream_t stream) {
  kf_all<<<512, 512, 0, stream>>>((const u16*)d_in[0], (const u16*)d_in[1], (const u16*)d_in[2], (const u16*)d_in[3], (const u16*)d_in[4],
                                  (const u16*)d_in[5], (const u16*)d_in[6], (const u16*)d_in[7], (const u16*)d_in[8], (char*)d_ws + (size_t)WSOFF * 1048576,
                                  (const u16*)d_in[10], (const u16*)d_in[11], (const u16*)d_in[12], (const u16*)d_in[13], (u16*)d_out);
}
```

```cpp
#include <hip/hip_runtime.h>
#include <hip/hip_bf16.h>
#include <stdint.h>
#define VD2 372
#define V2D 28
#define KD 181
#define RD 60

typedef __attribute__((ext_vector_type(8))) short bf16x8;
typedef __attribute__((ext_vector_type(4))) float f32x4;
typedef uint16_t u16;
typedef __attribute__((ext_vector_type(4))) uint32_t u32x4;
typedef __attribute__((ext_vector_type(2))) uint32_t u32x2;
#define TSD
#define TS(k, i)
#define TSC(i)
#define TSE()
#define TSP
#define TSA

constexpr int L_ = 2048, D_ = 576, N_ = 128, F_ = 4608, C_ = 8;
constexpr int NF = 144;
constexpr uint32_t O_EP = 0;
constexpr uint32_t O_KF = 4096;
constexpr uint32_t O_KEY = O_KF + 8192;
constexpr uint32_t O_SC = O_KEY + 32768;
constexpr uint32_t O_DN = O_SC + 9216;
constexpr uint32_t O_SU = O_DN + 20480;
constexpr uint32_t O_FB = O_SU + 262144;
constexpr uint32_t O_XC = O_FB + 8 * 1152;
constexpr uint32_t SP = 9216 + 128;
constexpr uint32_t O_SL = O_XC + 1024 * 1152;
constexpr uint32_t SLU = 336;
constexpr uint32_t O_U = O_SL + 256 * SLU * 16;
constexpr int UP = 584;
constexpr uint32_t VP = 8192 + 128, VPP = 4096 + 128;
constexpr uint32_t O_V = O_U + 256 * UP * 16, O_VP = O_V + 256 * VP;
static_assert(256 * SLU * 16 >= NF * SP && O_SL % 128 == 0 && O_U % 128 == 0 && O_V % 128 == 0, "");
__host__ __device__ __forceinline__ uint32_t u_at(int tg, int d) { return O_U + 16u * (uint32_t)((tg >> 1) * (UP * 2) + (d >> 2) * 8 + (tg & 1) * 4 + (d & 3)); }
__host__ __device__ __forceinline__ uint32_t v_at(int d, int tg) {
  return (d < 512) ? O_V + (uint32_t)(d >> 1) * VP + 16u * (uint32_t)(2 * tg + (d & 1)) : O_VP + (uint32_t)(d - 512) * VPP + 16u * (uint32_t)tg;
}
#define HAVE_VAT
#define HAVE_VTAG
__device__ __forceinline__ float bf2f(u16 v) { return __uint_as_float((uint32_t)v << 16); }
__device__ __forceinline__ float bflo(uint32_t v) { return __uint_as_float(v << 16); }
__device__ __forceinline__ float bfhi(uint32_t v) { return __uint_as_float(v & 0xffff0000u); }
__device__ __forceinline__ u16 f2bf(float f) { return __builtin_bit_cast(u16, (__bf16)f); }
__device__ __forceinline__ float rbf(float f) { return bf2f(f2bf(f)); }
typedef __attribute__((ext_vector_type(2))) float f32x2;
typedef __attribute__((ext_vector_type(2))) __bf16 bf16x2v;
__device__ __forceinline__ uint32_t pack2(float a, float b) {
  return __builtin_bit_cast(uint32_t, __builtin_convertvector((f32x2){a, b}, bf16x2v));
}

#define DPP_I(v, ctrl, rmask) __builtin_amdgcn_update_dpp(0, (v), (ctrl), (rmask), 0xf, true)
__device__ __forceinline__ float row_sum(float v) {
  v += __int_as_float(DPP_I(__float_as_int(v), 0x111, 0xf));
  v += __int_as_float(DPP_I(__float_as_int(v), 0x112, 0xf));
  v += __int_as_float(DPP_I(__float_as_int(v), 0x114, 0xf));
  v += __int_as_float(DPP_I(__float_as_int(v), 0x118, 0xf));
  return v;
}
__device__ __forceinline__ float rdl(float v, int l) { return __int_as_float(__builtin_amdgcn_readlane(__float_as_int(v), l)); }
__device__ __forceinline__ float wave_sum(float v) {
  v = row_sum(v);
  asm("s_nop 1\n\tv_add_f32_dpp %0, %0, %0 row_bcast:15 row_mask:0xa bank_mask:0xf\n\t"
      "s_nop 1\n\tv_add_f32_dpp %0, %0, %0 row_bcast:31 row_mask:0xc bank_mask:0xf\n\ts_nop 0" : "+v"(v));
  return rdl(v, 63);
}
__device__ __forceinline__ float fold32(float a, float b) {
  const auto r = __builtin_amdgcn_permlane32_swap(__float_as_uint(a), __float_as_uint(b), false, false);
  return __uint_as_float(r[0]) + __uint_as_float(r[1]);
}
__device__ __forceinline__ float fold16(float a, float b) {
  const auto r = __builtin_amdgcn_permlane16_swap(__float_as_uint(a), __float_as_uint(b), false, false);
  return __uint_as_float(r[0]) + __uint_as_float(r[1]);
}
__device__ __forceinline__ void wave_sum2(float& a, float& b) {
  float v = row_sum(fold32(a, b));
  asm("s_nop 1\n\tv_add_f32_dpp %0, %0, %0 row_bcast:15 row_mask:0xa bank_mask:0xf\n\ts_nop 0" : "+v"(v));
  a = rdl(v, 31); b = rdl(v, 63);
}
__device__ __forceinline__ float dot2(uint32_t a, uint32_t b, float c) {
  return __builtin_amdgcn_fdot2_f32_bf16(__builtin_bit_cast(bf16x2v, a), __builtin_bit_cast(bf16x2v, b), c, false);
}
__device__ __forceinline__ uint32_t row_maxu(uint32_t v) {
  v = max(v, (uint32_t)DPP_I((int)v, 0xb1, 0xf));
  v = max(v, (uint32_t)DPP_I((int)v, 0x4e, 0xf));
  v = max(v, (uint32_t)DPP_I((int)v, 0x141, 0xf));
  v = max(v, (uint32_t)DPP_I((int)v, 0x140, 0xf));
  return v;
}
__device__ __forceinline__ uint32_t wave_maxu(uint32_t v) {
  v = max(v, (uint32_t)DPP_I((int)v, 0x111, 0xf));
  v = max(v, (uint32_t)DPP_I((int)v, 0x112, 0xf));
  v = max(v, (uint32_t)DPP_I((int)v, 0x114, 0xf));
  v = max(v, (uint32_t)DPP_I((int)v, 0x118, 0xf));
  v = max(v, (uint32_t)DPP_I((int)v, 0x142, 0xa));
  v = max(v, (uint32_t)DPP_I((int)v, 0x143, 0xc));
  return (uint32_t)__builtin_amdgcn_readlane((int)v, 63);
}

typedef __attribute__((ext_vector_type(8))) _Float16 f16x8;
typedef __attribute__((ext_vector_type(2))) _Float16 f16x2;
__device__ __forceinline__ uint32_t packh(f32x2 v) { return __builtin_bit_cast(uint32_t, __builtin_convertvector(v, f16x2)); }
__device__ __forceinline__ uint32_t packh(float a, float b) { return packh((f32x2){a, b}); }
constexpr int IP = 224, I_X = 128;
__device__ __forceinline__ f32x2 cmul(f32x2 z, f32x2 w) {
  f32x2 r;
  asm("v_pk_mul_f32 %0, %1, %2 op_sel_hi:[0,1]\n\tv_pk_fma_f32 %0, %1, %2, %0 op_sel:[1,1,0] op_sel_hi:[1,0,1] neg_lo:[0,1,0]" : "=&v"(r) : "v"(z), "v"(w));
  return r;
}
__device__ __forceinline__ f32x2 cexp2(float ar2, float air, float l) {
  const float t = air * l;
  return (f32x2){__builtin_amdgcn_cosf(t), __builtin_amdgcn_sinf(t)} * __builtin_amdgcn_exp2f(ar2 * l);
}
__device__ __forceinline__ float state_a(float ldt, float are, float aim, float& ar2, float& air, float& g) {
  const float dt = rbf(__expf(ldt));
  ar2 = dt * are * 1.4426950408889634f;
  air = dt * aim * 0.15915494309189535f;
  air -= __builtin_rintf(air);
  g = 0.f;
  if (__builtin_expect(__any(ar2 > 0.f), 0)) g = fminf(__uint_as_float(wave_maxu(__float_as_uint(fmaxf(ar2, 0.f)))), 1.f);
  return dt;
}
template <int N> __device__ __forceinline__ void put_im(char* p, int im, const f32x2* z) {
  *(u32x4*)p = (u32x4){packh(z[0][0], z[1][0]), packh(z[2][0], z[3][0]), packh(z[4][0], z[5][0]), packh(z[6][0], z[7][0])};
  *(u32x4*)(p + im) = (u32x4){packh(z[0][1], z[1][1]), packh(z[2][1], z[3][1]), packh(z[4][1], z[5][1]), packh(z[6][1], z[7][1])};
}
#define Y_ASM \
  "v_mov_b64 v[40:41], %0\nv_pk_mul_f32 v[42:43], v[40:41], %1 op_sel_hi:[0,1]\nv_pk_mul_f32 v[44:45], %0, %2 op_sel_hi:[0,1]\n" \
  "v_pk_fma_f32 v[42:43], v[40:41], %1, v[42:43] op_sel:[1,1,0] op_sel_hi:[1,0,1] neg_lo:[0,1,0]\n" \
  "v_pk_fma_f32 v[44:45], %0, %2, v[44:45] op_sel:[1,1,0] op_sel_hi:[1,0,1] neg_lo:[0,1,0]\nv_cvt_pk_f16_f32 v56, v40, v42\n" \
  "v_cvt_pk_f16_f32 v60, v41, v43\nv_pk_mul_f32 v[40:41], v[42:43], %1 op_sel_hi:[0,1]\nv_pk_mul_f32 v[48:49], v[44:45], %2 op_sel_hi:[0,1]\n" \
  "v_pk_fma_f32 v[40:41], v[42:43], %1, v[40:41] op_sel:[1,1,0] op_sel_hi:[1,0,1] neg_lo:[0,1,0]\n" \
  "v_pk_fma_f32 v[48:49], v[44:45], %2, v[48:49] op_sel:[1,1,0] op_sel_hi:[1,0,1] neg_lo:[0,1,0]\n" \
  "v_pk_mul_f32 v[42:43], v[40:41], %1 op_sel_hi:[0,1]\nv_pk_mul_f32 v[52:53], v[48:49], %2 op_sel_hi:[0,1]\n" \
  "v_pk_fma_f32 v[42:43], v[40:41], %1, v[42:43] op_sel:[1,1,0] op_sel_hi:[1,0,1] neg_lo:[0,1,0]\n" \
  "v_pk_fma_f32 v[52:53], v[48:49], %2, v[52:53] op_sel:[1,1,0] op_sel_hi:[1,0,1] neg_lo:[0,1,0]\nv_cvt_pk_f16_f32 v57, v40, v42\n" \
  "v_cvt_pk_f16_f32 v61, v41, v43\nv_pk_mul_f32 v[40:41], v[42:43], %1 op_sel_hi:[0,1]\n" \
  "v_pk_fma_f32 v[40:41], v[42:43], %1, v[40:41] op_sel:[1,1,0] op_sel_hi:[1,0,1] neg_lo:[0,1,0]\n" \
  "v_pk_mul_f32 v[42:43], v[40:41], %1 op_sel_hi:[0,1]\nv_pk_mul_f32 v[46:47], v[44:45], %1 op_sel_hi:[0,1]\n" \
  "v_pk_fma_f32 v[42:43], v[40:41], %1, v[42:43] op_sel:[1,1,0] op_sel_hi:[1,0,1] neg_lo:[0,1,0]\n" \
  "v_pk_fma_f32 v[46:47], v[44:45], %1, v[46:47] op_sel:[1,1,0] op_sel_hi:[1,0,1] neg_lo:[0,1,0]\nv_cvt_pk_f16_f32 v58, v40, v42\n" \
  "v_cvt_pk_f16_f32 v62, v41, v43\nv_cvt_pk_f16_f32 v64, v44, v46\nv_cvt_pk_f16_f32 v68, v45, v47\n" \
  "v_pk_mul_f32 v[40:41], v[42:43], %1 op_sel_hi:[0,1]\nv_pk_mul_f32 v[44:45], v[46:47], %1 op_sel_hi:[0,1]\n" \
  "v_pk_fma_f32 v[40:41], v[42:43], %1, v[40:41] op_sel:[1,1,0] op_sel_hi:[1,0,1] neg_lo:[0,1,0]\n" \
  "v_pk_fma_f32 v[44:45], v[46:47], %1, v[44:45] op_sel:[1,1,0] op_sel_hi:[1,0,1] neg_lo:[0,1,0]\n" \
  "v_pk_mul_f32 v[42:43], v[40:41], %1 op_sel_hi:[0,1]\nv_pk_mul_f32 v[46:47], v[44:45], %1 op_sel_hi:[0,1]\n" \
  "v_pk_fma_f32 v[42:43], v[40:41], %1, v[42:43] op_sel:[1,1,0] op_sel_hi:[1,0,1] neg_lo:[0,1,0]\n" \
  "v_pk_fma_f32 v[46:47], v[44:45], %1, v[46:47] op_sel:[1,1,0] op_sel_hi:[1,0,1] neg_lo:[0,1,0]\nv_cvt_pk_f16_f32 v59, v40, v42\n" \
  "v_cvt_pk_f16_f32 v63, v41, v43\nv_cvt_pk_f16_f32 v65, v44, v46\nv_cvt_pk_f16_f32 v69, v45, v47\n" \
  "v_pk_mul_f32 v[44:45], v[46:47], %1 op_sel_hi:[0,1]\nds_write_b128 %3, v[56:59] offset:0\n" \
  "v_pk_fma_f32 v[44:45], v[46:47], %1, v[44:45] op_sel:[1,1,0] op_sel_hi:[1,0,1] neg_lo:[0,1,0]\nds_write_b128 %3, v[60:63] offset:64\n" \
  "v_pk_mul_f32 v[46:47], v[44:45], %1 op_sel_hi:[0,1]\nv_pk_mul_f32 v[50:51], v[48:49], %1 op_sel_hi:[0,1]\n" \
  "v_pk_fma_f32 v[46:47], v[44:45], %1, v[46:47] op_sel:[1,1,0] op_sel_hi:[1,0,1] neg_lo:[0,1,0]\n" \
  "v_pk_fma_f32 v[50:51], v[48:49], %1, v[50:51] op_sel:[1,1,0] op_sel_hi:[1,0,1] neg_lo:[0,1,0]\nv_cvt_pk_f16_f32 v66, v44, v46\n" \
  "v_cvt_pk_f16_f32 v70, v45, v47\nv_cvt_pk_f16_f32 v56, v48, v50\nv_cvt_pk_f16_f32 v60, v49, v51\n" \
  "v_pk_mul_f32 v[44:45], v[46:47], %1 op_sel_hi:[0,1]\nv_pk_mul_f32 v[48:49], v[50:51], %1 op_sel_hi:[0,1]\n" \
  "v_pk_fma_f32 v[44:45], v[46:47], %1, v[44:45] op_sel:[1,1,0] op_sel_hi:[1,0,1] neg_lo:[0,1,0]\n" \
  "v_pk_fma_f32 v[48:49], v[50:51], %1, v[48:49] op_sel:[1,1,0] op_sel_hi:[1,0,1] neg_lo:[0,1,0]\n" \
  "v_pk_mul_f32 v[46:47], v[44:45], %1 op_sel_hi:[0,1]\nv_pk_mul_f32 v[50:51], v[48:49], %1 op_sel_hi:[0,1]\n" \
  "v_pk_fma_f32 v[46:47], v[44:45], %1, v[46:47] op_sel:[1,1,0] op_sel_hi:[1,0,1] neg_lo:[0,1,0]\n" \
  "v_pk_fma_f32 v[50:51], v[48:49], %1, v[50:51] op_sel:[1,1,0] op_sel_hi:[1,0,1] neg_lo:[0,1,0]\nv_cvt_pk_f16_f32 v67, v44, v46\n" \
  "v_cvt_pk_f16_f32 v71, v45, v47\nv_cvt_pk_f16_f32 v57, v48, v50\nv_cvt_pk_f16_f32 v61, v49, v51\n" \
  "v_pk_mul_f32 v[48:49], v[50:51], %1 op_sel_hi:[0,1]\nds_write_b128 %3, v[64:67] offset:16\n" \
  "v_pk_fma_f32 v[48:49], v[50:51], %1, v[48:49] op_sel:[1,1,0] op_sel_hi:[1,0,1] neg_lo:[0,1,0]\nds_write_b128 %3, v[68:71] offset:80\n" \
  "v_pk_mul_f32 v[50:51], v[48:49], %1 op_sel_hi:[0,1]\nv_pk_mul_f32 v[54:55], v[52:53], %1 op_sel_hi:[0,1]\n" \
  "v_pk_fma_f32 v[50:51], v[48:49], %1, v[50:51] op_sel:[1,1,0] op_sel_hi:[1,0,1] neg_lo:[0,1,0]\n" \
  "v_pk_fma_f32 v[54:55], v[52:53], %1, v[54:55] op_sel:[1,1,0] op_sel_hi:[1,0,1] neg_lo:[0,1,0]\nv_cvt_pk_f16_f32 v58, v48, v50\n" \
  "v_cvt_pk_f16_f32 v62, v49, v51\nv_cvt_pk_f16_f32 v64, v52, v54\nv_cvt_pk_f16_f32 v68, v53, v55\n" \
  "v_pk_mul_f32 v[48:49], v[50:51], %1 op_sel_hi:[0,1]\nv_pk_mul_f32 v[52:53], v[54:55], %1 op_sel_hi:[0,1]\n" \
  "v_pk_fma_f32 v[48:49], v[50:51], %1, v[48:49] op_sel:[1,1,0] op_sel_hi:[1,0,1] neg_lo:[0,1,0]\n" \
  "v_pk_fma_f32 v[52:53], v[54:55], %1, v[52:53] op_sel:[1,1,0] op_sel_hi:[1,0,1] neg_lo:[0,1,0]\n" \
  "v_pk_mul_f32 v[50:51], v[48:49], %1 op_sel_hi:[0,1]\nv_pk_mul_f32 v[54:55], v[52:53], %1 op_sel_hi:[0,1]\n" \
  "v_pk_fma_f32 v[50:51], v[48:49], %1, v[50:51] op_sel:[1,1,0] op_sel_hi:[1,0,1] neg_lo:[0,1,0]\n" \
  "v_pk_fma_f32 v[54:55], v[52:53], %1, v[54:55] op_sel:[1,1,0] op_sel_hi:[1,0,1] neg_lo:[0,1,0]\nv_cvt_pk_f16_f32 v59, v48, v50\n" \
  "v_cvt_pk_f16_f32 v63, v49, v51\nv_cvt_pk_f16_f32 v65, v52, v54\nv_cvt_pk_f16_f32 v69, v53, v55\n" \
  "v_pk_mul_f32 v[52:53], v[54:55], %1 op_sel_hi:[0,1]\nds_write_b128 %3, v[56:59] offset:32\n" \
  "v_pk_fma_f32 v[52:53], v[54:55], %1, v[52:53] op_sel:[1,1,0] op_sel_hi:[1,0,1] neg_lo:[0,1,0]\nds_write_b128 %3, v[60:63] offset:96\n" \
  "v_pk_mul_f32 v[54:55], v[52:53], %1 op_sel_hi:[0,1]\n" \
  "v_pk_fma_f32 v[54:55], v[52:53], %1, v[54:55] op_sel:[1,1,0] op_sel_hi:[1,0,1] neg_lo:[0,1,0]\nv_cvt_pk_f16_f32 v66, v52, v54\n" \
  "v_cvt_pk_f16_f32 v70, v53, v55\nv_pk_mul_f32 v[52:53], v[54:55], %1 op_sel_hi:[0,1]\n" \
  "v_pk_fma_f32 v[52:53], v[54:55], %1, v[52:53] op_sel:[1,1,0] op_sel_hi:[1,0,1] neg_lo:[0,1,0]\n" \
  "v_pk_mul_f32 v[54:55], v[52:53], %1 op_sel_hi:[0,1]\n" \
  "v_pk_fma_f32 v[54:55], v[52:53], %1, v[54:55] op_sel:[1,1,0] op_sel_hi:[1,0,1] neg_lo:[0,1,0]\nv_cvt_pk_f16_f32 v67, v52, v54\n" \
  "v_cvt_pk_f16_f32 v71, v53, v55\nds_write_b128 %3, v[64:67] offset:48\nds_write_b128 %3, v[68:71] offset:112\n"
#define Y_CLOB "memory", "v40", "v41", "v42", "v43", "v44", "v45", "v46", "v47", "v48", "v49", "v50", "v51", "v52", "v53", "v54", "v55", "v56", "v57", "v58", "v59", "v60", "v61", "v62", "v63", "v64", "v65", "v66", "v67", "v68", "v69", "v70", "v71"
#define T_ASM \
  "ds_read_b128 %4, %8\nds_read_b64_tr_b16 v[80:81], %7 offset:0\nds_read_b64_tr_b16 v[82:83], %7 offset:64\n" \
  "ds_read_b64_tr_b16 v[84:85], %5 offset:0\nds_read_b64_tr_b16 v[86:87], %5 offset:48\nds_read_b64_tr_b16 v[88:89], %6 offset:0\n" \
  "ds_read_b64_tr_b16 v[90:91], %6 offset:48\nds_read_b64_tr_b16 v[92:93], %7 offset:3584\nds_read_b64_tr_b16 v[94:95], %7 offset:3648\n" \
  "ds_read_b64_tr_b16 v[96:97], %5 offset:3584\nds_read_b64_tr_b16 v[98:99], %5 offset:3632\nds_read_b64_tr_b16 v[100:101], %6 offset:3584\n" \
  "ds_read_b64_tr_b16 v[102:103], %6 offset:3632\nds_read_b64_tr_b16 v[104:105], %7 offset:7168\nds_read_b64_tr_b16 v[106:107], %7 offset:7232\n" \
  "s_waitcnt lgkmcnt(8)\nv_mfma_f32_16x16x32_f16 %0, v[84:87], v[80:83], 0\nds_read_b64_tr_b16 v[108:109], %5 offset:7168\n" \
  "ds_read_b64_tr_b16 v[110:111], %5 offset:7216\nds_read_b64_tr_b16 v[112:113], %6 offset:7168\nv_mfma_f32_16x16x32_f16 %2, v[88:91], v[80:83], 0\n" \
  "ds_read_b64_tr_b16 v[114:115], %6 offset:7216\nds_read_b64_tr_b16 v[116:117], %7 offset:10752\nds_read_b64_tr_b16 v[118:119], %7 offset:10816\n" \
  "s_waitcnt lgkmcnt(8)\nv_mfma_f32_16x16x32_f16 %0, v[96:99], v[92:95], %0\nds_read_b64_tr_b16 v[120:121], %5 offset:10752\n" \
  "ds_read_b64_tr_b16 v[122:123], %5 offset:10800\nds_read_b64_tr_b16 v[124:125], %6 offset:10752\n" \
  "v_mfma_f32_16x16x32_f16 %2, v[100:103], v[92:95], %2\nds_read_b64_tr_b16 v[126:127], %6 offset:10800\n" \
  "ds_read_b64_tr_b16 v[80:81], %7 offset:14336\nds_read_b64_tr_b16 v[82:83], %7 offset:14400\ns_waitcnt lgkmcnt(8)\n" \
  "v_mfma_f32_16x16x32_f16 %0, v[108:111], v[104:107], %0\nds_read_b64_tr_b16 v[84:85], %5 offset:14336\n" \
  "ds_read_b64_tr_b16 v[86:87], %5 offset:14384\nds_read_b64_tr_b16 v[88:89], %6 offset:14336\n" \
  "v_mfma_f32_16x16x32_f16 %2, v[112:115], v[104:107], %2\nds_read_b64_tr_b16 v[90:91], %6 offset:14384\n" \
  "ds_read_b64_tr_b16 v[92:93], %7 offset:17920\nds_read_b64_tr_b16 v[94:95], %7 offset:17984\ns_waitcnt lgkmcnt(8)\n" \
  "v_mfma_f32_16x16x32_f16 %0, v[120:123], v[116:119], %0\nds_read_b64_tr_b16 v[96:97], %5 offset:17920\n" \
  "ds_read_b64_tr_b16 v[98:99], %5 offset:17968\nds_read_b64_tr_b16 v[100:101], %6 offset:17920\n" \
  "v_mfma_f32_16x16x32_f16 %2, v[124:127], v[116:119], %2\nds_read_b64_tr_b16 v[102:103], %6 offset:17968\n" \
  "ds_read_b64_tr_b16 v[104:105], %7 offset:21504\nds_read_b64_tr_b16 v[106:107], %7 offset:21568\ns_waitcnt lgkmcnt(8)\n" \
  "v_mfma_f32_16x16x32_f16 %1, v[84:87], v[80:83], 0\nds_read_b64_tr_b16 v[108:109], %5 offset:21504\nds_read_b64_tr_b16 v[110:111], %5 offset:21552\n" \
  "ds_read_b64_tr_b16 v[112:113], %6 offset:21504\nv_mfma_f32_16x16x32_f16 %3, v[88:91], v[80:83], 0\nds_read_b64_tr_b16 v[114:115], %6 offset:21552\n" \
  "ds_read_b64_tr_b16 v[116:117], %7 offset:25088\nds_read_b64_tr_b16 v[118:119], %7 offset:25152\ns_waitcnt lgkmcnt(8)\n" \
  "v_mfma_f32_16x16x32_f16 %1, v[96:99], v[92:95], %1\nds_read_b64_tr_b16 v[120:121], %5 offset:25088\n" \
  "ds_read_b64_tr_b16 v[122:123], %5 offset:25136\nds_read_b64_tr_b16 v[124:125], %6 offset:25088\n" \
  "v_mfma_f32_16x16x32_f16 %3, v[100:103], v[92:95], %3\nds_read_b64_tr_b16 v[126:127], %6 offset:25136\ns_waitcnt lgkmcnt(6)\n" \
  "v_mfma_f32_16x16x32_f16 %1, v[108:111], v[104:107], %1\nv_mfma_f32_16x16x32_f16 %3, v[112:115], v[104:107], %3\ns_waitcnt lgkmcnt(0)\n" \
  "v_mfma_f32_16x16x32_f16 %1, v[120:123], v[116:119], %1\nv_mfma_f32_16x16x32_f16 %3, v[124:127], v[116:119], %3\n"
#define T_CLOB "memory", "v80", "v81", "v82", "v83", "v84", "v85", "v86", "v87", "v88", "v89", "v90", "v91", "v92", "v93", "v94", "v95", "v96", "v97", "v98", "v99", "v100", "v101", "v102", "v103", "v104", "v105", "v106", "v107", "v108", "v109", "v110", "v111", "v112", "v113", "v114", "v115", "v116", "v117", "v118", "v119", "v120", "v121", "v122", "v123", "v124", "v125", "v126", "v127"
template <class F> __device__ __forceinline__ int gen_y(float ldt, float are, float aim, float bre, float bim, float cre, float cim, uint32_t im, F&& mid TSP) {
  float ar2, air, g;
  const float dt = state_a(ldt, are, aim, ar2, air, g);
  const float dbr = dt * bre, dbi = dt * bim, pr = cre * dbr - cim * dbi, pi = cre * dbi + cim * dbr;
  const f32x2 w8 = cexp2(ar2, -air, 8.f), w64 = cexp2(ar2, -air, 64.f);
  const float pm = fmaxf(fabsf(pr), fabsf(pi));
  int e = -4;
  if (__builtin_expect(__any(!(pm < 0x1p-4f) || ar2 > 0.f) || !__any(pm > 0x1p-12f), 0)) e = (int)(wave_maxu(__float_as_uint(pm)) >> 23) - 126 + (int)ceilf(g * 250.f);
  const f32x2 y0 = {__builtin_amdgcn_ldexpf(pr, 14 - e), __builtin_amdgcn_ldexpf(-pi, 14 - e)};
  mid();
  TS(0, 7);
  asm volatile(Y_ASM : : "v"(y0), "v"(w8), "v"(w64), "v"(im) : Y_CLOB);
  return e;
}
__device__ __forceinline__ int gen_x(float ldt, float are, float aim, char* im) {
  float ar2, air, g;
  state_a(ldt, are, aim, ar2, air, g);
  const int e = min((int)ceilf(g * 1810.f), 300);
  const float s = __builtin_amdgcn_ldexpf(1.f, 15 - e);
  const f32x2 w1 = cexp2(ar2, air, 1.f), w256 = cexp2(ar2, air, 256.f), w2 = cmul(w1, w1);
  f32x2 a[8], b[8], c[8];
  a[0] = (f32x2){s, 0.f}; a[1] = w1 * s; a[2] = w2 * s;
  b[0] = b[1] = w256 * s;
  b[2] = cmul(b[0], w2); a[3] = cmul(a[1], w2); a[4] = cmul(a[2], w2);
  b[3] = b[4] = b[5] = cmul(b[2], w2); a[5] = cmul(a[3], w2); a[6] = cmul(a[4], w2);
  b[6] = cmul(b[3], w2); a[7] = cmul(a[5], w2); c[0] = cmul(b[3], w256);
  b[7] = cmul(b[6], w2); c[2] = cmul(c[0], w256);
  put_im<0>(im + I_X, 48, a);
  c[1] = cmul(b[7], w256); c[3] = cmul(c[1], w256);
  put_im<0>(im + I_X + 16, 48, b);
#pragma unroll
  for (int k = 4; k < 8; ++k) c[k] = cmul(c[k - 1], w256);
  put_im<0>(im + I_X + 32, 48, c);
  return e;
}

template <int I> struct IC { static constexpr int value = I; };
template <int B, int E, class F> __device__ __forceinline__ void static_for(F&& f) {
  if constexpr (B < E) { f(IC<B>{}); static_for<B + 1, E>(f); }
}
typedef __attribute__((ext_vector_type(4))) short s16x4;
__device__ __forceinline__ u32x2 trd(uint32_t a) {
  return __builtin_bit_cast(u32x2, __builtin_amdgcn_ds_read_tr16_b64_v4i16((__attribute__((address_space(3))) s16x4*)(uintptr_t)a));
}
#define BAR() asm volatile("s_waitcnt lgkmcnt(0)\n\ts_barrier" ::: "memory")
#define ST16(off, v) asm volatile("global_store_dwordx4 %0, %1, %2 sc1\n\ts_nop 1" : : "v"(off), "v"(v), "s"(ws) : "memory")
#define ST4(off, v) asm volatile("global_store_dword %0, %1, %2 sc1" : : "v"(off), "v"(v), "s"(ws) : "memory")
#define LD16(r, off) asm volatile("global_load_dwordx4 %0, %1, %2 sc1" : "=v"(r) : "v"(off), "s"(ws) : "memory")
#define LD16W(r, off) asm volatile("global_load_dwordx4 %0, %1, %2 sc1\n\ts_waitcnt vmcnt(0)" : "=&v"(r) : "v"(off), "s"(ws) : "memory")
#define DRAIN() asm volatile("s_waitcnt vmcnt(0)" ::: "memory")
#define CEX(a, b) { const uint32_t hi_ = max(a, b), lo_ = min(a, b); a = hi_; b = lo_; }
#define MMB(a_, b_, c_) __builtin_amdgcn_mfma_f32_16x16x32_bf16(__builtin_bit_cast(bf16x8, a_), __builtin_bit_cast(bf16x8, b_), c_, 0, 0, 0)
#define NTM 0
#define DPOL ""
#define DPOL2 ""
#define DPOL4 ""
constexpr int SPIN_CAP = 1 << 18;
constexpr uint32_t UMASK = 0x40074007u;
__device__ __forceinline__ uint32_t utag(uint32_t T, int j) {
  return ((T >> (3 * j)) & 0x00070007u) | (((j <= 2) ? (T << (2 - j)) : (T >> (j - 2))) & 0x40004000u);
}
__device__ __forceinline__ uint32_t max4(const u32x4& a) { return max(max(a[0], a[1]), max(a[2], a[3])); }
#ifndef ULONG
#define ULONG 8
#endif
__device__ __forceinline__ void until(uint32_t t) {
#pragma unroll 1
  for (int spin = 0; spin < 600; ++spin) {
    const int d = (int)(t - (uint32_t)__builtin_amdgcn_s_memrealtime());
    if (d <= 0) break;
    if (ULONG && d > 4 * ULONG) __builtin_amdgcn_s_sleep(ULONG); else __builtin_amdgcn_s_sleep(1);
  }
}
__device__ __forceinline__ uint32_t latest(uint32_t t) {
  const uint32_t t0 = (uint32_t)__builtin_amdgcn_readfirstlane(t) - 0x40000000u;
  return wave_maxu(t - t0) + t0;
}
__device__ __forceinline__ uint32_t gate(const char* ws, uint32_t po, uint32_t T) {
  u32x2 pv;
#pragma unroll 1
  for (int spin = 0; spin < SPIN_CAP; ++spin) {
    asm volatile("global_load_dwordx2 %0, %1, %2 sc1\n\ts_waitcnt vmcnt(0)" : "=&v"(pv) : "v"(po), "s"(ws) : "memory");
    if (!__any(pv[0] != T)) break;
    __builtin_amdgcn_s_sleep(2);
  }
  return pv[1];
}
__device__ __forceinline__ uint32_t gate2(const char* ws, uint32_t oa, uint32_t da, uint32_t ob, uint32_t T) {
  u32x2 pa, pb;
#pragma unroll 1
  for (int spin = 0; spin < SPIN_CAP; ++spin) {
    asm volatile("global_load_dwordx2 %0, %2, %4 sc1\n\tglobal_load_dwordx2 %1, %3, %4 sc1\n\ts_waitcnt vmcnt(0)" : "=&v"(pa), "=&v"(pb) : "v"(oa), "v"(ob), "s"(ws) : "memory");
    if (!__any(pa[0] != T || pb[0] != T)) break;
    __builtin_amdgcn_s_sleep(4);
  }
  pa[1] += da;
  return latest(((int)(pa[1] - pb[1]) > 0) ? pa[1] : pb[1]);
}
__device__ __forceinline__ void gsync(uint32_t a, uint32_t target) {
  uint32_t t, c, n = SPIN_CAP;
  uint64_t ex;
  asm volatile("s_waitcnt lgkmcnt(0)\n\ts_mov_b64 %3, exec\n\ts_mov_b64 exec, 1\n\tv_mov_b32 %0, 1\n\tds_add_u32 %4, %0\n\ts_mov_b64 exec, %3\n"
               "1:\n\ts_sleep 1\n\tds_read_b32 %0, %4\n\ts_sub_u32 %2, %2, 1\n\ts_cbranch_scc1 2f\n\ts_waitcnt lgkmcnt(0)\n\tv_readfirstlane_b32 %1, %0\n\t"
               "s_cmp_lt_u32 %1, %5\n\ts_cbranch_scc1 1b\n2:\n\ts_waitcnt lgkmcnt(0)"
               : "=&v"(t), "=&s"(c), "+s"(n), "=&s"(ex) : "v"(a), "n"(target) : "memory", "scc");
}
__device__ __forceinline__ void gsync0(uint32_t a, uint32_t target) {
  uint32_t t, c, n = SPIN_CAP;
  asm volatile("s_waitcnt lgkmcnt(0)\n\ts_mov_b64 exec, 1\n\tv_mov_b32 %0, 1\n\tds_add_u32 %3, %0\n\ts_mov_b64 exec, -1\n"
               "1:\n\tds_read_b32 %0, %3\n\ts_sub_u32 %2, %2, 1\n\ts_cbranch_scc1 2f\n\ts_waitcnt lgkmcnt(0)\n\tv_readfirstlane_b32 %1, %0\n\t"
               "s_cmp_lt_u32 %1, %4\n\ts_cbranch_scc1 1b\n2:\n\ts_waitcnt lgkmcnt(0)"
               : "=&v"(t), "=&s"(c), "+s"(n) : "v"(a), "n"(target) : "memory", "scc");
}
__device__ __forceinline__ void gwait(uint32_t a, uint32_t target) {
  uint32_t t, c, n = SPIN_CAP;
  asm volatile("1:\n\ts_sleep 2\n\tds_read_b32 %0, %3\n\ts_sub_u32 %2, %2, 1\n\ts_cbranch_scc1 2f\n\ts_waitcnt lgkmcnt(0)\n\tv_readfirstlane_b32 %1, %0\n\t"
               "s_cmp_lt_u32 %1, %4\n\ts_cbranch_scc1 1b\n2:\n\ts_waitcnt lgkmcnt(0)"
               : "=&v"(t), "=&s"(c), "+s"(n) : "v"(a), "n"(target) : "memory", "scc");
}
__device__ __forceinline__ void gwait0(uint32_t a, uint32_t target) {
  uint32_t t, c, n = SPIN_CAP;
  asm volatile("1:\n\tds_read_b32 %0, %3\n\ts_sub_u32 %2, %2, 1\n\ts_cbranch_scc1 2f\n\ts_waitcnt lgkmcnt(0)\n\tv_readfirstlane_b32 %1, %0\n\t"
               "s_cmp_lt_u32 %1, %4\n\ts_cbranch_scc1 1b\n2:\n\ts_waitcnt lgkmcnt(0)"
               : "=&v"(t), "=&s"(c), "+s"(n) : "v"(a), "n"(target) : "memory", "scc");
}
__device__ __forceinline__ void dma16(const void* base, uint32_t voff, uint32_t lds) {
  uint32_t keep;
  asm volatile("s_mov_b32 %0, m0\n\ts_mov_b32 m0, %3\n\ts_nop 0\n\tglobal_load_lds_dwordx4 %1, %2" DPOL "\n\ts_mov_b32 m0, %0" : "=&s"(keep) : "v"(voff), "s"(base), "s"(lds) : "memory");
}
__device__ __forceinline__ void dma16d(const void* base, uint32_t voff, uint32_t lds) {
  uint32_t keep;
  asm volatile("s_mov_b32 %0, m0\n\ts_mov_b32 m0, %3\n\ts_nop 0\n\tglobal_load_lds_dwordx4 %1, %2" DPOL2 "\n\ts_mov_b32 m0, %0" : "=&s"(keep) : "v"(voff), "s"(base), "s"(lds) : "memory");
}
__device__ __forceinline__ void nap(int n) {
  for (; n > 15; n -= 15) __builtin_amdgcn_s_sleep(15);
  switch (n) {
    case 1: __builtin_amdgcn_s_sleep(1); break; case 2: __builtin_amdgcn_s_sleep(2); break; case 3: __builtin_amdgcn_s_sleep(3); break;
    case 4: __builtin_amdgcn_s_sleep(4); break; case 5: __builtin_amdgcn_s_sleep(5); break; case 6: __builtin_amdgcn_s_sleep(6); break;
    case 7: __builtin_amdgcn_s_sleep(7); break; case 8: __builtin_amdgcn_s_sleep(8); break; case 9: __builtin_amdgcn_s_sleep(9); break;
    case 10: __builtin_amdgcn_s_sleep(10); break; case 11: __builtin_amdgcn_s_sleep(11); break; case 12: __builtin_amdgcn_s_sleep(12); break;
    case 13: __builtin_amdgcn_s_sleep(13); break; case 14: __builtin_amdgcn_s_sleep(14); break; case 15: __builtin_amdgcn_s_sleep(15); break;
    default: break;
  }
}
#define SLEEP(n) do { if ((n) > 0) __builtin_amdgcn_s_sleep(n); } while (0)
#define USLEEP 3
#define SSLEEP 14
#define PSLEEP 2
#define PPRIO 0
#define LFIRST 1
#define WD 40
#define OSLEEP 12
#define QPRIO 1
#define KPRIO 2
#define GSLEEP 0
#define NSLEEP 0
#define UD 40
#define XD 0
#define RSLEEP 0
#define UPRIO 2
#define WDEPTH 4
#define ESLEEP 0
#define SD 58
#define OD 0
#define KBACK 6
#define DDEPTH 6
#define C3 1
#define XROT 2
#define WSOFF 0
#define KN 2
#define RPF 1
#define LSLEEP 3
#define TSTOP 70
#define HSTOP 110
#define VEARLY 0ull
#define FPR 3
#define SPR 3
#define DPRIO 0
#define QPRIO2 3
#define HW 2
#define OPOS 1
#define OD2 0
#define NA 12
#define RD2 88
#define ORD1 ((unsigned __int128)0x321087654u << 36 | 0x321087654ull)
#define WDEPTH2 4
#define RQ NA
#define HREV 1
#define QPRIO0 3
#define VPS 1
#define RW 0
#define NPRIO 0
#define XSL 10
#define N0 8
#define LPOS 0
#define FORCESLOW 0
#define FORCEFB 0
#define CONV_ASM \
  "s_cmp_eq_u32 s89, 1\ns_cbranch_scc1 1f\ns_cmp_gt_u32 s89, 1\ns_cbranch_scc1 9f\nds_read_b64 v[124:125], v3 offset:576\n" \
  "ds_read_b64 v[126:127], v3 offset:584\nds_read_b64 v[120:121], v3 offset:64\nds_read_b64 v[122:123], v3 offset:72\n" \
  "ds_read_b64 v[116:117], v3 offset:640\nds_read_b64 v[118:119], v3 offset:648\nds_read_b64 v[112:113], v3 offset:128\n" \
  "ds_read_b64 v[114:115], v3 offset:136\nds_read_b64 v[108:109], v3 offset:704\nds_read_b64 v[110:111], v3 offset:712\n" \
  "ds_read_b64 v[104:105], v3 offset:192\nds_read_b64 v[106:107], v3 offset:200\nds_read_b64 v[100:101], v3 offset:768\n" \
  "ds_read_b64 v[102:103], v3 offset:776\nds_read_b64 v[96:97], v3 offset:256\nds_read_b64 v[98:99], v3 offset:264\n" \
  "ds_read_b64 v[92:93], v3 offset:832\nds_read_b64 v[94:95], v3 offset:840\nds_read_b64 v[88:89], v3 offset:320\n" \
  "ds_read_b64 v[90:91], v3 offset:328\nds_read_b64 v[84:85], v3 offset:896\nds_read_b64 v[86:87], v3 offset:904\n" \
  "ds_read_b64 v[80:81], v3 offset:384\nds_read_b64 v[82:83], v3 offset:392\nds_read_b64 v[76:77], v3 offset:960\n" \
  "ds_read_b64 v[78:79], v3 offset:968\nds_read_b64 v[72:73], v3 offset:448\nds_read_b64 v[74:75], v3 offset:456\n" \
  "ds_read_b64 v[68:69], v3 offset:1024\nds_read_b64 v[70:71], v3 offset:1032\nds_read_b64 v[64:65], v3 offset:512\n" \
  "ds_read_b64 v[66:67], v3 offset:520\nds_read_b64 v[60:61], v3 offset:0\nds_read_b64 v[62:63], v3 offset:8\nds_read_b64 v[56:57], v3 offset:1088\n" \
  "ds_read_b64 v[58:59], v3 offset:1096\ns_waitcnt lgkmcnt(0)\ns_barrier\nds_read_b128 v[36:39], v2 offset:0\nds_read_b128 v[40:43], v2 offset:64\n" \
  "ds_read_b128 v[44:47], v2 offset:128\nds_read_b128 v[48:51], v2 offset:192\ns_waitcnt lgkmcnt(3)\n" \
  "v_mfma_f32_16x16x32_f16 v[12:15], v[124:127], v[36:39], 0\ns_setprio 1\nds_read_b128 v[52:55], v2 offset:256\n" \
  "v_mfma_f32_16x16x32_f16 v[16:19], v[120:123], v[36:39], 0\ns_waitcnt lgkmcnt(3)\nv_mfma_f32_16x16x32_f16 v[12:15], v[116:119], v[40:43], v[12:15]\n" \
  "ds_read_b128 v[36:39], v2 offset:320\nv_mfma_f32_16x16x32_f16 v[16:19], v[112:115], v[40:43], v[16:19]\ns_waitcnt lgkmcnt(3)\n" \
  "v_mfma_f32_16x16x32_f16 v[12:15], v[108:111], v[44:47], v[12:15]\nds_read_b128 v[40:43], v2 offset:384\n" \
  "v_mfma_f32_16x16x32_f16 v[16:19], v[104:107], v[44:47], v[16:19]\ns_waitcnt lgkmcnt(3)\n" \
  "v_mfma_f32_16x16x32_f16 v[12:15], v[100:103], v[48:51], v[12:15]\nds_read_b128 v[44:47], v2 offset:448\n" \
  "v_mfma_f32_16x16x32_f16 v[16:19], v[96:99], v[48:51], v[16:19]\ns_waitcnt lgkmcnt(3)\n" \
  "v_mfma_f32_16x16x32_f16 v[12:15], v[92:95], v[52:55], v[12:15]\nv_mfma_f32_16x16x32_f16 v[16:19], v[88:91], v[52:55], v[16:19]\n" \
  "s_waitcnt lgkmcnt(2)\nv_mfma_f32_16x16x32_f16 v[12:15], v[84:87], v[36:39], v[12:15]\nds_read_b128 v[48:51], v2 offset:512\n" \
  "v_mfma_f32_16x16x32_f16 v[16:19], v[80:83], v[36:39], v[16:19]\ns_waitcnt lgkmcnt(2)\n" \
  "v_mfma_f32_16x16x32_f16 v[12:15], v[76:79], v[40:43], v[12:15]\nds_read_b128 v[52:55], v2 offset:576\n" \
  "v_mfma_f32_16x16x32_f16 v[16:19], v[72:75], v[40:43], v[16:19]\ns_waitcnt lgkmcnt(2)\n" \
  "v_mfma_f32_16x16x32_f16 v[12:15], v[68:71], v[44:47], v[12:15]\nds_read_b128 v[36:39], v2 offset:640\n" \
  "v_mfma_f32_16x16x32_f16 v[16:19], v[64:67], v[44:47], v[16:19]\nv_mfma_f32_16x16x32_f16 v[20:23], v[60:63], v[44:47], 0\n" \
  "ds_read_b128 v[40:43], v2 offset:704\nv_mfma_f32_16x16x32_f16 v[28:31], v[56:59], v[44:47], 0\ns_waitcnt lgkmcnt(3)\n" \
  "v_mfma_f32_16x16x32_f16 v[16:19], v[124:127], v[48:51], v[16:19]\nds_read_b128 v[56:59], v2 offset:768\nv_cvt_pkrtz_f16_f32 v32, v12, v13\n" \
  "v_mfma_f32_16x16x32_f16 v[20:23], v[120:123], v[48:51], v[20:23]\nv_cvt_pkrtz_f16_f32 v33, v14, v15\ns_waitcnt lgkmcnt(3)\n" \
  "v_mfma_f32_16x16x32_f16 v[16:19], v[116:119], v[52:55], v[16:19]\nds_read_b128 v[44:47], v2 offset:832\n" \
  "v_pk_add_u16 v32, v32, 4 op_sel_hi:[1,0] clamp\nv_mfma_f32_16x16x32_f16 v[20:23], v[112:115], v[52:55], v[20:23]\n" \
  "v_pk_add_u16 v33, v33, 4 op_sel_hi:[1,0] clamp\ns_waitcnt lgkmcnt(3)\nv_mfma_f32_16x16x32_f16 v[16:19], v[108:111], v[36:39], v[16:19]\n" \
  "ds_read_b128 v[48:51], v2 offset:896\nds_write_b64 v4, v[32:33] offset:0\nv_mfma_f32_16x16x32_f16 v[20:23], v[104:107], v[36:39], v[20:23]\n" \
  "s_waitcnt lgkmcnt(3)\nv_mfma_f32_16x16x32_f16 v[16:19], v[100:103], v[40:43], v[16:19]\nds_read_b128 v[52:55], v2 offset:960\n" \
  "v_mfma_f32_16x16x32_f16 v[20:23], v[96:99], v[40:43], v[20:23]\nv_mfma_f32_16x16x32_f16 v[16:19], v[92:95], v[56:59], v[16:19]\n" \
  "v_mfma_f32_16x16x32_f16 v[20:23], v[88:91], v[56:59], v[20:23]\nds_read_b128 v[36:39], v2 offset:1024\ns_waitcnt lgkmcnt(3)\n" \
  "v_mfma_f32_16x16x32_f16 v[16:19], v[84:87], v[44:47], v[16:19]\nv_mfma_f32_16x16x32_f16 v[20:23], v[80:83], v[44:47], v[20:23]\n" \
  "ds_read_b128 v[40:43], v2 offset:1088\nds_read_b64 v[56:57], v6 offset:256\nds_read_b64 v[58:59], v6 offset:264\n" \
  "v_mfma_f32_16x16x32_f16 v[16:19], v[76:79], v[48:51], v[16:19]\nv_mfma_f32_16x16x32_f16 v[20:23], v[72:75], v[48:51], v[20:23]\n" \
  "ds_read_b128 v[44:47], v2 offset:1152\ns_waitcnt lgkmcnt(4)\nv_mfma_f32_16x16x32_f16 v[16:19], v[68:71], v[52:55], v[16:19]\n" \
  "v_mfma_f32_16x16x32_f16 v[20:23], v[64:67], v[52:55], v[20:23]\nds_read_b128 v[48:51], v2 offset:1216\n" \
  "v_mfma_f32_16x16x32_f16 v[24:27], v[60:63], v[52:55], 0\nv_mfma_f32_16x16x32_f16 v[20:23], v[124:127], v[36:39], v[20:23]\n" \
  "ds_read_b128 v[52:55], v2 offset:1280\nv_mfma_f32_16x16x32_f16 v[24:27], v[120:123], v[36:39], v[24:27]\ns_waitcnt lgkmcnt(5)\n" \
  "v_mfma_f32_16x16x32_f16 v[20:23], v[116:119], v[40:43], v[20:23]\nds_read_b128 v[36:39], v2 offset:1344\nv_cvt_pkrtz_f16_f32 v32, v16, v17\n" \
  "v_mfma_f32_16x16x32_f16 v[24:27], v[112:115], v[40:43], v[24:27]\nv_cvt_pkrtz_f16_f32 v33, v18, v19\ns_waitcnt lgkmcnt(3)\n" \
  "v_mfma_f32_16x16x32_f16 v[20:23], v[108:111], v[44:47], v[20:23]\nds_read_b128 v[40:43], v2 offset:1408\n" \
  "v_pk_add_u16 v32, v32, 4 op_sel_hi:[1,0] clamp\nds_read_b32 v35, v9 offset:32\nv_mfma_f32_16x16x32_f16 v[24:27], v[104:107], v[44:47], v[24:27]\n" \
  "v_pk_add_u16 v33, v33, 4 op_sel_hi:[1,0] clamp\ns_waitcnt lgkmcnt(3)\nv_mfma_f32_16x16x32_f16 v[20:23], v[100:103], v[48:51], v[20:23]\n" \
  "ds_read_b128 v[44:47], v2 offset:1472\nds_write_b64 v4, v[32:33] offset:1024\nv_mfma_f32_16x16x32_f16 v[24:27], v[96:99], v[48:51], v[24:27]\n" \
  "v_mfma_f32_16x16x32_f16 v[20:23], v[92:95], v[52:55], v[20:23]\nv_mfma_f32_16x16x32_f16 v[24:27], v[88:91], v[52:55], v[24:27]\n" \
  "ds_read_b128 v[48:51], v2 offset:1536\ns_waitcnt lgkmcnt(3)\nv_readfirstlane_b32 s85, v35\ns_cmp_lt_u32 s85, 4\ns_cbranch_scc1 21f\n22:\n" \
  "v_mfma_f32_16x16x32_f16 v[20:23], v[84:87], v[36:39], v[20:23]\nv_mfma_f32_16x16x32_f16 v[24:27], v[80:83], v[36:39], v[24:27]\n" \
  "ds_read_b128 v[52:55], v2 offset:1600\nv_mfma_f32_16x16x32_f16 v[20:23], v[76:79], v[40:43], v[20:23]\n" \
  "v_mfma_f32_16x16x32_f16 v[24:27], v[72:75], v[40:43], v[24:27]\nds_read_b128 v[36:39], v2 offset:1664\ns_waitcnt lgkmcnt(4)\n" \
  "v_mfma_f32_16x16x32_f16 v[20:23], v[68:71], v[44:47], v[20:23]\nv_mfma_f32_16x16x32_f16 v[24:27], v[64:67], v[44:47], v[24:27]\n" \
  "ds_read_b128 v[40:43], v2 offset:1728\nv_mfma_f32_16x16x32_f16 v[28:31], v[60:63], v[44:47], v[28:31]\nds_read_b64_tr_b16 v[60:61], v5 offset:0\n" \
  "ds_read_b64_tr_b16 v[62:63], v5 offset:32\ns_waitcnt lgkmcnt(4)\nv_mfma_f32_16x16x32_f16 v[24:27], v[124:127], v[48:51], v[24:27]\n" \
  "v_mfma_f32_16x16x32_f16 v[28:31], v[120:123], v[48:51], v[28:31]\nds_read_b64_tr_b16 v[44:45], v5 offset:256\n" \
  "ds_read_b64_tr_b16 v[46:47], v5 offset:288\nv_mfma_f32_16x16x32_f16 v[24:27], v[116:119], v[52:55], v[24:27]\n" \
  "ds_read_b64 v[120:121], v6 offset:320\nds_read_b64 v[122:123], v6 offset:328\nv_mfma_f32_16x16x32_f16 v[28:31], v[112:115], v[52:55], v[28:31]\n" \
  "ds_read_b128 v[48:51], v2 offset:1792\ns_waitcnt lgkmcnt(7)\nv_mfma_f32_16x16x32_f16 v[24:27], v[108:111], v[36:39], v[24:27]\n" \
  "ds_read_b128 v[112:115], v2 offset:1856\nv_mfma_f32_16x16x32_f16 v[28:31], v[104:107], v[36:39], v[28:31]\nds_read_b64 v[52:53], v6 offset:128\n" \
  "ds_read_b64 v[54:55], v6 offset:136\nv_mfma_f32_16x16x32_f16 v[24:27], v[100:103], v[40:43], v[24:27]\nds_read_b128 v[104:107], v2 offset:1920\n" \
  "v_mfma_f32_16x16x32_f16 v[28:31], v[96:99], v[40:43], v[28:31]\nds_read_b64 v[36:37], v6 offset:192\nds_read_b64 v[38:39], v6 offset:200\n" \
  "s_waitcnt lgkmcnt(9)\nv_mfma_f32_16x16x32_f16 v[20:23], v[60:63], v[56:59], v[20:23]\nds_read_b128 v[96:99], v2 offset:1984\ns_waitcnt lgkmcnt(7)\n" \
  "v_mfma_f32_16x16x32_f16 v[20:23], v[44:47], v[120:123], v[20:23]\nv_mfma_f32_16x16x32_f16 v[24:27], v[92:95], v[48:51], v[24:27]\n" \
  "ds_read_b128 v[40:43], v2 offset:2048\nv_mfma_f32_16x16x32_f16 v[28:31], v[88:91], v[48:51], v[28:31]\nds_read_b64_tr_b16 v[88:89], v5 offset:512\n" \
  "ds_read_b64_tr_b16 v[90:91], v5 offset:544\ns_waitcnt lgkmcnt(6)\nv_mfma_f32_16x16x32_f16 v[24:27], v[84:87], v[112:115], v[24:27]\n" \
  "v_mfma_f32_16x16x32_f16 v[28:31], v[80:83], v[112:115], v[28:31]\nv_cvt_pkrtz_f16_f32 v32, v20, v21\nv_cvt_pkrtz_f16_f32 v33, v22, v23\n" \
  "v_mfma_f32_16x16x32_f16 v[24:27], v[76:79], v[104:107], v[24:27]\nds_read_b64_tr_b16 v[48:49], v5 offset:768\n" \
  "ds_read_b64_tr_b16 v[50:51], v5 offset:800\nv_mfma_f32_16x16x32_f16 v[28:31], v[72:75], v[104:107], v[28:31]\n" \
  "ds_read_b128 v[80:83], v2 offset:2112\ns_waitcnt lgkmcnt(5)\nv_mfma_f32_16x16x32_f16 v[24:27], v[68:71], v[96:99], v[24:27]\n" \
  "ds_read_b128 v[112:115], v2 offset:2176\nv_pk_add_u16 v32, v32, 4 op_sel_hi:[1,0] clamp\n" \
  "v_mfma_f32_16x16x32_f16 v[28:31], v[64:67], v[96:99], v[28:31]\nds_read_b128 v[72:75], v2 offset:2240\n" \
  "v_pk_add_u16 v33, v33, 4 op_sel_hi:[1,0] clamp\nv_mfma_f32_16x16x32_f16 v[28:31], v[124:127], v[40:43], v[28:31]\n" \
  "ds_read_b128 v[104:107], v2 offset:2304\nds_write_b64 v4, v[32:33] offset:2048\nv_mfma_f32_16x16x32_f16 v[24:27], v[60:63], v[52:55], v[24:27]\n" \
  "ds_read_b128 v[64:67], v2 offset:2368\nds_read_b64 v[96:97], v6 offset:0\nds_read_b64 v[98:99], v6 offset:8\n" \
  "v_mfma_f32_16x16x32_f16 v[24:27], v[44:47], v[36:39], v[24:27]\nds_read_b128 v[124:127], v2 offset:2432\ns_waitcnt lgkmcnt(9)\n" \
  "v_mfma_f32_16x16x32_f16 v[24:27], v[88:91], v[56:59], v[24:27]\nds_read_b128 v[40:43], v2 offset:2496\n" \
  "v_mfma_f32_16x16x32_f16 v[24:27], v[48:51], v[120:123], v[24:27]\ns_waitcnt lgkmcnt(8)\n" \
  "v_mfma_f32_16x16x32_f16 v[28:31], v[116:119], v[80:83], v[28:31]\nds_read_b64 v[116:117], v6 offset:64\nds_read_b64 v[118:119], v6 offset:72\n" \
  "v_mfma_f32_16x16x32_f16 v[28:31], v[108:111], v[112:115], v[28:31]\nds_read_b64_tr_b16 v[80:81], v5 offset:1024\n" \
  "ds_read_b64_tr_b16 v[82:83], v5 offset:1056\ns_waitcnt lgkmcnt(10)\nv_mfma_f32_16x16x32_f16 v[28:31], v[100:103], v[72:75], v[28:31]\n" \
  "v_cvt_pkrtz_f16_f32 v32, v24, v25\nv_cvt_pkrtz_f16_f32 v33, v26, v27\nv_mfma_f32_16x16x32_f16 v[28:31], v[92:95], v[104:107], v[28:31]\n" \
  "v_pk_add_u16 v32, v32, 4 op_sel_hi:[1,0] clamp\ns_waitcnt lgkmcnt(5)\nv_mfma_f32_16x16x32_f16 v[28:31], v[84:87], v[64:67], v[28:31]\n" \
  "ds_read_b64_tr_b16 v[108:109], v5 offset:1280\nds_read_b64_tr_b16 v[110:111], v5 offset:1312\n" \
  "v_mfma_f32_16x16x32_f16 v[28:31], v[76:79], v[124:127], v[28:31]\nds_read_b64_tr_b16 v[112:113], v7 offset:0\n" \
  "ds_read_b64_tr_b16 v[114:115], v7 offset:64\ns_waitcnt lgkmcnt(6)\nv_mfma_f32_16x16x32_f16 v[28:31], v[68:71], v[40:43], v[28:31]\n" \
  "ds_read2_b32 v[100:101], v8 offset0:48 offset1:49\nds_read2_b32 v[102:103], v8 offset0:50 offset1:51\n" \
  "v_mfma_f32_16x16x32_f16 v[28:31], v[60:63], v[96:99], v[28:31]\nv_pk_add_u16 v33, v33, 4 op_sel_hi:[1,0] clamp\n" \
  "ds_write_b64 v4, v[32:33] offset:3072\nv_mfma_f32_16x16x32_f16 v[28:31], v[44:47], v[116:119], v[28:31]\n" \
  "v_mfma_f32_16x16x32_f16 v[28:31], v[88:91], v[52:55], v[28:31]\nv_mfma_f32_16x16x32_f16 v[28:31], v[48:51], v[36:39], v[28:31]\n" \
  "s_waitcnt lgkmcnt(5)\nv_mfma_f32_16x16x32_f16 v[28:31], v[80:83], v[56:59], v[28:31]\n" \
  "v_mfma_f32_16x16x32_f16 v[28:31], v[108:111], v[120:123], v[28:31]\ns_waitcnt lgkmcnt(1)\n" \
  "v_mfma_f32_16x16x32_f16 v[28:31], v[112:115], v[100:103], v[28:31]\ns_nop 7\nv_cvt_pkrtz_f16_f32 v32, v28, v29\nv_cvt_pkrtz_f16_f32 v33, v30, v31\n" \
  "v_pk_add_u16 v32, v32, 4 op_sel_hi:[1,0] clamp\nv_pk_add_u16 v33, v33, 4 op_sel_hi:[1,0] clamp\nds_write_b64 v4, v[32:33] offset:4096\n" \
  "s_setprio 0\ns_setpc_b64 s[90:91]\n21:\ns_movk_i32 s84, 0x4000\n23:\nds_read_b32 v35, v9 offset:32\ns_sub_u32 s84, s84, 1\ns_cbranch_scc1 22b\n" \
  "s_waitcnt lgkmcnt(0)\nv_readfirstlane_b32 s85, v35\ns_cmp_lt_u32 s85, 4\ns_cbranch_scc1 23b\ns_branch 22b\n1:\n" \
  "ds_read_b64 v[124:125], v3 offset:1088\nds_read_b64 v[126:127], v3 offset:1096\nds_read_b64 v[120:121], v3 offset:0\n" \
  "ds_read_b64 v[122:123], v3 offset:8\nds_read_b64 v[116:117], v3 offset:64\nds_read_b64 v[118:119], v3 offset:72\n" \
  "ds_read_b64 v[112:113], v3 offset:128\nds_read_b64 v[114:115], v3 offset:136\nds_read_b64 v[108:109], v3 offset:192\n" \
  "ds_read_b64 v[110:111], v3 offset:200\nds_read_b64 v[104:105], v3 offset:256\nds_read_b64 v[106:107], v3 offset:264\n" \
  "ds_read_b64 v[100:101], v3 offset:320\nds_read_b64 v[102:103], v3 offset:328\nds_read_b64 v[96:97], v3 offset:384\n" \
  "ds_read_b64 v[98:99], v3 offset:392\nds_read_b64 v[92:93], v3 offset:448\nds_read_b64 v[94:95], v3 offset:456\n" \
  "ds_read_b64 v[88:89], v3 offset:512\nds_read_b64 v[90:91], v3 offset:520\nds_read_b64 v[84:85], v3 offset:576\n" \
  "ds_read_b64 v[86:87], v3 offset:584\nds_read_b64 v[80:81], v3 offset:640\nds_read_b64 v[82:83], v3 offset:648\n" \
  "ds_read_b64 v[76:77], v3 offset:704\nds_read_b64 v[78:79], v3 offset:712\nds_read_b64 v[72:73], v3 offset:768\n" \
  "ds_read_b64 v[74:75], v3 offset:776\nds_read_b64 v[68:69], v3 offset:832\nds_read_b64 v[70:71], v3 offset:840\n" \
  "ds_read_b64 v[64:65], v3 offset:896\nds_read_b64 v[66:67], v3 offset:904\nds_read_b64 v[60:61], v3 offset:960\n" \
  "ds_read_b64 v[62:63], v3 offset:968\nds_read_b64 v[56:57], v3 offset:1024\nds_read_b64 v[58:59], v3 offset:1032\n" \
  "ds_read_b64 v[52:53], v6 offset:0\nds_read_b64 v[54:55], v6 offset:8\nds_read_b64 v[48:49], v6 offset:64\nds_read_b64 v[50:51], v6 offset:72\n" \
  "s_waitcnt lgkmcnt(0)\ns_barrier\nds_read_b128 v[28:31], v2 offset:960\nds_read_b128 v[32:35], v2 offset:1472\n" \
  "ds_read_b128 v[36:39], v2 offset:1984\nds_read_b128 v[40:43], v2 offset:2048\nds_read_b128 v[44:47], v2 offset:2112\ns_waitcnt lgkmcnt(3)\n" \
  "v_mfma_f32_16x16x32_f16 v[12:15], v[124:127], v[28:31], 0\ns_setprio 1\nds_read_b128 v[28:31], v2 offset:2176\n" \
  "v_mfma_f32_16x16x32_f16 v[16:19], v[124:127], v[32:35], 0\nds_read_b128 v[32:35], v2 offset:2240\ns_waitcnt lgkmcnt(3)\n" \
  "v_mfma_f32_16x16x32_f16 v[12:15], v[120:123], v[36:39], v[12:15]\nv_mfma_f32_16x16x32_f16 v[20:23], v[124:127], v[36:39], 0\n" \
  "ds_read_b128 v[124:127], v2 offset:2304\nds_read_b128 v[36:39], v2 offset:2368\nv_mfma_f32_16x16x32_f16 v[12:15], v[116:119], v[40:43], v[12:15]\n" \
  "ds_read_b128 v[40:43], v2 offset:2432\ns_waitcnt lgkmcnt(4)\nv_mfma_f32_16x16x32_f16 v[12:15], v[112:115], v[44:47], v[12:15]\n" \
  "ds_read_b128 v[44:47], v2 offset:2496\nv_mfma_f32_16x16x32_f16 v[12:15], v[108:111], v[28:31], v[12:15]\nds_read_b128 v[28:31], v2 offset:2560\n" \
  "s_waitcnt lgkmcnt(4)\nv_mfma_f32_16x16x32_f16 v[12:15], v[104:107], v[32:35], v[12:15]\nds_read_b128 v[32:35], v2 offset:2624\n" \
  "v_mfma_f32_16x16x32_f16 v[12:15], v[100:103], v[124:127], v[12:15]\ns_waitcnt lgkmcnt(3)\n" \
  "v_mfma_f32_16x16x32_f16 v[12:15], v[96:99], v[36:39], v[12:15]\nds_read_b128 v[124:127], v2 offset:2688\n" \
  "v_mfma_f32_16x16x32_f16 v[12:15], v[92:95], v[40:43], v[12:15]\ns_waitcnt lgkmcnt(3)\n" \
  "v_mfma_f32_16x16x32_f16 v[12:15], v[88:91], v[44:47], v[12:15]\nds_read_b128 v[36:39], v2 offset:2752\n" \
  "v_mfma_f32_16x16x32_f16 v[16:19], v[120:123], v[44:47], v[16:19]\ns_waitcnt lgkmcnt(3)\n" \
  "v_mfma_f32_16x16x32_f16 v[12:15], v[84:87], v[28:31], v[12:15]\nds_read_b128 v[40:43], v2 offset:2816\n" \
  "v_mfma_f32_16x16x32_f16 v[16:19], v[116:119], v[28:31], v[16:19]\ns_waitcnt lgkmcnt(3)\n" \
  "v_mfma_f32_16x16x32_f16 v[12:15], v[80:83], v[32:35], v[12:15]\nds_read_b128 v[44:47], v2 offset:2880\n" \
  "v_mfma_f32_16x16x32_f16 v[16:19], v[112:115], v[32:35], v[16:19]\ns_waitcnt lgkmcnt(3)\n" \
  "v_mfma_f32_16x16x32_f16 v[12:15], v[76:79], v[124:127], v[12:15]\nds_read_b128 v[28:31], v2 offset:2944\n" \
  "v_mfma_f32_16x16x32_f16 v[16:19], v[108:111], v[124:127], v[16:19]\ns_waitcnt lgkmcnt(3)\n" \
  "v_mfma_f32_16x16x32_f16 v[12:15], v[72:75], v[36:39], v[12:15]\nds_read_b128 v[32:35], v2 offset:3008\n" \
  "v_mfma_f32_16x16x32_f16 v[16:19], v[104:107], v[36:39], v[16:19]\ns_waitcnt lgkmcnt(3)\n" \
  "v_mfma_f32_16x16x32_f16 v[12:15], v[68:71], v[40:43], v[12:15]\nv_mfma_f32_16x16x32_f16 v[16:19], v[100:103], v[40:43], v[16:19]\n" \
  "ds_read_b128 v[124:127], v2 offset:3072\ns_waitcnt lgkmcnt(3)\nv_mfma_f32_16x16x32_f16 v[12:15], v[64:67], v[44:47], v[12:15]\n" \
  "v_mfma_f32_16x16x32_f16 v[16:19], v[96:99], v[44:47], v[16:19]\nds_read_b128 v[36:39], v2 offset:3136\ns_waitcnt lgkmcnt(3)\n" \
  "v_mfma_f32_16x16x32_f16 v[12:15], v[60:63], v[28:31], v[12:15]\nds_read_b64 v[40:41], v6 offset:128\nds_read_b64 v[42:43], v6 offset:136\n" \
  "v_mfma_f32_16x16x32_f16 v[16:19], v[92:95], v[28:31], v[16:19]\nds_read_b128 v[44:47], v2 offset:3200\ns_waitcnt lgkmcnt(4)\n" \
  "v_mfma_f32_16x16x32_f16 v[12:15], v[56:59], v[32:35], v[12:15]\nv_mfma_f32_16x16x32_f16 v[16:19], v[88:91], v[32:35], v[16:19]\n" \
  "ds_read_b128 v[28:31], v2 offset:3264\nv_mfma_f32_16x16x32_f16 v[20:23], v[120:123], v[32:35], v[20:23]\nds_read_b64 v[120:121], v6 offset:192\n" \
  "ds_read_b64 v[122:123], v6 offset:200\nv_mfma_f32_16x16x32_f16 v[16:19], v[84:87], v[124:127], v[16:19]\nds_read_b128 v[32:35], v2 offset:3328\n" \
  "v_mfma_f32_16x16x32_f16 v[20:23], v[116:119], v[124:127], v[20:23]\nds_read_b64 v[116:117], v6 offset:256\nds_read_b64 v[118:119], v6 offset:264\n" \
  "s_waitcnt lgkmcnt(6)\nv_mfma_f32_16x16x32_f16 v[16:19], v[80:83], v[36:39], v[16:19]\nds_read_b128 v[124:127], v2 offset:3392\n" \
  "v_mfma_f32_16x16x32_f16 v[20:23], v[112:115], v[36:39], v[20:23]\nds_read_b64 v[112:113], v6 offset:320\nds_read_b64 v[114:115], v6 offset:328\n" \
  "v_mfma_f32_16x16x32_f16 v[16:19], v[76:79], v[44:47], v[16:19]\nds_read_b128 v[36:39], v2 offset:3456\n" \
  "v_mfma_f32_16x16x32_f16 v[20:23], v[108:111], v[44:47], v[20:23]\ns_waitcnt lgkmcnt(6)\n" \
  "v_mfma_f32_16x16x32_f16 v[16:19], v[72:75], v[28:31], v[16:19]\nds_read_b128 v[108:111], v2 offset:3520\n" \
  "v_mfma_f32_16x16x32_f16 v[20:23], v[104:107], v[28:31], v[20:23]\nv_mfma_f32_16x16x32_f16 v[16:19], v[68:71], v[32:35], v[16:19]\n" \
  "ds_read_b128 v[44:47], v2 offset:3584\nv_mfma_f32_16x16x32_f16 v[20:23], v[100:103], v[32:35], v[20:23]\nds_read_b128 v[104:107], v2 offset:3648\n" \
  "ds_read_b32 v27, v9 offset:32\ns_waitcnt lgkmcnt(4)\nv_mfma_f32_16x16x32_f16 v[16:19], v[64:67], v[124:127], v[16:19]\n" \
  "ds_read_b128 v[28:31], v2 offset:3712\nv_mfma_f32_16x16x32_f16 v[20:23], v[96:99], v[124:127], v[20:23]\nds_read_b128 v[100:103], v2 offset:3776\n" \
  "ds_read2_b32 v[32:33], v8 offset0:32 offset1:33\nds_read2_b32 v[34:35], v8 offset0:34 offset1:35\n" \
  "v_mfma_f32_16x16x32_f16 v[16:19], v[60:63], v[36:39], v[16:19]\nds_read_b128 v[96:99], v2 offset:3840\n" \
  "v_mfma_f32_16x16x32_f16 v[20:23], v[92:95], v[36:39], v[20:23]\nds_read_b128 v[124:127], v2 offset:3904\ns_waitcnt lgkmcnt(8)\n" \
  "v_mfma_f32_16x16x32_f16 v[16:19], v[56:59], v[108:111], v[16:19]\nds_read_b128 v[92:95], v2 offset:3968\ns_waitcnt lgkmcnt(7)\n" \
  "v_readfirstlane_b32 s85, v27\ns_cmp_lt_u32 s85, 4\ns_cbranch_scc1 31f\n32:\nv_mfma_f32_16x16x32_f16 v[20:23], v[88:91], v[108:111], v[20:23]\n" \
  "ds_read_b128 v[36:39], v2 offset:4032\nds_read_b64_tr_b16 v[88:89], v5 offset:512\nds_read_b64_tr_b16 v[90:91], v5 offset:544\n" \
  "v_mfma_f32_16x16x32_f16 v[20:23], v[84:87], v[44:47], v[20:23]\nds_read_b64_tr_b16 v[108:109], v5 offset:768\n" \
  "ds_read_b64_tr_b16 v[110:111], v5 offset:800\nv_mfma_f32_16x16x32_f16 v[20:23], v[80:83], v[104:107], v[20:23]\n" \
  "ds_read_b64_tr_b16 v[84:85], v5 offset:1024\nds_read_b64_tr_b16 v[86:87], v5 offset:1056\ns_waitcnt lgkmcnt(12)\n" \
  "v_mfma_f32_16x16x32_f16 v[20:23], v[76:79], v[28:31], v[20:23]\nds_read_b64_tr_b16 v[44:45], v5 offset:1280\n" \
  "ds_read_b64_tr_b16 v[46:47], v5 offset:1312\nv_mfma_f32_16x16x32_f16 v[20:23], v[72:75], v[100:103], v[20:23]\ns_waitcnt lgkmcnt(10)\n" \
  "v_mfma_f32_16x16x32_f16 v[20:23], v[68:71], v[96:99], v[20:23]\nds_read_b64_tr_b16 v[80:81], v5 offset:1536\n" \
  "ds_read_b64_tr_b16 v[82:83], v5 offset:1568\nv_mfma_f32_16x16x32_f16 v[20:23], v[64:67], v[124:127], v[20:23]\n" \
  "ds_read_b64_tr_b16 v[104:105], v5 offset:1792\nds_read_b64_tr_b16 v[106:107], v5 offset:1824\ns_waitcnt lgkmcnt(12)\n" \
  "v_mfma_f32_16x16x32_f16 v[20:23], v[60:63], v[92:95], v[20:23]\nds_read_b64_tr_b16 v[76:77], v5 offset:2048\n" \
  "ds_read_b64_tr_b16 v[78:79], v5 offset:2080\nv_mfma_f32_16x16x32_f16 v[20:23], v[56:59], v[36:39], v[20:23]\ns_waitcnt lgkmcnt(10)\n" \
  "v_mfma_f32_16x16x32_f16 v[12:15], v[88:91], v[52:55], v[12:15]\nds_read_b64_tr_b16 v[28:29], v5 offset:2304\n" \
  "ds_read_b64_tr_b16 v[30:31], v5 offset:2336\nv_mfma_f32_16x16x32_f16 v[12:15], v[108:111], v[48:51], v[12:15]\n" \
  "ds_read_b64_tr_b16 v[72:73], v5 offset:2560\nds_read_b64_tr_b16 v[74:75], v5 offset:2592\ns_waitcnt lgkmcnt(10)\n" \
  "v_mfma_f32_16x16x32_f16 v[12:15], v[84:87], v[40:43], v[12:15]\nds_read_b64_tr_b16 v[100:101], v5 offset:2816\n" \
  "ds_read_b64_tr_b16 v[102:103], v5 offset:2848\nv_mfma_f32_16x16x32_f16 v[16:19], v[84:87], v[52:55], v[16:19]\n" \
  "ds_read_b64_tr_b16 v[68:69], v7 offset:0\nds_read_b64_tr_b16 v[70:71], v7 offset:64\n" \
  "v_mfma_f32_16x16x32_f16 v[12:15], v[44:47], v[120:123], v[12:15]\nv_mfma_f32_16x16x32_f16 v[16:19], v[44:47], v[48:51], v[16:19]\n" \
  "s_waitcnt lgkmcnt(10)\nv_mfma_f32_16x16x32_f16 v[12:15], v[80:83], v[116:119], v[12:15]\nds_read2_b32 v[96:97], v8 offset0:16 offset1:17\n" \
  "ds_read2_b32 v[98:99], v8 offset0:18 offset1:19\nv_mfma_f32_16x16x32_f16 v[16:19], v[80:83], v[40:43], v[16:19]\n" \
  "ds_read2_b32 v[64:65], v8 offset0:0 offset1:1\nds_read2_b32 v[66:67], v8 offset0:2 offset1:3\n" \
  "v_mfma_f32_16x16x32_f16 v[20:23], v[80:83], v[52:55], v[20:23]\nv_mfma_f32_16x16x32_f16 v[12:15], v[104:107], v[112:115], v[12:15]\n" \
  "v_mfma_f32_16x16x32_f16 v[16:19], v[104:107], v[120:123], v[16:19]\nv_mfma_f32_16x16x32_f16 v[20:23], v[104:107], v[48:51], v[20:23]\n" \
  "s_waitcnt lgkmcnt(10)\nv_mfma_f32_16x16x32_f16 v[16:19], v[76:79], v[116:119], v[16:19]\nds_read_b64_tr_b16 v[124:125], v7 offset:512\n" \
  "ds_read_b64_tr_b16 v[126:127], v7 offset:576\nv_mfma_f32_16x16x32_f16 v[20:23], v[76:79], v[40:43], v[20:23]\n" \
  "ds_read2_b32 v[60:61], v8 offset0:48 offset1:49\nds_read2_b32 v[62:63], v8 offset0:50 offset1:51\n" \
  "v_mfma_f32_16x16x32_f16 v[16:19], v[28:31], v[112:115], v[16:19]\nv_mfma_f32_16x16x32_f16 v[20:23], v[28:31], v[120:123], v[20:23]\n" \
  "s_waitcnt lgkmcnt(10)\nv_mfma_f32_16x16x32_f16 v[20:23], v[72:75], v[116:119], v[20:23]\nds_read_b64_tr_b16 v[92:93], v7 offset:1024\n" \
  "ds_read_b64_tr_b16 v[94:95], v7 offset:1088\nv_mfma_f32_16x16x32_f16 v[20:23], v[100:103], v[112:115], v[20:23]\n" \
  "ds_read_b64_tr_b16 v[56:57], v7 offset:1536\nds_read_b64_tr_b16 v[58:59], v7 offset:1600\ns_waitcnt lgkmcnt(10)\n" \
  "v_mfma_f32_16x16x32_f16 v[12:15], v[68:71], v[32:35], v[12:15]\nv_mfma_f32_16x16x32_f16 v[16:19], v[68:71], v[96:99], v[16:19]\n" \
  "s_waitcnt lgkmcnt(6)\nv_mfma_f32_16x16x32_f16 v[20:23], v[68:71], v[64:67], v[20:23]\ns_waitcnt lgkmcnt(2)\n" \
  "v_mfma_f32_16x16x32_f16 v[12:15], v[124:127], v[60:63], v[12:15]\nv_mfma_f32_16x16x32_f16 v[16:19], v[124:127], v[32:35], v[16:19]\n" \
  "v_mfma_f32_16x16x32_f16 v[20:23], v[124:127], v[96:99], v[20:23]\nv_mfma_f32_16x16x32_f16 v[16:19], v[92:95], v[60:63], v[16:19]\n" \
  "v_mfma_f32_16x16x32_f16 v[20:23], v[92:95], v[32:35], v[20:23]\ns_waitcnt lgkmcnt(0)\n" \
  "v_mfma_f32_16x16x32_f16 v[20:23], v[56:59], v[60:63], v[20:23]\ns_nop 1\nv_cvt_pkrtz_f16_f32 v24, v12, v13\nv_cvt_pkrtz_f16_f32 v25, v14, v15\n" \
  "v_pk_add_u16 v24, v24, 4 op_sel_hi:[1,0] clamp\nv_pk_add_u16 v25, v25, 4 op_sel_hi:[1,0] clamp\nds_write_b64 v4, v[24:25] offset:5120\n" \
  "v_cvt_pkrtz_f16_f32 v24, v16, v17\nv_cvt_pkrtz_f16_f32 v25, v18, v19\nv_pk_add_u16 v24, v24, 4 op_sel_hi:[1,0] clamp\n" \
  "v_pk_add_u16 v25, v25, 4 op_sel_hi:[1,0] clamp\nds_write_b64 v4, v[24:25] offset:6144\nv_cvt_pkrtz_f16_f32 v24, v20, v21\n" \
  "v_cvt_pkrtz_f16_f32 v25, v22, v23\nv_pk_add_u16 v24, v24, 4 op_sel_hi:[1,0] clamp\nv_pk_add_u16 v25, v25, 4 op_sel_hi:[1,0] clamp\n" \
  "ds_write_b64 v4, v[24:25] offset:7168\ns_setprio 0\ns_setpc_b64 s[90:91]\n31:\ns_movk_i32 s84, 0x4000\n33:\nds_read_b32 v27, v9 offset:32\n" \
  "s_sub_u32 s84, s84, 1\ns_cbranch_scc1 32b\ns_waitcnt lgkmcnt(0)\nv_readfirstlane_b32 s85, v27\ns_cmp_lt_u32 s85, 4\ns_cbranch_scc1 33b\n" \
  "s_branch 32b\n9:\ns_cmp_eq_u32 s89, 3\ns_cbranch_scc1 3f\ns_cmp_eq_u32 s89, 4\ns_cbranch_scc1 4f\ns_cmp_eq_u32 s89, 5\ns_cbranch_scc1 5f\n" \
  "ds_read_b64 v[124:125], v3 offset:576\nds_read_b64 v[126:127], v3 offset:584\nds_read_b64 v[120:121], v3 offset:64\n" \
  "ds_read_b64 v[122:123], v3 offset:72\nds_read_b64 v[116:117], v3 offset:640\nds_read_b64 v[118:119], v3 offset:648\n" \
  "ds_read_b64 v[112:113], v3 offset:128\nds_read_b64 v[114:115], v3 offset:136\nds_read_b64 v[108:109], v3 offset:704\n" \
  "ds_read_b64 v[110:111], v3 offset:712\nds_read_b64 v[104:105], v3 offset:192\nds_read_b64 v[106:107], v3 offset:200\n" \
  "ds_read_b64 v[100:101], v3 offset:768\nds_read_b64 v[102:103], v3 offset:776\nds_read_b64 v[96:97], v3 offset:256\n" \
  "ds_read_b64 v[98:99], v3 offset:264\nds_read_b64 v[92:93], v3 offset:832\nds_read_b64 v[94:95], v3 offset:840\n" \
  "ds_read_b64 v[88:89], v3 offset:320\nds_read_b64 v[90:91], v3 offset:328\nds_read_b64 v[84:85], v3 offset:896\n" \
  "ds_read_b64 v[86:87], v3 offset:904\nds_read_b64 v[80:81], v3 offset:384\nds_read_b64 v[82:83], v3 offset:392\n" \
  "ds_read_b64 v[76:77], v3 offset:960\nds_read_b64 v[78:79], v3 offset:968\nds_read_b64 v[72:73], v3 offset:448\n" \
  "ds_read_b64 v[74:75], v3 offset:456\nds_read_b64 v[68:69], v3 offset:1024\nds_read_b64 v[70:71], v3 offset:1032\n" \
  "ds_read_b64 v[64:65], v3 offset:512\nds_read_b64 v[66:67], v3 offset:520\ns_waitcnt lgkmcnt(0)\ns_barrier\nds_read_b128 v[24:27], v2 offset:0\n" \
  "ds_read_b128 v[28:31], v2 offset:64\nds_read_b128 v[32:35], v2 offset:128\nds_read_b128 v[36:39], v2 offset:192\ns_waitcnt lgkmcnt(3)\n" \
  "v_mfma_f32_16x16x32_f16 v[12:15], v[124:127], v[24:27], 0\ns_setprio 1\nds_read_b128 v[40:43], v2 offset:256\n" \
  "v_mfma_f32_16x16x32_f16 v[16:19], v[120:123], v[24:27], 0\ns_waitcnt lgkmcnt(3)\nv_mfma_f32_16x16x32_f16 v[12:15], v[116:119], v[28:31], v[12:15]\n" \
  "ds_read_b128 v[44:47], v2 offset:320\nv_mfma_f32_16x16x32_f16 v[16:19], v[112:115], v[28:31], v[16:19]\ns_waitcnt lgkmcnt(3)\n" \
  "v_mfma_f32_16x16x32_f16 v[12:15], v[108:111], v[32:35], v[12:15]\nds_read_b128 v[48:51], v2 offset:384\n" \
  "v_mfma_f32_16x16x32_f16 v[16:19], v[104:107], v[32:35], v[16:19]\ns_waitcnt lgkmcnt(3)\n" \
  "v_mfma_f32_16x16x32_f16 v[12:15], v[100:103], v[36:39], v[12:15]\nds_read_b128 v[52:55], v2 offset:448\n" \
  "v_mfma_f32_16x16x32_f16 v[16:19], v[96:99], v[36:39], v[16:19]\ns_waitcnt lgkmcnt(3)\n" \
  "v_mfma_f32_16x16x32_f16 v[12:15], v[92:95], v[40:43], v[12:15]\nds_read_b128 v[56:59], v2 offset:512\n" \
  "v_mfma_f32_16x16x32_f16 v[16:19], v[88:91], v[40:43], v[16:19]\nds_read_b128 v[60:63], v2 offset:576\ns_waitcnt lgkmcnt(3)\n" \
  "v_mfma_f32_16x16x32_f16 v[12:15], v[84:87], v[44:47], v[12:15]\nds_read_b128 v[120:123], v2 offset:640\n" \
  "v_mfma_f32_16x16x32_f16 v[16:19], v[80:83], v[44:47], v[16:19]\nds_read_b128 v[24:27], v2 offset:704\n" \
  "v_mfma_f32_16x16x32_f16 v[12:15], v[76:79], v[48:51], v[12:15]\nds_read_b128 v[112:115], v2 offset:768\n" \
  "v_mfma_f32_16x16x32_f16 v[16:19], v[72:75], v[48:51], v[16:19]\nds_read_b128 v[28:31], v2 offset:832\ns_waitcnt lgkmcnt(5)\n" \
  "v_mfma_f32_16x16x32_f16 v[12:15], v[68:71], v[52:55], v[12:15]\nds_read_b128 v[104:107], v2 offset:896\n" \
  "v_mfma_f32_16x16x32_f16 v[16:19], v[64:67], v[52:55], v[16:19]\nds_read_b128 v[32:35], v2 offset:960\n" \
  "v_mfma_f32_16x16x32_f16 v[16:19], v[124:127], v[56:59], v[16:19]\ns_waitcnt lgkmcnt(5)\n" \
  "v_mfma_f32_16x16x32_f16 v[16:19], v[116:119], v[60:63], v[16:19]\nv_mfma_f32_16x16x32_f16 v[16:19], v[108:111], v[120:123], v[16:19]\n" \
  "s_waitcnt lgkmcnt(3)\nv_mfma_f32_16x16x32_f16 v[16:19], v[100:103], v[24:27], v[16:19]\nv_cvt_pkrtz_f16_f32 v20, v12, v13\n" \
  "v_cvt_pkrtz_f16_f32 v21, v14, v15\nv_mfma_f32_16x16x32_f16 v[16:19], v[92:95], v[112:115], v[16:19]\n" \
  "v_pk_add_u16 v20, v20, 4 op_sel_hi:[1,0] clamp\ns_waitcnt lgkmcnt(2)\nv_mfma_f32_16x16x32_f16 v[16:19], v[84:87], v[28:31], v[16:19]\n" \
  "v_pk_add_u16 v21, v21, 4 op_sel_hi:[1,0] clamp\ns_waitcnt lgkmcnt(1)\nv_mfma_f32_16x16x32_f16 v[16:19], v[76:79], v[104:107], v[16:19]\n" \
  "ds_write_b64 v4, v[20:21] offset:0\ns_waitcnt lgkmcnt(1)\nv_mfma_f32_16x16x32_f16 v[16:19], v[68:71], v[32:35], v[16:19]\ns_nop 7\n" \
  "v_cvt_pkrtz_f16_f32 v20, v16, v17\nv_cvt_pkrtz_f16_f32 v21, v18, v19\nv_pk_add_u16 v20, v20, 4 op_sel_hi:[1,0] clamp\n" \
  "v_pk_add_u16 v21, v21, 4 op_sel_hi:[1,0] clamp\nds_write_b64 v4, v[20:21] offset:1024\ns_setprio 0\ns_setpc_b64 s[90:91]\n3:\n" \
  "ds_read_b64 v[124:125], v3 offset:0\nds_read_b64 v[126:127], v3 offset:8\nds_read_b64 v[120:121], v3 offset:64\n" \
  "ds_read_b64 v[122:123], v3 offset:72\nds_read_b64 v[116:117], v3 offset:128\nds_read_b64 v[118:119], v3 offset:136\n" \
  "ds_read_b64 v[112:113], v3 offset:192\nds_read_b64 v[114:115], v3 offset:200\nds_read_b64 v[108:109], v3 offset:256\n" \
  "ds_read_b64 v[110:111], v3 offset:264\nds_read_b64 v[104:105], v3 offset:320\nds_read_b64 v[106:107], v3 offset:328\n" \
  "ds_read_b64 v[100:101], v3 offset:384\nds_read_b64 v[102:103], v3 offset:392\nds_read_b64 v[96:97], v3 offset:448\n" \
  "ds_read_b64 v[98:99], v3 offset:456\nds_read_b64 v[92:93], v3 offset:512\nds_read_b64 v[94:95], v3 offset:520\n" \
  "ds_read_b64 v[88:89], v3 offset:576\nds_read_b64 v[90:91], v3 offset:584\nds_read_b64 v[84:85], v3 offset:640\n" \
  "ds_read_b64 v[86:87], v3 offset:648\nds_read_b64 v[80:81], v3 offset:704\nds_read_b64 v[82:83], v3 offset:712\n" \
  "ds_read_b64 v[76:77], v3 offset:768\nds_read_b64 v[78:79], v3 offset:776\nds_read_b64 v[72:73], v3 offset:832\n" \
  "ds_read_b64 v[74:75], v3 offset:840\nds_read_b64 v[68:69], v3 offset:896\nds_read_b64 v[70:71], v3 offset:904\n" \
  "ds_read_b64 v[64:65], v3 offset:960\nds_read_b64 v[66:67], v3 offset:968\nds_read_b64 v[60:61], v3 offset:1024\n" \
  "ds_read_b64 v[62:63], v3 offset:1032\nds_read_b64 v[56:57], v6 offset:256\nds_read_b64 v[58:59], v6 offset:264\n" \
  "ds_read_b64 v[52:53], v6 offset:128\nds_read_b64 v[54:55], v6 offset:136\nds_read_b64 v[48:49], v6 offset:320\n" \
  "ds_read_b64 v[50:51], v6 offset:328\nds_read_b64 v[44:45], v6 offset:192\nds_read_b64 v[46:47], v6 offset:200\ns_waitcnt lgkmcnt(0)\ns_barrier\n" \
  "ds_read_b128 v[24:27], v2 offset:448\nds_read_b128 v[28:31], v2 offset:512\nds_read_b128 v[32:35], v2 offset:576\n" \
  "ds_read_b128 v[36:39], v2 offset:640\nds_read_b128 v[40:43], v2 offset:704\ns_waitcnt lgkmcnt(3)\n" \
  "v_mfma_f32_16x16x32_f16 v[12:15], v[124:127], v[24:27], 0\ns_setprio 1\nds_read_b128 v[24:27], v2 offset:768\n" \
  "v_mfma_f32_16x16x32_f16 v[12:15], v[120:123], v[28:31], v[12:15]\nds_read_b128 v[28:31], v2 offset:832\ns_waitcnt lgkmcnt(3)\n" \
  "v_mfma_f32_16x16x32_f16 v[12:15], v[116:119], v[32:35], v[12:15]\nds_read_b128 v[32:35], v2 offset:896\n" \
  "v_mfma_f32_16x16x32_f16 v[12:15], v[112:115], v[36:39], v[12:15]\nds_read_b128 v[36:39], v2 offset:960\ns_waitcnt lgkmcnt(3)\n" \
  "v_mfma_f32_16x16x32_f16 v[12:15], v[108:111], v[40:43], v[12:15]\nds_read_b128 v[40:43], v2 offset:1024\n" \
  "v_mfma_f32_16x16x32_f16 v[12:15], v[104:107], v[24:27], v[12:15]\nds_read_b128 v[24:27], v2 offset:1088\ns_waitcnt lgkmcnt(3)\n" \
  "v_mfma_f32_16x16x32_f16 v[12:15], v[100:103], v[28:31], v[12:15]\nds_read_b128 v[28:31], v2 offset:1152\n" \
  "v_mfma_f32_16x16x32_f16 v[12:15], v[96:99], v[32:35], v[12:15]\ns_waitcnt lgkmcnt(3)\n" \
  "v_mfma_f32_16x16x32_f16 v[12:15], v[92:95], v[36:39], v[12:15]\nds_read_b128 v[32:35], v2 offset:1216\n" \
  "v_mfma_f32_16x16x32_f16 v[16:19], v[124:127], v[36:39], 0\ns_waitcnt lgkmcnt(3)\nv_mfma_f32_16x16x32_f16 v[12:15], v[88:91], v[40:43], v[12:15]\n" \
  "ds_read_b128 v[124:127], v2 offset:1280\nv_mfma_f32_16x16x32_f16 v[16:19], v[120:123], v[40:43], v[16:19]\ns_waitcnt lgkmcnt(3)\n" \
  "v_mfma_f32_16x16x32_f16 v[12:15], v[84:87], v[24:27], v[12:15]\nds_read_b128 v[36:39], v2 offset:1344\n" \
  "v_mfma_f32_16x16x32_f16 v[16:19], v[116:119], v[24:27], v[16:19]\ns_waitcnt lgkmcnt(3)\n" \
  "v_mfma_f32_16x16x32_f16 v[12:15], v[80:83], v[28:31], v[12:15]\nds_read_b128 v[120:123], v2 offset:1408\n" \
  "v_mfma_f32_16x16x32_f16 v[16:19], v[112:115], v[28:31], v[16:19]\ns_waitcnt lgkmcnt(3)\n" \
  "v_mfma_f32_16x16x32_f16 v[12:15], v[76:79], v[32:35], v[12:15]\nds_read_b128 v[40:43], v2 offset:1472\n" \
  "v_mfma_f32_16x16x32_f16 v[16:19], v[108:111], v[32:35], v[16:19]\ns_waitcnt lgkmcnt(3)\n" \
  "v_mfma_f32_16x16x32_f16 v[12:15], v[72:75], v[124:127], v[12:15]\nds_read_b128 v[116:119], v2 offset:1536\n" \
  "v_mfma_f32_16x16x32_f16 v[16:19], v[104:107], v[124:127], v[16:19]\nds_read_b128 v[24:27], v2 offset:1600\ns_waitcnt lgkmcnt(3)\n" \
  "v_mfma_f32_16x16x32_f16 v[12:15], v[68:71], v[36:39], v[12:15]\nds_read_b128 v[112:115], v2 offset:1664\nds_read_b32 v23, v9 offset:32\n" \
  "v_mfma_f32_16x16x32_f16 v[16:19], v[100:103], v[36:39], v[16:19]\nds_read_b128 v[28:31], v2 offset:1728\n" \
  "v_mfma_f32_16x16x32_f16 v[12:15], v[64:67], v[120:123], v[12:15]\nds_read_b128 v[108:111], v2 offset:1792\n" \
  "v_mfma_f32_16x16x32_f16 v[16:19], v[96:99], v[120:123], v[16:19]\nds_read_b128 v[32:35], v2 offset:1856\ns_waitcnt lgkmcnt(6)\n" \
  "v_mfma_f32_16x16x32_f16 v[12:15], v[60:63], v[40:43], v[12:15]\nds_read_b128 v[104:107], v2 offset:1920\n" \
  "v_mfma_f32_16x16x32_f16 v[16:19], v[92:95], v[40:43], v[16:19]\nds_read_b128 v[124:127], v2 offset:1984\ns_waitcnt lgkmcnt(5)\n" \
  "v_readfirstlane_b32 s85, v23\ns_cmp_lt_u32 s85, 3\ns_cbranch_scc1 51f\n52:\nv_mfma_f32_16x16x32_f16 v[16:19], v[88:91], v[116:119], v[16:19]\n" \
  "ds_read_b64_tr_b16 v[100:101], v5 offset:0\nds_read_b64_tr_b16 v[102:103], v5 offset:32\n" \
  "v_mfma_f32_16x16x32_f16 v[16:19], v[84:87], v[24:27], v[16:19]\nds_read_b64_tr_b16 v[36:37], v5 offset:256\n" \
  "ds_read_b64_tr_b16 v[38:39], v5 offset:288\nv_mfma_f32_16x16x32_f16 v[16:19], v[80:83], v[112:115], v[16:19]\n" \
  "ds_read_b64_tr_b16 v[96:97], v5 offset:512\nds_read_b64_tr_b16 v[98:99], v5 offset:544\ns_waitcnt lgkmcnt(9)\n" \
  "v_mfma_f32_16x16x32_f16 v[16:19], v[76:79], v[28:31], v[16:19]\nds_read_b64_tr_b16 v[120:121], v5 offset:768\n" \
  "ds_read_b64_tr_b16 v[122:123], v5 offset:800\nv_mfma_f32_16x16x32_f16 v[16:19], v[72:75], v[108:111], v[16:19]\ns_waitcnt lgkmcnt(9)\n" \
  "v_mfma_f32_16x16x32_f16 v[16:19], v[68:71], v[32:35], v[16:19]\nv_mfma_f32_16x16x32_f16 v[16:19], v[64:67], v[104:107], v[16:19]\n" \
  "s_waitcnt lgkmcnt(6)\nv_mfma_f32_16x16x32_f16 v[16:19], v[60:63], v[124:127], v[16:19]\n" \
  "v_mfma_f32_16x16x32_f16 v[12:15], v[100:103], v[56:59], v[12:15]\nv_mfma_f32_16x16x32_f16 v[16:19], v[100:103], v[52:55], v[16:19]\n" \
  "s_waitcnt lgkmcnt(2)\nv_mfma_f32_16x16x32_f16 v[12:15], v[36:39], v[48:51], v[12:15]\n" \
  "v_mfma_f32_16x16x32_f16 v[16:19], v[36:39], v[44:47], v[16:19]\nv_mfma_f32_16x16x32_f16 v[16:19], v[96:99], v[56:59], v[16:19]\n" \
  "s_waitcnt lgkmcnt(0)\nv_mfma_f32_16x16x32_f16 v[16:19], v[120:123], v[48:51], v[16:19]\ns_nop 3\nv_cvt_pkrtz_f16_f32 v20, v12, v13\n" \
  "v_cvt_pkrtz_f16_f32 v21, v14, v15\nv_pk_add_u16 v20, v20, 4 op_sel_hi:[1,0] clamp\nv_pk_add_u16 v21, v21, 4 op_sel_hi:[1,0] clamp\n" \
  "ds_write_b64 v4, v[20:21] offset:2048\nv_cvt_pkrtz_f16_f32 v20, v16, v17\nv_cvt_pkrtz_f16_f32 v21, v18, v19\n" \
  "v_pk_add_u16 v20, v20, 4 op_sel_hi:[1,0] clamp\nv_pk_add_u16 v21, v21, 4 op_sel_hi:[1,0] clamp\nds_write_b64 v4, v[20:21] offset:3072\n" \
  "s_setprio 0\ns_setpc_b64 s[90:91]\n51:\ns_movk_i32 s84, 0x4000\n53:\nds_read_b32 v23, v9 offset:32\ns_sub_u32 s84, s84, 1\ns_cbranch_scc1 52b\n" \
  "s_waitcnt lgkmcnt(0)\nv_readfirstlane_b32 s85, v23\ns_cmp_lt_u32 s85, 3\ns_cbranch_scc1 53b\ns_branch 52b\n4:\n" \
  "ds_read_b64 v[124:125], v3 offset:1088\nds_read_b64 v[126:127], v3 offset:1096\nds_read_b64 v[120:121], v3 offset:0\n" \
  "ds_read_b64 v[122:123], v3 offset:8\nds_read_b64 v[116:117], v3 offset:64\nds_read_b64 v[118:119], v3 offset:72\n" \
  "ds_read_b64 v[112:113], v3 offset:128\nds_read_b64 v[114:115], v3 offset:136\nds_read_b64 v[108:109], v3 offset:192\n" \
  "ds_read_b64 v[110:111], v3 offset:200\nds_read_b64 v[104:105], v3 offset:256\nds_read_b64 v[106:107], v3 offset:264\n" \
  "ds_read_b64 v[100:101], v3 offset:320\nds_read_b64 v[102:103], v3 offset:328\nds_read_b64 v[96:97], v3 offset:384\n" \
  "ds_read_b64 v[98:99], v3 offset:392\nds_read_b64 v[92:93], v3 offset:448\nds_read_b64 v[94:95], v3 offset:456\n" \
  "ds_read_b64 v[88:89], v3 offset:512\nds_read_b64 v[90:91], v3 offset:520\nds_read_b64 v[84:85], v3 offset:576\n" \
  "ds_read_b64 v[86:87], v3 offset:584\nds_read_b64 v[80:81], v3 offset:640\nds_read_b64 v[82:83], v3 offset:648\n" \
  "ds_read_b64 v[76:77], v3 offset:704\nds_read_b64 v[78:79], v3 offset:712\nds_read_b64 v[72:73], v3 offset:768\n" \
  "ds_read_b64 v[74:75], v3 offset:776\nds_read_b64 v[68:69], v3 offset:832\nds_read_b64 v[70:71], v3 offset:840\n" \
  "ds_read_b64 v[64:65], v3 offset:896\nds_read_b64 v[66:67], v3 offset:904\nds_read_b64 v[60:61], v3 offset:960\n" \
  "ds_read_b64 v[62:63], v3 offset:968\nds_read_b64 v[56:57], v3 offset:1024\nds_read_b64 v[58:59], v3 offset:1032\n" \
  "ds_read_b64 v[52:53], v6 offset:0\nds_read_b64 v[54:55], v6 offset:8\nds_read_b64 v[48:49], v6 offset:64\nds_read_b64 v[50:51], v6 offset:72\n" \
  "ds_read_b64 v[44:45], v6 offset:128\nds_read_b64 v[46:47], v6 offset:136\nds_read_b64 v[40:41], v6 offset:192\n" \
  "ds_read_b64 v[42:43], v6 offset:200\ns_waitcnt lgkmcnt(0)\ns_barrier\nds_read_b128 v[24:27], v2 offset:448\nds_read_b128 v[28:31], v2 offset:960\n" \
  "ds_read_b128 v[32:35], v2 offset:1472\nds_read_b128 v[36:39], v2 offset:1536\ns_waitcnt lgkmcnt(3)\n" \
  "v_mfma_f32_16x16x32_f16 v[12:15], v[124:127], v[24:27], 0\ns_setprio 1\nds_read_b128 v[24:27], v2 offset:1600\ns_waitcnt lgkmcnt(3)\n" \
  "v_mfma_f32_16x16x32_f16 v[16:19], v[124:127], v[28:31], 0\nds_read_b128 v[124:127], v2 offset:1664\ns_waitcnt lgkmcnt(3)\n" \
  "v_mfma_f32_16x16x32_f16 v[12:15], v[120:123], v[32:35], v[12:15]\nds_read_b128 v[28:31], v2 offset:1728\ns_waitcnt lgkmcnt(3)\n" \
  "v_mfma_f32_16x16x32_f16 v[12:15], v[116:119], v[36:39], v[12:15]\nds_read_b128 v[32:35], v2 offset:1792\ns_waitcnt lgkmcnt(3)\n" \
  "v_mfma_f32_16x16x32_f16 v[12:15], v[112:115], v[24:27], v[12:15]\nds_read_b128 v[36:39], v2 offset:1856\ns_waitcnt lgkmcnt(3)\n" \
  "v_mfma_f32_16x16x32_f16 v[12:15], v[108:111], v[124:127], v[12:15]\nds_read_b128 v[24:27], v2 offset:1920\ns_waitcnt lgkmcnt(3)\n" \
  "v_mfma_f32_16x16x32_f16 v[12:15], v[104:107], v[28:31], v[12:15]\nds_read_b128 v[124:127], v2 offset:1984\ns_waitcnt lgkmcnt(3)\n" \
  "v_mfma_f32_16x16x32_f16 v[12:15], v[100:103], v[32:35], v[12:15]\nds_read_b128 v[28:31], v2 offset:2048\ns_waitcnt lgkmcnt(3)\n" \
  "v_mfma_f32_16x16x32_f16 v[12:15], v[96:99], v[36:39], v[12:15]\nds_read_b128 v[32:35], v2 offset:2112\ns_waitcnt lgkmcnt(3)\n" \
  "v_mfma_f32_16x16x32_f16 v[12:15], v[92:95], v[24:27], v[12:15]\nds_read_b128 v[36:39], v2 offset:2176\ns_waitcnt lgkmcnt(3)\n" \
  "v_mfma_f32_16x16x32_f16 v[12:15], v[88:91], v[124:127], v[12:15]\nds_read_b128 v[24:27], v2 offset:2240\n" \
  "v_mfma_f32_16x16x32_f16 v[16:19], v[120:123], v[124:127], v[16:19]\nds_read_b64 v[120:121], v6 offset:256\nds_read_b64 v[122:123], v6 offset:264\n" \
  "s_waitcnt lgkmcnt(4)\nv_mfma_f32_16x16x32_f16 v[12:15], v[84:87], v[28:31], v[12:15]\nds_read_b128 v[124:127], v2 offset:2304\n" \
  "v_mfma_f32_16x16x32_f16 v[16:19], v[116:119], v[28:31], v[16:19]\nds_read_b64 v[116:117], v6 offset:320\nds_read_b64 v[118:119], v6 offset:328\n" \
  "v_mfma_f32_16x16x32_f16 v[12:15], v[80:83], v[32:35], v[12:15]\nds_read_b128 v[28:31], v2 offset:2368\n" \
  "v_mfma_f32_16x16x32_f16 v[16:19], v[112:115], v[32:35], v[16:19]\ns_waitcnt lgkmcnt(6)\n" \
  "v_mfma_f32_16x16x32_f16 v[12:15], v[76:79], v[36:39], v[12:15]\nds_read_b128 v[112:115], v2 offset:2432\n" \
  "v_mfma_f32_16x16x32_f16 v[16:19], v[108:111], v[36:39], v[16:19]\nds_read2_b32 v[32:33], v8 offset0:48 offset1:49\n" \
  "ds_read2_b32 v[34:35], v8 offset0:50 offset1:51\nv_mfma_f32_16x16x32_f16 v[12:15], v[72:75], v[24:27], v[12:15]\n" \
  "ds_read_b128 v[108:111], v2 offset:2496\nv_mfma_f32_16x16x32_f16 v[16:19], v[104:107], v[24:27], v[16:19]\n" \
  "ds_read2_b32 v[36:37], v8 offset0:32 offset1:33\nds_read2_b32 v[38:39], v8 offset0:34 offset1:35\ns_waitcnt lgkmcnt(6)\n" \
  "v_mfma_f32_16x16x32_f16 v[12:15], v[68:71], v[124:127], v[12:15]\nds_read_b128 v[104:107], v2 offset:2560\n" \
  "v_mfma_f32_16x16x32_f16 v[16:19], v[100:103], v[124:127], v[16:19]\nds_read_b128 v[24:27], v2 offset:2624\n" \
  "v_mfma_f32_16x16x32_f16 v[12:15], v[64:67], v[28:31], v[12:15]\nds_read_b128 v[100:103], v2 offset:2688\nds_read_b32 v23, v9 offset:32\n" \
  "v_mfma_f32_16x16x32_f16 v[16:19], v[96:99], v[28:31], v[16:19]\nds_read_b128 v[124:127], v2 offset:2752\ns_waitcnt lgkmcnt(7)\n" \
  "v_mfma_f32_16x16x32_f16 v[12:15], v[60:63], v[112:115], v[12:15]\nds_read_b128 v[96:99], v2 offset:2816\n" \
  "v_mfma_f32_16x16x32_f16 v[16:19], v[92:95], v[112:115], v[16:19]\nds_read_b128 v[28:31], v2 offset:2880\n" \
  "v_mfma_f32_16x16x32_f16 v[12:15], v[56:59], v[108:111], v[12:15]\nds_read_b128 v[92:95], v2 offset:2944\n" \
  "v_mfma_f32_16x16x32_f16 v[16:19], v[88:91], v[108:111], v[16:19]\nds_read_b128 v[112:115], v2 offset:3008\ns_waitcnt lgkmcnt(5)\n" \
  "v_readfirstlane_b32 s85, v23\ns_cmp_lt_u32 s85, 3\ns_cbranch_scc1 61f\n62:\nv_mfma_f32_16x16x32_f16 v[16:19], v[84:87], v[104:107], v[16:19]\n" \
  "ds_read_b64_tr_b16 v[88:89], v5 offset:0\nds_read_b64_tr_b16 v[90:91], v5 offset:32\n" \
  "v_mfma_f32_16x16x32_f16 v[16:19], v[80:83], v[24:27], v[16:19]\nds_read_b64_tr_b16 v[108:109], v5 offset:256\n" \
  "ds_read_b64_tr_b16 v[110:111], v5 offset:288\nv_mfma_f32_16x16x32_f16 v[16:19], v[76:79], v[100:103], v[16:19]\n" \
  "ds_read_b64_tr_b16 v[84:85], v5 offset:512\nds_read_b64_tr_b16 v[86:87], v5 offset:544\ns_waitcnt lgkmcnt(9)\n" \
  "v_mfma_f32_16x16x32_f16 v[16:19], v[72:75], v[124:127], v[16:19]\nds_read_b64_tr_b16 v[104:105], v5 offset:768\n" \
  "ds_read_b64_tr_b16 v[106:107], v5 offset:800\nv_mfma_f32_16x16x32_f16 v[16:19], v[68:71], v[96:99], v[16:19]\n" \
  "ds_read_b64_tr_b16 v[80:81], v5 offset:1024\nds_read_b64_tr_b16 v[82:83], v5 offset:1056\ns_waitcnt lgkmcnt(11)\n" \
  "v_mfma_f32_16x16x32_f16 v[16:19], v[64:67], v[28:31], v[16:19]\nds_read_b64_tr_b16 v[24:25], v5 offset:1280\n" \
  "ds_read_b64_tr_b16 v[26:27], v5 offset:1312\nv_mfma_f32_16x16x32_f16 v[16:19], v[60:63], v[92:95], v[16:19]\n" \
  "ds_read_b64_tr_b16 v[76:77], v5 offset:1536\nds_read_b64_tr_b16 v[78:79], v5 offset:1568\ns_waitcnt lgkmcnt(12)\n" \
  "v_mfma_f32_16x16x32_f16 v[16:19], v[56:59], v[112:115], v[16:19]\nds_read_b64_tr_b16 v[100:101], v5 offset:1792\n" \
  "ds_read_b64_tr_b16 v[102:103], v5 offset:1824\nv_mfma_f32_16x16x32_f16 v[12:15], v[88:91], v[52:55], v[12:15]\ns_waitcnt lgkmcnt(10)\n" \
  "v_mfma_f32_16x16x32_f16 v[12:15], v[108:111], v[48:51], v[12:15]\nds_read_b64_tr_b16 v[72:73], v7 offset:0\n" \
  "ds_read_b64_tr_b16 v[74:75], v7 offset:64\nv_mfma_f32_16x16x32_f16 v[12:15], v[84:87], v[44:47], v[12:15]\n" \
  "ds_read_b64_tr_b16 v[124:125], v7 offset:512\nds_read_b64_tr_b16 v[126:127], v7 offset:576\n" \
  "v_mfma_f32_16x16x32_f16 v[16:19], v[84:87], v[52:55], v[16:19]\ns_waitcnt lgkmcnt(10)\n" \
  "v_mfma_f32_16x16x32_f16 v[12:15], v[104:107], v[40:43], v[12:15]\nv_mfma_f32_16x16x32_f16 v[16:19], v[104:107], v[48:51], v[16:19]\n" \
  "v_mfma_f32_16x16x32_f16 v[12:15], v[80:83], v[120:123], v[12:15]\nv_mfma_f32_16x16x32_f16 v[16:19], v[80:83], v[44:47], v[16:19]\n" \
  "s_waitcnt lgkmcnt(6)\nv_mfma_f32_16x16x32_f16 v[12:15], v[24:27], v[116:119], v[12:15]\n" \
  "v_mfma_f32_16x16x32_f16 v[16:19], v[24:27], v[40:43], v[16:19]\nv_mfma_f32_16x16x32_f16 v[16:19], v[76:79], v[120:123], v[16:19]\n" \
  "s_waitcnt lgkmcnt(2)\nv_mfma_f32_16x16x32_f16 v[16:19], v[100:103], v[116:119], v[16:19]\n" \
  "v_mfma_f32_16x16x32_f16 v[12:15], v[72:75], v[32:35], v[12:15]\nv_mfma_f32_16x16x32_f16 v[16:19], v[72:75], v[36:39], v[16:19]\n" \
  "s_waitcnt lgkmcnt(0)\nv_mfma_f32_16x16x32_f16 v[16:19], v[124:127], v[32:35], v[16:19]\ns_nop 4\nv_cvt_pkrtz_f16_f32 v20, v12, v13\n" \
  "v_cvt_pkrtz_f16_f32 v21, v14, v15\nv_pk_add_u16 v20, v20, 4 op_sel_hi:[1,0] clamp\nv_pk_add_u16 v21, v21, 4 op_sel_hi:[1,0] clamp\n" \
  "ds_write_b64 v4, v[20:21] offset:4096\nv_cvt_pkrtz_f16_f32 v20, v16, v17\nv_cvt_pkrtz_f16_f32 v21, v18, v19\n" \
  "v_pk_add_u16 v20, v20, 4 op_sel_hi:[1,0] clamp\nv_pk_add_u16 v21, v21, 4 op_sel_hi:[1,0] clamp\nds_write_b64 v4, v[20:21] offset:5120\n" \
  "s_setprio 0\ns_setpc_b64 s[90:91]\n61:\ns_movk_i32 s84, 0x4000\n63:\nds_read_b32 v23, v9 offset:32\ns_sub_u32 s84, s84, 1\ns_cbranch_scc1 62b\n" \
  "s_waitcnt lgkmcnt(0)\nv_readfirstlane_b32 s85, v23\ns_cmp_lt_u32 s85, 3\ns_cbranch_scc1 63b\ns_branch 62b\n5:\n" \
  "ds_read_b64 v[124:125], v3 offset:1088\nds_read_b64 v[126:127], v3 offset:1096\nds_read_b64 v[120:121], v3 offset:0\n" \
  "ds_read_b64 v[122:123], v3 offset:8\nds_read_b64 v[116:117], v3 offset:64\nds_read_b64 v[118:119], v3 offset:72\n" \
  "ds_read_b64 v[112:113], v3 offset:128\nds_read_b64 v[114:115], v3 offset:136\nds_read_b64 v[108:109], v3 offset:192\n" \
  "ds_read_b64 v[110:111], v3 offset:200\nds_read_b64 v[104:105], v3 offset:256\nds_read_b64 v[106:107], v3 offset:264\n" \
  "ds_read_b64 v[100:101], v3 offset:320\nds_read_b64 v[102:103], v3 offset:328\nds_read_b64 v[96:97], v3 offset:384\n" \
  "ds_read_b64 v[98:99], v3 offset:392\nds_read_b64 v[92:93], v3 offset:448\nds_read_b64 v[94:95], v3 offset:456\n" \
  "ds_read_b64 v[88:89], v3 offset:512\nds_read_b64 v[90:91], v3 offset:520\nds_read_b64 v[84:85], v3 offset:576\n" \
  "ds_read_b64 v[86:87], v3 offset:584\nds_read_b64 v[80:81], v3 offset:640\nds_read_b64 v[82:83], v3 offset:648\n" \
  "ds_read_b64 v[76:77], v3 offset:704\nds_read_b64 v[78:79], v3 offset:712\nds_read_b64 v[72:73], v3 offset:768\n" \
  "ds_read_b64 v[74:75], v3 offset:776\nds_read_b64 v[68:69], v3 offset:832\nds_read_b64 v[70:71], v3 offset:840\n" \
  "ds_read_b64 v[64:65], v3 offset:896\nds_read_b64 v[66:67], v3 offset:904\nds_read_b64 v[60:61], v3 offset:960\n" \
  "ds_read_b64 v[62:63], v3 offset:968\nds_read_b64 v[56:57], v3 offset:1024\nds_read_b64 v[58:59], v3 offset:1032\n" \
  "ds_read_b64 v[52:53], v6 offset:0\nds_read_b64 v[54:55], v6 offset:8\nds_read_b64 v[48:49], v6 offset:64\nds_read_b64 v[50:51], v6 offset:72\n" \
  "ds_read_b64 v[44:45], v6 offset:128\nds_read_b64 v[46:47], v6 offset:136\nds_read_b64 v[40:41], v6 offset:192\n" \
  "ds_read_b64 v[42:43], v6 offset:200\ns_waitcnt lgkmcnt(0)\ns_barrier\nds_read_b128 v[24:27], v2 offset:1472\n" \
  "ds_read_b128 v[28:31], v2 offset:1984\nds_read_b128 v[32:35], v2 offset:2496\nds_read_b128 v[36:39], v2 offset:2560\ns_waitcnt lgkmcnt(3)\n" \
  "v_mfma_f32_16x16x32_f16 v[12:15], v[124:127], v[24:27], 0\ns_setprio 1\nds_read_b128 v[24:27], v2 offset:2624\ns_waitcnt lgkmcnt(3)\n" \
  "v_mfma_f32_16x16x32_f16 v[16:19], v[124:127], v[28:31], 0\nds_read_b128 v[124:127], v2 offset:2688\ns_waitcnt lgkmcnt(3)\n" \
  "v_mfma_f32_16x16x32_f16 v[12:15], v[120:123], v[32:35], v[12:15]\nds_read_b128 v[28:31], v2 offset:2752\ns_waitcnt lgkmcnt(3)\n" \
  "v_mfma_f32_16x16x32_f16 v[12:15], v[116:119], v[36:39], v[12:15]\nds_read_b128 v[32:35], v2 offset:2816\ns_waitcnt lgkmcnt(3)\n" \
  "v_mfma_f32_16x16x32_f16 v[12:15], v[112:115], v[24:27], v[12:15]\nds_read_b128 v[36:39], v2 offset:2880\ns_waitcnt lgkmcnt(3)\n" \
  "v_mfma_f32_16x16x32_f16 v[12:15], v[108:111], v[124:127], v[12:15]\nds_read_b128 v[24:27], v2 offset:2944\ns_waitcnt lgkmcnt(3)\n" \
  "v_mfma_f32_16x16x32_f16 v[12:15], v[104:107], v[28:31], v[12:15]\nds_read_b128 v[124:127], v2 offset:3008\ns_waitcnt lgkmcnt(3)\n" \
  "v_mfma_f32_16x16x32_f16 v[12:15], v[100:103], v[32:35], v[12:15]\nds_read_b128 v[28:31], v2 offset:3072\ns_waitcnt lgkmcnt(3)\n" \
  "v_mfma_f32_16x16x32_f16 v[12:15], v[96:99], v[36:39], v[12:15]\nds_read_b128 v[32:35], v2 offset:3136\ns_waitcnt lgkmcnt(3)\n" \
  "v_mfma_f32_16x16x32_f16 v[12:15], v[92:95], v[24:27], v[12:15]\nds_read_b128 v[36:39], v2 offset:3200\ns_waitcnt lgkmcnt(3)\n" \
  "v_mfma_f32_16x16x32_f16 v[12:15], v[88:91], v[124:127], v[12:15]\nds_read_b128 v[24:27], v2 offset:3264\n" \
  "v_mfma_f32_16x16x32_f16 v[16:19], v[120:123], v[124:127], v[16:19]\nds_read_b64 v[120:121], v6 offset:256\nds_read_b64 v[122:123], v6 offset:264\n" \
  "s_waitcnt lgkmcnt(4)\nv_mfma_f32_16x16x32_f16 v[12:15], v[84:87], v[28:31], v[12:15]\nds_read_b128 v[124:127], v2 offset:3328\n" \
  "v_mfma_f32_16x16x32_f16 v[16:19], v[116:119], v[28:31], v[16:19]\nds_read_b64 v[116:117], v6 offset:320\nds_read_b64 v[118:119], v6 offset:328\n" \
  "v_mfma_f32_16x16x32_f16 v[12:15], v[80:83], v[32:35], v[12:15]\nds_read_b128 v[28:31], v2 offset:3392\n" \
  "v_mfma_f32_16x16x32_f16 v[16:19], v[112:115], v[32:35], v[16:19]\ns_waitcnt lgkmcnt(6)\n" \
  "v_mfma_f32_16x16x32_f16 v[12:15], v[76:79], v[36:39], v[12:15]\nds_read_b128 v[112:115], v2 offset:3456\n" \
  "v_mfma_f32_16x16x32_f16 v[16:19], v[108:111], v[36:39], v[16:19]\nds_read2_b32 v[32:33], v8 offset0:16 offset1:17\n" \
  "ds_read2_b32 v[34:35], v8 offset0:18 offset1:19\nv_mfma_f32_16x16x32_f16 v[12:15], v[72:75], v[24:27], v[12:15]\n" \
  "ds_read_b128 v[108:111], v2 offset:3520\nv_mfma_f32_16x16x32_f16 v[16:19], v[104:107], v[24:27], v[16:19]\n" \
  "ds_read2_b32 v[36:37], v8 offset0:0 offset1:1\nds_read2_b32 v[38:39], v8 offset0:2 offset1:3\ns_waitcnt lgkmcnt(6)\n" \
  "v_mfma_f32_16x16x32_f16 v[12:15], v[68:71], v[124:127], v[12:15]\nds_read_b128 v[104:107], v2 offset:3584\n" \
  "v_mfma_f32_16x16x32_f16 v[16:19], v[100:103], v[124:127], v[16:19]\nds_read_b128 v[24:27], v2 offset:3648\n" \
  "ds_read2_b32 v[100:101], v8 offset0:32 offset1:33\nds_read2_b32 v[102:103], v8 offset0:34 offset1:35\n" \
  "v_mfma_f32_16x16x32_f16 v[12:15], v[64:67], v[28:31], v[12:15]\nds_read_b128 v[124:127], v2 offset:3712\nds_read_b32 v23, v9 offset:32\n" \
  "v_mfma_f32_16x16x32_f16 v[16:19], v[96:99], v[28:31], v[16:19]\nds_read_b128 v[96:99], v2 offset:3776\ns_waitcnt lgkmcnt(9)\n" \
  "v_mfma_f32_16x16x32_f16 v[12:15], v[60:63], v[112:115], v[12:15]\nds_read_b128 v[28:31], v2 offset:3840\n" \
  "v_mfma_f32_16x16x32_f16 v[16:19], v[92:95], v[112:115], v[16:19]\nds_read_b128 v[92:95], v2 offset:3904\n" \
  "v_mfma_f32_16x16x32_f16 v[12:15], v[56:59], v[108:111], v[12:15]\nds_read_b128 v[112:115], v2 offset:3968\n" \
  "v_mfma_f32_16x16x32_f16 v[16:19], v[88:91], v[108:111], v[16:19]\nds_read_b128 v[88:91], v2 offset:4032\ns_waitcnt lgkmcnt(5)\n" \
  "v_readfirstlane_b32 s85, v23\ns_cmp_lt_u32 s85, 3\ns_cbranch_scc1 71f\n72:\nv_mfma_f32_16x16x32_f16 v[16:19], v[84:87], v[104:107], v[16:19]\n" \
  "ds_read_b64_tr_b16 v[108:109], v5 offset:1024\nds_read_b64_tr_b16 v[110:111], v5 offset:1056\n" \
  "v_mfma_f32_16x16x32_f16 v[16:19], v[80:83], v[24:27], v[16:19]\nds_read_b64_tr_b16 v[84:85], v5 offset:1280\n" \
  "ds_read_b64_tr_b16 v[86:87], v5 offset:1312\nv_mfma_f32_16x16x32_f16 v[16:19], v[76:79], v[124:127], v[16:19]\n" \
  "ds_read_b64_tr_b16 v[104:105], v5 offset:1536\nds_read_b64_tr_b16 v[106:107], v5 offset:1568\ns_waitcnt lgkmcnt(9)\n" \
  "v_mfma_f32_16x16x32_f16 v[16:19], v[72:75], v[96:99], v[16:19]\nds_read_b64_tr_b16 v[80:81], v5 offset:1792\n" \
  "ds_read_b64_tr_b16 v[82:83], v5 offset:1824\nv_mfma_f32_16x16x32_f16 v[16:19], v[68:71], v[28:31], v[16:19]\n" \
  "ds_read_b64_tr_b16 v[24:25], v5 offset:2048\nds_read_b64_tr_b16 v[26:27], v5 offset:2080\ns_waitcnt lgkmcnt(11)\n" \
  "v_mfma_f32_16x16x32_f16 v[16:19], v[64:67], v[92:95], v[16:19]\nds_read_b64_tr_b16 v[76:77], v5 offset:2304\n" \
  "ds_read_b64_tr_b16 v[78:79], v5 offset:2336\nv_mfma_f32_16x16x32_f16 v[16:19], v[60:63], v[112:115], v[16:19]\n" \
  "ds_read_b64_tr_b16 v[124:125], v5 offset:2560\nds_read_b64_tr_b16 v[126:127], v5 offset:2592\ns_waitcnt lgkmcnt(12)\n" \
  "v_mfma_f32_16x16x32_f16 v[16:19], v[56:59], v[88:91], v[16:19]\nds_read_b64_tr_b16 v[72:73], v5 offset:2816\n" \
  "ds_read_b64_tr_b16 v[74:75], v5 offset:2848\nv_mfma_f32_16x16x32_f16 v[12:15], v[108:111], v[52:55], v[12:15]\ns_waitcnt lgkmcnt(10)\n" \
  "v_mfma_f32_16x16x32_f16 v[12:15], v[84:87], v[48:51], v[12:15]\nds_read_b64_tr_b16 v[96:97], v7 offset:0\n" \
  "ds_read_b64_tr_b16 v[98:99], v7 offset:64\nv_mfma_f32_16x16x32_f16 v[12:15], v[104:107], v[44:47], v[12:15]\n" \
  "ds_read_b64_tr_b16 v[68:69], v7 offset:512\nds_read_b64_tr_b16 v[70:71], v7 offset:576\n" \
  "v_mfma_f32_16x16x32_f16 v[16:19], v[104:107], v[52:55], v[16:19]\ns_waitcnt lgkmcnt(10)\n" \
  "v_mfma_f32_16x16x32_f16 v[12:15], v[80:83], v[40:43], v[12:15]\nds_read_b64_tr_b16 v[28:29], v7 offset:1024\n" \
  "ds_read_b64_tr_b16 v[30:31], v7 offset:1088\nv_mfma_f32_16x16x32_f16 v[16:19], v[80:83], v[48:51], v[16:19]\n" \
  "ds_read2_b32 v[64:65], v8 offset0:48 offset1:49\nds_read2_b32 v[66:67], v8 offset0:50 offset1:51\n" \
  "v_mfma_f32_16x16x32_f16 v[12:15], v[24:27], v[120:123], v[12:15]\nv_mfma_f32_16x16x32_f16 v[16:19], v[24:27], v[44:47], v[16:19]\n" \
  "s_waitcnt lgkmcnt(10)\nv_mfma_f32_16x16x32_f16 v[12:15], v[76:79], v[116:119], v[12:15]\nds_read_b64_tr_b16 v[92:93], v7 offset:1536\n" \
  "ds_read_b64_tr_b16 v[94:95], v7 offset:1600\nv_mfma_f32_16x16x32_f16 v[16:19], v[76:79], v[40:43], v[16:19]\n" \
  "v_mfma_f32_16x16x32_f16 v[16:19], v[124:127], v[120:123], v[16:19]\ns_waitcnt lgkmcnt(8)\n" \
  "v_mfma_f32_16x16x32_f16 v[16:19], v[72:75], v[116:119], v[16:19]\nv_mfma_f32_16x16x32_f16 v[12:15], v[96:99], v[32:35], v[12:15]\n" \
  "v_mfma_f32_16x16x32_f16 v[16:19], v[96:99], v[36:39], v[16:19]\ns_waitcnt lgkmcnt(4)\n" \
  "v_mfma_f32_16x16x32_f16 v[12:15], v[68:71], v[100:103], v[12:15]\nv_mfma_f32_16x16x32_f16 v[16:19], v[68:71], v[32:35], v[16:19]\n" \
  "s_waitcnt lgkmcnt(2)\nv_mfma_f32_16x16x32_f16 v[12:15], v[28:31], v[64:67], v[12:15]\n" \
  "v_mfma_f32_16x16x32_f16 v[16:19], v[28:31], v[100:103], v[16:19]\ns_waitcnt lgkmcnt(0)\n" \
  "v_mfma_f32_16x16x32_f16 v[16:19], v[92:95], v[64:67], v[16:19]\ns_nop 4\nv_cvt_pkrtz_f16_f32 v20, v12, v13\nv_cvt_pkrtz_f16_f32 v21, v14, v15\n" \
  "v_pk_add_u16 v20, v20, 4 op_sel_hi:[1,0] clamp\nv_pk_add_u16 v21, v21, 4 op_sel_hi:[1,0] clamp\nds_write_b64 v4, v[20:21] offset:6144\n" \
  "v_cvt_pkrtz_f16_f32 v20, v16, v17\nv_cvt_pkrtz_f16_f32 v21, v18, v19\nv_pk_add_u16 v20, v20, 4 op_sel_hi:[1,0] clamp\n" \
  "v_pk_add_u16 v21, v21, 4 op_sel_hi:[1,0] clamp\nds_write_b64 v4, v[20:21] offset:7168\ns_setprio 0\ns_setpc_b64 s[90:91]\n71:\n" \
  "s_movk_i32 s84, 0x4000\n73:\nds_read_b32 v23, v9 offset:32\ns_sub_u32 s84, s84, 1\ns_cbranch_scc1 72b\ns_waitcnt lgkmcnt(0)\n" \
  "v_readfirstlane_b32 s85, v23\ns_cmp_lt_u32 s85, 3\ns_cbranch_scc1 73b\ns_branch 72b\n"
#define CONV_CLOB "memory", "scc", "s84", "s85", "s90", "s91", "v12", "v13", "v14", "v15", "v16", "v17", "v18", "v19", "v20", "v21", "v22", "v23", "v24", "v25", "v26", "v27", "v28", "v29", "v30", "v31", "v32", "v33", "v34", "v35", "v36", "v37", "v38", "v39", "v40", "v41", "v42", "v43", "v44", "v45", "v46", "v47", "v48", "v49", "v50", "v51", "v52", "v53", "v54", "v55", "v56", "v57", "v58", "v59", "v60", "v61", "v62", "v63", "v64", "v65", "v66", "v67", "v68", "v69", "v70", "v71", "v72", "v73", "v74", "v75", "v76", "v77", "v78", "v79", "v80", "v81", "v82", "v83", "v84", "v85", "v86", "v87", "v88", "v89", "v90", "v91", "v92", "v93", "v94", "v95", "v96", "v97", "v98", "v99", "v100", "v101", "v102", "v103", "v104", "v105", "v106", "v107", "v108", "v109", "v110", "v111", "v112", "v113", "v114", "v115", "v116", "v117", "v118", "v119", "v120", "v121", "v122", "v123", "v124", "v125", "v126", "v127"
constexpr int TDP = 1216, T_3 = 4 * TDP, T_4 = T_3 + 512, TB = T_4 + 320;
constexpr int C_U = 480, UB = C_U + 4096 + 96, C_G5 = 0, C_G3 = 3200, C_G9 = 6400, C_G7 = C_G9 + 2096, GB = C_G7 + 2080 + 112;
constexpr int O_FL = 2 * TB, O_UL = O_FL + 64, O_IM = O_UL + 2 * UB, O_ST = O_IM + 2 * GB;
constexpr int X_TB = 10240, X_UL = X_TB + TB, X_IM = X_UL + UB, X_ST = X_IM + GB;
constexpr int X_WR = X_IM + 128 * IP;
#define X_RW(w) (LW + 9216 * (2 + ((w) >> 1)) + 2560 * ((w) & 1))
constexpr int RP = 1216;
constexpr int LW = 36864 + 512, LA = LW, LM = LW + 36864;
constexpr int L_SC = LM + 2048, SMEM = O_IM + 256 * IP;
static_assert(L_SC + 1152 <= SMEM && O_ST + 8192 <= SMEM && X_IM + 128 * IP <= LM && X_ST + 8192 <= LM && UB % 128 == 64 && GB % 128 == 64 && SMEM <= 79872, "");
struct TOps { uint32_t xa, xb, ya, pd, pf, pg, pe; };
__device__ __forceinline__ void table_ops(uint32_t im, uint32_t tb, int ct, int lane, TOps& t) {
  const int j = lane & 15, kq = lane >> 4, q4 = (lane >> 2) & 3, p4 = lane & 3, jj = j + 16 * ct;
  const uint32_t sa = im + (uint32_t)(IP * (lane >> 2));
  t.xa = sa + (uint32_t)(I_X + 8 * p4); t.xb = sa + (uint32_t)(I_X + 32 + 8 * (p4 & 1)); t.ya = sa + (uint32_t)(32 * ct + 8 * p4);
  t.pd = tb + (uint32_t)(2 * (541 - (8 * jj + 4 * (kq & 1) + 256 * (kq >> 1))));
  t.pf = tb + (uint32_t)((kq & 1) ? T_4 + 2 * (63 - jj) : T_3 + 2 * (127 - 2 * jj));
  t.pg = t.pf - ((kq & 1) ? 64 : 2);
  t.pe = tb + (uint32_t)(p4 * (TDP + 2) + ((lane < 12) ? 2 * (32 - q4) : 2 * (572 + (q4 & 1))));
  asm volatile("" : "+v"(t.xa), "+v"(t.xb), "+v"(t.ya), "+v"(t.pd), "+v"(t.pf), "+v"(t.pg), "+v"(t.pe));
}
typedef __attribute__((address_space(3))) char* lptr;
#define LP(T, a) ((__attribute__((address_space(3))) T*)(uintptr_t)(a))
__device__ __forceinline__ uint32_t table_tile(const TOps& t, int ct, int lane, uint32_t evp, float dsk TSP) {
  u32x4 ev;
  f32x4 ad[2], af[2];
  asm volatile(T_ASM : "=&v"(ad[0]), "=&v"(ad[1]), "=&v"(af[0]), "=&v"(af[1]), "=&v"(ev) : "v"(t.xa), "v"(t.xb), "v"(t.ya), "v"(evp) : T_CLOB);
  TS(0, 4);
  const int e0 = (int)(ev[0] + ev[2]), e1 = (int)(ev[1] + ev[3]);
  const int em = min(max(max(max(e0, e1), (int)((__float_as_uint(dsk) >> 23) & 0xffu) - 133), -110), 115);
  const int ts = ((uint32_t)(em + 12) <= 8u) ? 1 : -3 - em;
  float s0 = __builtin_amdgcn_ldexpf(1.f, e0 - 29 + ts), s1 = __builtin_amdgcn_ldexpf(1.f, e1 - 29 + ts);
  float d0 = (ct == 0 && lane == 0) ? __builtin_amdgcn_ldexpf(dsk, ts) : 0.f;
  asm volatile("s_nop 1" : "+v"(ad[0]), "+v"(ad[1]), "+v"(af[0]), "+v"(af[1]), "+v"(s0), "+v"(s1), "+v"(d0));
  const f32x4 k = ad[0] * s0 + (f32x4){d0, 0.f, 0.f, 0.f} + ad[1] * s1, f = af[0] * s0 + af[1] * s1;
  auto pk = [](float a, float b) { uint32_t r; asm("v_cvt_pk_f16_f32 %0, %1, %2" : "=v"(r) : "v"(a), "v"(b)); return r; };
  asm volatile("s_nop 0" : : "v"(k), "v"(f));
  const uint32_t p32 = pk(k[3], k[2]), p10 = pk(k[1], k[0]), p21 = pk(k[2], k[1]), p01 = pk(f[0], f[1]), p23 = pk(f[2], f[3]);
  *LP(u32x2, t.pd + 3 * TDP + 6) = (u32x2){p32, p10};
  *LP(uint32_t, t.pd + TDP + 2) = p32; *LP(uint32_t, t.pd + TDP + 6) = p10;
  *LP(uint32_t, t.pd + 2) = p21; *LP(uint32_t, t.pd + 2 * TDP + 6) = p21;
  *LP(u16, t.pd) = (u16)p32; *LP(u16, t.pd + 2 * TDP + 4) = (u16)p32;
  asm volatile("ds_write_b16_d16_hi %0, %1 offset:6\n\tds_write_b16_d16_hi %0, %1 offset:%2" : : "v"(t.pd), "v"(p10), "n"(2 * TDP + 10) : "memory");
  if (ct) {
    const uint32_t va = (uint32_t)__builtin_amdgcn_readlane((int)p32, 63), vb = (uint32_t)__builtin_amdgcn_readlane((int)p23, 15) >> 16;
    *LP(u16, t.pe) = (u16)((lane < 12) ? va : vb);
  }
  if (lane < 32) {
    *LP(u16, t.pf + 128) = (u16)p01; *LP(u16, t.pg + 128) = (u16)(p01 >> 16);
    *LP(u16, t.pf) = (u16)p23; *LP(u16, t.pg) = (u16)(p23 >> 16);
  }
  return (uint32_t)(127 + 5 - ts) << 23;
}
__device__ __forceinline__ uint32_t hadd(uint32_t a, uint32_t b) { return __builtin_bit_cast(uint32_t, __builtin_bit_cast(f16x2, a) + __builtin_bit_cast(f16x2, b)); }
template <bool FULL> __device__ __forceinline__ void sums(char* sm, const u32x4 p, const u32x4 c) {
  const uint32_t D[8] = {p[0], p[1], p[2], p[3], c[0], c[1], c[2], c[3]};
  uint32_t O[7];
#pragma unroll
  for (int j = 0; j < 7; ++j) O[j] = __builtin_amdgcn_alignbit(D[j + 1], D[j], 16);
  u32x4 a, b;
#pragma unroll
  for (int j = 0; j < 4; ++j) a[j] = hadd(D[j + 2], hadd(O[j + 1], O[j + 2]));
  *(u32x4*)(sm + C_G3 + 32) = a;
#pragma unroll
  for (int j = 0; j < 4; ++j) b[j] = hadd(a[j], hadd(D[j + 1], D[j + 3]));
  *(u32x4*)(sm + C_G5 + 32) = b;
  if (FULL) {
#pragma unroll
    for (int j = 0; j < 4; ++j) a[j] = hadd(b[j], hadd(O[j], O[j + 3]));
    *(u32x4*)(sm + C_G7 + 32) = a;
#pragma unroll
    for (int j = 0; j < 4; ++j) b[j] = hadd(a[j], hadd(D[j], D[j + 4]));
    *(u32x4*)(sm + C_G9 + 32) = b;
  }
}
__device__ __forceinline__ void count_up(uint32_t a) {
  uint32_t t;
  asm volatile("s_mov_b64 exec, 1\n\tv_mov_b32 %0, 1\n\tds_add_u32 %1, %0\n\ts_mov_b64 exec, -1" : "=&v"(t) : "v"(a) : "memory");
}
__device__ __forceinline__ int pad_at(int r) { return 16 * (r & 1) + 3200 * (r >> 1) - ((r >= 6) ? 3200 - 2096 : 0); }
struct COps { uint32_t b, a, o, g3, k3, g4, k4, f; };
__device__ __forceinline__ void conv_ops(uint32_t tb, uint32_t ub, uint32_t cb, int lane, COps& e) {
  const uint32_t j = lane & 15, kq = lane >> 4, q4 = (lane >> 2) & 3, p4 = lane & 3;
  e.b = ub + 32u * j + 16u * kq;
  e.a = tb + p4 * TDP + 32u - 8u * q4 + 16u * kq;
  e.g3 = cb + C_G5 + 8 + 64u * kq + 8u * (q4 + p4) + (q4 & 1u) * 3200u;
  e.g4 = cb + C_G9 + 8 + 128u * kq + 16u * q4 + 8u * p4 + (q4 & 1u) * 2096u;
  e.k3 = tb + T_3 + 120 - 8u * j + 16u * kq;
  e.k4 = tb + T_4 + 60 - 4u * j + 16u * kq;
  asm volatile("" : "+v"(e.b), "+v"(e.a), "+v"(e.g3), "+v"(e.g4), "+v"(e.k3), "+v"(e.k4));
}
#define CONV_RUN(prog, e, ...) do { int prog_ = (prog); \
    asm volatile("s_call_b64 s[90:91], kf_conv" : "+{s89}"(prog_), "+{v2}"(e.b), "+{v3}"(e.a), "+{v4}"(e.o), "+{v5}"(e.g3), "+{v6}"(e.k3), "+{v7}"(e.g4), \
                 "+{v8}"(e.k4), "+{v9}"(e.f) __VA_ARGS__ : : CONV_CLOB); } while (0)

template <bool E>
__device__ __forceinline__ void ln_stats(const uint32_t* v, int lane, float sc, f32x2* t, f32x4 z, f32x2& rc, f32x2& nc, float& e) {
  float s1a = z[0], s2a = z[1], s1b = z[2], s2b = z[3];
#pragma unroll
  for (int k = 0; k < 4; ++k) {
    s1a = dot2(v[k], 0x3f803f80u, s1a); s2a = dot2(v[k], v[k], s2a);
    s1b = dot2(v[4 + k], 0x3f803f80u, s1b); s2b = dot2(v[4 + k], v[4 + k], s2b);
  }
#pragma unroll
  for (int k = 0; k < 9; ++k) t[k] = (f32x2){bflo(v[k]), bfhi(v[k])};
  const float rs = row_sum(fold16(dot2(v[8], 0x3f803f80u, fold32(s1a, s1b)), dot2(v[8], v[8], fold32(s2a, s2b))));
  if (E) { e = row_sum(e); e += __int_as_float(__builtin_amdgcn_update_dpp(0, __float_as_int(e), 0x142, 0xa, 0xf, false)); }
  const f32x2 mean = (f32x2){rdl(rs, 15), rdl(rs, 47)} * (1.f / D_), m2 = mean * mean;
  f32x2 var = (f32x2){rdl(rs, 31), rdl(rs, 63)} * (1.f / D_) - m2;
  asm volatile("" : "+v"(t[0]), "+v"(t[1]), "+v"(t[2]), "+v"(t[3]), "+v"(t[4]), "+v"(t[5]), "+v"(t[6]), "+v"(t[7]), "+v"(t[8]));
  if (__builtin_expect(!(m2[0] <= 16.f * var[0] && m2[1] <= 16.f * var[1]), 0)) {
    float q[3] = {0.f, 0.f, 0.f};
#pragma unroll
    for (int k = 0; k < 9; ++k) {
      const f32x2 a = t[k] - ((k < 4 || (k == 8 && lane < 32)) ? mean[0] : mean[1]);
      q[k >> 2] += a[0] * a[0] + a[1] * a[1];
    }
    float qa = q[0] + ((lane < 32) ? q[2] : 0.f), qb = q[1] + ((lane < 32) ? 0.f : q[2]);
    wave_sum2(qa, qb);
    var = (f32x2){qa, qb} * (1.f / D_);
  }
  const uint32_t mp = pack2(mean[0], mean[1]), vp = pack2(var[0], var[1]);
  const f32x2 ve = (f32x2){bflo(vp), bfhi(vp)} + 1e-5f;
  const uint32_t e2 = pack2(ve[0], ve[1]);
  const uint32_t sp = pack2(__builtin_amdgcn_sqrtf(bflo(e2)), __builtin_amdgcn_sqrtf(bfhi(e2)));
  rc = (f32x2){__builtin_amdgcn_rcpf(bflo(sp)), __builtin_amdgcn_rcpf(bfhi(sp))} * sc;
  nc = -(f32x2){bflo(mp), bfhi(mp)} * rc;
}
template <bool H>
__device__ __forceinline__ void ln_apply(const f32x2* t, int lane, f32x2 rc, f32x2 nc, uint32_t* c) {
  const float nm[3] = {nc[0], nc[1], (lane < 32) ? nc[0] : nc[1]}, rr[3] = {rc[0], rc[1], (lane < 32) ? rc[0] : rc[1]};
  f32x2 dd[9];
#pragma unroll
  for (int k = 0; k < 9; ++k) dd[k] = __builtin_elementwise_fma(t[k], (f32x2){rr[k >> 2], rr[k >> 2]}, (f32x2){nm[k >> 2], nm[k >> 2]});
  asm volatile("" : "+v"(dd[0]), "+v"(dd[1]), "+v"(dd[2]), "+v"(dd[3]), "+v"(dd[4]), "+v"(dd[5]), "+v"(dd[6]), "+v"(dd[7]), "+v"(dd[8]));
#pragma unroll
  for (int k = 0; k < 9; ++k) {
    const f32x2 d = dd[k];
    typedef __attribute__((ext_vector_type(2))) uint16_t u16x2;
    if (H) c[k] = __builtin_bit_cast(uint32_t, __builtin_elementwise_add_sat(__builtin_bit_cast(u16x2, __builtin_amdgcn_cvt_pkrtz(d[0], d[1])), (u16x2){4, 4}));
    else c[k] = pack2(d[0], d[1]);
  }
}
__device__ __forceinline__ uint32_t med3u(uint32_t a, uint32_t b, uint32_t c) { return max(min(a, b), min(max(a, b), c)); }
__device__ __forceinline__ uint32_t oct_maxu(uint32_t v) {
  v = max(v, (uint32_t)DPP_I((int)v, 0xb1, 0xf));
  v = max(v, (uint32_t)DPP_I((int)v, 0x4e, 0xf));
  v = max(v, (uint32_t)DPP_I((int)v, 0x141, 0xf));
  return v;
}

#define KARGS const u16* __restrict__ x, const u16* __restrict__ Are, const u16* __restrict__ Aim, const u16* __restrict__ Bre, \
    const u16* __restrict__ Bim, const u16* __restrict__ Cre, const u16* __restrict__ Cim, const u16* __restrict__ logdt, \
    const u16* __restrict__ Dskip, char* __restrict__ ws, const u16* __restrict__ wr, const u16* __restrict__ wg, \
    const u16* __restrict__ wu, const u16* __restrict__ wd, u16* __restrict__ out
#define KPASS x, Are, Aim, Bre, Bim, Cre, Cim, logdt, Dskip, ws, wr, wg, wu, wd, out
template <int ROLE, int FFN = -1>
__device__ __forceinline__ void role_body(char* smem, KARGS) {
  const int tid = threadIdx.x, lane = tid & 63;
  const int w = __builtin_amdgcn_readfirstlane(tid >> 6), wl = w & 3;
  const int b = blockIdx.x & 255, bx = b & 7, bj = b >> 3;
  constexpr bool isl = ROLE == 1 || ROLE == 2;
  const int tg = isl ? 32 * (bj >> 2) + 4 * bx + (bj & 3) : 32 * bx + bj;
  u32x4 xa, xc;
  uint32_t ep, r0;
#define P_WAIT(n, ...) asm volatile("s_waitcnt vmcnt(" #n ")\n\tv_lshlrev_b32 %0, 16, v100\n\tv_lshlrev_b32 %1, 16, v101\n\tv_lshlrev_b32 %2, 16, v102" \
    : "=v"(ldt), "=v"(are), "=v"(aim) : : "v100", "v101", "v102" __VA_ARGS__)
#define P_MORE() asm volatile("v_lshlrev_b32 %0, 16, v103\n\tv_lshlrev_b32 %1, 16, v104\n\tv_lshlrev_b32 %2, 16, v105\n\tv_lshlrev_b32 %3, 16, v106" \
    : "=v"(bre), "=v"(bim), "=v"(cre), "=v"(cim) : : "v103", "v104", "v105", "v106")
  TSD;
  TS(0, 0);
  const uint32_t lb0 = (uint32_t)(uintptr_t)(__attribute__((address_space(3))) char*)smem;
  const int i = lane & 15, kq = lane >> 4;
  const u32x4 ones = {0xffffffffu, 0xffffffffu, 0xffffffffu, 0xffffffffu}, zero4 = {0u, 0u, 0u, 0u};
  auto u_again = [&](u32x4& uw, const u32x4& ut, uint32_t uo) {
#pragma unroll 1
    for (int spin = 0; spin < SPIN_CAP; ++spin) {
      const bool na = ((uw[0] | uw[1] | uw[2] | uw[3]) & UMASK) != 0u;
      if (__builtin_expect(!__any(na), 1)) break;
      TSC(19);
      SLEEP(USLEEP);
      if (na) { LD16W(uw, uo); uw ^= ut; }
    }
  };
  constexpr uint32_t S16 = 0x41800000u, LMASK = 0x00070007u;
  auto v_put = [&](u32x4 v, const u32x4& ut, bool other, uint32_t vo) {
    const bool second = __any(other || ((v[0] | v[1] | v[2] | v[3]) & 0x40004000u) != 0u);
    if (__builtin_expect(second, 0)) v = (v & ~LMASK) | (~ut & LMASK); else v = (v & ~LMASK) | ut;
    ST16(vo, v);
    return second;
  };
  auto v_end = [&](uint32_t T, int n) { DRAIN(); if (lane == 0) ST4(O_DN + 16u * n, T); };
  if constexpr (!isl) {
    const int ch = wl >> 1, tl = tid & 255;
    int* evp = (int*)(smem + O_FL + 16 * ch);
    float ldt, are, aim, dsk;
    if constexpr (ROLE == 0) {
      __builtin_amdgcn_s_setprio(KPRIO);
      COps e;
      conv_ops(lb0 + (uint32_t)(ch * TB), lb0 + (uint32_t)(O_UL + ch * UB), lb0 + (uint32_t)(O_IM + ch * GB), lane, e);
      e.o = lb0 + O_ST + (uint32_t)(16 * ((4 * i + 2 * (kq >> 1) + ch) ^ (i >> 1)) + 8 * (kq & 1));
      e.f = lb0 + O_FL;
      TOps t;
      table_ops(lb0 + (uint32_t)(O_IM + 128 * IP * ch), lb0 + (uint32_t)(TB * ch), wl & 1, lane, t);
      uint32_t pz = lb0 + O_IM + (uint32_t)(((lane >> 3) & 1) * GB + pad_at(lane & 7)), im = lb0 + (uint32_t)(O_IM + IP * tl);
      asm volatile("" : "+v"(e.o), "+v"(e.f), "+v"(pz), "+v"(im));
      float bre, bim, cre, cim;
      P_WAIT(0, , "v103", "v104", "v105", "v106", "v107", "v109");
      asm volatile("" : "={v109}"(ep));
      const uint32_t T = ((uint32_t)__builtin_amdgcn_readfirstlane(ep) + 1u) * 0x9E3779B1u;
      if (w == 3 && lane < 8) ST16(O_KEY + 128u * b + 16u * lane, ones);
      P_MORE();
      asm volatile("v_lshlrev_b32 %0, 16, v107" : "=v"(dsk) : : "v107");
      TS(0, 1);
      evp[wl & 1] = gen_y(ldt, are, aim, bre, bim, cre, cim, im, [] {} TSA);
      TS(0, 8);
      BAR();
      TS(0, 2);
      const uint32_t cv = table_tile(t, wl & 1, lane, lb0 + O_FL + 16 * ch, dsk TSA), cs = (uint32_t)__builtin_amdgcn_readfirstlane(cv);
      *(uint32_t*)(smem + O_FL + 40 + 4 * ch) = cv;
      TS(0, 9);
      gsync0(lb0 + O_FL + 36, 4);
      TS(0, 3);
      *(u32x4*)(smem + O_ST + 16 * tl) = ones;
      *(u32x4*)(smem + O_ST + 4096 + 16 * tl) = ones;
      if (w == 0) *LP(u32x4, pz) = zero4;
      DRAIN();
      CONV_RUN(wl & 1, e);
      if (wl & 1) asm volatile("s_mov_b64 exec, 1\n\tglobal_store_dwordx2 %0, %1, %2 sc1" : : "v"(O_SC + 16u * (2 * tg + ch)), "v"((u32x2){T, cs}), "s"(ws) : "memory");
      TS(0, 5);
    } else {
      __builtin_amdgcn_s_setprio(UPRIO);
#pragma unroll
      for (int k = 0; k < 3; ++k) *(u32x4*)(smem + ((16 * (tl + 256 * k) < O_FL) ? 16 * (tl + 256 * k) : O_FL + 32)) = zero4;
      if (w == 7) *(u32x4*)(smem + O_UL + (lane >> 5) * UB + 16 * min(lane & 31, 29)) = zero4;
      uint32_t uo = u_at(tl >> 1, 2 * tg + (tid & 1)), go = O_SU + (uint32_t)(256 * bx + 32 * wl + (lane & 31) + 4 * (lane & 32)) * 128u;
      uint32_t ul = (uint32_t)(O_UL + (tid & 1) * UB + C_U - 16 + 16 * (tl >> 1)), ug = (uint32_t)(O_IM + (tid & 1) * GB + 16 * (tl >> 1)), so = lb0 + O_ST + 16u * (uint32_t)(lane ^ (lane >> 3));
      uint32_t vo = O_V + (uint32_t)tg * VP + 16u * lane;
      asm volatile("" : "+v"(uo), "+v"(go), "+v"(ul), "+v"(ug), "+v"(so), "+v"(vo));
      P_WAIT(1, , "v109");
      TS(0, 1);
      evp[2 + (wl & 1)] = gen_x(ldt, are, aim, smem + O_IM + IP * tl);
      asm volatile("s_waitcnt vmcnt(0)" : "={v109}"(ep));
      const uint32_t T = ((uint32_t)__builtin_amdgcn_readfirstlane(ep) + 1u) * 0x9E3779B1u;
      TS(0, 8);
      BAR();
      TS(0, 2);
      u32x4 ut = {utag(T, 0), utag(T, 1), utag(T, 2), utag(T, 3)};
      asm volatile("" : "+v"(ut));
      SLEEP(GSLEEP);
      u32x2 pv;
      pv[1] = gate(ws, go, T);
      TS(0, 3);
      until(latest(pv[1]) + UD);
      TS(0, 7);
      u32x4 uw, ux;
      asm volatile("global_load_dwordx4 %0, %2, %4 sc1\n\tglobal_load_dwordx4 %1, %3, %4 sc1"
                   : "=&v"(uw), "=&v"(ux) : "v"(uo), "v"(uo + 64 * UP * 32), "s"(ws) : "memory");
      __builtin_amdgcn_s_setprio(FPR);
      asm volatile("s_waitcnt vmcnt(1)" : "+v"(uw));
      uw ^= ut;
      *(u32x4*)(smem + ul + 16) = uw;
      asm volatile("s_waitcnt vmcnt(0)" : "+v"(ux));
      TS(0, 11);
      ux ^= ut;
      *(u32x4*)(smem + ul + 2048 + 16) = ux;
      if (__builtin_expect(__any(((uw[0] | uw[1] | uw[2] | uw[3] | ux[0] | ux[1] | ux[2] | ux[3]) & UMASK) != 0u), 0)) {
        u_again(uw, ut, uo);
        u_again(ux, ut, uo + 64 * UP * 32);
        *(u32x4*)(smem + ul + 16) = uw;
        *(u32x4*)(smem + ul + 2048 + 16) = ux;
      }
      BAR();
      TS(0, 4);
      const u32x4 pw = *(const u32x4*)(smem + ul);
      if (wl < 2) {
        const u32x4 px = *(const u32x4*)(smem + ul + 2048);
        sums<true>(smem + ug, pw, uw);
        sums<false>(smem + ug + 2048, px, ux);
      } else sums<true>(smem + ug, pw, uw);
      count_up(lb0 + O_FL + 32);
      __builtin_amdgcn_s_setprio(SPR);
      TS(0, 5);
      const bool other = *(const uint32_t*)(smem + O_FL + 40 + 4 * (lane & 1)) != S16;
      bool second = false;
#pragma unroll
      for (int k = 0; k < 2; ++k) {
        const uint32_t o = ((k ? 0x3247u : 0x1065u) >> (4 * wl) & 7u) * 1024u;
        u32x4 v;
#pragma unroll 1
        for (int spin = 0; spin < SPIN_CAP; ++spin) {
          asm volatile("ds_read_b128 %0, %1\n\ts_waitcnt lgkmcnt(0)" : "=&v"(v) : "v"(so + o) : "memory");
          if (!__any(max4(v) == 0xffffffffu)) break;
          __builtin_amdgcn_s_sleep(VPS);
        }
        TS(0, 12 + k);
        second |= v_put(v, ut, other, vo + o);
        TS(0, 10 - 4 * k);
      }
      if (__builtin_expect(second, 0)) v_end(T, 4 * tg + wl);
    }
    TSE();
  } else {
  uint32_t* cand = (uint32_t*)(smem + LM);
  uint32_t* sel = cand + 128;
  uint32_t* kl = sel + 48;
  uint32_t* cnt = sel + 64;
  const uint32_t ca = lb0 + LM + 4 * (128 + 64);
  u16* hm = (u16*)(cnt + 12);
  uint32_t T;
  const bool ffn = (FFN < 0) ? bj < 18 : FFN, isx = FFN != 1 && bj >= 24;
  const int fb = 18 * bx + bj;
  constexpr bool ln2 = ROLE == 2;
  const int g0 = tg, g2 = tg;
  u32x4 bw[18];
#pragma unroll
  for (int s = 0; s < 9; ++s) bw[s] = (u32x4){0u, 0u, 0u, 0u};
  int tid2 = threadIdx.x;
  uint32_t ts1 = 0u;
  auto rest = [&] {
    if (ts1 && (int)((uint32_t)__builtin_amdgcn_s_memrealtime() - ts1) > 0) { TS(0, 8); until(ts1 + TSTOP + RD); if (RW) gwait(ca, 4); ts1 = 0u; }
  };
  auto gu_dma = [&](int q0, int q1, auto depth) {
    const char* gs = (const char*)((w < 2) ? wg : wu) + (size_t)((144 * (w & 1)) * (F_ * 2) + fb * 64);
    const uint32_t gv = (uint32_t)((lane >> 2) * (F_ * 2) + 16 * ((lane & 3) ^ (lane >> 5) ^ (w & 2)));
#pragma unroll 1
    for (int q = q0; q < q1; ++q) {
      const int k = (int)((((bj & 1) ? ORD1 : ((unsigned __int128)0x876543210u << 36 | 0x876543210ull)) >> (4 * (q - (q >= 9 ? 9 : 0)) + (q >= 9 ? 36 : 0))) & 15u), h = q >= 9;
      if (q >= RQ) rest();
      dma16(gs + (size_t)(288 * h + 16 * k) * (F_ * 2), gv, lb0 + LW * h + 9216 * w + 1024 * k);
      asm volatile("s_waitcnt vmcnt(%0)" : : "n"(depth.value) : "memory");
    }
  };
  auto down_dma = [&](int k0, int k1, int lane) {
#pragma unroll 1
    for (int k = k0; k < k1; ++k) {
      const int o = 9216 * wl + 2304 * k;
      const char* ds = (const char*)wd + (size_t)(fb * 36864 + o);
      const uint32_t dl = lb0 + o + 32 * ((4 * wl + k + 1) >> 1);
      if (ROLE == 1) rest();
      dma16d(ds, 16u * lane, dl);
      dma16d(ds + 1024, 16u * lane, dl + 1024);
      uint32_t keep;
      asm volatile("s_mov_b32 %0, m0\n\ts_mov_b32 m0, %3\n\ts_mov_b64 exec, 0xffff\n\tglobal_load_lds_dwordx4 %1, %2" DPOL2 "\n\ts_mov_b64 exec, -1\n\ts_mov_b32 m0, %0\n\ts_waitcnt vmcnt(%4)"
                   : "=&s"(keep) : "v"(16u * lane), "s"(ds + 2048), "s"(dl + 2048), "n"(ROLE == 1 ? DDEPTH : 40) : "memory");
    }
  };
  auto gu_read = [&](int h) {
    const uint32_t ra = lb0 + LW * h + (uint32_t)(((lane >> 1) & 1) * 18432 + (8 * kq + ((lane >> 2) & 3)) * 64 + ((w ^ (kq & 1) ^ (lane & 2)) * 16) + 8 * (lane & 1));
#pragma unroll
    for (int s = 0; s < 9; ++s) {
      const u32x2 h0 = trd(ra + 2048 * s), h1 = trd(ra + 2048 * s + 256);
      bw[9 * h + s] = (u32x4){h0[0], h0[1], h1[0], h1[1]};
    }
#pragma unroll
    for (int s = 0; s < 9; ++s) asm volatile("" : "+v"(bw[9 * h + s]));
  };
  uint32_t tv = 0u, tk = 0u;
  const uint32_t vearly = (uint32_t)((VEARLY >> (8 * (g0 >> 5))) & 0xffull);
#define TOUCH(pa) asm volatile("global_load_dword v126, %0, off" DPOL4 : : "v"(pa) : "memory", "v126")
  uint32_t wv[5], xv[9];
  auto rows_xw = [&](int lane) {
    u32x4 wa, xr, xs;
    const uint32_t xo = (uint32_t)((8 * g2 + 2 * wl) * (D_ * 2));
    asm volatile("global_load_dwordx4 %0, %5, %9\n\tglobal_load_dword %1, %6, %9 offset:1024\n\t"
                 "global_load_dwordx4 %2, %7, %10\n\tglobal_load_dwordx4 %3, %7, %10 offset:1152\n\tglobal_load_dword %4, %8, %10 offset:1024\n\ts_waitcnt vmcnt(0)"
                 : "=&v"(wa), "=&v"(wv[4]), "=&v"(xr), "=&v"(xs), "=&v"(xv[8])
                 : "v"(16u * lane), "v"(4u * (lane & 31)), "v"(xo + 16u * lane), "v"(xo + 4u * lane + 32u * (lane & 32)), "s"(wr), "s"(x) : "memory");
#pragma unroll
    for (int k = 0; k < 4; ++k) { wv[k] = wa[k]; xv[k] = xr[k]; xv[4 + k] = xs[k]; }
  };
  auto ln1 = [&](int g, int o, uint32_t c0) {
    uint32_t uo = u_at(g, 64 * wl + lane);
    uint32_t ta = lb0 + (uint32_t)(o + ((lane >> 2) & 3) * RP + (64 * wl + 16 * kq + 4 * (lane & 3)) * 2);
    uint32_t la = (uint32_t)(o + 2 * wl * RP + 16 * lane), lt = (uint32_t)(o + (2 * wl + (lane >> 5)) * RP + 1024 + 4 * (lane & 31));
    uint32_t sg = O_SU + (uint32_t)(256 * (lane >> 3) + g) * 128u + 16u * (lane & 7);
    asm volatile("" : "+v"(uo), "+v"(ta), "+v"(la), "+v"(lt), "+v"(sg));
    f32x4 z4 = {0.f, 0.f, 0.f, 0.f};
    asm volatile("" : "+v"(z4));
    asm volatile("s_waitcnt vmcnt(0)" : "={v[100:103]}"(xa), "={v[104:107]}"(xc), "={v108}"(r0), "={v109}"(ep));
    TS(0, 1);
    const uint32_t v[9] = {xa[0], xa[1], xa[2], xa[3], xc[0], xc[1], xc[2], xc[3], r0};
    uint32_t c[9];
    f32x2 tf[9];
    T = ((uint32_t)__builtin_amdgcn_readfirstlane(ep) + 1u) * 0x9E3779B1u;
    if (wl == 3) { const uint32_t now = (uint32_t)__builtin_amdgcn_s_memrealtime() + SD; ST16(sg, ((u32x4){T, now, T, now})); }
    const uint32_t tj = ((T >> (3 * wl)) & 0x00070007u) | (((T >> (12 + wl)) & 0x00010001u) << 14);
    uint32_t tw[3] = {(tj & 0xffffu) * 0x10001u, (tj >> 16) * 0x10001u, 0u};
    tw[2] = (lane < 32) ? tw[0] : tw[1];
    asm volatile("" : "+v"(tw[0]), "+v"(tw[1]), "+v"(tw[2]));
    f32x2 rc, nc;
    float e_;
    ln_stats<false>(v, lane, 0.03125f, tf, z4, rc, nc, e_);
    ln_apply<true>(tf, lane, rc, nc, c);
#pragma unroll
    for (int k = 0; k < 9; ++k) c[k] = (c[k] & ~UMASK) | tw[k >> 2];
    *(u32x4*)(smem + la) = (u32x4){c[0], c[1], c[2], c[3]};
    *(u32x4*)(smem + la + RP) = (u32x4){c[4], c[5], c[6], c[7]};
    *(uint32_t*)(smem + lt) = c[8];
    u32x4 ha, hb;
    {
      uint32_t t, cc, n = SPIN_CAP;
      asm volatile("s_mov_b64 exec, 1\n\tv_mov_b32 %2, 1\n\tds_add_u32 %5, %2\n\ts_mov_b64 exec, -1\n"
                   "1:\n\tds_read_b32 %2, %5\n\tds_read_b64_tr_b16 v[40:41], %6\n\tds_read_b64_tr_b16 v[42:43], %6 offset:%7\n\tds_read_b64_tr_b16 v[44:45], %6 offset:512\n\t"
                   "ds_read_b64_tr_b16 v[46:47], %6 offset:%8\n\ts_sub_u32 %4, %4, 1\n\ts_cbranch_scc1 2f\n\ts_waitcnt lgkmcnt(4)\n\tv_readfirstlane_b32 %3, %2\n\t"
                   "s_cmp_lt_u32 %3, 4\n\ts_cbranch_scc1 1b\n2:\n\ts_waitcnt lgkmcnt(0)"
                   : "=&{v[40:43]}"(ha), "=&{v[44:47]}"(hb), "=&v"(t), "=&s"(cc), "+s"(n) : "v"(c0), "v"(ta), "n"(4 * RP), "n"(512 + 4 * RP) : "memory", "scc");
    }
    ST16(uo, ha);
    ST16(uo + 64 * 128, hb);
    if (wl == 0) {
      const u32x2 h4 = trd(ta + 1024), h5 = trd(ta + 1024 + 4 * RP);
      ST16(uo + 128 * 128, ((u32x4){h4[0], h4[1], h5[0], h5[1]}));
    }
  };
  const int dX = 512 - 24 + 8 * bx + bj;
  if constexpr (ROLE == 2) {
    const int tl = tid - 256;
    if (isx) {
      __builtin_amdgcn_s_setprio(QPRIO0);
      *(u32x4*)(smem + X_TB + 16 * tl) = zero4;
      *(u32x4*)(smem + X_TB + 16 * min(256 + tl, (TB + C_U) / 16 - 1)) = zero4;
      const int pr = (wl + XROT) & 3;
      COps e;
      conv_ops(lb0 + X_TB, lb0 + X_UL, lb0 + X_IM, lane, e);
      TOps t;
      table_ops(lb0 + X_IM, lb0 + X_TB, wl & 1, lane, t);
      e.o = lb0 + X_ST + (uint32_t)(16 * ((2 * i + (kq >> 1)) ^ ((i >> 2) & 1)) + 8 * (kq & 1));
      e.f = ca + 28 - 32;
      asm volatile("" : "+v"(e.o), "+v"(e.f));
      float ldt, are, aim, bre, bim, cre, cim, dsk;
      P_WAIT(0, , "v103", "v104", "v105", "v106", "v107", "v109");
      asm volatile("" : "={v109}"(ep));
      T = ((uint32_t)__builtin_amdgcn_readfirstlane(ep) + 1u) * 0x9E3779B1u;
      P_MORE();
      asm volatile("v_lshlrev_b32 %0, 16, v107" : "=v"(dsk) : : "v107");
      TS(0, 1);
      __builtin_amdgcn_s_setprio(QPRIO);
      int* evp = (int*)(sel + 44);
      char* im = smem + X_IM + IP * (tl & 127);
      evp[wl] = (wl < 2) ? gen_y(ldt, are, aim, bre, bim, cre, cim, lb0 + X_IM + IP * tl, [] {} TSA) : gen_x(ldt, are, aim, im);
      TS(0, 2);
      __builtin_amdgcn_s_setprio(QPRIO2);
      gsync(ca + 8, 4);
      if (wl < 2) evp[-4 + wl] = (int)table_tile(t, wl & 1, lane, lb0 + LM + 4 * (128 + 44), dsk TSA);
      if (wl < 2) gsync0(ca + 8, 8); else { count_up(ca + 8); SLEEP(XSL); gwait(ca + 8, 8); }
      const uint32_t cs = (uint32_t)__builtin_amdgcn_readfirstlane(evp[-4]);
      if (wl == 3) *(u32x4*)(smem + X_IM + pad_at(lane & 7)) = zero4;
      TS(0, 3);
      CONV_RUN(2 + pr, e, , "+{v11}"(tid2));
      __builtin_amdgcn_s_setprio(3);
      {
        const int lane = tid2 & 63;
        const u32x4 v = *(const u32x4*)(smem + X_ST + 2048 * pr + 1024 * (lane >> 5) + 16 * ((lane & 31) ^ ((lane >> 3) & 1)));
        if (__builtin_expect(v_put(v, (u32x4){utag(T, 0), utag(T, 1), utag(T, 2), utag(T, 3)}, cs != S16, v_at(dX, 64 * pr + lane)), 0)) {
          DRAIN();
          if (lane == 0) ST4(O_DN + 16u * (4 * (dX - 256) + pr), T);
        }
        if (pr == 0 && lane == 0) asm volatile("global_store_dwordx2 %0, %1, %2 sc1" : : "v"(O_SC + 16u * dX), "v"((u32x2){T, cs}), "s"(ws) : "memory");
      }
      TS(0, 6);
    }
    if (!isx) __builtin_amdgcn_s_setprio(PPRIO);
    asm volatile("" : "+v"(tid2));
    if (!isx) rows_xw(tid2 & 63);
    if (!isx && !ffn) {
      SLEEP(RSLEEP);
#pragma unroll
      for (int k = 0; k < 7; ++k) ST16(O_SL + (uint32_t)((6 * bx + bj - 18) * 1792 + 256 * k - 256 + tid2) * 16u, ones);
      DRAIN();
    }
    if (!isx) BAR();
    const uint32_t tp = (uint32_t)__builtin_amdgcn_readfirstlane(cnt[4]);
    if (!isx) T = (uint32_t)__builtin_amdgcn_readfirstlane(sel[43]);
    tv = tp + UD + VD2 - vearly;
  } else {
    __builtin_amdgcn_s_setprio(3);
    if (tid < 12) cnt[tid] = 0u;
    BAR();
    if (tid < 20) ((uint32_t*)(hm + 320))[tid] = 0u;
    if (tid >= 184) *(u32x4*)(smem + L_SC - 16 * 184 + 16 * tid) = (u32x4){0x41804180u, 0x41804180u, 0x41804180u, 0x41804180u};
    uint32_t sink = 0;
    ln1(tg, 0, ca + 12);
    TS(0, 2);
    __builtin_amdgcn_s_setprio(NPRIO);
    asm volatile("s_waitcnt vmcnt(0)" : "+v"(sink) : : "memory");
    TS(0, 13);
    if (w == 0) {
      const uint32_t xr = O_XC + (uint32_t)((511 - 2 * (g0 + w)) * 144 + lane) * 16u;
      ST16(xr, ones);
      ST16(xr + 1024u, ones);
      if (lane < 16) ST16(xr + 2048u, ones);
    }
    if (w == 2 && b < 72 && lane < 8) ST16(O_FB + (uint32_t)(8 * b + lane) * 16u, ones);
    if (isx) {
      rows_xw(lane);
      char* sp = smem + X_RW(w) + 16 * lane;
      *(u32x4*)sp = (u32x4){xv[0], xv[1], xv[2], xv[3]};
      *(u32x4*)(sp + 1024) = (u32x4){xv[4], xv[5], xv[6], xv[7]};
      *(u32x2*)(smem + X_RW(w) + 2048 + 8 * lane) = (u32x2){xv[8], wv[4]};
      if (w == 0) *(u32x4*)(smem + X_WR + 16 * lane) = (u32x4){wv[0], wv[1], wv[2], wv[3]};
    }
    SLEEP(ESLEEP);
    uint32_t tu = latest(gate(ws, O_SU + (uint32_t)(256 * bx + 64 * w + 4 * i + (bj & 3)) * 128u, T));
    asm volatile("s_mov_b64 exec, 1\n\tds_max_u32 %0, %1\n\ts_mov_b64 exec, -1" : : "v"(ca + 16), "v"(tu) : "memory");
    if (w == 0) sel[43] = T;
    if (isx) {
      const uint32_t xo = u_at(tid, dX);
      char* sm = smem + X_UL + C_U - 16 + 16 * tid;
      const u32x4 ut = {utag(T, 0), utag(T, 1), utag(T, 2), utag(T, 3)};
      __builtin_amdgcn_s_setprio(3);
      until(tu + UD + XD);
      u32x4 uw;
      asm volatile("global_load_dwordx4 %0, %1, %2 sc1" : "=&v"(uw) : "v"(xo), "s"(ws) : "memory");
      asm volatile("s_waitcnt vmcnt(0)" : "+v"(uw));
      uw ^= ut;
      u_again(uw, ut, xo);
      *(u32x4*)(sm + 16) = uw;
      BAR();
      if (w < 3) {
        const u32x4 pw = *(const u32x4*)sm;
        if (w < 2) sums<true>(smem + X_IM + 16 * tid, pw, uw); else sums<false>(smem + X_IM + 16 * tid, pw, uw);
        count_up(ca + 28);
      }
    } else {
      until(tu + UD + WD);
      BAR();
    }
    tu = (uint32_t)__builtin_amdgcn_readfirstlane(cnt[4]);
    TS(0, 3);
    __builtin_amdgcn_s_setprio(DPRIO);
    if (ffn) {
      ts1 = (tu + UD + VD2 - TSTOP) | 1u;
      gu_dma(0, N0, IC<40>{});
      gu_dma(N0, NA, IC<WDEPTH>{});
      asm volatile("s_waitcnt vmcnt(%0)" : : "n"(NA - 9) : "memory");
      gsync0(ca + 4, 4);
      gu_read(0);
      asm volatile("s_waitcnt lgkmcnt(0)" ::: "memory");
      count_up(ca + 36);
      TS(0, 4);
      gu_dma(NA, 18, IC<WDEPTH2>{});
      gwait(ca + 36, 4);
      down_dma(0, KN, lane);
      asm volatile("s_waitcnt vmcnt(%0)" : : "n"(3 * KN) : "memory");
      count_up(ca + 4);
      TS(0, 7);
    } else {
      if (!isx && C3) {
        const int n = 6 * min(tid, 191) + bj - 18, r = n >> 2;
        const char* wb = (const char*)((n & 1) ? wu : wg) + bx * 1152;
        const int c0 = (3 - (int)((uintptr_t)wb >> 7)) & 7;
        wb += (uint32_t)((r + 144 * ((r >= 144) + ((n >> 1) & 1))) * (F_ * 2) + 128 * c0);
        TOUCH(wb);
        if (c0 == 0) TOUCH(wb + 1024);
      }
      auto d_line = [&](int k) {
        const int n = min((k * 14 + bj - 18) * 64 + lane, 5183), q = n / 1296, r = n - 1296 * q;
        return (const char*)wd + (uint32_t)(bx * 5184 + (r / 18) * 72 + 18 * (HREV ? 3 - q : q) + r % 18) * 128u;
      };
      int k = w;
      if (w < HW) {
#pragma unroll 1
        for (; k < 6 && (int)((uint32_t)__builtin_amdgcn_s_memrealtime() - (tu + UD + VD2 - HSTOP)) < 0; k += HW) { TOUCH(d_line(k)); DRAIN(); }
      }
      TS(0, 4);
      until(tu + UD + VD2 + RD2);
      if (RW) gwait(ca, 4);
      if (w < HW) {
#pragma unroll 1
        for (; k < 6; k += HW) { TOUCH(d_line(k)); DRAIN(); }
      }
      TS(0, 7);
    }
    tv = tu + UD + VD2 - vearly;
    tk = tu + UD + VD2 + KD;
  }
  {
  const int tid = tid2, lane = tid & 63, i = lane & 15, kq = lane >> 4;
  __builtin_amdgcn_s_setprio(3);
  const int PL = ffn ? 6400 : LW + 6656;
  f32x2 xf[9];
  f32x4 za = {0.f, 0.f, 0.f, 0.f}, zb = za;
  uint32_t al, ha, h0a, h1a, so, da;
  auto ffn_terms = [&] {
    al = O_XC + 64u * (i >> 3) + 16u * kq + 128u * w;
    ha = (ROLE == 1) ? (uint32_t)((4 * (kq & 1) + 2 * (kq >> 1) + (i >> 3)) * 80 + 16 * w + 2 * (i & 7)) : 0u;
    h0a = (uint32_t)(min(i, 8) * 80 + 16 * kq); h1a = (uint32_t)(min(i ^ 8, 8) * 80 + 16 * kq);
    so = O_SL + (uint32_t)fb * SP + ((ROLE == 2) ? 16u * tid - 4096u : 8192u + 16u * lane);
    da = lb0 + (uint32_t)((8 * kq + ((lane >> 2) & 3)) * 1152 + 32 * ((8 * kq + ((lane >> 2) & 3) + 2) >> 2) + 8 * (lane & 3) + ((ROLE == 2) ? 128 * wl : 1024));
    if (ROLE == 2) asm volatile("" : "+v"(h0a), "+v"(h1a), "+v"(so), "+v"(da)); else asm volatile("" : "+v"(ha), "+v"(al), "+v"(da));
  };
  if (ROLE == 1) ffn_terms();
  uint32_t hv[9] = {}, nv[9] = {}, keya = 0u, keyb = 0u;
  int rka = 0, rkb = 0;
  auto put_row = [&](const uint32_t* r, int j, const void* base, uint32_t ro) {
    const u32x4 tl = {r[8], (uint32_t)DPP_I((int)r[8], 0x55, 0xf), (uint32_t)DPP_I((int)r[8], 0xaa, 0xf), (uint32_t)DPP_I((int)r[8], 0xff, 0xf)};
    asm volatile("global_store_dwordx4 %0, %1, %2 sc1\n\ts_nop 1" : : "v"(ro + 16u * lane), "v"((u32x4){r[4 * j], r[4 * j + 1], r[4 * j + 2], r[4 * j + 3]}), "s"(base) : "memory");
    if ((lane & 35) == 32 * j) asm volatile("global_store_dwordx4 %0, %1, %2 offset:1024 sc1\n\ts_nop 1" : : "v"(ro + 4u * (lane & 31)), "v"(tl), "s"(base) : "memory");
  };
  if (ln2) {
    const int tl = tid - 256, d1 = 64 * (tl >> 5) + (tl & 31), d3 = 64 * ((tl >> 4) & 7) + 32 + (tl & 15), d2 = (tl < 128) ? d3 + 16 : min(384 + tl, 575);
    uint32_t vo = v_at(d1, g0), vo2 = v_at(d2, g0), vo3 = v_at(d3, g0);
    u32x4 ut = {utag(T, 0), utag(T, 1), utag(T, 2), utag(T, 3)};
    u32x4 va, vc = ut, vb = ut;
    asm volatile("" : "+v"(vo), "+v"(vo2), "+v"(vo3), "+v"(ut), "+v"(vc), "+v"(vb));
    TS(0, 7);
    until(tv);
    TS(0, 8);
    asm volatile("global_load_dwordx4 %0, %2, %4 sc1\n\ts_cmp_lt_u32 %5, 2\n\ts_cbranch_scc1 1f\n\tglobal_load_dwordx4 %1, %3, %4 sc1\n1:"
                 : "=&v"(va), "+v"(vc) : "v"(vo), "v"(vo3), "s"(ws), "s"(wl) : "memory", "scc");
    until(tv + V2D);
    asm volatile("s_cmp_eq_u32 %3, 3\n\ts_cbranch_scc1 1f\n\tglobal_load_dwordx4 %0, %1, %2 sc1\n1:" : "+v"(vb) : "v"(vo2), "s"(ws), "s"(wl) : "memory", "scc");
    __builtin_amdgcn_sched_barrier(0);
    if (isx) {
      const u32x4 ra = *(const u32x4*)(smem + X_RW(wl) + 16 * lane), rb = *(const u32x4*)(smem + X_RW(wl) + 1024 + 16 * lane), rw = *(const u32x4*)(smem + X_WR + 16 * lane);
      const u32x2 rc = *(const u32x2*)(smem + X_RW(wl) + 2048 + 8 * lane);
#pragma unroll
      for (int k = 0; k < 4; ++k) { xv[k] = ra[k]; xv[4 + k] = rb[k]; wv[k] = rw[k]; }
      xv[8] = rc[0]; wv[4] = rc[1];
    }
    uint32_t pa = (uint32_t)(PL + 9216 * wl + 32 * lane), pb = (uint32_t)(PL + 9216 * wl + 2048 + 8 * (lane & 31)), ps = (lane < 32) ? 0u : 16u;
    uint32_t kid = (uint32_t)(((255 - g2) << 5) | (7 - 2 * wl - (lane >> 5)));
    uint32_t px = (uint32_t)(PL + 4 * d1), py = (uint32_t)(PL + 4 * d2), pz = (uint32_t)(PL + 4 * d3);
#pragma unroll
    for (int k = 0; k < 9; ++k) xf[k] = (f32x2){bflo(xv[k]), bfhi(xv[k])};
    asm volatile("" : "+v"(xf[0]), "+v"(xf[1]), "+v"(xf[2]), "+v"(xf[3]), "+v"(xf[4]), "+v"(xf[5]), "+v"(xf[6]), "+v"(xf[7]), "+v"(xf[8]));
    asm volatile("" : "+v"(za), "+v"(zb), "+v"(pa), "+v"(pb), "+v"(ps), "+v"(kid), "+v"(px), "+v"(py), "+v"(pz));
    float sv[10], ws_ = dot2(wv[4], 0x3f803f80u, 0.f) * 0.5f;
#pragma unroll
    for (int k = 0; k < 4; ++k) ws_ = dot2(wv[k], 0x3f803f80u, ws_);
    ws_ = wave_sum(ws_);
#pragma unroll
    for (int k = 0; k < 10; ++k) sv[k] = 16.f;
    uint32_t ka_ = lb0 + LM + 4u * (128 + 48 + 2 * wl + (lane >> 5)), kb_ = lb0 + LM + 4u * (128 + 48);
    uint32_t ko_ = O_KEY + (uint32_t)(256 * (lane & 7) + 8 * (g2 & 31) + (g2 >> 5)) * 16u;
    asm volatile("" : "+v"(ws_), "+v"(sv[0]), "+v"(sv[1]), "+v"(sv[2]), "+v"(sv[3]), "+v"(sv[4]), "+v"(sv[5]), "+v"(sv[6]), "+v"(sv[7]), "+v"(sv[8]), "+v"(sv[9]),
                 "+v"(ka_), "+v"(kb_), "+v"(ko_));
    if (ffn) gwait(ca + 36, 4);
    asm volatile("s_waitcnt vmcnt(1)\n\ts_cmp_eq_u32 %2, 3\n\ts_cbranch_scc0 1f\n\ts_waitcnt vmcnt(0)\n1:" : "+v"(va), "+v"(vc) : "s"(wl) : "memory", "scc");
    auto turn1 = [&] {
#pragma unroll
      for (int e = 0; e < 4; ++e) *(uint32_t*)(smem + px + 9216 * e) = va[e];
      if (wl >= 2) {
#pragma unroll
        for (int e = 0; e < 4; ++e) *(uint32_t*)(smem + pz + 9216 * e) = vc[e];
      }
    };
    auto turn = [&] {
      if (wl < 3) {
#pragma unroll
        for (int e = 0; e < 4; ++e) *(uint32_t*)(smem + py + 9216 * e) = vb[e];
      }
    };
    va ^= ut; vc ^= ut;
    turn1();
    asm volatile("s_waitcnt vmcnt(0)" : "+v"(vb) : : "memory");
    vb ^= ut;
    turn();
    const u32x4 vs = va | vb | vc;
    if (__builtin_expect(__any(((vs[0] | vs[1] | vs[2] | vs[3]) & UMASK) != 0u), 0)) {
#pragma unroll 1
      for (int spin = 0; spin < 4; ++spin) {
        const bool na = ((va[0] | va[1] | va[2] | va[3]) & UMASK) != 0u, nb = ((vb[0] | vb[1] | vb[2] | vb[3]) & UMASK) != 0u, nc = ((vc[0] | vc[1] | vc[2] | vc[3]) & UMASK) != 0u;
        if (!__any(na || nb || nc)) break;
        TSC(16);
        if (na) LD16(va, vo);
        if (nb) LD16(vb, vo2);
        if (nc) LD16(vc, vo3);
        asm volatile("s_waitcnt vmcnt(0)" : "+v"(va), "+v"(vb), "+v"(vc) : : "memory");
        if (na) va ^= ut;
        if (nb) vb ^= ut;
        if (nc) vc ^= ut;
      }
      auto again = [&](u32x4& v, uint32_t o, int d) {
        const int J = g0 >> 5;
        const uint32_t dn = O_DN + 16u * (uint32_t)((d < 512) ? 4 * (d >> 1) + ((0x01013232 >> (4 * J)) & 3) : 4 * (d - 256) + (J >> 1));
        bool pend = ((v[0] | v[1] | v[2] | v[3]) & UMASK) != 0u;
#pragma unroll 1
        for (int spin = 0; spin < SPIN_CAP && __any(pend); ++spin) {
          TSC(16);
          SLEEP(PSLEEP);
          if (pend) {
            uint32_t fin = ~T;
            if ((v[0] & v[1] & v[2] & v[3] & LMASK) == LMASK)
              asm volatile("global_load_dword %0, %1, %2 sc1\n\ts_waitcnt vmcnt(0)" : "=&v"(fin) : "v"(dn), "s"(ws) : "memory");
            LD16W(v, o);
            v ^= ut;
            pend = ((v[0] | v[1] | v[2] | v[3]) & UMASK) != 0u;
            if (pend && fin == T) {
              u32x2 sc;
              for (int k = 0; k < SPIN_CAP; ++k) {
                asm volatile("global_load_dwordx2 %0, %1, %2 sc1\n\ts_waitcnt vmcnt(0)" : "=&v"(sc) : "v"(O_SC + 16u * d), "s"(ws) : "memory");
                if (sc[0] == T) break;
              }
              *(u16*)(smem + L_SC + 2 * d) = (u16)(sc[1] >> 16);
              cnt[10] = 1u;
              v = (v ^ ut) & ~LMASK;
              pend = false;
            }
          }
        }
      };
      again(va, vo, d1); again(vc, vo3, d3); again(vb, vo2, d2);
      turn1();
      turn();
    }
    u32x4 a0, a1;
    u32x2 a2, cf;
    {
      uint32_t cc, n = SPIN_CAP;
      asm volatile("s_mov_b64 exec, 1\n\tv_mov_b32 v90, 1\n\tds_add_u32 %6, v90\n\ts_mov_b64 exec, -1\n"
                   "1:\n\tds_read2_b32 %3, %6 offset1:10\n\tds_read_b128 %0, %7\n\tds_read_b128 %1, %7 offset:16\n\tds_read_b64 %2, %8\n\t"
                   "s_sub_u32 %5, %5, 1\n\ts_cbranch_scc1 2f\n\ts_waitcnt lgkmcnt(3)\n\tv_readfirstlane_b32 %4, v90\n\t"
                   "s_cmp_lt_u32 %4, 4\n\ts_cbranch_scc1 1b\n2:\n\ts_waitcnt lgkmcnt(0)"
                   : "=&v"(a0), "=&v"(a1), "=&v"(a2), "=&{v[90:91]}"(cf), "=&s"(cc), "+s"(n) : "v"(ca), "v"(lb0 + pa), "v"(lb0 + pb) : "memory", "scc");
    }
    TS(0, 9);
    if (__builtin_expect(__builtin_amdgcn_readfirstlane(cf[1]) != 0, 0)) {
      const u32x4 q = *(const u32x4*)(smem + L_SC + 16 * lane);
      const uint32_t r = *(const uint32_t*)(smem + L_SC + 1024 + 4 * (lane & 31));
#pragma unroll
      for (int k = 0; k < 4; ++k) { sv[2 * k] = bflo(q[k]); sv[2 * k + 1] = bfhi(q[k]); }
      sv[8] = bflo(r); sv[9] = bfhi(r);
    }
    const uint32_t av[10] = {a0[0], a0[1], a0[2], a0[3], a1[0], a1[1], a1[2], a1[3], a2[0] >> ps, a2[1] >> ps};
    f32x2 h[9];
#pragma unroll
    for (int k = 0; k < 10; ++k) {
      asm("v_fma_mix_f32 %0, %1, %2, %3 op_sel_hi:[1,0,0]" : "=v"(h[(k < 8) ? k >> 1 : 8][k & 1]) : "v"(av[k]), "v"(sv[k]), "v"(xf[(k < 8) ? k >> 1 : 8][k & 1]));
      if (k < 8) asm("v_fma_mix_f32 %0, %1, %2, %3 op_sel:[1,0,0] op_sel_hi:[1,0,0]" : "=v"(h[4 + (k >> 1)][k & 1]) : "v"(av[k]), "v"(sv[k]), "v"(xf[4 + (k >> 1)][k & 1]));
    }
#pragma unroll
    for (int k = 0; k < 9; ++k) hv[k] = pack2(h[k][0], h[k][1]);
    const float da = dot2(hv[2], wv[2], dot2(hv[0], wv[0], zb[0])) + dot2(hv[3], wv[3], dot2(hv[1], wv[1], zb[1]));
    const float db = dot2(hv[6], wv[2], dot2(hv[4], wv[0], zb[2])) + dot2(hv[7], wv[3], dot2(hv[5], wv[1], zb[3]));
    float dt = dot2(hv[8], wv[4], fold32(da, db));
    f32x2 rc, nc;
    ln_stats<true>(hv, lane, 1.f, xf, za, rc, nc, dt);
    dt = (lane < 32) ? dt * rc[0] + ws_ * nc[0] : dt * rc[1] + ws_ * nc[1];
    const uint32_t bb = pack2(dt, dt) & 0xffff0000u;
    const uint32_t kk = (bb ^ (((uint32_t)((int)bb >> 31) & 0x7fff0000u) | 0x80000000u)) | kid;
    u32x4 ka, kb;
    uint32_t old;
    asm volatile("s_mov_b64 exec, %5\n\tds_write_b32 %3, %4\n\ts_mov_b64 exec, 1\n\tv_mov_b32 %2, 1\n\tds_add_rtn_u32 %2, %7, %2\n\ts_mov_b64 exec, -1\n\t"
                 "ds_read_b128 %0, %6\n\tds_read_b128 %1, %6 offset:16\n\ts_waitcnt lgkmcnt(0)"
                 : "=&v"(ka), "=&v"(kb), "=&v"(old) : "v"(ka_), "v"(kk), "s"(0x8000000080000000ull), "v"(kb_), "v"(ca) : "memory");
    if (__builtin_amdgcn_readfirstlane(old) == 7) {
      uint32_t m1 = max(max(ka[0], ka[1]), ka[2]), m2 = med3u(ka[0], ka[1], ka[2]), m3 = min(min(ka[0], ka[1]), ka[2]);
#pragma unroll
      for (int e = 0; e < 5; ++e) {
        const uint32_t kx = e ? kb[e - 1] : ka[3];
        m3 = med3u(m2, m3, kx); m2 = med3u(m1, m2, kx); m1 = max(m1, kx);
      }
      if (lane < 8) ST16(ko_, ((u32x4){m1 | 24u, m2 | 16u, m3 | 8u, T & ~1u}));
    }
    TS(0, 10);
    keya = (uint32_t)__builtin_amdgcn_readlane(kk, 31); keyb = (uint32_t)__builtin_amdgcn_readlane(kk, 63);
    ln_apply<false>(xf, lane, rc, nc, nv);
    gwait(ca, 8);
    if (wl == 1) {
      const u32x4 kv = *(const u32x4*)&kl[4 * (lane & 1)];
      if (lane < 2) ST16(O_KF + (uint32_t)(2 * g2 + lane) * 16u, ((u32x4){__builtin_amdgcn_perm(kv[1], kv[0], 0x07060302u), T, __builtin_amdgcn_perm(kv[3], kv[2], 0x07060302u), T}));
      if (ROLE == 2 && b == 0 && lane == 0) ST4(O_EP, T * 0x0E8B2F51u);
    }
    const uint32_t ko = kl[lane & 7];
    rka = FORCEFB + __popcll(__ballot(ko > keya) & 0xffull); rkb = FORCEFB + __popcll(__ballot(ko > keyb) & 0xffull);
#pragma unroll
    for (int k = 0; k < 9; ++k) {
      const f32x2 o = xf[k] + (f32x2){bflo(nv[k]), bfhi(nv[k])};
      hv[k] = pack2(o[0], o[1]);
      nv[k] = min(nv[k], 0xfffffffeu);
    }
    if (rka < 2) put_row(nv, 0, ws, O_XC + (uint32_t)(1023 - 4 * g2 - rka) * 1152u);
    if (rkb < 2) put_row(nv, 1, ws, O_XC + (uint32_t)(1023 - 4 * g2 - rkb) * 1152u);
    if (!LPOS && ffn) down_dma(KN, 4, lane);
    ffn_terms();
  }
  uint32_t k3rd = 0u, k4th = 0u;
  uint32_t q0 = lb0 + LM + 4u * lane, q1 = lb0 + LM + 32u * w, q2 = q1 + 4u * (lane & 7), q3 = lb0 + LM + 512u + 4u * (lane & 7), q4 = lb0 + LM + (uint32_t)(tid & ~7);
  uint32_t r8 = C_, sb = lb0 + LM + 512u;
  asm volatile("" : "+v"(q0), "+v"(q1), "+v"(q2), "+v"(q3), "+v"(q4), "+v"(r8), "+v"(sb));
  if constexpr (ROLE == 1) {
    u32x4 key;
    const uint32_t kof = O_KEY + (uint32_t)(256 * bx + tid) * 16u;
    until(tk);
    if (!ffn) SLEEP(NSLEEP);
    LD16(key, kof);
    if (ffn) {
      gwait0(ca + 4, 8);
      gu_read(1);
    }
    asm volatile("s_waitcnt vmcnt(0)" : "+v"(key) : : "memory");
    if (__builtin_expect(__any(key[3] != (T & ~1u)), 0)) {
#pragma unroll 1
      for (int spin = 0; spin < SPIN_CAP; ++spin) {
        const bool need = key[3] != (T & ~1u);
        if (!__any(need)) break;
        TSC(15);
        SLEEP(KBACK);
        if (need) LD16W(key, kof);
      }
    }
    TS(0, 11);
    const uint32_t m1 = oct_maxu(key[0]);
    const bool hit = key[0] == m1;
    const uint32_t k0 = hit ? key[1] : key[0], k1 = hit ? key[2] : key[1];
    const uint32_t m2 = oct_maxu(k0);
    *LP(u32x2, q4) = (u32x2){m1, m2};
    k3rd = (k0 == m2) ? k1 : k0;
    k4th = (k0 == m2) ? (hit ? 0u : key[2]) : k1;
    if (RPF == 1 && !ffn && w == (bj & 3)) {
      const uint32_t r1 = O_XC + ((m1 >> 3) & 0x3ffu) * 1152u + 128u * (lane & 7), r2 = O_XC + ((m2 >> 3) & 0x3ffu) * 1152u + 128u * (lane & 7);
      asm volatile("global_load_dword v126, %0, %3 sc1\n\tglobal_load_dword v126, %1, %3 sc1\n\tglobal_load_dword v126, %2, %3 offset:1024 sc1"
                   : : "v"(r1), "v"(r2), "v"(((lane & 1) ? r1 : r2) - 128u * (lane & 7)), "s"(ws) : "memory", "v126");
    }
  }
  auto pick = [&](auto co, auto so_) {
    BAR();
    const uint32_t c = *LP(uint32_t, q0 + co.value), own = *LP(uint32_t, q2 + co.value);
    const u32x4 la = *LP(u32x4, q1 + co.value), lb = *LP(u32x4, q1 + co.value + 16);
    uint32_t r = r8;
    asm("v_cmp_gt_u32 s[40:41], %1, %2\n\tv_cmp_gt_u32 s[42:43], %1, %3\n\tv_cmp_gt_u32 s[44:45], %1, %4\n\tv_cmp_gt_u32 s[46:47], %1, %5\n\t"
        "v_cmp_gt_u32 s[48:49], %1, %6\n\tv_cmp_gt_u32 s[50:51], %1, %7\n\tv_cmp_gt_u32 s[52:53], %1, %8\n\tv_cmp_gt_u32 s[54:55], %1, %9\n\t"
        "s_bcnt1_i32_b64 s40, s[40:41]\n\ts_bcnt1_i32_b64 s42, s[42:43]\n\tv_writelane_b32 %0, s40, 0\n\ts_bcnt1_i32_b64 s44, s[44:45]\n\t"
        "v_writelane_b32 %0, s42, 1\n\ts_bcnt1_i32_b64 s46, s[46:47]\n\tv_writelane_b32 %0, s44, 2\n\ts_bcnt1_i32_b64 s48, s[48:49]\n\t"
        "v_writelane_b32 %0, s46, 3\n\ts_bcnt1_i32_b64 s50, s[50:51]\n\tv_writelane_b32 %0, s48, 4\n\ts_bcnt1_i32_b64 s52, s[52:53]\n\t"
        "v_writelane_b32 %0, s50, 5\n\ts_bcnt1_i32_b64 s54, s[54:55]\n\tv_writelane_b32 %0, s52, 6\n\tv_writelane_b32 %0, s54, 7"
        : "+v"(r) : "v"(c), "v"(la[0]), "v"(la[1]), "v"(la[2]), "v"(la[3]), "v"(lb[0]), "v"(lb[1]), "v"(lb[2]), "v"(lb[3])
        : "scc", "s40", "s41", "s42", "s43", "s44", "s45", "s46", "s47", "s48", "s49", "s50", "s51", "s52", "s53", "s54", "s55");
    *LP(uint32_t, sb + 4u * min(r, (uint32_t)C_) + so_.value) = own;
    BAR();
  };
  auto rows = [&](uint32_t ao, auto&& meanwhile) {
    u32x4 aa, ab, ac = __builtin_bit_cast(u32x4, zb);
    asm volatile("global_load_dwordx4 %0, %3, %4 sc1\n\tglobal_load_dwordx4 %1, %3, %4 offset:512 sc1\n\t"
                 "s_cmp_lg_u32 %5, 0\n\ts_cbranch_scc1 1f\n\tglobal_load_dwordx4 %2, %3, %4 offset:1024 sc1\n1:"
                 : "=&v"(aa), "=&v"(ab), "+v"(ac) : "v"(ao), "s"(ws), "s"(w) : "memory", "scc");
    __builtin_amdgcn_sched_barrier(0);
    meanwhile();
    asm volatile("s_waitcnt vmcnt(0)" : "+v"(aa), "+v"(ab), "+v"(ac) : : "memory");
    if (__builtin_expect(__any(max(max(max4(aa), max4(ab)), max4(ac)) == 0xffffffffu), 0)) {
#pragma unroll 1
      for (int spin = 0; spin < SPIN_CAP; ++spin) {
        const bool na = max4(aa) == 0xffffffffu, nb = max4(ab) == 0xffffffffu, nc = max4(ac) == 0xffffffffu;
        if (!__any(na || nb || nc)) break;
        TSC(14);
        SLEEP(PSLEEP);
        if (na) LD16W(aa, ao);
        if (nb) LD16W(ab, ao + 512u);
        if (nc) LD16W(ac, ao + 1024u);
      }
    }
    *(u32x4*)(smem + LA + 16 * tid) = aa;
    *(u32x4*)(smem + LA + 4096 + 16 * tid) = ab;
    if (w == 0) *(u32x4*)(smem + LA + 8192 + 16 * tid) = ac;
  };
  auto rows_dma = [&](uint32_t ao) {
    uint32_t dl = lb0 + LA + 1024u * w, keep;
    asm volatile("s_mov_b32 %0, m0\n\ts_mov_b32 m0, %1\n\ts_add_u32 %1, %1, 4096 - 512\n\tglobal_load_lds_dwordx4 %2, %3 sc1\n\ts_mov_b32 m0, %1\n\ts_add_u32 %1, %1, 4096 - 512\n\t"
                 "global_load_lds_dwordx4 %2, %3 offset:512 sc1\n\ts_cmp_lg_u32 %4, 0\n\ts_cbranch_scc1 1f\n\ts_mov_b32 m0, %1\n\ts_nop 0\n\t"
                 "global_load_lds_dwordx4 %2, %3 offset:1024 sc1\n1:\n\ts_mov_b32 m0, %0" : "=&s"(keep), "+s"(dl) : "v"(ao), "s"(ws), "s"(w) : "memory", "scc");
  };
  auto get_bd = [&](int t0, int t1) {
#pragma unroll
    for (int t = t0; t < t1; ++t) {
      const u32x2 h0 = trd(da + 512 * (t >> 2) + 32 * (t & 3)), h1 = trd(da + 512 * (t >> 2) + 32 * (t & 3) + 4608 + 32);
      bw[t] = (u32x4){h0[0], h0[1], h1[0], h1[1]};
    }
#pragma unroll
    for (int t = t0; t < t1; ++t) asm volatile("" : "+v"(bw[t]));
  };
  pick(IC<0>{}, IC<0>{});
  uint32_t mykey = *LP(uint32_t, q3), ro = ((mykey >> 3) & 0x3ffu) * 1152u;
  TS(0, 12);
  auto check = [&] {
    cnt[(k3rd > (uint32_t)__builtin_amdgcn_readlane(mykey, 7)) ? 5 : 6] = 1u;
    const uint32_t m3 = oct_maxu(k3rd);
    if (k3rd == m3) k3rd = k4th;
    if ((lane & 7) == 0) cand[72 + (tid >> 3)] = m3;
    if (w == 0 && lane < 32) cand[(lane < 8) ? 64 + lane : 96 + lane] = (lane < 8) ? mykey : 0u;
  };
  if constexpr (ROLE == 1) {
    if (ffn && !FORCEFB) rows_dma(ro + al);
    check();
    DRAIN();
  } else if (ffn) {
    if (LPOS) { SLEEP(LSLEEP); down_dma(KN, 4, lane); } else { DRAIN(); gsync0(ca, 12); get_bd(0, 8); }
  }
  BAR();
  TS(0, 5);
  auto put_out = [&](int j, uint32_t k0, int rk) {
    const uint64_t mine = __ballot(((mykey ^ k0) & ~24u) == 0u) & 0xffull;
    if (mine == 0ull) put_row(hv, j, out, (uint32_t)((8 * g2 + 2 * wl + j) * (D_ * 2)));
    else if (rk >= 2) put_row(nv, j, ws, O_FB + 1152u * (uint32_t)__builtin_ctzll(mine));
  };
  auto rest_out = [&] { put_out(0, keya, rka); put_out(1, keyb, rkb); };
  f32x4 lo, hi;
  auto gate_up = [&] {
    u32x4 av[5];
    const uint32_t aa = lb0 + LA + 16u * lane;
#pragma unroll
    for (int j = 0; j < 5; ++j) asm volatile("ds_read_b128 %0, %1 offset:%2" : "=v"(av[j]) : "v"(aa), "n"(1024 * j) : "memory");
    lo = (f32x4){0.f, 0.f, 0.f, 0.f}; hi = lo;
#pragma unroll
    for (int j = 0; j < 9; ++j) {
      if (j < 5) asm volatile("s_waitcnt lgkmcnt(4)" : "+v"(av[j % 5]));
      else if (j == 5) asm volatile("s_waitcnt lgkmcnt(2)" : "+v"(av[0]), "+v"(av[1]));
      else if (j == 7) asm volatile("s_waitcnt lgkmcnt(0)" : "+v"(av[2]), "+v"(av[3]));
      lo = MMB(av[j % 5], bw[2 * j], lo);
      hi = MMB(av[j % 5], bw[2 * j + 1], hi);
      __builtin_amdgcn_sched_barrier(0);
      if (j < 4) asm volatile("ds_read_b128 %0, %1 offset:%2" : "=v"(av[j]) : "v"(aa), "n"(1024 * (j + 5)) : "memory");
    }
  };
  uint32_t fl;
  asm volatile("ds_read_b32 %0, %1" : "=v"(fl) : "v"(ca + 20) : "memory");
  if (ROLE == 1) gate_up();
  asm volatile("s_waitcnt lgkmcnt(0)" : "+v"(fl));
  const bool slow = FORCESLOW || FORCEFB || __builtin_amdgcn_readfirstlane(fl) != 0;
  if (__builtin_expect(slow, 0)) {
    TSC(17);
    pick(IC<256>{}, IC<64>{});
    mykey = sel[16 + (lane & 7)];
    ro = ((mykey >> 3) & 0x3ffu) * 1152u;
    if (ROLE == 1 && (k3rd > (uint32_t)__builtin_amdgcn_readlane(mykey, 7) || (mykey & 24u) == 8u)) cnt[11] = 1u;
    BAR();
    if (FORCESLOW > 1 || FORCEFB || __builtin_amdgcn_readfirstlane(cnt[11]) != 0) {
    u32x4 key;
#pragma unroll 1
    for (int spin = 0; spin < SPIN_CAP; ++spin) {
      LD16W(key, O_KF + 16u * tid);
      if (!__any(key[1] != T || key[3] != T)) break;
      SLEEP(KBACK);
    }
    const uint32_t ib = (uint32_t)(((255 - (tid >> 1)) << 5) | (7 - 4 * (tid & 1)));
    uint32_t k0 = (key[0] << 16) | ib, k1 = (key[0] & 0xffff0000u) | (ib - 1), k2 = (key[2] << 16) | (ib - 2), k3 = (key[2] & 0xffff0000u) | (ib - 3);
    CEX(k0, k1) CEX(k2, k3) CEX(k0, k2) CEX(k1, k3) CEX(k1, k2)
#pragma unroll 1
    for (int c = 0; c < C_; ++c) {
      const uint32_t mx = wave_maxu(k0);
      const bool hit = (k0 == mx);
      k0 = hit ? k1 : k0; k1 = hit ? k2 : k1; k2 = hit ? k3 : k2; k3 = hit ? 0u : k3;
      if (lane == 0) cand[64 + 8 * w + c] = mx;
    }
    pick(IC<256>{}, IC<128>{});
    mykey = sel[32 + (lane & 7)];
    const uint32_t okey = sel[32 + (lane >> 3)];
    const uint64_t same = __ballot(((mykey ^ okey) & 0x1fe0u) == 0u && okey > mykey);
    const int sl = FORCEFB + __popcll((same >> (lane & 7)) & 0x0101010101010101ull);
    ro = (sl < 2) ? (((mykey >> 3) & 0x3ffu) + 3 - sl) * 1152u : O_FB - O_XC + 1152u * (lane & 7);
    }
    if (ln2) rest_out();
    if (ffn) {
      if (ROLE == 1) rows(ro + al, [] {});
      BAR();
      if (ROLE == 1) gate_up();
    }
  }
  if (OPOS == 0 && ROLE == 2 && ffn && !slow) { SLEEP(OD); rest_out(); }
  if (!ffn) {
    if (ln2 && !slow) { SLEEP(OD2); rest_out(); }
    TSE();
    return;
  }
  uint32_t fx = 0, fv = 0, fn = 0, fo = 0, sa[9];
  float rs, hx, xs;
  u32x4 se;
  if constexpr (ROLE == 1) {
    f32x4 lh = (lane < 32) ? lo : hi;
    float fa = fold32(lh[0], lh[2]), fb2 = fold32(lh[1], lh[3]);
    if (__builtin_expect(__any(__builtin_isunordered(fa, fb2)) && !slow, 0)) {
      gsync0(ca + 4, 12);
      rows(ro + al, [] {});
      gsync0(ca + 4, 16);
      gate_up();
      lh = (lane < 32) ? lo : hi;
      fa = fold32(lh[0], lh[2]); fb2 = fold32(lh[1], lh[3]);
    }
    const float g = __int_as_float(__builtin_amdgcn_update_dpp(__float_as_int(fa), __float_as_int(fb2), 0x128, 0xf, 0xc, false));
    const float u = __int_as_float(__builtin_amdgcn_update_dpp(__float_as_int(fb2), __float_as_int(fa), 0x128, 0xf, 0x3, false));
    const uint32_t gu = pack2(g, u);
    const float gp = bflo(gu);
    const float sg = rbf(gp * __builtin_amdgcn_rcpf(1.f + __expf(-gp)));
    *(u16*)((char*)hm + ha) = f2bf(sg * bfhi(gu));
  } else if (LPOS) {
    DRAIN();
    gsync0(ca, 12);
    get_bd(0, 8);
  }
  BAR();
  TS(0, 6);
  if (ROLE == 2 || w == 2) {
    const u32x4 a0 = *(const u32x4*)((char*)hm + h0a), a1 = *(const u32x4*)((char*)hm + h1a);
    if (ROLE == 1) get_bd(0, 4);
    const f32x4 z = {0.f, 0.f, 0.f, 0.f};
    f32x4 o[4];
#pragma unroll
    for (int t = 0; t < 2 * ROLE; ++t) o[t] = MMB(a1, bw[2 * t + 1], MMB(a0, bw[2 * t], z));
#pragma unroll
    for (int t = 0; t < ROLE; ++t) {
      u32x4 sv;
#pragma unroll
      for (int r = 0; r < 4; ++r) sv[r] = min(pack2(o[2 * t][r], o[2 * t + 1][r]), 0xfffffffeu);
      ST16(so + 4096u * t, sv);
    }
    TS(0, 13);
  }
  if (OPOS == 1 && ROLE == 2) {
    SLEEP(OSLEEP);
    if (!slow) rest_out();
  }
  if (ROLE == 1 && w == 1) {
    const int q = (fb & 15) >> 2, ts = 4 * (q & 1) + ((i & 7) >> 1);
    const int dd = 64 * (fb >> 4) + 16 * (2 * (i & 1) + (q >> 1)) + 4 * (fb & 3) + 2 * (kq & 1) + (kq >> 1);
    const uint32_t fkey = (uint32_t)__builtin_amdgcn_ds_bpermute(4 * ts, (int)mykey), fr = (uint32_t)__builtin_amdgcn_ds_bpermute(4 * ts, (int)ro);
    const int ft = 2047 - (int)(((fkey >> 2) & 0x7f8u) | (fkey & 7u));
    fo = (uint32_t)(ft * D_ + dd) * 2u;
    u32x2 fs;
    asm volatile("global_load_ushort %0, %4, %8\n\tglobal_load_ushort %1, %5, %9 sc1\n\tglobal_load_ushort %2, %6, %9 sc1\n\tglobal_load_dwordx2 %3, %7, %9 sc1"
                 : "=&v"(fx), "=&v"(fv), "=&v"(fn), "=&v"(fs) : "v"(fo), "v"(v_at(dd, ft >> 3) + 2u * (ft & 7)), "v"(O_XC + fr + 2u * dd), "v"(O_SC + 16u * dd), "s"(x), "s"(ws) : "memory");
    uint32_t t16 = (((T >> (3 * ((ft >> 1) & 3))) & LMASK) | (((T >> (12 + ((ft >> 1) & 3))) & 0x00010001u) << 14)) >> (16 * (ft & 1)) & 0xffffu;
    const uint32_t o = fkey >> 16;
    const float rs0 = bf2f((u16)((o & 0x8000u) ? (o & 0x7fffu) : (~o & 0xffffu)));
    rs = rbf(__builtin_amdgcn_rcpf(1.f + __expf(-rs0)));
    const uint32_t one = (i < 8) ? (0x3f80u << (16 * (i & 1))) : 0u;
    se = (u32x4){(i >> 1) == 0 ? one : 0u, (i >> 1) == 1 ? one : 0u, (i >> 1) == 2 ? one : 0u, (i >> 1) == 3 ? one : 0u};
#pragma unroll
    for (int m = 0; m < 9; ++m) sa[m] = O_SL + (uint32_t)(16 * m + (lane >> 2)) * SP + (uint32_t)(4 * fb + (lane & 3)) * 16u;
    asm volatile("" : "+v"(t16), "+v"(rs), "+v"(se), "+v"(sa[0]), "+v"(sa[1]), "+v"(sa[2]), "+v"(sa[3]), "+v"(sa[4]), "+v"(sa[5]), "+v"(sa[6]), "+v"(sa[7]), "+v"(sa[8]));
    SLEEP(SSLEEP);
    TS(0, 1);
    float sum;
#pragma unroll 1
    for (int spin = 0; spin < SPIN_CAP; ++spin) {
      u32x4 sv[9];
#pragma unroll
      for (int m = 0; m < 9; ++m) LD16(sv[m], sa[m]);
      asm volatile("s_waitcnt vmcnt(9)" : "+v"(fx), "+v"(fv), "+v"(fn), "+v"(fs));
      if (__builtin_expect(__any(fs[0] != T), 0)) {
        for (int k = 0; k < SPIN_CAP && fs[0] != T; ++k)
          asm volatile("global_load_dwordx2 %0, %1, %2 sc1\n\ts_waitcnt vmcnt(0)" : "=&v"(fs) : "v"(O_SC + 16u * dd), "s"(ws) : "memory");
      }
      const uint32_t f1 = fv ^ t16, f2 = (f1 & 7u) ? fv & ~7u : f1;
      hx = rbf(bf2f((u16)fx) + (float)__builtin_bit_cast(_Float16, (u16)f2) * __uint_as_float(fs[1]));
      xs = bf2f((u16)fn);
      asm volatile("" : "+v"(hx), "+v"(xs));
      f32x4 dv = {0.f, 0.f, 0.f, 0.f};
      static_for<0, 9>([&](auto mm) {
        constexpr int m = mm.value;
        asm volatile("s_waitcnt vmcnt(%1)" : "+v"(sv[m]) : "n"(8 - m));
        dv = MMB(sv[m], se, dv);
        __builtin_amdgcn_sched_barrier(0);
      });
      sum = fold16(fold32(dv[0], dv[1]), fold32(dv[2], dv[3]));
      if (__builtin_expect(!__any(sum != sum), 1)) break;
      uint32_t mx = 0u;
#pragma unroll
      for (int m = 0; m < 9; ++m) mx = max(mx, max4(sv[m]));
      if (!__any(mx == 0xffffffffu)) break;
      TSC(18);
      SLEEP(PSLEEP);
    }
    const float o1 = rbf(xs + rbf(rbf(sum) * rs));
    if (i < 8) asm volatile("global_store_short %0, %1, %2 sc1" : : "v"(fo), "v"((uint32_t)f2bf(hx + o1)), "s"(out) : "memory");
    TS(0, 2);
  }
  TSE();
  }
  }
}
#define BROT "192"
#define CPF_A "s_getpc_b64 s[96:97]\n\ts_lshl_b32 s99, s2, 4\n\ts_lshl_b32 s84, s98, 7\n\ts_add_u32 s99, s99, s84\n\tv_and_b32 v112, 1, v0\n\t" \
              "v_lshl_add_u32 v112, v112, 12, s99\n\tv_min_u32 v112, kf_end-kf_all-0x80, v112\n\ts_mov_b64 exec, 3\n\tglobal_load_dword v112, v112, s[96:97]\n\ts_mov_b64 exec, -1\n\t"
extern "C" __global__ __launch_bounds__(512, 4) void kf_all(KARGS) {
  __shared__ __attribute__((aligned(16))) char smem[SMEM];
  asm volatile("s_load_dwordx16 s[64:79], s[0:1], 0x0\n\ts_load_dwordx4 s[80:83], s[0:1], 0x40\n\tv_readfirstlane_b32 s98, v0\n\t"
               "s_add_u32 s84, s2, " BROT "\n\ts_xor_b32 s84, s84, s2\n\ts_bitset0_b32 s84, 8\n\ts_bitset0_b32 s84, 9\n\ts_xor_b32 s2, s2, s84\n\t"
               "v_mov_b32 v109, 0\n\ts_and_b32 s85, s2, 7\n\ts_bitcmp1_b32 s2, 8\n\ts_cbranch_scc1 1f\n\t" CPF_A "s_bitcmp1_b32 s98, 8\n\ts_cbranch_scc1 2f\n\t"
               "s_andn2_b32 s84, s2, 31\n\ts_lshl2_add_u32 s84, s85, s84\n\ts_bfe_u32 s85, s2, 0x20003\n\ts_or_b32 s84, s84, s85\n\t"
               "s_mulk_i32 s84, 0x2400\n\ts_mulk_i32 s98, 36\n\ts_add_u32 s84, s84, s98\n\tv_and_b32 v110, 63, v0\n\tv_and_b32 v111, 32, v0\n\t"
               "v_lshlrev_b32 v111, 5, v111\n\tv_lshl_add_u32 v111, v110, 2, v111\n\tv_lshl_add_u32 v110, v110, 4, s84\n\tv_add_u32 v111, s84, v111\n\t"
               "s_cmpk_lt_u32 s2, 0x90\n\ts_waitcnt lgkmcnt(0)\n\t"
               "global_load_dwordx4 v[100:103], v110, s[64:65]\n\tglobal_load_dwordx4 v[104:107], v110, s[64:65] offset:1152\n\t"
               "global_load_dword v108, v111, s[64:65] offset:1024\n\tglobal_load_dword v109, v109, s[82:83] sc1\n\t"
               "s_cbranch_scc1 kf_n\n\ts_branch kf_h\n"
               "1:\n\ts_bfe_u32 s84, s2, 0x50003\n\ts_lshl_b32 s85, s85, 5\n\ts_or_b32 s84, s84, s85\n\ts_lshl_b32 s84, s84, 2\n\t"
               "v_and_b32 v110, 0xff, v0\n\tv_bfe_u32 v107, v0, 7, 1\n"
               "4:\n\ts_lshl_b32 s85, s84, 7\n\tv_lshl_add_u32 v110, v110, 1, s85\n\tv_lshl_add_u32 v107, v107, 1, s84\n\ts_and_b32 s85, s98, s2\n\ts_waitcnt lgkmcnt(0)\n\t"
               "global_load_ushort v100, v107, s[78:79]\n\tglobal_load_ushort v101, v110, s[66:67]\n\tglobal_load_ushort v102, v110, s[68:69]\n\t"
               "global_load_dword v109, v109, s[82:83] sc1\n\ts_bitcmp1_b32 s85, 8\n\ts_cbranch_scc1 kf_u\n\t"
               "global_load_ushort v103, v110, s[70:71]\n\tglobal_load_ushort v104, v110, s[72:73]\n\tglobal_load_ushort v105, v110, s[74:75]\n\t"
               "global_load_ushort v106, v110, s[76:77]\n\tglobal_load_ushort v107, v107, s[80:81]\n\ts_bitcmp1_b32 s2, 8\n\ts_cbranch_scc1 kf_c\n"
               "6:\n\ts_barrier\n\ts_branch kf_p\n"
               "2:\n\ts_cmpk_lt_u32 s2, 0xc0\n\ts_cbranch_scc1 6b\n\t"
               "s_lshr_b32 s84, s2, 3\n\ts_lshl3_add_u32 s84, s85, s84\n\ts_lshl1_add_u32 s84, s84, 976\n\t"
               "v_and_b32 v110, 0x7f, v0\n\tv_mov_b32 v107, 0\n\ts_branch 4b"
               : : "s"(__builtin_amdgcn_kernarg_segment_ptr()), "s"(blockIdx.x), "v"(threadIdx.x),
                   "s"((uint32_t)(uintptr_t)(__attribute__((address_space(3))) char*)smem) : "scc", "s96", "s97", "s98", "s99", "v127");
}
extern "C" __global__ __launch_bounds__(512, 4) void kf_c(KARGS) {
  __shared__ __attribute__((aligned(16))) char smem[SMEM];
  role_body<0>(smem, KPASS);
}
extern "C" __global__ __launch_bounds__(512, 4) void kf_u(KARGS) {
  __shared__ __attribute__((aligned(16))) char smem[SMEM];
  role_body<3>(smem, KPASS);
}
extern "C" __global__ __launch_bounds__(512, 4) void kf_n(KARGS) {
  __shared__ __attribute__((aligned(16))) char smem[SMEM];
  role_body<1, 1>(smem, KPASS);
}
extern "C" __global__ __launch_bounds__(512, 4) void kf_h(KARGS) {
  __shared__ __attribute__((aligned(16))) char smem[SMEM];
  role_body<1, 0>(smem, KPASS);
}
extern "C" __global__ __launch_bounds__(512, 4) void kf_p(KARGS) {
  __shared__ __attribute__((aligned(16))) char smem[SMEM];
  role_body<2>(smem, KPASS);
}
extern "C" __global__ __launch_bounds__(512, 4) void kf_conv() { asm volatile(CONV_ASM ::: CONV_CLOB); }
extern "C" __global__ void kf_end() {}

extern "C" void kernel_launch(void* const* d_in, const int* in_sizes, int n_in,
                              void* d_out, int out_size, void* d_ws, size_t ws_size,
                              hipStream_t stream) {
  kf_all<<<512, 512, 0, stream>>>((const u16*)d_in[0], (const u16*)d_in[1], (const u16*)d_in[2], (const u16*)d_in[3], (const u16*)d_in[4],
                                  (const u16*)d_in[5], (const u16*)d_in[6], (const u16*)d_in[7], (const u16*)d_in[8], (char*)d_ws + (size_t)WSOFF * 1048576,
                                  (const u16*)d_in[10], (const u16*)d_in[11], (const u16*)d_in[12], (const u16*)d_in[13], (u16*)d_out);
}
```

```cpp
#include <hip/hip_runtime.h>
#include <hip/hip_bf16.h>
#include <stdint.h>

typedef __attribute__((ext_vector_type(8))) short bf16x8;
typedef __attribute__((ext_vector_type(4))) float f32x4;
typedef uint16_t u16;
typedef __attribute__((ext_vector_type(4))) uint32_t u32x4;
typedef __attribute__((ext_vector_type(2))) uint32_t u32x2;
#define TSD
#define TS(k, i)
#define TSC(i)
#define TSE()
#define TSP
#define TSA

constexpr int L_ = 2048, D_ = 576, N_ = 128, F_ = 4608, C_ = 8;
constexpr int NF = 144;
constexpr uint32_t O_EP = 0;
constexpr uint32_t O_KF = 4096;
constexpr uint32_t O_KEY = O_KF + 8192;
constexpr uint32_t O_SC = O_KEY + 32768;
constexpr uint32_t O_DN = O_SC + 9216;
constexpr uint32_t O_SU = O_DN + 20480;
constexpr uint32_t O_FB = O_SU + 262144;
constexpr uint32_t O_XC = O_FB + 8 * 1152;
constexpr uint32_t SP = 9216 + 128;
constexpr uint32_t O_SL = O_XC + 1024 * 1152;
constexpr uint32_t SLU = 336;
constexpr uint32_t O_U = O_SL + 256 * SLU * 16;
constexpr int UP = 584;
constexpr uint32_t VP = 8192 + 128, VPP = 4096 + 128;
constexpr uint32_t O_V = O_U + 256 * UP * 16, O_VP = O_V + 256 * VP;
static_assert(256 * SLU * 16 >= NF * SP && O_SL % 128 == 0 && O_U % 128 == 0 && O_V % 128 == 0, "");
__host__ __device__ __forceinline__ uint32_t u_at(int tg, int d) { return O_U + 16u * (uint32_t)((tg >> 1) * (UP * 2) + (d >> 2) * 8 + (tg & 1) * 4 + (d & 3)); }
__host__ __device__ __forceinline__ uint32_t v_at(int d, int tg) {
  return (d < 512) ? O_V + (uint32_t)(d >> 1) * VP + 16u * (uint32_t)(2 * tg + (d & 1)) : O_VP + (uint32_t)(d - 512) * VPP + 16u * (uint32_t)tg;
}
#define HAVE_VAT
#define HAVE_VTAG
__device__ __forceinline__ float bf2f(u16 v) { return __uint_as_float((uint32_t)v << 16); }
__device__ __forceinline__ float bflo(uint32_t v) { return __uint_as_float(v << 16); }
__device__ __forceinline__ float bfhi(uint32_t v) { return __uint_as_float(v & 0xffff0000u); }
__device__ __forceinline__ u16 f2bf(float f) { return __builtin_bit_cast(u16, (__bf16)f); }
__device__ __forceinline__ float rbf(float f) { return bf2f(f2bf(f)); }
typedef __attribute__((ext_vector_type(2))) float f32x2;
typedef __attribute__((ext_vector_type(2))) __bf16 bf16x2v;
__device__ __forceinline__ uint32_t pack2(float a, float b) {
  return __builtin_bit_cast(uint32_t, __builtin_convertvector((f32x2){a, b}, bf16x2v));
}

#define DPP_I(v, ctrl, rmask) __builtin_amdgcn_update_dpp(0, (v), (ctrl), (rmask), 0xf, true)
__device__ __forceinline__ float row_sum(float v) {
  v += __int_as_float(DPP_I(__float_as_int(v), 0x111, 0xf));
  v += __int_as_float(DPP_I(__float_as_int(v), 0x112, 0xf));
  v += __int_as_float(DPP_I(__float_as_int(v), 0x114, 0xf));
  v += __int_as_float(DPP_I(__float_as_int(v), 0x118, 0xf));
  return v;
}
__device__ __forceinline__ float rdl(float v, int l) { return __int_as_float(__builtin_amdgcn_readlane(__float_as_int(v), l)); }
__device__ __forceinline__ float wave_sum(float v) {
  v = row_sum(v);
  asm("s_nop 1\n\tv_add_f32_dpp %0, %0, %0 row_bcast:15 row_mask:0xa bank_mask:0xf\n\t"
      "s_nop 1\n\tv_add_f32_dpp %0, %0, %0 row_bcast:31 row_mask:0xc bank_mask:0xf\n\ts_nop 0" : "+v"(v));
  return rdl(v, 63);
}
__device__ __forceinline__ float fold32(float a, float b) {
  const auto r = __builtin_amdgcn_permlane32_swap(__float_as_uint(a), __float_as_uint(b), false, false);
  return __uint_as_float(r[0]) + __uint_as_float(r[1]);
}
__device__ __forceinline__ float fold16(float a, float b) {
  const auto r = __builtin_amdgcn_permlane16_swap(__float_as_uint(a), __float_as_uint(b), false, false);
  return __uint_as_float(r[0]) + __uint_as_float(r[1]);
}
__device__ __forceinline__ void wave_sum2(float& a, float& b) {
  float v = row_sum(fold32(a, b));
  asm("s_nop 1\n\tv_add_f32_dpp %0, %0, %0 row_bcast:15 row_mask:0xa bank_mask:0xf\n\ts_nop 0" : "+v"(v));
  a = rdl(v, 31); b = rdl(v, 63);
}
__device__ __forceinline__ float dot2(uint32_t a, uint32_t b, float c) {
  return __builtin_amdgcn_fdot2_f32_bf16(__builtin_bit_cast(bf16x2v, a), __builtin_bit_cast(bf16x2v, b), c, false);
}
__device__ __forceinline__ uint32_t row_maxu(uint32_t v) {
  v = max(v, (uint32_t)DPP_I((int)v, 0xb1, 0xf));
  v = max(v, (uint32_t)DPP_I((int)v, 0x4e, 0xf));
  v = max(v, (uint32_t)DPP_I((int)v, 0x141, 0xf));
  v = max(v, (uint32_t)DPP_I((int)v, 0x140, 0xf));
  return v;
}
__device__ __forceinline__ uint32_t wave_maxu(uint32_t v) {
  v = max(v, (uint32_t)DPP_I((int)v, 0x111, 0xf));
  v = max(v, (uint32_t)DPP_I((int)v, 0x112, 0xf));
  v = max(v, (uint32_t)DPP_I((int)v, 0x114, 0xf));
  v = max(v, (uint32_t)DPP_I((int)v, 0x118, 0xf));
  v = max(v, (uint32_t)DPP_I((int)v, 0x142, 0xa));
  v = max(v, (uint32_t)DPP_I((int)v, 0x143, 0xc));
  return (uint32_t)__builtin_amdgcn_readlane((int)v, 63);
}

typedef __attribute__((ext_vector_type(8))) _Float16 f16x8;
typedef __attribute__((ext_vector_type(2))) _Float16 f16x2;
__device__ __forceinline__ uint32_t packh(f32x2 v) { return __builtin_bit_cast(uint32_t, __builtin_convertvector(v, f16x2)); }
__device__ __forceinline__ uint32_t packh(float a, float b) { return packh((f32x2){a, b}); }
constexpr int IP = 224, I_X = 128;
__device__ __forceinline__ f32x2 cmul(f32x2 z, f32x2 w) {
  f32x2 r;
  asm("v_pk_mul_f32 %0, %1, %2 op_sel_hi:[0,1]\n\tv_pk_fma_f32 %0, %1, %2, %0 op_sel:[1,1,0] op_sel_hi:[1,0,1] neg_lo:[0,1,0]" : "=&v"(r) : "v"(z), "v"(w));
  return r;
}
__device__ __forceinline__ f32x2 cexp2(float ar2, float air, float l) {
  const float t = air * l;
  return (f32x2){__builtin_amdgcn_cosf(t), __builtin_amdgcn_sinf(t)} * __builtin_amdgcn_exp2f(ar2 * l);
}
__device__ __forceinline__ float state_a(float ldt, float are, float aim, float& ar2, float& air, float& g) {
  const float dt = rbf(__expf(ldt));
  ar2 = dt * are * 1.4426950408889634f;
  air = dt * aim * 0.15915494309189535f;
  air -= __builtin_rintf(air);
  g = 0.f;
  if (__builtin_expect(__any(ar2 > 0.f), 0)) g = fminf(__uint_as_float(wave_maxu(__float_as_uint(fmaxf(ar2, 0.f)))), 1.f);
  return dt;
}
template <int N> __device__ __forceinline__ void put_im(char* p, int im, const f32x2* z) {
  *(u32x4*)p = (u32x4){packh(z[0][0], z[1][0]), packh(z[2][0], z[3][0]), packh(z[4][0], z[5][0]), packh(z[6][0], z[7][0])};
  *(u32x4*)(p + im) = (u32x4){packh(z[0][1], z[1][1]), packh(z[2][1], z[3][1]), packh(z[4][1], z[5][1]), packh(z[6][1], z[7][1])};
}
#define Y_ASM \
  "v_mov_b64 v[40:41], %0\nv_pk_mul_f32 v[42:43], v[40:41], %1 op_sel_hi:[0,1]\nv_pk_mul_f32 v[44:45], %0, %2 op_sel_hi:[0,1]\n" \
  "v_pk_fma_f32 v[42:43], v[40:41], %1, v[42:43] op_sel:[1,1,0] op_sel_hi:[1,0,1] neg_lo:[0,1,0]\n" \
  "v_pk_fma_f32 v[44:45], %0, %2, v[44:45] op_sel:[1,1,0] op_sel_hi:[1,0,1] neg_lo:[0,1,0]\nv_cvt_pk_f16_f32 v56, v40, v42\n" \
  "v_cvt_pk_f16_f32 v60, v41, v43\nv_pk_mul_f32 v[40:41], v[42:43], %1 op_sel_hi:[0,1]\nv_pk_mul_f32 v[48:49], v[44:45], %2 op_sel_hi:[0,1]\n" \
  "v_pk_fma_f32 v[40:41], v[42:43], %1, v[40:41] op_sel:[1,1,0] op_sel_hi:[1,0,1] neg_lo:[0,1,0]\n" \
  "v_pk_fma_f32 v[48:49], v[44:45], %2, v[48:49] op_sel:[1,1,0] op_sel_hi:[1,0,1] neg_lo:[0,1,0]\n" \
  "v_pk_mul_f32 v[42:43], v[40:41], %1 op_sel_hi:[0,1]\nv_pk_mul_f32 v[52:53], v[48:49], %2 op_sel_hi:[0,1]\n" \
  "v_pk_fma_f32 v[42:43], v[40:41], %1, v[42:43] op_sel:[1,1,0] op_sel_hi:[1,0,1] neg_lo:[0,1,0]\n" \
  "v_pk_fma_f32 v[52:53], v[48:49], %2, v[52:53] op_sel:[1,1,0] op_sel_hi:[1,0,1] neg_lo:[0,1,0]\nv_cvt_pk_f16_f32 v57, v40, v42\n" \
  "v_cvt_pk_f16_f32 v61, v41, v43\nv_pk_mul_f32 v[40:41], v[42:43], %1 op_sel_hi:[0,1]\n" \
  "v_pk_fma_f32 v[40:41], v[42:43], %1, v[40:41] op_sel:[1,1,0] op_sel_hi:[1,0,1] neg_lo:[0,1,0]\n" \
  "v_pk_mul_f32 v[42:43], v[40:41], %1 op_sel_hi:[0,1]\nv_pk_mul_f32 v[46:47], v[44:45], %1 op_sel_hi:[0,1]\n" \
  "v_pk_fma_f32 v[42:43], v[40:41], %1, v[42:43] op_sel:[1,1,0] op_sel_hi:[1,0,1] neg_lo:[0,1,0]\n" \
  "v_pk_fma_f32 v[46:47], v[44:45], %1, v[46:47] op_sel:[1,1,0] op_sel_hi:[1,0,1] neg_lo:[0,1,0]\nv_cvt_pk_f16_f32 v58, v40, v42\n" \
  "v_cvt_pk_f16_f32 v62, v41, v43\nv_cvt_pk_f16_f32 v64, v44, v46\nv_cvt_pk_f16_f32 v68, v45, v47\n" \
  "v_pk_mul_f32 v[40:41], v[42:43], %1 op_sel_hi:[0,1]\nv_pk_mul_f32 v[44:45], v[46:47], %1 op_sel_hi:[0,1]\n" \
  "v_pk_fma_f32 v[40:41], v[42:43], %1, v[40:41] op_sel:[1,1,0] op_sel_hi:[1,0,1] neg_lo:[0,1,0]\n" \
  "v_pk_fma_f32 v[44:45], v[46:47], %1, v[44:45] op_sel:[1,1,0] op_sel_hi:[1,0,1] neg_lo:[0,1,0]\n" \
  "v_pk_mul_f32 v[42:43], v[40:41], %1 op_sel_hi:[0,1]\nv_pk_mul_f32 v[46:47], v[44:45], %1 op_sel_hi:[0,1]\n" \
  "v_pk_fma_f32 v[42:43], v[40:41], %1, v[42:43] op_sel:[1,1,0] op_sel_hi:[1,0,1] neg_lo:[0,1,0]\n" \
  "v_pk_fma_f32 v[46:47], v[44:45], %1, v[46:47] op_sel:[1,1,0] op_sel_hi:[1,0,1] neg_lo:[0,1,0]\nv_cvt_pk_f16_f32 v59, v40, v42\n" \
  "v_cvt_pk_f16_f32 v63, v41, v43\nv_cvt_pk_f16_f32 v65, v44, v46\nv_cvt_pk_f16_f32 v69, v45, v47\n" \
  "v_pk_mul_f32 v[44:45], v[46:47], %1 op_sel_hi:[0,1]\nds_write_b128 %3, v[56:59] offset:0\n" \
  "v_pk_fma_f32 v[44:45], v[46:47], %1, v[44:45] op_sel:[1,1,0] op_sel_hi:[1,0,1] neg_lo:[0,1,0]\nds_write_b128 %3, v[60:63] offset:64\n" \
  "v_pk_mul_f32 v[46:47], v[44:45], %1 op_sel_hi:[0,1]\nv_pk_mul_f32 v[50:51], v[48:49], %1 op_sel_hi:[0,1]\n" \
  "v_pk_fma_f32 v[46:47], v[44:45], %1, v[46:47] op_sel:[1,1,0] op_sel_hi:[1,0,1] neg_lo:[0,1,0]\n" \
  "v_pk_fma_f32 v[50:51], v[48:49], %1, v[50:51] op_sel:[1,1,0] op_sel_hi:[1,0,1] neg_lo:[0,1,0]\nv_cvt_pk_f16_f32 v66, v44, v46\n" \
  "v_cvt_pk_f16_f32 v70, v45, v47\nv_cvt_pk_f16_f32 v56, v48, v50\nv_cvt_pk_f16_f32 v60, v49, v51\n" \
  "v_pk_mul_f32 v[44:45], v[46:47], %1 op_sel_hi:[0,1]\nv_pk_mul_f32 v[48:49], v[50:51], %1 op_sel_hi:[0,1]\n" \
  "v_pk_fma_f32 v[44:45], v[46:47], %1, v[44:45] op_sel:[1,1,0] op_sel_hi:[1,0,1] neg_lo:[0,1,0]\n" \
  "v_pk_fma_f32 v[48:49], v[50:51], %1, v[48:49] op_sel:[1,1,0] op_sel_hi:[1,0,1] neg_lo:[0,1,0]\n" \
  "v_pk_mul_f32 v[46:47], v[44:45], %1 op_sel_hi:[0,1]\nv_pk_mul_f32 v[50:51], v[48:49], %1 op_sel_hi:[0,1]\n" \
  "v_pk_fma_f32 v[46:47], v[44:45], %1, v[46:47] op_sel:[1,1,0] op_sel_hi:[1,0,1] neg_lo:[0,1,0]\n" \
  "v_pk_fma_f32 v[50:51], v[48:49], %1, v[50:51] op_sel:[1,1,0] op_sel_hi:[1,0,1] neg_lo:[0,1,0]\nv_cvt_pk_f16_f32 v67, v44, v46\n" \
  "v_cvt_pk_f16_f32 v71, v45, v47\nv_cvt_pk_f16_f32 v57, v48, v50\nv_cvt_pk_f16_f32 v61, v49, v51\n" \
  "v_pk_mul_f32 v[48:49], v[50:51], %1 op_sel_hi:[0,1]\nds_write_b128 %3, v[64:67] offset:16\n" \
  "v_pk_fma_f32 v[48:49], v[50:51], %1, v[48:49] op_sel:[1,1,0] op_sel_hi:[1,0,1] neg_lo:[0,1,0]\nds_write_b128 %3, v[68:71] offset:80\n" \
  "v_pk_mul_f32 v[50:51], v[48:49], %1 op_sel_hi:[0,1]\nv_pk_mul_f32 v[54:55], v[52:53], %1 op_sel_hi:[0,1]\n" \
  "v_pk_fma_f32 v[50:51], v[48:49], %1, v[50:51] op_sel:[1,1,0] op_sel_hi:[1,0,1] neg_lo:[0,1,0]\n" \
  "v_pk_fma_f32 v[54:55], v[52:53], %1, v[54:55] op_sel:[1,1,0] op_sel_hi:[1,0,1] neg_lo:[0,1,0]\nv_cvt_pk_f16_f32 v58, v48, v50\n" \
  "v_cvt_pk_f16_f32 v62, v49, v51\nv_cvt_pk_f16_f32 v64, v52, v54\nv_cvt_pk_f16_f32 v68, v53, v55\n" \
  "v_pk_mul_f32 v[48:49], v[50:51], %1 op_sel_hi:[0,1]\nv_pk_mul_f32 v[52:53], v[54:55], %1 op_sel_hi:[0,1]\n" \
  "v_pk_fma_f32 v[48:49], v[50:51], %1, v[48:49] op_sel:[1,1,0] op_sel_hi:[1,0,1] neg_lo:[0,1,0]\n" \
  "v_pk_fma_f32 v[52:53], v[54:55], %1, v[52:53] op_sel:[1,1,0] op_sel_hi:[1,0,1] neg_lo:[0,1,0]\n" \
  "v_pk_mul_f32 v[50:51], v[48:49], %1 op_sel_hi:[0,1]\nv_pk_mul_f32 v[54:55], v[52:53], %1 op_sel_hi:[0,1]\n" \
  "v_pk_fma_f32 v[50:51], v[48:49], %1, v[50:51] op_sel:[1,1,0] op_sel_hi:[1,0,1] neg_lo:[0,1,0]\n" \
  "v_pk_fma_f32 v[54:55], v[52:53], %1, v[54:55] op_sel:[1,1,0] op_sel_hi:[1,0,1] neg_lo:[0,1,0]\nv_cvt_pk_f16_f32 v59, v48, v50\n" \
  "v_cvt_pk_f16_f32 v63, v49, v51\nv_cvt_pk_f16_f32 v65, v52, v54\nv_cvt_pk_f16_f32 v69, v53, v55\n" \
  "v_pk_mul_f32 v[52:53], v[54:55], %1 op_sel_hi:[0,1]\nds_write_b128 %3, v[56:59] offset:32\n" \
  "v_pk_fma_f32 v[52:53], v[54:55], %1, v[52:53] op_sel:[1,1,0] op_sel_hi:[1,0,1] neg_lo:[0,1,0]\nds_write_b128 %3, v[60:63] offset:96\n" \
  "v_pk_mul_f32 v[54:55], v[52:53], %1 op_sel_hi:[0,1]\n" \
  "v_pk_fma_f32 v[54:55], v[52:53], %1, v[54:55] op_sel:[1,1,0] op_sel_hi:[1,0,1] neg_lo:[0,1,0]\nv_cvt_pk_f16_f32 v66, v52, v54\n" \
  "v_cvt_pk_f16_f32 v70, v53, v55\nv_pk_mul_f32 v[52:53], v[54:55], %1 op_sel_hi:[0,1]\n" \
  "v_pk_fma_f32 v[52:53], v[54:55], %1, v[52:53] op_sel:[1,1,0] op_sel_hi:[1,0,1] neg_lo:[0,1,0]\n" \
  "v_pk_mul_f32 v[54:55], v[52:53], %1 op_sel_hi:[0,1]\n" \
  "v_pk_fma_f32 v[54:55], v[52:53], %1, v[54:55] op_sel:[1,1,0] op_sel_hi:[1,0,1] neg_lo:[0,1,0]\nv_cvt_pk_f16_f32 v67, v52, v54\n" \
  "v_cvt_pk_f16_f32 v71, v53, v55\nds_write_b128 %3, v[64:67] offset:48\nds_write_b128 %3, v[68:71] offset:112\n"
#define Y_CLOB "memory", "v40", "v41", "v42", "v43", "v44", "v45", "v46", "v47", "v48", "v49", "v50", "v51", "v52", "v53", "v54", "v55", "v56", "v57", "v58", "v59", "v60", "v61", "v62", "v63", "v64", "v65", "v66", "v67", "v68", "v69", "v70", "v71"
#define T_ASM \
  "ds_read_b128 %4, %8\nds_read_b64_tr_b16 v[80:81], %7 offset:0\nds_read_b64_tr_b16 v[82:83], %7 offset:64\n" \
  "ds_read_b64_tr_b16 v[84:85], %5 offset:0\nds_read_b64_tr_b16 v[86:87], %5 offset:48\nds_read_b64_tr_b16 v[88:89], %6 offset:0\n" \
  "ds_read_b64_tr_b16 v[90:91], %6 offset:48\nds_read_b64_tr_b16 v[92:93], %7 offset:3584\nds_read_b64_tr_b16 v[94:95], %7 offset:3648\n" \
  "ds_read_b64_tr_b16 v[96:97], %5 offset:3584\nds_read_b64_tr_b16 v[98:99], %5 offset:3632\nds_read_b64_tr_b16 v[100:101], %6 offset:3584\n" \
  "ds_read_b64_tr_b16 v[102:103], %6 offset:3632\nds_read_b64_tr_b16 v[104:105], %7 offset:7168\nds_read_b64_tr_b16 v[106:107], %7 offset:7232\n" \
  "s_waitcnt lgkmcnt(8)\nv_mfma_f32_16x16x32_f16 %0, v[84:87], v[80:83], 0\nds_read_b64_tr_b16 v[108:109], %5 offset:7168\n" \
  "ds_read_b64_tr_b16 v[110:111], %5 offset:7216\nds_read_b64_tr_b16 v[112:113], %6 offset:7168\nv_mfma_f32_16x16x32_f16 %2, v[88:91], v[80:83], 0\n" \
  "ds_read_b64_tr_b16 v[114:115], %6 offset:7216\nds_read_b64_tr_b16 v[116:117], %7 offset:10752\nds_read_b64_tr_b16 v[118:119], %7 offset:10816\n" \
  "s_waitcnt lgkmcnt(8)\nv_mfma_f32_16x16x32_f16 %0, v[96:99], v[92:95], %0\nds_read_b64_tr_b16 v[120:121], %5 offset:10752\n" \
  "ds_read_b64_tr_b16 v[122:123], %5 offset:10800\nds_read_b64_tr_b16 v[124:125], %6 offset:10752\n" \
  "v_mfma_f32_16x16x32_f16 %2, v[100:103], v[92:95], %2\nds_read_b64_tr_b16 v[126:127], %6 offset:10800\n" \
  "ds_read_b64_tr_b16 v[80:81], %7 offset:14336\nds_read_b64_tr_b16 v[82:83], %7 offset:14400\ns_waitcnt lgkmcnt(8)\n" \
  "v_mfma_f32_16x16x32_f16 %0, v[108:111], v[104:107], %0\nds_read_b64_tr_b16 v[84:85], %5 offset:14336\n" \
  "ds_read_b64_tr_b16 v[86:87], %5 offset:14384\nds_read_b64_tr_b16 v[88:89], %6 offset:14336\n" \
  "v_mfma_f32_16x16x32_f16 %2, v[112:115], v[104:107], %2\nds_read_b64_tr_b16 v[90:91], %6 offset:14384\n" \
  "ds_read_b64_tr_b16 v[92:93], %7 offset:17920\nds_read_b64_tr_b16 v[94:95], %7 offset:17984\ns_waitcnt lgkmcnt(8)\n" \
  "v_mfma_f32_16x16x32_f16 %0, v[120:123], v[116:119], %0\nds_read_b64_tr_b16 v[96:97], %5 offset:17920\n" \
  "ds_read_b64_tr_b16 v[98:99], %5 offset:17968\nds_read_b64_tr_b16 v[100:101], %6 offset:17920\n" \
  "v_mfma_f32_16x16x32_f16 %2, v[124:127], v[116:119], %2\nds_read_b64_tr_b16 v[102:103], %6 offset:17968\n" \
  "ds_read_b64_tr_b16 v[104:105], %7 offset:21504\nds_read_b64_tr_b16 v[106:107], %7 offset:21568\ns_waitcnt lgkmcnt(8)\n" \
  "v_mfma_f32_16x16x32_f16 %1, v[84:87], v[80:83], 0\nds_read_b64_tr_b16 v[108:109], %5 offset:21504\nds_read_b64_tr_b16 v[110:111], %5 offset:21552\n" \
  "ds_read_b64_tr_b16 v[112:113], %6 offset:21504\nv_mfma_f32_16x16x32_f16 %3, v[88:91], v[80:83], 0\nds_read_b64_tr_b16 v[114:115], %6 offset:21552\n" \
  "ds_read_b64_tr_b16 v[116:117], %7 offset:25088\nds_read_b64_tr_b16 v[118:119], %7 offset:25152\ns_waitcnt lgkmcnt(8)\n" \
  "v_mfma_f32_16x16x32_f16 %1, v[96:99], v[92:95], %1\nds_read_b64_tr_b16 v[120:121], %5 offset:25088\n" \
  "ds_read_b64_tr_b16 v[122:123], %5 offset:25136\nds_read_b64_tr_b16 v[124:125], %6 offset:25088\n" \
  "v_mfma_f32_16x16x32_f16 %3, v[100:103], v[92:95], %3\nds_read_b64_tr_b16 v[126:127], %6 offset:25136\ns_waitcnt lgkmcnt(6)\n" \
  "v_mfma_f32_16x16x32_f16 %1, v[108:111], v[104:107], %1\nv_mfma_f32_16x16x32_f16 %3, v[112:115], v[104:107], %3\ns_waitcnt lgkmcnt(0)\n" \
  "v_mfma_f32_16x16x32_f16 %1, v[120:123], v[116:119], %1\nv_mfma_f32_16x16x32_f16 %3, v[124:127], v[116:119], %3\n"
#define T_CLOB "memory", "v80", "v81", "v82", "v83", "v84", "v85", "v86", "v87", "v88", "v89", "v90", "v91", "v92", "v93", "v94", "v95", "v96", "v97", "v98", "v99", "v100", "v101", "v102", "v103", "v104", "v105", "v106", "v107", "v108", "v109", "v110", "v111", "v112", "v113", "v114", "v115", "v116", "v117", "v118", "v119", "v120", "v121", "v122", "v123", "v124", "v125", "v126", "v127"
template <class F> __device__ __forceinline__ int gen_y(float ldt, float are, float aim, float bre, float bim, float cre, float cim, uint32_t im, F&& mid TSP) {
  float ar2, air, g;
  const float dt = state_a(ldt, are, aim, ar2, air, g);
  const float dbr = dt * bre, dbi = dt * bim, pr = cre * dbr - cim * dbi, pi = cre * dbi + cim * dbr;
  const f32x2 w8 = cexp2(ar2, -air, 8.f), w64 = cexp2(ar2, -air, 64.f);
  const float pm = fmaxf(fabsf(pr), fabsf(pi));
  int e = -4;
  if (__builtin_expect(__any(!(pm < 0x1p-4f) || ar2 > 0.f) || !__any(pm > 0x1p-12f), 0)) e = (int)(wave_maxu(__float_as_uint(pm)) >> 23) - 126 + (int)ceilf(g * 250.f);
  const f32x2 y0 = {__builtin_amdgcn_ldexpf(pr, 14 - e), __builtin_amdgcn_ldexpf(-pi, 14 - e)};
  mid();
  TS(0, 7);
  asm volatile(Y_ASM : : "v"(y0), "v"(w8), "v"(w64), "v"(im) : Y_CLOB);
  return e;
}
__device__ __forceinline__ int gen_x(float ldt, float are, float aim, char* im) {
  float ar2, air, g;
  state_a(ldt, are, aim, ar2, air, g);
  const int e = min((int)ceilf(g * 1810.f), 300);
  const float s = __builtin_amdgcn_ldexpf(1.f, 15 - e);
  const f32x2 w1 = cexp2(ar2, air, 1.f), w256 = cexp2(ar2, air, 256.f), w2 = cmul(w1, w1);
  f32x2 a[8], b[8], c[8];
  a[0] = (f32x2){s, 0.f}; a[1] = w1 * s; a[2] = w2 * s;
  b[0] = b[1] = w256 * s;
  b[2] = cmul(b[0], w2); a[3] = cmul(a[1], w2); a[4] = cmul(a[2], w2);
  b[3] = b[4] = b[5] = cmul(b[2], w2); a[5] = cmul(a[3], w2); a[6] = cmul(a[4], w2);
  b[6] = cmul(b[3], w2); a[7] = cmul(a[5], w2); c[0] = cmul(b[3], w256);
  b[7] = cmul(b[6], w2); c[2] = cmul(c[0], w256);
  put_im<0>(im + I_X, 48, a);
  c[1] = cmul(b[7], w256); c[3] = cmul(c[1], w256);
  put_im<0>(im + I_X + 16, 48, b);
#pragma unroll
  for (int k = 4; k < 8; ++k) c[k] = cmul(c[k - 1], w256);
  put_im<0>(im + I_X + 32, 48, c);
  return e;
}

template <int I> struct IC { static constexpr int value = I; };
template <int B, int E, class F> __device__ __forceinline__ void static_for(F&& f) {
  if constexpr (B < E) { f(IC<B>{}); static_for<B + 1, E>(f); }
}
typedef __attribute__((ext_vector_type(4))) short s16x4;
__device__ __forceinline__ u32x2 trd(uint32_t a) {
  return __builtin_bit_cast(u32x2, __builtin_amdgcn_ds_read_tr16_b64_v4i16((__attribute__((address_space(3))) s16x4*)(uintptr_t)a));
}
#define BAR() asm volatile("s_waitcnt lgkmcnt(0)\n\ts_barrier" ::: "memory")
#define ST16(off, v) asm volatile("global_store_dwordx4 %0, %1, %2 sc1\n\ts_nop 1" : : "v"(off), "v"(v), "s"(ws) : "memory")
#define ST4(off, v) asm volatile("global_store_dword %0, %1, %2 sc1" : : "v"(off), "v"(v), "s"(ws) : "memory")
#define LD16(r, off) asm volatile("global_load_dwordx4 %0, %1, %2 sc1" : "=v"(r) : "v"(off), "s"(ws) : "memory")
#define LD16W(r, off) asm volatile("global_load_dwordx4 %0, %1, %2 sc1\n\ts_waitcnt vmcnt(0)" : "=&v"(r) : "v"(off), "s"(ws) : "memory")
#define DRAIN() asm volatile("s_waitcnt vmcnt(0)" ::: "memory")
#define CEX(a, b) { const uint32_t hi_ = max(a, b), lo_ = min(a, b); a = hi_; b = lo_; }
#define MMB(a_, b_, c_) __builtin_amdgcn_mfma_f32_16x16x32_bf16(__builtin_bit_cast(bf16x8, a_), __builtin_bit_cast(bf16x8, b_), c_, 0, 0, 0)
#define NTM 0
#define DPOL ""
#define DPOL2 ""
#define DPOL4 ""
constexpr int SPIN_CAP = 1 << 18;
constexpr uint32_t UMASK = 0x40074007u;
__device__ __forceinline__ uint32_t utag(uint32_t T, int j) {
  return ((T >> (3 * j)) & 0x00070007u) | (((j <= 2) ? (T << (2 - j)) : (T >> (j - 2))) & 0x40004000u);
}
__device__ __forceinline__ uint32_t max4(const u32x4& a) { return max(max(a[0], a[1]), max(a[2], a[3])); }
#ifndef ULONG
#define ULONG 8
#endif
__device__ __forceinline__ void until(uint32_t t) {
#pragma unroll 1
  for (int spin = 0; spin < 600; ++spin) {
    const int d = (int)(t - (uint32_t)__builtin_amdgcn_s_memrealtime());
    if (d <= 0) break;
    if (ULONG && d > 4 * ULONG) __builtin_amdgcn_s_sleep(ULONG); else __builtin_amdgcn_s_sleep(1);
  }
}
__device__ __forceinline__ uint32_t latest(uint32_t t) {
  const uint32_t t0 = (uint32_t)__builtin_amdgcn_readfirstlane(t) - 0x40000000u;
  return wave_maxu(t - t0) + t0;
}
__device__ __forceinline__ uint32_t gate(const char* ws, uint32_t po, uint32_t T) {
  u32x2 pv;
#pragma unroll 1
  for (int spin = 0; spin < SPIN_CAP; ++spin) {
    asm volatile("global_load_dwordx2 %0, %1, %2 sc1\n\ts_waitcnt vmcnt(0)" : "=&v"(pv) : "v"(po), "s"(ws) : "memory");
    if (!__any(pv[0] != T)) break;
    __builtin_amdgcn_s_sleep(2);
  }
  return pv[1];
}
__device__ __forceinline__ uint32_t gate2(const char* ws, uint32_t oa, uint32_t da, uint32_t ob, uint32_t T) {
  u32x2 pa, pb;
#pragma unroll 1
  for (int spin = 0; spin < SPIN_CAP; ++spin) {
    asm volatile("global_load_dwordx2 %0, %2, %4 sc1\n\tglobal_load_dwordx2 %1, %3, %4 sc1\n\ts_waitcnt vmcnt(0)" : "=&v"(pa), "=&v"(pb) : "v"(oa), "v"(ob), "s"(ws) : "memory");
    if (!__any(pa[0] != T || pb[0] != T)) break;
    __builtin_amdgcn_s_sleep(4);
  }
  pa[1] += da;
  return latest(((int)(pa[1] - pb[1]) > 0) ? pa[1] : pb[1]);
}
__device__ __forceinline__ void gsync(uint32_t a, uint32_t target) {
  uint32_t t, c, n = SPIN_CAP;
  uint64_t ex;
  asm volatile("s_waitcnt lgkmcnt(0)\n\ts_mov_b64 %3, exec\n\ts_mov_b64 exec, 1\n\tv_mov_b32 %0, 1\n\tds_add_u32 %4, %0\n\ts_mov_b64 exec, %3\n"
               "1:\n\ts_sleep 1\n\tds_read_b32 %0, %4\n\ts_sub_u32 %2, %2, 1\n\ts_cbranch_scc1 2f\n\ts_waitcnt lgkmcnt(0)\n\tv_readfirstlane_b32 %1, %0\n\t"
               "s_cmp_lt_u32 %1, %5\n\ts_cbranch_scc1 1b\n2:\n\ts_waitcnt lgkmcnt(0)"
               : "=&v"(t), "=&s"(c), "+s"(n), "=&s"(ex) : "v"(a), "n"(target) : "memory", "scc");
}
__device__ __forceinline__ void gsync0(uint32_t a, uint32_t target) {
  uint32_t t, c, n = SPIN_CAP;
  asm volatile("s_waitcnt lgkmcnt(0)\n\ts_mov_b64 exec, 1\n\tv_mov_b32 %0, 1\n\tds_add_u32 %3, %0\n\ts_mov_b64 exec, -1\n"
               "1:\n\tds_read_b32 %0, %3\n\ts_sub_u32 %2, %2, 1\n\ts_cbranch_scc1 2f\n\ts_waitcnt lgkmcnt(0)\n\tv_readfirstlane_b32 %1, %0\n\t"
               "s_cmp_lt_u32 %1, %4\n\ts_cbranch_scc1 1b\n2:\n\ts_waitcnt lgkmcnt(0)"
               : "=&v"(t), "=&s"(c), "+s"(n) : "v"(a), "n"(target) : "memory", "scc");
}
__device__ __forceinline__ void gwait(uint32_t a, uint32_t target) {
  uint32_t t, c, n = SPIN_CAP;
  asm volatile("1:\n\ts_sleep 2\n\tds_read_b32 %0, %3\n\ts_sub_u32 %2, %2, 1\n\ts_cbranch_scc1 2f\n\ts_waitcnt lgkmcnt(0)\n\tv_readfirstlane_b32 %1, %0\n\t"
               "s_cmp_lt_u32 %1, %4\n\ts_cbranch_scc1 1b\n2:\n\ts_waitcnt lgkmcnt(0)"
               : "=&v"(t), "=&s"(c), "+s"(n) : "v"(a), "n"(target) : "memory", "scc");
}
__device__ __forceinline__ void gwait0(uint32_t a, uint32_t target) {
  uint32_t t, c, n = SPIN_CAP;
  asm volatile("1:\n\tds_read_b32 %0, %3\n\ts_sub_u32 %2, %2, 1\n\ts_cbranch_scc1 2f\n\ts_waitcnt lgkmcnt(0)\n\tv_readfirstlane_b32 %1, %0\n\t"
               "s_cmp_lt_u32 %1, %4\n\ts_cbranch_scc1 1b\n2:\n\ts_waitcnt lgkmcnt(0)"
               : "=&v"(t), "=&s"(c), "+s"(n) : "v"(a), "n"(target) : "memory", "scc");
}
__device__ __forceinline__ void dma16(const void* base, uint32_t voff, uint32_t lds) {
  uint32_t keep;
  asm volatile("s_mov_b32 %0, m0\n\ts_mov_b32 m0, %3\n\ts_nop 0\n\tglobal_load_lds_dwordx4 %1, %2" DPOL "\n\ts_mov_b32 m0, %0" : "=&s"(keep) : "v"(voff), "s"(base), "s"(lds) : "memory");
}
__device__ __forceinline__ void dma16d(const void* base, uint32_t voff, uint32_t lds) {
  uint32_t keep;
  asm volatile("s_mov_b32 %0, m0\n\ts_mov_b32 m0, %3\n\ts_nop 0\n\tglobal_load_lds_dwordx4 %1, %2" DPOL2 "\n\ts_mov_b32 m0, %0" : "=&s"(keep) : "v"(voff), "s"(base), "s"(lds) : "memory");
}
__device__ __forceinline__ void nap(int n) {
  for (; n > 15; n -= 15) __builtin_amdgcn_s_sleep(15);
  switch (n) {
    case 1: __builtin_amdgcn_s_sleep(1); break; case 2: __builtin_amdgcn_s_sleep(2); break; case 3: __builtin_amdgcn_s_sleep(3); break;
    case 4: __builtin_amdgcn_s_sleep(4); break; case 5: __builtin_amdgcn_s_sleep(5); break; case 6: __builtin_amdgcn_s_sleep(6); break;
    case 7: __builtin_amdgcn_s_sleep(7); break; case 8: __builtin_amdgcn_s_sleep(8); break; case 9: __builtin_amdgcn_s_sleep(9); break;
    case 10: __builtin_amdgcn_s_sleep(10); break; case 11: __builtin_amdgcn_s_sleep(11); break; case 12: __builtin_amdgcn_s_sleep(12); break;
    case 13: __builtin_amdgcn_s_sleep(13); break; case 14: __builtin_amdgcn_s_sleep(14); break; case 15: __builtin_amdgcn_s_sleep(15); break;
    default: break;
  }
}
#define SLEEP(n) do { if ((n) > 0) __builtin_amdgcn_s_sleep(n); } while (0)
#define USLEEP 3
#define SSLEEP 12
#define PSLEEP 2
#define PPRIO 0
#define LFIRST 1
#define WD 40
#define OSLEEP 12
#define QPRIO 1
#define KPRIO 2
#define GSLEEP 0
#define NSLEEP 0
#define UD 40
#define XD 0
#define RSLEEP 0
#define UPRIO 2
#define VD2 375
#define WDEPTH 4
#define ESLEEP 0
#define SD 58
#define KD 172
#define OD 0
#define KBACK 6
#define DDEPTH 6
#define C3 1
#define XROT 2
#define WSOFF 0
#define KN 2
#define RPF 1
#define LSLEEP 3
#define TSTOP 72
#define HSTOP 110
#define VEARLY 0ull
#define FPR 3
#define SPR 3
#define DPRIO 0
#define QPRIO2 3
#define HW 2
#define RD 55
#define OPOS 1
#define OD2 6
#define NA 12
#define RD2 88
#define ORD1 ((unsigned __int128)0x321087654u << 36 | 0x321087654ull)
#define WDEPTH2 4
#define RQ NA
#define HREV 1
#define QPRIO0 3
#define VPS 1
#define RW 0
#define NPRIO 0
#define XSL 10
#define V2D 22
#define N0 8
#define LPOS 0
#define ALLP 1
#define FORCESLOW 0
#define FORCEFB 0
#define CONV_ASM \
  "s_cmp_eq_u32 s89, 1\ns_cbranch_scc1 1f\ns_cmp_gt_u32 s89, 1\ns_cbranch_scc1 9f\nds_read_b64 v[124:125], v3 offset:576\n" \
  "ds_read_b64 v[126:127], v3 offset:584\nds_read_b64 v[120:121], v3 offset:64\nds_read_b64 v[122:123], v3 offset:72\n" \
  "ds_read_b64 v[116:117], v3 offset:640\nds_read_b64 v[118:119], v3 offset:648\nds_read_b64 v[112:113], v3 offset:128\n" \
  "ds_read_b64 v[114:115], v3 offset:136\nds_read_b64 v[108:109], v3 offset:704\nds_read_b64 v[110:111], v3 offset:712\n" \
  "ds_read_b64 v[104:105], v3 offset:192\nds_read_b64 v[106:107], v3 offset:200\nds_read_b64 v[100:101], v3 offset:768\n" \
  "ds_read_b64 v[102:103], v3 offset:776\nds_read_b64 v[96:97], v3 offset:256\nds_read_b64 v[98:99], v3 offset:264\n" \
  "ds_read_b64 v[92:93], v3 offset:832\nds_read_b64 v[94:95], v3 offset:840\nds_read_b64 v[88:89], v3 offset:320\n" \
  "ds_read_b64 v[90:91], v3 offset:328\nds_read_b64 v[84:85], v3 offset:896\nds_read_b64 v[86:87], v3 offset:904\n" \
  "ds_read_b64 v[80:81], v3 offset:384\nds_read_b64 v[82:83], v3 offset:392\nds_read_b64 v[76:77], v3 offset:960\n" \
  "ds_read_b64 v[78:79], v3 offset:968\nds_read_b64 v[72:73], v3 offset:448\nds_read_b64 v[74:75], v3 offset:456\n" \
  "ds_read_b64 v[68:69], v3 offset:1024\nds_read_b64 v[70:71], v3 offset:1032\nds_read_b64 v[64:65], v3 offset:512\n" \
  "ds_read_b64 v[66:67], v3 offset:520\nds_read_b64 v[60:61], v3 offset:0\nds_read_b64 v[62:63], v3 offset:8\nds_read_b64 v[56:57], v3 offset:1088\n" \
  "ds_read_b64 v[58:59], v3 offset:1096\ns_waitcnt lgkmcnt(0)\ns_barrier\nds_read_b128 v[36:39], v2 offset:0\nds_read_b128 v[40:43], v2 offset:64\n" \
  "ds_read_b128 v[44:47], v2 offset:128\nds_read_b128 v[48:51], v2 offset:192\ns_waitcnt lgkmcnt(3)\n" \
  "v_mfma_f32_16x16x32_f16 v[12:15], v[124:127], v[36:39], 0\ns_setprio 1\nds_read_b128 v[52:55], v2 offset:256\n" \
  "v_mfma_f32_16x16x32_f16 v[16:19], v[120:123], v[36:39], 0\ns_waitcnt lgkmcnt(3)\nv_mfma_f32_16x16x32_f16 v[12:15], v[116:119], v[40:43], v[12:15]\n" \
  "ds_read_b128 v[36:39], v2 offset:320\nv_mfma_f32_16x16x32_f16 v[16:19], v[112:115], v[40:43], v[16:19]\ns_waitcnt lgkmcnt(3)\n" \
  "v_mfma_f32_16x16x32_f16 v[12:15], v[108:111], v[44:47], v[12:15]\nds_read_b128 v[40:43], v2 offset:384\n" \
  "v_mfma_f32_16x16x32_f16 v[16:19], v[104:107], v[44:47], v[16:19]\ns_waitcnt lgkmcnt(3)\n" \
  "v_mfma_f32_16x16x32_f16 v[12:15], v[100:103], v[48:51], v[12:15]\nds_read_b128 v[44:47], v2 offset:448\n" \
  "v_mfma_f32_16x16x32_f16 v[16:19], v[96:99], v[48:51], v[16:19]\ns_waitcnt lgkmcnt(3)\n" \
  "v_mfma_f32_16x16x32_f16 v[12:15], v[92:95], v[52:55], v[12:15]\nv_mfma_f32_16x16x32_f16 v[16:19], v[88:91], v[52:55], v[16:19]\n" \
  "s_waitcnt lgkmcnt(2)\nv_mfma_f32_16x16x32_f16 v[12:15], v[84:87], v[36:39], v[12:15]\nds_read_b128 v[48:51], v2 offset:512\n" \
  "v_mfma_f32_16x16x32_f16 v[16:19], v[80:83], v[36:39], v[16:19]\ns_waitcnt lgkmcnt(2)\n" \
  "v_mfma_f32_16x16x32_f16 v[12:15], v[76:79], v[40:43], v[12:15]\nds_read_b128 v[52:55], v2 offset:576\n" \
  "v_mfma_f32_16x16x32_f16 v[16:19], v[72:75], v[40:43], v[16:19]\ns_waitcnt lgkmcnt(2)\n" \
  "v_mfma_f32_16x16x32_f16 v[12:15], v[68:71], v[44:47], v[12:15]\nds_read_b128 v[36:39], v2 offset:640\n" \
  "v_mfma_f32_16x16x32_f16 v[16:19], v[64:67], v[44:47], v[16:19]\nv_mfma_f32_16x16x32_f16 v[20:23], v[60:63], v[44:47], 0\n" \
  "ds_read_b128 v[40:43], v2 offset:704\nv_mfma_f32_16x16x32_f16 v[28:31], v[56:59], v[44:47], 0\ns_waitcnt lgkmcnt(3)\n" \
  "v_mfma_f32_16x16x32_f16 v[16:19], v[124:127], v[48:51], v[16:19]\nds_read_b128 v[56:59], v2 offset:768\nv_cvt_pkrtz_f16_f32 v32, v12, v13\n" \
  "v_mfma_f32_16x16x32_f16 v[20:23], v[120:123], v[48:51], v[20:23]\nv_cvt_pkrtz_f16_f32 v33, v14, v15\ns_waitcnt lgkmcnt(3)\n" \
  "v_mfma_f32_16x16x32_f16 v[16:19], v[116:119], v[52:55], v[16:19]\nds_read_b128 v[44:47], v2 offset:832\n" \
  "v_pk_add_u16 v32, v32, 4 op_sel_hi:[1,0] clamp\nv_mfma_f32_16x16x32_f16 v[20:23], v[112:115], v[52:55], v[20:23]\n" \
  "v_pk_add_u16 v33, v33, 4 op_sel_hi:[1,0] clamp\ns_waitcnt lgkmcnt(3)\nv_mfma_f32_16x16x32_f16 v[16:19], v[108:111], v[36:39], v[16:19]\n" \
  "ds_read_b128 v[48:51], v2 offset:896\nds_write_b64 v4, v[32:33] offset:0\nv_mfma_f32_16x16x32_f16 v[20:23], v[104:107], v[36:39], v[20:23]\n" \
  "s_waitcnt lgkmcnt(3)\nv_mfma_f32_16x16x32_f16 v[16:19], v[100:103], v[40:43], v[16:19]\nds_read_b128 v[52:55], v2 offset:960\n" \
  "v_mfma_f32_16x16x32_f16 v[20:23], v[96:99], v[40:43], v[20:23]\nv_mfma_f32_16x16x32_f16 v[16:19], v[92:95], v[56:59], v[16:19]\n" \
  "v_mfma_f32_16x16x32_f16 v[20:23], v[88:91], v[56:59], v[20:23]\nds_read_b128 v[36:39], v2 offset:1024\ns_waitcnt lgkmcnt(3)\n" \
  "v_mfma_f32_16x16x32_f16 v[16:19], v[84:87], v[44:47], v[16:19]\nv_mfma_f32_16x16x32_f16 v[20:23], v[80:83], v[44:47], v[20:23]\n" \
  "ds_read_b128 v[40:43], v2 offset:1088\nds_read_b64 v[56:57], v6 offset:256\nds_read_b64 v[58:59], v6 offset:264\n" \
  "v_mfma_f32_16x16x32_f16 v[16:19], v[76:79], v[48:51], v[16:19]\nv_mfma_f32_16x16x32_f16 v[20:23], v[72:75], v[48:51], v[20:23]\n" \
  "ds_read_b128 v[44:47], v2 offset:1152\ns_waitcnt lgkmcnt(4)\nv_mfma_f32_16x16x32_f16 v[16:19], v[68:71], v[52:55], v[16:19]\n" \
  "v_mfma_f32_16x16x32_f16 v[20:23], v[64:67], v[52:55], v[20:23]\nds_read_b128 v[48:51], v2 offset:1216\n" \
  "v_mfma_f32_16x16x32_f16 v[24:27], v[60:63], v[52:55], 0\nv_mfma_f32_16x16x32_f16 v[20:23], v[124:127], v[36:39], v[20:23]\n" \
  "ds_read_b128 v[52:55], v2 offset:1280\nv_mfma_f32_16x16x32_f16 v[24:27], v[120:123], v[36:39], v[24:27]\ns_waitcnt lgkmcnt(5)\n" \
  "v_mfma_f32_16x16x32_f16 v[20:23], v[116:119], v[40:43], v[20:23]\nds_read_b128 v[36:39], v2 offset:1344\nv_cvt_pkrtz_f16_f32 v32, v16, v17\n" \
  "v_mfma_f32_16x16x32_f16 v[24:27], v[112:115], v[40:43], v[24:27]\nv_cvt_pkrtz_f16_f32 v33, v18, v19\ns_waitcnt lgkmcnt(3)\n" \
  "v_mfma_f32_16x16x32_f16 v[20:23], v[108:111], v[44:47], v[20:23]\nds_read_b128 v[40:43], v2 offset:1408\n" \
  "v_pk_add_u16 v32, v32, 4 op_sel_hi:[1,0] clamp\nds_read_b32 v35, v9 offset:32\nv_mfma_f32_16x16x32_f16 v[24:27], v[104:107], v[44:47], v[24:27]\n" \
  "v_pk_add_u16 v33, v33, 4 op_sel_hi:[1,0] clamp\ns_waitcnt lgkmcnt(3)\nv_mfma_f32_16x16x32_f16 v[20:23], v[100:103], v[48:51], v[20:23]\n" \
  "ds_read_b128 v[44:47], v2 offset:1472\nds_write_b64 v4, v[32:33] offset:1024\nv_mfma_f32_16x16x32_f16 v[24:27], v[96:99], v[48:51], v[24:27]\n" \
  "v_mfma_f32_16x16x32_f16 v[20:23], v[92:95], v[52:55], v[20:23]\nv_mfma_f32_16x16x32_f16 v[24:27], v[88:91], v[52:55], v[24:27]\n" \
  "ds_read_b128 v[48:51], v2 offset:1536\ns_waitcnt lgkmcnt(3)\nv_readfirstlane_b32 s85, v35\ns_cmp_lt_u32 s85, 4\ns_cbranch_scc1 21f\n22:\n" \
  "v_mfma_f32_16x16x32_f16 v[20:23], v[84:87], v[36:39], v[20:23]\nv_mfma_f32_16x16x32_f16 v[24:27], v[80:83], v[36:39], v[24:27]\n" \
  "ds_read_b128 v[52:55], v2 offset:1600\nv_mfma_f32_16x16x32_f16 v[20:23], v[76:79], v[40:43], v[20:23]\n" \
  "v_mfma_f32_16x16x32_f16 v[24:27], v[72:75], v[40:43], v[24:27]\nds_read_b128 v[36:39], v2 offset:1664\ns_waitcnt lgkmcnt(4)\n" \
  "v_mfma_f32_16x16x32_f16 v[20:23], v[68:71], v[44:47], v[20:23]\nv_mfma_f32_16x16x32_f16 v[24:27], v[64:67], v[44:47], v[24:27]\n" \
  "ds_read_b128 v[40:43], v2 offset:1728\nv_mfma_f32_16x16x32_f16 v[28:31], v[60:63], v[44:47], v[28:31]\nds_read_b64_tr_b16 v[60:61], v5 offset:0\n" \
  "ds_read_b64_tr_b16 v[62:63], v5 offset:32\ns_waitcnt lgkmcnt(4)\nv_mfma_f32_16x16x32_f16 v[24:27], v[124:127], v[48:51], v[24:27]\n" \
  "v_mfma_f32_16x16x32_f16 v[28:31], v[120:123], v[48:51], v[28:31]\nds_read_b64_tr_b16 v[44:45], v5 offset:256\n" \
  "ds_read_b64_tr_b16 v[46:47], v5 offset:288\nv_mfma_f32_16x16x32_f16 v[24:27], v[116:119], v[52:55], v[24:27]\n" \
  "ds_read_b64 v[120:121], v6 offset:320\nds_read_b64 v[122:123], v6 offset:328\nv_mfma_f32_16x16x32_f16 v[28:31], v[112:115], v[52:55], v[28:31]\n" \
  "ds_read_b128 v[48:51], v2 offset:1792\ns_waitcnt lgkmcnt(7)\nv_mfma_f32_16x16x32_f16 v[24:27], v[108:111], v[36:39], v[24:27]\n" \
  "ds_read_b128 v[112:115], v2 offset:1856\nv_mfma_f32_16x16x32_f16 v[28:31], v[104:107], v[36:39], v[28:31]\nds_read_b64 v[52:53], v6 offset:128\n" \
  "ds_read_b64 v[54:55], v6 offset:136\nv_mfma_f32_16x16x32_f16 v[24:27], v[100:103], v[40:43], v[24:27]\nds_read_b128 v[104:107], v2 offset:1920\n" \
  "v_mfma_f32_16x16x32_f16 v[28:31], v[96:99], v[40:43], v[28:31]\nds_read_b64 v[36:37], v6 offset:192\nds_read_b64 v[38:39], v6 offset:200\n" \
  "s_waitcnt lgkmcnt(9)\nv_mfma_f32_16x16x32_f16 v[20:23], v[60:63], v[56:59], v[20:23]\nds_read_b128 v[96:99], v2 offset:1984\ns_waitcnt lgkmcnt(7)\n" \
  "v_mfma_f32_16x16x32_f16 v[20:23], v[44:47], v[120:123], v[20:23]\nv_mfma_f32_16x16x32_f16 v[24:27], v[92:95], v[48:51], v[24:27]\n" \
  "ds_read_b128 v[40:43], v2 offset:2048\nv_mfma_f32_16x16x32_f16 v[28:31], v[88:91], v[48:51], v[28:31]\nds_read_b64_tr_b16 v[88:89], v5 offset:512\n" \
  "ds_read_b64_tr_b16 v[90:91], v5 offset:544\ns_waitcnt lgkmcnt(6)\nv_mfma_f32_16x16x32_f16 v[24:27], v[84:87], v[112:115], v[24:27]\n" \
  "v_mfma_f32_16x16x32_f16 v[28:31], v[80:83], v[112:115], v[28:31]\nv_cvt_pkrtz_f16_f32 v32, v20, v21\nv_cvt_pkrtz_f16_f32 v33, v22, v23\n" \
  "v_mfma_f32_16x16x32_f16 v[24:27], v[76:79], v[104:107], v[24:27]\nds_read_b64_tr_b16 v[48:49], v5 offset:768\n" \
  "ds_read_b64_tr_b16 v[50:51], v5 offset:800\nv_mfma_f32_16x16x32_f16 v[28:31], v[72:75], v[104:107], v[28:31]\n" \
  "ds_read_b128 v[80:83], v2 offset:2112\ns_waitcnt lgkmcnt(5)\nv_mfma_f32_16x16x32_f16 v[24:27], v[68:71], v[96:99], v[24:27]\n" \
  "ds_read_b128 v[112:115], v2 offset:2176\nv_pk_add_u16 v32, v32, 4 op_sel_hi:[1,0] clamp\n" \
  "v_mfma_f32_16x16x32_f16 v[28:31], v[64:67], v[96:99], v[28:31]\nds_read_b128 v[72:75], v2 offset:2240\n" \
  "v_pk_add_u16 v33, v33, 4 op_sel_hi:[1,0] clamp\nv_mfma_f32_16x16x32_f16 v[28:31], v[124:127], v[40:43], v[28:31]\n" \
  "ds_read_b128 v[104:107], v2 offset:2304\nds_write_b64 v4, v[32:33] offset:2048\nv_mfma_f32_16x16x32_f16 v[24:27], v[60:63], v[52:55], v[24:27]\n" \
  "ds_read_b128 v[64:67], v2 offset:2368\nds_read_b64 v[96:97], v6 offset:0\nds_read_b64 v[98:99], v6 offset:8\n" \
  "v_mfma_f32_16x16x32_f16 v[24:27], v[44:47], v[36:39], v[24:27]\nds_read_b128 v[124:127], v2 offset:2432\ns_waitcnt lgkmcnt(9)\n" \
  "v_mfma_f32_16x16x32_f16 v[24:27], v[88:91], v[56:59], v[24:27]\nds_read_b128 v[40:43], v2 offset:2496\n" \
  "v_mfma_f32_16x16x32_f16 v[24:27], v[48:51], v[120:123], v[24:27]\ns_waitcnt lgkmcnt(8)\n" \
  "v_mfma_f32_16x16x32_f16 v[28:31], v[116:119], v[80:83], v[28:31]\nds_read_b64 v[116:117], v6 offset:64\nds_read_b64 v[118:119], v6 offset:72\n" \
  "v_mfma_f32_16x16x32_f16 v[28:31], v[108:111], v[112:115], v[28:31]\nds_read_b64_tr_b16 v[80:81], v5 offset:1024\n" \
  "ds_read_b64_tr_b16 v[82:83], v5 offset:1056\ns_waitcnt lgkmcnt(10)\nv_mfma_f32_16x16x32_f16 v[28:31], v[100:103], v[72:75], v[28:31]\n" \
  "v_cvt_pkrtz_f16_f32 v32, v24, v25\nv_cvt_pkrtz_f16_f32 v33, v26, v27\nv_mfma_f32_16x16x32_f16 v[28:31], v[92:95], v[104:107], v[28:31]\n" \
  "v_pk_add_u16 v32, v32, 4 op_sel_hi:[1,0] clamp\ns_waitcnt lgkmcnt(5)\nv_mfma_f32_16x16x32_f16 v[28:31], v[84:87], v[64:67], v[28:31]\n" \
  "ds_read_b64_tr_b16 v[108:109], v5 offset:1280\nds_read_b64_tr_b16 v[110:111], v5 offset:1312\n" \
  "v_mfma_f32_16x16x32_f16 v[28:31], v[76:79], v[124:127], v[28:31]\nds_read_b64_tr_b16 v[112:113], v7 offset:0\n" \
  "ds_read_b64_tr_b16 v[114:115], v7 offset:64\ns_waitcnt lgkmcnt(6)\nv_mfma_f32_16x16x32_f16 v[28:31], v[68:71], v[40:43], v[28:31]\n" \
  "ds_read2_b32 v[100:101], v8 offset0:48 offset1:49\nds_read2_b32 v[102:103], v8 offset0:50 offset1:51\n" \
  "v_mfma_f32_16x16x32_f16 v[28:31], v[60:63], v[96:99], v[28:31]\nv_pk_add_u16 v33, v33, 4 op_sel_hi:[1,0] clamp\n" \
  "ds_write_b64 v4, v[32:33] offset:3072\nv_mfma_f32_16x16x32_f16 v[28:31], v[44:47], v[116:119], v[28:31]\n" \
  "v_mfma_f32_16x16x32_f16 v[28:31], v[88:91], v[52:55], v[28:31]\nv_mfma_f32_16x16x32_f16 v[28:31], v[48:51], v[36:39], v[28:31]\n" \
  "s_waitcnt lgkmcnt(5)\nv_mfma_f32_16x16x32_f16 v[28:31], v[80:83], v[56:59], v[28:31]\n" \
  "v_mfma_f32_16x16x32_f16 v[28:31], v[108:111], v[120:123], v[28:31]\ns_waitcnt lgkmcnt(1)\n" \
  "v_mfma_f32_16x16x32_f16 v[28:31], v[112:115], v[100:103], v[28:31]\ns_nop 7\nv_cvt_pkrtz_f16_f32 v32, v28, v29\nv_cvt_pkrtz_f16_f32 v33, v30, v31\n" \
  "v_pk_add_u16 v32, v32, 4 op_sel_hi:[1,0] clamp\nv_pk_add_u16 v33, v33, 4 op_sel_hi:[1,0] clamp\nds_write_b64 v4, v[32:33] offset:4096\n" \
  "s_setprio 0\ns_setpc_b64 s[90:91]\n21:\ns_movk_i32 s84, 0x4000\n23:\nds_read_b32 v35, v9 offset:32\ns_sub_u32 s84, s84, 1\ns_cbranch_scc1 22b\n" \
  "s_waitcnt lgkmcnt(0)\nv_readfirstlane_b32 s85, v35\ns_cmp_lt_u32 s85, 4\ns_cbranch_scc1 23b\ns_branch 22b\n1:\n" \
  "ds_read_b64 v[124:125], v3 offset:1088\nds_read_b64 v[126:127], v3 offset:1096\nds_read_b64 v[120:121], v3 offset:0\n" \
  "ds_read_b64 v[122:123], v3 offset:8\nds_read_b64 v[116:117], v3 offset:64\nds_read_b64 v[118:119], v3 offset:72\n" \
  "ds_read_b64 v[112:113], v3 offset:128\nds_read_b64 v[114:115], v3 offset:136\nds_read_b64 v[108:109], v3 offset:192\n" \
  "ds_read_b64 v[110:111], v3 offset:200\nds_read_b64 v[104:105], v3 offset:256\nds_read_b64 v[106:107], v3 offset:264\n" \
  "ds_read_b64 v[100:101], v3 offset:320\nds_read_b64 v[102:103], v3 offset:328\nds_read_b64 v[96:97], v3 offset:384\n" \
  "ds_read_b64 v[98:99], v3 offset:392\nds_read_b64 v[92:93], v3 offset:448\nds_read_b64 v[94:95], v3 offset:456\n" \
  "ds_read_b64 v[88:89], v3 offset:512\nds_read_b64 v[90:91], v3 offset:520\nds_read_b64 v[84:85], v3 offset:576\n" \
  "ds_read_b64 v[86:87], v3 offset:584\nds_read_b64 v[80:81], v3 offset:640\nds_read_b64 v[82:83], v3 offset:648\n" \
  "ds_read_b64 v[76:77], v3 offset:704\nds_read_b64 v[78:79], v3 offset:712\nds_read_b64 v[72:73], v3 offset:768\n" \
  "ds_read_b64 v[74:75], v3 offset:776\nds_read_b64 v[68:69], v3 offset:832\nds_read_b64 v[70:71], v3 offset:840\n" \
  "ds_read_b64 v[64:65], v3 offset:896\nds_read_b64 v[66:67], v3 offset:904\nds_read_b64 v[60:61], v3 offset:960\n" \
  "ds_read_b64 v[62:63], v3 offset:968\nds_read_b64 v[56:57], v3 offset:1024\nds_read_b64 v[58:59], v3 offset:1032\n" \
  "ds_read_b64 v[52:53], v6 offset:0\nds_read_b64 v[54:55], v6 offset:8\nds_read_b64 v[48:49], v6 offset:64\nds_read_b64 v[50:51], v6 offset:72\n" \
  "s_waitcnt lgkmcnt(0)\ns_barrier\nds_read_b128 v[28:31], v2 offset:960\nds_read_b128 v[32:35], v2 offset:1472\n" \
  "ds_read_b128 v[36:39], v2 offset:1984\nds_read_b128 v[40:43], v2 offset:2048\nds_read_b128 v[44:47], v2 offset:2112\ns_waitcnt lgkmcnt(3)\n" \
  "v_mfma_f32_16x16x32_f16 v[12:15], v[124:127], v[28:31], 0\ns_setprio 1\nds_read_b128 v[28:31], v2 offset:2176\n" \
  "v_mfma_f32_16x16x32_f16 v[16:19], v[124:127], v[32:35], 0\nds_read_b128 v[32:35], v2 offset:2240\ns_waitcnt lgkmcnt(3)\n" \
  "v_mfma_f32_16x16x32_f16 v[12:15], v[120:123], v[36:39], v[12:15]\nv_mfma_f32_16x16x32_f16 v[20:23], v[124:127], v[36:39], 0\n" \
  "ds_read_b128 v[124:127], v2 offset:2304\nds_read_b128 v[36:39], v2 offset:2368\nv_mfma_f32_16x16x32_f16 v[12:15], v[116:119], v[40:43], v[12:15]\n" \
  "ds_read_b128 v[40:43], v2 offset:2432\ns_waitcnt lgkmcnt(4)\nv_mfma_f32_16x16x32_f16 v[12:15], v[112:115], v[44:47], v[12:15]\n" \
  "ds_read_b128 v[44:47], v2 offset:2496\nv_mfma_f32_16x16x32_f16 v[12:15], v[108:111], v[28:31], v[12:15]\nds_read_b128 v[28:31], v2 offset:2560\n" \
  "s_waitcnt lgkmcnt(4)\nv_mfma_f32_16x16x32_f16 v[12:15], v[104:107], v[32:35], v[12:15]\nds_read_b128 v[32:35], v2 offset:2624\n" \
  "v_mfma_f32_16x16x32_f16 v[12:15], v[100:103], v[124:127], v[12:15]\ns_waitcnt lgkmcnt(3)\n" \
  "v_mfma_f32_16x16x32_f16 v[12:15], v[96:99], v[36:39], v[12:15]\nds_read_b128 v[124:127], v2 offset:2688\n" \
  "v_mfma_f32_16x16x32_f16 v[12:15], v[92:95], v[40:43], v[12:15]\ns_waitcnt lgkmcnt(3)\n" \
  "v_mfma_f32_16x16x32_f16 v[12:15], v[88:91], v[44:47], v[12:15]\nds_read_b128 v[36:39], v2 offset:2752\n" \
  "v_mfma_f32_16x16x32_f16 v[16:19], v[120:123], v[44:47], v[16:19]\ns_waitcnt lgkmcnt(3)\n" \
  "v_mfma_f32_16x16x32_f16 v[12:15], v[84:87], v[28:31], v[12:15]\nds_read_b128 v[40:43], v2 offset:2816\n" \
  "v_mfma_f32_16x16x32_f16 v[16:19], v[116:119], v[28:31], v[16:19]\ns_waitcnt lgkmcnt(3)\n" \
  "v_mfma_f32_16x16x32_f16 v[12:15], v[80:83], v[32:35], v[12:15]\nds_read_b128 v[44:47], v2 offset:2880\n" \
  "v_mfma_f32_16x16x32_f16 v[16:19], v[112:115], v[32:35], v[16:19]\ns_waitcnt lgkmcnt(3)\n" \
  "v_mfma_f32_16x16x32_f16 v[12:15], v[76:79], v[124:127], v[12:15]\nds_read_b128 v[28:31], v2 offset:2944\n" \
  "v_mfma_f32_16x16x32_f16 v[16:19], v[108:111], v[124:127], v[16:19]\ns_waitcnt lgkmcnt(3)\n" \
  "v_mfma_f32_16x16x32_f16 v[12:15], v[72:75], v[36:39], v[12:15]\nds_read_b128 v[32:35], v2 offset:3008\n" \
  "v_mfma_f32_16x16x32_f16 v[16:19], v[104:107], v[36:39], v[16:19]\ns_waitcnt lgkmcnt(3)\n" \
  "v_mfma_f32_16x16x32_f16 v[12:15], v[68:71], v[40:43], v[12:15]\nv_mfma_f32_16x16x32_f16 v[16:19], v[100:103], v[40:43], v[16:19]\n" \
  "ds_read_b128 v[124:127], v2 offset:3072\ns_waitcnt lgkmcnt(3)\nv_mfma_f32_16x16x32_f16 v[12:15], v[64:67], v[44:47], v[12:15]\n" \
  "v_mfma_f32_16x16x32_f16 v[16:19], v[96:99], v[44:47], v[16:19]\nds_read_b128 v[36:39], v2 offset:3136\ns_waitcnt lgkmcnt(3)\n" \
  "v_mfma_f32_16x16x32_f16 v[12:15], v[60:63], v[28:31], v[12:15]\nds_read_b64 v[40:41], v6 offset:128\nds_read_b64 v[42:43], v6 offset:136\n" \
  "v_mfma_f32_16x16x32_f16 v[16:19], v[92:95], v[28:31], v[16:19]\nds_read_b128 v[44:47], v2 offset:3200\ns_waitcnt lgkmcnt(4)\n" \
  "v_mfma_f32_16x16x32_f16 v[12:15], v[56:59], v[32:35], v[12:15]\nv_mfma_f32_16x16x32_f16 v[16:19], v[88:91], v[32:35], v[16:19]\n" \
  "ds_read_b128 v[28:31], v2 offset:3264\nv_mfma_f32_16x16x32_f16 v[20:23], v[120:123], v[32:35], v[20:23]\nds_read_b64 v[120:121], v6 offset:192\n" \
  "ds_read_b64 v[122:123], v6 offset:200\nv_mfma_f32_16x16x32_f16 v[16:19], v[84:87], v[124:127], v[16:19]\nds_read_b128 v[32:35], v2 offset:3328\n" \
  "v_mfma_f32_16x16x32_f16 v[20:23], v[116:119], v[124:127], v[20:23]\nds_read_b64 v[116:117], v6 offset:256\nds_read_b64 v[118:119], v6 offset:264\n" \
  "s_waitcnt lgkmcnt(6)\nv_mfma_f32_16x16x32_f16 v[16:19], v[80:83], v[36:39], v[16:19]\nds_read_b128 v[124:127], v2 offset:3392\n" \
  "v_mfma_f32_16x16x32_f16 v[20:23], v[112:115], v[36:39], v[20:23]\nds_read_b64 v[112:113], v6 offset:320\nds_read_b64 v[114:115], v6 offset:328\n" \
  "v_mfma_f32_16x16x32_f16 v[16:19], v[76:79], v[44:47], v[16:19]\nds_read_b128 v[36:39], v2 offset:3456\n" \
  "v_mfma_f32_16x16x32_f16 v[20:23], v[108:111], v[44:47], v[20:23]\ns_waitcnt lgkmcnt(6)\n" \
  "v_mfma_f32_16x16x32_f16 v[16:19], v[72:75], v[28:31], v[16:19]\nds_read_b128 v[108:111], v2 offset:3520\n" \
  "v_mfma_f32_16x16x32_f16 v[20:23], v[104:107], v[28:31], v[20:23]\nv_mfma_f32_16x16x32_f16 v[16:19], v[68:71], v[32:35], v[16:19]\n" \
  "ds_read_b128 v[44:47], v2 offset:3584\nv_mfma_f32_16x16x32_f16 v[20:23], v[100:103], v[32:35], v[20:23]\nds_read_b128 v[104:107], v2 offset:3648\n" \
  "ds_read_b32 v27, v9 offset:32\ns_waitcnt lgkmcnt(4)\nv_mfma_f32_16x16x32_f16 v[16:19], v[64:67], v[124:127], v[16:19]\n" \
  "ds_read_b128 v[28:31], v2 offset:3712\nv_mfma_f32_16x16x32_f16 v[20:23], v[96:99], v[124:127], v[20:23]\nds_read_b128 v[100:103], v2 offset:3776\n" \
  "ds_read2_b32 v[32:33], v8 offset0:32 offset1:33\nds_read2_b32 v[34:35], v8 offset0:34 offset1:35\n" \
  "v_mfma_f32_16x16x32_f16 v[16:19], v[60:63], v[36:39], v[16:19]\nds_read_b128 v[96:99], v2 offset:3840\n" \
  "v_mfma_f32_16x16x32_f16 v[20:23], v[92:95], v[36:39], v[20:23]\nds_read_b128 v[124:127], v2 offset:3904\ns_waitcnt lgkmcnt(8)\n" \
  "v_mfma_f32_16x16x32_f16 v[16:19], v[56:59], v[108:111], v[16:19]\nds_read_b128 v[92:95], v2 offset:3968\ns_waitcnt lgkmcnt(7)\n" \
  "v_readfirstlane_b32 s85, v27\ns_cmp_lt_u32 s85, 4\ns_cbranch_scc1 31f\n32:\nv_mfma_f32_16x16x32_f16 v[20:23], v[88:91], v[108:111], v[20:23]\n" \
  "ds_read_b128 v[36:39], v2 offset:4032\nds_read_b64_tr_b16 v[88:89], v5 offset:512\nds_read_b64_tr_b16 v[90:91], v5 offset:544\n" \
  "v_mfma_f32_16x16x32_f16 v[20:23], v[84:87], v[44:47], v[20:23]\nds_read_b64_tr_b16 v[108:109], v5 offset:768\n" \
  "ds_read_b64_tr_b16 v[110:111], v5 offset:800\nv_mfma_f32_16x16x32_f16 v[20:23], v[80:83], v[104:107], v[20:23]\n" \
  "ds_read_b64_tr_b16 v[84:85], v5 offset:1024\nds_read_b64_tr_b16 v[86:87], v5 offset:1056\ns_waitcnt lgkmcnt(12)\n" \
  "v_mfma_f32_16x16x32_f16 v[20:23], v[76:79], v[28:31], v[20:23]\nds_read_b64_tr_b16 v[44:45], v5 offset:1280\n" \
  "ds_read_b64_tr_b16 v[46:47], v5 offset:1312\nv_mfma_f32_16x16x32_f16 v[20:23], v[72:75], v[100:103], v[20:23]\ns_waitcnt lgkmcnt(10)\n" \
  "v_mfma_f32_16x16x32_f16 v[20:23], v[68:71], v[96:99], v[20:23]\nds_read_b64_tr_b16 v[80:81], v5 offset:1536\n" \
  "ds_read_b64_tr_b16 v[82:83], v5 offset:1568\nv_mfma_f32_16x16x32_f16 v[20:23], v[64:67], v[124:127], v[20:23]\n" \
  "ds_read_b64_tr_b16 v[104:105], v5 offset:1792\nds_read_b64_tr_b16 v[106:107], v5 offset:1824\ns_waitcnt lgkmcnt(12)\n" \
  "v_mfma_f32_16x16x32_f16 v[20:23], v[60:63], v[92:95], v[20:23]\nds_read_b64_tr_b16 v[76:77], v5 offset:2048\n" \
  "ds_read_b64_tr_b16 v[78:79], v5 offset:2080\nv_mfma_f32_16x16x32_f16 v[20:23], v[56:59], v[36:39], v[20:23]\ns_waitcnt lgkmcnt(10)\n" \
  "v_mfma_f32_16x16x32_f16 v[12:15], v[88:91], v[52:55], v[12:15]\nds_read_b64_tr_b16 v[28:29], v5 offset:2304\n" \
  "ds_read_b64_tr_b16 v[30:31], v5 offset:2336\nv_mfma_f32_16x16x32_f16 v[12:15], v[108:111], v[48:51], v[12:15]\n" \
  "ds_read_b64_tr_b16 v[72:73], v5 offset:2560\nds_read_b64_tr_b16 v[74:75], v5 offset:2592\ns_waitcnt lgkmcnt(10)\n" \
  "v_mfma_f32_16x16x32_f16 v[12:15], v[84:87], v[40:43], v[12:15]\nds_read_b64_tr_b16 v[100:101], v5 offset:2816\n" \
  "ds_read_b64_tr_b16 v[102:103], v5 offset:2848\nv_mfma_f32_16x16x32_f16 v[16:19], v[84:87], v[52:55], v[16:19]\n" \
  "ds_read_b64_tr_b16 v[68:69], v7 offset:0\nds_read_b64_tr_b16 v[70:71], v7 offset:64\n" \
  "v_mfma_f32_16x16x32_f16 v[12:15], v[44:47], v[120:123], v[12:15]\nv_mfma_f32_16x16x32_f16 v[16:19], v[44:47], v[48:51], v[16:19]\n" \
  "s_waitcnt lgkmcnt(10)\nv_mfma_f32_16x16x32_f16 v[12:15], v[80:83], v[116:119], v[12:15]\nds_read2_b32 v[96:97], v8 offset0:16 offset1:17\n" \
  "ds_read2_b32 v[98:99], v8 offset0:18 offset1:19\nv_mfma_f32_16x16x32_f16 v[16:19], v[80:83], v[40:43], v[16:19]\n" \
  "ds_read2_b32 v[64:65], v8 offset0:0 offset1:1\nds_read2_b32 v[66:67], v8 offset0:2 offset1:3\n" \
  "v_mfma_f32_16x16x32_f16 v[20:23], v[80:83], v[52:55], v[20:23]\nv_mfma_f32_16x16x32_f16 v[12:15], v[104:107], v[112:115], v[12:15]\n" \
  "v_mfma_f32_16x16x32_f16 v[16:19], v[104:107], v[120:123], v[16:19]\nv_mfma_f32_16x16x32_f16 v[20:23], v[104:107], v[48:51], v[20:23]\n" \
  "s_waitcnt lgkmcnt(10)\nv_mfma_f32_16x16x32_f16 v[16:19], v[76:79], v[116:119], v[16:19]\nds_read_b64_tr_b16 v[124:125], v7 offset:512\n" \
  "ds_read_b64_tr_b16 v[126:127], v7 offset:576\nv_mfma_f32_16x16x32_f16 v[20:23], v[76:79], v[40:43], v[20:23]\n" \
  "ds_read2_b32 v[60:61], v8 offset0:48 offset1:49\nds_read2_b32 v[62:63], v8 offset0:50 offset1:51\n" \
  "v_mfma_f32_16x16x32_f16 v[16:19], v[28:31], v[112:115], v[16:19]\nv_mfma_f32_16x16x32_f16 v[20:23], v[28:31], v[120:123], v[20:23]\n" \
  "s_waitcnt lgkmcnt(10)\nv_mfma_f32_16x16x32_f16 v[20:23], v[72:75], v[116:119], v[20:23]\nds_read_b64_tr_b16 v[92:93], v7 offset:1024\n" \
  "ds_read_b64_tr_b16 v[94:95], v7 offset:1088\nv_mfma_f32_16x16x32_f16 v[20:23], v[100:103], v[112:115], v[20:23]\n" \
  "ds_read_b64_tr_b16 v[56:57], v7 offset:1536\nds_read_b64_tr_b16 v[58:59], v7 offset:1600\ns_waitcnt lgkmcnt(10)\n" \
  "v_mfma_f32_16x16x32_f16 v[12:15], v[68:71], v[32:35], v[12:15]\nv_mfma_f32_16x16x32_f16 v[16:19], v[68:71], v[96:99], v[16:19]\n" \
  "s_waitcnt lgkmcnt(6)\nv_mfma_f32_16x16x32_f16 v[20:23], v[68:71], v[64:67], v[20:23]\ns_waitcnt lgkmcnt(2)\n" \
  "v_mfma_f32_16x16x32_f16 v[12:15], v[124:127], v[60:63], v[12:15]\nv_mfma_f32_16x16x32_f16 v[16:19], v[124:127], v[32:35], v[16:19]\n" \
  "v_mfma_f32_16x16x32_f16 v[20:23], v[124:127], v[96:99], v[20:23]\nv_mfma_f32_16x16x32_f16 v[16:19], v[92:95], v[60:63], v[16:19]\n" \
  "v_mfma_f32_16x16x32_f16 v[20:23], v[92:95], v[32:35], v[20:23]\ns_waitcnt lgkmcnt(0)\n" \
  "v_mfma_f32_16x16x32_f16 v[20:23], v[56:59], v[60:63], v[20:23]\ns_nop 1\nv_cvt_pkrtz_f16_f32 v24, v12, v13\nv_cvt_pkrtz_f16_f32 v25, v14, v15\n" \
  "v_pk_add_u16 v24, v24, 4 op_sel_hi:[1,0] clamp\nv_pk_add_u16 v25, v25, 4 op_sel_hi:[1,0] clamp\nds_write_b64 v4, v[24:25] offset:5120\n" \
  "v_cvt_pkrtz_f16_f32 v24, v16, v17\nv_cvt_pkrtz_f16_f32 v25, v18, v19\nv_pk_add_u16 v24, v24, 4 op_sel_hi:[1,0] clamp\n" \
  "v_pk_add_u16 v25, v25, 4 op_sel_hi:[1,0] clamp\nds_write_b64 v4, v[24:25] offset:6144\nv_cvt_pkrtz_f16_f32 v24, v20, v21\n" \
  "v_cvt_pkrtz_f16_f32 v25, v22, v23\nv_pk_add_u16 v24, v24, 4 op_sel_hi:[1,0] clamp\nv_pk_add_u16 v25, v25, 4 op_sel_hi:[1,0] clamp\n" \
  "ds_write_b64 v4, v[24:25] offset:7168\ns_setprio 0\ns_setpc_b64 s[90:91]\n31:\ns_movk_i32 s84, 0x4000\n33:\nds_read_b32 v27, v9 offset:32\n" \
  "s_sub_u32 s84, s84, 1\ns_cbranch_scc1 32b\ns_waitcnt lgkmcnt(0)\nv_readfirstlane_b32 s85, v27\ns_cmp_lt_u32 s85, 4\ns_cbranch_scc1 33b\n" \
  "s_branch 32b\n9:\ns_cmp_eq_u32 s89, 3\ns_cbranch_scc1 3f\ns_cmp_eq_u32 s89, 4\ns_cbranch_scc1 4f\ns_cmp_eq_u32 s89, 5\ns_cbranch_scc1 5f\n" \
  "ds_read_b64 v[124:125], v3 offset:576\nds_read_b64 v[126:127], v3 offset:584\nds_read_b64 v[120:121], v3 offset:64\n" \
  "ds_read_b64 v[122:123], v3 offset:72\nds_read_b64 v[116:117], v3 offset:640\nds_read_b64 v[118:119], v3 offset:648\n" \
  "ds_read_b64 v[112:113], v3 offset:128\nds_read_b64 v[114:115], v3 offset:136\nds_read_b64 v[108:109], v3 offset:704\n" \
  "ds_read_b64 v[110:111], v3 offset:712\nds_read_b64 v[104:105], v3 offset:192\nds_read_b64 v[106:107], v3 offset:200\n" \
  "ds_read_b64 v[100:101], v3 offset:768\nds_read_b64 v[102:103], v3 offset:776\nds_read_b64 v[96:97], v3 offset:256\n" \
  "ds_read_b64 v[98:99], v3 offset:264\nds_read_b64 v[92:93], v3 offset:832\nds_read_b64 v[94:95], v3 offset:840\n" \
  "ds_read_b64 v[88:89], v3 offset:320\nds_read_b64 v[90:91], v3 offset:328\nds_read_b64 v[84:85], v3 offset:896\n" \
  "ds_read_b64 v[86:87], v3 offset:904\nds_read_b64 v[80:81], v3 offset:384\nds_read_b64 v[82:83], v3 offset:392\n" \
  "ds_read_b64 v[76:77], v3 offset:960\nds_read_b64 v[78:79], v3 offset:968\nds_read_b64 v[72:73], v3 offset:448\n" \
  "ds_read_b64 v[74:75], v3 offset:456\nds_read_b64 v[68:69], v3 offset:1024\nds_read_b64 v[70:71], v3 offset:1032\n" \
  "ds_read_b64 v[64:65], v3 offset:512\nds_read_b64 v[66:67], v3 offset:520\ns_waitcnt lgkmcnt(0)\ns_barrier\nds_read_b128 v[24:27], v2 offset:0\n" \
  "ds_read_b128 v[28:31], v2 offset:64\nds_read_b128 v[32:35], v2 offset:128\nds_read_b128 v[36:39], v2 offset:192\ns_waitcnt lgkmcnt(3)\n" \
  "v_mfma_f32_16x16x32_f16 v[12:15], v[124:127], v[24:27], 0\ns_setprio 1\nds_read_b128 v[40:43], v2 offset:256\n" \
  "v_mfma_f32_16x16x32_f16 v[16:19], v[120:123], v[24:27], 0\ns_waitcnt lgkmcnt(3)\nv_mfma_f32_16x16x32_f16 v[12:15], v[116:119], v[28:31], v[12:15]\n" \
  "ds_read_b128 v[44:47], v2 offset:320\nv_mfma_f32_16x16x32_f16 v[16:19], v[112:115], v[28:31], v[16:19]\ns_waitcnt lgkmcnt(3)\n" \
  "v_mfma_f32_16x16x32_f16 v[12:15], v[108:111], v[32:35], v[12:15]\nds_read_b128 v[48:51], v2 offset:384\n" \
  "v_mfma_f32_16x16x32_f16 v[16:19], v[104:107], v[32:35], v[16:19]\ns_waitcnt lgkmcnt(3)\n" \
  "v_mfma_f32_16x16x32_f16 v[12:15], v[100:103], v[36:39], v[12:15]\nds_read_b128 v[52:55], v2 offset:448\n" \
  "v_mfma_f32_16x16x32_f16 v[16:19], v[96:99], v[36:39], v[16:19]\ns_waitcnt lgkmcnt(3)\n" \
  "v_mfma_f32_16x16x32_f16 v[12:15], v[92:95], v[40:43], v[12:15]\nds_read_b128 v[56:59], v2 offset:512\n" \
  "v_mfma_f32_16x16x32_f16 v[16:19], v[88:91], v[40:43], v[16:19]\nds_read_b128 v[60:63], v2 offset:576\ns_waitcnt lgkmcnt(3)\n" \
  "v_mfma_f32_16x16x32_f16 v[12:15], v[84:87], v[44:47], v[12:15]\nds_read_b128 v[120:123], v2 offset:640\n" \
  "v_mfma_f32_16x16x32_f16 v[16:19], v[80:83], v[44:47], v[16:19]\nds_read_b128 v[24:27], v2 offset:704\n" \
  "v_mfma_f32_16x16x32_f16 v[12:15], v[76:79], v[48:51], v[12:15]\nds_read_b128 v[112:115], v2 offset:768\n" \
  "v_mfma_f32_16x16x32_f16 v[16:19], v[72:75], v[48:51], v[16:19]\nds_read_b128 v[28:31], v2 offset:832\ns_waitcnt lgkmcnt(5)\n" \
  "v_mfma_f32_16x16x32_f16 v[12:15], v[68:71], v[52:55], v[12:15]\nds_read_b128 v[104:107], v2 offset:896\n" \
  "v_mfma_f32_16x16x32_f16 v[16:19], v[64:67], v[52:55], v[16:19]\nds_read_b128 v[32:35], v2 offset:960\n" \
  "v_mfma_f32_16x16x32_f16 v[16:19], v[124:127], v[56:59], v[16:19]\ns_waitcnt lgkmcnt(5)\n" \
  "v_mfma_f32_16x16x32_f16 v[16:19], v[116:119], v[60:63], v[16:19]\nv_mfma_f32_16x16x32_f16 v[16:19], v[108:111], v[120:123], v[16:19]\n" \
  "s_waitcnt lgkmcnt(3)\nv_mfma_f32_16x16x32_f16 v[16:19], v[100:103], v[24:27], v[16:19]\nv_cvt_pkrtz_f16_f32 v20, v12, v13\n" \
  "v_cvt_pkrtz_f16_f32 v21, v14, v15\nv_mfma_f32_16x16x32_f16 v[16:19], v[92:95], v[112:115], v[16:19]\n" \
  "v_pk_add_u16 v20, v20, 4 op_sel_hi:[1,0] clamp\ns_waitcnt lgkmcnt(2)\nv_mfma_f32_16x16x32_f16 v[16:19], v[84:87], v[28:31], v[16:19]\n" \
  "v_pk_add_u16 v21, v21, 4 op_sel_hi:[1,0] clamp\ns_waitcnt lgkmcnt(1)\nv_mfma_f32_16x16x32_f16 v[16:19], v[76:79], v[104:107], v[16:19]\n" \
  "ds_write_b64 v4, v[20:21] offset:0\ns_waitcnt lgkmcnt(1)\nv_mfma_f32_16x16x32_f16 v[16:19], v[68:71], v[32:35], v[16:19]\ns_nop 7\n" \
  "v_cvt_pkrtz_f16_f32 v20, v16, v17\nv_cvt_pkrtz_f16_f32 v21, v18, v19\nv_pk_add_u16 v20, v20, 4 op_sel_hi:[1,0] clamp\n" \
  "v_pk_add_u16 v21, v21, 4 op_sel_hi:[1,0] clamp\nds_write_b64 v4, v[20:21] offset:1024\ns_setprio 0\ns_setpc_b64 s[90:91]\n3:\n" \
  "ds_read_b64 v[124:125], v3 offset:0\nds_read_b64 v[126:127], v3 offset:8\nds_read_b64 v[120:121], v3 offset:64\n" \
  "ds_read_b64 v[122:123], v3 offset:72\nds_read_b64 v[116:117], v3 offset:128\nds_read_b64 v[118:119], v3 offset:136\n" \
  "ds_read_b64 v[112:113], v3 offset:192\nds_read_b64 v[114:115], v3 offset:200\nds_read_b64 v[108:109], v3 offset:256\n" \
  "ds_read_b64 v[110:111], v3 offset:264\nds_read_b64 v[104:105], v3 offset:320\nds_read_b64 v[106:107], v3 offset:328\n" \
  "ds_read_b64 v[100:101], v3 offset:384\nds_read_b64 v[102:103], v3 offset:392\nds_read_b64 v[96:97], v3 offset:448\n" \
  "ds_read_b64 v[98:99], v3 offset:456\nds_read_b64 v[92:93], v3 offset:512\nds_read_b64 v[94:95], v3 offset:520\n" \
  "ds_read_b64 v[88:89], v3 offset:576\nds_read_b64 v[90:91], v3 offset:584\nds_read_b64 v[84:85], v3 offset:640\n" \
  "ds_read_b64 v[86:87], v3 offset:648\nds_read_b64 v[80:81], v3 offset:704\nds_read_b64 v[82:83], v3 offset:712\n" \
  "ds_read_b64 v[76:77], v3 offset:768\nds_read_b64 v[78:79], v3 offset:776\nds_read_b64 v[72:73], v3 offset:832\n" \
  "ds_read_b64 v[74:75], v3 offset:840\nds_read_b64 v[68:69], v3 offset:896\nds_read_b64 v[70:71], v3 offset:904\n" \
  "ds_read_b64 v[64:65], v3 offset:960\nds_read_b64 v[66:67], v3 offset:968\nds_read_b64 v[60:61], v3 offset:1024\n" \
  "ds_read_b64 v[62:63], v3 offset:1032\nds_read_b64 v[56:57], v6 offset:256\nds_read_b64 v[58:59], v6 offset:264\n" \
  "ds_read_b64 v[52:53], v6 offset:128\nds_read_b64 v[54:55], v6 offset:136\nds_read_b64 v[48:49], v6 offset:320\n" \
  "ds_read_b64 v[50:51], v6 offset:328\nds_read_b64 v[44:45], v6 offset:192\nds_read_b64 v[46:47], v6 offset:200\ns_waitcnt lgkmcnt(0)\ns_barrier\n" \
  "ds_read_b128 v[24:27], v2 offset:448\nds_read_b128 v[28:31], v2 offset:512\nds_read_b128 v[32:35], v2 offset:576\n" \
  "ds_read_b128 v[36:39], v2 offset:640\nds_read_b128 v[40:43], v2 offset:704\ns_waitcnt lgkmcnt(3)\n" \
  "v_mfma_f32_16x16x32_f16 v[12:15], v[124:127], v[24:27], 0\ns_setprio 1\nds_read_b128 v[24:27], v2 offset:768\n" \
  "v_mfma_f32_16x16x32_f16 v[12:15], v[120:123], v[28:31], v[12:15]\nds_read_b128 v[28:31], v2 offset:832\ns_waitcnt lgkmcnt(3)\n" \
  "v_mfma_f32_16x16x32_f16 v[12:15], v[116:119], v[32:35], v[12:15]\nds_read_b128 v[32:35], v2 offset:896\n" \
  "v_mfma_f32_16x16x32_f16 v[12:15], v[112:115], v[36:39], v[12:15]\nds_read_b128 v[36:39], v2 offset:960\ns_waitcnt lgkmcnt(3)\n" \
  "v_mfma_f32_16x16x32_f16 v[12:15], v[108:111], v[40:43], v[12:15]\nds_read_b128 v[40:43], v2 offset:1024\n" \
  "v_mfma_f32_16x16x32_f16 v[12:15], v[104:107], v[24:27], v[12:15]\nds_read_b128 v[24:27], v2 offset:1088\ns_waitcnt lgkmcnt(3)\n" \
  "v_mfma_f32_16x16x32_f16 v[12:15], v[100:103], v[28:31], v[12:15]\nds_read_b128 v[28:31], v2 offset:1152\n" \
  "v_mfma_f32_16x16x32_f16 v[12:15], v[96:99], v[32:35], v[12:15]\ns_waitcnt lgkmcnt(3)\n" \
  "v_mfma_f32_16x16x32_f16 v[12:15], v[92:95], v[36:39], v[12:15]\nds_read_b128 v[32:35], v2 offset:1216\n" \
  "v_mfma_f32_16x16x32_f16 v[16:19], v[124:127], v[36:39], 0\ns_waitcnt lgkmcnt(3)\nv_mfma_f32_16x16x32_f16 v[12:15], v[88:91], v[40:43], v[12:15]\n" \
  "ds_read_b128 v[124:127], v2 offset:1280\nv_mfma_f32_16x16x32_f16 v[16:19], v[120:123], v[40:43], v[16:19]\ns_waitcnt lgkmcnt(3)\n" \
  "v_mfma_f32_16x16x32_f16 v[12:15], v[84:87], v[24:27], v[12:15]\nds_read_b128 v[36:39], v2 offset:1344\n" \
  "v_mfma_f32_16x16x32_f16 v[16:19], v[116:119], v[24:27], v[16:19]\ns_waitcnt lgkmcnt(3)\n" \
  "v_mfma_f32_16x16x32_f16 v[12:15], v[80:83], v[28:31], v[12:15]\nds_read_b128 v[120:123], v2 offset:1408\n" \
  "v_mfma_f32_16x16x32_f16 v[16:19], v[112:115], v[28:31], v[16:19]\ns_waitcnt lgkmcnt(3)\n" \
  "v_mfma_f32_16x16x32_f16 v[12:15], v[76:79], v[32:35], v[12:15]\nds_read_b128 v[40:43], v2 offset:1472\n" \
  "v_mfma_f32_16x16x32_f16 v[16:19], v[108:111], v[32:35], v[16:19]\ns_waitcnt lgkmcnt(3)\n" \
  "v_mfma_f32_16x16x32_f16 v[12:15], v[72:75], v[124:127], v[12:15]\nds_read_b128 v[116:119], v2 offset:1536\n" \
  "v_mfma_f32_16x16x32_f16 v[16:19], v[104:107], v[124:127], v[16:19]\nds_read_b128 v[24:27], v2 offset:1600\ns_waitcnt lgkmcnt(3)\n" \
  "v_mfma_f32_16x16x32_f16 v[12:15], v[68:71], v[36:39], v[12:15]\nds_read_b128 v[112:115], v2 offset:1664\nds_read_b32 v23, v9 offset:32\n" \
  "v_mfma_f32_16x16x32_f16 v[16:19], v[100:103], v[36:39], v[16:19]\nds_read_b128 v[28:31], v2 offset:1728\n" \
  "v_mfma_f32_16x16x32_f16 v[12:15], v[64:67], v[120:123], v[12:15]\nds_read_b128 v[108:111], v2 offset:1792\n" \
  "v_mfma_f32_16x16x32_f16 v[16:19], v[96:99], v[120:123], v[16:19]\nds_read_b128 v[32:35], v2 offset:1856\ns_waitcnt lgkmcnt(6)\n" \
  "v_mfma_f32_16x16x32_f16 v[12:15], v[60:63], v[40:43], v[12:15]\nds_read_b128 v[104:107], v2 offset:1920\n" \
  "v_mfma_f32_16x16x32_f16 v[16:19], v[92:95], v[40:43], v[16:19]\nds_read_b128 v[124:127], v2 offset:1984\ns_waitcnt lgkmcnt(5)\n" \
  "v_readfirstlane_b32 s85, v23\ns_cmp_lt_u32 s85, 3\ns_cbranch_scc1 51f\n52:\nv_mfma_f32_16x16x32_f16 v[16:19], v[88:91], v[116:119], v[16:19]\n" \
  "ds_read_b64_tr_b16 v[100:101], v5 offset:0\nds_read_b64_tr_b16 v[102:103], v5 offset:32\n" \
  "v_mfma_f32_16x16x32_f16 v[16:19], v[84:87], v[24:27], v[16:19]\nds_read_b64_tr_b16 v[36:37], v5 offset:256\n" \
  "ds_read_b64_tr_b16 v[38:39], v5 offset:288\nv_mfma_f32_16x16x32_f16 v[16:19], v[80:83], v[112:115], v[16:19]\n" \
  "ds_read_b64_tr_b16 v[96:97], v5 offset:512\nds_read_b64_tr_b16 v[98:99], v5 offset:544\ns_waitcnt lgkmcnt(9)\n" \
  "v_mfma_f32_16x16x32_f16 v[16:19], v[76:79], v[28:31], v[16:19]\nds_read_b64_tr_b16 v[120:121], v5 offset:768\n" \
  "ds_read_b64_tr_b16 v[122:123], v5 offset:800\nv_mfma_f32_16x16x32_f16 v[16:19], v[72:75], v[108:111], v[16:19]\ns_waitcnt lgkmcnt(9)\n" \
  "v_mfma_f32_16x16x32_f16 v[16:19], v[68:71], v[32:35], v[16:19]\nv_mfma_f32_16x16x32_f16 v[16:19], v[64:67], v[104:107], v[16:19]\n" \
  "s_waitcnt lgkmcnt(6)\nv_mfma_f32_16x16x32_f16 v[16:19], v[60:63], v[124:127], v[16:19]\n" \
  "v_mfma_f32_16x16x32_f16 v[12:15], v[100:103], v[56:59], v[12:15]\nv_mfma_f32_16x16x32_f16 v[16:19], v[100:103], v[52:55], v[16:19]\n" \
  "s_waitcnt lgkmcnt(2)\nv_mfma_f32_16x16x32_f16 v[12:15], v[36:39], v[48:51], v[12:15]\n" \
  "v_mfma_f32_16x16x32_f16 v[16:19], v[36:39], v[44:47], v[16:19]\nv_mfma_f32_16x16x32_f16 v[16:19], v[96:99], v[56:59], v[16:19]\n" \
  "s_waitcnt lgkmcnt(0)\nv_mfma_f32_16x16x32_f16 v[16:19], v[120:123], v[48:51], v[16:19]\ns_nop 3\nv_cvt_pkrtz_f16_f32 v20, v12, v13\n" \
  "v_cvt_pkrtz_f16_f32 v21, v14, v15\nv_pk_add_u16 v20, v20, 4 op_sel_hi:[1,0] clamp\nv_pk_add_u16 v21, v21, 4 op_sel_hi:[1,0] clamp\n" \
  "ds_write_b64 v4, v[20:21] offset:2048\nv_cvt_pkrtz_f16_f32 v20, v16, v17\nv_cvt_pkrtz_f16_f32 v21, v18, v19\n" \
  "v_pk_add_u16 v20, v20, 4 op_sel_hi:[1,0] clamp\nv_pk_add_u16 v21, v21, 4 op_sel_hi:[1,0] clamp\nds_write_b64 v4, v[20:21] offset:3072\n" \
  "s_setprio 0\ns_setpc_b64 s[90:91]\n51:\ns_movk_i32 s84, 0x4000\n53:\nds_read_b32 v23, v9 offset:32\ns_sub_u32 s84, s84, 1\ns_cbranch_scc1 52b\n" \
  "s_waitcnt lgkmcnt(0)\nv_readfirstlane_b32 s85, v23\ns_cmp_lt_u32 s85, 3\ns_cbranch_scc1 53b\ns_branch 52b\n4:\n" \
  "ds_read_b64 v[124:125], v3 offset:1088\nds_read_b64 v[126:127], v3 offset:1096\nds_read_b64 v[120:121], v3 offset:0\n" \
  "ds_read_b64 v[122:123], v3 offset:8\nds_read_b64 v[116:117], v3 offset:64\nds_read_b64 v[118:119], v3 offset:72\n" \
  "ds_read_b64 v[112:113], v3 offset:128\nds_read_b64 v[114:115], v3 offset:136\nds_read_b64 v[108:109], v3 offset:192\n" \
  "ds_read_b64 v[110:111], v3 offset:200\nds_read_b64 v[104:105], v3 offset:256\nds_read_b64 v[106:107], v3 offset:264\n" \
  "ds_read_b64 v[100:101], v3 offset:320\nds_read_b64 v[102:103], v3 offset:328\nds_read_b64 v[96:97], v3 offset:384\n" \
  "ds_read_b64 v[98:99], v3 offset:392\nds_read_b64 v[92:93], v3 offset:448\nds_read_b64 v[94:95], v3 offset:456\n" \
  "ds_read_b64 v[88:89], v3 offset:512\nds_read_b64 v[90:91], v3 offset:520\nds_read_b64 v[84:85], v3 offset:576\n" \
  "ds_read_b64 v[86:87], v3 offset:584\nds_read_b64 v[80:81], v3 offset:640\nds_read_b64 v[82:83], v3 offset:648\n" \
  "ds_read_b64 v[76:77], v3 offset:704\nds_read_b64 v[78:79], v3 offset:712\nds_read_b64 v[72:73], v3 offset:768\n" \
  "ds_read_b64 v[74:75], v3 offset:776\nds_read_b64 v[68:69], v3 offset:832\nds_read_b64 v[70:71], v3 offset:840\n" \
  "ds_read_b64 v[64:65], v3 offset:896\nds_read_b64 v[66:67], v3 offset:904\nds_read_b64 v[60:61], v3 offset:960\n" \
  "ds_read_b64 v[62:63], v3 offset:968\nds_read_b64 v[56:57], v3 offset:1024\nds_read_b64 v[58:59], v3 offset:1032\n" \
  "ds_read_b64 v[52:53], v6 offset:0\nds_read_b64 v[54:55], v6 offset:8\nds_read_b64 v[48:49], v6 offset:64\nds_read_b64 v[50:51], v6 offset:72\n" \
  "ds_read_b64 v[44:45], v6 offset:128\nds_read_b64 v[46:47], v6 offset:136\nds_read_b64 v[40:41], v6 offset:192\n" \
  "ds_read_b64 v[42:43], v6 offset:200\ns_waitcnt lgkmcnt(0)\ns_barrier\nds_read_b128 v[24:27], v2 offset:448\nds_read_b128 v[28:31], v2 offset:960\n" \
  "ds_read_b128 v[32:35], v2 offset:1472\nds_read_b128 v[36:39], v2 offset:1536\ns_waitcnt lgkmcnt(3)\n" \
  "v_mfma_f32_16x16x32_f16 v[12:15], v[124:127], v[24:27], 0\ns_setprio 1\nds_read_b128 v[24:27], v2 offset:1600\ns_waitcnt lgkmcnt(3)\n" \
  "v_mfma_f32_16x16x32_f16 v[16:19], v[124:127], v[28:31], 0\nds_read_b128 v[124:127], v2 offset:1664\ns_waitcnt lgkmcnt(3)\n" \
  "v_mfma_f32_16x16x32_f16 v[12:15], v[120:123], v[32:35], v[12:15]\nds_read_b128 v[28:31], v2 offset:1728\ns_waitcnt lgkmcnt(3)\n" \
  "v_mfma_f32_16x16x32_f16 v[12:15], v[116:119], v[36:39], v[12:15]\nds_read_b128 v[32:35], v2 offset:1792\ns_waitcnt lgkmcnt(3)\n" \
  "v_mfma_f32_16x16x32_f16 v[12:15], v[112:115], v[24:27], v[12:15]\nds_read_b128 v[36:39], v2 offset:1856\ns_waitcnt lgkmcnt(3)\n" \
  "v_mfma_f32_16x16x32_f16 v[12:15], v[108:111], v[124:127], v[12:15]\nds_read_b128 v[24:27], v2 offset:1920\ns_waitcnt lgkmcnt(3)\n" \
  "v_mfma_f32_16x16x32_f16 v[12:15], v[104:107], v[28:31], v[12:15]\nds_read_b128 v[124:127], v2 offset:1984\ns_waitcnt lgkmcnt(3)\n" \
  "v_mfma_f32_16x16x32_f16 v[12:15], v[100:103], v[32:35], v[12:15]\nds_read_b128 v[28:31], v2 offset:2048\ns_waitcnt lgkmcnt(3)\n" \
  "v_mfma_f32_16x16x32_f16 v[12:15], v[96:99], v[36:39], v[12:15]\nds_read_b128 v[32:35], v2 offset:2112\ns_waitcnt lgkmcnt(3)\n" \
  "v_mfma_f32_16x16x32_f16 v[12:15], v[92:95], v[24:27], v[12:15]\nds_read_b128 v[36:39], v2 offset:2176\ns_waitcnt lgkmcnt(3)\n" \
  "v_mfma_f32_16x16x32_f16 v[12:15], v[88:91], v[124:127], v[12:15]\nds_read_b128 v[24:27], v2 offset:2240\n" \
  "v_mfma_f32_16x16x32_f16 v[16:19], v[120:123], v[124:127], v[16:19]\nds_read_b64 v[120:121], v6 offset:256\nds_read_b64 v[122:123], v6 offset:264\n" \
  "s_waitcnt lgkmcnt(4)\nv_mfma_f32_16x16x32_f16 v[12:15], v[84:87], v[28:31], v[12:15]\nds_read_b128 v[124:127], v2 offset:2304\n" \
  "v_mfma_f32_16x16x32_f16 v[16:19], v[116:119], v[28:31], v[16:19]\nds_read_b64 v[116:117], v6 offset:320\nds_read_b64 v[118:119], v6 offset:328\n" \
  "v_mfma_f32_16x16x32_f16 v[12:15], v[80:83], v[32:35], v[12:15]\nds_read_b128 v[28:31], v2 offset:2368\n" \
  "v_mfma_f32_16x16x32_f16 v[16:19], v[112:115], v[32:35], v[16:19]\ns_waitcnt lgkmcnt(6)\n" \
  "v_mfma_f32_16x16x32_f16 v[12:15], v[76:79], v[36:39], v[12:15]\nds_read_b128 v[112:115], v2 offset:2432\n" \
  "v_mfma_f32_16x16x32_f16 v[16:19], v[108:111], v[36:39], v[16:19]\nds_read2_b32 v[32:33], v8 offset0:48 offset1:49\n" \
  "ds_read2_b32 v[34:35], v8 offset0:50 offset1:51\nv_mfma_f32_16x16x32_f16 v[12:15], v[72:75], v[24:27], v[12:15]\n" \
  "ds_read_b128 v[108:111], v2 offset:2496\nv_mfma_f32_16x16x32_f16 v[16:19], v[104:107], v[24:27], v[16:19]\n" \
  "ds_read2_b32 v[36:37], v8 offset0:32 offset1:33\nds_read2_b32 v[38:39], v8 offset0:34 offset1:35\ns_waitcnt lgkmcnt(6)\n" \
  "v_mfma_f32_16x16x32_f16 v[12:15], v[68:71], v[124:127], v[12:15]\nds_read_b128 v[104:107], v2 offset:2560\n" \
  "v_mfma_f32_16x16x32_f16 v[16:19], v[100:103], v[124:127], v[16:19]\nds_read_b128 v[24:27], v2 offset:2624\n" \
  "v_mfma_f32_16x16x32_f16 v[12:15], v[64:67], v[28:31], v[12:15]\nds_read_b128 v[100:103], v2 offset:2688\nds_read_b32 v23, v9 offset:32\n" \
  "v_mfma_f32_16x16x32_f16 v[16:19], v[96:99], v[28:31], v[16:19]\nds_read_b128 v[124:127], v2 offset:2752\ns_waitcnt lgkmcnt(7)\n" \
  "v_mfma_f32_16x16x32_f16 v[12:15], v[60:63], v[112:115], v[12:15]\nds_read_b128 v[96:99], v2 offset:2816\n" \
  "v_mfma_f32_16x16x32_f16 v[16:19], v[92:95], v[112:115], v[16:19]\nds_read_b128 v[28:31], v2 offset:2880\n" \
  "v_mfma_f32_16x16x32_f16 v[12:15], v[56:59], v[108:111], v[12:15]\nds_read_b128 v[92:95], v2 offset:2944\n" \
  "v_mfma_f32_16x16x32_f16 v[16:19], v[88:91], v[108:111], v[16:19]\nds_read_b128 v[112:115], v2 offset:3008\ns_waitcnt lgkmcnt(5)\n" \
  "v_readfirstlane_b32 s85, v23\ns_cmp_lt_u32 s85, 3\ns_cbranch_scc1 61f\n62:\nv_mfma_f32_16x16x32_f16 v[16:19], v[84:87], v[104:107], v[16:19]\n" \
  "ds_read_b64_tr_b16 v[88:89], v5 offset:0\nds_read_b64_tr_b16 v[90:91], v5 offset:32\n" \
  "v_mfma_f32_16x16x32_f16 v[16:19], v[80:83], v[24:27], v[16:19]\nds_read_b64_tr_b16 v[108:109], v5 offset:256\n" \
  "ds_read_b64_tr_b16 v[110:111], v5 offset:288\nv_mfma_f32_16x16x32_f16 v[16:19], v[76:79], v[100:103], v[16:19]\n" \
  "ds_read_b64_tr_b16 v[84:85], v5 offset:512\nds_read_b64_tr_b16 v[86:87], v5 offset:544\ns_waitcnt lgkmcnt(9)\n" \
  "v_mfma_f32_16x16x32_f16 v[16:19], v[72:75], v[124:127], v[16:19]\nds_read_b64_tr_b16 v[104:105], v5 offset:768\n" \
  "ds_read_b64_tr_b16 v[106:107], v5 offset:800\nv_mfma_f32_16x16x32_f16 v[16:19], v[68:71], v[96:99], v[16:19]\n" \
  "ds_read_b64_tr_b16 v[80:81], v5 offset:1024\nds_read_b64_tr_b16 v[82:83], v5 offset:1056\ns_waitcnt lgkmcnt(11)\n" \
  "v_mfma_f32_16x16x32_f16 v[16:19], v[64:67], v[28:31], v[16:19]\nds_read_b64_tr_b16 v[24:25], v5 offset:1280\n" \
  "ds_read_b64_tr_b16 v[26:27], v5 offset:1312\nv_mfma_f32_16x16x32_f16 v[16:19], v[60:63], v[92:95], v[16:19]\n" \
  "ds_read_b64_tr_b16 v[76:77], v5 offset:1536\nds_read_b64_tr_b16 v[78:79], v5 offset:1568\ns_waitcnt lgkmcnt(12)\n" \
  "v_mfma_f32_16x16x32_f16 v[16:19], v[56:59], v[112:115], v[16:19]\nds_read_b64_tr_b16 v[100:101], v5 offset:1792\n" \
  "ds_read_b64_tr_b16 v[102:103], v5 offset:1824\nv_mfma_f32_16x16x32_f16 v[12:15], v[88:91], v[52:55], v[12:15]\ns_waitcnt lgkmcnt(10)\n" \
  "v_mfma_f32_16x16x32_f16 v[12:15], v[108:111], v[48:51], v[12:15]\nds_read_b64_tr_b16 v[72:73], v7 offset:0\n" \
  "ds_read_b64_tr_b16 v[74:75], v7 offset:64\nv_mfma_f32_16x16x32_f16 v[12:15], v[84:87], v[44:47], v[12:15]\n" \
  "ds_read_b64_tr_b16 v[124:125], v7 offset:512\nds_read_b64_tr_b16 v[126:127], v7 offset:576\n" \
  "v_mfma_f32_16x16x32_f16 v[16:19], v[84:87], v[52:55], v[16:19]\ns_waitcnt lgkmcnt(10)\n" \
  "v_mfma_f32_16x16x32_f16 v[12:15], v[104:107], v[40:43], v[12:15]\nv_mfma_f32_16x16x32_f16 v[16:19], v[104:107], v[48:51], v[16:19]\n" \
  "v_mfma_f32_16x16x32_f16 v[12:15], v[80:83], v[120:123], v[12:15]\nv_mfma_f32_16x16x32_f16 v[16:19], v[80:83], v[44:47], v[16:19]\n" \
  "s_waitcnt lgkmcnt(6)\nv_mfma_f32_16x16x32_f16 v[12:15], v[24:27], v[116:119], v[12:15]\n" \
  "v_mfma_f32_16x16x32_f16 v[16:19], v[24:27], v[40:43], v[16:19]\nv_mfma_f32_16x16x32_f16 v[16:19], v[76:79], v[120:123], v[16:19]\n" \
  "s_waitcnt lgkmcnt(2)\nv_mfma_f32_16x16x32_f16 v[16:19], v[100:103], v[116:119], v[16:19]\n" \
  "v_mfma_f32_16x16x32_f16 v[12:15], v[72:75], v[32:35], v[12:15]\nv_mfma_f32_16x16x32_f16 v[16:19], v[72:75], v[36:39], v[16:19]\n" \
  "s_waitcnt lgkmcnt(0)\nv_mfma_f32_16x16x32_f16 v[16:19], v[124:127], v[32:35], v[16:19]\ns_nop 4\nv_cvt_pkrtz_f16_f32 v20, v12, v13\n" \
  "v_cvt_pkrtz_f16_f32 v21, v14, v15\nv_pk_add_u16 v20, v20, 4 op_sel_hi:[1,0] clamp\nv_pk_add_u16 v21, v21, 4 op_sel_hi:[1,0] clamp\n" \
  "ds_write_b64 v4, v[20:21] offset:4096\nv_cvt_pkrtz_f16_f32 v20, v16, v17\nv_cvt_pkrtz_f16_f32 v21, v18, v19\n" \
  "v_pk_add_u16 v20, v20, 4 op_sel_hi:[1,0] clamp\nv_pk_add_u16 v21, v21, 4 op_sel_hi:[1,0] clamp\nds_write_b64 v4, v[20:21] offset:5120\n" \
  "s_setprio 0\ns_setpc_b64 s[90:91]\n61:\ns_movk_i32 s84, 0x4000\n63:\nds_read_b32 v23, v9 offset:32\ns_sub_u32 s84, s84, 1\ns_cbranch_scc1 62b\n" \
  "s_waitcnt lgkmcnt(0)\nv_readfirstlane_b32 s85, v23\ns_cmp_lt_u32 s85, 3\ns_cbranch_scc1 63b\ns_branch 62b\n5:\n" \
  "ds_read_b64 v[124:125], v3 offset:1088\nds_read_b64 v[126:127], v3 offset:1096\nds_read_b64 v[120:121], v3 offset:0\n" \
  "ds_read_b64 v[122:123], v3 offset:8\nds_read_b64 v[116:117], v3 offset:64\nds_read_b64 v[118:119], v3 offset:72\n" \
  "ds_read_b64 v[112:113], v3 offset:128\nds_read_b64 v[114:115], v3 offset:136\nds_read_b64 v[108:109], v3 offset:192\n" \
  "ds_read_b64 v[110:111], v3 offset:200\nds_read_b64 v[104:105], v3 offset:256\nds_read_b64 v[106:107], v3 offset:264\n" \
  "ds_read_b64 v[100:101], v3 offset:320\nds_read_b64 v[102:103], v3 offset:328\nds_read_b64 v[96:97], v3 offset:384\n" \
  "ds_read_b64 v[98:99], v3 offset:392\nds_read_b64 v[92:93], v3 offset:448\nds_read_b64 v[94:95], v3 offset:456\n" \
  "ds_read_b64 v[88:89], v3 offset:512\nds_read_b64 v[90:91], v3 offset:520\nds_read_b64 v[84:85], v3 offset:576\n" \
  "ds_read_b64 v[86:87], v3 offset:584\nds_read_b64 v[80:81], v3 offset:640\nds_read_b64 v[82:83], v3 offset:648\n" \
  "ds_read_b64 v[76:77], v3 offset:704\nds_read_b64 v[78:79], v3 offset:712\nds_read_b64 v[72:73], v3 offset:768\n" \
  "ds_read_b64 v[74:75], v3 offset:776\nds_read_b64 v[68:69], v3 offset:832\nds_read_b64 v[70:71], v3 offset:840\n" \
  "ds_read_b64 v[64:65], v3 offset:896\nds_read_b64 v[66:67], v3 offset:904\nds_read_b64 v[60:61], v3 offset:960\n" \
  "ds_read_b64 v[62:63], v3 offset:968\nds_read_b64 v[56:57], v3 offset:1024\nds_read_b64 v[58:59], v3 offset:1032\n" \
  "ds_read_b64 v[52:53], v6 offset:0\nds_read_b64 v[54:55], v6 offset:8\nds_read_b64 v[48:49], v6 offset:64\nds_read_b64 v[50:51], v6 offset:72\n" \
  "ds_read_b64 v[44:45], v6 offset:128\nds_read_b64 v[46:47], v6 offset:136\nds_read_b64 v[40:41], v6 offset:192\n" \
  "ds_read_b64 v[42:43], v6 offset:200\ns_waitcnt lgkmcnt(0)\ns_barrier\nds_read_b128 v[24:27], v2 offset:1472\n" \
  "ds_read_b128 v[28:31], v2 offset:1984\nds_read_b128 v[32:35], v2 offset:2496\nds_read_b128 v[36:39], v2 offset:2560\ns_waitcnt lgkmcnt(3)\n" \
  "v_mfma_f32_16x16x32_f16 v[12:15], v[124:127], v[24:27], 0\ns_setprio 1\nds_read_b128 v[24:27], v2 offset:2624\ns_waitcnt lgkmcnt(3)\n" \
  "v_mfma_f32_16x16x32_f16 v[16:19], v[124:127], v[28:31], 0\nds_read_b128 v[124:127], v2 offset:2688\ns_waitcnt lgkmcnt(3)\n" \
  "v_mfma_f32_16x16x32_f16 v[12:15], v[120:123], v[32:35], v[12:15]\nds_read_b128 v[28:31], v2 offset:2752\ns_waitcnt lgkmcnt(3)\n" \
  "v_mfma_f32_16x16x32_f16 v[12:15], v[116:119], v[36:39], v[12:15]\nds_read_b128 v[32:35], v2 offset:2816\ns_waitcnt lgkmcnt(3)\n" \
  "v_mfma_f32_16x16x32_f16 v[12:15], v[112:115], v[24:27], v[12:15]\nds_read_b128 v[36:39], v2 offset:2880\ns_waitcnt lgkmcnt(3)\n" \
  "v_mfma_f32_16x16x32_f16 v[12:15], v[108:111], v[124:127], v[12:15]\nds_read_b128 v[24:27], v2 offset:2944\ns_waitcnt lgkmcnt(3)\n" \
  "v_mfma_f32_16x16x32_f16 v[12:15], v[104:107], v[28:31], v[12:15]\nds_read_b128 v[124:127], v2 offset:3008\ns_waitcnt lgkmcnt(3)\n" \
  "v_mfma_f32_16x16x32_f16 v[12:15], v[100:103], v[32:35], v[12:15]\nds_read_b128 v[28:31], v2 offset:3072\ns_waitcnt lgkmcnt(3)\n" \
  "v_mfma_f32_16x16x32_f16 v[12:15], v[96:99], v[36:39], v[12:15]\nds_read_b128 v[32:35], v2 offset:3136\ns_waitcnt lgkmcnt(3)\n" \
  "v_mfma_f32_16x16x32_f16 v[12:15], v[92:95], v[24:27], v[12:15]\nds_read_b128 v[36:39], v2 offset:3200\ns_waitcnt lgkmcnt(3)\n" \
  "v_mfma_f32_16x16x32_f16 v[12:15], v[88:91], v[124:127], v[12:15]\nds_read_b128 v[24:27], v2 offset:3264\n" \
  "v_mfma_f32_16x16x32_f16 v[16:19], v[120:123], v[124:127], v[16:19]\nds_read_b64 v[120:121], v6 offset:256\nds_read_b64 v[122:123], v6 offset:264\n" \
  "s_waitcnt lgkmcnt(4)\nv_mfma_f32_16x16x32_f16 v[12:15], v[84:87], v[28:31], v[12:15]\nds_read_b128 v[124:127], v2 offset:3328\n" \
  "v_mfma_f32_16x16x32_f16 v[16:19], v[116:119], v[28:31], v[16:19]\nds_read_b64 v[116:117], v6 offset:320\nds_read_b64 v[118:119], v6 offset:328\n" \
  "v_mfma_f32_16x16x32_f16 v[12:15], v[80:83], v[32:35], v[12:15]\nds_read_b128 v[28:31], v2 offset:3392\n" \
  "v_mfma_f32_16x16x32_f16 v[16:19], v[112:115], v[32:35], v[16:19]\ns_waitcnt lgkmcnt(6)\n" \
  "v_mfma_f32_16x16x32_f16 v[12:15], v[76:79], v[36:39], v[12:15]\nds_read_b128 v[112:115], v2 offset:3456\n" \
  "v_mfma_f32_16x16x32_f16 v[16:19], v[108:111], v[36:39], v[16:19]\nds_read2_b32 v[32:33], v8 offset0:16 offset1:17\n" \
  "ds_read2_b32 v[34:35], v8 offset0:18 offset1:19\nv_mfma_f32_16x16x32_f16 v[12:15], v[72:75], v[24:27], v[12:15]\n" \
  "ds_read_b128 v[108:111], v2 offset:3520\nv_mfma_f32_16x16x32_f16 v[16:19], v[104:107], v[24:27], v[16:19]\n" \
  "ds_read2_b32 v[36:37], v8 offset0:0 offset1:1\nds_read2_b32 v[38:39], v8 offset0:2 offset1:3\ns_waitcnt lgkmcnt(6)\n" \
  "v_mfma_f32_16x16x32_f16 v[12:15], v[68:71], v[124:127], v[12:15]\nds_read_b128 v[104:107], v2 offset:3584\n" \
  "v_mfma_f32_16x16x32_f16 v[16:19], v[100:103], v[124:127], v[16:19]\nds_read_b128 v[24:27], v2 offset:3648\n" \
  "ds_read2_b32 v[100:101], v8 offset0:32 offset1:33\nds_read2_b32 v[102:103], v8 offset0:34 offset1:35\n" \
  "v_mfma_f32_16x16x32_f16 v[12:15], v[64:67], v[28:31], v[12:15]\nds_read_b128 v[124:127], v2 offset:3712\nds_read_b32 v23, v9 offset:32\n" \
  "v_mfma_f32_16x16x32_f16 v[16:19], v[96:99], v[28:31], v[16:19]\nds_read_b128 v[96:99], v2 offset:3776\ns_waitcnt lgkmcnt(9)\n" \
  "v_mfma_f32_16x16x32_f16 v[12:15], v[60:63], v[112:115], v[12:15]\nds_read_b128 v[28:31], v2 offset:3840\n" \
  "v_mfma_f32_16x16x32_f16 v[16:19], v[92:95], v[112:115], v[16:19]\nds_read_b128 v[92:95], v2 offset:3904\n" \
  "v_mfma_f32_16x16x32_f16 v[12:15], v[56:59], v[108:111], v[12:15]\nds_read_b128 v[112:115], v2 offset:3968\n" \
  "v_mfma_f32_16x16x32_f16 v[16:19], v[88:91], v[108:111], v[16:19]\nds_read_b128 v[88:91], v2 offset:4032\ns_waitcnt lgkmcnt(5)\n" \
  "v_readfirstlane_b32 s85, v23\ns_cmp_lt_u32 s85, 3\ns_cbranch_scc1 71f\n72:\nv_mfma_f32_16x16x32_f16 v[16:19], v[84:87], v[104:107], v[16:19]\n" \
  "ds_read_b64_tr_b16 v[108:109], v5 offset:1024\nds_read_b64_tr_b16 v[110:111], v5 offset:1056\n" \
  "v_mfma_f32_16x16x32_f16 v[16:19], v[80:83], v[24:27], v[16:19]\nds_read_b64_tr_b16 v[84:85], v5 offset:1280\n" \
  "ds_read_b64_tr_b16 v[86:87], v5 offset:1312\nv_mfma_f32_16x16x32_f16 v[16:19], v[76:79], v[124:127], v[16:19]\n" \
  "ds_read_b64_tr_b16 v[104:105], v5 offset:1536\nds_read_b64_tr_b16 v[106:107], v5 offset:1568\ns_waitcnt lgkmcnt(9)\n" \
  "v_mfma_f32_16x16x32_f16 v[16:19], v[72:75], v[96:99], v[16:19]\nds_read_b64_tr_b16 v[80:81], v5 offset:1792\n" \
  "ds_read_b64_tr_b16 v[82:83], v5 offset:1824\nv_mfma_f32_16x16x32_f16 v[16:19], v[68:71], v[28:31], v[16:19]\n" \
  "ds_read_b64_tr_b16 v[24:25], v5 offset:2048\nds_read_b64_tr_b16 v[26:27], v5 offset:2080\ns_waitcnt lgkmcnt(11)\n" \
  "v_mfma_f32_16x16x32_f16 v[16:19], v[64:67], v[92:95], v[16:19]\nds_read_b64_tr_b16 v[76:77], v5 offset:2304\n" \
  "ds_read_b64_tr_b16 v[78:79], v5 offset:2336\nv_mfma_f32_16x16x32_f16 v[16:19], v[60:63], v[112:115], v[16:19]\n" \
  "ds_read_b64_tr_b16 v[124:125], v5 offset:2560\nds_read_b64_tr_b16 v[126:127], v5 offset:2592\ns_waitcnt lgkmcnt(12)\n" \
  "v_mfma_f32_16x16x32_f16 v[16:19], v[56:59], v[88:91], v[16:19]\nds_read_b64_tr_b16 v[72:73], v5 offset:2816\n" \
  "ds_read_b64_tr_b16 v[74:75], v5 offset:2848\nv_mfma_f32_16x16x32_f16 v[12:15], v[108:111], v[52:55], v[12:15]\ns_waitcnt lgkmcnt(10)\n" \
  "v_mfma_f32_16x16x32_f16 v[12:15], v[84:87], v[48:51], v[12:15]\nds_read_b64_tr_b16 v[96:97], v7 offset:0\n" \
  "ds_read_b64_tr_b16 v[98:99], v7 offset:64\nv_mfma_f32_16x16x32_f16 v[12:15], v[104:107], v[44:47], v[12:15]\n" \
  "ds_read_b64_tr_b16 v[68:69], v7 offset:512\nds_read_b64_tr_b16 v[70:71], v7 offset:576\n" \
  "v_mfma_f32_16x16x32_f16 v[16:19], v[104:107], v[52:55], v[16:19]\ns_waitcnt lgkmcnt(10)\n" \
  "v_mfma_f32_16x16x32_f16 v[12:15], v[80:83], v[40:43], v[12:15]\nds_read_b64_tr_b16 v[28:29], v7 offset:1024\n" \
  "ds_read_b64_tr_b16 v[30:31], v7 offset:1088\nv_mfma_f32_16x16x32_f16 v[16:19], v[80:83], v[48:51], v[16:19]\n" \
  "ds_read2_b32 v[64:65], v8 offset0:48 offset1:49\nds_read2_b32 v[66:67], v8 offset0:50 offset1:51\n" \
  "v_mfma_f32_16x16x32_f16 v[12:15], v[24:27], v[120:123], v[12:15]\nv_mfma_f32_16x16x32_f16 v[16:19], v[24:27], v[44:47], v[16:19]\n" \
  "s_waitcnt lgkmcnt(10)\nv_mfma_f32_16x16x32_f16 v[12:15], v[76:79], v[116:119], v[12:15]\nds_read_b64_tr_b16 v[92:93], v7 offset:1536\n" \
  "ds_read_b64_tr_b16 v[94:95], v7 offset:1600\nv_mfma_f32_16x16x32_f16 v[16:19], v[76:79], v[40:43], v[16:19]\n" \
  "v_mfma_f32_16x16x32_f16 v[16:19], v[124:127], v[120:123], v[16:19]\ns_waitcnt lgkmcnt(8)\n" \
  "v_mfma_f32_16x16x32_f16 v[16:19], v[72:75], v[116:119], v[16:19]\nv_mfma_f32_16x16x32_f16 v[12:15], v[96:99], v[32:35], v[12:15]\n" \
  "v_mfma_f32_16x16x32_f16 v[16:19], v[96:99], v[36:39], v[16:19]\ns_waitcnt lgkmcnt(4)\n" \
  "v_mfma_f32_16x16x32_f16 v[12:15], v[68:71], v[100:103], v[12:15]\nv_mfma_f32_16x16x32_f16 v[16:19], v[68:71], v[32:35], v[16:19]\n" \
  "s_waitcnt lgkmcnt(2)\nv_mfma_f32_16x16x32_f16 v[12:15], v[28:31], v[64:67], v[12:15]\n" \
  "v_mfma_f32_16x16x32_f16 v[16:19], v[28:31], v[100:103], v[16:19]\ns_waitcnt lgkmcnt(0)\n" \
  "v_mfma_f32_16x16x32_f16 v[16:19], v[92:95], v[64:67], v[16:19]\ns_nop 4\nv_cvt_pkrtz_f16_f32 v20, v12, v13\nv_cvt_pkrtz_f16_f32 v21, v14, v15\n" \
  "v_pk_add_u16 v20, v20, 4 op_sel_hi:[1,0] clamp\nv_pk_add_u16 v21, v21, 4 op_sel_hi:[1,0] clamp\nds_write_b64 v4, v[20:21] offset:6144\n" \
  "v_cvt_pkrtz_f16_f32 v20, v16, v17\nv_cvt_pkrtz_f16_f32 v21, v18, v19\nv_pk_add_u16 v20, v20, 4 op_sel_hi:[1,0] clamp\n" \
  "v_pk_add_u16 v21, v21, 4 op_sel_hi:[1,0] clamp\nds_write_b64 v4, v[20:21] offset:7168\ns_setprio 0\ns_setpc_b64 s[90:91]\n71:\n" \
  "s_movk_i32 s84, 0x4000\n73:\nds_read_b32 v23, v9 offset:32\ns_sub_u32 s84, s84, 1\ns_cbranch_scc1 72b\ns_waitcnt lgkmcnt(0)\n" \
  "v_readfirstlane_b32 s85, v23\ns_cmp_lt_u32 s85, 3\ns_cbranch_scc1 73b\ns_branch 72b\n"
#define CONV_CLOB "memory", "scc", "s84", "s85", "s90", "s91", "v12", "v13", "v14", "v15", "v16", "v17", "v18", "v19", "v20", "v21", "v22", "v23", "v24", "v25", "v26", "v27", "v28", "v29", "v30", "v31", "v32", "v33", "v34", "v35", "v36", "v37", "v38", "v39", "v40", "v41", "v42", "v43", "v44", "v45", "v46", "v47", "v48", "v49", "v50", "v51", "v52", "v53", "v54", "v55", "v56", "v57", "v58", "v59", "v60", "v61", "v62", "v63", "v64", "v65", "v66", "v67", "v68", "v69", "v70", "v71", "v72", "v73", "v74", "v75", "v76", "v77", "v78", "v79", "v80", "v81", "v82", "v83", "v84", "v85", "v86", "v87", "v88", "v89", "v90", "v91", "v92", "v93", "v94", "v95", "v96", "v97", "v98", "v99", "v100", "v101", "v102", "v103", "v104", "v105", "v106", "v107", "v108", "v109", "v110", "v111", "v112", "v113", "v114", "v115", "v116", "v117", "v118", "v119", "v120", "v121", "v122", "v123", "v124", "v125", "v126", "v127"
constexpr int TDP = 1216, T_3 = 4 * TDP, T_4 = T_3 + 512, TB = T_4 + 320;
constexpr int C_U = 480, UB = C_U + 4096 + 96, C_G5 = 0, C_G3 = 3200, C_G9 = 6400, C_G7 = C_G9 + 2096, GB = C_G7 + 2080 + 112;
constexpr int O_FL = 2 * TB, O_UL = O_FL + 64, O_IM = O_UL + 2 * UB, O_ST = O_IM + 2 * GB;
constexpr int X_TB = 10240, X_UL = X_TB + TB, X_IM = X_UL + UB, X_ST = X_IM + GB;
constexpr int X_WR = X_IM + 128 * IP;
#define X_RW(w) (LW + 9216 * (2 + ((w) >> 1)) + 2560 * ((w) & 1))
constexpr int RP = 1216;
constexpr int LW = 36864 + 512, LA = LW, LM = LW + 36864;
constexpr int L_SC = LM + 2048, SMEM = O_IM + 256 * IP;
static_assert(L_SC + 1152 <= SMEM && O_ST + 8192 <= SMEM && X_IM + 128 * IP <= LM && X_ST + 8192 <= LM && UB % 128 == 64 && GB % 128 == 64 && SMEM <= 79872, "");
struct TOps { uint32_t xa, xb, ya, pd, pf, pg, pe; };
__device__ __forceinline__ void table_ops(uint32_t im, uint32_t tb, int ct, int lane, TOps& t) {
  const int j = lane & 15, kq = lane >> 4, q4 = (lane >> 2) & 3, p4 = lane & 3, jj = j + 16 * ct;
  const uint32_t sa = im + (uint32_t)(IP * (lane >> 2));
  t.xa = sa + (uint32_t)(I_X + 8 * p4); t.xb = sa + (uint32_t)(I_X + 32 + 8 * (p4 & 1)); t.ya = sa + (uint32_t)(32 * ct + 8 * p4);
  t.pd = tb + (uint32_t)(2 * (541 - (8 * jj + 4 * (kq & 1) + 256 * (kq >> 1))));
  t.pf = tb + (uint32_t)((kq & 1) ? T_4 + 2 * (63 - jj) : T_3 + 2 * (127 - 2 * jj));
  t.pg = t.pf - ((kq & 1) ? 64 : 2);
  t.pe = tb + (uint32_t)(p4 * (TDP + 2) + ((lane < 12) ? 2 * (32 - q4) : 2 * (572 + (q4 & 1))));
  asm volatile("" : "+v"(t.xa), "+v"(t.xb), "+v"(t.ya), "+v"(t.pd), "+v"(t.pf), "+v"(t.pg), "+v"(t.pe));
}
typedef __attribute__((address_space(3))) char* lptr;
#define LP(T, a) ((__attribute__((address_space(3))) T*)(uintptr_t)(a))
__device__ __forceinline__ uint32_t table_tile(const TOps& t, int ct, int lane, uint32_t evp, float dsk TSP) {
  u32x4 ev;
  f32x4 ad[2], af[2];
  asm volatile(T_ASM : "=&v"(ad[0]), "=&v"(ad[1]), "=&v"(af[0]), "=&v"(af[1]), "=&v"(ev) : "v"(t.xa), "v"(t.xb), "v"(t.ya), "v"(evp) : T_CLOB);
  TS(0, 4);
  const int e0 = (int)(ev[0] + ev[2]), e1 = (int)(ev[1] + ev[3]);
  const int em = min(max(max(max(e0, e1), (int)((__float_as_uint(dsk) >> 23) & 0xffu) - 133), -110), 115);
  const int ts = ((uint32_t)(em + 12) <= 8u) ? 1 : -3 - em;
  float s0 = __builtin_amdgcn_ldexpf(1.f, e0 - 29 + ts), s1 = __builtin_amdgcn_ldexpf(1.f, e1 - 29 + ts);
  float d0 = (ct == 0 && lane == 0) ? __builtin_amdgcn_ldexpf(dsk, ts) : 0.f;
  asm volatile("s_nop 1" : "+v"(ad[0]), "+v"(ad[1]), "+v"(af[0]), "+v"(af[1]), "+v"(s0), "+v"(s1), "+v"(d0));
  const f32x4 k = ad[0] * s0 + (f32x4){d0, 0.f, 0.f, 0.f} + ad[1] * s1, f = af[0] * s0 + af[1] * s1;
  auto pk = [](float a, float b) { uint32_t r; asm("v_cvt_pk_f16_f32 %0, %1, %2" : "=v"(r) : "v"(a), "v"(b)); return r; };
  asm volatile("s_nop 0" : : "v"(k), "v"(f));
  const uint32_t p32 = pk(k[3], k[2]), p10 = pk(k[1], k[0]), p21 = pk(k[2], k[1]), p01 = pk(f[0], f[1]), p23 = pk(f[2], f[3]);
  *LP(u32x2, t.pd + 3 * TDP + 6) = (u32x2){p32, p10};
  *LP(uint32_t, t.pd + TDP + 2) = p32; *LP(uint32_t, t.pd + TDP + 6) = p10;
  *LP(uint32_t, t.pd + 2) = p21; *LP(uint32_t, t.pd + 2 * TDP + 6) = p21;
  *LP(u16, t.pd) = (u16)p32; *LP(u16, t.pd + 2 * TDP + 4) = (u16)p32;
  asm volatile("ds_write_b16_d16_hi %0, %1 offset:6\n\tds_write_b16_d16_hi %0, %1 offset:%2" : : "v"(t.pd), "v"(p10), "n"(2 * TDP + 10) : "memory");
  if (ct) {
    const uint32_t va = (uint32_t)__builtin_amdgcn_readlane((int)p32, 63), vb = (uint32_t)__builtin_amdgcn_readlane((int)p23, 15) >> 16;
    *LP(u16, t.pe) = (u16)((lane < 12) ? va : vb);
  }
  if (lane < 32) {
    *LP(u16, t.pf + 128) = (u16)p01; *LP(u16, t.pg + 128) = (u16)(p01 >> 16);
    *LP(u16, t.pf) = (u16)p23; *LP(u16, t.pg) = (u16)(p23 >> 16);
  }
  return (uint32_t)(127 + 5 - ts) << 23;
}
__device__ __forceinline__ uint32_t hadd(uint32_t a, uint32_t b) { return __builtin_bit_cast(uint32_t, __builtin_bit_cast(f16x2, a) + __builtin_bit_cast(f16x2, b)); }
template <bool FULL> __device__ __forceinline__ void sums(char* sm, const u32x4 p, const u32x4 c) {
  const uint32_t D[8] = {p[0], p[1], p[2], p[3], c[0], c[1], c[2], c[3]};
  uint32_t O[7];
#pragma unroll
  for (int j = 0; j < 7; ++j) O[j] = __builtin_amdgcn_alignbit(D[j + 1], D[j], 16);
  u32x4 a, b;
#pragma unroll
  for (int j = 0; j < 4; ++j) a[j] = hadd(D[j + 2], hadd(O[j + 1], O[j + 2]));
  *(u32x4*)(sm + C_G3 + 32) = a;
#pragma unroll
  for (int j = 0; j < 4; ++j) b[j] = hadd(a[j], hadd(D[j + 1], D[j + 3]));
  *(u32x4*)(sm + C_G5 + 32) = b;
  if (FULL) {
#pragma unroll
    for (int j = 0; j < 4; ++j) a[j] = hadd(b[j], hadd(O[j], O[j + 3]));
    *(u32x4*)(sm + C_G7 + 32) = a;
#pragma unroll
    for (int j = 0; j < 4; ++j) b[j] = hadd(a[j], hadd(D[j], D[j + 4]));
    *(u32x4*)(sm + C_G9 + 32) = b;
  }
}
__device__ __forceinline__ void count_up(uint32_t a) {
  uint32_t t;
  asm volatile("s_mov_b64 exec, 1\n\tv_mov_b32 %0, 1\n\tds_add_u32 %1, %0\n\ts_mov_b64 exec, -1" : "=&v"(t) : "v"(a) : "memory");
}
__device__ __forceinline__ int pad_at(int r) { return 16 * (r & 1) + 3200 * (r >> 1) - ((r >= 6) ? 3200 - 2096 : 0); }
struct COps { uint32_t b, a, o, g3, k3, g4, k4, f; };
__device__ __forceinline__ void conv_ops(uint32_t tb, uint32_t ub, uint32_t cb, int lane, COps& e) {
  const uint32_t j = lane & 15, kq = lane >> 4, q4 = (lane >> 2) & 3, p4 = lane & 3;
  e.b = ub + 32u * j + 16u * kq;
  e.a = tb + p4 * TDP + 32u - 8u * q4 + 16u * kq;
  e.g3 = cb + C_G5 + 8 + 64u * kq + 8u * (q4 + p4) + (q4 & 1u) * 3200u;
  e.g4 = cb + C_G9 + 8 + 128u * kq + 16u * q4 + 8u * p4 + (q4 & 1u) * 2096u;
  e.k3 = tb + T_3 + 120 - 8u * j + 16u * kq;
  e.k4 = tb + T_4 + 60 - 4u * j + 16u * kq;
  asm volatile("" : "+v"(e.b), "+v"(e.a), "+v"(e.g3), "+v"(e.g4), "+v"(e.k3), "+v"(e.k4));
}
#define CONV_RUN(prog, e, ...) do { int prog_ = (prog); \
    asm volatile("s_call_b64 s[90:91], kf_conv" : "+{s89}"(prog_), "+{v2}"(e.b), "+{v3}"(e.a), "+{v4}"(e.o), "+{v5}"(e.g3), "+{v6}"(e.k3), "+{v7}"(e.g4), \
                 "+{v8}"(e.k4), "+{v9}"(e.f) __VA_ARGS__ : : CONV_CLOB); } while (0)

template <bool E>
__device__ __forceinline__ void ln_stats(const uint32_t* v, int lane, float sc, f32x2* t, f32x4 z, f32x2& rc, f32x2& nc, float& e) {
  float s1a = z[0], s2a = z[1], s1b = z[2], s2b = z[3];
#pragma unroll
  for (int k = 0; k < 4; ++k) {
    s1a = dot2(v[k], 0x3f803f80u, s1a); s2a = dot2(v[k], v[k], s2a);
    s1b = dot2(v[4 + k], 0x3f803f80u, s1b); s2b = dot2(v[4 + k], v[4 + k], s2b);
  }
#pragma unroll
  for (int k = 0; k < 9; ++k) t[k] = (f32x2){bflo(v[k]), bfhi(v[k])};
  const float rs = row_sum(fold16(dot2(v[8], 0x3f803f80u, fold32(s1a, s1b)), dot2(v[8], v[8], fold32(s2a, s2b))));
  if (E) { e = row_sum(e); e += __int_as_float(__builtin_amdgcn_update_dpp(0, __float_as_int(e), 0x142, 0xa, 0xf, false)); }
  const f32x2 mean = (f32x2){rdl(rs, 15), rdl(rs, 47)} * (1.f / D_), m2 = mean * mean;
  f32x2 var = (f32x2){rdl(rs, 31), rdl(rs, 63)} * (1.f / D_) - m2;
  asm volatile("" : "+v"(t[0]), "+v"(t[1]), "+v"(t[2]), "+v"(t[3]), "+v"(t[4]), "+v"(t[5]), "+v"(t[6]), "+v"(t[7]), "+v"(t[8]));
  if (__builtin_expect(!(m2[0] <= 16.f * var[0] && m2[1] <= 16.f * var[1]), 0)) {
    float q[3] = {0.f, 0.f, 0.f};
#pragma unroll
    for (int k = 0; k < 9; ++k) {
      const f32x2 a = t[k] - ((k < 4 || (k == 8 && lane < 32)) ? mean[0] : mean[1]);
      q[k >> 2] += a[0] * a[0] + a[1] * a[1];
    }
    float qa = q[0] + ((lane < 32) ? q[2] : 0.f), qb = q[1] + ((lane < 32) ? 0.f : q[2]);
    wave_sum2(qa, qb);
    var = (f32x2){qa, qb} * (1.f / D_);
  }
  const uint32_t mp = pack2(mean[0], mean[1]), vp = pack2(var[0], var[1]);
  const f32x2 ve = (f32x2){bflo(vp), bfhi(vp)} + 1e-5f;
  const uint32_t e2 = pack2(ve[0], ve[1]);
  const uint32_t sp = pack2(__builtin_amdgcn_sqrtf(bflo(e2)), __builtin_amdgcn_sqrtf(bfhi(e2)));
  rc = (f32x2){__builtin_amdgcn_rcpf(bflo(sp)), __builtin_amdgcn_rcpf(bfhi(sp))} * sc;
  nc = -(f32x2){bflo(mp), bfhi(mp)} * rc;
}
template <bool H>
__device__ __forceinline__ void ln_apply(const f32x2* t, int lane, f32x2 rc, f32x2 nc, uint32_t* c) {
  const float nm[3] = {nc[0], nc[1], (lane < 32) ? nc[0] : nc[1]}, rr[3] = {rc[0], rc[1], (lane < 32) ? rc[0] : rc[1]};
  f32x2 dd[9];
#pragma unroll
  for (int k = 0; k < 9; ++k) dd[k] = __builtin_elementwise_fma(t[k], (f32x2){rr[k >> 2], rr[k >> 2]}, (f32x2){nm[k >> 2], nm[k >> 2]});
  asm volatile("" : "+v"(dd[0]), "+v"(dd[1]), "+v"(dd[2]), "+v"(dd[3]), "+v"(dd[4]), "+v"(dd[5]), "+v"(dd[6]), "+v"(dd[7]), "+v"(dd[8]));
#pragma unroll
  for (int k = 0; k < 9; ++k) {
    const f32x2 d = dd[k];
    typedef __attribute__((ext_vector_type(2))) uint16_t u16x2;
    if (H) c[k] = __builtin_bit_cast(uint32_t, __builtin_elementwise_add_sat(__builtin_bit_cast(u16x2, __builtin_amdgcn_cvt_pkrtz(d[0], d[1])), (u16x2){4, 4}));
    else c[k] = pack2(d[0], d[1]);
  }
}
__device__ __forceinline__ uint32_t med3u(uint32_t a, uint32_t b, uint32_t c) { return max(min(a, b), min(max(a, b), c)); }
__device__ __forceinline__ uint32_t oct_maxu(uint32_t v) {
  v = max(v, (uint32_t)DPP_I((int)v, 0xb1, 0xf));
  v = max(v, (uint32_t)DPP_I((int)v, 0x4e, 0xf));
  v = max(v, (uint32_t)DPP_I((int)v, 0x141, 0xf));
  return v;
}

#define KARGS const u16* __restrict__ x, const u16* __restrict__ Are, const u16* __restrict__ Aim, const u16* __restrict__ Bre, \
    const u16* __restrict__ Bim, const u16* __restrict__ Cre, const u16* __restrict__ Cim, const u16* __restrict__ logdt, \
    const u16* __restrict__ Dskip, char* __restrict__ ws, const u16* __restrict__ wr, const u16* __restrict__ wg, \
    const u16* __restrict__ wu, const u16* __restrict__ wd, u16* __restrict__ out
#define KPASS x, Are, Aim, Bre, Bim, Cre, Cim, logdt, Dskip, ws, wr, wg, wu, wd, out
template <int ROLE, int FFN = -1>
__device__ __forceinline__ void role_body(char* smem, KARGS) {
  const int tid = threadIdx.x, lane = tid & 63;
  const int w = __builtin_amdgcn_readfirstlane(tid >> 6), wl = w & 3;
  const int b = blockIdx.x & 255, bx = b & 7, bj = b >> 3;
  constexpr bool isl = ROLE == 1 || ROLE == 2;
  const int tg = isl ? 32 * (bj >> 2) + 4 * bx + (bj & 3) : 32 * bx + bj;
  u32x4 xa, xc;
  uint32_t ep, r0;
#define P_WAIT(n, ...) asm volatile("s_waitcnt vmcnt(" #n ")\n\tv_lshlrev_b32 %0, 16, v100\n\tv_lshlrev_b32 %1, 16, v101\n\tv_lshlrev_b32 %2, 16, v102" \
    : "=v"(ldt), "=v"(are), "=v"(aim) : : "v100", "v101", "v102" __VA_ARGS__)
#define P_MORE() asm volatile("v_lshlrev_b32 %0, 16, v103\n\tv_lshlrev_b32 %1, 16, v104\n\tv_lshlrev_b32 %2, 16, v105\n\tv_lshlrev_b32 %3, 16, v106" \
    : "=v"(bre), "=v"(bim), "=v"(cre), "=v"(cim) : : "v103", "v104", "v105", "v106")
  TSD;
  TS(0, 0);
  const uint32_t lb0 = (uint32_t)(uintptr_t)(__attribute__((address_space(3))) char*)smem;
  const int i = lane & 15, kq = lane >> 4;
  const u32x4 ones = {0xffffffffu, 0xffffffffu, 0xffffffffu, 0xffffffffu}, zero4 = {0u, 0u, 0u, 0u};
  auto u_again = [&](u32x4& uw, const u32x4& ut, uint32_t uo) {
#pragma unroll 1
    for (int spin = 0; spin < SPIN_CAP; ++spin) {
      const bool na = ((uw[0] | uw[1] | uw[2] | uw[3]) & UMASK) != 0u;
      if (__builtin_expect(!__any(na), 1)) break;
      TSC(19);
      SLEEP(USLEEP);
      if (na) { LD16W(uw, uo); uw ^= ut; }
    }
  };
  constexpr uint32_t S16 = 0x41800000u, LMASK = 0x00070007u;
  auto v_put = [&](u32x4 v, const u32x4& ut, bool other, uint32_t vo) {
    const bool second = __any(other || ((v[0] | v[1] | v[2] | v[3]) & 0x40004000u) != 0u);
    if (__builtin_expect(second, 0)) v = (v & ~LMASK) | (~ut & LMASK); else v = (v & ~LMASK) | ut;
    ST16(vo, v);
    return second;
  };
  auto v_end = [&](uint32_t T, int n) { DRAIN(); if (lane == 0) ST4(O_DN + 16u * n, T); };
  if constexpr (!isl) {
    const int ch = wl >> 1, tl = tid & 255;
    int* evp = (int*)(smem + O_FL + 16 * ch);
    float ldt, are, aim, dsk;
    if constexpr (ROLE == 0) {
      __builtin_amdgcn_s_setprio(KPRIO);
      COps e;
      conv_ops(lb0 + (uint32_t)(ch * TB), lb0 + (uint32_t)(O_UL + ch * UB), lb0 + (uint32_t)(O_IM + ch * GB), lane, e);
      e.o = lb0 + O_ST + (uint32_t)(16 * ((4 * i + 2 * (kq >> 1) + ch) ^ (i >> 1)) + 8 * (kq & 1));
      e.f = lb0 + O_FL;
      TOps t;
      table_ops(lb0 + (uint32_t)(O_IM + 128 * IP * ch), lb0 + (uint32_t)(TB * ch), wl & 1, lane, t);
      uint32_t pz = lb0 + O_IM + (uint32_t)(((lane >> 3) & 1) * GB + pad_at(lane & 7)), im = lb0 + (uint32_t)(O_IM + IP * tl);
      asm volatile("" : "+v"(e.o), "+v"(e.f), "+v"(pz), "+v"(im));
      float bre, bim, cre, cim;
      P_WAIT(0, , "v103", "v104", "v105", "v106", "v107", "v109");
      asm volatile("" : "={v109}"(ep));
      const uint32_t T = ((uint32_t)__builtin_amdgcn_readfirstlane(ep) + 1u) * 0x9E3779B1u;
      if (w == 3 && lane < 8) ST16(O_KEY + 128u * b + 16u * lane, ones);
      P_MORE();
      asm volatile("v_lshlrev_b32 %0, 16, v107" : "=v"(dsk) : : "v107");
      TS(0, 1);
      evp[wl & 1] = gen_y(ldt, are, aim, bre, bim, cre, cim, im, [] {} TSA);
      TS(0, 8);
      BAR();
      TS(0, 2);
      const uint32_t cv = table_tile(t, wl & 1, lane, lb0 + O_FL + 16 * ch, dsk TSA), cs = (uint32_t)__builtin_amdgcn_readfirstlane(cv);
      *(uint32_t*)(smem + O_FL + 40 + 4 * ch) = cv;
      TS(0, 9);
      gsync0(lb0 + O_FL + 36, 4);
      TS(0, 3);
      *(u32x4*)(smem + O_ST + 16 * tl) = ones;
      *(u32x4*)(smem + O_ST + 4096 + 16 * tl) = ones;
      if (w == 0) *LP(u32x4, pz) = zero4;
      DRAIN();
      CONV_RUN(wl & 1, e);
      if (wl & 1) asm volatile("s_mov_b64 exec, 1\n\tglobal_store_dwordx2 %0, %1, %2 sc1" : : "v"(O_SC + 16u * (2 * tg + ch)), "v"((u32x2){T, cs}), "s"(ws) : "memory");
      TS(0, 5);
    } else {
      __builtin_amdgcn_s_setprio(UPRIO);
#pragma unroll
      for (int k = 0; k < 3; ++k) *(u32x4*)(smem + ((16 * (tl + 256 * k) < O_FL) ? 16 * (tl + 256 * k) : O_FL + 32)) = zero4;
      if (w == 7) *(u32x4*)(smem + O_UL + (lane >> 5) * UB + 16 * min(lane & 31, 29)) = zero4;
      uint32_t uo = u_at(tl >> 1, 2 * tg + (tid & 1)), go = O_SU + (uint32_t)(256 * bx + 32 * wl + (lane & 31) + 4 * (lane & 32)) * 128u;
      uint32_t ul = (uint32_t)(O_UL + (tid & 1) * UB + C_U - 16 + 16 * (tl >> 1)), ug = (uint32_t)(O_IM + (tid & 1) * GB + 16 * (tl >> 1)), so = lb0 + O_ST + 16u * (uint32_t)(lane ^ (lane >> 3));
      uint32_t vo = O_V + (uint32_t)tg * VP + 16u * lane;
      asm volatile("" : "+v"(uo), "+v"(go), "+v"(ul), "+v"(ug), "+v"(so), "+v"(vo));
      P_WAIT(1, , "v109");
      TS(0, 1);
      evp[2 + (wl & 1)] = gen_x(ldt, are, aim, smem + O_IM + IP * tl);
      asm volatile("s_waitcnt vmcnt(0)" : "={v109}"(ep));
      const uint32_t T = ((uint32_t)__builtin_amdgcn_readfirstlane(ep) + 1u) * 0x9E3779B1u;
      TS(0, 8);
      BAR();
      TS(0, 2);
      u32x4 ut = {utag(T, 0), utag(T, 1), utag(T, 2), utag(T, 3)};
      asm volatile("" : "+v"(ut));
      SLEEP(GSLEEP);
      u32x2 pv;
      pv[1] = gate(ws, go, T);
      TS(0, 3);
      until(latest(pv[1]) + UD);
      TS(0, 7);
      u32x4 uw, ux;
      asm volatile("global_load_dwordx4 %0, %2, %4 sc1\n\tglobal_load_dwordx4 %1, %3, %4 sc1"
                   : "=&v"(uw), "=&v"(ux) : "v"(uo), "v"(uo + 64 * UP * 32), "s"(ws) : "memory");
      __builtin_amdgcn_s_setprio(FPR);
      asm volatile("s_waitcnt vmcnt(1)" : "+v"(uw));
      uw ^= ut;
      *(u32x4*)(smem + ul + 16) = uw;
      asm volatile("s_waitcnt vmcnt(0)" : "+v"(ux));
      TS(0, 11);
      ux ^= ut;
      *(u32x4*)(smem + ul + 2048 + 16) = ux;
      if (__builtin_expect(__any(((uw[0] | uw[1] | uw[2] | uw[3] | ux[0] | ux[1] | ux[2] | ux[3]) & UMASK) != 0u), 0)) {
        u_again(uw, ut, uo);
        u_again(ux, ut, uo + 64 * UP * 32);
        *(u32x4*)(smem + ul + 16) = uw;
        *(u32x4*)(smem + ul + 2048 + 16) = ux;
      }
      BAR();
      TS(0, 4);
      const u32x4 pw = *(const u32x4*)(smem + ul);
      if (wl < 2) {
        const u32x4 px = *(const u32x4*)(smem + ul + 2048);
        sums<true>(smem + ug, pw, uw);
        sums<false>(smem + ug + 2048, px, ux);
      } else sums<true>(smem + ug, pw, uw);
      count_up(lb0 + O_FL + 32);
      __builtin_amdgcn_s_setprio(SPR);
      TS(0, 5);
      const bool other = *(const uint32_t*)(smem + O_FL + 40 + 4 * (lane & 1)) != S16;
      bool second = false;
#pragma unroll
      for (int k = 0; k < 2; ++k) {
        const uint32_t o = ((k ? 0x3247u : 0x1065u) >> (4 * wl) & 7u) * 1024u;
        u32x4 v;
#pragma unroll 1
        for (int spin = 0; spin < SPIN_CAP; ++spin) {
          asm volatile("ds_read_b128 %0, %1\n\ts_waitcnt lgkmcnt(0)" : "=&v"(v) : "v"(so + o) : "memory");
          if (!__any(max4(v) == 0xffffffffu)) break;
          __builtin_amdgcn_s_sleep(VPS);
        }
        TS(0, 12 + k);
        second |= v_put(v, ut, other, vo + o);
        TS(0, 10 - 4 * k);
      }
      if (__builtin_expect(second, 0)) v_end(T, 4 * tg + wl);
    }
    TSE();
  } else {
  uint32_t* cand = (uint32_t*)(smem + LM);
  uint32_t* sel = cand + 128;
  uint32_t* kl = sel + 48;
  uint32_t* cnt = sel + 64;
  const uint32_t ca = lb0 + LM + 4 * (128 + 64);
  u16* hm = (u16*)(cnt + 12);
  uint32_t T;
  const bool ffn = (FFN < 0) ? bj < 18 : FFN, isx = FFN != 1 && bj >= 24;
  const int fb = 18 * bx + bj;
  constexpr bool ln2 = ROLE == 2;
  const int g0 = tg, g2 = tg;
  u32x4 bw[18];
#pragma unroll
  for (int s = 0; s < 9; ++s) bw[s] = (u32x4){0u, 0u, 0u, 0u};
  int tid2 = threadIdx.x;
  uint32_t ts1 = 0u;
  auto rest = [&] {
    if (ts1 && (int)((uint32_t)__builtin_amdgcn_s_memrealtime() - ts1) > 0) { TS(0, 8); until(ts1 + TSTOP + RD); if (RW) gwait(ca, 4); ts1 = 0u; }
  };
  auto gu_dma = [&](int q0, int q1, auto depth) {
    const char* gs = (const char*)((w < 2) ? wg : wu) + (size_t)((144 * (w & 1)) * (F_ * 2) + fb * 64);
    const uint32_t gv = (uint32_t)((lane >> 2) * (F_ * 2) + 16 * ((lane & 3) ^ (lane >> 5) ^ (w & 2)));
#pragma unroll 1
    for (int q = q0; q < q1; ++q) {
      const int k = (int)((((bj & 1) ? ORD1 : ((unsigned __int128)0x876543210u << 36 | 0x876543210ull)) >> (4 * (q - (q >= 9 ? 9 : 0)) + (q >= 9 ? 36 : 0))) & 15u), h = q >= 9;
      if (q >= RQ) rest();
      dma16(gs + (size_t)(288 * h + 16 * k) * (F_ * 2), gv, lb0 + LW * h + 9216 * w + 1024 * k);
      asm volatile("s_waitcnt vmcnt(%0)" : : "n"(depth.value) : "memory");
    }
  };
  auto down_dma = [&](int k0, int k1, int lane) {
#pragma unroll 1
    for (int k = k0; k < k1; ++k) {
      const int o = 9216 * wl + 2304 * k;
      const char* ds = (const char*)wd + (size_t)(fb * 36864 + o);
      const uint32_t dl = lb0 + o + 32 * ((4 * wl + k + 1) >> 1);
      if (ROLE == 1) rest();
      dma16d(ds, 16u * lane, dl);
      dma16d(ds + 1024, 16u * lane, dl + 1024);
      uint32_t keep;
      asm volatile("s_mov_b32 %0, m0\n\ts_mov_b32 m0, %3\n\ts_mov_b64 exec, 0xffff\n\tglobal_load_lds_dwordx4 %1, %2" DPOL2 "\n\ts_mov_b64 exec, -1\n\ts_mov_b32 m0, %0\n\ts_waitcnt vmcnt(%4)"
                   : "=&s"(keep) : "v"(16u * lane), "s"(ds + 2048), "s"(dl + 2048), "n"(ROLE == 1 ? DDEPTH : 40) : "memory");
    }
  };
  auto gu_read = [&](int h) {
    const uint32_t ra = lb0 + LW * h + (uint32_t)(((lane >> 1) & 1) * 18432 + (8 * kq + ((lane >> 2) & 3)) * 64 + ((w ^ (kq & 1) ^ (lane & 2)) * 16) + 8 * (lane & 1));
#pragma unroll
    for (int s = 0; s < 9; ++s) {
      const u32x2 h0 = trd(ra + 2048 * s), h1 = trd(ra + 2048 * s + 256);
      bw[9 * h + s] = (u32x4){h0[0], h0[1], h1[0], h1[1]};
    }
#pragma unroll
    for (int s = 0; s < 9; ++s) asm volatile("" : "+v"(bw[9 * h + s]));
  };
  uint32_t tv = 0u, tk = 0u;
  const uint32_t vearly = (uint32_t)((VEARLY >> (8 * (g0 >> 5))) & 0xffull);
#define TOUCH(pa) asm volatile("global_load_dword v126, %0, off" DPOL4 : : "v"(pa) : "memory", "v126")
  uint32_t wv[5], xv[9];
  auto rows_xw = [&](int lane) {
    u32x4 wa, xr, xs;
    const uint32_t xo = (uint32_t)((8 * g2 + 2 * wl) * (D_ * 2));
    asm volatile("global_load_dwordx4 %0, %5, %9\n\tglobal_load_dword %1, %6, %9 offset:1024\n\t"
                 "global_load_dwordx4 %2, %7, %10\n\tglobal_load_dwordx4 %3, %7, %10 offset:1152\n\tglobal_load_dword %4, %8, %10 offset:1024\n\ts_waitcnt vmcnt(0)"
                 : "=&v"(wa), "=&v"(wv[4]), "=&v"(xr), "=&v"(xs), "=&v"(xv[8])
                 : "v"(16u * lane), "v"(4u * (lane & 31)), "v"(xo + 16u * lane), "v"(xo + 4u * lane + 32u * (lane & 32)), "s"(wr), "s"(x) : "memory");
#pragma unroll
    for (int k = 0; k < 4; ++k) { wv[k] = wa[k]; xv[k] = xr[k]; xv[4 + k] = xs[k]; }
  };
  auto ln1 = [&](int g, int o, uint32_t c0) {
    uint32_t uo = u_at(g, 64 * wl + lane);
    uint32_t ta = lb0 + (uint32_t)(o + ((lane >> 2) & 3) * RP + (64 * wl + 16 * kq + 4 * (lane & 3)) * 2);
    uint32_t la = (uint32_t)(o + 2 * wl * RP + 16 * lane), lt = (uint32_t)(o + (2 * wl + (lane >> 5)) * RP + 1024 + 4 * (lane & 31));
    uint32_t sg = O_SU + (uint32_t)(256 * (lane >> 3) + g) * 128u + 16u * (lane & 7);
    asm volatile("" : "+v"(uo), "+v"(ta), "+v"(la), "+v"(lt), "+v"(sg));
    f32x4 z4 = {0.f, 0.f, 0.f, 0.f};
    asm volatile("" : "+v"(z4));
    asm volatile("s_waitcnt vmcnt(0)" : "={v[100:103]}"(xa), "={v[104:107]}"(xc), "={v108}"(r0), "={v109}"(ep));
    TS(0, 1);
    const uint32_t v[9] = {xa[0], xa[1], xa[2], xa[3], xc[0], xc[1], xc[2], xc[3], r0};
    uint32_t c[9];
    f32x2 tf[9];
    T = ((uint32_t)__builtin_amdgcn_readfirstlane(ep) + 1u) * 0x9E3779B1u;
    if (wl == 3) { const uint32_t now = (uint32_t)__builtin_amdgcn_s_memrealtime() + SD; ST16(sg, ((u32x4){T, now, T, now})); }
    const uint32_t tj = ((T >> (3 * wl)) & 0x00070007u) | (((T >> (12 + wl)) & 0x00010001u) << 14);
    uint32_t tw[3] = {(tj & 0xffffu) * 0x10001u, (tj >> 16) * 0x10001u, 0u};
    tw[2] = (lane < 32) ? tw[0] : tw[1];
    asm volatile("" : "+v"(tw[0]), "+v"(tw[1]), "+v"(tw[2]));
    f32x2 rc, nc;
    float e_;
    ln_stats<false>(v, lane, 0.03125f, tf, z4, rc, nc, e_);
    ln_apply<true>(tf, lane, rc, nc, c);
#pragma unroll
    for (int k = 0; k < 9; ++k) c[k] = (c[k] & ~UMASK) | tw[k >> 2];
    *(u32x4*)(smem + la) = (u32x4){c[0], c[1], c[2], c[3]};
    *(u32x4*)(smem + la + RP) = (u32x4){c[4], c[5], c[6], c[7]};
    *(uint32_t*)(smem + lt) = c[8];
    u32x4 ha, hb;
    {
      uint32_t t, cc, n = SPIN_CAP;
      asm volatile("s_mov_b64 exec, 1\n\tv_mov_b32 %2, 1\n\tds_add_u32 %5, %2\n\ts_mov_b64 exec, -1\n"
                   "1:\n\tds_read_b32 %2, %5\n\tds_read_b64_tr_b16 v[40:41], %6\n\tds_read_b64_tr_b16 v[42:43], %6 offset:%7\n\tds_read_b64_tr_b16 v[44:45], %6 offset:512\n\t"
                   "ds_read_b64_tr_b16 v[46:47], %6 offset:%8\n\ts_sub_u32 %4, %4, 1\n\ts_cbranch_scc1 2f\n\ts_waitcnt lgkmcnt(4)\n\tv_readfirstlane_b32 %3, %2\n\t"
                   "s_cmp_lt_u32 %3, 4\n\ts_cbranch_scc1 1b\n2:\n\ts_waitcnt lgkmcnt(0)"
                   : "=&{v[40:43]}"(ha), "=&{v[44:47]}"(hb), "=&v"(t), "=&s"(cc), "+s"(n) : "v"(c0), "v"(ta), "n"(4 * RP), "n"(512 + 4 * RP) : "memory", "scc");
    }
    ST16(uo, ha);
    ST16(uo + 64 * 128, hb);
    if (wl == 0) {
      const u32x2 h4 = trd(ta + 1024), h5 = trd(ta + 1024 + 4 * RP);
      ST16(uo + 128 * 128, ((u32x4){h4[0], h4[1], h5[0], h5[1]}));
    }
  };
  const int dX = 512 - 24 + 8 * bx + bj;
  if constexpr (ROLE == 2) {
    const int tl = tid - 256;
    if (isx) {
      __builtin_amdgcn_s_setprio(QPRIO0);
      *(u32x4*)(smem + X_TB + 16 * tl) = zero4;
      *(u32x4*)(smem + X_TB + 16 * min(256 + tl, (TB + C_U) / 16 - 1)) = zero4;
      const int pr = (wl + XROT) & 3;
      COps e;
      conv_ops(lb0 + X_TB, lb0 + X_UL, lb0 + X_IM, lane, e);
      TOps t;
      table_ops(lb0 + X_IM, lb0 + X_TB, wl & 1, lane, t);
      e.o = lb0 + X_ST + (uint32_t)(16 * ((2 * i + (kq >> 1)) ^ ((i >> 2) & 1)) + 8 * (kq & 1));
      e.f = ca + 28 - 32;
      asm volatile("" : "+v"(e.o), "+v"(e.f));
      float ldt, are, aim, bre, bim, cre, cim, dsk;
      P_WAIT(0, , "v103", "v104", "v105", "v106", "v107", "v109");
      asm volatile("" : "={v109}"(ep));
      T = ((uint32_t)__builtin_amdgcn_readfirstlane(ep) + 1u) * 0x9E3779B1u;
      P_MORE();
      asm volatile("v_lshlrev_b32 %0, 16, v107" : "=v"(dsk) : : "v107");
      TS(0, 1);
      __builtin_amdgcn_s_setprio(QPRIO);
      int* evp = (int*)(sel + 44);
      char* im = smem + X_IM + IP * (tl & 127);
      evp[wl] = (wl < 2) ? gen_y(ldt, are, aim, bre, bim, cre, cim, lb0 + X_IM + IP * tl, [] {} TSA) : gen_x(ldt, are, aim, im);
      TS(0, 2);
      __builtin_amdgcn_s_setprio(QPRIO2);
      gsync(ca + 8, 4);
      if (wl < 2) evp[-4 + wl] = (int)table_tile(t, wl & 1, lane, lb0 + LM + 4 * (128 + 44), dsk TSA);
      if (wl < 2) gsync0(ca + 8, 8); else { count_up(ca + 8); SLEEP(XSL); gwait(ca + 8, 8); }
      const uint32_t cs = (uint32_t)__builtin_amdgcn_readfirstlane(evp[-4]);
      if (wl == 3) *(u32x4*)(smem + X_IM + pad_at(lane & 7)) = zero4;
      TS(0, 3);
      CONV_RUN(2 + pr, e, , "+{v11}"(tid2));
      __builtin_amdgcn_s_setprio(3);
      {
        const int lane = tid2 & 63;
        const u32x4 v = *(const u32x4*)(smem + X_ST + 2048 * pr + 1024 * (lane >> 5) + 16 * ((lane & 31) ^ ((lane >> 3) & 1)));
        if (__builtin_expect(v_put(v, (u32x4){utag(T, 0), utag(T, 1), utag(T, 2), utag(T, 3)}, cs != S16, v_at(dX, 64 * pr + lane)), 0)) {
          DRAIN();
          if (lane == 0) ST4(O_DN + 16u * (4 * (dX - 256) + pr), T);
        }
        if (pr == 0 && lane == 0) asm volatile("global_store_dwordx2 %0, %1, %2 sc1" : : "v"(O_SC + 16u * dX), "v"((u32x2){T, cs}), "s"(ws) : "memory");
      }
      TS(0, 6);
    }
    if (!isx) __builtin_amdgcn_s_setprio(PPRIO);
    asm volatile("" : "+v"(tid2));
    if (!isx) rows_xw(tid2 & 63);
    if (!isx && !ffn) {
      SLEEP(RSLEEP);
#pragma unroll
      for (int k = 0; k < 7; ++k) ST16(O_SL + (uint32_t)((6 * bx + bj - 18) * 1792 + 256 * k - 256 + tid2) * 16u, ones);
      DRAIN();
    }
    if (!isx) BAR();
    const uint32_t tp = (uint32_t)__builtin_amdgcn_readfirstlane(cnt[4]);
    if (!isx) T = (uint32_t)__builtin_amdgcn_readfirstlane(sel[43]);
    tv = tp + UD + VD2 - vearly;
  } else {
    __builtin_amdgcn_s_setprio(3);
    if (tid < 12) cnt[tid] = 0u;
    BAR();
    if (tid < 20) ((uint32_t*)(hm + 320))[tid] = 0u;
    if (tid >= 184) *(u32x4*)(smem + L_SC - 16 * 184 + 16 * tid) = (u32x4){0x41804180u, 0x41804180u, 0x41804180u, 0x41804180u};
    uint32_t sink = 0;
    ln1(tg, 0, ca + 12);
    TS(0, 2);
    __builtin_amdgcn_s_setprio(NPRIO);
    asm volatile("s_waitcnt vmcnt(0)" : "+v"(sink) : : "memory");
    TS(0, 13);
    if (w == 0) {
      const uint32_t xr = O_XC + (uint32_t)((511 - 2 * (g0 + w)) * 144 + lane) * 16u;
      ST16(xr, ones);
      ST16(xr + 1024u, ones);
      if (lane < 16) ST16(xr + 2048u, ones);
    }
    if (w == 2 && b < 72 && lane < 8) ST16(O_FB + (uint32_t)(8 * b + lane) * 16u, ones);
    if (isx) {
      rows_xw(lane);
      char* sp = smem + X_RW(w) + 16 * lane;
      *(u32x4*)sp = (u32x4){xv[0], xv[1], xv[2], xv[3]};
      *(u32x4*)(sp + 1024) = (u32x4){xv[4], xv[5], xv[6], xv[7]};
      *(u32x2*)(smem + X_RW(w) + 2048 + 8 * lane) = (u32x2){xv[8], wv[4]};
      if (w == 0) *(u32x4*)(smem + X_WR + 16 * lane) = (u32x4){wv[0], wv[1], wv[2], wv[3]};
    }
    SLEEP(ESLEEP);
    uint32_t tu = latest(gate(ws, O_SU + (uint32_t)(256 * bx + (ALLP ? tid : 64 * w + 4 * i + (bj & 3))) * 128u, T));
    asm volatile("s_mov_b64 exec, 1\n\tds_max_u32 %0, %1\n\ts_mov_b64 exec, -1" : : "v"(ca + 16), "v"(tu) : "memory");
    if (w == 0) sel[43] = T;
    if (isx) {
      const uint32_t xo = u_at(tid, dX);
      char* sm = smem + X_UL + C_U - 16 + 16 * tid;
      const u32x4 ut = {utag(T, 0), utag(T, 1), utag(T, 2), utag(T, 3)};
      __builtin_amdgcn_s_setprio(3);
      until(tu + UD + XD);
      u32x4 uw;
      asm volatile("global_load_dwordx4 %0, %1, %2 sc1" : "=&v"(uw) : "v"(xo), "s"(ws) : "memory");
      asm volatile("s_waitcnt vmcnt(0)" : "+v"(uw));
      uw ^= ut;
      u_again(uw, ut, xo);
      *(u32x4*)(sm + 16) = uw;
      BAR();
      if (w < 3) {
        const u32x4 pw = *(const u32x4*)sm;
        if (w < 2) sums<true>(smem + X_IM + 16 * tid, pw, uw); else sums<false>(smem + X_IM + 16 * tid, pw, uw);
        count_up(ca + 28);
      }
    } else {
      until(tu + UD + WD);
      BAR();
    }
    tu = (uint32_t)__builtin_amdgcn_readfirstlane(cnt[4]);
    TS(0, 3);
    __builtin_amdgcn_s_setprio(DPRIO);
    if (ffn) {
      ts1 = (tu + UD + VD2 - TSTOP) | 1u;
      gu_dma(0, N0, IC<40>{});
      gu_dma(N0, NA, IC<WDEPTH>{});
      asm volatile("s_waitcnt vmcnt(%0)" : : "n"(NA - 9) : "memory");
      gsync0(ca + 4, 4);
      gu_read(0);
      asm volatile("s_waitcnt lgkmcnt(0)" ::: "memory");
      count_up(ca + 36);
      TS(0, 4);
      gu_dma(NA, 18, IC<WDEPTH2>{});
      gwait(ca + 36, 4);
      down_dma(0, KN, lane);
      asm volatile("s_waitcnt vmcnt(%0)" : : "n"(3 * KN) : "memory");
      count_up(ca + 4);
      TS(0, 7);
    } else {
      if (!isx && C3) {
        const int n = 6 * min(tid, 191) + bj - 18, r = n >> 2;
        const char* wb = (const char*)((n & 1) ? wu : wg) + bx * 1152;
        const int c0 = (3 - (int)((uintptr_t)wb >> 7)) & 7;
        wb += (uint32_t)((r + 144 * ((r >= 144) + ((n >> 1) & 1))) * (F_ * 2) + 128 * c0);
        TOUCH(wb);
        if (c0 == 0) TOUCH(wb + 1024);
      }
      auto d_line = [&](int k) {
        const int n = min((k * 14 + bj - 18) * 64 + lane, 5183), q = n / 1296, r = n - 1296 * q;
        return (const char*)wd + (uint32_t)(bx * 5184 + (r / 18) * 72 + 18 * (HREV ? 3 - q : q) + r % 18) * 128u;
      };
      int k = w;
      if (w < HW) {
#pragma unroll 1
        for (; k < 6 && (int)((uint32_t)__builtin_amdgcn_s_memrealtime() - (tu + UD + VD2 - HSTOP)) < 0; k += HW) { TOUCH(d_line(k)); DRAIN(); }
      }
      TS(0, 4);
      until(tu + UD + VD2 + RD2);
      if (RW) gwait(ca, 4);
      if (w < HW) {
#pragma unroll 1
        for (; k < 6; k += HW) { TOUCH(d_line(k)); DRAIN(); }
      }
      TS(0, 7);
    }
    tv = tu + UD + VD2 - vearly;
    tk = tu + UD + VD2 + KD;
  }
  {
  const int tid = tid2, lane = tid & 63, i = lane & 15, kq = lane >> 4;
  __builtin_amdgcn_s_setprio(3);
  const int PL = ffn ? 6400 : LW + 6656;
  f32x2 xf[9];
  f32x4 za = {0.f, 0.f, 0.f, 0.f}, zb = za;
  uint32_t al, ha, h0a, h1a, so, da;
  auto ffn_terms = [&] {
    al = O_XC + 64u * (i >> 3) + 16u * kq + 128u * w;
    ha = (ROLE == 1) ? (uint32_t)((4 * (kq & 1) + 2 * (kq >> 1) + (i >> 3)) * 80 + 16 * w + 2 * (i & 7)) : 0u;
    h0a = (uint32_t)(min(i, 8) * 80 + 16 * kq); h1a = (uint32_t)(min(i ^ 8, 8) * 80 + 16 * kq);
    so = O_SL + (uint32_t)fb * SP + ((ROLE == 2) ? 16u * tid - 4096u : 8192u + 16u * lane);
    da = lb0 + (uint32_t)((8 * kq + ((lane >> 2) & 3)) * 1152 + 32 * ((8 * kq + ((lane >> 2) & 3) + 2) >> 2) + 8 * (lane & 3) + ((ROLE == 2) ? 128 * wl : 1024));
    if (ROLE == 2) asm volatile("" : "+v"(h0a), "+v"(h1a), "+v"(so), "+v"(da)); else asm volatile("" : "+v"(ha), "+v"(al), "+v"(da));
  };
  if (ROLE == 1) ffn_terms();
  uint32_t hv[9] = {}, nv[9] = {}, keya = 0u, keyb = 0u;
  int rka = 0, rkb = 0;
  auto put_row = [&](const uint32_t* r, int j, const void* base, uint32_t ro) {
    const u32x4 tl = {r[8], (uint32_t)DPP_I((int)r[8], 0x55, 0xf), (uint32_t)DPP_I((int)r[8], 0xaa, 0xf), (uint32_t)DPP_I((int)r[8], 0xff, 0xf)};
    asm volatile("global_store_dwordx4 %0, %1, %2 sc1\n\ts_nop 1" : : "v"(ro + 16u * lane), "v"((u32x4){r[4 * j], r[4 * j + 1], r[4 * j + 2], r[4 * j + 3]}), "s"(base) : "memory");
    if ((lane & 35) == 32 * j) asm volatile("global_store_dwordx4 %0, %1, %2 offset:1024 sc1\n\ts_nop 1" : : "v"(ro + 4u * (lane & 31)), "v"(tl), "s"(base) : "memory");
  };
  if (ln2) {
    const int tl = tid - 256, d1 = 64 * (tl >> 5) + (tl & 31), d3 = 64 * ((tl >> 4) & 7) + 32 + (tl & 15), d2 = (tl < 128) ? d3 + 16 : min(384 + tl, 575);
    uint32_t vo = v_at(d1, g0), vo2 = v_at(d2, g0), vo3 = v_at(d3, g0);
    u32x4 ut = {utag(T, 0), utag(T, 1), utag(T, 2), utag(T, 3)};
    u32x4 va, vc = ut, vb = ut;
    asm volatile("" : "+v"(vo), "+v"(vo2), "+v"(vo3), "+v"(ut), "+v"(vc), "+v"(vb));
    TS(0, 7);
    until(tv);
    TS(0, 8);
    asm volatile("global_load_dwordx4 %0, %2, %4 sc1\n\ts_cmp_lt_u32 %5, 2\n\ts_cbranch_scc1 1f\n\tglobal_load_dwordx4 %1, %3, %4 sc1\n1:"
                 : "=&v"(va), "+v"(vc) : "v"(vo), "v"(vo3), "s"(ws), "s"(wl) : "memory", "scc");
    until(tv + V2D);
    asm volatile("s_cmp_eq_u32 %3, 3\n\ts_cbranch_scc1 1f\n\tglobal_load_dwordx4 %0, %1, %2 sc1\n1:" : "+v"(vb) : "v"(vo2), "s"(ws), "s"(wl) : "memory", "scc");
    __builtin_amdgcn_sched_barrier(0);
    if (isx) {
      const u32x4 ra = *(const u32x4*)(smem + X_RW(wl) + 16 * lane), rb = *(const u32x4*)(smem + X_RW(wl) + 1024 + 16 * lane), rw = *(const u32x4*)(smem + X_WR + 16 * lane);
      const u32x2 rc = *(const u32x2*)(smem + X_RW(wl) + 2048 + 8 * lane);
#pragma unroll
      for (int k = 0; k < 4; ++k) { xv[k] = ra[k]; xv[4 + k] = rb[k]; wv[k] = rw[k]; }
      xv[8] = rc[0]; wv[4] = rc[1];
    }
    uint32_t pa = (uint32_t)(PL + 9216 * wl + 32 * lane), pb = (uint32_t)(PL + 9216 * wl + 2048 + 8 * (lane & 31)), ps = (lane < 32) ? 0u : 16u;
    uint32_t kid = (uint32_t)(((255 - g2) << 5) | (7 - 2 * wl - (lane >> 5)));
    uint32_t px = (uint32_t)(PL + 4 * d1), py = (uint32_t)(PL + 4 * d2), pz = (uint32_t)(PL + 4 * d3);
#pragma unroll
    for (int k = 0; k < 9; ++k) xf[k] = (f32x2){bflo(xv[k]), bfhi(xv[k])};
    asm volatile("" : "+v"(xf[0]), "+v"(xf[1]), "+v"(xf[2]), "+v"(xf[3]), "+v"(xf[4]), "+v"(xf[5]), "+v"(xf[6]), "+v"(xf[7]), "+v"(xf[8]));
    asm volatile("" : "+v"(za), "+v"(zb), "+v"(pa), "+v"(pb), "+v"(ps), "+v"(kid), "+v"(px), "+v"(py), "+v"(pz));
    float sv[10], ws_ = dot2(wv[4], 0x3f803f80u, 0.f) * 0.5f;
#pragma unroll
    for (int k = 0; k < 4; ++k) ws_ = dot2(wv[k], 0x3f803f80u, ws_);
    ws_ = wave_sum(ws_);
#pragma unroll
    for (int k = 0; k < 10; ++k) sv[k] = 16.f;
    uint32_t ka_ = lb0 + LM + 4u * (128 + 48 + 2 * wl + (lane >> 5)), kb_ = lb0 + LM + 4u * (128 + 48);
    uint32_t ko_ = O_KEY + (uint32_t)(256 * (lane & 7) + 8 * (g2 & 31) + (g2 >> 5)) * 16u;
    asm volatile("" : "+v"(ws_), "+v"(sv[0]), "+v"(sv[1]), "+v"(sv[2]), "+v"(sv[3]), "+v"(sv[4]), "+v"(sv[5]), "+v"(sv[6]), "+v"(sv[7]), "+v"(sv[8]), "+v"(sv[9]),
                 "+v"(ka_), "+v"(kb_), "+v"(ko_));
    if (ffn) gwait(ca + 36, 4);
    asm volatile("s_waitcnt vmcnt(1)\n\ts_cmp_eq_u32 %2, 3\n\ts_cbranch_scc0 1f\n\ts_waitcnt vmcnt(0)\n1:" : "+v"(va), "+v"(vc) : "s"(wl) : "memory", "scc");
    auto turn1 = [&] {
#pragma unroll
      for (int e = 0; e < 4; ++e) *(uint32_t*)(smem + px + 9216 * e) = va[e];
      if (wl >= 2) {
#pragma unroll
        for (int e = 0; e < 4; ++e) *(uint32_t*)(smem + pz + 9216 * e) = vc[e];
      }
    };
    auto turn = [&] {
      if (wl < 3) {
#pragma unroll
        for (int e = 0; e < 4; ++e) *(uint32_t*)(smem + py + 9216 * e) = vb[e];
      }
    };
    va ^= ut; vc ^= ut;
    turn1();
    asm volatile("s_waitcnt vmcnt(0)" : "+v"(vb) : : "memory");
    vb ^= ut;
    turn();
    const u32x4 vs = va | vb | vc;
    if (__builtin_expect(__any(((vs[0] | vs[1] | vs[2] | vs[3]) & UMASK) != 0u), 0)) {
#pragma unroll 1
      for (int spin = 0; spin < 4; ++spin) {
        const bool na = ((va[0] | va[1] | va[2] | va[3]) & UMASK) != 0u, nb = ((vb[0] | vb[1] | vb[2] | vb[3]) & UMASK) != 0u, nc = ((vc[0] | vc[1] | vc[2] | vc[3]) & UMASK) != 0u;
        if (!__any(na || nb || nc)) break;
        TSC(16);
        if (na) LD16(va, vo);
        if (nb) LD16(vb, vo2);
        if (nc) LD16(vc, vo3);
        asm volatile("s_waitcnt vmcnt(0)" : "+v"(va), "+v"(vb), "+v"(vc) : : "memory");
        if (na) va ^= ut;
        if (nb) vb ^= ut;
        if (nc) vc ^= ut;
      }
      auto again = [&](u32x4& v, uint32_t o, int d) {
        const int J = g0 >> 5;
        const uint32_t dn = O_DN + 16u * (uint32_t)((d < 512) ? 4 * (d >> 1) + ((0x01013232 >> (4 * J)) & 3) : 4 * (d - 256) + (J >> 1));
        bool pend = ((v[0] | v[1] | v[2] | v[3]) & UMASK) != 0u;
#pragma unroll 1
        for (int spin = 0; spin < SPIN_CAP && __any(pend); ++spin) {
          TSC(16);
          SLEEP(PSLEEP);
          if (pend) {
            uint32_t fin = ~T;
            if ((v[0] & v[1] & v[2] & v[3] & LMASK) == LMASK)
              asm volatile("global_load_dword %0, %1, %2 sc1\n\ts_waitcnt vmcnt(0)" : "=&v"(fin) : "v"(dn), "s"(ws) : "memory");
            LD16W(v, o);
            v ^= ut;
            pend = ((v[0] | v[1] | v[2] | v[3]) & UMASK) != 0u;
            if (pend && fin == T) {
              u32x2 sc;
              for (int k = 0; k < SPIN_CAP; ++k) {
                asm volatile("global_load_dwordx2 %0, %1, %2 sc1\n\ts_waitcnt vmcnt(0)" : "=&v"(sc) : "v"(O_SC + 16u * d), "s"(ws) : "memory");
                if (sc[0] == T) break;
              }
              *(u16*)(smem + L_SC + 2 * d) = (u16)(sc[1] >> 16);
              cnt[10] = 1u;
              v = (v ^ ut) & ~LMASK;
              pend = false;
            }
          }
        }
      };
      again(va, vo, d1); again(vc, vo3, d3); again(vb, vo2, d2);
      turn1();
      turn();
    }
    u32x4 a0, a1;
    u32x2 a2, cf;
    {
      uint32_t cc, n = SPIN_CAP;
      asm volatile("s_mov_b64 exec, 1\n\tv_mov_b32 v90, 1\n\tds_add_u32 %6, v90\n\ts_mov_b64 exec, -1\n"
                   "1:\n\tds_read2_b32 %3, %6 offset1:10\n\tds_read_b128 %0, %7\n\tds_read_b128 %1, %7 offset:16\n\tds_read_b64 %2, %8\n\t"
                   "s_sub_u32 %5, %5, 1\n\ts_cbranch_scc1 2f\n\ts_waitcnt lgkmcnt(3)\n\tv_readfirstlane_b32 %4, v90\n\t"
                   "s_cmp_lt_u32 %4, 4\n\ts_cbranch_scc1 1b\n2:\n\ts_waitcnt lgkmcnt(0)"
                   : "=&v"(a0), "=&v"(a1), "=&v"(a2), "=&{v[90:91]}"(cf), "=&s"(cc), "+s"(n) : "v"(ca), "v"(lb0 + pa), "v"(lb0 + pb) : "memory", "scc");
    }
    TS(0, 9);
    if (__builtin_expect(__builtin_amdgcn_readfirstlane(cf[1]) != 0, 0)) {
      const u32x4 q = *(const u32x4*)(smem + L_SC + 16 * lane);
      const uint32_t r = *(const uint32_t*)(smem + L_SC + 1024 + 4 * (lane & 31));
#pragma unroll
      for (int k = 0; k < 4; ++k) { sv[2 * k] = bflo(q[k]); sv[2 * k + 1] = bfhi(q[k]); }
      sv[8] = bflo(r); sv[9] = bfhi(r);
    }
    const uint32_t av[10] = {a0[0], a0[1], a0[2], a0[3], a1[0], a1[1], a1[2], a1[3], a2[0] >> ps, a2[1] >> ps};
    f32x2 h[9];
#pragma unroll
    for (int k = 0; k < 10; ++k) {
      asm("v_fma_mix_f32 %0, %1, %2, %3 op_sel_hi:[1,0,0]" : "=v"(h[(k < 8) ? k >> 1 : 8][k & 1]) : "v"(av[k]), "v"(sv[k]), "v"(xf[(k < 8) ? k >> 1 : 8][k & 1]));
      if (k < 8) asm("v_fma_mix_f32 %0, %1, %2, %3 op_sel:[1,0,0] op_sel_hi:[1,0,0]" : "=v"(h[4 + (k >> 1)][k & 1]) : "v"(av[k]), "v"(sv[k]), "v"(xf[4 + (k >> 1)][k & 1]));
    }
#pragma unroll
    for (int k = 0; k < 9; ++k) hv[k] = pack2(h[k][0], h[k][1]);
    const float da = dot2(hv[2], wv[2], dot2(hv[0], wv[0], zb[0])) + dot2(hv[3], wv[3], dot2(hv[1], wv[1], zb[1]));
    const float db = dot2(hv[6], wv[2], dot2(hv[4], wv[0], zb[2])) + dot2(hv[7], wv[3], dot2(hv[5], wv[1], zb[3]));
    float dt = dot2(hv[8], wv[4], fold32(da, db));
    f32x2 rc, nc;
    ln_stats<true>(hv, lane, 1.f, xf, za, rc, nc, dt);
    dt = (lane < 32) ? dt * rc[0] + ws_ * nc[0] : dt * rc[1] + ws_ * nc[1];
    const uint32_t bb = pack2(dt, dt) & 0xffff0000u;
    const uint32_t kk = (bb ^ (((uint32_t)((int)bb >> 31) & 0x7fff0000u) | 0x80000000u)) | kid;
    u32x4 ka, kb;
    uint32_t old;
    asm volatile("s_mov_b64 exec, %5\n\tds_write_b32 %3, %4\n\ts_mov_b64 exec, 1\n\tv_mov_b32 %2, 1\n\tds_add_rtn_u32 %2, %7, %2\n\ts_mov_b64 exec, -1\n\t"
                 "ds_read_b128 %0, %6\n\tds_read_b128 %1, %6 offset:16\n\ts_waitcnt lgkmcnt(0)"
                 : "=&v"(ka), "=&v"(kb), "=&v"(old) : "v"(ka_), "v"(kk), "s"(0x8000000080000000ull), "v"(kb_), "v"(ca) : "memory");
    if (__builtin_amdgcn_readfirstlane(old) == 7) {
      uint32_t m1 = max(max(ka[0], ka[1]), ka[2]), m2 = med3u(ka[0], ka[1], ka[2]), m3 = min(min(ka[0], ka[1]), ka[2]);
#pragma unroll
      for (int e = 0; e < 5; ++e) {
        const uint32_t kx = e ? kb[e - 1] : ka[3];
        m3 = med3u(m2, m3, kx); m2 = med3u(m1, m2, kx); m1 = max(m1, kx);
      }
      if (lane < 8) ST16(ko_, ((u32x4){m1 | 24u, m2 | 16u, m3 | 8u, T & ~1u}));
    }
    TS(0, 10);
    keya = (uint32_t)__builtin_amdgcn_readlane(kk, 31); keyb = (uint32_t)__builtin_amdgcn_readlane(kk, 63);
    ln_apply<false>(xf, lane, rc, nc, nv);
    gwait(ca, 8);
    if (wl == 1) {
      const u32x4 kv = *(const u32x4*)&kl[4 * (lane & 1)];
      if (lane < 2) ST16(O_KF + (uint32_t)(2 * g2 + lane) * 16u, ((u32x4){__builtin_amdgcn_perm(kv[1], kv[0], 0x07060302u), T, __builtin_amdgcn_perm(kv[3], kv[2], 0x07060302u), T}));
      if (ROLE == 2 && b == 0 && lane == 0) ST4(O_EP, T * 0x0E8B2F51u);
    }
    const uint32_t ko = kl[lane & 7];
    rka = FORCEFB + __popcll(__ballot(ko > keya) & 0xffull); rkb = FORCEFB + __popcll(__ballot(ko > keyb) & 0xffull);
#pragma unroll
    for (int k = 0; k < 9; ++k) {
      const f32x2 o = xf[k] + (f32x2){bflo(nv[k]), bfhi(nv[k])};
      hv[k] = pack2(o[0], o[1]);
      nv[k] = min(nv[k], 0xfffffffeu);
    }
    if (rka < 2) put_row(nv, 0, ws, O_XC + (uint32_t)(1023 - 4 * g2 - rka) * 1152u);
    if (rkb < 2) put_row(nv, 1, ws, O_XC + (uint32_t)(1023 - 4 * g2 - rkb) * 1152u);
    if (!LPOS && ffn) down_dma(KN, 4, lane);
    ffn_terms();
  }
  uint32_t k3rd = 0u, k4th = 0u;
  uint32_t q0 = lb0 + LM + 4u * lane, q1 = lb0 + LM + 32u * w, q2 = q1 + 4u * (lane & 7), q3 = lb0 + LM + 512u + 4u * (lane & 7), q4 = lb0 + LM + (uint32_t)(tid & ~7);
  uint32_t r8 = C_, sb = lb0 + LM + 512u;
  asm volatile("" : "+v"(q0), "+v"(q1), "+v"(q2), "+v"(q3), "+v"(q4), "+v"(r8), "+v"(sb));
  if constexpr (ROLE == 1) {
    u32x4 key;
    const uint32_t kof = O_KEY + (uint32_t)(256 * bx + tid) * 16u;
    until(tk);
    if (!ffn) SLEEP(NSLEEP);
    LD16(key, kof);
    if (ffn) {
      gwait0(ca + 4, 8);
      gu_read(1);
    }
    asm volatile("s_waitcnt vmcnt(0)" : "+v"(key) : : "memory");
    if (__builtin_expect(__any(key[3] != (T & ~1u)), 0)) {
#pragma unroll 1
      for (int spin = 0; spin < SPIN_CAP; ++spin) {
        const bool need = key[3] != (T & ~1u);
        if (!__any(need)) break;
        TSC(15);
        SLEEP(KBACK);
        if (need) LD16W(key, kof);
      }
    }
    TS(0, 11);
    const uint32_t m1 = oct_maxu(key[0]);
    const bool hit = key[0] == m1;
    const uint32_t k0 = hit ? key[1] : key[0], k1 = hit ? key[2] : key[1];
    const uint32_t m2 = oct_maxu(k0);
    *LP(u32x2, q4) = (u32x2){m1, m2};
    k3rd = (k0 == m2) ? k1 : k0;
    k4th = (k0 == m2) ? (hit ? 0u : key[2]) : k1;
    if (RPF == 1 && !ffn && w == (bj & 3)) {
      const uint32_t r1 = O_XC + ((m1 >> 3) & 0x3ffu) * 1152u + 128u * (lane & 7), r2 = O_XC + ((m2 >> 3) & 0x3ffu) * 1152u + 128u * (lane & 7);
      asm volatile("global_load_dword v126, %0, %3 sc1\n\tglobal_load_dword v126, %1, %3 sc1\n\tglobal_load_dword v126, %2, %3 offset:1024 sc1"
                   : : "v"(r1), "v"(r2), "v"(((lane & 1) ? r1 : r2) - 128u * (lane & 7)), "s"(ws) : "memory", "v126");
    }
  }
  auto pick = [&](auto co, auto so_) {
    BAR();
    const uint32_t c = *LP(uint32_t, q0 + co.value), own = *LP(uint32_t, q2 + co.value);
    const u32x4 la = *LP(u32x4, q1 + co.value), lb = *LP(u32x4, q1 + co.value + 16);
    uint32_t r = r8;
    asm("v_cmp_gt_u32 s[40:41], %1, %2\n\tv_cmp_gt_u32 s[42:43], %1, %3\n\tv_cmp_gt_u32 s[44:45], %1, %4\n\tv_cmp_gt_u32 s[46:47], %1, %5\n\t"
        "v_cmp_gt_u32 s[48:49], %1, %6\n\tv_cmp_gt_u32 s[50:51], %1, %7\n\tv_cmp_gt_u32 s[52:53], %1, %8\n\tv_cmp_gt_u32 s[54:55], %1, %9\n\t"
        "s_bcnt1_i32_b64 s40, s[40:41]\n\ts_bcnt1_i32_b64 s42, s[42:43]\n\tv_writelane_b32 %0, s40, 0\n\ts_bcnt1_i32_b64 s44, s[44:45]\n\t"
        "v_writelane_b32 %0, s42, 1\n\ts_bcnt1_i32_b64 s46, s[46:47]\n\tv_writelane_b32 %0, s44, 2\n\ts_bcnt1_i32_b64 s48, s[48:49]\n\t"
        "v_writelane_b32 %0, s46, 3\n\ts_bcnt1_i32_b64 s50, s[50:51]\n\tv_writelane_b32 %0, s48, 4\n\ts_bcnt1_i32_b64 s52, s[52:53]\n\t"
        "v_writelane_b32 %0, s50, 5\n\ts_bcnt1_i32_b64 s54, s[54:55]\n\tv_writelane_b32 %0, s52, 6\n\tv_writelane_b32 %0, s54, 7"
        : "+v"(r) : "v"(c), "v"(la[0]), "v"(la[1]), "v"(la[2]), "v"(la[3]), "v"(lb[0]), "v"(lb[1]), "v"(lb[2]), "v"(lb[3])
        : "scc", "s40", "s41", "s42", "s43", "s44", "s45", "s46", "s47", "s48", "s49", "s50", "s51", "s52", "s53", "s54", "s55");
    *LP(uint32_t, sb + 4u * min(r, (uint32_t)C_) + so_.value) = own;
    BAR();
  };
  auto rows = [&](uint32_t ao, auto&& meanwhile) {
    u32x4 aa, ab, ac = __builtin_bit_cast(u32x4, zb);
    asm volatile("global_load_dwordx4 %0, %3, %4 sc1\n\tglobal_load_dwordx4 %1, %3, %4 offset:512 sc1\n\t"
                 "s_cmp_lg_u32 %5, 0\n\ts_cbranch_scc1 1f\n\tglobal_load_dwordx4 %2, %3, %4 offset:1024 sc1\n1:"
                 : "=&v"(aa), "=&v"(ab), "+v"(ac) : "v"(ao), "s"(ws), "s"(w) : "memory", "scc");
    __builtin_amdgcn_sched_barrier(0);
    meanwhile();
    asm volatile("s_waitcnt vmcnt(0)" : "+v"(aa), "+v"(ab), "+v"(ac) : : "memory");
    if (__builtin_expect(__any(max(max(max4(aa), max4(ab)), max4(ac)) == 0xffffffffu), 0)) {
#pragma unroll 1
      for (int spin = 0; spin < SPIN_CAP; ++spin) {
        const bool na = max4(aa) == 0xffffffffu, nb = max4(ab) == 0xffffffffu, nc = max4(ac) == 0xffffffffu;
        if (!__any(na || nb || nc)) break;
        TSC(14);
        SLEEP(PSLEEP);
        if (na) LD16W(aa, ao);
        if (nb) LD16W(ab, ao + 512u);
        if (nc) LD16W(ac, ao + 1024u);
      }
    }
    *(u32x4*)(smem + LA + 16 * tid) = aa;
    *(u32x4*)(smem + LA + 4096 + 16 * tid) = ab;
    if (w == 0) *(u32x4*)(smem + LA + 8192 + 16 * tid) = ac;
  };
  auto rows_dma = [&](uint32_t ao) {
    uint32_t dl = lb0 + LA + 1024u * w, keep;
    asm volatile("s_mov_b32 %0, m0\n\ts_mov_b32 m0, %1\n\ts_add_u32 %1, %1, 4096 - 512\n\tglobal_load_lds_dwordx4 %2, %3 sc1\n\ts_mov_b32 m0, %1\n\ts_add_u32 %1, %1, 4096 - 512\n\t"
                 "global_load_lds_dwordx4 %2, %3 offset:512 sc1\n\ts_cmp_lg_u32 %4, 0\n\ts_cbranch_scc1 1f\n\ts_mov_b32 m0, %1\n\ts_nop 0\n\t"
                 "global_load_lds_dwordx4 %2, %3 offset:1024 sc1\n1:\n\ts_mov_b32 m0, %0" : "=&s"(keep), "+s"(dl) : "v"(ao), "s"(ws), "s"(w) : "memory", "scc");
  };
  auto get_bd = [&](int t0, int t1) {
#pragma unroll
    for (int t = t0; t < t1; ++t) {
      const u32x2 h0 = trd(da + 512 * (t >> 2) + 32 * (t & 3)), h1 = trd(da + 512 * (t >> 2) + 32 * (t & 3) + 4608 + 32);
      bw[t] = (u32x4){h0[0], h0[1], h1[0], h1[1]};
    }
#pragma unroll
    for (int t = t0; t < t1; ++t) asm volatile("" : "+v"(bw[t]));
  };
  pick(IC<0>{}, IC<0>{});
  uint32_t mykey = *LP(uint32_t, q3), ro = ((mykey >> 3) & 0x3ffu) * 1152u;
  TS(0, 12);
  auto check = [&] {
    cnt[(k3rd > (uint32_t)__builtin_amdgcn_readlane(mykey, 7)) ? 5 : 6] = 1u;
    const uint32_t m3 = oct_maxu(k3rd);
    if (k3rd == m3) k3rd = k4th;
    if ((lane & 7) == 0) cand[72 + (tid >> 3)] = m3;
    if (w == 0 && lane < 32) cand[(lane < 8) ? 64 + lane : 96 + lane] = (lane < 8) ? mykey : 0u;
  };
  if constexpr (ROLE == 1) {
    if (ffn && !FORCEFB) rows_dma(ro + al);
    check();
    DRAIN();
  } else if (ffn) {
    if (LPOS) { SLEEP(LSLEEP); down_dma(KN, 4, lane); } else { DRAIN(); gsync0(ca, 12); get_bd(0, 8); }
  }
  BAR();
  TS(0, 5);
  auto put_out = [&](int j, uint32_t k0, int rk) {
    const uint64_t mine = __ballot(((mykey ^ k0) & ~24u) == 0u) & 0xffull;
    if (mine == 0ull) put_row(hv, j, out, (uint32_t)((8 * g2 + 2 * wl + j) * (D_ * 2)));
    else if (rk >= 2) put_row(nv, j, ws, O_FB + 1152u * (uint32_t)__builtin_ctzll(mine));
  };
  auto rest_out = [&] { put_out(0, keya, rka); put_out(1, keyb, rkb); };
  f32x4 lo, hi;
  auto gate_up = [&] {
    u32x4 av[5];
    const uint32_t aa = lb0 + LA + 16u * lane;
#pragma unroll
    for (int j = 0; j < 5; ++j) asm volatile("ds_read_b128 %0, %1 offset:%2" : "=v"(av[j]) : "v"(aa), "n"(1024 * j) : "memory");
    lo = (f32x4){0.f, 0.f, 0.f, 0.f}; hi = lo;
#pragma unroll
    for (int j = 0; j < 9; ++j) {
      if (j < 5) asm volatile("s_waitcnt lgkmcnt(4)" : "+v"(av[j % 5]));
      else if (j == 5) asm volatile("s_waitcnt lgkmcnt(2)" : "+v"(av[0]), "+v"(av[1]));
      else if (j == 7) asm volatile("s_waitcnt lgkmcnt(0)" : "+v"(av[2]), "+v"(av[3]));
      lo = MMB(av[j % 5], bw[2 * j], lo);
      hi = MMB(av[j % 5], bw[2 * j + 1], hi);
      __builtin_amdgcn_sched_barrier(0);
      if (j < 4) asm volatile("ds_read_b128 %0, %1 offset:%2" : "=v"(av[j]) : "v"(aa), "n"(1024 * (j + 5)) : "memory");
    }
  };
  uint32_t fl;
  asm volatile("ds_read_b32 %0, %1" : "=v"(fl) : "v"(ca + 20) : "memory");
  if (ROLE == 1) gate_up();
  asm volatile("s_waitcnt lgkmcnt(0)" : "+v"(fl));
  const bool slow = FORCESLOW || FORCEFB || __builtin_amdgcn_readfirstlane(fl) != 0;
  if (__builtin_expect(slow, 0)) {
    TSC(17);
    pick(IC<256>{}, IC<64>{});
    mykey = sel[16 + (lane & 7)];
    ro = ((mykey >> 3) & 0x3ffu) * 1152u;
    if (ROLE == 1 && (k3rd > (uint32_t)__builtin_amdgcn_readlane(mykey, 7) || (mykey & 24u) == 8u)) cnt[11] = 1u;
    BAR();
    if (FORCESLOW > 1 || FORCEFB || __builtin_amdgcn_readfirstlane(cnt[11]) != 0) {
    u32x4 key;
#pragma unroll 1
    for (int spin = 0; spin < SPIN_CAP; ++spin) {
      LD16W(key, O_KF + 16u * tid);
      if (!__any(key[1] != T || key[3] != T)) break;
      SLEEP(KBACK);
    }
    const uint32_t ib = (uint32_t)(((255 - (tid >> 1)) << 5) | (7 - 4 * (tid & 1)));
    uint32_t k0 = (key[0] << 16) | ib, k1 = (key[0] & 0xffff0000u) | (ib - 1), k2 = (key[2] << 16) | (ib - 2), k3 = (key[2] & 0xffff0000u) | (ib - 3);
    CEX(k0, k1) CEX(k2, k3) CEX(k0, k2) CEX(k1, k3) CEX(k1, k2)
#pragma unroll 1
    for (int c = 0; c < C_; ++c) {
      const uint32_t mx = wave_maxu(k0);
      const bool hit = (k0 == mx);
      k0 = hit ? k1 : k0; k1 = hit ? k2 : k1; k2 = hit ? k3 : k2; k3 = hit ? 0u : k3;
      if (lane == 0) cand[64 + 8 * w + c] = mx;
    }
    pick(IC<256>{}, IC<128>{});
    mykey = sel[32 + (lane & 7)];
    const uint32_t okey = sel[32 + (lane >> 3)];
    const uint64_t same = __ballot(((mykey ^ okey) & 0x1fe0u) == 0u && okey > mykey);
    const int sl = FORCEFB + __popcll((same >> (lane & 7)) & 0x0101010101010101ull);
    ro = (sl < 2) ? (((mykey >> 3) & 0x3ffu) + 3 - sl) * 1152u : O_FB - O_XC + 1152u * (lane & 7);
    }
    if (ln2) rest_out();
    if (ffn) {
      if (ROLE == 1) rows(ro + al, [] {});
      BAR();
      if (ROLE == 1) gate_up();
    }
  }
  if (OPOS == 0 && ROLE == 2 && ffn && !slow) { SLEEP(OD); rest_out(); }
  if (!ffn) {
    if (ln2 && !slow) { SLEEP(OD2); rest_out(); }
    TSE();
    return;
  }
  uint32_t fx = 0, fv = 0, fn = 0, fo = 0, sa[9];
  float rs, hx, xs;
  u32x4 se;
  if constexpr (ROLE == 1) {
    f32x4 lh = (lane < 32) ? lo : hi;
    float fa = fold32(lh[0], lh[2]), fb2 = fold32(lh[1], lh[3]);
    if (__builtin_expect(__any(__builtin_isunordered(fa, fb2)) && !slow, 0)) {
      gsync0(ca + 4, 12);
      rows(ro + al, [] {});
      gsync0(ca + 4, 16);
      gate_up();
      lh = (lane < 32) ? lo : hi;
      fa = fold32(lh[0], lh[2]); fb2 = fold32(lh[1], lh[3]);
    }
    const float g = __int_as_float(__builtin_amdgcn_update_dpp(__float_as_int(fa), __float_as_int(fb2), 0x128, 0xf, 0xc, false));
    const float u = __int_as_float(__builtin_amdgcn_update_dpp(__float_as_int(fb2), __float_as_int(fa), 0x128, 0xf, 0x3, false));
    const uint32_t gu = pack2(g, u);
    const float gp = bflo(gu);
    const float sg = rbf(gp * __builtin_amdgcn_rcpf(1.f + __expf(-gp)));
    *(u16*)((char*)hm + ha) = f2bf(sg * bfhi(gu));
  } else if (LPOS) {
    DRAIN();
    gsync0(ca, 12);
    get_bd(0, 8);
  }
  BAR();
  TS(0, 6);
  if (ROLE == 2 || w == 2) {
    const u32x4 a0 = *(const u32x4*)((char*)hm + h0a), a1 = *(const u32x4*)((char*)hm + h1a);
    if (ROLE == 1) get_bd(0, 4);
    const f32x4 z = {0.f, 0.f, 0.f, 0.f};
    f32x4 o[4];
#pragma unroll
    for (int t = 0; t < 2 * ROLE; ++t) o[t] = MMB(a1, bw[2 * t + 1], MMB(a0, bw[2 * t], z));
#pragma unroll
    for (int t = 0; t < ROLE; ++t) {
      u32x4 sv;
#pragma unroll
      for (int r = 0; r < 4; ++r) sv[r] = min(pack2(o[2 * t][r], o[2 * t + 1][r]), 0xfffffffeu);
      ST16(so + 4096u * t, sv);
    }
    TS(0, 13);
  }
  if (OPOS == 1 && ROLE == 2) {
    SLEEP(OSLEEP);
    if (!slow) rest_out();
  }
  if (ROLE == 1 && w == 1) {
    const int q = (fb & 15) >> 2, ts = 4 * (q & 1) + ((i & 7) >> 1);
    const int dd = 64 * (fb >> 4) + 16 * (2 * (i & 1) + (q >> 1)) + 4 * (fb & 3) + 2 * (kq & 1) + (kq >> 1);
    const uint32_t fkey = (uint32_t)__builtin_amdgcn_ds_bpermute(4 * ts, (int)mykey), fr = (uint32_t)__builtin_amdgcn_ds_bpermute(4 * ts, (int)ro);
    const int ft = 2047 - (int)(((fkey >> 2) & 0x7f8u) | (fkey & 7u));
    fo = (uint32_t)(ft * D_ + dd) * 2u;
    u32x2 fs;
    asm volatile("global_load_ushort %0, %4, %8\n\tglobal_load_ushort %1, %5, %9 sc1\n\tglobal_load_ushort %2, %6, %9 sc1\n\tglobal_load_dwordx2 %3, %7, %9 sc1"
                 : "=&v"(fx), "=&v"(fv), "=&v"(fn), "=&v"(fs) : "v"(fo), "v"(v_at(dd, ft >> 3) + 2u * (ft & 7)), "v"(O_XC + fr + 2u * dd), "v"(O_SC + 16u * dd), "s"(x), "s"(ws) : "memory");
    uint32_t t16 = (((T >> (3 * ((ft >> 1) & 3))) & LMASK) | (((T >> (12 + ((ft >> 1) & 3))) & 0x00010001u) << 14)) >> (16 * (ft & 1)) & 0xffffu;
    const uint32_t o = fkey >> 16;
    const float rs0 = bf2f((u16)((o & 0x8000u) ? (o & 0x7fffu) : (~o & 0xffffu)));
    rs = rbf(__builtin_amdgcn_rcpf(1.f + __expf(-rs0)));
    const uint32_t one = (i < 8) ? (0x3f80u << (16 * (i & 1))) : 0u;
    se = (u32x4){(i >> 1) == 0 ? one : 0u, (i >> 1) == 1 ? one : 0u, (i >> 1) == 2 ? one : 0u, (i >> 1) == 3 ? one : 0u};
#pragma unroll
    for (int m = 0; m < 9; ++m) sa[m] = O_SL + (uint32_t)(16 * m + (lane >> 2)) * SP + (uint32_t)(4 * fb + (lane & 3)) * 16u;
    asm volatile("" : "+v"(t16), "+v"(rs), "+v"(se), "+v"(sa[0]), "+v"(sa[1]), "+v"(sa[2]), "+v"(sa[3]), "+v"(sa[4]), "+v"(sa[5]), "+v"(sa[6]), "+v"(sa[7]), "+v"(sa[8]));
    SLEEP(SSLEEP);
    TS(0, 1);
    float sum;
#pragma unroll 1
    for (int spin = 0; spin < SPIN_CAP; ++spin) {
      u32x4 sv[9];
#pragma unroll
      for (int m = 0; m < 9; ++m) LD16(sv[m], sa[m]);
      asm volatile("s_waitcnt vmcnt(9)" : "+v"(fx), "+v"(fv), "+v"(fn), "+v"(fs));
      if (__builtin_expect(__any(fs[0] != T), 0)) {
        for (int k = 0; k < SPIN_CAP && fs[0] != T; ++k)
          asm volatile("global_load_dwordx2 %0, %1, %2 sc1\n\ts_waitcnt vmcnt(0)" : "=&v"(fs) : "v"(O_SC + 16u * dd), "s"(ws) : "memory");
      }
      const uint32_t f1 = fv ^ t16, f2 = (f1 & 7u) ? fv & ~7u : f1;
      hx = rbf(bf2f((u16)fx) + (float)__builtin_bit_cast(_Float16, (u16)f2) * __uint_as_float(fs[1]));
      xs = bf2f((u16)fn);
      asm volatile("" : "+v"(hx), "+v"(xs));
      f32x4 dv = {0.f, 0.f, 0.f, 0.f};
      static_for<0, 9>([&](auto mm) {
        constexpr int m = mm.value;
        asm volatile("s_waitcnt vmcnt(%1)" : "+v"(sv[m]) : "n"(8 - m));
        dv = MMB(sv[m], se, dv);
        __builtin_amdgcn_sched_barrier(0);
      });
      sum = fold16(fold32(dv[0], dv[1]), fold32(dv[2], dv[3]));
      if (__builtin_expect(!__any(sum != sum), 1)) break;
      uint32_t mx = 0u;
#pragma unroll
      for (int m = 0; m < 9; ++m) mx = max(mx, max4(sv[m]));
      if (!__any(mx == 0xffffffffu)) break;
      TSC(18);
      SLEEP(PSLEEP);
    }
    const float o1 = rbf(xs + rbf(rbf(sum) * rs));
    if (i < 8) asm volatile("global_store_short %0, %1, %2 sc1" : : "v"(fo), "v"((uint32_t)f2bf(hx + o1)), "s"(out) : "memory");
    TS(0, 2);
  }
  TSE();
  }
  }
}
#define BROT "192"
#define CPF_A "s_getpc_b64 s[96:97]\n\ts_lshl_b32 s99, s2, 4\n\ts_lshl_b32 s84, s98, 7\n\ts_add_u32 s99, s99, s84\n\tv_and_b32 v112, 1, v0\n\t" \
              "v_lshl_add_u32 v112, v112, 12, s99\n\tv_min_u32 v112, kf_end-kf_all-0x80, v112\n\ts_mov_b64 exec, 3\n\tglobal_load_dword v112, v112, s[96:97]\n\ts_mov_b64 exec, -1\n\t"
extern "C" __global__ __launch_bounds__(512, 4) void kf_all(KARGS) {
  __shared__ __attribute__((aligned(16))) char smem[SMEM];
  asm volatile("s_load_dwordx16 s[64:79], s[0:1], 0x0\n\ts_load_dwordx4 s[80:83], s[0:1], 0x40\n\tv_readfirstlane_b32 s98, v0\n\t"
               "s_add_u32 s84, s2, " BROT "\n\ts_xor_b32 s84, s84, s2\n\ts_bitset0_b32 s84, 8\n\ts_bitset0_b32 s84, 9\n\ts_xor_b32 s2, s2, s84\n\t"
               "v_mov_b32 v109, 0\n\ts_and_b32 s85, s2, 7\n\ts_bitcmp1_b32 s2, 8\n\ts_cbranch_scc1 1f\n\t" CPF_A "s_bitcmp1_b32 s98, 8\n\ts_cbranch_scc1 2f\n\t"
               "s_andn2_b32 s84, s2, 31\n\ts_lshl2_add_u32 s84, s85, s84\n\ts_bfe_u32 s85, s2, 0x20003\n\ts_or_b32 s84, s84, s85\n\t"
               "s_mulk_i32 s84, 0x2400\n\ts_mulk_i32 s98, 36\n\ts_add_u32 s84, s84, s98\n\tv_and_b32 v110, 63, v0\n\tv_and_b32 v111, 32, v0\n\t"
               "v_lshlrev_b32 v111, 5, v111\n\tv_lshl_add_u32 v111, v110, 2, v111\n\tv_lshl_add_u32 v110, v110, 4, s84\n\tv_add_u32 v111, s84, v111\n\t"
               "s_cmpk_lt_u32 s2, 0x90\n\ts_waitcnt lgkmcnt(0)\n\t"
               "global_load_dwordx4 v[100:103], v110, s[64:65]\n\tglobal_load_dwordx4 v[104:107], v110, s[64:65] offset:1152\n\t"
               "global_load_dword v108, v111, s[64:65] offset:1024\n\tglobal_load_dword v109, v109, s[82:83] sc1\n\t"
               "s_cbranch_scc1 kf_n\n\ts_branch kf_h\n"
               "1:\n\ts_bfe_u32 s84, s2, 0x50003\n\ts_lshl_b32 s85, s85, 5\n\ts_or_b32 s84, s84, s85\n\ts_lshl_b32 s84, s84, 2\n\t"
               "v_and_b32 v110, 0xff, v0\n\tv_bfe_u32 v107, v0, 7, 1\n"
               "4:\n\ts_lshl_b32 s85, s84, 7\n\tv_lshl_add_u32 v110, v110, 1, s85\n\tv_lshl_add_u32 v107, v107, 1, s84\n\ts_and_b32 s85, s98, s2\n\ts_waitcnt lgkmcnt(0)\n\t"
               "global_load_ushort v100, v107, s[78:79]\n\tglobal_load_ushort v101, v110, s[66:67]\n\tglobal_load_ushort v102, v110, s[68:69]\n\t"
               "global_load_dword v109, v109, s[82:83] sc1\n\ts_bitcmp1_b32 s85, 8\n\ts_cbranch_scc1 kf_u\n\t"
               "global_load_ushort v103, v110, s[70:71]\n\tglobal_load_ushort v104, v110, s[72:73]\n\tglobal_load_ushort v105, v110, s[74:75]\n\t"
               "global_load_ushort v106, v110, s[76:77]\n\tglobal_load_ushort v107, v107, s[80:81]\n\ts_bitcmp1_b32 s2, 8\n\ts_cbranch_scc1 kf_c\n"
               "6:\n\ts_barrier\n\ts_branch kf_p\n"
               "2:\n\ts_cmpk_lt_u32 s2, 0xc0\n\ts_cbranch_scc1 6b\n\t"
               "s_lshr_b32 s84, s2, 3\n\ts_lshl3_add_u32 s84, s85, s84\n\ts_lshl1_add_u32 s84, s84, 976\n\t"
               "v_and_b32 v110, 0x7f, v0\n\tv_mov_b32 v107, 0\n\ts_branch 4b"
               : : "s"(__builtin_amdgcn_kernarg_segment_ptr()), "s"(blockIdx.x), "v"(threadIdx.x),
                   "s"((uint32_t)(uintptr_t)(__attribute__((address_space(3))) char*)smem) : "scc", "s96", "s97", "s98", "s99", "v127");
}
extern "C" __global__ __launch_bounds__(512, 4) void kf_c(KARGS) {
  __shared__ __attribute__((aligned(16))) char smem[SMEM];
  role_body<0>(smem, KPASS);
}
extern "C" __global__ __launch_bounds__(512, 4) void kf_u(KARGS) {
  __shared__ __attribute__((aligned(16))) char smem[SMEM];
  role_body<3>(smem, KPASS);
}
extern "C" __global__ __launch_bounds__(512, 4) void kf_n(KARGS) {
  __shared__ __attribute__((aligned(16))) char smem[SMEM];
  role_body<1, 1>(smem, KPASS);
}
extern "C" __global__ __launch_bounds__(512, 4) void kf_h(KARGS) {
  __shared__ __attribute__((aligned(16))) char smem[SMEM];
  role_body<1, 0>(smem, KPASS);
}
extern "C" __global__ __launch_bounds__(512, 4) void kf_p(KARGS) {
  __shared__ __attribute__((aligned(16))) char smem[SMEM];
  role_body<2>(smem, KPASS);
}
extern "C" __global__ __launch_bounds__(512, 4) void kf_conv() { asm volatile(CONV_ASM ::: CONV_CLOB); }
extern "C" __global__ void kf_end() {}

extern "C" void kernel_launch(void* const* d_in, const int* in_sizes, int n_in,
                              void* d_out, int out_size, void* d_ws, size_t ws_size,
                              hipStream_t stream) {
  kf_all<<<512, 512, 0, stream>>>((const u16*)d_in[0], (const u16*)d_in[1], (const u16*)d_in[2], (const u16*)d_in[3], (const u16*)d_in[4],
                                  (const u16*)d_in[5], (const u16*)d_in[6], (const u16*)d_in[7], (const u16*)d_in[8], (char*)d_ws + (size_t)WSOFF * 1048576,
                                  (const u16*)d_in[10], (const u16*)d_in[11], (const u16*)d_in[12], (const u16*)d_in[13], (u16*)d_out);
}
```

```cpp
#include <hip/hip_runtime.h>
#include <hip/hip_bf16.h>
#include <stdint.h>
#define LPOS 1
#define LSLEEP 1
#define VD2 390
#define V2D 22
#define KD 178

typedef __attribute__((ext_vector_type(8))) short bf16x8;
typedef __attribute__((ext_vector_type(4))) float f32x4;
typedef uint16_t u16;
typedef __attribute__((ext_vector_type(4))) uint32_t u32x4;
typedef __attribute__((ext_vector_type(2))) uint32_t u32x2;
#define TSD uint32_t pr0_ = 0u, pr1_ = 0u
__device__ unsigned long long g_prb[4096];
#define TS(k, i) do { if ((i) == 0) pr0_ = (uint32_t)__builtin_amdgcn_s_memrealtime() | 1u; \
    if (ROLE == 1 && (i) == 1 && (2 < 0 || w == 2) && true) pr1_ = (uint32_t)__builtin_amdgcn_s_memrealtime() | 1u; } while (0)
#define TSC(i)
#define TSE() do { if (__builtin_amdgcn_mbcnt_hi(-1, __builtin_amdgcn_mbcnt_lo(-1, 0)) == 0) \
    __hip_atomic_store(&g_prb[blockIdx.x * 8 + w], ((unsigned long long)pr1_ << 32) | pr0_, __ATOMIC_RELAXED, __HIP_MEMORY_SCOPE_AGENT); } while (0)
#define TSP
#define TSA

constexpr int L_ = 2048, D_ = 576, N_ = 128, F_ = 4608, C_ = 8;
constexpr int NF = 144;
constexpr uint32_t O_EP = 0;
constexpr uint32_t O_KF = 4096;
constexpr uint32_t O_KEY = O_KF + 8192;
constexpr uint32_t O_SC = O_KEY + 32768;
constexpr uint32_t O_DN = O_SC + 9216;
constexpr uint32_t O_SU = O_DN + 20480;
constexpr uint32_t O_FB = O_SU + 262144;
constexpr uint32_t O_XC = O_FB + 8 * 1152;
constexpr uint32_t SP = 9216 + 128;
constexpr uint32_t O_SL = O_XC + 1024 * 1152;
constexpr uint32_t SLU = 336;
constexpr uint32_t O_U = O_SL + 256 * SLU * 16;
constexpr int UP = 584;
constexpr uint32_t VP = 8192 + 128, VPP = 4096 + 128;
constexpr uint32_t O_V = O_U + 256 * UP * 16, O_VP = O_V + 256 * VP;
static_assert(256 * SLU * 16 >= NF * SP && O_SL % 128 == 0 && O_U % 128 == 0 && O_V % 128 == 0, "");
__host__ __device__ __forceinline__ uint32_t u_at(int tg, int d) { return O_U + 16u * (uint32_t)((tg >> 1) * (UP * 2) + (d >> 2) * 8 + (tg & 1) * 4 + (d & 3)); }
__host__ __device__ __forceinline__ uint32_t v_at(int d, int tg) {
  return (d < 512) ? O_V + (uint32_t)(d >> 1) * VP + 16u * (uint32_t)(2 * tg + (d & 1)) : O_VP + (uint32_t)(d - 512) * VPP + 16u * (uint32_t)tg;
}
#define HAVE_VAT
#define HAVE_VTAG
__device__ __forceinline__ float bf2f(u16 v) { return __uint_as_float((uint32_t)v << 16); }
__device__ __forceinline__ float bflo(uint32_t v) { return __uint_as_float(v << 16); }
__device__ __forceinline__ float bfhi(uint32_t v) { return __uint_as_float(v & 0xffff0000u); }
__device__ __forceinline__ u16 f2bf(float f) { return __builtin_bit_cast(u16, (__bf16)f); }
__device__ __forceinline__ float rbf(float f) { return bf2f(f2bf(f)); }
typedef __attribute__((ext_vector_type(2))) float f32x2;
typedef __attribute__((ext_vector_type(2))) __bf16 bf16x2v;
__device__ __forceinline__ uint32_t pack2(float a, float b) {
  return __builtin_bit_cast(uint32_t, __builtin_convertvector((f32x2){a, b}, bf16x2v));
}

#define DPP_I(v, ctrl, rmask) __builtin_amdgcn_update_dpp(0, (v), (ctrl), (rmask), 0xf, true)
__device__ __forceinline__ float row_sum(float v) {
  v += __int_as_float(DPP_I(__float_as_int(v), 0x111, 0xf));
  v += __int_as_float(DPP_I(__float_as_int(v), 0x112, 0xf));
  v += __int_as_float(DPP_I(__float_as_int(v), 0x114, 0xf));
  v += __int_as_float(DPP_I(__float_as_int(v), 0x118, 0xf));
  return v;
}
__device__ __forceinline__ float rdl(float v, int l) { return __int_as_float(__builtin_amdgcn_readlane(__float_as_int(v), l)); }
__device__ __forceinline__ float wave_sum(float v) {
  v = row_sum(v);
  asm("s_nop 1\n\tv_add_f32_dpp %0, %0, %0 row_bcast:15 row_mask:0xa bank_mask:0xf\n\t"
      "s_nop 1\n\tv_add_f32_dpp %0, %0, %0 row_bcast:31 row_mask:0xc bank_mask:0xf\n\ts_nop 0" : "+v"(v));
  return rdl(v, 63);
}
__device__ __forceinline__ float fold32(float a, float b) {
  const auto r = __builtin_amdgcn_permlane32_swap(__float_as_uint(a), __float_as_uint(b), false, false);
  return __uint_as_float(r[0]) + __uint_as_float(r[1]);
}
__device__ __forceinline__ float fold16(float a, float b) {
  const auto r = __builtin_amdgcn_permlane16_swap(__float_as_uint(a), __float_as_uint(b), false, false);
  return __uint_as_float(r[0]) + __uint_as_float(r[1]);
}
__device__ __forceinline__ void wave_sum2(float& a, float& b) {
  float v = row_sum(fold32(a, b));
  asm("s_nop 1\n\tv_add_f32_dpp %0, %0, %0 row_bcast:15 row_mask:0xa bank_mask:0xf\n\ts_nop 0" : "+v"(v));
  a = rdl(v, 31); b = rdl(v, 63);
}
__device__ __forceinline__ float dot2(uint32_t a, uint32_t b, float c) {
  return __builtin_amdgcn_fdot2_f32_bf16(__builtin_bit_cast(bf16x2v, a), __builtin_bit_cast(bf16x2v, b), c, false);
}
__device__ __forceinline__ uint32_t row_maxu(uint32_t v) {
  v = max(v, (uint32_t)DPP_I((int)v, 0xb1, 0xf));
  v = max(v, (uint32_t)DPP_I((int)v, 0x4e, 0xf));
  v = max(v, (uint32_t)DPP_I((int)v, 0x141, 0xf));
  v = max(v, (uint32_t)DPP_I((int)v, 0x140, 0xf));
  return v;
}
__device__ __forceinline__ uint32_t wave_maxu(uint32_t v) {
  v = max(v, (uint32_t)DPP_I((int)v, 0x111, 0xf));
  v = max(v, (uint32_t)DPP_I((int)v, 0x112, 0xf));
  v = max(v, (uint32_t)DPP_I((int)v, 0x114, 0xf));
  v = max(v, (uint32_t)DPP_I((int)v, 0x118, 0xf));
  v = max(v, (uint32_t)DPP_I((int)v, 0x142, 0xa));
  v = max(v, (uint32_t)DPP_I((int)v, 0x143, 0xc));
  return (uint32_t)__builtin_amdgcn_readlane((int)v, 63);
}

typedef __attribute__((ext_vector_type(8))) _Float16 f16x8;
typedef __attribute__((ext_vector_type(2))) _Float16 f16x2;
__device__ __forceinline__ uint32_t packh(f32x2 v) { return __builtin_bit_cast(uint32_t, __builtin_convertvector(v, f16x2)); }
__device__ __forceinline__ uint32_t packh(float a, float b) { return packh((f32x2){a, b}); }
constexpr int IP = 224, I_X = 128;
__device__ __forceinline__ f32x2 cmul(f32x2 z, f32x2 w) {
  f32x2 r;
  asm("v_pk_mul_f32 %0, %1, %2 op_sel_hi:[0,1]\n\tv_pk_fma_f32 %0, %1, %2, %0 op_sel:[1,1,0] op_sel_hi:[1,0,1] neg_lo:[0,1,0]" : "=&v"(r) : "v"(z), "v"(w));
  return r;
}
__device__ __forceinline__ f32x2 cexp2(float ar2, float air, float l) {
  const float t = air * l;
  return (f32x2){__builtin_amdgcn_cosf(t), __builtin_amdgcn_sinf(t)} * __builtin_amdgcn_exp2f(ar2 * l);
}
__device__ __forceinline__ float state_a(float ldt, float are, float aim, float& ar2, float& air, float& g) {
  const float dt = rbf(__expf(ldt));
  ar2 = dt * are * 1.4426950408889634f;
  air = dt * aim * 0.15915494309189535f;
  air -= __builtin_rintf(air);
  g = 0.f;
  if (__builtin_expect(__any(ar2 > 0.f), 0)) g = fminf(__uint_as_float(wave_maxu(__float_as_uint(fmaxf(ar2, 0.f)))), 1.f);
  return dt;
}
template <int N> __device__ __forceinline__ void put_im(char* p, int im, const f32x2* z) {
  *(u32x4*)p = (u32x4){packh(z[0][0], z[1][0]), packh(z[2][0], z[3][0]), packh(z[4][0], z[5][0]), packh(z[6][0], z[7][0])};
  *(u32x4*)(p + im) = (u32x4){packh(z[0][1], z[1][1]), packh(z[2][1], z[3][1]), packh(z[4][1], z[5][1]), packh(z[6][1], z[7][1])};
}
#define Y_ASM \
  "v_mov_b64 v[40:41], %0\nv_pk_mul_f32 v[42:43], v[40:41], %1 op_sel_hi:[0,1]\nv_pk_mul_f32 v[44:45], %0, %2 op_sel_hi:[0,1]\n" \
  "v_pk_fma_f32 v[42:43], v[40:41], %1, v[42:43] op_sel:[1,1,0] op_sel_hi:[1,0,1] neg_lo:[0,1,0]\n" \
  "v_pk_fma_f32 v[44:45], %0, %2, v[44:45] op_sel:[1,1,0] op_sel_hi:[1,0,1] neg_lo:[0,1,0]\nv_cvt_pk_f16_f32 v56, v40, v42\n" \
  "v_cvt_pk_f16_f32 v60, v41, v43\nv_pk_mul_f32 v[40:41], v[42:43], %1 op_sel_hi:[0,1]\nv_pk_mul_f32 v[48:49], v[44:45], %2 op_sel_hi:[0,1]\n" \
  "v_pk_fma_f32 v[40:41], v[42:43], %1, v[40:41] op_sel:[1,1,0] op_sel_hi:[1,0,1] neg_lo:[0,1,0]\n" \
  "v_pk_fma_f32 v[48:49], v[44:45], %2, v[48:49] op_sel:[1,1,0] op_sel_hi:[1,0,1] neg_lo:[0,1,0]\n" \
  "v_pk_mul_f32 v[42:43], v[40:41], %1 op_sel_hi:[0,1]\nv_pk_mul_f32 v[52:53], v[48:49], %2 op_sel_hi:[0,1]\n" \
  "v_pk_fma_f32 v[42:43], v[40:41], %1, v[42:43] op_sel:[1,1,0] op_sel_hi:[1,0,1] neg_lo:[0,1,0]\n" \
  "v_pk_fma_f32 v[52:53], v[48:49], %2, v[52:53] op_sel:[1,1,0] op_sel_hi:[1,0,1] neg_lo:[0,1,0]\nv_cvt_pk_f16_f32 v57, v40, v42\n" \
  "v_cvt_pk_f16_f32 v61, v41, v43\nv_pk_mul_f32 v[40:41], v[42:43], %1 op_sel_hi:[0,1]\n" \
  "v_pk_fma_f32 v[40:41], v[42:43], %1, v[40:41] op_sel:[1,1,0] op_sel_hi:[1,0,1] neg_lo:[0,1,0]\n" \
  "v_pk_mul_f32 v[42:43], v[40:41], %1 op_sel_hi:[0,1]\nv_pk_mul_f32 v[46:47], v[44:45], %1 op_sel_hi:[0,1]\n" \
  "v_pk_fma_f32 v[42:43], v[40:41], %1, v[42:43] op_sel:[1,1,0] op_sel_hi:[1,0,1] neg_lo:[0,1,0]\n" \
  "v_pk_fma_f32 v[46:47], v[44:45], %1, v[46:47] op_sel:[1,1,0] op_sel_hi:[1,0,1] neg_lo:[0,1,0]\nv_cvt_pk_f16_f32 v58, v40, v42\n" \
  "v_cvt_pk_f16_f32 v62, v41, v43\nv_cvt_pk_f16_f32 v64, v44, v46\nv_cvt_pk_f16_f32 v68, v45, v47\n" \
  "v_pk_mul_f32 v[40:41], v[42:43], %1 op_sel_hi:[0,1]\nv_pk_mul_f32 v[44:45], v[46:47], %1 op_sel_hi:[0,1]\n" \
  "v_pk_fma_f32 v[40:41], v[42:43], %1, v[40:41] op_sel:[1,1,0] op_sel_hi:[1,0,1] neg_lo:[0,1,0]\n" \
  "v_pk_fma_f32 v[44:45], v[46:47], %1, v[44:45] op_sel:[1,1,0] op_sel_hi:[1,0,1] neg_lo:[0,1,0]\n" \
  "v_pk_mul_f32 v[42:43], v[40:41], %1 op_sel_hi:[0,1]\nv_pk_mul_f32 v[46:47], v[44:45], %1 op_sel_hi:[0,1]\n" \
  "v_pk_fma_f32 v[42:43], v[40:41], %1, v[42:43] op_sel:[1,1,0] op_sel_hi:[1,0,1] neg_lo:[0,1,0]\n" \
  "v_pk_fma_f32 v[46:47], v[44:45], %1, v[46:47] op_sel:[1,1,0] op_sel_hi:[1,0,1] neg_lo:[0,1,0]\nv_cvt_pk_f16_f32 v59, v40, v42\n" \
  "v_cvt_pk_f16_f32 v63, v41, v43\nv_cvt_pk_f16_f32 v65, v44, v46\nv_cvt_pk_f16_f32 v69, v45, v47\n" \
  "v_pk_mul_f32 v[44:45], v[46:47], %1 op_sel_hi:[0,1]\nds_write_b128 %3, v[56:59] offset:0\n" \
  "v_pk_fma_f32 v[44:45], v[46:47], %1, v[44:45] op_sel:[1,1,0] op_sel_hi:[1,0,1] neg_lo:[0,1,0]\nds_write_b128 %3, v[60:63] offset:64\n" \
  "v_pk_mul_f32 v[46:47], v[44:45], %1 op_sel_hi:[0,1]\nv_pk_mul_f32 v[50:51], v[48:49], %1 op_sel_hi:[0,1]\n" \
  "v_pk_fma_f32 v[46:47], v[44:45], %1, v[46:47] op_sel:[1,1,0] op_sel_hi:[1,0,1] neg_lo:[0,1,0]\n" \
  "v_pk_fma_f32 v[50:51], v[48:49], %1, v[50:51] op_sel:[1,1,0] op_sel_hi:[1,0,1] neg_lo:[0,1,0]\nv_cvt_pk_f16_f32 v66, v44, v46\n" \
  "v_cvt_pk_f16_f32 v70, v45, v47\nv_cvt_pk_f16_f32 v56, v48, v50\nv_cvt_pk_f16_f32 v60, v49, v51\n" \
  "v_pk_mul_f32 v[44:45], v[46:47], %1 op_sel_hi:[0,1]\nv_pk_mul_f32 v[48:49], v[50:51], %1 op_sel_hi:[0,1]\n" \
  "v_pk_fma_f32 v[44:45], v[46:47], %1, v[44:45] op_sel:[1,1,0] op_sel_hi:[1,0,1] neg_lo:[0,1,0]\n" \
  "v_pk_fma_f32 v[48:49], v[50:51], %1, v[48:49] op_sel:[1,1,0] op_sel_hi:[1,0,1] neg_lo:[0,1,0]\n" \
  "v_pk_mul_f32 v[46:47], v[44:45], %1 op_sel_hi:[0,1]\nv_pk_mul_f32 v[50:51], v[48:49], %1 op_sel_hi:[0,1]\n" \
  "v_pk_fma_f32 v[46:47], v[44:45], %1, v[46:47] op_sel:[1,1,0] op_sel_hi:[1,0,1] neg_lo:[0,1,0]\n" \
  "v_pk_fma_f32 v[50:51], v[48:49], %1, v[50:51] op_sel:[1,1,0] op_sel_hi:[1,0,1] neg_lo:[0,1,0]\nv_cvt_pk_f16_f32 v67, v44, v46\n" \
  "v_cvt_pk_f16_f32 v71, v45, v47\nv_cvt_pk_f16_f32 v57, v48, v50\nv_cvt_pk_f16_f32 v61, v49, v51\n" \
  "v_pk_mul_f32 v[48:49], v[50:51], %1 op_sel_hi:[0,1]\nds_write_b128 %3, v[64:67] offset:16\n" \
  "v_pk_fma_f32 v[48:49], v[50:51], %1, v[48:49] op_sel:[1,1,0] op_sel_hi:[1,0,1] neg_lo:[0,1,0]\nds_write_b128 %3, v[68:71] offset:80\n" \
  "v_pk_mul_f32 v[50:51], v[48:49], %1 op_sel_hi:[0,1]\nv_pk_mul_f32 v[54:55], v[52:53], %1 op_sel_hi:[0,1]\n" \
  "v_pk_fma_f32 v[50:51], v[48:49], %1, v[50:51] op_sel:[1,1,0] op_sel_hi:[1,0,1] neg_lo:[0,1,0]\n" \
  "v_pk_fma_f32 v[54:55], v[52:53], %1, v[54:55] op_sel:[1,1,0] op_sel_hi:[1,0,1] neg_lo:[0,1,0]\nv_cvt_pk_f16_f32 v58, v48, v50\n" \
  "v_cvt_pk_f16_f32 v62, v49, v51\nv_cvt_pk_f16_f32 v64, v52, v54\nv_cvt_pk_f16_f32 v68, v53, v55\n" \
  "v_pk_mul_f32 v[48:49], v[50:51], %1 op_sel_hi:[0,1]\nv_pk_mul_f32 v[52:53], v[54:55], %1 op_sel_hi:[0,1]\n" \
  "v_pk_fma_f32 v[48:49], v[50:51], %1, v[48:49] op_sel:[1,1,0] op_sel_hi:[1,0,1] neg_lo:[0,1,0]\n" \
  "v_pk_fma_f32 v[52:53], v[54:55], %1, v[52:53] op_sel:[1,1,0] op_sel_hi:[1,0,1] neg_lo:[0,1,0]\n" \
  "v_pk_mul_f32 v[50:51], v[48:49], %1 op_sel_hi:[0,1]\nv_pk_mul_f32 v[54:55], v[52:53], %1 op_sel_hi:[0,1]\n" \
  "v_pk_fma_f32 v[50:51], v[48:49], %1, v[50:51] op_sel:[1,1,0] op_sel_hi:[1,0,1] neg_lo:[0,1,0]\n" \
  "v_pk_fma_f32 v[54:55], v[52:53], %1, v[54:55] op_sel:[1,1,0] op_sel_hi:[1,0,1] neg_lo:[0,1,0]\nv_cvt_pk_f16_f32 v59, v48, v50\n" \
  "v_cvt_pk_f16_f32 v63, v49, v51\nv_cvt_pk_f16_f32 v65, v52, v54\nv_cvt_pk_f16_f32 v69, v53, v55\n" \
  "v_pk_mul_f32 v[52:53], v[54:55], %1 op_sel_hi:[0,1]\nds_write_b128 %3, v[56:59] offset:32\n" \
  "v_pk_fma_f32 v[52:53], v[54:55], %1, v[52:53] op_sel:[1,1,0] op_sel_hi:[1,0,1] neg_lo:[0,1,0]\nds_write_b128 %3, v[60:63] offset:96\n" \
  "v_pk_mul_f32 v[54:55], v[52:53], %1 op_sel_hi:[0,1]\n" \
  "v_pk_fma_f32 v[54:55], v[52:53], %1, v[54:55] op_sel:[1,1,0] op_sel_hi:[1,0,1] neg_lo:[0,1,0]\nv_cvt_pk_f16_f32 v66, v52, v54\n" \
  "v_cvt_pk_f16_f32 v70, v53, v55\nv_pk_mul_f32 v[52:53], v[54:55], %1 op_sel_hi:[0,1]\n" \
  "v_pk_fma_f32 v[52:53], v[54:55], %1, v[52:53] op_sel:[1,1,0] op_sel_hi:[1,0,1] neg_lo:[0,1,0]\n" \
  "v_pk_mul_f32 v[54:55], v[52:53], %1 op_sel_hi:[0,1]\n" \
  "v_pk_fma_f32 v[54:55], v[52:53], %1, v[54:55] op_sel:[1,1,0] op_sel_hi:[1,0,1] neg_lo:[0,1,0]\nv_cvt_pk_f16_f32 v67, v52, v54\n" \
  "v_cvt_pk_f16_f32 v71, v53, v55\nds_write_b128 %3, v[64:67] offset:48\nds_write_b128 %3, v[68:71] offset:112\n"
#define Y_CLOB "memory", "v40", "v41", "v42", "v43", "v44", "v45", "v46", "v47", "v48", "v49", "v50", "v51", "v52", "v53", "v54", "v55", "v56", "v57", "v58", "v59", "v60", "v61", "v62", "v63", "v64", "v65", "v66", "v67", "v68", "v69", "v70", "v71"
#define T_ASM \
  "ds_read_b128 %4, %8\nds_read_b64_tr_b16 v[80:81], %7 offset:0\nds_read_b64_tr_b16 v[82:83], %7 offset:64\n" \
  "ds_read_b64_tr_b16 v[84:85], %5 offset:0\nds_read_b64_tr_b16 v[86:87], %5 offset:48\nds_read_b64_tr_b16 v[88:89], %6 offset:0\n" \
  "ds_read_b64_tr_b16 v[90:91], %6 offset:48\nds_read_b64_tr_b16 v[92:93], %7 offset:3584\nds_read_b64_tr_b16 v[94:95], %7 offset:3648\n" \
  "ds_read_b64_tr_b16 v[96:97], %5 offset:3584\nds_read_b64_tr_b16 v[98:99], %5 offset:3632\nds_read_b64_tr_b16 v[100:101], %6 offset:3584\n" \
  "ds_read_b64_tr_b16 v[102:103], %6 offset:3632\nds_read_b64_tr_b16 v[104:105], %7 offset:7168\nds_read_b64_tr_b16 v[106:107], %7 offset:7232\n" \
  "s_waitcnt lgkmcnt(8)\nv_mfma_f32_16x16x32_f16 %0, v[84:87], v[80:83], 0\nds_read_b64_tr_b16 v[108:109], %5 offset:7168\n" \
  "ds_read_b64_tr_b16 v[110:111], %5 offset:7216\nds_read_b64_tr_b16 v[112:113], %6 offset:7168\nv_mfma_f32_16x16x32_f16 %2, v[88:91], v[80:83], 0\n" \
  "ds_read_b64_tr_b16 v[114:115], %6 offset:7216\nds_read_b64_tr_b16 v[116:117], %7 offset:10752\nds_read_b64_tr_b16 v[118:119], %7 offset:10816\n" \
  "s_waitcnt lgkmcnt(8)\nv_mfma_f32_16x16x32_f16 %0, v[96:99], v[92:95], %0\nds_read_b64_tr_b16 v[120:121], %5 offset:10752\n" \
  "ds_read_b64_tr_b16 v[122:123], %5 offset:10800\nds_read_b64_tr_b16 v[124:125], %6 offset:10752\n" \
  "v_mfma_f32_16x16x32_f16 %2, v[100:103], v[92:95], %2\nds_read_b64_tr_b16 v[126:127], %6 offset:10800\n" \
  "ds_read_b64_tr_b16 v[80:81], %7 offset:14336\nds_read_b64_tr_b16 v[82:83], %7 offset:14400\ns_waitcnt lgkmcnt(8)\n" \
  "v_mfma_f32_16x16x32_f16 %0, v[108:111], v[104:107], %0\nds_read_b64_tr_b16 v[84:85], %5 offset:14336\n" \
  "ds_read_b64_tr_b16 v[86:87], %5 offset:14384\nds_read_b64_tr_b16 v[88:89], %6 offset:14336\n" \
  "v_mfma_f32_16x16x32_f16 %2, v[112:115], v[104:107], %2\nds_read_b64_tr_b16 v[90:91], %6 offset:14384\n" \
  "ds_read_b64_tr_b16 v[92:93], %7 offset:17920\nds_read_b64_tr_b16 v[94:95], %7 offset:17984\ns_waitcnt lgkmcnt(8)\n" \
  "v_mfma_f32_16x16x32_f16 %0, v[120:123], v[116:119], %0\nds_read_b64_tr_b16 v[96:97], %5 offset:17920\n" \
  "ds_read_b64_tr_b16 v[98:99], %5 offset:17968\nds_read_b64_tr_b16 v[100:101], %6 offset:17920\n" \
  "v_mfma_f32_16x16x32_f16 %2, v[124:127], v[116:119], %2\nds_read_b64_tr_b16 v[102:103], %6 offset:17968\n" \
  "ds_read_b64_tr_b16 v[104:105], %7 offset:21504\nds_read_b64_tr_b16 v[106:107], %7 offset:21568\ns_waitcnt lgkmcnt(8)\n" \
  "v_mfma_f32_16x16x32_f16 %1, v[84:87], v[80:83], 0\nds_read_b64_tr_b16 v[108:109], %5 offset:21504\nds_read_b64_tr_b16 v[110:111], %5 offset:21552\n" \
  "ds_read_b64_tr_b16 v[112:113], %6 offset:21504\nv_mfma_f32_16x16x32_f16 %3, v[88:91], v[80:83], 0\nds_read_b64_tr_b16 v[114:115], %6 offset:21552\n" \
  "ds_read_b64_tr_b16 v[116:117], %7 offset:25088\nds_read_b64_tr_b16 v[118:119], %7 offset:25152\ns_waitcnt lgkmcnt(8)\n" \
  "v_mfma_f32_16x16x32_f16 %1, v[96:99], v[92:95], %1\nds_read_b64_tr_b16 v[120:121], %5 offset:25088\n" \
  "ds_read_b64_tr_b16 v[122:123], %5 offset:25136\nds_read_b64_tr_b16 v[124:125], %6 offset:25088\n" \
  "v_mfma_f32_16x16x32_f16 %3, v[100:103], v[92:95], %3\nds_read_b64_tr_b16 v[126:127], %6 offset:25136\ns_waitcnt lgkmcnt(6)\n" \
  "v_mfma_f32_16x16x32_f16 %1, v[108:111], v[104:107], %1\nv_mfma_f32_16x16x32_f16 %3, v[112:115], v[104:107], %3\ns_waitcnt lgkmcnt(0)\n" \
  "v_mfma_f32_16x16x32_f16 %1, v[120:123], v[116:119], %1\nv_mfma_f32_16x16x32_f16 %3, v[124:127], v[116:119], %3\n"
#define T_CLOB "memory", "v80", "v81", "v82", "v83", "v84", "v85", "v86", "v87", "v88", "v89", "v90", "v91", "v92", "v93", "v94", "v95", "v96", "v97", "v98", "v99", "v100", "v101", "v102", "v103", "v104", "v105", "v106", "v107", "v108", "v109", "v110", "v111", "v112", "v113", "v114", "v115", "v116", "v117", "v118", "v119", "v120", "v121", "v122", "v123", "v124", "v125", "v126", "v127"
template <class F> __device__ __forceinline__ int gen_y(float ldt, float are, float aim, float bre, float bim, float cre, float cim, uint32_t im, F&& mid TSP) {
  float ar2, air, g;
  const float dt = state_a(ldt, are, aim, ar2, air, g);
  const float dbr = dt * bre, dbi = dt * bim, pr = cre * dbr - cim * dbi, pi = cre * dbi + cim * dbr;
  const f32x2 w8 = cexp2(ar2, -air, 8.f), w64 = cexp2(ar2, -air, 64.f);
  const float pm = fmaxf(fabsf(pr), fabsf(pi));
  int e = -4;
  if (__builtin_expect(__any(!(pm < 0x1p-4f) || ar2 > 0.f) || !__any(pm > 0x1p-12f), 0)) e = (int)(wave_maxu(__float_as_uint(pm)) >> 23) - 126 + (int)ceilf(g * 250.f);
  const f32x2 y0 = {__builtin_amdgcn_ldexpf(pr, 14 - e), __builtin_amdgcn_ldexpf(-pi, 14 - e)};
  mid();
  asm volatile(Y_ASM : : "v"(y0), "v"(w8), "v"(w64), "v"(im) : Y_CLOB);
  return e;
}
__device__ __forceinline__ int gen_x(float ldt, float are, float aim, char* im) {
  float ar2, air, g;
  state_a(ldt, are, aim, ar2, air, g);
  const int e = min((int)ceilf(g * 1810.f), 300);
  const float s = __builtin_amdgcn_ldexpf(1.f, 15 - e);
  const f32x2 w1 = cexp2(ar2, air, 1.f), w256 = cexp2(ar2, air, 256.f), w2 = cmul(w1, w1);
  f32x2 a[8], b[8], c[8];
  a[0] = (f32x2){s, 0.f}; a[1] = w1 * s; a[2] = w2 * s;
  b[0] = b[1] = w256 * s;
  b[2] = cmul(b[0], w2); a[3] = cmul(a[1], w2); a[4] = cmul(a[2], w2);
  b[3] = b[4] = b[5] = cmul(b[2], w2); a[5] = cmul(a[3], w2); a[6] = cmul(a[4], w2);
  b[6] = cmul(b[3], w2); a[7] = cmul(a[5], w2); c[0] = cmul(b[3], w256);
  b[7] = cmul(b[6], w2); c[2] = cmul(c[0], w256);
  put_im<0>(im + I_X, 48, a);
  c[1] = cmul(b[7], w256); c[3] = cmul(c[1], w256);
  put_im<0>(im + I_X + 16, 48, b);
#pragma unroll
  for (int k = 4; k < 8; ++k) c[k] = cmul(c[k - 1], w256);
  put_im<0>(im + I_X + 32, 48, c);
  return e;
}

template <int I> struct IC { static constexpr int value = I; };
template <int B, int E, class F> __device__ __forceinline__ void static_for(F&& f) {
  if constexpr (B < E) { f(IC<B>{}); static_for<B + 1, E>(f); }
}
typedef __attribute__((ext_vector_type(4))) short s16x4;
__device__ __forceinline__ u32x2 trd(uint32_t a) {
  return __builtin_bit_cast(u32x2, __builtin_amdgcn_ds_read_tr16_b64_v4i16((__attribute__((address_space(3))) s16x4*)(uintptr_t)a));
}
#define BAR() asm volatile("s_waitcnt lgkmcnt(0)\n\ts_barrier" ::: "memory")
#define ST16(off, v) asm volatile("global_store_dwordx4 %0, %1, %2 sc1\n\ts_nop 1" : : "v"(off), "v"(v), "s"(ws) : "memory")
#define ST4(off, v) asm volatile("global_store_dword %0, %1, %2 sc1" : : "v"(off), "v"(v), "s"(ws) : "memory")
#define LD16(r, off) asm volatile("global_load_dwordx4 %0, %1, %2 sc1" : "=v"(r) : "v"(off), "s"(ws) : "memory")
#define LD16W(r, off) asm volatile("global_load_dwordx4 %0, %1, %2 sc1\n\ts_waitcnt vmcnt(0)" : "=&v"(r) : "v"(off), "s"(ws) : "memory")
#define DRAIN() asm volatile("s_waitcnt vmcnt(0)" ::: "memory")
#define CEX(a, b) { const uint32_t hi_ = max(a, b), lo_ = min(a, b); a = hi_; b = lo_; }
#define MMB(a_, b_, c_) __builtin_amdgcn_mfma_f32_16x16x32_bf16(__builtin_bit_cast(bf16x8, a_), __builtin_bit_cast(bf16x8, b_), c_, 0, 0, 0)
#define NTM 0
#define DPOL ""
#define DPOL2 ""
#define DPOL4 ""
constexpr int SPIN_CAP = 1 << 18;
constexpr uint32_t UMASK = 0x40074007u;
__device__ __forceinline__ uint32_t utag(uint32_t T, int j) {
  return ((T >> (3 * j)) & 0x00070007u) | (((j <= 2) ? (T << (2 - j)) : (T >> (j - 2))) & 0x40004000u);
}
__device__ __forceinline__ uint32_t max4(const u32x4& a) { return max(max(a[0], a[1]), max(a[2], a[3])); }
__device__ __forceinline__ void until(uint32_t t) {
#pragma unroll 1
  for (int spin = 0; spin < 600 && (int)((uint32_t)__builtin_amdgcn_s_memrealtime() - t) < 0; ++spin) __builtin_amdgcn_s_sleep(1);
}
__device__ __forceinline__ uint32_t latest(uint32_t t) {
  const uint32_t t0 = (uint32_t)__builtin_amdgcn_readfirstlane(t) - 0x40000000u;
  return wave_maxu(t - t0) + t0;
}
__device__ __forceinline__ uint32_t gate(const char* ws, uint32_t po, uint32_t T) {
  u32x2 pv;
#pragma unroll 1
  for (int spin = 0; spin < SPIN_CAP; ++spin) {
    asm volatile("global_load_dwordx2 %0, %1, %2 sc1\n\ts_waitcnt vmcnt(0)" : "=&v"(pv) : "v"(po), "s"(ws) : "memory");
    if (!__any(pv[0] != T)) break;
    __builtin_amdgcn_s_sleep(2);
  }
  return pv[1];
}
__device__ __forceinline__ uint32_t gate2(const char* ws, uint32_t oa, uint32_t da, uint32_t ob, uint32_t T) {
  u32x2 pa, pb;
#pragma unroll 1
  for (int spin = 0; spin < SPIN_CAP; ++spin) {
    asm volatile("global_load_dwordx2 %0, %2, %4 sc1\n\tglobal_load_dwordx2 %1, %3, %4 sc1\n\ts_waitcnt vmcnt(0)" : "=&v"(pa), "=&v"(pb) : "v"(oa), "v"(ob), "s"(ws) : "memory");
    if (!__any(pa[0] != T || pb[0] != T)) break;
    __builtin_amdgcn_s_sleep(4);
  }
  pa[1] += da;
  return latest(((int)(pa[1] - pb[1]) > 0) ? pa[1] : pb[1]);
}
__device__ __forceinline__ void gsync(uint32_t a, uint32_t target) {
  uint32_t t, c, n = SPIN_CAP;
  uint64_t ex;
  asm volatile("s_waitcnt lgkmcnt(0)\n\ts_mov_b64 %3, exec\n\ts_mov_b64 exec, 1\n\tv_mov_b32 %0, 1\n\tds_add_u32 %4, %0\n\ts_mov_b64 exec, %3\n"
               "1:\n\ts_sleep 1\n\tds_read_b32 %0, %4\n\ts_sub_u32 %2, %2, 1\n\ts_cbranch_scc1 2f\n\ts_waitcnt lgkmcnt(0)\n\tv_readfirstlane_b32 %1, %0\n\t"
               "s_cmp_lt_u32 %1, %5\n\ts_cbranch_scc1 1b\n2:\n\ts_waitcnt lgkmcnt(0)"
               : "=&v"(t), "=&s"(c), "+s"(n), "=&s"(ex) : "v"(a), "n"(target) : "memory", "scc");
}
__device__ __forceinline__ void gsync0(uint32_t a, uint32_t target) {
  uint32_t t, c, n = SPIN_CAP;
  asm volatile("s_waitcnt lgkmcnt(0)\n\ts_mov_b64 exec, 1\n\tv_mov_b32 %0, 1\n\tds_add_u32 %3, %0\n\ts_mov_b64 exec, -1\n"
               "1:\n\tds_read_b32 %0, %3\n\ts_sub_u32 %2, %2, 1\n\ts_cbranch_scc1 2f\n\ts_waitcnt lgkmcnt(0)\n\tv_readfirstlane_b32 %1, %0\n\t"
               "s_cmp_lt_u32 %1, %4\n\ts_cbranch_scc1 1b\n2:\n\ts_waitcnt lgkmcnt(0)"
               : "=&v"(t), "=&s"(c), "+s"(n) : "v"(a), "n"(target) : "memory", "scc");
}
__device__ __forceinline__ void gwait(uint32_t a, uint32_t target) {
  uint32_t t, c, n = SPIN_CAP;
  asm volatile("1:\n\ts_sleep 2\n\tds_read_b32 %0, %3\n\ts_sub_u32 %2, %2, 1\n\ts_cbranch_scc1 2f\n\ts_waitcnt lgkmcnt(0)\n\tv_readfirstlane_b32 %1, %0\n\t"
               "s_cmp_lt_u32 %1, %4\n\ts_cbranch_scc1 1b\n2:\n\ts_waitcnt lgkmcnt(0)"
               : "=&v"(t), "=&s"(c), "+s"(n) : "v"(a), "n"(target) : "memory", "scc");
}
__device__ __forceinline__ void gwait0(uint32_t a, uint32_t target) {
  uint32_t t, c, n = SPIN_CAP;
  asm volatile("1:\n\tds_read_b32 %0, %3\n\ts_sub_u32 %2, %2, 1\n\ts_cbranch_scc1 2f\n\ts_waitcnt lgkmcnt(0)\n\tv_readfirstlane_b32 %1, %0\n\t"
               "s_cmp_lt_u32 %1, %4\n\ts_cbranch_scc1 1b\n2:\n\ts_waitcnt lgkmcnt(0)"
               : "=&v"(t), "=&s"(c), "+s"(n) : "v"(a), "n"(target) : "memory", "scc");
}
__device__ __forceinline__ void dma16(const void* base, uint32_t voff, uint32_t lds) {
  uint32_t keep;
  asm volatile("s_mov_b32 %0, m0\n\ts_mov_b32 m0, %3\n\ts_nop 0\n\tglobal_load_lds_dwordx4 %1, %2" DPOL "\n\ts_mov_b32 m0, %0" : "=&s"(keep) : "v"(voff), "s"(base), "s"(lds) : "memory");
}
__device__ __forceinline__ void dma16d(const void* base, uint32_t voff, uint32_t lds) {
  uint32_t keep;
  asm volatile("s_mov_b32 %0, m0\n\ts_mov_b32 m0, %3\n\ts_nop 0\n\tglobal_load_lds_dwordx4 %1, %2" DPOL2 "\n\ts_mov_b32 m0, %0" : "=&s"(keep) : "v"(voff), "s"(base), "s"(lds) : "memory");
}
__device__ __forceinline__ void nap(int n) {
  for (; n > 15; n -= 15) __builtin_amdgcn_s_sleep(15);
  switch (n) {
    case 1: __builtin_amdgcn_s_sleep(1); break; case 2: __builtin_amdgcn_s_sleep(2); break; case 3: __builtin_amdgcn_s_sleep(3); break;
    case 4: __builtin_amdgcn_s_sleep(4); break; case 5: __builtin_amdgcn_s_sleep(5); break; case 6: __builtin_amdgcn_s_sleep(6); break;
    case 7: __builtin_amdgcn_s_sleep(7); break; case 8: __builtin_amdgcn_s_sleep(8); break; case 9: __builtin_amdgcn_s_sleep(9); break;
    case 10: __builtin_amdgcn_s_sleep(10); break; case 11: __builtin_amdgcn_s_sleep(11); break; case 12: __builtin_amdgcn_s_sleep(12); break;
    case 13: __builtin_amdgcn_s_sleep(13); break; case 14: __builtin_amdgcn_s_sleep(14); break; case 15: __builtin_amdgcn_s_sleep(15); break;
    default: break;
  }
}
#define SLEEP(n) do { if ((n) > 0) __builtin_amdgcn_s_sleep(n); } while (0)
#define USLEEP 3
#define SSLEEP 11
#define PSLEEP 2
#define PPRIO 0
#define LFIRST 1
#define WD 40
#define OSLEEP 12
#define QPRIO 1
#define KPRIO 2
#define GSLEEP 0
#define NSLEEP 0
#define UD 40
#define XD 0
#define RSLEEP 0
#define UPRIO 2
#define WDEPTH 4
#define ESLEEP 0
#define SD 58
#define OD 0
#define KBACK 6
#define DDEPTH 6
#define C3 1
#define XOFF 0
#define XROT 2
#define WSOFF 0
#define T0 0
#define TB1 0
#define T0SLEEP 0
#define KN 2
#define RPF 1
#define TSTOP 70
#define HSTOP 110
#define VEARLY 0ull
#define FPR 3
#define SPR 3
#define DPRIO 0
#define T1 0
#define T1SLEEP 0
#define QPRIO2 3
#define HW 2
#define RD 62
#define OPOS 1
#define OD2 0
#define NA 12
#define RD2 88
#define ORD1 ((unsigned __int128)0x321087654u << 36 | 0x321087654ull)
#define WDEPTH2 4
#define RQ NA
#define HREV 1
#define QPRIO0 3
#define VPS 1
#define RW 1
#define NPRIO 0
#define XSL 10
#define XF 0
#define N0 8
#define FORCESLOW 0
#define FORCEFB 0
#define CONV_ASM \
  "s_cmp_eq_u32 s89, 1\ns_cbranch_scc1 1f\ns_cmp_gt_u32 s89, 1\ns_cbranch_scc1 9f\nds_read_b64 v[124:125], v3 offset:576\n" \
  "ds_read_b64 v[126:127], v3 offset:584\nds_read_b64 v[120:121], v3 offset:64\nds_read_b64 v[122:123], v3 offset:72\n" \
  "ds_read_b64 v[116:117], v3 offset:640\nds_read_b64 v[118:119], v3 offset:648\nds_read_b64 v[112:113], v3 offset:128\n" \
  "ds_read_b64 v[114:115], v3 offset:136\nds_read_b64 v[108:109], v3 offset:704\nds_read_b64 v[110:111], v3 offset:712\n" \
  "ds_read_b64 v[104:105], v3 offset:192\nds_read_b64 v[106:107], v3 offset:200\nds_read_b64 v[100:101], v3 offset:768\n" \
  "ds_read_b64 v[102:103], v3 offset:776\nds_read_b64 v[96:97], v3 offset:256\nds_read_b64 v[98:99], v3 offset:264\n" \
  "ds_read_b64 v[92:93], v3 offset:832\nds_read_b64 v[94:95], v3 offset:840\nds_read_b64 v[88:89], v3 offset:320\n" \
  "ds_read_b64 v[90:91], v3 offset:328\nds_read_b64 v[84:85], v3 offset:896\nds_read_b64 v[86:87], v3 offset:904\n" \
  "ds_read_b64 v[80:81], v3 offset:384\nds_read_b64 v[82:83], v3 offset:392\nds_read_b64 v[76:77], v3 offset:960\n" \
  "ds_read_b64 v[78:79], v3 offset:968\nds_read_b64 v[72:73], v3 offset:448\nds_read_b64 v[74:75], v3 offset:456\n" \
  "ds_read_b64 v[68:69], v3 offset:1024\nds_read_b64 v[70:71], v3 offset:1032\nds_read_b64 v[64:65], v3 offset:512\n" \
  "ds_read_b64 v[66:67], v3 offset:520\nds_read_b64 v[60:61], v3 offset:0\nds_read_b64 v[62:63], v3 offset:8\nds_read_b64 v[56:57], v3 offset:1088\n" \
  "ds_read_b64 v[58:59], v3 offset:1096\ns_waitcnt lgkmcnt(0)\ns_barrier\nds_read_b128 v[36:39], v2 offset:0\nds_read_b128 v[40:43], v2 offset:64\n" \
  "ds_read_b128 v[44:47], v2 offset:128\nds_read_b128 v[48:51], v2 offset:192\ns_waitcnt lgkmcnt(3)\n" \
  "v_mfma_f32_16x16x32_f16 v[12:15], v[124:127], v[36:39], 0\ns_setprio 1\nds_read_b128 v[52:55], v2 offset:256\n" \
  "v_mfma_f32_16x16x32_f16 v[16:19], v[120:123], v[36:39], 0\ns_waitcnt lgkmcnt(3)\nv_mfma_f32_16x16x32_f16 v[12:15], v[116:119], v[40:43], v[12:15]\n" \
  "ds_read_b128 v[36:39], v2 offset:320\nv_mfma_f32_16x16x32_f16 v[16:19], v[112:115], v[40:43], v[16:19]\ns_waitcnt lgkmcnt(3)\n" \
  "v_mfma_f32_16x16x32_f16 v[12:15], v[108:111], v[44:47], v[12:15]\nds_read_b128 v[40:43], v2 offset:384\n" \
  "v_mfma_f32_16x16x32_f16 v[16:19], v[104:107], v[44:47], v[16:19]\ns_waitcnt lgkmcnt(3)\n" \
  "v_mfma_f32_16x16x32_f16 v[12:15], v[100:103], v[48:51], v[12:15]\nds_read_b128 v[44:47], v2 offset:448\n" \
  "v_mfma_f32_16x16x32_f16 v[16:19], v[96:99], v[48:51], v[16:19]\ns_waitcnt lgkmcnt(3)\n" \
  "v_mfma_f32_16x16x32_f16 v[12:15], v[92:95], v[52:55], v[12:15]\nv_mfma_f32_16x16x32_f16 v[16:19], v[88:91], v[52:55], v[16:19]\n" \
  "s_waitcnt lgkmcnt(2)\nv_mfma_f32_16x16x32_f16 v[12:15], v[84:87], v[36:39], v[12:15]\nds_read_b128 v[48:51], v2 offset:512\n" \
  "v_mfma_f32_16x16x32_f16 v[16:19], v[80:83], v[36:39], v[16:19]\ns_waitcnt lgkmcnt(2)\n" \
  "v_mfma_f32_16x16x32_f16 v[12:15], v[76:79], v[40:43], v[12:15]\nds_read_b128 v[52:55], v2 offset:576\n" \
  "v_mfma_f32_16x16x32_f16 v[16:19], v[72:75], v[40:43], v[16:19]\ns_waitcnt lgkmcnt(2)\n" \
  "v_mfma_f32_16x16x32_f16 v[12:15], v[68:71], v[44:47], v[12:15]\nds_read_b128 v[36:39], v2 offset:640\n" \
  "v_mfma_f32_16x16x32_f16 v[16:19], v[64:67], v[44:47], v[16:19]\nv_mfma_f32_16x16x32_f16 v[20:23], v[60:63], v[44:47], 0\n" \
  "ds_read_b128 v[40:43], v2 offset:704\nv_mfma_f32_16x16x32_f16 v[28:31], v[56:59], v[44:47], 0\ns_waitcnt lgkmcnt(3)\n" \
  "v_mfma_f32_16x16x32_f16 v[16:19], v[124:127], v[48:51], v[16:19]\nds_read_b128 v[56:59], v2 offset:768\nv_cvt_pkrtz_f16_f32 v32, v12, v13\n" \
  "v_mfma_f32_16x16x32_f16 v[20:23], v[120:123], v[48:51], v[20:23]\nv_cvt_pkrtz_f16_f32 v33, v14, v15\ns_waitcnt lgkmcnt(3)\n" \
  "v_mfma_f32_16x16x32_f16 v[16:19], v[116:119], v[52:55], v[16:19]\nds_read_b128 v[44:47], v2 offset:832\n" \
  "v_pk_add_u16 v32, v32, 4 op_sel_hi:[1,0] clamp\nv_mfma_f32_16x16x32_f16 v[20:23], v[112:115], v[52:55], v[20:23]\n" \
  "v_pk_add_u16 v33, v33, 4 op_sel_hi:[1,0] clamp\ns_waitcnt lgkmcnt(3)\nv_mfma_f32_16x16x32_f16 v[16:19], v[108:111], v[36:39], v[16:19]\n" \
  "ds_read_b128 v[48:51], v2 offset:896\nds_write_b64 v4, v[32:33] offset:0\nv_mfma_f32_16x16x32_f16 v[20:23], v[104:107], v[36:39], v[20:23]\n" \
  "s_waitcnt lgkmcnt(3)\nv_mfma_f32_16x16x32_f16 v[16:19], v[100:103], v[40:43], v[16:19]\nds_read_b128 v[52:55], v2 offset:960\n" \
  "v_mfma_f32_16x16x32_f16 v[20:23], v[96:99], v[40:43], v[20:23]\nv_mfma_f32_16x16x32_f16 v[16:19], v[92:95], v[56:59], v[16:19]\n" \
  "v_mfma_f32_16x16x32_f16 v[20:23], v[88:91], v[56:59], v[20:23]\nds_read_b128 v[36:39], v2 offset:1024\ns_waitcnt lgkmcnt(3)\n" \
  "v_mfma_f32_16x16x32_f16 v[16:19], v[84:87], v[44:47], v[16:19]\nv_mfma_f32_16x16x32_f16 v[20:23], v[80:83], v[44:47], v[20:23]\n" \
  "ds_read_b128 v[40:43], v2 offset:1088\nds_read_b64 v[56:57], v6 offset:256\nds_read_b64 v[58:59], v6 offset:264\n" \
  "v_mfma_f32_16x16x32_f16 v[16:19], v[76:79], v[48:51], v[16:19]\nv_mfma_f32_16x16x32_f16 v[20:23], v[72:75], v[48:51], v[20:23]\n" \
  "ds_read_b128 v[44:47], v2 offset:1152\ns_waitcnt lgkmcnt(4)\nv_mfma_f32_16x16x32_f16 v[16:19], v[68:71], v[52:55], v[16:19]\n" \
  "v_mfma_f32_16x16x32_f16 v[20:23], v[64:67], v[52:55], v[20:23]\nds_read_b128 v[48:51], v2 offset:1216\n" \
  "v_mfma_f32_16x16x32_f16 v[24:27], v[60:63], v[52:55], 0\nv_mfma_f32_16x16x32_f16 v[20:23], v[124:127], v[36:39], v[20:23]\n" \
  "ds_read_b128 v[52:55], v2 offset:1280\nv_mfma_f32_16x16x32_f16 v[24:27], v[120:123], v[36:39], v[24:27]\ns_waitcnt lgkmcnt(5)\n" \
  "v_mfma_f32_16x16x32_f16 v[20:23], v[116:119], v[40:43], v[20:23]\nds_read_b128 v[36:39], v2 offset:1344\nv_cvt_pkrtz_f16_f32 v32, v16, v17\n" \
  "v_mfma_f32_16x16x32_f16 v[24:27], v[112:115], v[40:43], v[24:27]\nv_cvt_pkrtz_f16_f32 v33, v18, v19\ns_waitcnt lgkmcnt(3)\n" \
  "v_mfma_f32_16x16x32_f16 v[20:23], v[108:111], v[44:47], v[20:23]\nds_read_b128 v[40:43], v2 offset:1408\n" \
  "v_pk_add_u16 v32, v32, 4 op_sel_hi:[1,0] clamp\nds_read_b32 v35, v9 offset:32\nv_mfma_f32_16x16x32_f16 v[24:27], v[104:107], v[44:47], v[24:27]\n" \
  "v_pk_add_u16 v33, v33, 4 op_sel_hi:[1,0] clamp\ns_waitcnt lgkmcnt(3)\nv_mfma_f32_16x16x32_f16 v[20:23], v[100:103], v[48:51], v[20:23]\n" \
  "ds_read_b128 v[44:47], v2 offset:1472\nds_write_b64 v4, v[32:33] offset:1024\nv_mfma_f32_16x16x32_f16 v[24:27], v[96:99], v[48:51], v[24:27]\n" \
  "v_mfma_f32_16x16x32_f16 v[20:23], v[92:95], v[52:55], v[20:23]\nv_mfma_f32_16x16x32_f16 v[24:27], v[88:91], v[52:55], v[24:27]\n" \
  "ds_read_b128 v[48:51], v2 offset:1536\ns_waitcnt lgkmcnt(3)\nv_readfirstlane_b32 s85, v35\ns_cmp_lt_u32 s85, 4\ns_cbranch_scc1 21f\n22:\n" \
  "v_mfma_f32_16x16x32_f16 v[20:23], v[84:87], v[36:39], v[20:23]\nv_mfma_f32_16x16x32_f16 v[24:27], v[80:83], v[36:39], v[24:27]\n" \
  "ds_read_b128 v[52:55], v2 offset:1600\nv_mfma_f32_16x16x32_f16 v[20:23], v[76:79], v[40:43], v[20:23]\n" \
  "v_mfma_f32_16x16x32_f16 v[24:27], v[72:75], v[40:43], v[24:27]\nds_read_b128 v[36:39], v2 offset:1664\ns_waitcnt lgkmcnt(4)\n" \
  "v_mfma_f32_16x16x32_f16 v[20:23], v[68:71], v[44:47], v[20:23]\nv_mfma_f32_16x16x32_f16 v[24:27], v[64:67], v[44:47], v[24:27]\n" \
  "ds_read_b128 v[40:43], v2 offset:1728\nv_mfma_f32_16x16x32_f16 v[28:31], v[60:63], v[44:47], v[28:31]\nds_read_b64_tr_b16 v[60:61], v5 offset:0\n" \
  "ds_read_b64_tr_b16 v[62:63], v5 offset:32\ns_waitcnt lgkmcnt(4)\nv_mfma_f32_16x16x32_f16 v[24:27], v[124:127], v[48:51], v[24:27]\n" \
  "v_mfma_f32_16x16x32_f16 v[28:31], v[120:123], v[48:51], v[28:31]\nds_read_b64_tr_b16 v[44:45], v5 offset:256\n" \
  "ds_read_b64_tr_b16 v[46:47], v5 offset:288\nv_mfma_f32_16x16x32_f16 v[24:27], v[116:119], v[52:55], v[24:27]\n" \
  "ds_read_b64 v[120:121], v6 offset:320\nds_read_b64 v[122:123], v6 offset:328\nv_mfma_f32_16x16x32_f16 v[28:31], v[112:115], v[52:55], v[28:31]\n" \
  "ds_read_b128 v[48:51], v2 offset:1792\ns_waitcnt lgkmcnt(7)\nv_mfma_f32_16x16x32_f16 v[24:27], v[108:111], v[36:39], v[24:27]\n" \
  "ds_read_b128 v[112:115], v2 offset:1856\nv_mfma_f32_16x16x32_f16 v[28:31], v[104:107], v[36:39], v[28:31]\nds_read_b64 v[52:53], v6 offset:128\n" \
  "ds_read_b64 v[54:55], v6 offset:136\nv_mfma_f32_16x16x32_f16 v[24:27], v[100:103], v[40:43], v[24:27]\nds_read_b128 v[104:107], v2 offset:1920\n" \
  "v_mfma_f32_16x16x32_f16 v[28:31], v[96:99], v[40:43], v[28:31]\nds_read_b64 v[36:37], v6 offset:192\nds_read_b64 v[38:39], v6 offset:200\n" \
  "s_waitcnt lgkmcnt(9)\nv_mfma_f32_16x16x32_f16 v[20:23], v[60:63], v[56:59], v[20:23]\nds_read_b128 v[96:99], v2 offset:1984\ns_waitcnt lgkmcnt(7)\n" \
  "v_mfma_f32_16x16x32_f16 v[20:23], v[44:47], v[120:123], v[20:23]\nv_mfma_f32_16x16x32_f16 v[24:27], v[92:95], v[48:51], v[24:27]\n" \
  "ds_read_b128 v[40:43], v2 offset:2048\nv_mfma_f32_16x16x32_f16 v[28:31], v[88:91], v[48:51], v[28:31]\nds_read_b64_tr_b16 v[88:89], v5 offset:512\n" \
  "ds_read_b64_tr_b16 v[90:91], v5 offset:544\ns_waitcnt lgkmcnt(6)\nv_mfma_f32_16x16x32_f16 v[24:27], v[84:87], v[112:115], v[24:27]\n" \
  "v_mfma_f32_16x16x32_f16 v[28:31], v[80:83], v[112:115], v[28:31]\nv_cvt_pkrtz_f16_f32 v32, v20, v21\nv_cvt_pkrtz_f16_f32 v33, v22, v23\n" \
  "v_mfma_f32_16x16x32_f16 v[24:27], v[76:79], v[104:107], v[24:27]\nds_read_b64_tr_b16 v[48:49], v5 offset:768\n" \
  "ds_read_b64_tr_b16 v[50:51], v5 offset:800\nv_mfma_f32_16x16x32_f16 v[28:31], v[72:75], v[104:107], v[28:31]\n" \
  "ds_read_b128 v[80:83], v2 offset:2112\ns_waitcnt lgkmcnt(5)\nv_mfma_f32_16x16x32_f16 v[24:27], v[68:71], v[96:99], v[24:27]\n" \
  "ds_read_b128 v[112:115], v2 offset:2176\nv_pk_add_u16 v32, v32, 4 op_sel_hi:[1,0] clamp\n" \
  "v_mfma_f32_16x16x32_f16 v[28:31], v[64:67], v[96:99], v[28:31]\nds_read_b128 v[72:75], v2 offset:2240\n" \
  "v_pk_add_u16 v33, v33, 4 op_sel_hi:[1,0] clamp\nv_mfma_f32_16x16x32_f16 v[28:31], v[124:127], v[40:43], v[28:31]\n" \
  "ds_read_b128 v[104:107], v2 offset:2304\nds_write_b64 v4, v[32:33] offset:2048\nv_mfma_f32_16x16x32_f16 v[24:27], v[60:63], v[52:55], v[24:27]\n" \
  "ds_read_b128 v[64:67], v2 offset:2368\nds_read_b64 v[96:97], v6 offset:0\nds_read_b64 v[98:99], v6 offset:8\n" \
  "v_mfma_f32_16x16x32_f16 v[24:27], v[44:47], v[36:39], v[24:27]\nds_read_b128 v[124:127], v2 offset:2432\ns_waitcnt lgkmcnt(9)\n" \
  "v_mfma_f32_16x16x32_f16 v[24:27], v[88:91], v[56:59], v[24:27]\nds_read_b128 v[40:43], v2 offset:2496\n" \
  "v_mfma_f32_16x16x32_f16 v[24:27], v[48:51], v[120:123], v[24:27]\ns_waitcnt lgkmcnt(8)\n" \
  "v_mfma_f32_16x16x32_f16 v[28:31], v[116:119], v[80:83], v[28:31]\nds_read_b64 v[116:117], v6 offset:64\nds_read_b64 v[118:119], v6 offset:72\n" \
  "v_mfma_f32_16x16x32_f16 v[28:31], v[108:111], v[112:115], v[28:31]\nds_read_b64_tr_b16 v[80:81], v5 offset:1024\n" \
  "ds_read_b64_tr_b16 v[82:83], v5 offset:1056\ns_waitcnt lgkmcnt(10)\nv_mfma_f32_16x16x32_f16 v[28:31], v[100:103], v[72:75], v[28:31]\n" \
  "v_cvt_pkrtz_f16_f32 v32, v24, v25\nv_cvt_pkrtz_f16_f32 v33, v26, v27\nv_mfma_f32_16x16x32_f16 v[28:31], v[92:95], v[104:107], v[28:31]\n" \
  "v_pk_add_u16 v32, v32, 4 op_sel_hi:[1,0] clamp\ns_waitcnt lgkmcnt(5)\nv_mfma_f32_16x16x32_f16 v[28:31], v[84:87], v[64:67], v[28:31]\n" \
  "ds_read_b64_tr_b16 v[108:109], v5 offset:1280\nds_read_b64_tr_b16 v[110:111], v5 offset:1312\n" \
  "v_mfma_f32_16x16x32_f16 v[28:31], v[76:79], v[124:127], v[28:31]\nds_read_b64_tr_b16 v[112:113], v7 offset:0\n" \
  "ds_read_b64_tr_b16 v[114:115], v7 offset:64\ns_waitcnt lgkmcnt(6)\nv_mfma_f32_16x16x32_f16 v[28:31], v[68:71], v[40:43], v[28:31]\n" \
  "ds_read2_b32 v[100:101], v8 offset0:48 offset1:49\nds_read2_b32 v[102:103], v8 offset0:50 offset1:51\n" \
  "v_mfma_f32_16x16x32_f16 v[28:31], v[60:63], v[96:99], v[28:31]\nv_pk_add_u16 v33, v33, 4 op_sel_hi:[1,0] clamp\n" \
  "ds_write_b64 v4, v[32:33] offset:3072\nv_mfma_f32_16x16x32_f16 v[28:31], v[44:47], v[116:119], v[28:31]\n" \
  "v_mfma_f32_16x16x32_f16 v[28:31], v[88:91], v[52:55], v[28:31]\nv_mfma_f32_16x16x32_f16 v[28:31], v[48:51], v[36:39], v[28:31]\n" \
  "s_waitcnt lgkmcnt(5)\nv_mfma_f32_16x16x32_f16 v[28:31], v[80:83], v[56:59], v[28:31]\n" \
  "v_mfma_f32_16x16x32_f16 v[28:31], v[108:111], v[120:123], v[28:31]\ns_waitcnt lgkmcnt(1)\n" \
  "v_mfma_f32_16x16x32_f16 v[28:31], v[112:115], v[100:103], v[28:31]\ns_nop 7\nv_cvt_pkrtz_f16_f32 v32, v28, v29\nv_cvt_pkrtz_f16_f32 v33, v30, v31\n" \
  "v_pk_add_u16 v32, v32, 4 op_sel_hi:[1,0] clamp\nv_pk_add_u16 v33, v33, 4 op_sel_hi:[1,0] clamp\nds_write_b64 v4, v[32:33] offset:4096\n" \
  "s_setprio 0\ns_setpc_b64 s[90:91]\n21:\ns_movk_i32 s84, 0x4000\n23:\nds_read_b32 v35, v9 offset:32\ns_sub_u32 s84, s84, 1\ns_cbranch_scc1 22b\n" \
  "s_waitcnt lgkmcnt(0)\nv_readfirstlane_b32 s85, v35\ns_cmp_lt_u32 s85, 4\ns_cbranch_scc1 23b\ns_branch 22b\n1:\n" \
  "ds_read_b64 v[124:125], v3 offset:1088\nds_read_b64 v[126:127], v3 offset:1096\nds_read_b64 v[120:121], v3 offset:0\n" \
  "ds_read_b64 v[122:123], v3 offset:8\nds_read_b64 v[116:117], v3 offset:64\nds_read_b64 v[118:119], v3 offset:72\n" \
  "ds_read_b64 v[112:113], v3 offset:128\nds_read_b64 v[114:115], v3 offset:136\nds_read_b64 v[108:109], v3 offset:192\n" \
  "ds_read_b64 v[110:111], v3 offset:200\nds_read_b64 v[104:105], v3 offset:256\nds_read_b64 v[106:107], v3 offset:264\n" \
  "ds_read_b64 v[100:101], v3 offset:320\nds_read_b64 v[102:103], v3 offset:328\nds_read_b64 v[96:97], v3 offset:384\n" \
  "ds_read_b64 v[98:99], v3 offset:392\nds_read_b64 v[92:93], v3 offset:448\nds_read_b64 v[94:95], v3 offset:456\n" \
  "ds_read_b64 v[88:89], v3 offset:512\nds_read_b64 v[90:91], v3 offset:520\nds_read_b64 v[84:85], v3 offset:576\n" \
  "ds_read_b64 v[86:87], v3 offset:584\nds_read_b64 v[80:81], v3 offset:640\nds_read_b64 v[82:83], v3 offset:648\n" \
  "ds_read_b64 v[76:77], v3 offset:704\nds_read_b64 v[78:79], v3 offset:712\nds_read_b64 v[72:73], v3 offset:768\n" \
  "ds_read_b64 v[74:75], v3 offset:776\nds_read_b64 v[68:69], v3 offset:832\nds_read_b64 v[70:71], v3 offset:840\n" \
  "ds_read_b64 v[64:65], v3 offset:896\nds_read_b64 v[66:67], v3 offset:904\nds_read_b64 v[60:61], v3 offset:960\n" \
  "ds_read_b64 v[62:63], v3 offset:968\nds_read_b64 v[56:57], v3 offset:1024\nds_read_b64 v[58:59], v3 offset:1032\n" \
  "ds_read_b64 v[52:53], v6 offset:0\nds_read_b64 v[54:55], v6 offset:8\nds_read_b64 v[48:49], v6 offset:64\nds_read_b64 v[50:51], v6 offset:72\n" \
  "s_waitcnt lgkmcnt(0)\ns_barrier\nds_read_b128 v[28:31], v2 offset:960\nds_read_b128 v[32:35], v2 offset:1472\n" \
  "ds_read_b128 v[36:39], v2 offset:1984\nds_read_b128 v[40:43], v2 offset:2048\nds_read_b128 v[44:47], v2 offset:2112\ns_waitcnt lgkmcnt(3)\n" \
  "v_mfma_f32_16x16x32_f16 v[12:15], v[124:127], v[28:31], 0\ns_setprio 1\nds_read_b128 v[28:31], v2 offset:2176\n" \
  "v_mfma_f32_16x16x32_f16 v[16:19], v[124:127], v[32:35], 0\nds_read_b128 v[32:35], v2 offset:2240\ns_waitcnt lgkmcnt(3)\n" \
  "v_mfma_f32_16x16x32_f16 v[12:15], v[120:123], v[36:39], v[12:15]\nv_mfma_f32_16x16x32_f16 v[20:23], v[124:127], v[36:39], 0\n" \
  "ds_read_b128 v[124:127], v2 offset:2304\nds_read_b128 v[36:39], v2 offset:2368\nv_mfma_f32_16x16x32_f16 v[12:15], v[116:119], v[40:43], v[12:15]\n" \
  "ds_read_b128 v[40:43], v2 offset:2432\ns_waitcnt lgkmcnt(4)\nv_mfma_f32_16x16x32_f16 v[12:15], v[112:115], v[44:47], v[12:15]\n" \
  "ds_read_b128 v[44:47], v2 offset:2496\nv_mfma_f32_16x16x32_f16 v[12:15], v[108:111], v[28:31], v[12:15]\nds_read_b128 v[28:31], v2 offset:2560\n" \
  "s_waitcnt lgkmcnt(4)\nv_mfma_f32_16x16x32_f16 v[12:15], v[104:107], v[32:35], v[12:15]\nds_read_b128 v[32:35], v2 offset:2624\n" \
  "v_mfma_f32_16x16x32_f16 v[12:15], v[100:103], v[124:127], v[12:15]\ns_waitcnt lgkmcnt(3)\n" \
  "v_mfma_f32_16x16x32_f16 v[12:15], v[96:99], v[36:39], v[12:15]\nds_read_b128 v[124:127], v2 offset:2688\n" \
  "v_mfma_f32_16x16x32_f16 v[12:15], v[92:95], v[40:43], v[12:15]\ns_waitcnt lgkmcnt(3)\n" \
  "v_mfma_f32_16x16x32_f16 v[12:15], v[88:91], v[44:47], v[12:15]\nds_read_b128 v[36:39], v2 offset:2752\n" \
  "v_mfma_f32_16x16x32_f16 v[16:19], v[120:123], v[44:47], v[16:19]\ns_waitcnt lgkmcnt(3)\n" \
  "v_mfma_f32_16x16x32_f16 v[12:15], v[84:87], v[28:31], v[12:15]\nds_read_b128 v[40:43], v2 offset:2816\n" \
  "v_mfma_f32_16x16x32_f16 v[16:19], v[116:119], v[28:31], v[16:19]\ns_waitcnt lgkmcnt(3)\n" \
  "v_mfma_f32_16x16x32_f16 v[12:15], v[80:83], v[32:35], v[12:15]\nds_read_b128 v[44:47], v2 offset:2880\n" \
  "v_mfma_f32_16x16x32_f16 v[16:19], v[112:115], v[32:35], v[16:19]\ns_waitcnt lgkmcnt(3)\n" \
  "v_mfma_f32_16x16x32_f16 v[12:15], v[76:79], v[124:127], v[12:15]\nds_read_b128 v[28:31], v2 offset:2944\n" \
  "v_mfma_f32_16x16x32_f16 v[16:19], v[108:111], v[124:127], v[16:19]\ns_waitcnt lgkmcnt(3)\n" \
  "v_mfma_f32_16x16x32_f16 v[12:15], v[72:75], v[36:39], v[12:15]\nds_read_b128 v[32:35], v2 offset:3008\n" \
  "v_mfma_f32_16x16x32_f16 v[16:19], v[104:107], v[36:39], v[16:19]\ns_waitcnt lgkmcnt(3)\n" \
  "v_mfma_f32_16x16x32_f16 v[12:15], v[68:71], v[40:43], v[12:15]\nv_mfma_f32_16x16x32_f16 v[16:19], v[100:103], v[40:43], v[16:19]\n" \
  "ds_read_b128 v[124:127], v2 offset:3072\ns_waitcnt lgkmcnt(3)\nv_mfma_f32_16x16x32_f16 v[12:15], v[64:67], v[44:47], v[12:15]\n" \
  "v_mfma_f32_16x16x32_f16 v[16:19], v[96:99], v[44:47], v[16:19]\nds_read_b128 v[36:39], v2 offset:3136\ns_waitcnt lgkmcnt(3)\n" \
  "v_mfma_f32_16x16x32_f16 v[12:15], v[60:63], v[28:31], v[12:15]\nds_read_b64 v[40:41], v6 offset:128\nds_read_b64 v[42:43], v6 offset:136\n" \
  "v_mfma_f32_16x16x32_f16 v[16:19], v[92:95], v[28:31], v[16:19]\nds_read_b128 v[44:47], v2 offset:3200\ns_waitcnt lgkmcnt(4)\n" \
  "v_mfma_f32_16x16x32_f16 v[12:15], v[56:59], v[32:35], v[12:15]\nv_mfma_f32_16x16x32_f16 v[16:19], v[88:91], v[32:35], v[16:19]\n" \
  "ds_read_b128 v[28:31], v2 offset:3264\nv_mfma_f32_16x16x32_f16 v[20:23], v[120:123], v[32:35], v[20:23]\nds_read_b64 v[120:121], v6 offset:192\n" \
  "ds_read_b64 v[122:123], v6 offset:200\nv_mfma_f32_16x16x32_f16 v[16:19], v[84:87], v[124:127], v[16:19]\nds_read_b128 v[32:35], v2 offset:3328\n" \
  "v_mfma_f32_16x16x32_f16 v[20:23], v[116:119], v[124:127], v[20:23]\nds_read_b64 v[116:117], v6 offset:256\nds_read_b64 v[118:119], v6 offset:264\n" \
  "s_waitcnt lgkmcnt(6)\nv_mfma_f32_16x16x32_f16 v[16:19], v[80:83], v[36:39], v[16:19]\nds_read_b128 v[124:127], v2 offset:3392\n" \
  "v_mfma_f32_16x16x32_f16 v[20:23], v[112:115], v[36:39], v[20:23]\nds_read_b64 v[112:113], v6 offset:320\nds_read_b64 v[114:115], v6 offset:328\n" \
  "v_mfma_f32_16x16x32_f16 v[16:19], v[76:79], v[44:47], v[16:19]\nds_read_b128 v[36:39], v2 offset:3456\n" \
  "v_mfma_f32_16x16x32_f16 v[20:23], v[108:111], v[44:47], v[20:23]\ns_waitcnt lgkmcnt(6)\n" \
  "v_mfma_f32_16x16x32_f16 v[16:19], v[72:75], v[28:31], v[16:19]\nds_read_b128 v[108:111], v2 offset:3520\n" \
  "v_mfma_f32_16x16x32_f16 v[20:23], v[104:107], v[28:31], v[20:23]\nv_mfma_f32_16x16x32_f16 v[16:19], v[68:71], v[32:35], v[16:19]\n" \
  "ds_read_b128 v[44:47], v2 offset:3584\nv_mfma_f32_16x16x32_f16 v[20:23], v[100:103], v[32:35], v[20:23]\nds_read_b128 v[104:107], v2 offset:3648\n" \
  "ds_read_b32 v27, v9 offset:32\ns_waitcnt lgkmcnt(4)\nv_mfma_f32_16x16x32_f16 v[16:19], v[64:67], v[124:127], v[16:19]\n" \
  "ds_read_b128 v[28:31], v2 offset:3712\nv_mfma_f32_16x16x32_f16 v[20:23], v[96:99], v[124:127], v[20:23]\nds_read_b128 v[100:103], v2 offset:3776\n" \
  "ds_read2_b32 v[32:33], v8 offset0:32 offset1:33\nds_read2_b32 v[34:35], v8 offset0:34 offset1:35\n" \
  "v_mfma_f32_16x16x32_f16 v[16:19], v[60:63], v[36:39], v[16:19]\nds_read_b128 v[96:99], v2 offset:3840\n" \
  "v_mfma_f32_16x16x32_f16 v[20:23], v[92:95], v[36:39], v[20:23]\nds_read_b128 v[124:127], v2 offset:3904\ns_waitcnt lgkmcnt(8)\n" \
  "v_mfma_f32_16x16x32_f16 v[16:19], v[56:59], v[108:111], v[16:19]\nds_read_b128 v[92:95], v2 offset:3968\ns_waitcnt lgkmcnt(7)\n" \
  "v_readfirstlane_b32 s85, v27\ns_cmp_lt_u32 s85, 4\ns_cbranch_scc1 31f\n32:\nv_mfma_f32_16x16x32_f16 v[20:23], v[88:91], v[108:111], v[20:23]\n" \
  "ds_read_b128 v[36:39], v2 offset:4032\nds_read_b64_tr_b16 v[88:89], v5 offset:512\nds_read_b64_tr_b16 v[90:91], v5 offset:544\n" \
  "v_mfma_f32_16x16x32_f16 v[20:23], v[84:87], v[44:47], v[20:23]\nds_read_b64_tr_b16 v[108:109], v5 offset:768\n" \
  "ds_read_b64_tr_b16 v[110:111], v5 offset:800\nv_mfma_f32_16x16x32_f16 v[20:23], v[80:83], v[104:107], v[20:23]\n" \
  "ds_read_b64_tr_b16 v[84:85], v5 offset:1024\nds_read_b64_tr_b16 v[86:87], v5 offset:1056\ns_waitcnt lgkmcnt(12)\n" \
  "v_mfma_f32_16x16x32_f16 v[20:23], v[76:79], v[28:31], v[20:23]\nds_read_b64_tr_b16 v[44:45], v5 offset:1280\n" \
  "ds_read_b64_tr_b16 v[46:47], v5 offset:1312\nv_mfma_f32_16x16x32_f16 v[20:23], v[72:75], v[100:103], v[20:23]\ns_waitcnt lgkmcnt(10)\n" \
  "v_mfma_f32_16x16x32_f16 v[20:23], v[68:71], v[96:99], v[20:23]\nds_read_b64_tr_b16 v[80:81], v5 offset:1536\n" \
  "ds_read_b64_tr_b16 v[82:83], v5 offset:1568\nv_mfma_f32_16x16x32_f16 v[20:23], v[64:67], v[124:127], v[20:23]\n" \
  "ds_read_b64_tr_b16 v[104:105], v5 offset:1792\nds_read_b64_tr_b16 v[106:107], v5 offset:1824\ns_waitcnt lgkmcnt(12)\n" \
  "v_mfma_f32_16x16x32_f16 v[20:23], v[60:63], v[92:95], v[20:23]\nds_read_b64_tr_b16 v[76:77], v5 offset:2048\n" \
  "ds_read_b64_tr_b16 v[78:79], v5 offset:2080\nv_mfma_f32_16x16x32_f16 v[20:23], v[56:59], v[36:39], v[20:23]\ns_waitcnt lgkmcnt(10)\n" \
  "v_mfma_f32_16x16x32_f16 v[12:15], v[88:91], v[52:55], v[12:15]\nds_read_b64_tr_b16 v[28:29], v5 offset:2304\n" \
  "ds_read_b64_tr_b16 v[30:31], v5 offset:2336\nv_mfma_f32_16x16x32_f16 v[12:15], v[108:111], v[48:51], v[12:15]\n" \
  "ds_read_b64_tr_b16 v[72:73], v5 offset:2560\nds_read_b64_tr_b16 v[74:75], v5 offset:2592\ns_waitcnt lgkmcnt(10)\n" \
  "v_mfma_f32_16x16x32_f16 v[12:15], v[84:87], v[40:43], v[12:15]\nds_read_b64_tr_b16 v[100:101], v5 offset:2816\n" \
  "ds_read_b64_tr_b16 v[102:103], v5 offset:2848\nv_mfma_f32_16x16x32_f16 v[16:19], v[84:87], v[52:55], v[16:19]\n" \
  "ds_read_b64_tr_b16 v[68:69], v7 offset:0\nds_read_b64_tr_b16 v[70:71], v7 offset:64\n" \
  "v_mfma_f32_16x16x32_f16 v[12:15], v[44:47], v[120:123], v[12:15]\nv_mfma_f32_16x16x32_f16 v[16:19], v[44:47], v[48:51], v[16:19]\n" \
  "s_waitcnt lgkmcnt(10)\nv_mfma_f32_16x16x32_f16 v[12:15], v[80:83], v[116:119], v[12:15]\nds_read2_b32 v[96:97], v8 offset0:16 offset1:17\n" \
  "ds_read2_b32 v[98:99], v8 offset0:18 offset1:19\nv_mfma_f32_16x16x32_f16 v[16:19], v[80:83], v[40:43], v[16:19]\n" \
  "ds_read2_b32 v[64:65], v8 offset0:0 offset1:1\nds_read2_b32 v[66:67], v8 offset0:2 offset1:3\n" \
  "v_mfma_f32_16x16x32_f16 v[20:23], v[80:83], v[52:55], v[20:23]\nv_mfma_f32_16x16x32_f16 v[12:15], v[104:107], v[112:115], v[12:15]\n" \
  "v_mfma_f32_16x16x32_f16 v[16:19], v[104:107], v[120:123], v[16:19]\nv_mfma_f32_16x16x32_f16 v[20:23], v[104:107], v[48:51], v[20:23]\n" \
  "s_waitcnt lgkmcnt(10)\nv_mfma_f32_16x16x32_f16 v[16:19], v[76:79], v[116:119], v[16:19]\nds_read_b64_tr_b16 v[124:125], v7 offset:512\n" \
  "ds_read_b64_tr_b16 v[126:127], v7 offset:576\nv_mfma_f32_16x16x32_f16 v[20:23], v[76:79], v[40:43], v[20:23]\n" \
  "ds_read2_b32 v[60:61], v8 offset0:48 offset1:49\nds_read2_b32 v[62:63], v8 offset0:50 offset1:51\n" \
  "v_mfma_f32_16x16x32_f16 v[16:19], v[28:31], v[112:115], v[16:19]\nv_mfma_f32_16x16x32_f16 v[20:23], v[28:31], v[120:123], v[20:23]\n" \
  "s_waitcnt lgkmcnt(10)\nv_mfma_f32_16x16x32_f16 v[20:23], v[72:75], v[116:119], v[20:23]\nds_read_b64_tr_b16 v[92:93], v7 offset:1024\n" \
  "ds_read_b64_tr_b16 v[94:95], v7 offset:1088\nv_mfma_f32_16x16x32_f16 v[20:23], v[100:103], v[112:115], v[20:23]\n" \
  "ds_read_b64_tr_b16 v[56:57], v7 offset:1536\nds_read_b64_tr_b16 v[58:59], v7 offset:1600\ns_waitcnt lgkmcnt(10)\n" \
  "v_mfma_f32_16x16x32_f16 v[12:15], v[68:71], v[32:35], v[12:15]\nv_mfma_f32_16x16x32_f16 v[16:19], v[68:71], v[96:99], v[16:19]\n" \
  "s_waitcnt lgkmcnt(6)\nv_mfma_f32_16x16x32_f16 v[20:23], v[68:71], v[64:67], v[20:23]\ns_waitcnt lgkmcnt(2)\n" \
  "v_mfma_f32_16x16x32_f16 v[12:15], v[124:127], v[60:63], v[12:15]\nv_mfma_f32_16x16x32_f16 v[16:19], v[124:127], v[32:35], v[16:19]\n" \
  "v_mfma_f32_16x16x32_f16 v[20:23], v[124:127], v[96:99], v[20:23]\nv_mfma_f32_16x16x32_f16 v[16:19], v[92:95], v[60:63], v[16:19]\n" \
  "v_mfma_f32_16x16x32_f16 v[20:23], v[92:95], v[32:35], v[20:23]\ns_waitcnt lgkmcnt(0)\n" \
  "v_mfma_f32_16x16x32_f16 v[20:23], v[56:59], v[60:63], v[20:23]\ns_nop 1\nv_cvt_pkrtz_f16_f32 v24, v12, v13\nv_cvt_pkrtz_f16_f32 v25, v14, v15\n" \
  "v_pk_add_u16 v24, v24, 4 op_sel_hi:[1,0] clamp\nv_pk_add_u16 v25, v25, 4 op_sel_hi:[1,0] clamp\nds_write_b64 v4, v[24:25] offset:5120\n" \
  "v_cvt_pkrtz_f16_f32 v24, v16, v17\nv_cvt_pkrtz_f16_f32 v25, v18, v19\nv_pk_add_u16 v24, v24, 4 op_sel_hi:[1,0] clamp\n" \
  "v_pk_add_u16 v25, v25, 4 op_sel_hi:[1,0] clamp\nds_write_b64 v4, v[24:25] offset:6144\nv_cvt_pkrtz_f16_f32 v24, v20, v21\n" \
  "v_cvt_pkrtz_f16_f32 v25, v22, v23\nv_pk_add_u16 v24, v24, 4 op_sel_hi:[1,0] clamp\nv_pk_add_u16 v25, v25, 4 op_sel_hi:[1,0] clamp\n" \
  "ds_write_b64 v4, v[24:25] offset:7168\ns_setprio 0\ns_setpc_b64 s[90:91]\n31:\ns_movk_i32 s84, 0x4000\n33:\nds_read_b32 v27, v9 offset:32\n" \
  "s_sub_u32 s84, s84, 1\ns_cbranch_scc1 32b\ns_waitcnt lgkmcnt(0)\nv_readfirstlane_b32 s85, v27\ns_cmp_lt_u32 s85, 4\ns_cbranch_scc1 33b\n" \
  "s_branch 32b\n9:\ns_cmp_eq_u32 s89, 3\ns_cbranch_scc1 3f\ns_cmp_eq_u32 s89, 4\ns_cbranch_scc1 4f\ns_cmp_eq_u32 s89, 5\ns_cbranch_scc1 5f\n" \
  "ds_read_b64 v[124:125], v3 offset:576\nds_read_b64 v[126:127], v3 offset:584\nds_read_b64 v[120:121], v3 offset:64\n" \
  "ds_read_b64 v[122:123], v3 offset:72\nds_read_b64 v[116:117], v3 offset:640\nds_read_b64 v[118:119], v3 offset:648\n" \
  "ds_read_b64 v[112:113], v3 offset:128\nds_read_b64 v[114:115], v3 offset:136\nds_read_b64 v[108:109], v3 offset:704\n" \
  "ds_read_b64 v[110:111], v3 offset:712\nds_read_b64 v[104:105], v3 offset:192\nds_read_b64 v[106:107], v3 offset:200\n" \
  "ds_read_b64 v[100:101], v3 offset:768\nds_read_b64 v[102:103], v3 offset:776\nds_read_b64 v[96:97], v3 offset:256\n" \
  "ds_read_b64 v[98:99], v3 offset:264\nds_read_b64 v[92:93], v3 offset:832\nds_read_b64 v[94:95], v3 offset:840\n" \
  "ds_read_b64 v[88:89], v3 offset:320\nds_read_b64 v[90:91], v3 offset:328\nds_read_b64 v[84:85], v3 offset:896\n" \
  "ds_read_b64 v[86:87], v3 offset:904\nds_read_b64 v[80:81], v3 offset:384\nds_read_b64 v[82:83], v3 offset:392\n" \
  "ds_read_b64 v[76:77], v3 offset:960\nds_read_b64 v[78:79], v3 offset:968\nds_read_b64 v[72:73], v3 offset:448\n" \
  "ds_read_b64 v[74:75], v3 offset:456\nds_read_b64 v[68:69], v3 offset:1024\nds_read_b64 v[70:71], v3 offset:1032\n" \
  "ds_read_b64 v[64:65], v3 offset:512\nds_read_b64 v[66:67], v3 offset:520\ns_waitcnt lgkmcnt(0)\ns_barrier\nds_read_b128 v[24:27], v2 offset:0\n" \
  "ds_read_b128 v[28:31], v2 offset:64\nds_read_b128 v[32:35], v2 offset:128\nds_read_b128 v[36:39], v2 offset:192\ns_waitcnt lgkmcnt(3)\n" \
  "v_mfma_f32_16x16x32_f16 v[12:15], v[124:127], v[24:27], 0\ns_setprio 1\nds_read_b128 v[40:43], v2 offset:256\n" \
  "v_mfma_f32_16x16x32_f16 v[16:19], v[120:123], v[24:27], 0\ns_waitcnt lgkmcnt(3)\nv_mfma_f32_16x16x32_f16 v[12:15], v[116:119], v[28:31], v[12:15]\n" \
  "ds_read_b128 v[44:47], v2 offset:320\nv_mfma_f32_16x16x32_f16 v[16:19], v[112:115], v[28:31], v[16:19]\ns_waitcnt lgkmcnt(3)\n" \
  "v_mfma_f32_16x16x32_f16 v[12:15], v[108:111], v[32:35], v[12:15]\nds_read_b128 v[48:51], v2 offset:384\n" \
  "v_mfma_f32_16x16x32_f16 v[16:19], v[104:107], v[32:35], v[16:19]\ns_waitcnt lgkmcnt(3)\n" \
  "v_mfma_f32_16x16x32_f16 v[12:15], v[100:103], v[36:39], v[12:15]\nds_read_b128 v[52:55], v2 offset:448\n" \
  "v_mfma_f32_16x16x32_f16 v[16:19], v[96:99], v[36:39], v[16:19]\ns_waitcnt lgkmcnt(3)\n" \
  "v_mfma_f32_16x16x32_f16 v[12:15], v[92:95], v[40:43], v[12:15]\nds_read_b128 v[56:59], v2 offset:512\n" \
  "v_mfma_f32_16x16x32_f16 v[16:19], v[88:91], v[40:43], v[16:19]\nds_read_b128 v[60:63], v2 offset:576\ns_waitcnt lgkmcnt(3)\n" \
  "v_mfma_f32_16x16x32_f16 v[12:15], v[84:87], v[44:47], v[12:15]\nds_read_b128 v[120:123], v2 offset:640\n" \
  "v_mfma_f32_16x16x32_f16 v[16:19], v[80:83], v[44:47], v[16:19]\nds_read_b128 v[24:27], v2 offset:704\n" \
  "v_mfma_f32_16x16x32_f16 v[12:15], v[76:79], v[48:51], v[12:15]\nds_read_b128 v[112:115], v2 offset:768\n" \
  "v_mfma_f32_16x16x32_f16 v[16:19], v[72:75], v[48:51], v[16:19]\nds_read_b128 v[28:31], v2 offset:832\ns_waitcnt lgkmcnt(5)\n" \
  "v_mfma_f32_16x16x32_f16 v[12:15], v[68:71], v[52:55], v[12:15]\nds_read_b128 v[104:107], v2 offset:896\n" \
  "v_mfma_f32_16x16x32_f16 v[16:19], v[64:67], v[52:55], v[16:19]\nds_read_b128 v[32:35], v2 offset:960\n" \
  "v_mfma_f32_16x16x32_f16 v[16:19], v[124:127], v[56:59], v[16:19]\ns_waitcnt lgkmcnt(5)\n" \
  "v_mfma_f32_16x16x32_f16 v[16:19], v[116:119], v[60:63], v[16:19]\nv_mfma_f32_16x16x32_f16 v[16:19], v[108:111], v[120:123], v[16:19]\n" \
  "s_waitcnt lgkmcnt(3)\nv_mfma_f32_16x16x32_f16 v[16:19], v[100:103], v[24:27], v[16:19]\nv_cvt_pkrtz_f16_f32 v20, v12, v13\n" \
  "v_cvt_pkrtz_f16_f32 v21, v14, v15\nv_mfma_f32_16x16x32_f16 v[16:19], v[92:95], v[112:115], v[16:19]\n" \
  "v_pk_add_u16 v20, v20, 4 op_sel_hi:[1,0] clamp\ns_waitcnt lgkmcnt(2)\nv_mfma_f32_16x16x32_f16 v[16:19], v[84:87], v[28:31], v[16:19]\n" \
  "v_pk_add_u16 v21, v21, 4 op_sel_hi:[1,0] clamp\ns_waitcnt lgkmcnt(1)\nv_mfma_f32_16x16x32_f16 v[16:19], v[76:79], v[104:107], v[16:19]\n" \
  "ds_write_b64 v4, v[20:21] offset:0\ns_waitcnt lgkmcnt(1)\nv_mfma_f32_16x16x32_f16 v[16:19], v[68:71], v[32:35], v[16:19]\ns_nop 7\n" \
  "v_cvt_pkrtz_f16_f32 v20, v16, v17\nv_cvt_pkrtz_f16_f32 v21, v18, v19\nv_pk_add_u16 v20, v20, 4 op_sel_hi:[1,0] clamp\n" \
  "v_pk_add_u16 v21, v21, 4 op_sel_hi:[1,0] clamp\nds_write_b64 v4, v[20:21] offset:1024\ns_setprio 0\ns_setpc_b64 s[90:91]\n3:\n" \
  "ds_read_b64 v[124:125], v3 offset:0\nds_read_b64 v[126:127], v3 offset:8\nds_read_b64 v[120:121], v3 offset:64\n" \
  "ds_read_b64 v[122:123], v3 offset:72\nds_read_b64 v[116:117], v3 offset:128\nds_read_b64 v[118:119], v3 offset:136\n" \
  "ds_read_b64 v[112:113], v3 offset:192\nds_read_b64 v[114:115], v3 offset:200\nds_read_b64 v[108:109], v3 offset:256\n" \
  "ds_read_b64 v[110:111], v3 offset:264\nds_read_b64 v[104:105], v3 offset:320\nds_read_b64 v[106:107], v3 offset:328\n" \
  "ds_read_b64 v[100:101], v3 offset:384\nds_read_b64 v[102:103], v3 offset:392\nds_read_b64 v[96:97], v3 offset:448\n" \
  "ds_read_b64 v[98:99], v3 offset:456\nds_read_b64 v[92:93], v3 offset:512\nds_read_b64 v[94:95], v3 offset:520\n" \
  "ds_read_b64 v[88:89], v3 offset:576\nds_read_b64 v[90:91], v3 offset:584\nds_read_b64 v[84:85], v3 offset:640\n" \
  "ds_read_b64 v[86:87], v3 offset:648\nds_read_b64 v[80:81], v3 offset:704\nds_read_b64 v[82:83], v3 offset:712\n" \
  "ds_read_b64 v[76:77], v3 offset:768\nds_read_b64 v[78:79], v3 offset:776\nds_read_b64 v[72:73], v3 offset:832\n" \
  "ds_read_b64 v[74:75], v3 offset:840\nds_read_b64 v[68:69], v3 offset:896\nds_read_b64 v[70:71], v3 offset:904\n" \
  "ds_read_b64 v[64:65], v3 offset:960\nds_read_b64 v[66:67], v3 offset:968\nds_read_b64 v[60:61], v3 offset:1024\n" \
  "ds_read_b64 v[62:63], v3 offset:1032\nds_read_b64 v[56:57], v6 offset:256\nds_read_b64 v[58:59], v6 offset:264\n" \
  "ds_read_b64 v[52:53], v6 offset:128\nds_read_b64 v[54:55], v6 offset:136\nds_read_b64 v[48:49], v6 offset:320\n" \
  "ds_read_b64 v[50:51], v6 offset:328\nds_read_b64 v[44:45], v6 offset:192\nds_read_b64 v[46:47], v6 offset:200\ns_waitcnt lgkmcnt(0)\ns_barrier\n" \
  "ds_read_b128 v[24:27], v2 offset:448\nds_read_b128 v[28:31], v2 offset:512\nds_read_b128 v[32:35], v2 offset:576\n" \
  "ds_read_b128 v[36:39], v2 offset:640\nds_read_b128 v[40:43], v2 offset:704\ns_waitcnt lgkmcnt(3)\n" \
  "v_mfma_f32_16x16x32_f16 v[12:15], v[124:127], v[24:27], 0\ns_setprio 1\nds_read_b128 v[24:27], v2 offset:768\n" \
  "v_mfma_f32_16x16x32_f16 v[12:15], v[120:123], v[28:31], v[12:15]\nds_read_b128 v[28:31], v2 offset:832\ns_waitcnt lgkmcnt(3)\n" \
  "v_mfma_f32_16x16x32_f16 v[12:15], v[116:119], v[32:35], v[12:15]\nds_read_b128 v[32:35], v2 offset:896\n" \
  "v_mfma_f32_16x16x32_f16 v[12:15], v[112:115], v[36:39], v[12:15]\nds_read_b128 v[36:39], v2 offset:960\ns_waitcnt lgkmcnt(3)\n" \
  "v_mfma_f32_16x16x32_f16 v[12:15], v[108:111], v[40:43], v[12:15]\nds_read_b128 v[40:43], v2 offset:1024\n" \
  "v_mfma_f32_16x16x32_f16 v[12:15], v[104:107], v[24:27], v[12:15]\nds_read_b128 v[24:27], v2 offset:1088\ns_waitcnt lgkmcnt(3)\n" \
  "v_mfma_f32_16x16x32_f16 v[12:15], v[100:103], v[28:31], v[12:15]\nds_read_b128 v[28:31], v2 offset:1152\n" \
  "v_mfma_f32_16x16x32_f16 v[12:15], v[96:99], v[32:35], v[12:15]\ns_waitcnt lgkmcnt(3)\n" \
  "v_mfma_f32_16x16x32_f16 v[12:15], v[92:95], v[36:39], v[12:15]\nds_read_b128 v[32:35], v2 offset:1216\n" \
  "v_mfma_f32_16x16x32_f16 v[16:19], v[124:127], v[36:39], 0\ns_waitcnt lgkmcnt(3)\nv_mfma_f32_16x16x32_f16 v[12:15], v[88:91], v[40:43], v[12:15]\n" \
  "ds_read_b128 v[124:127], v2 offset:1280\nv_mfma_f32_16x16x32_f16 v[16:19], v[120:123], v[40:43], v[16:19]\ns_waitcnt lgkmcnt(3)\n" \
  "v_mfma_f32_16x16x32_f16 v[12:15], v[84:87], v[24:27], v[12:15]\nds_read_b128 v[36:39], v2 offset:1344\n" \
  "v_mfma_f32_16x16x32_f16 v[16:19], v[116:119], v[24:27], v[16:19]\ns_waitcnt lgkmcnt(3)\n" \
  "v_mfma_f32_16x16x32_f16 v[12:15], v[80:83], v[28:31], v[12:15]\nds_read_b128 v[120:123], v2 offset:1408\n" \
  "v_mfma_f32_16x16x32_f16 v[16:19], v[112:115], v[28:31], v[16:19]\ns_waitcnt lgkmcnt(3)\n" \
  "v_mfma_f32_16x16x32_f16 v[12:15], v[76:79], v[32:35], v[12:15]\nds_read_b128 v[40:43], v2 offset:1472\n" \
  "v_mfma_f32_16x16x32_f16 v[16:19], v[108:111], v[32:35], v[16:19]\ns_waitcnt lgkmcnt(3)\n" \
  "v_mfma_f32_16x16x32_f16 v[12:15], v[72:75], v[124:127], v[12:15]\nds_read_b128 v[116:119], v2 offset:1536\n" \
  "v_mfma_f32_16x16x32_f16 v[16:19], v[104:107], v[124:127], v[16:19]\nds_read_b128 v[24:27], v2 offset:1600\ns_waitcnt lgkmcnt(3)\n" \
  "v_mfma_f32_16x16x32_f16 v[12:15], v[68:71], v[36:39], v[12:15]\nds_read_b128 v[112:115], v2 offset:1664\nds_read_b32 v23, v9 offset:32\n" \
  "v_mfma_f32_16x16x32_f16 v[16:19], v[100:103], v[36:39], v[16:19]\nds_read_b128 v[28:31], v2 offset:1728\n" \
  "v_mfma_f32_16x16x32_f16 v[12:15], v[64:67], v[120:123], v[12:15]\nds_read_b128 v[108:111], v2 offset:1792\n" \
  "v_mfma_f32_16x16x32_f16 v[16:19], v[96:99], v[120:123], v[16:19]\nds_read_b128 v[32:35], v2 offset:1856\ns_waitcnt lgkmcnt(6)\n" \
  "v_mfma_f32_16x16x32_f16 v[12:15], v[60:63], v[40:43], v[12:15]\nds_read_b128 v[104:107], v2 offset:1920\n" \
  "v_mfma_f32_16x16x32_f16 v[16:19], v[92:95], v[40:43], v[16:19]\nds_read_b128 v[124:127], v2 offset:1984\ns_waitcnt lgkmcnt(5)\n" \
  "v_readfirstlane_b32 s85, v23\ns_cmp_lt_u32 s85, 3\ns_cbranch_scc1 51f\n52:\nv_mfma_f32_16x16x32_f16 v[16:19], v[88:91], v[116:119], v[16:19]\n" \
  "ds_read_b64_tr_b16 v[100:101], v5 offset:0\nds_read_b64_tr_b16 v[102:103], v5 offset:32\n" \
  "v_mfma_f32_16x16x32_f16 v[16:19], v[84:87], v[24:27], v[16:19]\nds_read_b64_tr_b16 v[36:37], v5 offset:256\n" \
  "ds_read_b64_tr_b16 v[38:39], v5 offset:288\nv_mfma_f32_16x16x32_f16 v[16:19], v[80:83], v[112:115], v[16:19]\n" \
  "ds_read_b64_tr_b16 v[96:97], v5 offset:512\nds_read_b64_tr_b16 v[98:99], v5 offset:544\ns_waitcnt lgkmcnt(9)\n" \
  "v_mfma_f32_16x16x32_f16 v[16:19], v[76:79], v[28:31], v[16:19]\nds_read_b64_tr_b16 v[120:121], v5 offset:768\n" \
  "ds_read_b64_tr_b16 v[122:123], v5 offset:800\nv_mfma_f32_16x16x32_f16 v[16:19], v[72:75], v[108:111], v[16:19]\ns_waitcnt lgkmcnt(9)\n" \
  "v_mfma_f32_16x16x32_f16 v[16:19], v[68:71], v[32:35], v[16:19]\nv_mfma_f32_16x16x32_f16 v[16:19], v[64:67], v[104:107], v[16:19]\n" \
  "s_waitcnt lgkmcnt(6)\nv_mfma_f32_16x16x32_f16 v[16:19], v[60:63], v[124:127], v[16:19]\n" \
  "v_mfma_f32_16x16x32_f16 v[12:15], v[100:103], v[56:59], v[12:15]\nv_mfma_f32_16x16x32_f16 v[16:19], v[100:103], v[52:55], v[16:19]\n" \
  "s_waitcnt lgkmcnt(2)\nv_mfma_f32_16x16x32_f16 v[12:15], v[36:39], v[48:51], v[12:15]\n" \
  "v_mfma_f32_16x16x32_f16 v[16:19], v[36:39], v[44:47], v[16:19]\nv_mfma_f32_16x16x32_f16 v[16:19], v[96:99], v[56:59], v[16:19]\n" \
  "s_waitcnt lgkmcnt(0)\nv_mfma_f32_16x16x32_f16 v[16:19], v[120:123], v[48:51], v[16:19]\ns_nop 3\nv_cvt_pkrtz_f16_f32 v20, v12, v13\n" \
  "v_cvt_pkrtz_f16_f32 v21, v14, v15\nv_pk_add_u16 v20, v20, 4 op_sel_hi:[1,0] clamp\nv_pk_add_u16 v21, v21, 4 op_sel_hi:[1,0] clamp\n" \
  "ds_write_b64 v4, v[20:21] offset:2048\nv_cvt_pkrtz_f16_f32 v20, v16, v17\nv_cvt_pkrtz_f16_f32 v21, v18, v19\n" \
  "v_pk_add_u16 v20, v20, 4 op_sel_hi:[1,0] clamp\nv_pk_add_u16 v21, v21, 4 op_sel_hi:[1,0] clamp\nds_write_b64 v4, v[20:21] offset:3072\n" \
  "s_setprio 0\ns_setpc_b64 s[90:91]\n51:\ns_movk_i32 s84, 0x4000\n53:\nds_read_b32 v23, v9 offset:32\ns_sub_u32 s84, s84, 1\ns_cbranch_scc1 52b\n" \
  "s_waitcnt lgkmcnt(0)\nv_readfirstlane_b32 s85, v23\ns_cmp_lt_u32 s85, 3\ns_cbranch_scc1 53b\ns_branch 52b\n4:\n" \
  "ds_read_b64 v[124:125], v3 offset:1088\nds_read_b64 v[126:127], v3 offset:1096\nds_read_b64 v[120:121], v3 offset:0\n" \
  "ds_read_b64 v[122:123], v3 offset:8\nds_read_b64 v[116:117], v3 offset:64\nds_read_b64 v[118:119], v3 offset:72\n" \
  "ds_read_b64 v[112:113], v3 offset:128\nds_read_b64 v[114:115], v3 offset:136\nds_read_b64 v[108:109], v3 offset:192\n" \
  "ds_read_b64 v[110:111], v3 offset:200\nds_read_b64 v[104:105], v3 offset:256\nds_read_b64 v[106:107], v3 offset:264\n" \
  "ds_read_b64 v[100:101], v3 offset:320\nds_read_b64 v[102:103], v3 offset:328\nds_read_b64 v[96:97], v3 offset:384\n" \
  "ds_read_b64 v[98:99], v3 offset:392\nds_read_b64 v[92:93], v3 offset:448\nds_read_b64 v[94:95], v3 offset:456\n" \
  "ds_read_b64 v[88:89], v3 offset:512\nds_read_b64 v[90:91], v3 offset:520\nds_read_b64 v[84:85], v3 offset:576\n" \
  "ds_read_b64 v[86:87], v3 offset:584\nds_read_b64 v[80:81], v3 offset:640\nds_read_b64 v[82:83], v3 offset:648\n" \
  "ds_read_b64 v[76:77], v3 offset:704\nds_read_b64 v[78:79], v3 offset:712\nds_read_b64 v[72:73], v3 offset:768\n" \
  "ds_read_b64 v[74:75], v3 offset:776\nds_read_b64 v[68:69], v3 offset:832\nds_read_b64 v[70:71], v3 offset:840\n" \
  "ds_read_b64 v[64:65], v3 offset:896\nds_read_b64 v[66:67], v3 offset:904\nds_read_b64 v[60:61], v3 offset:960\n" \
  "ds_read_b64 v[62:63], v3 offset:968\nds_read_b64 v[56:57], v3 offset:1024\nds_read_b64 v[58:59], v3 offset:1032\n" \
  "ds_read_b64 v[52:53], v6 offset:0\nds_read_b64 v[54:55], v6 offset:8\nds_read_b64 v[48:49], v6 offset:64\nds_read_b64 v[50:51], v6 offset:72\n" \
  "ds_read_b64 v[44:45], v6 offset:128\nds_read_b64 v[46:47], v6 offset:136\nds_read_b64 v[40:41], v6 offset:192\n" \
  "ds_read_b64 v[42:43], v6 offset:200\ns_waitcnt lgkmcnt(0)\ns_barrier\nds_read_b128 v[24:27], v2 offset:448\nds_read_b128 v[28:31], v2 offset:960\n" \
  "ds_read_b128 v[32:35], v2 offset:1472\nds_read_b128 v[36:39], v2 offset:1536\ns_waitcnt lgkmcnt(3)\n" \
  "v_mfma_f32_16x16x32_f16 v[12:15], v[124:127], v[24:27], 0\ns_setprio 1\nds_read_b128 v[24:27], v2 offset:1600\ns_waitcnt lgkmcnt(3)\n" \
  "v_mfma_f32_16x16x32_f16 v[16:19], v[124:127], v[28:31], 0\nds_read_b128 v[124:127], v2 offset:1664\ns_waitcnt lgkmcnt(3)\n" \
  "v_mfma_f32_16x16x32_f16 v[12:15], v[120:123], v[32:35], v[12:15]\nds_read_b128 v[28:31], v2 offset:1728\ns_waitcnt lgkmcnt(3)\n" \
  "v_mfma_f32_16x16x32_f16 v[12:15], v[116:119], v[36:39], v[12:15]\nds_read_b128 v[32:35], v2 offset:1792\ns_waitcnt lgkmcnt(3)\n" \
  "v_mfma_f32_16x16x32_f16 v[12:15], v[112:115], v[24:27], v[12:15]\nds_read_b128 v[36:39], v2 offset:1856\ns_waitcnt lgkmcnt(3)\n" \
  "v_mfma_f32_16x16x32_f16 v[12:15], v[108:111], v[124:127], v[12:15]\nds_read_b128 v[24:27], v2 offset:1920\ns_waitcnt lgkmcnt(3)\n" \
  "v_mfma_f32_16x16x32_f16 v[12:15], v[104:107], v[28:31], v[12:15]\nds_read_b128 v[124:127], v2 offset:1984\ns_waitcnt lgkmcnt(3)\n" \
  "v_mfma_f32_16x16x32_f16 v[12:15], v[100:103], v[32:35], v[12:15]\nds_read_b128 v[28:31], v2 offset:2048\ns_waitcnt lgkmcnt(3)\n" \
  "v_mfma_f32_16x16x32_f16 v[12:15], v[96:99], v[36:39], v[12:15]\nds_read_b128 v[32:35], v2 offset:2112\ns_waitcnt lgkmcnt(3)\n" \
  "v_mfma_f32_16x16x32_f16 v[12:15], v[92:95], v[24:27], v[12:15]\nds_read_b128 v[36:39], v2 offset:2176\ns_waitcnt lgkmcnt(3)\n" \
  "v_mfma_f32_16x16x32_f16 v[12:15], v[88:91], v[124:127], v[12:15]\nds_read_b128 v[24:27], v2 offset:2240\n" \
  "v_mfma_f32_16x16x32_f16 v[16:19], v[120:123], v[124:127], v[16:19]\nds_read_b64 v[120:121], v6 offset:256\nds_read_b64 v[122:123], v6 offset:264\n" \
  "s_waitcnt lgkmcnt(4)\nv_mfma_f32_16x16x32_f16 v[12:15], v[84:87], v[28:31], v[12:15]\nds_read_b128 v[124:127], v2 offset:2304\n" \
  "v_mfma_f32_16x16x32_f16 v[16:19], v[116:119], v[28:31], v[16:19]\nds_read_b64 v[116:117], v6 offset:320\nds_read_b64 v[118:119], v6 offset:328\n" \
  "v_mfma_f32_16x16x32_f16 v[12:15], v[80:83], v[32:35], v[12:15]\nds_read_b128 v[28:31], v2 offset:2368\n" \
  "v_mfma_f32_16x16x32_f16 v[16:19], v[112:115], v[32:35], v[16:19]\ns_waitcnt lgkmcnt(6)\n" \
  "v_mfma_f32_16x16x32_f16 v[12:15], v[76:79], v[36:39], v[12:15]\nds_read_b128 v[112:115], v2 offset:2432\n" \
  "v_mfma_f32_16x16x32_f16 v[16:19], v[108:111], v[36:39], v[16:19]\nds_read2_b32 v[32:33], v8 offset0:48 offset1:49\n" \
  "ds_read2_b32 v[34:35], v8 offset0:50 offset1:51\nv_mfma_f32_16x16x32_f16 v[12:15], v[72:75], v[24:27], v[12:15]\n" \
  "ds_read_b128 v[108:111], v2 offset:2496\nv_mfma_f32_16x16x32_f16 v[16:19], v[104:107], v[24:27], v[16:19]\n" \
  "ds_read2_b32 v[36:37], v8 offset0:32 offset1:33\nds_read2_b32 v[38:39], v8 offset0:34 offset1:35\ns_waitcnt lgkmcnt(6)\n" \
  "v_mfma_f32_16x16x32_f16 v[12:15], v[68:71], v[124:127], v[12:15]\nds_read_b128 v[104:107], v2 offset:2560\n" \
  "v_mfma_f32_16x16x32_f16 v[16:19], v[100:103], v[124:127], v[16:19]\nds_read_b128 v[24:27], v2 offset:2624\n" \
  "v_mfma_f32_16x16x32_f16 v[12:15], v[64:67], v[28:31], v[12:15]\nds_read_b128 v[100:103], v2 offset:2688\nds_read_b32 v23, v9 offset:32\n" \
  "v_mfma_f32_16x16x32_f16 v[16:19], v[96:99], v[28:31], v[16:19]\nds_read_b128 v[124:127], v2 offset:2752\ns_waitcnt lgkmcnt(7)\n" \
  "v_mfma_f32_16x16x32_f16 v[12:15], v[60:63], v[112:115], v[12:15]\nds_read_b128 v[96:99], v2 offset:2816\n" \
  "v_mfma_f32_16x16x32_f16 v[16:19], v[92:95], v[112:115], v[16:19]\nds_read_b128 v[28:31], v2 offset:2880\n" \
  "v_mfma_f32_16x16x32_f16 v[12:15], v[56:59], v[108:111], v[12:15]\nds_read_b128 v[92:95], v2 offset:2944\n" \
  "v_mfma_f32_16x16x32_f16 v[16:19], v[88:91], v[108:111], v[16:19]\nds_read_b128 v[112:115], v2 offset:3008\ns_waitcnt lgkmcnt(5)\n" \
  "v_readfirstlane_b32 s85, v23\ns_cmp_lt_u32 s85, 3\ns_cbranch_scc1 61f\n62:\nv_mfma_f32_16x16x32_f16 v[16:19], v[84:87], v[104:107], v[16:19]\n" \
  "ds_read_b64_tr_b16 v[88:89], v5 offset:0\nds_read_b64_tr_b16 v[90:91], v5 offset:32\n" \
  "v_mfma_f32_16x16x32_f16 v[16:19], v[80:83], v[24:27], v[16:19]\nds_read_b64_tr_b16 v[108:109], v5 offset:256\n" \
  "ds_read_b64_tr_b16 v[110:111], v5 offset:288\nv_mfma_f32_16x16x32_f16 v[16:19], v[76:79], v[100:103], v[16:19]\n" \
  "ds_read_b64_tr_b16 v[84:85], v5 offset:512\nds_read_b64_tr_b16 v[86:87], v5 offset:544\ns_waitcnt lgkmcnt(9)\n" \
  "v_mfma_f32_16x16x32_f16 v[16:19], v[72:75], v[124:127], v[16:19]\nds_read_b64_tr_b16 v[104:105], v5 offset:768\n" \
  "ds_read_b64_tr_b16 v[106:107], v5 offset:800\nv_mfma_f32_16x16x32_f16 v[16:19], v[68:71], v[96:99], v[16:19]\n" \
  "ds_read_b64_tr_b16 v[80:81], v5 offset:1024\nds_read_b64_tr_b16 v[82:83], v5 offset:1056\ns_waitcnt lgkmcnt(11)\n" \
  "v_mfma_f32_16x16x32_f16 v[16:19], v[64:67], v[28:31], v[16:19]\nds_read_b64_tr_b16 v[24:25], v5 offset:1280\n" \
  "ds_read_b64_tr_b16 v[26:27], v5 offset:1312\nv_mfma_f32_16x16x32_f16 v[16:19], v[60:63], v[92:95], v[16:19]\n" \
  "ds_read_b64_tr_b16 v[76:77], v5 offset:1536\nds_read_b64_tr_b16 v[78:79], v5 offset:1568\ns_waitcnt lgkmcnt(12)\n" \
  "v_mfma_f32_16x16x32_f16 v[16:19], v[56:59], v[112:115], v[16:19]\nds_read_b64_tr_b16 v[100:101], v5 offset:1792\n" \
  "ds_read_b64_tr_b16 v[102:103], v5 offset:1824\nv_mfma_f32_16x16x32_f16 v[12:15], v[88:91], v[52:55], v[12:15]\ns_waitcnt lgkmcnt(10)\n" \
  "v_mfma_f32_16x16x32_f16 v[12:15], v[108:111], v[48:51], v[12:15]\nds_read_b64_tr_b16 v[72:73], v7 offset:0\n" \
  "ds_read_b64_tr_b16 v[74:75], v7 offset:64\nv_mfma_f32_16x16x32_f16 v[12:15], v[84:87], v[44:47], v[12:15]\n" \
  "ds_read_b64_tr_b16 v[124:125], v7 offset:512\nds_read_b64_tr_b16 v[126:127], v7 offset:576\n" \
  "v_mfma_f32_16x16x32_f16 v[16:19], v[84:87], v[52:55], v[16:19]\ns_waitcnt lgkmcnt(10)\n" \
  "v_mfma_f32_16x16x32_f16 v[12:15], v[104:107], v[40:43], v[12:15]\nv_mfma_f32_16x16x32_f16 v[16:19], v[104:107], v[48:51], v[16:19]\n" \
  "v_mfma_f32_16x16x32_f16 v[12:15], v[80:83], v[120:123], v[12:15]\nv_mfma_f32_16x16x32_f16 v[16:19], v[80:83], v[44:47], v[16:19]\n" \
  "s_waitcnt lgkmcnt(6)\nv_mfma_f32_16x16x32_f16 v[12:15], v[24:27], v[116:119], v[12:15]\n" \
  "v_mfma_f32_16x16x32_f16 v[16:19], v[24:27], v[40:43], v[16:19]\nv_mfma_f32_16x16x32_f16 v[16:19], v[76:79], v[120:123], v[16:19]\n" \
  "s_waitcnt lgkmcnt(2)\nv_mfma_f32_16x16x32_f16 v[16:19], v[100:103], v[116:119], v[16:19]\n" \
  "v_mfma_f32_16x16x32_f16 v[12:15], v[72:75], v[32:35], v[12:15]\nv_mfma_f32_16x16x32_f16 v[16:19], v[72:75], v[36:39], v[16:19]\n" \
  "s_waitcnt lgkmcnt(0)\nv_mfma_f32_16x16x32_f16 v[16:19], v[124:127], v[32:35], v[16:19]\ns_nop 4\nv_cvt_pkrtz_f16_f32 v20, v12, v13\n" \
  "v_cvt_pkrtz_f16_f32 v21, v14, v15\nv_pk_add_u16 v20, v20, 4 op_sel_hi:[1,0] clamp\nv_pk_add_u16 v21, v21, 4 op_sel_hi:[1,0] clamp\n" \
  "ds_write_b64 v4, v[20:21] offset:4096\nv_cvt_pkrtz_f16_f32 v20, v16, v17\nv_cvt_pkrtz_f16_f32 v21, v18, v19\n" \
  "v_pk_add_u16 v20, v20, 4 op_sel_hi:[1,0] clamp\nv_pk_add_u16 v21, v21, 4 op_sel_hi:[1,0] clamp\nds_write_b64 v4, v[20:21] offset:5120\n" \
  "s_setprio 0\ns_setpc_b64 s[90:91]\n61:\ns_movk_i32 s84, 0x4000\n63:\nds_read_b32 v23, v9 offset:32\ns_sub_u32 s84, s84, 1\ns_cbranch_scc1 62b\n" \
  "s_waitcnt lgkmcnt(0)\nv_readfirstlane_b32 s85, v23\ns_cmp_lt_u32 s85, 3\ns_cbranch_scc1 63b\ns_branch 62b\n5:\n" \
  "ds_read_b64 v[124:125], v3 offset:1088\nds_read_b64 v[126:127], v3 offset:1096\nds_read_b64 v[120:121], v3 offset:0\n" \
  "ds_read_b64 v[122:123], v3 offset:8\nds_read_b64 v[116:117], v3 offset:64\nds_read_b64 v[118:119], v3 offset:72\n" \
  "ds_read_b64 v[112:113], v3 offset:128\nds_read_b64 v[114:115], v3 offset:136\nds_read_b64 v[108:109], v3 offset:192\n" \
  "ds_read_b64 v[110:111], v3 offset:200\nds_read_b64 v[104:105], v3 offset:256\nds_read_b64 v[106:107], v3 offset:264\n" \
  "ds_read_b64 v[100:101], v3 offset:320\nds_read_b64 v[102:103], v3 offset:328\nds_read_b64 v[96:97], v3 offset:384\n" \
  "ds_read_b64 v[98:99], v3 offset:392\nds_read_b64 v[92:93], v3 offset:448\nds_read_b64 v[94:95], v3 offset:456\n" \
  "ds_read_b64 v[88:89], v3 offset:512\nds_read_b64 v[90:91], v3 offset:520\nds_read_b64 v[84:85], v3 offset:576\n" \
  "ds_read_b64 v[86:87], v3 offset:584\nds_read_b64 v[80:81], v3 offset:640\nds_read_b64 v[82:83], v3 offset:648\n" \
  "ds_read_b64 v[76:77], v3 offset:704\nds_read_b64 v[78:79], v3 offset:712\nds_read_b64 v[72:73], v3 offset:768\n" \
  "ds_read_b64 v[74:75], v3 offset:776\nds_read_b64 v[68:69], v3 offset:832\nds_read_b64 v[70:71], v3 offset:840\n" \
  "ds_read_b64 v[64:65], v3 offset:896\nds_read_b64 v[66:67], v3 offset:904\nds_read_b64 v[60:61], v3 offset:960\n" \
  "ds_read_b64 v[62:63], v3 offset:968\nds_read_b64 v[56:57], v3 offset:1024\nds_read_b64 v[58:59], v3 offset:1032\n" \
  "ds_read_b64 v[52:53], v6 offset:0\nds_read_b64 v[54:55], v6 offset:8\nds_read_b64 v[48:49], v6 offset:64\nds_read_b64 v[50:51], v6 offset:72\n" \
  "ds_read_b64 v[44:45], v6 offset:128\nds_read_b64 v[46:47], v6 offset:136\nds_read_b64 v[40:41], v6 offset:192\n" \
  "ds_read_b64 v[42:43], v6 offset:200\ns_waitcnt lgkmcnt(0)\ns_barrier\nds_read_b128 v[24:27], v2 offset:1472\n" \
  "ds_read_b128 v[28:31], v2 offset:1984\nds_read_b128 v[32:35], v2 offset:2496\nds_read_b128 v[36:39], v2 offset:2560\ns_waitcnt lgkmcnt(3)\n" \
  "v_mfma_f32_16x16x32_f16 v[12:15], v[124:127], v[24:27], 0\ns_setprio 1\nds_read_b128 v[24:27], v2 offset:2624\ns_waitcnt lgkmcnt(3)\n" \
  "v_mfma_f32_16x16x32_f16 v[16:19], v[124:127], v[28:31], 0\nds_read_b128 v[124:127], v2 offset:2688\ns_waitcnt lgkmcnt(3)\n" \
  "v_mfma_f32_16x16x32_f16 v[12:15], v[120:123], v[32:35], v[12:15]\nds_read_b128 v[28:31], v2 offset:2752\ns_waitcnt lgkmcnt(3)\n" \
  "v_mfma_f32_16x16x32_f16 v[12:15], v[116:119], v[36:39], v[12:15]\nds_read_b128 v[32:35], v2 offset:2816\ns_waitcnt lgkmcnt(3)\n" \
  "v_mfma_f32_16x16x32_f16 v[12:15], v[112:115], v[24:27], v[12:15]\nds_read_b128 v[36:39], v2 offset:2880\ns_waitcnt lgkmcnt(3)\n" \
  "v_mfma_f32_16x16x32_f16 v[12:15], v[108:111], v[124:127], v[12:15]\nds_read_b128 v[24:27], v2 offset:2944\ns_waitcnt lgkmcnt(3)\n" \
  "v_mfma_f32_16x16x32_f16 v[12:15], v[104:107], v[28:31], v[12:15]\nds_read_b128 v[124:127], v2 offset:3008\ns_waitcnt lgkmcnt(3)\n" \
  "v_mfma_f32_16x16x32_f16 v[12:15], v[100:103], v[32:35], v[12:15]\nds_read_b128 v[28:31], v2 offset:3072\ns_waitcnt lgkmcnt(3)\n" \
  "v_mfma_f32_16x16x32_f16 v[12:15], v[96:99], v[36:39], v[12:15]\nds_read_b128 v[32:35], v2 offset:3136\ns_waitcnt lgkmcnt(3)\n" \
  "v_mfma_f32_16x16x32_f16 v[12:15], v[92:95], v[24:27], v[12:15]\nds_read_b128 v[36:39], v2 offset:3200\ns_waitcnt lgkmcnt(3)\n" \
  "v_mfma_f32_16x16x32_f16 v[12:15], v[88:91], v[124:127], v[12:15]\nds_read_b128 v[24:27], v2 offset:3264\n" \
  "v_mfma_f32_16x16x32_f16 v[16:19], v[120:123], v[124:127], v[16:19]\nds_read_b64 v[120:121], v6 offset:256\nds_read_b64 v[122:123], v6 offset:264\n" \
  "s_waitcnt lgkmcnt(4)\nv_mfma_f32_16x16x32_f16 v[12:15], v[84:87], v[28:31], v[12:15]\nds_read_b128 v[124:127], v2 offset:3328\n" \
  "v_mfma_f32_16x16x32_f16 v[16:19], v[116:119], v[28:31], v[16:19]\nds_read_b64 v[116:117], v6 offset:320\nds_read_b64 v[118:119], v6 offset:328\n" \
  "v_mfma_f32_16x16x32_f16 v[12:15], v[80:83], v[32:35], v[12:15]\nds_read_b128 v[28:31], v2 offset:3392\n" \
  "v_mfma_f32_16x16x32_f16 v[16:19], v[112:115], v[32:35], v[16:19]\ns_waitcnt lgkmcnt(6)\n" \
  "v_mfma_f32_16x16x32_f16 v[12:15], v[76:79], v[36:39], v[12:15]\nds_read_b128 v[112:115], v2 offset:3456\n" \
  "v_mfma_f32_16x16x32_f16 v[16:19], v[108:111], v[36:39], v[16:19]\nds_read2_b32 v[32:33], v8 offset0:16 offset1:17\n" \
  "ds_read2_b32 v[34:35], v8 offset0:18 offset1:19\nv_mfma_f32_16x16x32_f16 v[12:15], v[72:75], v[24:27], v[12:15]\n" \
  "ds_read_b128 v[108:111], v2 offset:3520\nv_mfma_f32_16x16x32_f16 v[16:19], v[104:107], v[24:27], v[16:19]\n" \
  "ds_read2_b32 v[36:37], v8 offset0:0 offset1:1\nds_read2_b32 v[38:39], v8 offset0:2 offset1:3\ns_waitcnt lgkmcnt(6)\n" \
  "v_mfma_f32_16x16x32_f16 v[12:15], v[68:71], v[124:127], v[12:15]\nds_read_b128 v[104:107], v2 offset:3584\n" \
  "v_mfma_f32_16x16x32_f16 v[16:19], v[100:103], v[124:127], v[16:19]\nds_read_b128 v[24:27], v2 offset:3648\n" \
  "ds_read2_b32 v[100:101], v8 offset0:32 offset1:33\nds_read2_b32 v[102:103], v8 offset0:34 offset1:35\n" \
  "v_mfma_f32_16x16x32_f16 v[12:15], v[64:67], v[28:31], v[12:15]\nds_read_b128 v[124:127], v2 offset:3712\nds_read_b32 v23, v9 offset:32\n" \
  "v_mfma_f32_16x16x32_f16 v[16:19], v[96:99], v[28:31], v[16:19]\nds_read_b128 v[96:99], v2 offset:3776\ns_waitcnt lgkmcnt(9)\n" \
  "v_mfma_f32_16x16x32_f16 v[12:15], v[60:63], v[112:115], v[12:15]\nds_read_b128 v[28:31], v2 offset:3840\n" \
  "v_mfma_f32_16x16x32_f16 v[16:19], v[92:95], v[112:115], v[16:19]\nds_read_b128 v[92:95], v2 offset:3904\n" \
  "v_mfma_f32_16x16x32_f16 v[12:15], v[56:59], v[108:111], v[12:15]\nds_read_b128 v[112:115], v2 offset:3968\n" \
  "v_mfma_f32_16x16x32_f16 v[16:19], v[88:91], v[108:111], v[16:19]\nds_read_b128 v[88:91], v2 offset:4032\ns_waitcnt lgkmcnt(5)\n" \
  "v_readfirstlane_b32 s85, v23\ns_cmp_lt_u32 s85, 3\ns_cbranch_scc1 71f\n72:\nv_mfma_f32_16x16x32_f16 v[16:19], v[84:87], v[104:107], v[16:19]\n" \
  "ds_read_b64_tr_b16 v[108:109], v5 offset:1024\nds_read_b64_tr_b16 v[110:111], v5 offset:1056\n" \
  "v_mfma_f32_16x16x32_f16 v[16:19], v[80:83], v[24:27], v[16:19]\nds_read_b64_tr_b16 v[84:85], v5 offset:1280\n" \
  "ds_read_b64_tr_b16 v[86:87], v5 offset:1312\nv_mfma_f32_16x16x32_f16 v[16:19], v[76:79], v[124:127], v[16:19]\n" \
  "ds_read_b64_tr_b16 v[104:105], v5 offset:1536\nds_read_b64_tr_b16 v[106:107], v5 offset:1568\ns_waitcnt lgkmcnt(9)\n" \
  "v_mfma_f32_16x16x32_f16 v[16:19], v[72:75], v[96:99], v[16:19]\nds_read_b64_tr_b16 v[80:81], v5 offset:1792\n" \
  "ds_read_b64_tr_b16 v[82:83], v5 offset:1824\nv_mfma_f32_16x16x32_f16 v[16:19], v[68:71], v[28:31], v[16:19]\n" \
  "ds_read_b64_tr_b16 v[24:25], v5 offset:2048\nds_read_b64_tr_b16 v[26:27], v5 offset:2080\ns_waitcnt lgkmcnt(11)\n" \
  "v_mfma_f32_16x16x32_f16 v[16:19], v[64:67], v[92:95], v[16:19]\nds_read_b64_tr_b16 v[76:77], v5 offset:2304\n" \
  "ds_read_b64_tr_b16 v[78:79], v5 offset:2336\nv_mfma_f32_16x16x32_f16 v[16:19], v[60:63], v[112:115], v[16:19]\n" \
  "ds_read_b64_tr_b16 v[124:125], v5 offset:2560\nds_read_b64_tr_b16 v[126:127], v5 offset:2592\ns_waitcnt lgkmcnt(12)\n" \
  "v_mfma_f32_16x16x32_f16 v[16:19], v[56:59], v[88:91], v[16:19]\nds_read_b64_tr_b16 v[72:73], v5 offset:2816\n" \
  "ds_read_b64_tr_b16 v[74:75], v5 offset:2848\nv_mfma_f32_16x16x32_f16 v[12:15], v[108:111], v[52:55], v[12:15]\ns_waitcnt lgkmcnt(10)\n" \
  "v_mfma_f32_16x16x32_f16 v[12:15], v[84:87], v[48:51], v[12:15]\nds_read_b64_tr_b16 v[96:97], v7 offset:0\n" \
  "ds_read_b64_tr_b16 v[98:99], v7 offset:64\nv_mfma_f32_16x16x32_f16 v[12:15], v[104:107], v[44:47], v[12:15]\n" \
  "ds_read_b64_tr_b16 v[68:69], v7 offset:512\nds_read_b64_tr_b16 v[70:71], v7 offset:576\n" \
  "v_mfma_f32_16x16x32_f16 v[16:19], v[104:107], v[52:55], v[16:19]\ns_waitcnt lgkmcnt(10)\n" \
  "v_mfma_f32_16x16x32_f16 v[12:15], v[80:83], v[40:43], v[12:15]\nds_read_b64_tr_b16 v[28:29], v7 offset:1024\n" \
  "ds_read_b64_tr_b16 v[30:31], v7 offset:1088\nv_mfma_f32_16x16x32_f16 v[16:19], v[80:83], v[48:51], v[16:19]\n" \
  "ds_read2_b32 v[64:65], v8 offset0:48 offset1:49\nds_read2_b32 v[66:67], v8 offset0:50 offset1:51\n" \
  "v_mfma_f32_16x16x32_f16 v[12:15], v[24:27], v[120:123], v[12:15]\nv_mfma_f32_16x16x32_f16 v[16:19], v[24:27], v[44:47], v[16:19]\n" \
  "s_waitcnt lgkmcnt(10)\nv_mfma_f32_16x16x32_f16 v[12:15], v[76:79], v[116:119], v[12:15]\nds_read_b64_tr_b16 v[92:93], v7 offset:1536\n" \
  "ds_read_b64_tr_b16 v[94:95], v7 offset:1600\nv_mfma_f32_16x16x32_f16 v[16:19], v[76:79], v[40:43], v[16:19]\n" \
  "v_mfma_f32_16x16x32_f16 v[16:19], v[124:127], v[120:123], v[16:19]\ns_waitcnt lgkmcnt(8)\n" \
  "v_mfma_f32_16x16x32_f16 v[16:19], v[72:75], v[116:119], v[16:19]\nv_mfma_f32_16x16x32_f16 v[12:15], v[96:99], v[32:35], v[12:15]\n" \
  "v_mfma_f32_16x16x32_f16 v[16:19], v[96:99], v[36:39], v[16:19]\ns_waitcnt lgkmcnt(4)\n" \
  "v_mfma_f32_16x16x32_f16 v[12:15], v[68:71], v[100:103], v[12:15]\nv_mfma_f32_16x16x32_f16 v[16:19], v[68:71], v[32:35], v[16:19]\n" \
  "s_waitcnt lgkmcnt(2)\nv_mfma_f32_16x16x32_f16 v[12:15], v[28:31], v[64:67], v[12:15]\n" \
  "v_mfma_f32_16x16x32_f16 v[16:19], v[28:31], v[100:103], v[16:19]\ns_waitcnt lgkmcnt(0)\n" \
  "v_mfma_f32_16x16x32_f16 v[16:19], v[92:95], v[64:67], v[16:19]\ns_nop 4\nv_cvt_pkrtz_f16_f32 v20, v12, v13\nv_cvt_pkrtz_f16_f32 v21, v14, v15\n" \
  "v_pk_add_u16 v20, v20, 4 op_sel_hi:[1,0] clamp\nv_pk_add_u16 v21, v21, 4 op_sel_hi:[1,0] clamp\nds_write_b64 v4, v[20:21] offset:6144\n" \
  "v_cvt_pkrtz_f16_f32 v20, v16, v17\nv_cvt_pkrtz_f16_f32 v21, v18, v19\nv_pk_add_u16 v20, v20, 4 op_sel_hi:[1,0] clamp\n" \
  "v_pk_add_u16 v21, v21, 4 op_sel_hi:[1,0] clamp\nds_write_b64 v4, v[20:21] offset:7168\ns_setprio 0\ns_setpc_b64 s[90:91]\n71:\n" \
  "s_movk_i32 s84, 0x4000\n73:\nds_read_b32 v23, v9 offset:32\ns_sub_u32 s84, s84, 1\ns_cbranch_scc1 72b\ns_waitcnt lgkmcnt(0)\n" \
  "v_readfirstlane_b32 s85, v23\ns_cmp_lt_u32 s85, 3\ns_cbranch_scc1 73b\ns_branch 72b\n"
#define CONV_CLOB "memory", "scc", "s84", "s85", "s90", "s91", "v12", "v13", "v14", "v15", "v16", "v17", "v18", "v19", "v20", "v21", "v22", "v23", "v24", "v25", "v26", "v27", "v28", "v29", "v30", "v31", "v32", "v33", "v34", "v35", "v36", "v37", "v38", "v39", "v40", "v41", "v42", "v43", "v44", "v45", "v46", "v47", "v48", "v49", "v50", "v51", "v52", "v53", "v54", "v55", "v56", "v57", "v58", "v59", "v60", "v61", "v62", "v63", "v64", "v65", "v66", "v67", "v68", "v69", "v70", "v71", "v72", "v73", "v74", "v75", "v76", "v77", "v78", "v79", "v80", "v81", "v82", "v83", "v84", "v85", "v86", "v87", "v88", "v89", "v90", "v91", "v92", "v93", "v94", "v95", "v96", "v97", "v98", "v99", "v100", "v101", "v102", "v103", "v104", "v105", "v106", "v107", "v108", "v109", "v110", "v111", "v112", "v113", "v114", "v115", "v116", "v117", "v118", "v119", "v120", "v121", "v122", "v123", "v124", "v125", "v126", "v127"
constexpr int TDP = 1216, T_3 = 4 * TDP, T_4 = T_3 + 512, TB = T_4 + 320;
constexpr int C_U = 480, UB = C_U + 4096 + 96, C_G5 = 0, C_G3 = 3200, C_G9 = 6400, C_G7 = C_G9 + 2096, GB = C_G7 + 2080 + 112;
constexpr int O_FL = 2 * TB, O_UL = O_FL + 64, O_IM = O_UL + 2 * UB, O_ST = O_IM + 2 * GB;
constexpr int X_TB = 10240, X_UL = X_TB + TB, X_IM = X_UL + UB, X_ST = X_IM + GB;
constexpr int X_WR = X_IM + 128 * IP;
#define X_RW(w) (LW + 9216 * (2 + ((w) >> 1)) + 2560 * ((w) & 1))
constexpr int RP = 1216;
constexpr int LW = 36864 + 512, LA = LW, LM = LW + 36864;
constexpr int L_SC = LM + 2048, SMEM = O_IM + 256 * IP;
static_assert(L_SC + 1152 <= SMEM && O_ST + 8192 <= SMEM && X_IM + 128 * IP <= LM && X_ST + 8192 <= LM && UB % 128 == 64 && GB % 128 == 64 && SMEM <= 79872, "");
struct TOps { uint32_t xa, xb, ya, pd, pf, pg, pe; };
__device__ __forceinline__ void table_ops(uint32_t im, uint32_t tb, int ct, int lane, TOps& t) {
  const int j = lane & 15, kq = lane >> 4, q4 = (lane >> 2) & 3, p4 = lane & 3, jj = j + 16 * ct;
  const uint32_t sa = im + (uint32_t)(IP * (lane >> 2));
  t.xa = sa + (uint32_t)(I_X + 8 * p4); t.xb = sa + (uint32_t)(I_X + 32 + 8 * (p4 & 1)); t.ya = sa + (uint32_t)(32 * ct + 8 * p4);
  t.pd = tb + (uint32_t)(2 * (541 - (8 * jj + 4 * (kq & 1) + 256 * (kq >> 1))));
  t.pf = tb + (uint32_t)((kq & 1) ? T_4 + 2 * (63 - jj) : T_3 + 2 * (127 - 2 * jj));
  t.pg = t.pf - ((kq & 1) ? 64 : 2);
  t.pe = tb + (uint32_t)(p4 * (TDP + 2) + ((lane < 12) ? 2 * (32 - q4) : 2 * (572 + (q4 & 1))));
  asm volatile("" : "+v"(t.xa), "+v"(t.xb), "+v"(t.ya), "+v"(t.pd), "+v"(t.pf), "+v"(t.pg), "+v"(t.pe));
}
typedef __attribute__((address_space(3))) char* lptr;
#define LP(T, a) ((__attribute__((address_space(3))) T*)(uintptr_t)(a))
__device__ __forceinline__ uint32_t table_tile(const TOps& t, int ct, int lane, uint32_t evp, float dsk TSP) {
  u32x4 ev;
  f32x4 ad[2], af[2];
  asm volatile(T_ASM : "=&v"(ad[0]), "=&v"(ad[1]), "=&v"(af[0]), "=&v"(af[1]), "=&v"(ev) : "v"(t.xa), "v"(t.xb), "v"(t.ya), "v"(evp) : T_CLOB);
  const int e0 = (int)(ev[0] + ev[2]), e1 = (int)(ev[1] + ev[3]);
  const int em = min(max(max(max(e0, e1), (int)((__float_as_uint(dsk) >> 23) & 0xffu) - 133), -110), 115);
  const int ts = ((uint32_t)(em + 12) <= 8u) ? 1 : -3 - em;
  float s0 = __builtin_amdgcn_ldexpf(1.f, e0 - 29 + ts), s1 = __builtin_amdgcn_ldexpf(1.f, e1 - 29 + ts);
  float d0 = (ct == 0 && lane == 0) ? __builtin_amdgcn_ldexpf(dsk, ts) : 0.f;
  asm volatile("s_nop 1" : "+v"(ad[0]), "+v"(ad[1]), "+v"(af[0]), "+v"(af[1]), "+v"(s0), "+v"(s1), "+v"(d0));
  const f32x4 k = ad[0] * s0 + (f32x4){d0, 0.f, 0.f, 0.f} + ad[1] * s1, f = af[0] * s0 + af[1] * s1;
  auto pk = [](float a, float b) { uint32_t r; asm("v_cvt_pk_f16_f32 %0, %1, %2" : "=v"(r) : "v"(a), "v"(b)); return r; };
  asm volatile("s_nop 0" : : "v"(k), "v"(f));
  const uint32_t p32 = pk(k[3], k[2]), p10 = pk(k[1], k[0]), p21 = pk(k[2], k[1]), p01 = pk(f[0], f[1]), p23 = pk(f[2], f[3]);
  *LP(u32x2, t.pd + 3 * TDP + 6) = (u32x2){p32, p10};
  *LP(uint32_t, t.pd + TDP + 2) = p32; *LP(uint32_t, t.pd + TDP + 6) = p10;
  *LP(uint32_t, t.pd + 2) = p21; *LP(uint32_t, t.pd + 2 * TDP + 6) = p21;
  *LP(u16, t.pd) = (u16)p32; *LP(u16, t.pd + 2 * TDP + 4) = (u16)p32;
  asm volatile("ds_write_b16_d16_hi %0, %1 offset:6\n\tds_write_b16_d16_hi %0, %1 offset:%2" : : "v"(t.pd), "v"(p10), "n"(2 * TDP + 10) : "memory");
  if (ct) {
    const uint32_t va = (uint32_t)__builtin_amdgcn_readlane((int)p32, 63), vb = (uint32_t)__builtin_amdgcn_readlane((int)p23, 15) >> 16;
    *LP(u16, t.pe) = (u16)((lane < 12) ? va : vb);
  }
  if (lane < 32) {
    *LP(u16, t.pf + 128) = (u16)p01; *LP(u16, t.pg + 128) = (u16)(p01 >> 16);
    *LP(u16, t.pf) = (u16)p23; *LP(u16, t.pg) = (u16)(p23 >> 16);
  }
  return (uint32_t)(127 + 5 - ts) << 23;
}
__device__ __forceinline__ uint32_t hadd(uint32_t a, uint32_t b) { return __builtin_bit_cast(uint32_t, __builtin_bit_cast(f16x2, a) + __builtin_bit_cast(f16x2, b)); }
template <bool FULL> __device__ __forceinline__ void sums(char* sm, const u32x4 p, const u32x4 c) {
  const uint32_t D[8] = {p[0], p[1], p[2], p[3], c[0], c[1], c[2], c[3]};
  uint32_t O[7];
#pragma unroll
  for (int j = 0; j < 7; ++j) O[j] = __builtin_amdgcn_alignbit(D[j + 1], D[j], 16);
  u32x4 a, b;
#pragma unroll
  for (int j = 0; j < 4; ++j) a[j] = hadd(D[j + 2], hadd(O[j + 1], O[j + 2]));
  *(u32x4*)(sm + C_G3 + 32) = a;
#pragma unroll
  for (int j = 0; j < 4; ++j) b[j] = hadd(a[j], hadd(D[j + 1], D[j + 3]));
  *(u32x4*)(sm + C_G5 + 32) = b;
  if (FULL) {
#pragma unroll
    for (int j = 0; j < 4; ++j) a[j] = hadd(b[j], hadd(O[j], O[j + 3]));
    *(u32x4*)(sm + C_G7 + 32) = a;
#pragma unroll
    for (int j = 0; j < 4; ++j) b[j] = hadd(a[j], hadd(D[j], D[j + 4]));
    *(u32x4*)(sm + C_G9 + 32) = b;
  }
}
__device__ __forceinline__ void count_up(uint32_t a) {
  uint32_t t;
  asm volatile("s_mov_b64 exec, 1\n\tv_mov_b32 %0, 1\n\tds_add_u32 %1, %0\n\ts_mov_b64 exec, -1" : "=&v"(t) : "v"(a) : "memory");
}
__device__ __forceinline__ int pad_at(int r) { return 16 * (r & 1) + 3200 * (r >> 1) - ((r >= 6) ? 3200 - 2096 : 0); }
struct COps { uint32_t b, a, o, g3, k3, g4, k4, f; };
__device__ __forceinline__ void conv_ops(uint32_t tb, uint32_t ub, uint32_t cb, int lane, COps& e) {
  const uint32_t j = lane & 15, kq = lane >> 4, q4 = (lane >> 2) & 3, p4 = lane & 3;
  e.b = ub + 32u * j + 16u * kq;
  e.a = tb + p4 * TDP + 32u - 8u * q4 + 16u * kq;
  e.g3 = cb + C_G5 + 8 + 64u * kq + 8u * (q4 + p4) + (q4 & 1u) * 3200u;
  e.g4 = cb + C_G9 + 8 + 128u * kq + 16u * q4 + 8u * p4 + (q4 & 1u) * 2096u;
  e.k3 = tb + T_3 + 120 - 8u * j + 16u * kq;
  e.k4 = tb + T_4 + 60 - 4u * j + 16u * kq;
  asm volatile("" : "+v"(e.b), "+v"(e.a), "+v"(e.g3), "+v"(e.g4), "+v"(e.k3), "+v"(e.k4));
}
#define CONV_RUN(prog, e, ...) do { int prog_ = (prog); \
    asm volatile("s_call_b64 s[90:91], kf_conv" : "+{s89}"(prog_), "+{v2}"(e.b), "+{v3}"(e.a), "+{v4}"(e.o), "+{v5}"(e.g3), "+{v6}"(e.k3), "+{v7}"(e.g4), \
                 "+{v8}"(e.k4), "+{v9}"(e.f) __VA_ARGS__ : : CONV_CLOB); } while (0)

template <bool E>
__device__ __forceinline__ void ln_stats(const uint32_t* v, int lane, float sc, f32x2* t, f32x4 z, f32x2& rc, f32x2& nc, float& e) {
  float s1a = z[0], s2a = z[1], s1b = z[2], s2b = z[3];
#pragma unroll
  for (int k = 0; k < 4; ++k) {
    s1a = dot2(v[k], 0x3f803f80u, s1a); s2a = dot2(v[k], v[k], s2a);
    s1b = dot2(v[4 + k], 0x3f803f80u, s1b); s2b = dot2(v[4 + k], v[4 + k], s2b);
  }
#pragma unroll
  for (int k = 0; k < 9; ++k) t[k] = (f32x2){bflo(v[k]), bfhi(v[k])};
  const float rs = row_sum(fold16(dot2(v[8], 0x3f803f80u, fold32(s1a, s1b)), dot2(v[8], v[8], fold32(s2a, s2b))));
  if (E) { e = row_sum(e); e += __int_as_float(__builtin_amdgcn_update_dpp(0, __float_as_int(e), 0x142, 0xa, 0xf, false)); }
  const f32x2 mean = (f32x2){rdl(rs, 15), rdl(rs, 47)} * (1.f / D_), m2 = mean * mean;
  f32x2 var = (f32x2){rdl(rs, 31), rdl(rs, 63)} * (1.f / D_) - m2;
  asm volatile("" : "+v"(t[0]), "+v"(t[1]), "+v"(t[2]), "+v"(t[3]), "+v"(t[4]), "+v"(t[5]), "+v"(t[6]), "+v"(t[7]), "+v"(t[8]));
  if (__builtin_expect(!(m2[0] <= 16.f * var[0] && m2[1] <= 16.f * var[1]), 0)) {
    float q[3] = {0.f, 0.f, 0.f};
#pragma unroll
    for (int k = 0; k < 9; ++k) {
      const f32x2 a = t[k] - ((k < 4 || (k == 8 && lane < 32)) ? mean[0] : mean[1]);
      q[k >> 2] += a[0] * a[0] + a[1] * a[1];
    }
    float qa = q[0] + ((lane < 32) ? q[2] : 0.f), qb = q[1] + ((lane < 32) ? 0.f : q[2]);
    wave_sum2(qa, qb);
    var = (f32x2){qa, qb} * (1.f / D_);
  }
  const uint32_t mp = pack2(mean[0], mean[1]), vp = pack2(var[0], var[1]);
  const f32x2 ve = (f32x2){bflo(vp), bfhi(vp)} + 1e-5f;
  const uint32_t e2 = pack2(ve[0], ve[1]);
  const uint32_t sp = pack2(__builtin_amdgcn_sqrtf(bflo(e2)), __builtin_amdgcn_sqrtf(bfhi(e2)));
  rc = (f32x2){__builtin_amdgcn_rcpf(bflo(sp)), __builtin_amdgcn_rcpf(bfhi(sp))} * sc;
  nc = -(f32x2){bflo(mp), bfhi(mp)} * rc;
}
template <bool H>
__device__ __forceinline__ void ln_apply(const f32x2* t, int lane, f32x2 rc, f32x2 nc, uint32_t* c) {
  const float nm[3] = {nc[0], nc[1], (lane < 32) ? nc[0] : nc[1]}, rr[3] = {rc[0], rc[1], (lane < 32) ? rc[0] : rc[1]};
  f32x2 dd[9];
#pragma unroll
  for (int k = 0; k < 9; ++k) dd[k] = __builtin_elementwise_fma(t[k], (f32x2){rr[k >> 2], rr[k >> 2]}, (f32x2){nm[k >> 2], nm[k >> 2]});
  asm volatile("" : "+v"(dd[0]), "+v"(dd[1]), "+v"(dd[2]), "+v"(dd[3]), "+v"(dd[4]), "+v"(dd[5]), "+v"(dd[6]), "+v"(dd[7]), "+v"(dd[8]));
#pragma unroll
  for (int k = 0; k < 9; ++k) {
    const f32x2 d = dd[k];
    typedef __attribute__((ext_vector_type(2))) uint16_t u16x2;
    if (H) c[k] = __builtin_bit_cast(uint32_t, __builtin_elementwise_add_sat(__builtin_bit_cast(u16x2, __builtin_amdgcn_cvt_pkrtz(d[0], d[1])), (u16x2){4, 4}));
    else c[k] = pack2(d[0], d[1]);
  }
}
__device__ __forceinline__ uint32_t med3u(uint32_t a, uint32_t b, uint32_t c) { return max(min(a, b), min(max(a, b), c)); }
__device__ __forceinline__ uint32_t oct_maxu(uint32_t v) {
  v = max(v, (uint32_t)DPP_I((int)v, 0xb1, 0xf));
  v = max(v, (uint32_t)DPP_I((int)v, 0x4e, 0xf));
  v = max(v, (uint32_t)DPP_I((int)v, 0x141, 0xf));
  return v;
}

#define KARGS const u16* __restrict__ x, const u16* __restrict__ Are, const u16* __restrict__ Aim, const u16* __restrict__ Bre, \
    const u16* __restrict__ Bim, const u16* __restrict__ Cre, const u16* __restrict__ Cim, const u16* __restrict__ logdt, \
    const u16* __restrict__ Dskip, char* __restrict__ ws, const u16* __restrict__ wr, const u16* __restrict__ wg, \
    const u16* __restrict__ wu, const u16* __restrict__ wd, u16* __restrict__ out
#define KPASS x, Are, Aim, Bre, Bim, Cre, Cim, logdt, Dskip, ws, wr, wg, wu, wd, out
template <int ROLE, int FFN = -1>
__device__ __forceinline__ void role_body(char* smem, KARGS) {
  const int tid = threadIdx.x, lane = tid & 63;
  const int w = __builtin_amdgcn_readfirstlane(tid >> 6), wl = w & 3;
  const int b = blockIdx.x & 255, bx = b & 7, bj = b >> 3;
  constexpr bool isl = ROLE == 1 || ROLE == 2;
  const int tg = isl ? 32 * (bj >> 2) + 4 * bx + (bj & 3) : 32 * bx + bj;
  u32x4 xa, xc;
  uint32_t ep, r0;
#define P_WAIT(n, ...) asm volatile("s_waitcnt vmcnt(" #n ")\n\tv_lshlrev_b32 %0, 16, v100\n\tv_lshlrev_b32 %1, 16, v101\n\tv_lshlrev_b32 %2, 16, v102" \
    : "=v"(ldt), "=v"(are), "=v"(aim) : : "v100", "v101", "v102" __VA_ARGS__)
#define P_MORE() asm volatile("v_lshlrev_b32 %0, 16, v103\n\tv_lshlrev_b32 %1, 16, v104\n\tv_lshlrev_b32 %2, 16, v105\n\tv_lshlrev_b32 %3, 16, v106" \
    : "=v"(bre), "=v"(bim), "=v"(cre), "=v"(cim) : : "v103", "v104", "v105", "v106")
  TSD;
  TS(0, 0);
  const uint32_t lb0 = (uint32_t)(uintptr_t)(__attribute__((address_space(3))) char*)smem;
  const int i = lane & 15, kq = lane >> 4;
  const u32x4 ones = {0xffffffffu, 0xffffffffu, 0xffffffffu, 0xffffffffu}, zero4 = {0u, 0u, 0u, 0u};
  auto u_again = [&](u32x4& uw, const u32x4& ut, uint32_t uo) {
#pragma unroll 1
    for (int spin = 0; spin < SPIN_CAP; ++spin) {
      const bool na = ((uw[0] | uw[1] | uw[2] | uw[3]) & UMASK) != 0u;
      if (__builtin_expect(!__any(na), 1)) break;
      TSC(19);
      SLEEP(USLEEP);
      if (na) { LD16W(uw, uo); uw ^= ut; }
    }
  };
  constexpr uint32_t S16 = 0x41800000u, LMASK = 0x00070007u;
  auto v_put = [&](u32x4 v, const u32x4& ut, bool other, uint32_t vo) {
    const bool second = __any(other || ((v[0] | v[1] | v[2] | v[3]) & 0x40004000u) != 0u);
    if (__builtin_expect(second, 0)) v = (v & ~LMASK) | (~ut & LMASK); else v = (v & ~LMASK) | ut;
    ST16(vo, v);
    return second;
  };
  auto v_end = [&](uint32_t T, int n) { DRAIN(); if (lane == 0) ST4(O_DN + 16u * n, T); };
  if constexpr (!isl) {
    const int ch = wl >> 1, tl = tid & 255;
    int* evp = (int*)(smem + O_FL + 16 * ch);
    float ldt, are, aim, dsk;
    if constexpr (ROLE == 0) {
      __builtin_amdgcn_s_setprio(KPRIO);
      COps e;
      conv_ops(lb0 + (uint32_t)(ch * TB), lb0 + (uint32_t)(O_UL + ch * UB), lb0 + (uint32_t)(O_IM + ch * GB), lane, e);
      e.o = lb0 + O_ST + (uint32_t)(16 * ((4 * i + 2 * (kq >> 1) + ch) ^ (i >> 1)) + 8 * (kq & 1));
      e.f = lb0 + O_FL;
      TOps t;
      table_ops(lb0 + (uint32_t)(O_IM + 128 * IP * ch), lb0 + (uint32_t)(TB * ch), wl & 1, lane, t);
      uint32_t pz = lb0 + O_IM + (uint32_t)(((lane >> 3) & 1) * GB + pad_at(lane & 7)), im = lb0 + (uint32_t)(O_IM + IP * tl);
      asm volatile("" : "+v"(e.o), "+v"(e.f), "+v"(pz), "+v"(im));
      float bre, bim, cre, cim;
      P_WAIT(0, , "v103", "v104", "v105", "v106", "v107", "v109");
      asm volatile("" : "={v109}"(ep));
      const uint32_t T = ((uint32_t)__builtin_amdgcn_readfirstlane(ep) + 1u) * 0x9E3779B1u;
      if (w == 3 && lane < 8) ST16(O_KEY + 128u * b + 16u * lane, ones);
      P_MORE();
      asm volatile("v_lshlrev_b32 %0, 16, v107" : "=v"(dsk) : : "v107");
      TS(0, 1);
      evp[wl & 1] = gen_y(ldt, are, aim, bre, bim, cre, cim, im, [] {} TSA);
      TS(0, 8);
      BAR();
      TS(0, 2);
      const uint32_t cs = table_tile(t, wl & 1, lane, lb0 + O_FL + 16 * ch, dsk TSA);
      *(uint32_t*)(smem + O_FL + 40 + 4 * ch) = cs;
      DRAIN();
      if ((wl & 1) && lane == 0) asm volatile("global_store_dwordx2 %0, %1, %2 sc1" : : "v"(O_SC + 16u * (2 * tg + ch)), "v"((u32x2){T, cs}), "s"(ws) : "memory");
      TS(0, 9);
      gsync0(lb0 + O_FL + 36, 4);
      TS(0, 3);
      *(u32x4*)(smem + O_ST + 16 * tl) = ones;
      *(u32x4*)(smem + O_ST + 4096 + 16 * tl) = ones;
      if (w == 0) *LP(u32x4, pz) = zero4;
      CONV_RUN(wl & 1, e);
      TS(0, 5);
    } else {
      __builtin_amdgcn_s_setprio(UPRIO);
#pragma unroll
      for (int k = 0; k < 3; ++k) *(u32x4*)(smem + ((16 * (tl + 256 * k) < O_FL) ? 16 * (tl + 256 * k) : O_FL + 32)) = zero4;
      if (w == 7) *(u32x4*)(smem + O_UL + (lane >> 5) * UB + 16 * min(lane & 31, 29)) = zero4;
      uint32_t uo = u_at(tl >> 1, 2 * tg + (tid & 1)), go = O_SU + (uint32_t)(256 * bx + 32 * wl + (lane & 31) + 4 * (lane & 32)) * 128u;
      uint32_t ul = (uint32_t)(O_UL + (tid & 1) * UB + C_U - 16 + 16 * (tl >> 1)), ug = (uint32_t)(O_IM + (tid & 1) * GB + 16 * (tl >> 1)), so = lb0 + O_ST + 16u * (uint32_t)(lane ^ (lane >> 3));
      uint32_t vo = O_V + (uint32_t)tg * VP + 16u * lane;
      asm volatile("" : "+v"(uo), "+v"(go), "+v"(ul), "+v"(ug), "+v"(so), "+v"(vo));
      P_WAIT(1, , "v109");
      TS(0, 1);
      evp[2 + (wl & 1)] = gen_x(ldt, are, aim, smem + O_IM + IP * tl);
      asm volatile("s_waitcnt vmcnt(0)" : "={v109}"(ep));
      const uint32_t T = ((uint32_t)__builtin_amdgcn_readfirstlane(ep) + 1u) * 0x9E3779B1u;
      TS(0, 8);
      BAR();
      TS(0, 2);
      u32x4 ut = {utag(T, 0), utag(T, 1), utag(T, 2), utag(T, 3)};
      asm volatile("" : "+v"(ut));
      SLEEP(GSLEEP);
      u32x2 pv;
      pv[1] = gate(ws, go, T);
      TS(0, 3);
      until(latest(pv[1]) + UD);
      TS(0, 7);
      u32x4 uw, ux;
      asm volatile("global_load_dwordx4 %0, %2, %4 sc1\n\tglobal_load_dwordx4 %1, %3, %4 sc1"
                   : "=&v"(uw), "=&v"(ux) : "v"(uo), "v"(uo + 64 * UP * 32), "s"(ws) : "memory");
      __builtin_amdgcn_s_setprio(FPR);
      asm volatile("s_waitcnt vmcnt(1)" : "+v"(uw));
      uw ^= ut;
      *(u32x4*)(smem + ul + 16) = uw;
      asm volatile("s_waitcnt vmcnt(0)" : "+v"(ux));
      TS(0, 11);
      ux ^= ut;
      *(u32x4*)(smem + ul + 2048 + 16) = ux;
      if (__builtin_expect(__any(((uw[0] | uw[1] | uw[2] | uw[3] | ux[0] | ux[1] | ux[2] | ux[3]) & UMASK) != 0u), 0)) {
        u_again(uw, ut, uo);
        u_again(ux, ut, uo + 64 * UP * 32);
        *(u32x4*)(smem + ul + 16) = uw;
        *(u32x4*)(smem + ul + 2048 + 16) = ux;
      }
      BAR();
      TS(0, 4);
      const u32x4 pw = *(const u32x4*)(smem + ul);
      if (wl < 2) {
        const u32x4 px = *(const u32x4*)(smem + ul + 2048);
        sums<true>(smem + ug, pw, uw);
        sums<false>(smem + ug + 2048, px, ux);
      } else sums<true>(smem + ug, pw, uw);
      count_up(lb0 + O_FL + 32);
      __builtin_amdgcn_s_setprio(SPR);
      TS(0, 5);
      const bool other = *(const uint32_t*)(smem + O_FL + 40 + 4 * (lane & 1)) != S16;
      bool second = false;
#pragma unroll
      for (int k = 0; k < 2; ++k) {
        const uint32_t o = ((k ? 0x3247u : 0x1065u) >> (4 * wl) & 7u) * 1024u;
        u32x4 v;
#pragma unroll 1
        for (int spin = 0; spin < SPIN_CAP; ++spin) {
          asm volatile("ds_read_b128 %0, %1\n\ts_waitcnt lgkmcnt(0)" : "=&v"(v) : "v"(so + o) : "memory");
          if (!__any(max4(v) == 0xffffffffu)) break;
          __builtin_amdgcn_s_sleep(VPS);
        }
        TS(0, 12 + k);
        second |= v_put(v, ut, other, vo + o);
        TS(0, 10 - 4 * k);
      }
      if (__builtin_expect(second, 0)) v_end(T, 4 * tg + wl);
    }
    TSE();
  } else {
  uint32_t* cand = (uint32_t*)(smem + LM);
  uint32_t* sel = cand + 128;
  uint32_t* kl = sel + 48;
  uint32_t* cnt = sel + 64;
  const uint32_t ca = lb0 + LM + 4 * (128 + 64);
  u16* hm = (u16*)(cnt + 12);
  uint32_t T;
  const bool ffn = (FFN < 0) ? bj < 18 : FFN, isx = FFN != 1 && bj >= 24;
  const int fb = 18 * bx + bj;
  constexpr bool ln2 = ROLE == 2;
  const int g0 = tg, g2 = tg;
  u32x4 bw[18];
#pragma unroll
  for (int s = 0; s < 9; ++s) bw[s] = (u32x4){0u, 0u, 0u, 0u};
  int tid2 = threadIdx.x;
  uint32_t ts1 = 0u;
  auto rest = [&] {
    if (ts1 && (int)((uint32_t)__builtin_amdgcn_s_memrealtime() - ts1) > 0) { TS(0, 8); until(ts1 + TSTOP + RD); if (RW) gwait(ca, 4); ts1 = 0u; }
  };
  auto gu_dma = [&](int q0, int q1, auto depth) {
    const char* gs = (const char*)((w < 2) ? wg : wu) + (size_t)((144 * (w & 1)) * (F_ * 2) + fb * 64);
    const uint32_t gv = (uint32_t)((lane >> 2) * (F_ * 2) + 16 * ((lane & 3) ^ (lane >> 5) ^ (w & 2)));
#pragma unroll 1
    for (int q = q0; q < q1; ++q) {
      const int k = (int)((((bj & 1) ? ORD1 : ((unsigned __int128)0x876543210u << 36 | 0x876543210ull)) >> (4 * (q - (q >= 9 ? 9 : 0)) + (q >= 9 ? 36 : 0))) & 15u), h = q >= 9;
      if (q >= RQ) rest();
      dma16(gs + (size_t)(288 * h + 16 * k) * (F_ * 2), gv, lb0 + LW * h + 9216 * w + 1024 * k);
      asm volatile("s_waitcnt vmcnt(%0)" : : "n"(depth.value) : "memory");
    }
  };
  auto down_dma = [&](int k0, int k1, int lane) {
#pragma unroll 1
    for (int k = k0; k < k1; ++k) {
      const int o = 9216 * wl + 2304 * k;
      const char* ds = (const char*)wd + (size_t)(fb * 36864 + o);
      const uint32_t dl = lb0 + o + 32 * ((4 * wl + k + 1) >> 1);
      if (ROLE == 1) rest();
      dma16d(ds, 16u * lane, dl);
      dma16d(ds + 1024, 16u * lane, dl + 1024);
      uint32_t keep;
      asm volatile("s_mov_b32 %0, m0\n\ts_mov_b32 m0, %3\n\ts_mov_b64 exec, 0xffff\n\tglobal_load_lds_dwordx4 %1, %2" DPOL2 "\n\ts_mov_b64 exec, -1\n\ts_mov_b32 m0, %0\n\ts_waitcnt vmcnt(%4)"
                   : "=&s"(keep) : "v"(16u * lane), "s"(ds + 2048), "s"(dl + 2048), "n"(ROLE == 1 ? DDEPTH : 40) : "memory");
    }
  };
  auto gu_read = [&](int h) {
    const uint32_t ra = lb0 + LW * h + (uint32_t)(((lane >> 1) & 1) * 18432 + (8 * kq + ((lane >> 2) & 3)) * 64 + ((w ^ (kq & 1) ^ (lane & 2)) * 16) + 8 * (lane & 1));
#pragma unroll
    for (int s = 0; s < 9; ++s) {
      const u32x2 h0 = trd(ra + 2048 * s), h1 = trd(ra + 2048 * s + 256);
      bw[9 * h + s] = (u32x4){h0[0], h0[1], h1[0], h1[1]};
    }
#pragma unroll
    for (int s = 0; s < 9; ++s) asm volatile("" : "+v"(bw[9 * h + s]));
  };
  uint32_t tv = 0u, tk = 0u;
  const uint32_t vearly = (uint32_t)((VEARLY >> (8 * (g0 >> 5))) & 0xffull);
  auto w_line = [&](int n) {
    const int q = min(lane, 62) / 9, r = min(7 * (n >> 2) + q, 287), h = r >= 144;
    return (const char*)((n & 1) ? wu : wg) + (uint32_t)((r + 144 * (h + ((n >> 1) & 1))) * (F_ * 2) + bx * 1152 + (min(lane, 62) - 9 * q) * 128);
  };
#define TOUCH(pa) asm volatile("global_load_dword v126, %0, off" DPOL4 : : "v"(pa) : "memory", "v126")
  uint32_t wv[5], xv[9];
  auto rows_xw = [&](int lane) {
    u32x4 wa, xr, xs;
    const uint32_t xo = (uint32_t)((8 * g2 + 2 * wl) * (D_ * 2));
    asm volatile("global_load_dwordx4 %0, %5, %9\n\tglobal_load_dword %1, %6, %9 offset:1024\n\t"
                 "global_load_dwordx4 %2, %7, %10\n\tglobal_load_dwordx4 %3, %7, %10 offset:1152\n\tglobal_load_dword %4, %8, %10 offset:1024\n\ts_waitcnt vmcnt(0)"
                 : "=&v"(wa), "=&v"(wv[4]), "=&v"(xr), "=&v"(xs), "=&v"(xv[8])
                 : "v"(16u * lane), "v"(4u * (lane & 31)), "v"(xo + 16u * lane), "v"(xo + 4u * lane + 32u * (lane & 32)), "s"(wr), "s"(x) : "memory");
#pragma unroll
    for (int k = 0; k < 4; ++k) { wv[k] = wa[k]; xv[k] = xr[k]; xv[4 + k] = xs[k]; }
  };
  auto ln1 = [&](int g, int o, uint32_t c0) {
    uint32_t uo = u_at(g, 64 * wl + lane);
    uint32_t ta = lb0 + (uint32_t)(o + ((lane >> 2) & 3) * RP + (64 * wl + 16 * kq + 4 * (lane & 3)) * 2);
    uint32_t la = (uint32_t)(o + 2 * wl * RP + 16 * lane), lt = (uint32_t)(o + (2 * wl + (lane >> 5)) * RP + 1024 + 4 * (lane & 31));
    uint32_t sg = O_SU + (uint32_t)(256 * (lane >> 3) + g) * 128u + 16u * (lane & 7);
    asm volatile("" : "+v"(uo), "+v"(ta), "+v"(la), "+v"(lt), "+v"(sg));
    f32x4 z4 = {0.f, 0.f, 0.f, 0.f};
    asm volatile("" : "+v"(z4));
    asm volatile("s_waitcnt vmcnt(0)" : "={v[100:103]}"(xa), "={v[104:107]}"(xc), "={v108}"(r0), "={v109}"(ep));
    TS(0, 1);
    const uint32_t v[9] = {xa[0], xa[1], xa[2], xa[3], xc[0], xc[1], xc[2], xc[3], r0};
    uint32_t c[9];
    f32x2 tf[9];
    T = ((uint32_t)__builtin_amdgcn_readfirstlane(ep) + 1u) * 0x9E3779B1u;
    if (wl == 3) { const uint32_t now = (uint32_t)__builtin_amdgcn_s_memrealtime() + SD; ST16(sg, ((u32x4){T, now, T, now})); }
    const uint32_t tj = ((T >> (3 * wl)) & 0x00070007u) | (((T >> (12 + wl)) & 0x00010001u) << 14);
    uint32_t tw[3] = {(tj & 0xffffu) * 0x10001u, (tj >> 16) * 0x10001u, 0u};
    tw[2] = (lane < 32) ? tw[0] : tw[1];
    asm volatile("" : "+v"(tw[0]), "+v"(tw[1]), "+v"(tw[2]));
    f32x2 rc, nc;
    float e_;
    ln_stats<false>(v, lane, 0.03125f, tf, z4, rc, nc, e_);
    ln_apply<true>(tf, lane, rc, nc, c);
#pragma unroll
    for (int k = 0; k < 9; ++k) c[k] = (c[k] & ~UMASK) | tw[k >> 2];
    *(u32x4*)(smem + la) = (u32x4){c[0], c[1], c[2], c[3]};
    *(u32x4*)(smem + la + RP) = (u32x4){c[4], c[5], c[6], c[7]};
    *(uint32_t*)(smem + lt) = c[8];
    u32x4 ha, hb;
    {
      uint32_t t, cc, n = SPIN_CAP;
      asm volatile("s_mov_b64 exec, 1\n\tv_mov_b32 %2, 1\n\tds_add_u32 %5, %2\n\ts_mov_b64 exec, -1\n"
                   "1:\n\tds_read_b32 %2, %5\n\tds_read_b64_tr_b16 v[40:41], %6\n\tds_read_b64_tr_b16 v[42:43], %6 offset:%7\n\tds_read_b64_tr_b16 v[44:45], %6 offset:512\n\t"
                   "ds_read_b64_tr_b16 v[46:47], %6 offset:%8\n\ts_sub_u32 %4, %4, 1\n\ts_cbranch_scc1 2f\n\ts_waitcnt lgkmcnt(4)\n\tv_readfirstlane_b32 %3, %2\n\t"
                   "s_cmp_lt_u32 %3, 4\n\ts_cbranch_scc1 1b\n2:\n\ts_waitcnt lgkmcnt(0)"
                   : "=&{v[40:43]}"(ha), "=&{v[44:47]}"(hb), "=&v"(t), "=&s"(cc), "+s"(n) : "v"(c0), "v"(ta), "n"(4 * RP), "n"(512 + 4 * RP) : "memory", "scc");
    }
    ST16(uo, ha);
    ST16(uo + 64 * 128, hb);
    if (wl == 0) {
      const u32x2 h4 = trd(ta + 1024), h5 = trd(ta + 1024 + 4 * RP);
      ST16(uo + 128 * 128, ((u32x4){h4[0], h4[1], h5[0], h5[1]}));
    }
  };
  const int dX = 512 - 24 + 8 * bx + bj;
  if constexpr (ROLE == 2) {
    const int tl = tid - 256;
    if (XOFF && (uint32_t)(bj - 4) < 8u) {
      __builtin_amdgcn_s_setprio(3);
      ln1(tg + 160, 10240, ca + 32);
      TS(0, 2);
    }
    if (!isx && T0) {
      SLEEP(T0SLEEP);
      if (wl < T0) TOUCH(w_line(24 * wl + bj));
      if (T1) { asm volatile("s_waitcnt vmcnt(0)" ::: "memory"); SLEEP(T1SLEEP); if (wl < T1) TOUCH(w_line(24 * (T0 + wl) + bj)); }
    }
    if (isx) {
      __builtin_amdgcn_s_setprio(QPRIO0);
      *(u32x4*)(smem + X_TB + 16 * tl) = zero4;
      *(u32x4*)(smem + X_TB + 16 * min(256 + tl, (TB + C_U) / 16 - 1)) = zero4;
      const int pr = (wl + XROT) & 3;
      COps e;
      conv_ops(lb0 + X_TB, lb0 + X_UL, lb0 + X_IM, lane, e);
      TOps t;
      table_ops(lb0 + X_IM, lb0 + X_TB, wl & 1, lane, t);
      e.o = lb0 + X_ST + (uint32_t)(16 * ((2 * i + (kq >> 1)) ^ ((i >> 2) & 1)) + 8 * (kq & 1));
      e.f = ca + 28 - 32;
      asm volatile("" : "+v"(e.o), "+v"(e.f));
      float ldt, are, aim, bre, bim, cre, cim, dsk;
      P_WAIT(0, , "v103", "v104", "v105", "v106", "v107", "v109");
      asm volatile("" : "={v109}"(ep));
      T = ((uint32_t)__builtin_amdgcn_readfirstlane(ep) + 1u) * 0x9E3779B1u;
      P_MORE();
      asm volatile("v_lshlrev_b32 %0, 16, v107" : "=v"(dsk) : : "v107");
      TS(0, 1);
      __builtin_amdgcn_s_setprio(QPRIO);
      int* evp = (int*)(sel + 44);
      char* im = smem + X_IM + IP * (tl & 127);
      evp[wl] = (wl < 2) ? gen_y(ldt, are, aim, bre, bim, cre, cim, lb0 + X_IM + IP * tl, [] {} TSA) : gen_x(ldt, are, aim, im);
      TS(0, 2);
      __builtin_amdgcn_s_setprio(QPRIO2);
      gsync(ca + 8, 4);
      if (wl < 2) evp[-4 + wl] = (int)table_tile(t, wl & 1, lane, lb0 + LM + 4 * (128 + 44), dsk TSA);
      if (wl < 2) gsync0(ca + 8, 8); else { count_up(ca + 8); SLEEP(XSL); gwait(ca + 8, 8); }
      const uint32_t cs = (uint32_t)__builtin_amdgcn_readfirstlane(evp[-4]);
      if (wl == 2 && lane == 0) asm volatile("global_store_dwordx2 %0, %1, %2 sc1" : : "v"(O_SC + 16u * dX), "v"((u32x2){T, cs}), "s"(ws) : "memory");
      if (wl == 3) *(u32x4*)(smem + X_IM + pad_at(lane & 7)) = zero4;
      TS(0, 3);
      CONV_RUN(2 + pr, e, , "+{v11}"(tid2));
      {
        const int lane = tid2 & 63;
        const u32x4 v = *(const u32x4*)(smem + X_ST + 2048 * pr + 1024 * (lane >> 5) + 16 * ((lane & 31) ^ ((lane >> 3) & 1)));
        if (__builtin_expect(v_put(v, (u32x4){utag(T, 0), utag(T, 1), utag(T, 2), utag(T, 3)}, cs != S16, v_at(dX, 64 * pr + lane)), 0)) {
          DRAIN();
          if (lane == 0) ST4(O_DN + 16u * (4 * (dX - 256) + pr), T);
        }
      }
      TS(0, 6);
    }
    __builtin_amdgcn_s_setprio(PPRIO);
    asm volatile("" : "+v"(tid2));
    if (isx) {
      const int lane = tid2 & 63;
      const u32x4 ra = *(const u32x4*)(smem + X_RW(wl) + 16 * lane), rb = *(const u32x4*)(smem + X_RW(wl) + 1024 + 16 * lane), rw = *(const u32x4*)(smem + X_WR + 16 * lane);
      const u32x2 rc = *(const u32x2*)(smem + X_RW(wl) + 2048 + 8 * lane);
#pragma unroll
      for (int k = 0; k < 4; ++k) { xv[k] = ra[k]; xv[4 + k] = rb[k]; wv[k] = rw[k]; }
      xv[8] = rc[0]; wv[4] = rc[1];
    } else rows_xw(tid2 & 63);
    if (!isx && !ffn) {
      SLEEP(RSLEEP);
#pragma unroll
      for (int k = 0; k < 7; ++k) ST16(O_SL + (uint32_t)((6 * bx + bj - 18) * 1792 + 256 * k - 256 + tid2) * 16u, ones);
      DRAIN();
    }
    if (!isx) BAR();
    const uint32_t tp = (uint32_t)__builtin_amdgcn_readfirstlane(cnt[4]);
    if (!isx) T = (uint32_t)__builtin_amdgcn_readfirstlane(sel[43]);
    tv = tp + UD + VD2 - vearly;
  } else {
    __builtin_amdgcn_s_setprio(3);
    if (tid < 12) cnt[tid] = 0u;
    BAR();
    if (tid < 20) ((uint32_t*)(hm + 320))[tid] = 0u;
    if (tid >= 184) *(u32x4*)(smem + L_SC - 16 * 184 + 16 * tid) = (u32x4){0x41804180u, 0x41804180u, 0x41804180u, 0x41804180u};
    uint32_t sink = 0;
    if (XOFF && isx) {
      asm volatile("s_waitcnt vmcnt(0)" : "={v109}"(ep));
      T = ((uint32_t)__builtin_amdgcn_readfirstlane(ep) + 1u) * 0x9E3779B1u;
    } else ln1(tg, 0, ca + 12);
    TS(0, 2);
    __builtin_amdgcn_s_setprio(NPRIO);
    if (!isx && 3 - w < TB1) TOUCH(w_line(24 * (3 - w) + 23 - bj));
    asm volatile("s_waitcnt vmcnt(0)" : "+v"(sink) : : "memory");
    TS(0, 13);
    if (w == 0) {
      const uint32_t xr = O_XC + (uint32_t)((511 - 2 * (g0 + w)) * 144 + lane) * 16u;
      ST16(xr, ones);
      ST16(xr + 1024u, ones);
      if (lane < 16) ST16(xr + 2048u, ones);
    }
    if (w == 2 && b < 72 && lane < 8) ST16(O_FB + (uint32_t)(8 * b + lane) * 16u, ones);
    if (isx) {
      rows_xw(lane);
      char* sp = smem + X_RW(w) + 16 * lane;
      *(u32x4*)sp = (u32x4){xv[0], xv[1], xv[2], xv[3]};
      *(u32x4*)(sp + 1024) = (u32x4){xv[4], xv[5], xv[6], xv[7]};
      *(u32x2*)(smem + X_RW(w) + 2048 + 8 * lane) = (u32x2){xv[8], wv[4]};
      if (w == 0) *(u32x4*)(smem + X_WR + 16 * lane) = (u32x4){wv[0], wv[1], wv[2], wv[3]};
    }
    SLEEP(ESLEEP);
    uint32_t tu = latest(gate(ws, O_SU + (uint32_t)(256 * bx + 64 * w + 4 * i + (bj & 3)) * 128u, T));
    asm volatile("s_mov_b64 exec, 1\n\tds_max_u32 %0, %1\n\ts_mov_b64 exec, -1" : : "v"(ca + 16), "v"(tu) : "memory");
    if (w == 0) sel[43] = T;
    if (isx) {
      const uint32_t xo = u_at(tid, dX);
      char* sm = smem + X_UL + C_U - 16 + 16 * tid;
      const u32x4 ut = {utag(T, 0), utag(T, 1), utag(T, 2), utag(T, 3)};
      __builtin_amdgcn_s_setprio(3);
      until(tu + UD + XD);
      u32x4 uw;
      asm volatile("global_load_dwordx4 %0, %1, %2 sc1" : "=&v"(uw) : "v"(xo), "s"(ws) : "memory");
      asm volatile("s_waitcnt vmcnt(0)" : "+v"(uw));
      uw ^= ut;
      u_again(uw, ut, xo);
      *(u32x4*)(sm + 16) = uw;
      if (XF) { gsync0(ca + 12, XOFF ? 4 : 8); gwait(ca + 8, 8); } else BAR();
      if (w < 3) {
        const u32x4 pw = *(const u32x4*)sm;
        if (w < 2) sums<true>(smem + X_IM + 16 * tid, pw, uw); else sums<false>(smem + X_IM + 16 * tid, pw, uw);
        count_up(ca + 28);
      }
      if (XF) BAR();
    } else {
      until(tu + UD + WD);
      BAR();
    }
    tu = (uint32_t)__builtin_amdgcn_readfirstlane(cnt[4]);
    TS(0, 3);
    __builtin_amdgcn_s_setprio(DPRIO);
    if (ffn) {
      ts1 = (tu + UD + VD2 - TSTOP) | 1u;
      gu_dma(0, N0, IC<40>{});
      gu_dma(N0, NA, IC<WDEPTH>{});
      asm volatile("s_waitcnt vmcnt(%0)" : : "n"(NA - 9) : "memory");
      gsync0(ca + 4, 4);
      gu_read(0);
      asm volatile("s_waitcnt lgkmcnt(0)" ::: "memory");
      count_up(ca + 36);
      TS(0, 4);
      gu_dma(NA, 18, IC<WDEPTH2>{});
      gwait(ca + 36, 4);
      down_dma(0, KN, lane);
      asm volatile("s_waitcnt vmcnt(%0)" : : "n"(3 * KN) : "memory");
      count_up(ca + 4);
      TS(0, 7);
    } else {
      if (!isx && C3) {
        const int n = 6 * min(tid, 191) + bj - 18, r = n >> 2;
        const char* wb = (const char*)((n & 1) ? wu : wg) + bx * 1152;
        const int c0 = (3 - (int)((uintptr_t)wb >> 7)) & 7;
        wb += (uint32_t)((r + 144 * ((r >= 144) + ((n >> 1) & 1))) * (F_ * 2) + 128 * c0);
        TOUCH(wb);
        if (c0 == 0) TOUCH(wb + 1024);
      }
      auto d_line = [&](int k) {
        const int n = min((k * 14 + bj - 18) * 64 + lane, 5183), q = n / 1296, r = n - 1296 * q;
        return (const char*)wd + (uint32_t)(bx * 5184 + (r / 18) * 72 + 18 * (HREV ? 3 - q : q) + r % 18) * 128u;
      };
      int k = w;
      if (w < HW) {
#pragma unroll 1
        for (; k < 6 && (int)((uint32_t)__builtin_amdgcn_s_memrealtime() - (tu + UD + VD2 - HSTOP)) < 0; k += HW) { TOUCH(d_line(k)); DRAIN(); }
      }
      TS(0, 4);
      until(tu + UD + VD2 + RD2);
      if (RW) gwait(ca, 4);
      if (w < HW) {
#pragma unroll 1
        for (; k < 6; k += HW) { TOUCH(d_line(k)); DRAIN(); }
      }
      TS(0, 7);
    }
    tv = tu + UD + VD2 - vearly;
    tk = tu + UD + VD2 + KD;
  }
  {
  const int tid = tid2, lane = tid & 63, i = lane & 15, kq = lane >> 4;
  __builtin_amdgcn_s_setprio(3);
  const int PL = ffn ? 6400 : LW + 6656;
  f32x2 xf[9];
  f32x4 za = {0.f, 0.f, 0.f, 0.f}, zb = za;
  uint32_t al = O_XC + 64u * (i >> 3) + 16u * kq + 128u * w;
  uint32_t ha = (ROLE == 1) ? (uint32_t)((4 * (kq & 1) + 2 * (kq >> 1) + (i >> 3)) * 80 + 16 * w + 2 * (i & 7)) : 0u;
  uint32_t h0a = (uint32_t)(min(i, 8) * 80 + 16 * kq), h1a = (uint32_t)(min(i ^ 8, 8) * 80 + 16 * kq);
  uint32_t so = O_SL + (uint32_t)fb * SP + ((ROLE == 2) ? 16u * tid - 4096u : 8192u + 16u * lane);
  uint32_t da = lb0 + (uint32_t)((8 * kq + ((lane >> 2) & 3)) * 1152 + 32 * ((8 * kq + ((lane >> 2) & 3) + 2) >> 2) + 8 * (lane & 3) + ((ROLE == 2) ? 128 * wl : 1024));
  if (ROLE == 2) asm volatile("" : "+v"(h0a), "+v"(h1a), "+v"(so), "+v"(da)); else asm volatile("" : "+v"(ha), "+v"(al), "+v"(da));
  uint32_t hv[9] = {}, nv[9] = {}, keya = 0u, keyb = 0u;
  int rka = 0, rkb = 0;
  auto put_row = [&](const uint32_t* r, int j, const void* base, uint32_t ro) {
    const u32x4 tl = {r[8], (uint32_t)DPP_I((int)r[8], 0x55, 0xf), (uint32_t)DPP_I((int)r[8], 0xaa, 0xf), (uint32_t)DPP_I((int)r[8], 0xff, 0xf)};
    asm volatile("global_store_dwordx4 %0, %1, %2 sc1\n\ts_nop 1" : : "v"(ro + 16u * lane), "v"((u32x4){r[4 * j], r[4 * j + 1], r[4 * j + 2], r[4 * j + 3]}), "s"(base) : "memory");
    if ((lane & 35) == 32 * j) asm volatile("global_store_dwordx4 %0, %1, %2 offset:1024 sc1\n\ts_nop 1" : : "v"(ro + 4u * (lane & 31)), "v"(tl), "s"(base) : "memory");
  };
  if (ln2) {
    const int po = PL - 2560 * (ROLE == 1);
    uint32_t pa = (uint32_t)(po + 9216 * wl + 32 * lane), pb = (uint32_t)(po + 9216 * wl + 2048 + 8 * (lane & 31)), ps = (lane < 32) ? 0u : 16u;
    uint32_t kid = (uint32_t)(((255 - g2) << 5) | (7 - 2 * wl - (lane >> 5)));
    const int tl = tid - 256, t2 = tl + 128, d1 = 64 * (tl / 48) + tl % 48, d3 = 64 * (t2 / 48) + t2 % 48;
    const int d2 = (tl < 128) ? 64 * (tl >> 4) + 48 + (tl & 15) : min(384 + tl, 575);
    uint32_t vo = v_at(d1, g0), vo2 = v_at(d2, g0), vo3 = v_at(d3, g0), px = (uint32_t)(PL + 4 * d1), py = (uint32_t)(PL + 4 * d2), pz = (uint32_t)(PL + 4 * d3);
#pragma unroll
    for (int k = 0; k < 9; ++k) xf[k] = (f32x2){bflo(xv[k]), bfhi(xv[k])};
    asm volatile("" : "+v"(xf[0]), "+v"(xf[1]), "+v"(xf[2]), "+v"(xf[3]), "+v"(xf[4]), "+v"(xf[5]), "+v"(xf[6]), "+v"(xf[7]), "+v"(xf[8]));
    asm volatile("" : "+v"(za), "+v"(zb), "+v"(pa), "+v"(pb), "+v"(ps), "+v"(kid), "+v"(vo), "+v"(vo2), "+v"(vo3), "+v"(px), "+v"(py), "+v"(pz));
    u32x4 ut = {utag(T, 0), utag(T, 1), utag(T, 2), utag(T, 3)};
    float sv[10], ws_ = dot2(wv[4], 0x3f803f80u, 0.f) * 0.5f;
#pragma unroll
    for (int k = 0; k < 4; ++k) ws_ = dot2(wv[k], 0x3f803f80u, ws_);
    ws_ = wave_sum(ws_);
#pragma unroll
    for (int k = 0; k < 10; ++k) sv[k] = 16.f;
    uint32_t ka_ = lb0 + LM + 4u * (128 + 48 + 2 * wl + (lane >> 5)), kb_ = lb0 + LM + 4u * (128 + 48);
    uint32_t ko_ = O_KEY + (uint32_t)(256 * (lane & 7) + 8 * (g2 & 31) + (g2 >> 5)) * 16u;
    asm volatile("" : "+v"(ut), "+v"(ws_), "+v"(sv[0]), "+v"(sv[1]), "+v"(sv[2]), "+v"(sv[3]), "+v"(sv[4]), "+v"(sv[5]), "+v"(sv[6]), "+v"(sv[7]), "+v"(sv[8]), "+v"(sv[9]),
                 "+v"(ka_), "+v"(kb_), "+v"(ko_));
    if (ffn) gwait(ca + 36, 4);
    TS(0, 7);
    until(tv);
    TS(0, 8);
    u32x4 va, vc = ut, vb = ut;
    asm volatile("global_load_dwordx4 %0, %2, %4 sc1\n\ts_cmp_lt_u32 %5, 2\n\ts_cbranch_scc1 1f\n\tglobal_load_dwordx4 %1, %3, %4 sc1\n1:"
                 : "=&v"(va), "+v"(vc) : "v"(vo), "v"(vo3), "s"(ws), "s"(wl) : "memory", "scc");
    until(tv + V2D);
    asm volatile("s_cmp_eq_u32 %5, 3\n\ts_cbranch_scc1 1f\n\tglobal_load_dwordx4 %2, %3, %4 sc1\n\ts_waitcnt vmcnt(1)\n\ts_branch 2f\n1:\n\ts_waitcnt vmcnt(0)\n2:"
                 : "+v"(va), "+v"(vc), "+v"(vb) : "v"(vo2), "s"(ws), "s"(wl) : "memory", "scc");
    auto turn1 = [&] {
#pragma unroll
      for (int e = 0; e < 4; ++e) *(uint32_t*)(smem + px + 9216 * e) = va[e];
      if (wl >= 2) {
#pragma unroll
        for (int e = 0; e < 4; ++e) *(uint32_t*)(smem + pz + 9216 * e) = vc[e];
      }
    };
    auto turn = [&] {
      if (wl < 3) {
#pragma unroll
        for (int e = 0; e < 4; ++e) *(uint32_t*)(smem + py + 9216 * e) = vb[e];
      }
    };
    va ^= ut; vc ^= ut;
    turn1();
    asm volatile("s_waitcnt vmcnt(0)" : "+v"(vb) : : "memory");
    vb ^= ut;
    turn();
    const u32x4 vs = va | vb | vc;
    if (__builtin_expect(__any(((vs[0] | vs[1] | vs[2] | vs[3]) & UMASK) != 0u), 0)) {
#pragma unroll 1
      for (int spin = 0; spin < 4; ++spin) {
        const bool na = ((va[0] | va[1] | va[2] | va[3]) & UMASK) != 0u, nb = ((vb[0] | vb[1] | vb[2] | vb[3]) & UMASK) != 0u, nc = ((vc[0] | vc[1] | vc[2] | vc[3]) & UMASK) != 0u;
        if (!__any(na || nb || nc)) break;
        TSC(16);
        if (na) LD16(va, vo);
        if (nb) LD16(vb, vo2);
        if (nc) LD16(vc, vo3);
        asm volatile("s_waitcnt vmcnt(0)" : "+v"(va), "+v"(vb), "+v"(vc) : : "memory");
        if (na) va ^= ut;
        if (nb) vb ^= ut;
        if (nc) vc ^= ut;
      }
      auto again = [&](u32x4& v, uint32_t o, int d) {
        const int J = g0 >> 5;
        const uint32_t dn = O_DN + 16u * (uint32_t)((d < 512) ? 4 * (d >> 1) + ((0x01013232 >> (4 * J)) & 3) : 4 * (d - 256) + (J >> 1));
        bool pend = ((v[0] | v[1] | v[2] | v[3]) & UMASK) != 0u;
#pragma unroll 1
        for (int spin = 0; spin < SPIN_CAP && __any(pend); ++spin) {
          TSC(16);
          SLEEP(PSLEEP);
          if (pend) {
            uint32_t fin = ~T;
            if ((v[0] & v[1] & v[2] & v[3] & LMASK) == LMASK)
              asm volatile("global_load_dword %0, %1, %2 sc1\n\ts_waitcnt vmcnt(0)" : "=&v"(fin) : "v"(dn), "s"(ws) : "memory");
            LD16W(v, o);
            v ^= ut;
            pend = ((v[0] | v[1] | v[2] | v[3]) & UMASK) != 0u;
            if (pend && fin == T) {
              u32x2 sc;
              for (int k = 0; k < SPIN_CAP; ++k) {
                asm volatile("global_load_dwordx2 %0, %1, %2 sc1\n\ts_waitcnt vmcnt(0)" : "=&v"(sc) : "v"(O_SC + 16u * d), "s"(ws) : "memory");
                if (sc[0] == T) break;
              }
              *(u16*)(smem + L_SC + 2 * d) = (u16)(sc[1] >> 16);
              cnt[10] = 1u;
              v = (v ^ ut) & ~LMASK;
              pend = false;
            }
          }
        }
      };
      again(va, vo, d1); again(vc, vo3, d3); again(vb, vo2, d2);
      turn1();
      turn();
    }
    u32x4 a0, a1;
    u32x2 a2, cf;
    {
      uint32_t cc, n = SPIN_CAP;
      asm volatile("s_mov_b64 exec, 1\n\tv_mov_b32 v90, 1\n\tds_add_u32 %6, v90\n\ts_mov_b64 exec, -1\n"
                   "1:\n\tds_read2_b32 %3, %6 offset1:10\n\tds_read_b128 %0, %7\n\tds_read_b128 %1, %7 offset:16\n\tds_read_b64 %2, %8\n\t"
                   "s_sub_u32 %5, %5, 1\n\ts_cbranch_scc1 2f\n\ts_waitcnt lgkmcnt(3)\n\tv_readfirstlane_b32 %4, v90\n\t"
                   "s_cmp_lt_u32 %4, 4\n\ts_cbranch_scc1 1b\n2:\n\ts_waitcnt lgkmcnt(0)"
                   : "=&v"(a0), "=&v"(a1), "=&v"(a2), "=&{v[90:91]}"(cf), "=&s"(cc), "+s"(n) : "v"(ca), "v"(lb0 + pa), "v"(lb0 + pb) : "memory", "scc");
    }
    TS(0, 9);
    if (__builtin_expect(__builtin_amdgcn_readfirstlane(cf[1]) != 0, 0)) {
      const u32x4 q = *(const u32x4*)(smem + L_SC + 16 * lane);
      const uint32_t r = *(const uint32_t*)(smem + L_SC + 1024 + 4 * (lane & 31));
#pragma unroll
      for (int k = 0; k < 4; ++k) { sv[2 * k] = bflo(q[k]); sv[2 * k + 1] = bfhi(q[k]); }
      sv[8] = bflo(r); sv[9] = bfhi(r);
    }
    const uint32_t av[10] = {a0[0], a0[1], a0[2], a0[3], a1[0], a1[1], a1[2], a1[3], a2[0] >> ps, a2[1] >> ps};
    f32x2 h[9];
#pragma unroll
    for (int k = 0; k < 10; ++k) {
      asm("v_fma_mix_f32 %0, %1, %2, %3 op_sel_hi:[1,0,0]" : "=v"(h[(k < 8) ? k >> 1 : 8][k & 1]) : "v"(av[k]), "v"(sv[k]), "v"(xf[(k < 8) ? k >> 1 : 8][k & 1]));
      if (k < 8) asm("v_fma_mix_f32 %0, %1, %2, %3 op_sel:[1,0,0] op_sel_hi:[1,0,0]" : "=v"(h[4 + (k >> 1)][k & 1]) : "v"(av[k]), "v"(sv[k]), "v"(xf[4 + (k >> 1)][k & 1]));
    }
#pragma unroll
    for (int k = 0; k < 9; ++k) hv[k] = pack2(h[k][0], h[k][1]);
    const float da = dot2(hv[2], wv[2], dot2(hv[0], wv[0], zb[0])) + dot2(hv[3], wv[3], dot2(hv[1], wv[1], zb[1]));
    const float db = dot2(hv[6], wv[2], dot2(hv[4], wv[0], zb[2])) + dot2(hv[7], wv[3], dot2(hv[5], wv[1], zb[3]));
    float dt = dot2(hv[8], wv[4], fold32(da, db));
    f32x2 rc, nc;
    ln_stats<true>(hv, lane, 1.f, xf, za, rc, nc, dt);
    dt = (lane < 32) ? dt * rc[0] + ws_ * nc[0] : dt * rc[1] + ws_ * nc[1];
    const uint32_t bb = pack2(dt, dt) & 0xffff0000u;
    const uint32_t kk = (bb ^ (((uint32_t)((int)bb >> 31) & 0x7fff0000u) | 0x80000000u)) | kid;
    u32x4 ka, kb;
    uint32_t old;
    asm volatile("s_mov_b64 exec, %5\n\tds_write_b32 %3, %4\n\ts_mov_b64 exec, 1\n\tv_mov_b32 %2, 1\n\tds_add_rtn_u32 %2, %7, %2\n\ts_mov_b64 exec, -1\n\t"
                 "ds_read_b128 %0, %6\n\tds_read_b128 %1, %6 offset:16\n\ts_waitcnt lgkmcnt(0)"
                 : "=&v"(ka), "=&v"(kb), "=&v"(old) : "v"(ka_), "v"(kk), "s"(0x8000000080000000ull), "v"(kb_), "v"(ca) : "memory");
    if (__builtin_amdgcn_readfirstlane(old) == 7) {
      uint32_t m1 = max(max(ka[0], ka[1]), ka[2]), m2 = med3u(ka[0], ka[1], ka[2]), m3 = min(min(ka[0], ka[1]), ka[2]);
#pragma unroll
      for (int e = 0; e < 5; ++e) {
        const uint32_t kx = e ? kb[e - 1] : ka[3];
        m3 = med3u(m2, m3, kx); m2 = med3u(m1, m2, kx); m1 = max(m1, kx);
      }
      if (lane < 8) ST16(ko_, ((u32x4){m1 | 24u, m2 | 16u, m3 | 8u, T & ~1u}));
    }
    TS(0, 10);
    keya = (uint32_t)__builtin_amdgcn_readlane(kk, 31); keyb = (uint32_t)__builtin_amdgcn_readlane(kk, 63);
    ln_apply<false>(xf, lane, rc, nc, nv);
    gwait(ca, 8);
    if (wl == 1) {
      const u32x4 kv = *(const u32x4*)&kl[4 * (lane & 1)];
      if (lane < 2) ST16(O_KF + (uint32_t)(2 * g2 + lane) * 16u, ((u32x4){__builtin_amdgcn_perm(kv[1], kv[0], 0x07060302u), T, __builtin_amdgcn_perm(kv[3], kv[2], 0x07060302u), T}));
      if (ROLE == 2 && b == 0 && lane == 0) ST4(O_EP, T * 0x0E8B2F51u);
    }
    const uint32_t ko = kl[lane & 7];
    rka = FORCEFB + __popcll(__ballot(ko > keya) & 0xffull); rkb = FORCEFB + __popcll(__ballot(ko > keyb) & 0xffull);
#pragma unroll
    for (int k = 0; k < 9; ++k) {
      const f32x2 o = xf[k] + (f32x2){bflo(nv[k]), bfhi(nv[k])};
      hv[k] = pack2(o[0], o[1]);
      nv[k] = min(nv[k], 0xfffffffeu);
    }
    if (rka < 2) put_row(nv, 0, ws, O_XC + (uint32_t)(1023 - 4 * g2 - rka) * 1152u);
    if (rkb < 2) put_row(nv, 1, ws, O_XC + (uint32_t)(1023 - 4 * g2 - rkb) * 1152u);
    if (!LPOS && ffn) down_dma(KN, 4, lane);
  }
  uint32_t k3rd = 0u, k4th = 0u;
  if constexpr (ROLE == 1) {
    u32x4 key;
    const uint32_t kof = O_KEY + (uint32_t)(256 * bx + tid) * 16u;
    until(tk);
    if (!ffn) SLEEP(NSLEEP);
    LD16(key, kof);
    if (ffn) {
      gwait0(ca + 4, 8);
      gu_read(1);
    }
    asm volatile("s_waitcnt vmcnt(0)" : "+v"(key) : : "memory");
    if (__builtin_expect(__any(key[3] != (T & ~1u)), 0)) {
#pragma unroll 1
      for (int spin = 0; spin < SPIN_CAP; ++spin) {
        const bool need = key[3] != (T & ~1u);
        if (!__any(need)) break;
        TSC(15);
        SLEEP(KBACK);
        if (need) LD16W(key, kof);
      }
    }
    TS(0, 11);
    const uint32_t m1 = oct_maxu(key[0]);
    const bool hit = key[0] == m1;
    const uint32_t k0 = hit ? key[1] : key[0], k1 = hit ? key[2] : key[1];
    const uint32_t m2 = oct_maxu(k0);
    if ((lane & 7) == 0) *(u32x2*)&cand[tid >> 2] = (u32x2){m1, m2};
    k3rd = (k0 == m2) ? k1 : k0;
    k4th = (k0 == m2) ? (hit ? 0u : key[2]) : k1;
    if (RPF == 1 && !ffn && w == (bj & 3)) {
      const uint32_t r1 = O_XC + ((m1 >> 3) & 0x3ffu) * 1152u + 128u * (lane & 7), r2 = O_XC + ((m2 >> 3) & 0x3ffu) * 1152u + 128u * (lane & 7);
      asm volatile("global_load_dword v126, %0, %3 sc1\n\tglobal_load_dword v126, %1, %3 sc1\n\tglobal_load_dword v126, %2, %3 offset:1024 sc1"
                   : : "v"(r1), "v"(r2), "v"(((lane & 1) ? r1 : r2) - 128u * (lane & 7)), "s"(ws) : "memory", "v126");
    }
  }
  auto pick = [&](const uint32_t* cd, uint32_t* sl, bool pf = false) {
    BAR();
    if (RPF == 2 && ROLE == 2 && pf && ffn && w == 7) {
      const uint32_t r1 = O_XC + ((cd[16 * (bj & 3) + (lane >> 2)] >> 3) & 0x3ffu) * 1152u + 128u * (lane & 3);
      asm volatile("global_load_dword v126, %0, %1 sc1\n\tglobal_load_dword v126, %0, %1 offset:512 sc1\n\tglobal_load_dword v126, %0, %1 offset:640 sc1"
                   : : "v"(r1), "s"(ws) : "memory", "v126");
    }
    const uint32_t c = cd[lane], own = cd[8 * w + (lane & 7)];
    const u32x4 la = *(const u32x4*)&cd[8 * w], lb = *(const u32x4*)&cd[8 * w + 4];
    uint32_t r = C_;
    asm("v_cmp_gt_u32 s[40:41], %1, %2\n\tv_cmp_gt_u32 s[42:43], %1, %3\n\tv_cmp_gt_u32 s[44:45], %1, %4\n\tv_cmp_gt_u32 s[46:47], %1, %5\n\t"
        "v_cmp_gt_u32 s[48:49], %1, %6\n\tv_cmp_gt_u32 s[50:51], %1, %7\n\tv_cmp_gt_u32 s[52:53], %1, %8\n\tv_cmp_gt_u32 s[54:55], %1, %9\n\t"
        "s_bcnt1_i32_b64 s40, s[40:41]\n\ts_bcnt1_i32_b64 s42, s[42:43]\n\tv_writelane_b32 %0, s40, 0\n\ts_bcnt1_i32_b64 s44, s[44:45]\n\t"
        "v_writelane_b32 %0, s42, 1\n\ts_bcnt1_i32_b64 s46, s[46:47]\n\tv_writelane_b32 %0, s44, 2\n\ts_bcnt1_i32_b64 s48, s[48:49]\n\t"
        "v_writelane_b32 %0, s46, 3\n\ts_bcnt1_i32_b64 s50, s[50:51]\n\tv_writelane_b32 %0, s48, 4\n\ts_bcnt1_i32_b64 s52, s[52:53]\n\t"
        "v_writelane_b32 %0, s50, 5\n\ts_bcnt1_i32_b64 s54, s[54:55]\n\tv_writelane_b32 %0, s52, 6\n\tv_writelane_b32 %0, s54, 7"
        : "+v"(r) : "v"(c), "v"(la[0]), "v"(la[1]), "v"(la[2]), "v"(la[3]), "v"(lb[0]), "v"(lb[1]), "v"(lb[2]), "v"(lb[3])
        : "scc", "s40", "s41", "s42", "s43", "s44", "s45", "s46", "s47", "s48", "s49", "s50", "s51", "s52", "s53", "s54", "s55");
    sl[min(r, (uint32_t)C_)] = own;
    BAR();
  };
  auto rows = [&](uint32_t ao, auto&& meanwhile) {
    u32x4 aa, ab, ac = __builtin_bit_cast(u32x4, zb);
    asm volatile("global_load_dwordx4 %0, %3, %4 sc1\n\tglobal_load_dwordx4 %1, %3, %4 offset:512 sc1\n\t"
                 "s_cmp_lg_u32 %5, 0\n\ts_cbranch_scc1 1f\n\tglobal_load_dwordx4 %2, %3, %4 offset:1024 sc1\n1:"
                 : "=&v"(aa), "=&v"(ab), "+v"(ac) : "v"(ao), "s"(ws), "s"(w) : "memory", "scc");
    __builtin_amdgcn_sched_barrier(0);
    meanwhile();
    asm volatile("s_waitcnt vmcnt(0)" : "+v"(aa), "+v"(ab), "+v"(ac) : : "memory");
    if (__builtin_expect(__any(max(max(max4(aa), max4(ab)), max4(ac)) == 0xffffffffu), 0)) {
#pragma unroll 1
      for (int spin = 0; spin < SPIN_CAP; ++spin) {
        const bool na = max4(aa) == 0xffffffffu, nb = max4(ab) == 0xffffffffu, nc = max4(ac) == 0xffffffffu;
        if (!__any(na || nb || nc)) break;
        TSC(14);
        SLEEP(PSLEEP);
        if (na) LD16W(aa, ao);
        if (nb) LD16W(ab, ao + 512u);
        if (nc) LD16W(ac, ao + 1024u);
      }
    }
    *(u32x4*)(smem + LA + 16 * tid) = aa;
    *(u32x4*)(smem + LA + 4096 + 16 * tid) = ab;
    if (w == 0) *(u32x4*)(smem + LA + 8192 + 16 * tid) = ac;
  };
  auto rows_dma = [&](uint32_t ao) {
    uint32_t dl = lb0 + LA + 1024u * w, keep;
    asm volatile("s_mov_b32 %0, m0\n\ts_mov_b32 m0, %1\n\ts_add_u32 %1, %1, 4096 - 512\n\tglobal_load_lds_dwordx4 %2, %3 sc1\n\ts_mov_b32 m0, %1\n\ts_add_u32 %1, %1, 4096 - 512\n\t"
                 "global_load_lds_dwordx4 %2, %3 offset:512 sc1\n\ts_cmp_lg_u32 %4, 0\n\ts_cbranch_scc1 1f\n\ts_mov_b32 m0, %1\n\ts_nop 0\n\t"
                 "global_load_lds_dwordx4 %2, %3 offset:1024 sc1\n1:\n\ts_mov_b32 m0, %0" : "=&s"(keep), "+s"(dl) : "v"(ao), "s"(ws), "s"(w) : "memory", "scc");
  };
  auto get_bd = [&](int t0, int t1) {
#pragma unroll
    for (int t = t0; t < t1; ++t) {
      const u32x2 h0 = trd(da + 512 * (t >> 2) + 32 * (t & 3)), h1 = trd(da + 512 * (t >> 2) + 32 * (t & 3) + 4608 + 32);
      bw[t] = (u32x4){h0[0], h0[1], h1[0], h1[1]};
    }
#pragma unroll
    for (int t = t0; t < t1; ++t) asm volatile("" : "+v"(bw[t]));
  };
  pick(cand, sel, true);
  uint32_t mykey = sel[lane & 7], ro = ((mykey >> 3) & 0x3ffu) * 1152u;
  TS(0, 12);
  auto check = [&] {
    cnt[(k3rd > (uint32_t)__builtin_amdgcn_readlane(mykey, 7)) ? 5 : 6] = 1u;
    const uint32_t m3 = oct_maxu(k3rd);
    if (k3rd == m3) k3rd = k4th;
    if ((lane & 7) == 0) cand[72 + (tid >> 3)] = m3;
    if (w == 0 && lane < 32) cand[(lane < 8) ? 64 + lane : 96 + lane] = (lane < 8) ? mykey : 0u;
  };
  if constexpr (ROLE == 1) {
    if (ffn && !FORCEFB) rows_dma(ro + al);
    check();
    DRAIN();
  } else if (ffn) {
    if (LPOS) { SLEEP(LSLEEP); down_dma(KN, 4, lane); } else { DRAIN(); gsync0(ca, 12); get_bd(0, 8); }
  }
  BAR();
  TS(0, 5);
  auto put_out = [&](int j, uint32_t k0, int rk) {
    const uint64_t mine = __ballot(((mykey ^ k0) & ~24u) == 0u) & 0xffull;
    if (mine == 0ull) put_row(hv, j, out, (uint32_t)((8 * g2 + 2 * wl + j) * (D_ * 2)));
    else if (rk >= 2) put_row(nv, j, ws, O_FB + 1152u * (uint32_t)__builtin_ctzll(mine));
  };
  auto rest_out = [&] { put_out(0, keya, rka); put_out(1, keyb, rkb); };
  f32x4 lo, hi;
  auto gate_up = [&] {
    u32x4 av[5];
    const uint32_t aa = lb0 + LA + 16u * lane;
#pragma unroll
    for (int j = 0; j < 5; ++j) asm volatile("ds_read_b128 %0, %1 offset:%2" : "=v"(av[j]) : "v"(aa), "n"(1024 * j) : "memory");
    lo = (f32x4){0.f, 0.f, 0.f, 0.f}; hi = lo;
#pragma unroll
    for (int j = 0; j < 9; ++j) {
      if (j < 5) asm volatile("s_waitcnt lgkmcnt(4)" : "+v"(av[j % 5]));
      else if (j == 5) asm volatile("s_waitcnt lgkmcnt(2)" : "+v"(av[0]), "+v"(av[1]));
      else if (j == 7) asm volatile("s_waitcnt lgkmcnt(0)" : "+v"(av[2]), "+v"(av[3]));
      lo = MMB(av[j % 5], bw[2 * j], lo);
      hi = MMB(av[j % 5], bw[2 * j + 1], hi);
      __builtin_amdgcn_sched_barrier(0);
      if (j < 4) asm volatile("ds_read_b128 %0, %1 offset:%2" : "=v"(av[j]) : "v"(aa), "n"(1024 * (j + 5)) : "memory");
    }
  };
  uint32_t fl;
  asm volatile("ds_read_b32 %0, %1" : "=v"(fl) : "v"(ca + 20) : "memory");
  if (ROLE == 1) gate_up();
  asm volatile("s_waitcnt lgkmcnt(0)" : "+v"(fl));
  const bool slow = FORCESLOW || FORCEFB || __builtin_amdgcn_readfirstlane(fl) != 0;
  if (__builtin_expect(slow, 0)) {
    TSC(17);
    pick(cand + 64, sel + 16);
    mykey = sel[16 + (lane & 7)];
    ro = ((mykey >> 3) & 0x3ffu) * 1152u;
    if (ROLE == 1 && (k3rd > (uint32_t)__builtin_amdgcn_readlane(mykey, 7) || (mykey & 24u) == 8u)) cnt[11] = 1u;
    BAR();
    if (FORCESLOW > 1 || FORCEFB || __builtin_amdgcn_readfirstlane(cnt[11]) != 0) {
    u32x4 key;
#pragma unroll 1
    for (int spin = 0; spin < SPIN_CAP; ++spin) {
      LD16W(key, O_KF + 16u * tid);
      if (!__any(key[1] != T || key[3] != T)) break;
      SLEEP(KBACK);
    }
    const uint32_t ib = (uint32_t)(((255 - (tid >> 1)) << 5) | (7 - 4 * (tid & 1)));
    uint32_t k0 = (key[0] << 16) | ib, k1 = (key[0] & 0xffff0000u) | (ib - 1), k2 = (key[2] << 16) | (ib - 2), k3 = (key[2] & 0xffff0000u) | (ib - 3);
    CEX(k0, k1) CEX(k2, k3) CEX(k0, k2) CEX(k1, k3) CEX(k1, k2)
#pragma unroll 1
    for (int c = 0; c < C_; ++c) {
      const uint32_t mx = wave_maxu(k0);
      const bool hit = (k0 == mx);
      k0 = hit ? k1 : k0; k1 = hit ? k2 : k1; k2 = hit ? k3 : k2; k3 = hit ? 0u : k3;
      if (lane == 0) cand[64 + 8 * w + c] = mx;
    }
    pick(cand + 64, sel + 32);
    mykey = sel[32 + (lane & 7)];
    const uint32_t okey = sel[32 + (lane >> 3)];
    const uint64_t same = __ballot(((mykey ^ okey) & 0x1fe0u) == 0u && okey > mykey);
    const int sl = FORCEFB + __popcll((same >> (lane & 7)) & 0x0101010101010101ull);
    ro = (sl < 2) ? (((mykey >> 3) & 0x3ffu) + 3 - sl) * 1152u : O_FB - O_XC + 1152u * (lane & 7);
    }
    if (ln2) rest_out();
    if (ffn) {
      if (ROLE == 1) rows(ro + al, [] {});
      BAR();
      if (ROLE == 1) gate_up();
    }
  }
  if (ROLE == 1 && ffn && !slow && __builtin_expect(__any(lo[0] != lo[0] || lo[1] != lo[1] || lo[2] != lo[2] || lo[3] != lo[3] ||
                                                           hi[0] != hi[0] || hi[1] != hi[1] || hi[2] != hi[2] || hi[3] != hi[3]), 0)) {
    gsync0(ca + 4, 12);
    rows(ro + al, [] {});
    gsync0(ca + 4, 16);
    gate_up();
  }
  if (OPOS == 0 && ROLE == 2 && ffn && !slow) { SLEEP(OD); rest_out(); }
  if (!ffn) {
    if (ln2 && !slow) { SLEEP(OD2); rest_out(); }
    TSE();
    return;
  }
  uint32_t fx = 0, fv = 0, fn = 0, fo = 0, sa[9];
  float rs, hx, xs;
  u32x4 se;
  if constexpr (ROLE == 1) {
    const f32x4 lh = (lane < 32) ? lo : hi;
    const float fa = fold32(lh[0], lh[2]), fb2 = fold32(lh[1], lh[3]);
    const float g = __int_as_float(__builtin_amdgcn_update_dpp(__float_as_int(fa), __float_as_int(fb2), 0x128, 0xf, 0xc, false));
    const float u = __int_as_float(__builtin_amdgcn_update_dpp(__float_as_int(fb2), __float_as_int(fa), 0x128, 0xf, 0x3, false));
    const uint32_t gu = pack2(g, u);
    const float gp = bflo(gu);
    const float sg = rbf(gp * __builtin_amdgcn_rcpf(1.f + __expf(-gp)));
    *(u16*)((char*)hm + ha) = f2bf(sg * bfhi(gu));
  } else if (LPOS) {
    DRAIN();
    gsync0(ca, 12);
    get_bd(0, 8);
  }
  BAR();
  TS(0, 6);
  if (ROLE == 2 || w == 2) {
    const u32x4 a0 = *(const u32x4*)((char*)hm + h0a), a1 = *(const u32x4*)((char*)hm + h1a);
    if (ROLE == 1) get_bd(0, 4);
    const f32x4 z = {0.f, 0.f, 0.f, 0.f};
    f32x4 o[4];
#pragma unroll
    for (int t = 0; t < 2 * ROLE; ++t) o[t] = MMB(a1, bw[2 * t + 1], MMB(a0, bw[2 * t], z));
#pragma unroll
    for (int t = 0; t < ROLE; ++t) {
      u32x4 sv;
#pragma unroll
      for (int r = 0; r < 4; ++r) sv[r] = min(pack2(o[2 * t][r], o[2 * t + 1][r]), 0xfffffffeu);
      ST16(so + 4096u * t, sv);
    }
    TS(0, 13);
  }
  if (OPOS == 1 && ROLE == 2) {
    SLEEP(OSLEEP);
    if (!slow) rest_out();
  }
  if (ROLE == 1 && w == 1) {
    const int q = (fb & 15) >> 2, ts = 4 * (q & 1) + ((i & 7) >> 1);
    const int dd = 64 * (fb >> 4) + 16 * (2 * (i & 1) + (q >> 1)) + 4 * (fb & 3) + 2 * (kq & 1) + (kq >> 1);
    const uint32_t fkey = (uint32_t)__builtin_amdgcn_ds_bpermute(4 * ts, (int)mykey), fr = (uint32_t)__builtin_amdgcn_ds_bpermute(4 * ts, (int)ro);
    const int ft = 2047 - (int)(((fkey >> 2) & 0x7f8u) | (fkey & 7u));
    fo = (uint32_t)(ft * D_ + dd) * 2u;
    u32x2 fs;
    asm volatile("global_load_ushort %0, %4, %8\n\tglobal_load_ushort %1, %5, %9 sc1\n\tglobal_load_ushort %2, %6, %9 sc1\n\tglobal_load_dwordx2 %3, %7, %9 sc1"
                 : "=&v"(fx), "=&v"(fv), "=&v"(fn), "=&v"(fs) : "v"(fo), "v"(v_at(dd, ft >> 3) + 2u * (ft & 7)), "v"(O_XC + fr + 2u * dd), "v"(O_SC + 16u * dd), "s"(x), "s"(ws) : "memory");
    uint32_t t16 = (((T >> (3 * ((ft >> 1) & 3))) & LMASK) | (((T >> (12 + ((ft >> 1) & 3))) & 0x00010001u) << 14)) >> (16 * (ft & 1)) & 0xffffu;
    const uint32_t o = fkey >> 16;
    const float rs0 = bf2f((u16)((o & 0x8000u) ? (o & 0x7fffu) : (~o & 0xffffu)));
    rs = rbf(__builtin_amdgcn_rcpf(1.f + __expf(-rs0)));
    const uint32_t one = (i < 8) ? (0x3f80u << (16 * (i & 1))) : 0u;
    se = (u32x4){(i >> 1) == 0 ? one : 0u, (i >> 1) == 1 ? one : 0u, (i >> 1) == 2 ? one : 0u, (i >> 1) == 3 ? one : 0u};
#pragma unroll
    for (int m = 0; m < 9; ++m) sa[m] = O_SL + (uint32_t)(16 * m + (lane >> 2)) * SP + (uint32_t)(4 * fb + (lane & 3)) * 16u;
    asm volatile("" : "+v"(t16), "+v"(rs), "+v"(se), "+v"(sa[0]), "+v"(sa[1]), "+v"(sa[2]), "+v"(sa[3]), "+v"(sa[4]), "+v"(sa[5]), "+v"(sa[6]), "+v"(sa[7]), "+v"(sa[8]));
    SLEEP(SSLEEP);
    TS(0, 1);
    float sum;
#pragma unroll 1
    for (int spin = 0; spin < SPIN_CAP; ++spin) {
      u32x4 sv[9];
#pragma unroll
      for (int m = 0; m < 9; ++m) LD16(sv[m], sa[m]);
      asm volatile("s_waitcnt vmcnt(9)" : "+v"(fx), "+v"(fv), "+v"(fn), "+v"(fs));
      if (__builtin_expect(__any(fs[0] != T), 0)) {
        for (int k = 0; k < SPIN_CAP && fs[0] != T; ++k)
          asm volatile("global_load_dwordx2 %0, %1, %2 sc1\n\ts_waitcnt vmcnt(0)" : "=&v"(fs) : "v"(O_SC + 16u * dd), "s"(ws) : "memory");
      }
      const uint32_t f1 = fv ^ t16, f2 = (f1 & 7u) ? fv & ~7u : f1;
      hx = rbf(bf2f((u16)fx) + (float)__builtin_bit_cast(_Float16, (u16)f2) * __uint_as_float(fs[1]));
      xs = bf2f((u16)fn);
      asm volatile("" : "+v"(hx), "+v"(xs));
      f32x4 dv = {0.f, 0.f, 0.f, 0.f};
      static_for<0, 9>([&](auto mm) {
        constexpr int m = mm.value;
        asm volatile("s_waitcnt vmcnt(%1)" : "+v"(sv[m]) : "n"(8 - m));
        dv = MMB(sv[m], se, dv);
        __builtin_amdgcn_sched_barrier(0);
      });
      sum = fold16(fold32(dv[0], dv[1]), fold32(dv[2], dv[3]));
      if (__builtin_expect(!__any(sum != sum), 1)) break;
      uint32_t mx = 0u;
#pragma unroll
      for (int m = 0; m < 9; ++m) mx = max(mx, max4(sv[m]));
      if (!__any(mx == 0xffffffffu)) break;
      TSC(18);
      SLEEP(PSLEEP);
    }
    const float o1 = rbf(xs + rbf(rbf(sum) * rs));
    if (i < 8) asm volatile("global_store_short %0, %1, %2 sc1" : : "v"(fo), "v"((uint32_t)f2bf(hx + o1)), "s"(out) : "memory");
    TS(0, 2);
  }
  TSE();
  }
  }
}
#define BROT "192"
#define CPF_V ""
#define CPF_A "s_getpc_b64 s[96:97]\n\ts_lshl_b32 s99, s2, 4\n\ts_lshl_b32 s84, s98, 7\n\ts_add_u32 s99, s99, s84\n\tv_and_b32 v112, 1, v0\n\t" \
              "v_lshl_add_u32 v112, v112, 12, s99\n\tv_min_u32 v112, kf_end-kf_all-0x80, v112\n\ts_mov_b64 exec, 3\n\tglobal_load_dword v112, v112, s[96:97]\n\ts_mov_b64 exec, -1\n\t"
extern "C" __global__ __launch_bounds__(512, 4) void kf_all(KARGS) {
  __shared__ __attribute__((aligned(16))) char smem[SMEM];
  asm volatile("s_load_dwordx16 s[64:79], s[0:1], 0x0\n\ts_load_dwordx4 s[80:83], s[0:1], 0x40\n\tv_readfirstlane_b32 s98, v0\n\t"
               "s_add_u32 s84, s2, " BROT "\n\ts_xor_b32 s84, s84, s2\n\ts_bitset0_b32 s84, 8\n\ts_bitset0_b32 s84, 9\n\ts_xor_b32 s2, s2, s84\n\t"
               "v_mov_b32 v109, 0\n\ts_and_b32 s85, s2, 7\n\ts_bitcmp1_b32 s2, 8\n\ts_cbranch_scc1 1f\n\t" CPF_A "s_bitcmp1_b32 s98, 8\n\ts_cbranch_scc1 2f\n\t"
               "s_andn2_b32 s84, s2, 31\n\ts_lshl2_add_u32 s84, s85, s84\n\ts_bfe_u32 s85, s2, 0x20003\n\ts_or_b32 s84, s84, s85\n\t"
               "s_mulk_i32 s84, 0x2400\n\ts_mulk_i32 s98, 36\n\ts_add_u32 s84, s84, s98\n\tv_and_b32 v110, 63, v0\n\tv_and_b32 v111, 32, v0\n\t"
               "v_lshlrev_b32 v111, 5, v111\n\tv_lshl_add_u32 v111, v110, 2, v111\n\tv_lshl_add_u32 v110, v110, 4, s84\n\tv_add_u32 v111, s84, v111\n\t"
               "s_cmpk_lt_u32 s2, 0x90\n\ts_waitcnt lgkmcnt(0)\n\t"
               "global_load_dwordx4 v[100:103], v110, s[64:65]\n\tglobal_load_dwordx4 v[104:107], v110, s[64:65] offset:1152\n\t"
               "global_load_dword v108, v111, s[64:65] offset:1024\n\tglobal_load_dword v109, v109, s[82:83] sc1\n\t"
               "s_cbranch_scc1 kf_n\n\ts_branch kf_h\n"
               "1:\n\ts_bfe_u32 s84, s2, 0x50003\n\ts_lshl_b32 s85, s85, 5\n\ts_or_b32 s84, s84, s85\n\ts_lshl_b32 s84, s84, 2\n\t"
               "v_and_b32 v110, 0xff, v0\n\tv_bfe_u32 v107, v0, 7, 1\n"
               "4:\n\ts_lshl_b32 s85, s84, 7\n\tv_lshl_add_u32 v110, v110, 1, s85\n\tv_lshl_add_u32 v107, v107, 1, s84\n\ts_and_b32 s85, s98, s2\n\ts_waitcnt lgkmcnt(0)\n\t"
               "global_load_ushort v100, v107, s[78:79]\n\tglobal_load_ushort v101, v110, s[66:67]\n\tglobal_load_ushort v102, v110, s[68:69]\n\t"
               "global_load_dword v109, v109, s[82:83] sc1\n\ts_bitcmp1_b32 s85, 8\n\ts_cbranch_scc1 kf_u\n\t"
               "global_load_ushort v103, v110, s[70:71]\n\tglobal_load_ushort v104, v110, s[72:73]\n\tglobal_load_ushort v105, v110, s[74:75]\n\t"
               "global_load_ushort v106, v110, s[76:77]\n\tglobal_load_ushort v107, v107, s[80:81]\n\ts_bitcmp1_b32 s2, 8\n\ts_cbranch_scc1 kf_c\n"
               "6:\n\ts_barrier\n\ts_branch kf_p\n"
               "2:\n\t" CPF_V "s_cmpk_lt_u32 s2, 0xc0\n\ts_cbranch_scc1 6b\n\t"
               "s_lshr_b32 s84, s2, 3\n\ts_lshl3_add_u32 s84, s85, s84\n\ts_lshl1_add_u32 s84, s84, 976\n\t"
               "v_and_b32 v110, 0x7f, v0\n\tv_mov_b32 v107, 0\n\ts_branch 4b"
               : : "s"(__builtin_amdgcn_kernarg_segment_ptr()), "s"(blockIdx.x), "v"(threadIdx.x),
                   "s"((uint32_t)(uintptr_t)(__attribute__((address_space(3))) char*)smem) : "scc", "s96", "s97", "s98", "s99", "v127");
}
extern "C" __global__ __launch_bounds__(512, 4) void kf_c(KARGS) {
  __shared__ __attribute__((aligned(16))) char smem[SMEM];
  role_body<0>(smem, KPASS);
}
extern "C" __global__ __launch_bounds__(512, 4) void kf_u(KARGS) {
  __shared__ __attribute__((aligned(16))) char smem[SMEM];
  role_body<3>(smem, KPASS);
}
extern "C" __global__ __launch_bounds__(512, 4) void kf_n(KARGS) {
  __shared__ __attribute__((aligned(16))) char smem[SMEM];
  role_body<1, 1>(smem, KPASS);
}
extern "C" __global__ __launch_bounds__(512, 4) void kf_h(KARGS) {
  __shared__ __attribute__((aligned(16))) char smem[SMEM];
  role_body<1, 0>(smem, KPASS);
}
extern "C" __global__ __launch_bounds__(512, 4) void kf_p(KARGS) {
  __shared__ __attribute__((aligned(16))) char smem[SMEM];
  role_body<2>(smem, KPASS);
}
extern "C" __global__ __launch_bounds__(512, 4) void kf_conv() { asm volatile(CONV_ASM ::: CONV_CLOB); }
extern "C" __global__ void kf_end() {}

extern "C" __global__ __launch_bounds__(1024) void k_probe(int* dummy) {
  __shared__ int sm[2];
  const uint32_t now = (uint32_t)__builtin_amdgcn_s_memrealtime();
  __shared__ int sn;
  if (threadIdx.x == 0) { sm[0] = 0; sm[1] = 1 == 1 ? 0x7fffffff : 0; sn = 0; }
  __syncthreads();
  for (int k = threadIdx.x; k < 4096; k += 1024) {
    const uint32_t a = (uint32_t)g_prb[k], b = (uint32_t)(g_prb[k] >> 32);
    if (a) atomicMax(&sm[0], (int)(now - a));
    if (b) { if (1 == 1) atomicMin(&sm[1], (int)(now - b)); else if (1 == 0) atomicMax(&sm[1], (int)(now - b)); else { atomicAdd(&sm[1], (int)(now - b)); atomicAdd(&sn, 1); } }
  }
  __syncthreads();
  const int q = min(max(sm[0] * 10 - (1 == 2 ? sm[1] * 10 / max(sn, 1) : sm[1] * 10), 0), 30000);
  if (threadIdx.x == 0) while ((int)((uint32_t)__builtin_amdgcn_s_memrealtime() - now) < q) __builtin_amdgcn_s_sleep(2);
}
extern "C" void kernel_launch(void* const* d_in, const int* in_sizes, int n_in,
                              void* d_out, int out_size, void* d_ws, size_t ws_size,
                              hipStream_t stream) {
  kf_all<<<512, 512, 0, stream>>>((const u16*)d_in[0], (const u16*)d_in[1], (const u16*)d_in[2], (const u16*)d_in[3], (const u16*)d_in[4],
                                  (const u16*)d_in[5], (const u16*)d_in[6], (const u16*)d_in[7], (const u16*)d_in[8], (char*)d_ws + (size_t)WSOFF * 1048576,
                                  (const u16*)d_in[10], (const u16*)d_in[11], (const u16*)d_in[12], (const u16*)d_in[13], (u16*)d_out);
  k_probe<<<1, 1024, 0, stream>>>((int*)d_ws);
}
```

```cpp
#include <hip/hip_runtime.h>
#include <hip/hip_bf16.h>
#include <stdint.h>
#define LPOS 1
#define LSLEEP 1
#define VD2 390
#define V2D 22
#define KD 178

typedef __attribute__((ext_vector_type(8))) short bf16x8;
typedef __attribute__((ext_vector_type(4))) float f32x4;
typedef uint16_t u16;
typedef __attribute__((ext_vector_type(4))) uint32_t u32x4;
typedef __attribute__((ext_vector_type(2))) uint32_t u32x2;
#define TSD uint32_t pr0_ = 0u, pr1_ = 0u
__device__ unsigned long long g_prb[4096];
#define TS(k, i) do { if ((i) == 0) pr0_ = (uint32_t)__builtin_amdgcn_s_memrealtime() | 1u; \
    if (ROLE == 1 && (i) == 11 && (2 < 0 || w == 2) && (blockIdx.x & 255) < 144) pr1_ = (uint32_t)__builtin_amdgcn_s_memrealtime() | 1u; } while (0)
#define TSC(i)
#define TSE() do { if (__builtin_amdgcn_mbcnt_hi(-1, __builtin_amdgcn_mbcnt_lo(-1, 0)) == 0) \
    __hip_atomic_store(&g_prb[blockIdx.x * 8 + w], ((unsigned long long)pr1_ << 32) | pr0_, __ATOMIC_RELAXED, __HIP_MEMORY_SCOPE_AGENT); } while (0)
#define TSP
#define TSA

constexpr int L_ = 2048, D_ = 576, N_ = 128, F_ = 4608, C_ = 8;
constexpr int NF = 144;
constexpr uint32_t O_EP = 0;
constexpr uint32_t O_KF = 4096;
constexpr uint32_t O_KEY = O_KF + 8192;
constexpr uint32_t O_SC = O_KEY + 32768;
constexpr uint32_t O_DN = O_SC + 9216;
constexpr uint32_t O_SU = O_DN + 20480;
constexpr uint32_t O_FB = O_SU + 262144;
constexpr uint32_t O_XC = O_FB + 8 * 1152;
constexpr uint32_t SP = 9216 + 128;
constexpr uint32_t O_SL = O_XC + 1024 * 1152;
constexpr uint32_t SLU = 336;
constexpr uint32_t O_U = O_SL + 256 * SLU * 16;
constexpr int UP = 584;
constexpr uint32_t VP = 8192 + 128, VPP = 4096 + 128;
constexpr uint32_t O_V = O_U + 256 * UP * 16, O_VP = O_V + 256 * VP;
static_assert(256 * SLU * 16 >= NF * SP && O_SL % 128 == 0 && O_U % 128 == 0 && O_V % 128 == 0, "");
__host__ __device__ __forceinline__ uint32_t u_at(int tg, int d) { return O_U + 16u * (uint32_t)((tg >> 1) * (UP * 2) + (d >> 2) * 8 + (tg & 1) * 4 + (d & 3)); }
__host__ __device__ __forceinline__ uint32_t v_at(int d, int tg) {
  return (d < 512) ? O_V + (uint32_t)(d >> 1) * VP + 16u * (uint32_t)(2 * tg + (d & 1)) : O_VP + (uint32_t)(d - 512) * VPP + 16u * (uint32_t)tg;
}
#define HAVE_VAT
#define HAVE_VTAG
__device__ __forceinline__ float bf2f(u16 v) { return __uint_as_float((uint32_t)v << 16); }
__device__ __forceinline__ float bflo(uint32_t v) { return __uint_as_float(v << 16); }
__device__ __forceinline__ float bfhi(uint32_t v) { return __uint_as_float(v & 0xffff0000u); }
__device__ __forceinline__ u16 f2bf(float f) { return __builtin_bit_cast(u16, (__bf16)f); }
__device__ __forceinline__ float rbf(float f) { return bf2f(f2bf(f)); }
typedef __attribute__((ext_vector_type(2))) float f32x2;
typedef __attribute__((ext_vector_type(2))) __bf16 bf16x2v;
__device__ __forceinline__ uint32_t pack2(float a, float b) {
  return __builtin_bit_cast(uint32_t, __builtin_convertvector((f32x2){a, b}, bf16x2v));
}

#define DPP_I(v, ctrl, rmask) __builtin_amdgcn_update_dpp(0, (v), (ctrl), (rmask), 0xf, true)
__device__ __forceinline__ float row_sum(float v) {
  v += __int_as_float(DPP_I(__float_as_int(v), 0x111, 0xf));
  v += __int_as_float(DPP_I(__float_as_int(v), 0x112, 0xf));
  v += __int_as_float(DPP_I(__float_as_int(v), 0x114, 0xf));
  v += __int_as_float(DPP_I(__float_as_int(v), 0x118, 0xf));
  return v;
}
__device__ __forceinline__ float rdl(float v, int l) { return __int_as_float(__builtin_amdgcn_readlane(__float_as_int(v), l)); }
__device__ __forceinline__ float wave_sum(float v) {
  v = row_sum(v);
  asm("s_nop 1\n\tv_add_f32_dpp %0, %0, %0 row_bcast:15 row_mask:0xa bank_mask:0xf\n\t"
      "s_nop 1\n\tv_add_f32_dpp %0, %0, %0 row_bcast:31 row_mask:0xc bank_mask:0xf\n\ts_nop 0" : "+v"(v));
  return rdl(v, 63);
}
__device__ __forceinline__ float fold32(float a, float b) {
  const auto r = __builtin_amdgcn_permlane32_swap(__float_as_uint(a), __float_as_uint(b), false, false);
  return __uint_as_float(r[0]) + __uint_as_float(r[1]);
}
__device__ __forceinline__ float fold16(float a, float b) {
  const auto r = __builtin_amdgcn_permlane16_swap(__float_as_uint(a), __float_as_uint(b), false, false);
  return __uint_as_float(r[0]) + __uint_as_float(r[1]);
}
__device__ __forceinline__ void wave_sum2(float& a, float& b) {
  float v = row_sum(fold32(a, b));
  asm("s_nop 1\n\tv_add_f32_dpp %0, %0, %0 row_bcast:15 row_mask:0xa bank_mask:0xf\n\ts_nop 0" : "+v"(v));
  a = rdl(v, 31); b = rdl(v, 63);
}
__device__ __forceinline__ float dot2(uint32_t a, uint32_t b, float c) {
  return __builtin_amdgcn_fdot2_f32_bf16(__builtin_bit_cast(bf16x2v, a), __builtin_bit_cast(bf16x2v, b), c, false);
}
__device__ __forceinline__ uint32_t row_maxu(uint32_t v) {
  v = max(v, (uint32_t)DPP_I((int)v, 0xb1, 0xf));
  v = max(v, (uint32_t)DPP_I((int)v, 0x4e, 0xf));
  v = max(v, (uint32_t)DPP_I((int)v, 0x141, 0xf));
  v = max(v, (uint32_t)DPP_I((int)v, 0x140, 0xf));
  return v;
}
__device__ __forceinline__ uint32_t wave_maxu(uint32_t v) {
  v = max(v, (uint32_t)DPP_I((int)v, 0x111, 0xf));
  v = max(v, (uint32_t)DPP_I((int)v, 0x112, 0xf));
  v = max(v, (uint32_t)DPP_I((int)v, 0x114, 0xf));
  v = max(v, (uint32_t)DPP_I((int)v, 0x118, 0xf));
  v = max(v, (uint32_t)DPP_I((int)v, 0x142, 0xa));
  v = max(v, (uint32_t)DPP_I((int)v, 0x143, 0xc));
  return (uint32_t)__builtin_amdgcn_readlane((int)v, 63);
}

typedef __attribute__((ext_vector_type(8))) _Float16 f16x8;
typedef __attribute__((ext_vector_type(2))) _Float16 f16x2;
__device__ __forceinline__ uint32_t packh(f32x2 v) { return __builtin_bit_cast(uint32_t, __builtin_convertvector(v, f16x2)); }
__device__ __forceinline__ uint32_t packh(float a, float b) { return packh((f32x2){a, b}); }
constexpr int IP = 224, I_X = 128;
__device__ __forceinline__ f32x2 cmul(f32x2 z, f32x2 w) {
  f32x2 r;
  asm("v_pk_mul_f32 %0, %1, %2 op_sel_hi:[0,1]\n\tv_pk_fma_f32 %0, %1, %2, %0 op_sel:[1,1,0] op_sel_hi:[1,0,1] neg_lo:[0,1,0]" : "=&v"(r) : "v"(z), "v"(w));
  return r;
}
__device__ __forceinline__ f32x2 cexp2(float ar2, float air, float l) {
  const float t = air * l;
  return (f32x2){__builtin_amdgcn_cosf(t), __builtin_amdgcn_sinf(t)} * __builtin_amdgcn_exp2f(ar2 * l);
}
__device__ __forceinline__ float state_a(float ldt, float are, float aim, float& ar2, float& air, float& g) {
  const float dt = rbf(__expf(ldt));
  ar2 = dt * are * 1.4426950408889634f;
  air = dt * aim * 0.15915494309189535f;
  air -= __builtin_rintf(air);
  g = 0.f;
  if (__builtin_expect(__any(ar2 > 0.f), 0)) g = fminf(__uint_as_float(wave_maxu(__float_as_uint(fmaxf(ar2, 0.f)))), 1.f);
  return dt;
}
template <int N> __device__ __forceinline__ void put_im(char* p, int im, const f32x2* z) {
  *(u32x4*)p = (u32x4){packh(z[0][0], z[1][0]), packh(z[2][0], z[3][0]), packh(z[4][0], z[5][0]), packh(z[6][0], z[7][0])};
  *(u32x4*)(p + im) = (u32x4){packh(z[0][1], z[1][1]), packh(z[2][1], z[3][1]), packh(z[4][1], z[5][1]), packh(z[6][1], z[7][1])};
}
#define Y_ASM \
  "v_mov_b64 v[40:41], %0\nv_pk_mul_f32 v[42:43], v[40:41], %1 op_sel_hi:[0,1]\nv_pk_mul_f32 v[44:45], %0, %2 op_sel_hi:[0,1]\n" \
  "v_pk_fma_f32 v[42:43], v[40:41], %1, v[42:43] op_sel:[1,1,0] op_sel_hi:[1,0,1] neg_lo:[0,1,0]\n" \
  "v_pk_fma_f32 v[44:45], %0, %2, v[44:45] op_sel:[1,1,0] op_sel_hi:[1,0,1] neg_lo:[0,1,0]\nv_cvt_pk_f16_f32 v56, v40, v42\n" \
  "v_cvt_pk_f16_f32 v60, v41, v43\nv_pk_mul_f32 v[40:41], v[42:43], %1 op_sel_hi:[0,1]\nv_pk_mul_f32 v[48:49], v[44:45], %2 op_sel_hi:[0,1]\n" \
  "v_pk_fma_f32 v[40:41], v[42:43], %1, v[40:41] op_sel:[1,1,0] op_sel_hi:[1,0,1] neg_lo:[0,1,0]\n" \
  "v_pk_fma_f32 v[48:49], v[44:45], %2, v[48:49] op_sel:[1,1,0] op_sel_hi:[1,0,1] neg_lo:[0,1,0]\n" \
  "v_pk_mul_f32 v[42:43], v[40:41], %1 op_sel_hi:[0,1]\nv_pk_mul_f32 v[52:53], v[48:49], %2 op_sel_hi:[0,1]\n" \
  "v_pk_fma_f32 v[42:43], v[40:41], %1, v[42:43] op_sel:[1,1,0] op_sel_hi:[1,0,1] neg_lo:[0,1,0]\n" \
  "v_pk_fma_f32 v[52:53], v[48:49], %2, v[52:53] op_sel:[1,1,0] op_sel_hi:[1,0,1] neg_lo:[0,1,0]\nv_cvt_pk_f16_f32 v57, v40, v42\n" \
  "v_cvt_pk_f16_f32 v61, v41, v43\nv_pk_mul_f32 v[40:41], v[42:43], %1 op_sel_hi:[0,1]\n" \
  "v_pk_fma_f32 v[40:41], v[42:43], %1, v[40:41] op_sel:[1,1,0] op_sel_hi:[1,0,1] neg_lo:[0,1,0]\n" \
  "v_pk_mul_f32 v[42:43], v[40:41], %1 op_sel_hi:[0,1]\nv_pk_mul_f32 v[46:47], v[44:45], %1 op_sel_hi:[0,1]\n" \
  "v_pk_fma_f32 v[42:43], v[40:41], %1, v[42:43] op_sel:[1,1,0] op_sel_hi:[1,0,1] neg_lo:[0,1,0]\n" \
  "v_pk_fma_f32 v[46:47], v[44:45], %1, v[46:47] op_sel:[1,1,0] op_sel_hi:[1,0,1] neg_lo:[0,1,0]\nv_cvt_pk_f16_f32 v58, v40, v42\n" \
  "v_cvt_pk_f16_f32 v62, v41, v43\nv_cvt_pk_f16_f32 v64, v44, v46\nv_cvt_pk_f16_f32 v68, v45, v47\n" \
  "v_pk_mul_f32 v[40:41], v[42:43], %1 op_sel_hi:[0,1]\nv_pk_mul_f32 v[44:45], v[46:47], %1 op_sel_hi:[0,1]\n" \
  "v_pk_fma_f32 v[40:41], v[42:43], %1, v[40:41] op_sel:[1,1,0] op_sel_hi:[1,0,1] neg_lo:[0,1,0]\n" \
  "v_pk_fma_f32 v[44:45], v[46:47], %1, v[44:45] op_sel:[1,1,0] op_sel_hi:[1,0,1] neg_lo:[0,1,0]\n" \
  "v_pk_mul_f32 v[42:43], v[40:41], %1 op_sel_hi:[0,1]\nv_pk_mul_f32 v[46:47], v[44:45], %1 op_sel_hi:[0,1]\n" \
  "v_pk_fma_f32 v[42:43], v[40:41], %1, v[42:43] op_sel:[1,1,0] op_sel_hi:[1,0,1] neg_lo:[0,1,0]\n" \
  "v_pk_fma_f32 v[46:47], v[44:45], %1, v[46:47] op_sel:[1,1,0] op_sel_hi:[1,0,1] neg_lo:[0,1,0]\nv_cvt_pk_f16_f32 v59, v40, v42\n" \
  "v_cvt_pk_f16_f32 v63, v41, v43\nv_cvt_pk_f16_f32 v65, v44, v46\nv_cvt_pk_f16_f32 v69, v45, v47\n" \
  "v_pk_mul_f32 v[44:45], v[46:47], %1 op_sel_hi:[0,1]\nds_write_b128 %3, v[56:59] offset:0\n" \
  "v_pk_fma_f32 v[44:45], v[46:47], %1, v[44:45] op_sel:[1,1,0] op_sel_hi:[1,0,1] neg_lo:[0,1,0]\nds_write_b128 %3, v[60:63] offset:64\n" \
  "v_pk_mul_f32 v[46:47], v[44:45], %1 op_sel_hi:[0,1]\nv_pk_mul_f32 v[50:51], v[48:49], %1 op_sel_hi:[0,1]\n" \
  "v_pk_fma_f32 v[46:47], v[44:45], %1, v[46:47] op_sel:[1,1,0] op_sel_hi:[1,0,1] neg_lo:[0,1,0]\n" \
  "v_pk_fma_f32 v[50:51], v[48:49], %1, v[50:51] op_sel:[1,1,0] op_sel_hi:[1,0,1] neg_lo:[0,1,0]\nv_cvt_pk_f16_f32 v66, v44, v46\n" \
  "v_cvt_pk_f16_f32 v70, v45, v47\nv_cvt_pk_f16_f32 v56, v48, v50\nv_cvt_pk_f16_f32 v60, v49, v51\n" \
  "v_pk_mul_f32 v[44:45], v[46:47], %1 op_sel_hi:[0,1]\nv_pk_mul_f32 v[48:49], v[50:51], %1 op_sel_hi:[0,1]\n" \
  "v_pk_fma_f32 v[44:45], v[46:47], %1, v[44:45] op_sel:[1,1,0] op_sel_hi:[1,0,1] neg_lo:[0,1,0]\n" \
  "v_pk_fma_f32 v[48:49], v[50:51], %1, v[48:49] op_sel:[1,1,0] op_sel_hi:[1,0,1] neg_lo:[0,1,0]\n" \
  "v_pk_mul_f32 v[46:47], v[44:45], %1 op_sel_hi:[0,1]\nv_pk_mul_f32 v[50:51], v[48:49], %1 op_sel_hi:[0,1]\n" \
  "v_pk_fma_f32 v[46:47], v[44:45], %1, v[46:47] op_sel:[1,1,0] op_sel_hi:[1,0,1] neg_lo:[0,1,0]\n" \
  "v_pk_fma_f32 v[50:51], v[48:49], %1, v[50:51] op_sel:[1,1,0] op_sel_hi:[1,0,1] neg_lo:[0,1,0]\nv_cvt_pk_f16_f32 v67, v44, v46\n" \
  "v_cvt_pk_f16_f32 v71, v45, v47\nv_cvt_pk_f16_f32 v57, v48, v50\nv_cvt_pk_f16_f32 v61, v49, v51\n" \
  "v_pk_mul_f32 v[48:49], v[50:51], %1 op_sel_hi:[0,1]\nds_write_b128 %3, v[64:67] offset:16\n" \
  "v_pk_fma_f32 v[48:49], v[50:51], %1, v[48:49] op_sel:[1,1,0] op_sel_hi:[1,0,1] neg_lo:[0,1,0]\nds_write_b128 %3, v[68:71] offset:80\n" \
  "v_pk_mul_f32 v[50:51], v[48:49], %1 op_sel_hi:[0,1]\nv_pk_mul_f32 v[54:55], v[52:53], %1 op_sel_hi:[0,1]\n" \
  "v_pk_fma_f32 v[50:51], v[48:49], %1, v[50:51] op_sel:[1,1,0] op_sel_hi:[1,0,1] neg_lo:[0,1,0]\n" \
  "v_pk_fma_f32 v[54:55], v[52:53], %1, v[54:55] op_sel:[1,1,0] op_sel_hi:[1,0,1] neg_lo:[0,1,0]\nv_cvt_pk_f16_f32 v58, v48, v50\n" \
  "v_cvt_pk_f16_f32 v62, v49, v51\nv_cvt_pk_f16_f32 v64, v52, v54\nv_cvt_pk_f16_f32 v68, v53, v55\n" \
  "v_pk_mul_f32 v[48:49], v[50:51], %1 op_sel_hi:[0,1]\nv_pk_mul_f32 v[52:53], v[54:55], %1 op_sel_hi:[0,1]\n" \
  "v_pk_fma_f32 v[48:49], v[50:51], %1, v[48:49] op_sel:[1,1,0] op_sel_hi:[1,0,1] neg_lo:[0,1,0]\n" \
  "v_pk_fma_f32 v[52:53], v[54:55], %1, v[52:53] op_sel:[1,1,0] op_sel_hi:[1,0,1] neg_lo:[0,1,0]\n" \
  "v_pk_mul_f32 v[50:51], v[48:49], %1 op_sel_hi:[0,1]\nv_pk_mul_f32 v[54:55], v[52:53], %1 op_sel_hi:[0,1]\n" \
  "v_pk_fma_f32 v[50:51], v[48:49], %1, v[50:51] op_sel:[1,1,0] op_sel_hi:[1,0,1] neg_lo:[0,1,0]\n" \
  "v_pk_fma_f32 v[54:55], v[52:53], %1, v[54:55] op_sel:[1,1,0] op_sel_hi:[1,0,1] neg_lo:[0,1,0]\nv_cvt_pk_f16_f32 v59, v48, v50\n" \
  "v_cvt_pk_f16_f32 v63, v49, v51\nv_cvt_pk_f16_f32 v65, v52, v54\nv_cvt_pk_f16_f32 v69, v53, v55\n" \
  "v_pk_mul_f32 v[52:53], v[54:55], %1 op_sel_hi:[0,1]\nds_write_b128 %3, v[56:59] offset:32\n" \
  "v_pk_fma_f32 v[52:53], v[54:55], %1, v[52:53] op_sel:[1,1,0] op_sel_hi:[1,0,1] neg_lo:[0,1,0]\nds_write_b128 %3, v[60:63] offset:96\n" \
  "v_pk_mul_f32 v[54:55], v[52:53], %1 op_sel_hi:[0,1]\n" \
  "v_pk_fma_f32 v[54:55], v[52:53], %1, v[54:55] op_sel:[1,1,0] op_sel_hi:[1,0,1] neg_lo:[0,1,0]\nv_cvt_pk_f16_f32 v66, v52, v54\n" \
  "v_cvt_pk_f16_f32 v70, v53, v55\nv_pk_mul_f32 v[52:53], v[54:55], %1 op_sel_hi:[0,1]\n" \
  "v_pk_fma_f32 v[52:53], v[54:55], %1, v[52:53] op_sel:[1,1,0] op_sel_hi:[1,0,1] neg_lo:[0,1,0]\n" \
  "v_pk_mul_f32 v[54:55], v[52:53], %1 op_sel_hi:[0,1]\n" \
  "v_pk_fma_f32 v[54:55], v[52:53], %1, v[54:55] op_sel:[1,1,0] op_sel_hi:[1,0,1] neg_lo:[0,1,0]\nv_cvt_pk_f16_f32 v67, v52, v54\n" \
  "v_cvt_pk_f16_f32 v71, v53, v55\nds_write_b128 %3, v[64:67] offset:48\nds_write_b128 %3, v[68:71] offset:112\n"
#define Y_CLOB "memory", "v40", "v41", "v42", "v43", "v44", "v45", "v46", "v47", "v48", "v49", "v50", "v51", "v52", "v53", "v54", "v55", "v56", "v57", "v58", "v59", "v60", "v61", "v62", "v63", "v64", "v65", "v66", "v67", "v68", "v69", "v70", "v71"
#define T_ASM \
  "ds_read_b128 %4, %8\nds_read_b64_tr_b16 v[80:81], %7 offset:0\nds_read_b64_tr_b16 v[82:83], %7 offset:64\n" \
  "ds_read_b64_tr_b16 v[84:85], %5 offset:0\nds_read_b64_tr_b16 v[86:87], %5 offset:48\nds_read_b64_tr_b16 v[88:89], %6 offset:0\n" \
  "ds_read_b64_tr_b16 v[90:91], %6 offset:48\nds_read_b64_tr_b16 v[92:93], %7 offset:3584\nds_read_b64_tr_b16 v[94:95], %7 offset:3648\n" \
  "ds_read_b64_tr_b16 v[96:97], %5 offset:3584\nds_read_b64_tr_b16 v[98:99], %5 offset:3632\nds_read_b64_tr_b16 v[100:101], %6 offset:3584\n" \
  "ds_read_b64_tr_b16 v[102:103], %6 offset:3632\nds_read_b64_tr_b16 v[104:105], %7 offset:7168\nds_read_b64_tr_b16 v[106:107], %7 offset:7232\n" \
  "s_waitcnt lgkmcnt(8)\nv_mfma_f32_16x16x32_f16 %0, v[84:87], v[80:83], 0\nds_read_b64_tr_b16 v[108:109], %5 offset:7168\n" \
  "ds_read_b64_tr_b16 v[110:111], %5 offset:7216\nds_read_b64_tr_b16 v[112:113], %6 offset:7168\nv_mfma_f32_16x16x32_f16 %2, v[88:91], v[80:83], 0\n" \
  "ds_read_b64_tr_b16 v[114:115], %6 offset:7216\nds_read_b64_tr_b16 v[116:117], %7 offset:10752\nds_read_b64_tr_b16 v[118:119], %7 offset:10816\n" \
  "s_waitcnt lgkmcnt(8)\nv_mfma_f32_16x16x32_f16 %0, v[96:99], v[92:95], %0\nds_read_b64_tr_b16 v[120:121], %5 offset:10752\n" \
  "ds_read_b64_tr_b16 v[122:123], %5 offset:10800\nds_read_b64_tr_b16 v[124:125], %6 offset:10752\n" \
  "v_mfma_f32_16x16x32_f16 %2, v[100:103], v[92:95], %2\nds_read_b64_tr_b16 v[126:127], %6 offset:10800\n" \
  "ds_read_b64_tr_b16 v[80:81], %7 offset:14336\nds_read_b64_tr_b16 v[82:83], %7 offset:14400\ns_waitcnt lgkmcnt(8)\n" \
  "v_mfma_f32_16x16x32_f16 %0, v[108:111], v[104:107], %0\nds_read_b64_tr_b16 v[84:85], %5 offset:14336\n" \
  "ds_read_b64_tr_b16 v[86:87], %5 offset:14384\nds_read_b64_tr_b16 v[88:89], %6 offset:14336\n" \
  "v_mfma_f32_16x16x32_f16 %2, v[112:115], v[104:107], %2\nds_read_b64_tr_b16 v[90:91], %6 offset:14384\n" \
  "ds_read_b64_tr_b16 v[92:93], %7 offset:17920\nds_read_b64_tr_b16 v[94:95], %7 offset:17984\ns_waitcnt lgkmcnt(8)\n" \
  "v_mfma_f32_16x16x32_f16 %0, v[120:123], v[116:119], %0\nds_read_b64_tr_b16 v[96:97], %5 offset:17920\n" \
  "ds_read_b64_tr_b16 v[98:99], %5 offset:17968\nds_read_b64_tr_b16 v[100:101], %6 offset:17920\n" \
  "v_mfma_f32_16x16x32_f16 %2, v[124:127], v[116:119], %2\nds_read_b64_tr_b16 v[102:103], %6 offset:17968\n" \
  "ds_read_b64_tr_b16 v[104:105], %7 offset:21504\nds_read_b64_tr_b16 v[106:107], %7 offset:21568\ns_waitcnt lgkmcnt(8)\n" \
  "v_mfma_f32_16x16x32_f16 %1, v[84:87], v[80:83], 0\nds_read_b64_tr_b16 v[108:109], %5 offset:21504\nds_read_b64_tr_b16 v[110:111], %5 offset:21552\n" \
  "ds_read_b64_tr_b16 v[112:113], %6 offset:21504\nv_mfma_f32_16x16x32_f16 %3, v[88:91], v[80:83], 0\nds_read_b64_tr_b16 v[114:115], %6 offset:21552\n" \
  "ds_read_b64_tr_b16 v[116:117], %7 offset:25088\nds_read_b64_tr_b16 v[118:119], %7 offset:25152\ns_waitcnt lgkmcnt(8)\n" \
  "v_mfma_f32_16x16x32_f16 %1, v[96:99], v[92:95], %1\nds_read_b64_tr_b16 v[120:121], %5 offset:25088\n" \
  "ds_read_b64_tr_b16 v[122:123], %5 offset:25136\nds_read_b64_tr_b16 v[124:125], %6 offset:25088\n" \
  "v_mfma_f32_16x16x32_f16 %3, v[100:103], v[92:95], %3\nds_read_b64_tr_b16 v[126:127], %6 offset:25136\ns_waitcnt lgkmcnt(6)\n" \
  "v_mfma_f32_16x16x32_f16 %1, v[108:111], v[104:107], %1\nv_mfma_f32_16x16x32_f16 %3, v[112:115], v[104:107], %3\ns_waitcnt lgkmcnt(0)\n" \
  "v_mfma_f32_16x16x32_f16 %1, v[120:123], v[116:119], %1\nv_mfma_f32_16x16x32_f16 %3, v[124:127], v[116:119], %3\n"
#define T_CLOB "memory", "v80", "v81", "v82", "v83", "v84", "v85", "v86", "v87", "v88", "v89", "v90", "v91", "v92", "v93", "v94", "v95", "v96", "v97", "v98", "v99", "v100", "v101", "v102", "v103", "v104", "v105", "v106", "v107", "v108", "v109", "v110", "v111", "v112", "v113", "v114", "v115", "v116", "v117", "v118", "v119", "v120", "v121", "v122", "v123", "v124", "v125", "v126", "v127"
template <class F> __device__ __forceinline__ int gen_y(float ldt, float are, float aim, float bre, float bim, float cre, float cim, uint32_t im, F&& mid TSP) {
  float ar2, air, g;
  const float dt = state_a(ldt, are, aim, ar2, air, g);
  const float dbr = dt * bre, dbi = dt * bim, pr = cre * dbr - cim * dbi, pi = cre * dbi + cim * dbr;
  const f32x2 w8 = cexp2(ar2, -air, 8.f), w64 = cexp2(ar2, -air, 64.f);
  const float pm = fmaxf(fabsf(pr), fabsf(pi));
  int e = -4;
  if (__builtin_expect(__any(!(pm < 0x1p-4f) || ar2 > 0.f) || !__any(pm > 0x1p-12f), 0)) e = (int)(wave_maxu(__float_as_uint(pm)) >> 23) - 126 + (int)ceilf(g * 250.f);
  const f32x2 y0 = {__builtin_amdgcn_ldexpf(pr, 14 - e), __builtin_amdgcn_ldexpf(-pi, 14 - e)};
  mid();
  asm volatile(Y_ASM : : "v"(y0), "v"(w8), "v"(w64), "v"(im) : Y_CLOB);
  return e;
}
__device__ __forceinline__ int gen_x(float ldt, float are, float aim, char* im) {
  float ar2, air, g;
  state_a(ldt, are, aim, ar2, air, g);
  const int e = min((int)ceilf(g * 1810.f), 300);
  const float s = __builtin_amdgcn_ldexpf(1.f, 15 - e);
  const f32x2 w1 = cexp2(ar2, air, 1.f), w256 = cexp2(ar2, air, 256.f), w2 = cmul(w1, w1);
  f32x2 a[8], b[8], c[8];
  a[0] = (f32x2){s, 0.f}; a[1] = w1 * s; a[2] = w2 * s;
  b[0] = b[1] = w256 * s;
  b[2] = cmul(b[0], w2); a[3] = cmul(a[1], w2); a[4] = cmul(a[2], w2);
  b[3] = b[4] = b[5] = cmul(b[2], w2); a[5] = cmul(a[3], w2); a[6] = cmul(a[4], w2);
  b[6] = cmul(b[3], w2); a[7] = cmul(a[5], w2); c[0] = cmul(b[3], w256);
  b[7] = cmul(b[6], w2); c[2] = cmul(c[0], w256);
  put_im<0>(im + I_X, 48, a);
  c[1] = cmul(b[7], w256); c[3] = cmul(c[1], w256);
  put_im<0>(im + I_X + 16, 48, b);
#pragma unroll
  for (int k = 4; k < 8; ++k) c[k] = cmul(c[k - 1], w256);
  put_im<0>(im + I_X + 32, 48, c);
  return e;
}

template <int I> struct IC { static constexpr int value = I; };
template <int B, int E, class F> __device__ __forceinline__ void static_for(F&& f) {
  if constexpr (B < E) { f(IC<B>{}); static_for<B + 1, E>(f); }
}
typedef __attribute__((ext_vector_type(4))) short s16x4;
__device__ __forceinline__ u32x2 trd(uint32_t a) {
  return __builtin_bit_cast(u32x2, __builtin_amdgcn_ds_read_tr16_b64_v4i16((__attribute__((address_space(3))) s16x4*)(uintptr_t)a));
}
#define BAR() asm volatile("s_waitcnt lgkmcnt(0)\n\ts_barrier" ::: "memory")
#define ST16(off, v) asm volatile("global_store_dwordx4 %0, %1, %2 sc1\n\ts_nop 1" : : "v"(off), "v"(v), "s"(ws) : "memory")
#define ST4(off, v) asm volatile("global_store_dword %0, %1, %2 sc1" : : "v"(off), "v"(v), "s"(ws) : "memory")
#define LD16(r, off) asm volatile("global_load_dwordx4 %0, %1, %2 sc1" : "=v"(r) : "v"(off), "s"(ws) : "memory")
#define LD16W(r, off) asm volatile("global_load_dwordx4 %0, %1, %2 sc1\n\ts_waitcnt vmcnt(0)" : "=&v"(r) : "v"(off), "s"(ws) : "memory")
#define DRAIN() asm volatile("s_waitcnt vmcnt(0)" ::: "memory")
#define CEX(a, b) { const uint32_t hi_ = max(a, b), lo_ = min(a, b); a = hi_; b = lo_; }
#define MMB(a_, b_, c_) __builtin_amdgcn_mfma_f32_16x16x32_bf16(__builtin_bit_cast(bf16x8, a_), __builtin_bit_cast(bf16x8, b_), c_, 0, 0, 0)
#define NTM 0
#define DPOL ""
#define DPOL2 ""
#define DPOL4 ""
constexpr int SPIN_CAP = 1 << 18;
constexpr uint32_t UMASK = 0x40074007u;
__device__ __forceinline__ uint32_t utag(uint32_t T, int j) {
  return ((T >> (3 * j)) & 0x00070007u) | (((j <= 2) ? (T << (2 - j)) : (T >> (j - 2))) & 0x40004000u);
}
__device__ __forceinline__ uint32_t max4(const u32x4& a) { return max(max(a[0], a[1]), max(a[2], a[3])); }
__device__ __forceinline__ void until(uint32_t t) {
#pragma unroll 1
  for (int spin = 0; spin < 600 && (int)((uint32_t)__builtin_amdgcn_s_memrealtime() - t) < 0; ++spin) __builtin_amdgcn_s_sleep(1);
}
__device__ __forceinline__ uint32_t latest(uint32_t t) {
  const uint32_t t0 = (uint32_t)__builtin_amdgcn_readfirstlane(t) - 0x40000000u;
  return wave_maxu(t - t0) + t0;
}
__device__ __forceinline__ uint32_t gate(const char* ws, uint32_t po, uint32_t T) {
  u32x2 pv;
#pragma unroll 1
  for (int spin = 0; spin < SPIN_CAP; ++spin) {
    asm volatile("global_load_dwordx2 %0, %1, %2 sc1\n\ts_waitcnt vmcnt(0)" : "=&v"(pv) : "v"(po), "s"(ws) : "memory");
    if (!__any(pv[0] != T)) break;
    __builtin_amdgcn_s_sleep(2);
  }
  return pv[1];
}
__device__ __forceinline__ uint32_t gate2(const char* ws, uint32_t oa, uint32_t da, uint32_t ob, uint32_t T) {
  u32x2 pa, pb;
#pragma unroll 1
  for (int spin = 0; spin < SPIN_CAP; ++spin) {
    asm volatile("global_load_dwordx2 %0, %2, %4 sc1\n\tglobal_load_dwordx2 %1, %3, %4 sc1\n\ts_waitcnt vmcnt(0)" : "=&v"(pa), "=&v"(pb) : "v"(oa), "v"(ob), "s"(ws) : "memory");
    if (!__any(pa[0] != T || pb[0] != T)) break;
    __builtin_amdgcn_s_sleep(4);
  }
  pa[1] += da;
  return latest(((int)(pa[1] - pb[1]) > 0) ? pa[1] : pb[1]);
}
__device__ __forceinline__ void gsync(uint32_t a, uint32_t target) {
  uint32_t t, c, n = SPIN_CAP;
  uint64_t ex;
  asm volatile("s_waitcnt lgkmcnt(0)\n\ts_mov_b64 %3, exec\n\ts_mov_b64 exec, 1\n\tv_mov_b32 %0, 1\n\tds_add_u32 %4, %0\n\ts_mov_b64 exec, %3\n"
               "1:\n\ts_sleep 1\n\tds_read_b32 %0, %4\n\ts_sub_u32 %2, %2, 1\n\ts_cbranch_scc1 2f\n\ts_waitcnt lgkmcnt(0)\n\tv_readfirstlane_b32 %1, %0\n\t"
               "s_cmp_lt_u32 %1, %5\n\ts_cbranch_scc1 1b\n2:\n\ts_waitcnt lgkmcnt(0)"
               : "=&v"(t), "=&s"(c), "+s"(n), "=&s"(ex) : "v"(a), "n"(target) : "memory", "scc");
}
__device__ __forceinline__ void gsync0(uint32_t a, uint32_t target) {
  uint32_t t, c, n = SPIN_CAP;
  asm volatile("s_waitcnt lgkmcnt(0)\n\ts_mov_b64 exec, 1\n\tv_mov_b32 %0, 1\n\tds_add_u32 %3, %0\n\ts_mov_b64 exec, -1\n"
               "1:\n\tds_read_b32 %0, %3\n\ts_sub_u32 %2, %2, 1\n\ts_cbranch_scc1 2f\n\ts_waitcnt lgkmcnt(0)\n\tv_readfirstlane_b32 %1, %0\n\t"
               "s_cmp_lt_u32 %1, %4\n\ts_cbranch_scc1 1b\n2:\n\ts_waitcnt lgkmcnt(0)"
               : "=&v"(t), "=&s"(c), "+s"(n) : "v"(a), "n"(target) : "memory", "scc");
}
__device__ __forceinline__ void gwait(uint32_t a, uint32_t target) {
  uint32_t t, c, n = SPIN_CAP;
  asm volatile("1:\n\ts_sleep 2\n\tds_read_b32 %0, %3\n\ts_sub_u32 %2, %2, 1\n\ts_cbranch_scc1 2f\n\ts_waitcnt lgkmcnt(0)\n\tv_readfirstlane_b32 %1, %0\n\t"
               "s_cmp_lt_u32 %1, %4\n\ts_cbranch_scc1 1b\n2:\n\ts_waitcnt lgkmcnt(0)"
               : "=&v"(t), "=&s"(c), "+s"(n) : "v"(a), "n"(target) : "memory", "scc");
}
__device__ __forceinline__ void gwait0(uint32_t a, uint32_t target) {
  uint32_t t, c, n = SPIN_CAP;
  asm volatile("1:\n\tds_read_b32 %0, %3\n\ts_sub_u32 %2, %2, 1\n\ts_cbranch_scc1 2f\n\ts_waitcnt lgkmcnt(0)\n\tv_readfirstlane_b32 %1, %0\n\t"
               "s_cmp_lt_u32 %1, %4\n\ts_cbranch_scc1 1b\n2:\n\ts_waitcnt lgkmcnt(0)"
               : "=&v"(t), "=&s"(c), "+s"(n) : "v"(a), "n"(target) : "memory", "scc");
}
__device__ __forceinline__ void dma16(const void* base, uint32_t voff, uint32_t lds) {
  uint32_t keep;
  asm volatile("s_mov_b32 %0, m0\n\ts_mov_b32 m0, %3\n\ts_nop 0\n\tglobal_load_lds_dwordx4 %1, %2" DPOL "\n\ts_mov_b32 m0, %0" : "=&s"(keep) : "v"(voff), "s"(base), "s"(lds) : "memory");
}
__device__ __forceinline__ void dma16d(const void* base, uint32_t voff, uint32_t lds) {
  uint32_t keep;
  asm volatile("s_mov_b32 %0, m0\n\ts_mov_b32 m0, %3\n\ts_nop 0\n\tglobal_load_lds_dwordx4 %1, %2" DPOL2 "\n\ts_mov_b32 m0, %0" : "=&s"(keep) : "v"(voff), "s"(base), "s"(lds) : "memory");
}
__device__ __forceinline__ void nap(int n) {
  for (; n > 15; n -= 15) __builtin_amdgcn_s_sleep(15);
  switch (n) {
    case 1: __builtin_amdgcn_s_sleep(1); break; case 2: __builtin_amdgcn_s_sleep(2); break; case 3: __builtin_amdgcn_s_sleep(3); break;
    case 4: __builtin_amdgcn_s_sleep(4); break; case 5: __builtin_amdgcn_s_sleep(5); break; case 6: __builtin_amdgcn_s_sleep(6); break;
    case 7: __builtin_amdgcn_s_sleep(7); break; case 8: __builtin_amdgcn_s_sleep(8); break; case 9: __builtin_amdgcn_s_sleep(9); break;
    case 10: __builtin_amdgcn_s_sleep(10); break; case 11: __builtin_amdgcn_s_sleep(11); break; case 12: __builtin_amdgcn_s_sleep(12); break;
    case 13: __builtin_amdgcn_s_sleep(13); break; case 14: __builtin_amdgcn_s_sleep(14); break; case 15: __builtin_amdgcn_s_sleep(15); break;
    default: break;
  }
}
#define SLEEP(n) do { if ((n) > 0) __builtin_amdgcn_s_sleep(n); } while (0)
#define USLEEP 3
#define SSLEEP 11
#define PSLEEP 2
#define PPRIO 0
#define LFIRST 1
#define WD 40
#define OSLEEP 12
#define QPRIO 1
#define KPRIO 2
#define GSLEEP 0
#define NSLEEP 0
#define UD 40
#define XD 0
#define RSLEEP 0
#define UPRIO 2
#define WDEPTH 4
#define ESLEEP 0
#define SD 58
#define OD 0
#define KBACK 6
#define DDEPTH 6
#define C3 1
#define XOFF 0
#define XROT 2
#define WSOFF 0
#define T0 0
#define TB1 0
#define T0SLEEP 0
#define KN 2
#define RPF 1
#define TSTOP 70
#define HSTOP 110
#define VEARLY 0ull
#define FPR 3
#define SPR 3
#define DPRIO 0
#define T1 0
#define T1SLEEP 0
#define QPRIO2 3
#define HW 2
#define RD 62
#define OPOS 1
#define OD2 0
#define NA 12
#define RD2 88
#define ORD1 ((unsigned __int128)0x321087654u << 36 | 0x321087654ull)
#define WDEPTH2 4
#define RQ NA
#define HREV 1
#define QPRIO0 3
#define VPS 1
#define RW 1
#define NPRIO 0
#define XSL 10
#define XF 0
#define N0 8
#define FORCESLOW 0
#define FORCEFB 0
#define CONV_ASM \
  "s_cmp_eq_u32 s89, 1\ns_cbranch_scc1 1f\ns_cmp_gt_u32 s89, 1\ns_cbranch_scc1 9f\nds_read_b64 v[124:125], v3 offset:576\n" \
  "ds_read_b64 v[126:127], v3 offset:584\nds_read_b64 v[120:121], v3 offset:64\nds_read_b64 v[122:123], v3 offset:72\n" \
  "ds_read_b64 v[116:117], v3 offset:640\nds_read_b64 v[118:119], v3 offset:648\nds_read_b64 v[112:113], v3 offset:128\n" \
  "ds_read_b64 v[114:115], v3 offset:136\nds_read_b64 v[108:109], v3 offset:704\nds_read_b64 v[110:111], v3 offset:712\n" \
  "ds_read_b64 v[104:105], v3 offset:192\nds_read_b64 v[106:107], v3 offset:200\nds_read_b64 v[100:101], v3 offset:768\n" \
  "ds_read_b64 v[102:103], v3 offset:776\nds_read_b64 v[96:97], v3 offset:256\nds_read_b64 v[98:99], v3 offset:264\n" \
  "ds_read_b64 v[92:93], v3 offset:832\nds_read_b64 v[94:95], v3 offset:840\nds_read_b64 v[88:89], v3 offset:320\n" \
  "ds_read_b64 v[90:91], v3 offset:328\nds_read_b64 v[84:85], v3 offset:896\nds_read_b64 v[86:87], v3 offset:904\n" \
  "ds_read_b64 v[80:81], v3 offset:384\nds_read_b64 v[82:83], v3 offset:392\nds_read_b64 v[76:77], v3 offset:960\n" \
  "ds_read_b64 v[78:79], v3 offset:968\nds_read_b64 v[72:73], v3 offset:448\nds_read_b64 v[74:75], v3 offset:456\n" \
  "ds_read_b64 v[68:69], v3 offset:1024\nds_read_b64 v[70:71], v3 offset:1032\nds_read_b64 v[64:65], v3 offset:512\n" \
  "ds_read_b64 v[66:67], v3 offset:520\nds_read_b64 v[60:61], v3 offset:0\nds_read_b64 v[62:63], v3 offset:8\nds_read_b64 v[56:57], v3 offset:1088\n" \
  "ds_read_b64 v[58:59], v3 offset:1096\ns_waitcnt lgkmcnt(0)\ns_barrier\nds_read_b128 v[36:39], v2 offset:0\nds_read_b128 v[40:43], v2 offset:64\n" \
  "ds_read_b128 v[44:47], v2 offset:128\nds_read_b128 v[48:51], v2 offset:192\ns_waitcnt lgkmcnt(3)\n" \
  "v_mfma_f32_16x16x32_f16 v[12:15], v[124:127], v[36:39], 0\ns_setprio 1\nds_read_b128 v[52:55], v2 offset:256\n" \
  "v_mfma_f32_16x16x32_f16 v[16:19], v[120:123], v[36:39], 0\ns_waitcnt lgkmcnt(3)\nv_mfma_f32_16x16x32_f16 v[12:15], v[116:119], v[40:43], v[12:15]\n" \
  "ds_read_b128 v[36:39], v2 offset:320\nv_mfma_f32_16x16x32_f16 v[16:19], v[112:115], v[40:43], v[16:19]\ns_waitcnt lgkmcnt(3)\n" \
  "v_mfma_f32_16x16x32_f16 v[12:15], v[108:111], v[44:47], v[12:15]\nds_read_b128 v[40:43], v2 offset:384\n" \
  "v_mfma_f32_16x16x32_f16 v[16:19], v[104:107], v[44:47], v[16:19]\ns_waitcnt lgkmcnt(3)\n" \
  "v_mfma_f32_16x16x32_f16 v[12:15], v[100:103], v[48:51], v[12:15]\nds_read_b128 v[44:47], v2 offset:448\n" \
  "v_mfma_f32_16x16x32_f16 v[16:19], v[96:99], v[48:51], v[16:19]\ns_waitcnt lgkmcnt(3)\n" \
  "v_mfma_f32_16x16x32_f16 v[12:15], v[92:95], v[52:55], v[12:15]\nv_mfma_f32_16x16x32_f16 v[16:19], v[88:91], v[52:55], v[16:19]\n" \
  "s_waitcnt lgkmcnt(2)\nv_mfma_f32_16x16x32_f16 v[12:15], v[84:87], v[36:39], v[12:15]\nds_read_b128 v[48:51], v2 offset:512\n" \
  "v_mfma_f32_16x16x32_f16 v[16:19], v[80:83], v[36:39], v[16:19]\ns_waitcnt lgkmcnt(2)\n" \
  "v_mfma_f32_16x16x32_f16 v[12:15], v[76:79], v[40:43], v[12:15]\nds_read_b128 v[52:55], v2 offset:576\n" \
  "v_mfma_f32_16x16x32_f16 v[16:19], v[72:75], v[40:43], v[16:19]\ns_waitcnt lgkmcnt(2)\n" \
  "v_mfma_f32_16x16x32_f16 v[12:15], v[68:71], v[44:47], v[12:15]\nds_read_b128 v[36:39], v2 offset:640\n" \
  "v_mfma_f32_16x16x32_f16 v[16:19], v[64:67], v[44:47], v[16:19]\nv_mfma_f32_16x16x32_f16 v[20:23], v[60:63], v[44:47], 0\n" \
  "ds_read_b128 v[40:43], v2 offset:704\nv_mfma_f32_16x16x32_f16 v[28:31], v[56:59], v[44:47], 0\ns_waitcnt lgkmcnt(3)\n" \
  "v_mfma_f32_16x16x32_f16 v[16:19], v[124:127], v[48:51], v[16:19]\nds_read_b128 v[56:59], v2 offset:768\nv_cvt_pkrtz_f16_f32 v32, v12, v13\n" \
  "v_mfma_f32_16x16x32_f16 v[20:23], v[120:123], v[48:51], v[20:23]\nv_cvt_pkrtz_f16_f32 v33, v14, v15\ns_waitcnt lgkmcnt(3)\n" \
  "v_mfma_f32_16x16x32_f16 v[16:19], v[116:119], v[52:55], v[16:19]\nds_read_b128 v[44:47], v2 offset:832\n" \
  "v_pk_add_u16 v32, v32, 4 op_sel_hi:[1,0] clamp\nv_mfma_f32_16x16x32_f16 v[20:23], v[112:115], v[52:55], v[20:23]\n" \
  "v_pk_add_u16 v33, v33, 4 op_sel_hi:[1,0] clamp\ns_waitcnt lgkmcnt(3)\nv_mfma_f32_16x16x32_f16 v[16:19], v[108:111], v[36:39], v[16:19]\n" \
  "ds_read_b128 v[48:51], v2 offset:896\nds_write_b64 v4, v[32:33] offset:0\nv_mfma_f32_16x16x32_f16 v[20:23], v[104:107], v[36:39], v[20:23]\n" \
  "s_waitcnt lgkmcnt(3)\nv_mfma_f32_16x16x32_f16 v[16:19], v[100:103], v[40:43], v[16:19]\nds_read_b128 v[52:55], v2 offset:960\n" \
  "v_mfma_f32_16x16x32_f16 v[20:23], v[96:99], v[40:43], v[20:23]\nv_mfma_f32_16x16x32_f16 v[16:19], v[92:95], v[56:59], v[16:19]\n" \
  "v_mfma_f32_16x16x32_f16 v[20:23], v[88:91], v[56:59], v[20:23]\nds_read_b128 v[36:39], v2 offset:1024\ns_waitcnt lgkmcnt(3)\n" \
  "v_mfma_f32_16x16x32_f16 v[16:19], v[84:87], v[44:47], v[16:19]\nv_mfma_f32_16x16x32_f16 v[20:23], v[80:83], v[44:47], v[20:23]\n" \
  "ds_read_b128 v[40:43], v2 offset:1088\nds_read_b64 v[56:57], v6 offset:256\nds_read_b64 v[58:59], v6 offset:264\n" \
  "v_mfma_f32_16x16x32_f16 v[16:19], v[76:79], v[48:51], v[16:19]\nv_mfma_f32_16x16x32_f16 v[20:23], v[72:75], v[48:51], v[20:23]\n" \
  "ds_read_b128 v[44:47], v2 offset:1152\ns_waitcnt lgkmcnt(4)\nv_mfma_f32_16x16x32_f16 v[16:19], v[68:71], v[52:55], v[16:19]\n" \
  "v_mfma_f32_16x16x32_f16 v[20:23], v[64:67], v[52:55], v[20:23]\nds_read_b128 v[48:51], v2 offset:1216\n" \
  "v_mfma_f32_16x16x32_f16 v[24:27], v[60:63], v[52:55], 0\nv_mfma_f32_16x16x32_f16 v[20:23], v[124:127], v[36:39], v[20:23]\n" \
  "ds_read_b128 v[52:55], v2 offset:1280\nv_mfma_f32_16x16x32_f16 v[24:27], v[120:123], v[36:39], v[24:27]\ns_waitcnt lgkmcnt(5)\n" \
  "v_mfma_f32_16x16x32_f16 v[20:23], v[116:119], v[40:43], v[20:23]\nds_read_b128 v[36:39], v2 offset:1344\nv_cvt_pkrtz_f16_f32 v32, v16, v17\n" \
  "v_mfma_f32_16x16x32_f16 v[24:27], v[112:115], v[40:43], v[24:27]\nv_cvt_pkrtz_f16_f32 v33, v18, v19\ns_waitcnt lgkmcnt(3)\n" \
  "v_mfma_f32_16x16x32_f16 v[20:23], v[108:111], v[44:47], v[20:23]\nds_read_b128 v[40:43], v2 offset:1408\n" \
  "v_pk_add_u16 v32, v32, 4 op_sel_hi:[1,0] clamp\nds_read_b32 v35, v9 offset:32\nv_mfma_f32_16x16x32_f16 v[24:27], v[104:107], v[44:47], v[24:27]\n" \
  "v_pk_add_u16 v33, v33, 4 op_sel_hi:[1,0] clamp\ns_waitcnt lgkmcnt(3)\nv_mfma_f32_16x16x32_f16 v[20:23], v[100:103], v[48:51], v[20:23]\n" \
  "ds_read_b128 v[44:47], v2 offset:1472\nds_write_b64 v4, v[32:33] offset:1024\nv_mfma_f32_16x16x32_f16 v[24:27], v[96:99], v[48:51], v[24:27]\n" \
  "v_mfma_f32_16x16x32_f16 v[20:23], v[92:95], v[52:55], v[20:23]\nv_mfma_f32_16x16x32_f16 v[24:27], v[88:91], v[52:55], v[24:27]\n" \
  "ds_read_b128 v[48:51], v2 offset:1536\ns_waitcnt lgkmcnt(3)\nv_readfirstlane_b32 s85, v35\ns_cmp_lt_u32 s85, 4\ns_cbranch_scc1 21f\n22:\n" \
  "v_mfma_f32_16x16x32_f16 v[20:23], v[84:87], v[36:39], v[20:23]\nv_mfma_f32_16x16x32_f16 v[24:27], v[80:83], v[36:39], v[24:27]\n" \
  "ds_read_b128 v[52:55], v2 offset:1600\nv_mfma_f32_16x16x32_f16 v[20:23], v[76:79], v[40:43], v[20:23]\n" \
  "v_mfma_f32_16x16x32_f16 v[24:27], v[72:75], v[40:43], v[24:27]\nds_read_b128 v[36:39], v2 offset:1664\ns_waitcnt lgkmcnt(4)\n" \
  "v_mfma_f32_16x16x32_f16 v[20:23], v[68:71], v[44:47], v[20:23]\nv_mfma_f32_16x16x32_f16 v[24:27], v[64:67], v[44:47], v[24:27]\n" \
  "ds_read_b128 v[40:43], v2 offset:1728\nv_mfma_f32_16x16x32_f16 v[28:31], v[60:63], v[44:47], v[28:31]\nds_read_b64_tr_b16 v[60:61], v5 offset:0\n" \
  "ds_read_b64_tr_b16 v[62:63], v5 offset:32\ns_waitcnt lgkmcnt(4)\nv_mfma_f32_16x16x32_f16 v[24:27], v[124:127], v[48:51], v[24:27]\n" \
  "v_mfma_f32_16x16x32_f16 v[28:31], v[120:123], v[48:51], v[28:31]\nds_read_b64_tr_b16 v[44:45], v5 offset:256\n" \
  "ds_read_b64_tr_b16 v[46:47], v5 offset:288\nv_mfma_f32_16x16x32_f16 v[24:27], v[116:119], v[52:55], v[24:27]\n" \
  "ds_read_b64 v[120:121], v6 offset:320\nds_read_b64 v[122:123], v6 offset:328\nv_mfma_f32_16x16x32_f16 v[28:31], v[112:115], v[52:55], v[28:31]\n" \
  "ds_read_b128 v[48:51], v2 offset:1792\ns_waitcnt lgkmcnt(7)\nv_mfma_f32_16x16x32_f16 v[24:27], v[108:111], v[36:39], v[24:27]\n" \
  "ds_read_b128 v[112:115], v2 offset:1856\nv_mfma_f32_16x16x32_f16 v[28:31], v[104:107], v[36:39], v[28:31]\nds_read_b64 v[52:53], v6 offset:128\n" \
  "ds_read_b64 v[54:55], v6 offset:136\nv_mfma_f32_16x16x32_f16 v[24:27], v[100:103], v[40:43], v[24:27]\nds_read_b128 v[104:107], v2 offset:1920\n" \
  "v_mfma_f32_16x16x32_f16 v[28:31], v[96:99], v[40:43], v[28:31]\nds_read_b64 v[36:37], v6 offset:192\nds_read_b64 v[38:39], v6 offset:200\n" \
  "s_waitcnt lgkmcnt(9)\nv_mfma_f32_16x16x32_f16 v[20:23], v[60:63], v[56:59], v[20:23]\nds_read_b128 v[96:99], v2 offset:1984\ns_waitcnt lgkmcnt(7)\n" \
  "v_mfma_f32_16x16x32_f16 v[20:23], v[44:47], v[120:123], v[20:23]\nv_mfma_f32_16x16x32_f16 v[24:27], v[92:95], v[48:51], v[24:27]\n" \
  "ds_read_b128 v[40:43], v2 offset:2048\nv_mfma_f32_16x16x32_f16 v[28:31], v[88:91], v[48:51], v[28:31]\nds_read_b64_tr_b16 v[88:89], v5 offset:512\n" \
  "ds_read_b64_tr_b16 v[90:91], v5 offset:544\ns_waitcnt lgkmcnt(6)\nv_mfma_f32_16x16x32_f16 v[24:27], v[84:87], v[112:115], v[24:27]\n" \
  "v_mfma_f32_16x16x32_f16 v[28:31], v[80:83], v[112:115], v[28:31]\nv_cvt_pkrtz_f16_f32 v32, v20, v21\nv_cvt_pkrtz_f16_f32 v33, v22, v23\n" \
  "v_mfma_f32_16x16x32_f16 v[24:27], v[76:79], v[104:107], v[24:27]\nds_read_b64_tr_b16 v[48:49], v5 offset:768\n" \
  "ds_read_b64_tr_b16 v[50:51], v5 offset:800\nv_mfma_f32_16x16x32_f16 v[28:31], v[72:75], v[104:107], v[28:31]\n" \
  "ds_read_b128 v[80:83], v2 offset:2112\ns_waitcnt lgkmcnt(5)\nv_mfma_f32_16x16x32_f16 v[24:27], v[68:71], v[96:99], v[24:27]\n" \
  "ds_read_b128 v[112:115], v2 offset:2176\nv_pk_add_u16 v32, v32, 4 op_sel_hi:[1,0] clamp\n" \
  "v_mfma_f32_16x16x32_f16 v[28:31], v[64:67], v[96:99], v[28:31]\nds_read_b128 v[72:75], v2 offset:2240\n" \
  "v_pk_add_u16 v33, v33, 4 op_sel_hi:[1,0] clamp\nv_mfma_f32_16x16x32_f16 v[28:31], v[124:127], v[40:43], v[28:31]\n" \
  "ds_read_b128 v[104:107], v2 offset:2304\nds_write_b64 v4, v[32:33] offset:2048\nv_mfma_f32_16x16x32_f16 v[24:27], v[60:63], v[52:55], v[24:27]\n" \
  "ds_read_b128 v[64:67], v2 offset:2368\nds_read_b64 v[96:97], v6 offset:0\nds_read_b64 v[98:99], v6 offset:8\n" \
  "v_mfma_f32_16x16x32_f16 v[24:27], v[44:47], v[36:39], v[24:27]\nds_read_b128 v[124:127], v2 offset:2432\ns_waitcnt lgkmcnt(9)\n" \
  "v_mfma_f32_16x16x32_f16 v[24:27], v[88:91], v[56:59], v[24:27]\nds_read_b128 v[40:43], v2 offset:2496\n" \
  "v_mfma_f32_16x16x32_f16 v[24:27], v[48:51], v[120:123], v[24:27]\ns_waitcnt lgkmcnt(8)\n" \
  "v_mfma_f32_16x16x32_f16 v[28:31], v[116:119], v[80:83], v[28:31]\nds_read_b64 v[116:117], v6 offset:64\nds_read_b64 v[118:119], v6 offset:72\n" \
  "v_mfma_f32_16x16x32_f16 v[28:31], v[108:111], v[112:115], v[28:31]\nds_read_b64_tr_b16 v[80:81], v5 offset:1024\n" \
  "ds_read_b64_tr_b16 v[82:83], v5 offset:1056\ns_waitcnt lgkmcnt(10)\nv_mfma_f32_16x16x32_f16 v[28:31], v[100:103], v[72:75], v[28:31]\n" \
  "v_cvt_pkrtz_f16_f32 v32, v24, v25\nv_cvt_pkrtz_f16_f32 v33, v26, v27\nv_mfma_f32_16x16x32_f16 v[28:31], v[92:95], v[104:107], v[28:31]\n" \
  "v_pk_add_u16 v32, v32, 4 op_sel_hi:[1,0] clamp\ns_waitcnt lgkmcnt(5)\nv_mfma_f32_16x16x32_f16 v[28:31], v[84:87], v[64:67], v[28:31]\n" \
  "ds_read_b64_tr_b16 v[108:109], v5 offset:1280\nds_read_b64_tr_b16 v[110:111], v5 offset:1312\n" \
  "v_mfma_f32_16x16x32_f16 v[28:31], v[76:79], v[124:127], v[28:31]\nds_read_b64_tr_b16 v[112:113], v7 offset:0\n" \
  "ds_read_b64_tr_b16 v[114:115], v7 offset:64\ns_waitcnt lgkmcnt(6)\nv_mfma_f32_16x16x32_f16 v[28:31], v[68:71], v[40:43], v[28:31]\n" \
  "ds_read2_b32 v[100:101], v8 offset0:48 offset1:49\nds_read2_b32 v[102:103], v8 offset0:50 offset1:51\n" \
  "v_mfma_f32_16x16x32_f16 v[28:31], v[60:63], v[96:99], v[28:31]\nv_pk_add_u16 v33, v33, 4 op_sel_hi:[1,0] clamp\n" \
  "ds_write_b64 v4, v[32:33] offset:3072\nv_mfma_f32_16x16x32_f16 v[28:31], v[44:47], v[116:119], v[28:31]\n" \
  "v_mfma_f32_16x16x32_f16 v[28:31], v[88:91], v[52:55], v[28:31]\nv_mfma_f32_16x16x32_f16 v[28:31], v[48:51], v[36:39], v[28:31]\n" \
  "s_waitcnt lgkmcnt(5)\nv_mfma_f32_16x16x32_f16 v[28:31], v[80:83], v[56:59], v[28:31]\n" \
  "v_mfma_f32_16x16x32_f16 v[28:31], v[108:111], v[120:123], v[28:31]\ns_waitcnt lgkmcnt(1)\n" \
  "v_mfma_f32_16x16x32_f16 v[28:31], v[112:115], v[100:103], v[28:31]\ns_nop 7\nv_cvt_pkrtz_f16_f32 v32, v28, v29\nv_cvt_pkrtz_f16_f32 v33, v30, v31\n" \
  "v_pk_add_u16 v32, v32, 4 op_sel_hi:[1,0] clamp\nv_pk_add_u16 v33, v33, 4 op_sel_hi:[1,0] clamp\nds_write_b64 v4, v[32:33] offset:4096\n" \
  "s_setprio 0\ns_setpc_b64 s[90:91]\n21:\ns_movk_i32 s84, 0x4000\n23:\nds_read_b32 v35, v9 offset:32\ns_sub_u32 s84, s84, 1\ns_cbranch_scc1 22b\n" \
  "s_waitcnt lgkmcnt(0)\nv_readfirstlane_b32 s85, v35\ns_cmp_lt_u32 s85, 4\ns_cbranch_scc1 23b\ns_branch 22b\n1:\n" \
  "ds_read_b64 v[124:125], v3 offset:1088\nds_read_b64 v[126:127], v3 offset:1096\nds_read_b64 v[120:121], v3 offset:0\n" \
  "ds_read_b64 v[122:123], v3 offset:8\nds_read_b64 v[116:117], v3 offset:64\nds_read_b64 v[118:119], v3 offset:72\n" \
  "ds_read_b64 v[112:113], v3 offset:128\nds_read_b64 v[114:115], v3 offset:136\nds_read_b64 v[108:109], v3 offset:192\n" \
  "ds_read_b64 v[110:111], v3 offset:200\nds_read_b64 v[104:105], v3 offset:256\nds_read_b64 v[106:107], v3 offset:264\n" \
  "ds_read_b64 v[100:101], v3 offset:320\nds_read_b64 v[102:103], v3 offset:328\nds_read_b64 v[96:97], v3 offset:384\n" \
  "ds_read_b64 v[98:99], v3 offset:392\nds_read_b64 v[92:93], v3 offset:448\nds_read_b64 v[94:95], v3 offset:456\n" \
  "ds_read_b64 v[88:89], v3 offset:512\nds_read_b64 v[90:91], v3 offset:520\nds_read_b64 v[84:85], v3 offset:576\n" \
  "ds_read_b64 v[86:87], v3 offset:584\nds_read_b64 v[80:81], v3 offset:640\nds_read_b64 v[82:83], v3 offset:648\n" \
  "ds_read_b64 v[76:77], v3 offset:704\nds_read_b64 v[78:79], v3 offset:712\nds_read_b64 v[72:73], v3 offset:768\n" \
  "ds_read_b64 v[74:75], v3 offset:776\nds_read_b64 v[68:69], v3 offset:832\nds_read_b64 v[70:71], v3 offset:840\n" \
  "ds_read_b64 v[64:65], v3 offset:896\nds_read_b64 v[66:67], v3 offset:904\nds_read_b64 v[60:61], v3 offset:960\n" \
  "ds_read_b64 v[62:63], v3 offset:968\nds_read_b64 v[56:57], v3 offset:1024\nds_read_b64 v[58:59], v3 offset:1032\n" \
  "ds_read_b64 v[52:53], v6 offset:0\nds_read_b64 v[54:55], v6 offset:8\nds_read_b64 v[48:49], v6 offset:64\nds_read_b64 v[50:51], v6 offset:72\n" \
  "s_waitcnt lgkmcnt(0)\ns_barrier\nds_read_b128 v[28:31], v2 offset:960\nds_read_b128 v[32:35], v2 offset:1472\n" \
  "ds_read_b128 v[36:39], v2 offset:1984\nds_read_b128 v[40:43], v2 offset:2048\nds_read_b128 v[44:47], v2 offset:2112\ns_waitcnt lgkmcnt(3)\n" \
  "v_mfma_f32_16x16x32_f16 v[12:15], v[124:127], v[28:31], 0\ns_setprio 1\nds_read_b128 v[28:31], v2 offset:2176\n" \
  "v_mfma_f32_16x16x32_f16 v[16:19], v[124:127], v[32:35], 0\nds_read_b128 v[32:35], v2 offset:2240\ns_waitcnt lgkmcnt(3)\n" \
  "v_mfma_f32_16x16x32_f16 v[12:15], v[120:123], v[36:39], v[12:15]\nv_mfma_f32_16x16x32_f16 v[20:23], v[124:127], v[36:39], 0\n" \
  "ds_read_b128 v[124:127], v2 offset:2304\nds_read_b128 v[36:39], v2 offset:2368\nv_mfma_f32_16x16x32_f16 v[12:15], v[116:119], v[40:43], v[12:15]\n" \
  "ds_read_b128 v[40:43], v2 offset:2432\ns_waitcnt lgkmcnt(4)\nv_mfma_f32_16x16x32_f16 v[12:15], v[112:115], v[44:47], v[12:15]\n" \
  "ds_read_b128 v[44:47], v2 offset:2496\nv_mfma_f32_16x16x32_f16 v[12:15], v[108:111], v[28:31], v[12:15]\nds_read_b128 v[28:31], v2 offset:2560\n" \
  "s_waitcnt lgkmcnt(4)\nv_mfma_f32_16x16x32_f16 v[12:15], v[104:107], v[32:35], v[12:15]\nds_read_b128 v[32:35], v2 offset:2624\n" \
  "v_mfma_f32_16x16x32_f16 v[12:15], v[100:103], v[124:127], v[12:15]\ns_waitcnt lgkmcnt(3)\n" \
  "v_mfma_f32_16x16x32_f16 v[12:15], v[96:99], v[36:39], v[12:15]\nds_read_b128 v[124:127], v2 offset:2688\n" \
  "v_mfma_f32_16x16x32_f16 v[12:15], v[92:95], v[40:43], v[12:15]\ns_waitcnt lgkmcnt(3)\n" \
  "v_mfma_f32_16x16x32_f16 v[12:15], v[88:91], v[44:47], v[12:15]\nds_read_b128 v[36:39], v2 offset:2752\n" \
  "v_mfma_f32_16x16x32_f16 v[16:19], v[120:123], v[44:47], v[16:19]\ns_waitcnt lgkmcnt(3)\n" \
  "v_mfma_f32_16x16x32_f16 v[12:15], v[84:87], v[28:31], v[12:15]\nds_read_b128 v[40:43], v2 offset:2816\n" \
  "v_mfma_f32_16x16x32_f16 v[16:19], v[116:119], v[28:31], v[16:19]\ns_waitcnt lgkmcnt(3)\n" \
  "v_mfma_f32_16x16x32_f16 v[12:15], v[80:83], v[32:35], v[12:15]\nds_read_b128 v[44:47], v2 offset:2880\n" \
  "v_mfma_f32_16x16x32_f16 v[16:19], v[112:115], v[32:35], v[16:19]\ns_waitcnt lgkmcnt(3)\n" \
  "v_mfma_f32_16x16x32_f16 v[12:15], v[76:79], v[124:127], v[12:15]\nds_read_b128 v[28:31], v2 offset:2944\n" \
  "v_mfma_f32_16x16x32_f16 v[16:19], v[108:111], v[124:127], v[16:19]\ns_waitcnt lgkmcnt(3)\n" \
  "v_mfma_f32_16x16x32_f16 v[12:15], v[72:75], v[36:39], v[12:15]\nds_read_b128 v[32:35], v2 offset:3008\n" \
  "v_mfma_f32_16x16x32_f16 v[16:19], v[104:107], v[36:39], v[16:19]\ns_waitcnt lgkmcnt(3)\n" \
  "v_mfma_f32_16x16x32_f16 v[12:15], v[68:71], v[40:43], v[12:15]\nv_mfma_f32_16x16x32_f16 v[16:19], v[100:103], v[40:43], v[16:19]\n" \
  "ds_read_b128 v[124:127], v2 offset:3072\ns_waitcnt lgkmcnt(3)\nv_mfma_f32_16x16x32_f16 v[12:15], v[64:67], v[44:47], v[12:15]\n" \
  "v_mfma_f32_16x16x32_f16 v[16:19], v[96:99], v[44:47], v[16:19]\nds_read_b128 v[36:39], v2 offset:3136\ns_waitcnt lgkmcnt(3)\n" \
  "v_mfma_f32_16x16x32_f16 v[12:15], v[60:63], v[28:31], v[12:15]\nds_read_b64 v[40:41], v6 offset:128\nds_read_b64 v[42:43], v6 offset:136\n" \
  "v_mfma_f32_16x16x32_f16 v[16:19], v[92:95], v[28:31], v[16:19]\nds_read_b128 v[44:47], v2 offset:3200\ns_waitcnt lgkmcnt(4)\n" \
  "v_mfma_f32_16x16x32_f16 v[12:15], v[56:59], v[32:35], v[12:15]\nv_mfma_f32_16x16x32_f16 v[16:19], v[88:91], v[32:35], v[16:19]\n" \
  "ds_read_b128 v[28:31], v2 offset:3264\nv_mfma_f32_16x16x32_f16 v[20:23], v[120:123], v[32:35], v[20:23]\nds_read_b64 v[120:121], v6 offset:192\n" \
  "ds_read_b64 v[122:123], v6 offset:200\nv_mfma_f32_16x16x32_f16 v[16:19], v[84:87], v[124:127], v[16:19]\nds_read_b128 v[32:35], v2 offset:3328\n" \
  "v_mfma_f32_16x16x32_f16 v[20:23], v[116:119], v[124:127], v[20:23]\nds_read_b64 v[116:117], v6 offset:256\nds_read_b64 v[118:119], v6 offset:264\n" \
  "s_waitcnt lgkmcnt(6)\nv_mfma_f32_16x16x32_f16 v[16:19], v[80:83], v[36:39], v[16:19]\nds_read_b128 v[124:127], v2 offset:3392\n" \
  "v_mfma_f32_16x16x32_f16 v[20:23], v[112:115], v[36:39], v[20:23]\nds_read_b64 v[112:113], v6 offset:320\nds_read_b64 v[114:115], v6 offset:328\n" \
  "v_mfma_f32_16x16x32_f16 v[16:19], v[76:79], v[44:47], v[16:19]\nds_read_b128 v[36:39], v2 offset:3456\n" \
  "v_mfma_f32_16x16x32_f16 v[20:23], v[108:111], v[44:47], v[20:23]\ns_waitcnt lgkmcnt(6)\n" \
  "v_mfma_f32_16x16x32_f16 v[16:19], v[72:75], v[28:31], v[16:19]\nds_read_b128 v[108:111], v2 offset:3520\n" \
  "v_mfma_f32_16x16x32_f16 v[20:23], v[104:107], v[28:31], v[20:23]\nv_mfma_f32_16x16x32_f16 v[16:19], v[68:71], v[32:35], v[16:19]\n" \
  "ds_read_b128 v[44:47], v2 offset:3584\nv_mfma_f32_16x16x32_f16 v[20:23], v[100:103], v[32:35], v[20:23]\nds_read_b128 v[104:107], v2 offset:3648\n" \
  "ds_read_b32 v27, v9 offset:32\ns_waitcnt lgkmcnt(4)\nv_mfma_f32_16x16x32_f16 v[16:19], v[64:67], v[124:127], v[16:19]\n" \
  "ds_read_b128 v[28:31], v2 offset:3712\nv_mfma_f32_16x16x32_f16 v[20:23], v[96:99], v[124:127], v[20:23]\nds_read_b128 v[100:103], v2 offset:3776\n" \
  "ds_read2_b32 v[32:33], v8 offset0:32 offset1:33\nds_read2_b32 v[34:35], v8 offset0:34 offset1:35\n" \
  "v_mfma_f32_16x16x32_f16 v[16:19], v[60:63], v[36:39], v[16:19]\nds_read_b128 v[96:99], v2 offset:3840\n" \
  "v_mfma_f32_16x16x32_f16 v[20:23], v[92:95], v[36:39], v[20:23]\nds_read_b128 v[124:127], v2 offset:3904\ns_waitcnt lgkmcnt(8)\n" \
  "v_mfma_f32_16x16x32_f16 v[16:19], v[56:59], v[108:111], v[16:19]\nds_read_b128 v[92:95], v2 offset:3968\ns_waitcnt lgkmcnt(7)\n" \
  "v_readfirstlane_b32 s85, v27\ns_cmp_lt_u32 s85, 4\ns_cbranch_scc1 31f\n32:\nv_mfma_f32_16x16x32_f16 v[20:23], v[88:91], v[108:111], v[20:23]\n" \
  "ds_read_b128 v[36:39], v2 offset:4032\nds_read_b64_tr_b16 v[88:89], v5 offset:512\nds_read_b64_tr_b16 v[90:91], v5 offset:544\n" \
  "v_mfma_f32_16x16x32_f16 v[20:23], v[84:87], v[44:47], v[20:23]\nds_read_b64_tr_b16 v[108:109], v5 offset:768\n" \
  "ds_read_b64_tr_b16 v[110:111], v5 offset:800\nv_mfma_f32_16x16x32_f16 v[20:23], v[80:83], v[104:107], v[20:23]\n" \
  "ds_read_b64_tr_b16 v[84:85], v5 offset:1024\nds_read_b64_tr_b16 v[86:87], v5 offset:1056\ns_waitcnt lgkmcnt(12)\n" \
  "v_mfma_f32_16x16x32_f16 v[20:23], v[76:79], v[28:31], v[20:23]\nds_read_b64_tr_b16 v[44:45], v5 offset:1280\n" \
  "ds_read_b64_tr_b16 v[46:47], v5 offset:1312\nv_mfma_f32_16x16x32_f16 v[20:23], v[72:75], v[100:103], v[20:23]\ns_waitcnt lgkmcnt(10)\n" \
  "v_mfma_f32_16x16x32_f16 v[20:23], v[68:71], v[96:99], v[20:23]\nds_read_b64_tr_b16 v[80:81], v5 offset:1536\n" \
  "ds_read_b64_tr_b16 v[82:83], v5 offset:1568\nv_mfma_f32_16x16x32_f16 v[20:23], v[64:67], v[124:127], v[20:23]\n" \
  "ds_read_b64_tr_b16 v[104:105], v5 offset:1792\nds_read_b64_tr_b16 v[106:107], v5 offset:1824\ns_waitcnt lgkmcnt(12)\n" \
  "v_mfma_f32_16x16x32_f16 v[20:23], v[60:63], v[92:95], v[20:23]\nds_read_b64_tr_b16 v[76:77], v5 offset:2048\n" \
  "ds_read_b64_tr_b16 v[78:79], v5 offset:2080\nv_mfma_f32_16x16x32_f16 v[20:23], v[56:59], v[36:39], v[20:23]\ns_waitcnt lgkmcnt(10)\n" \
  "v_mfma_f32_16x16x32_f16 v[12:15], v[88:91], v[52:55], v[12:15]\nds_read_b64_tr_b16 v[28:29], v5 offset:2304\n" \
  "ds_read_b64_tr_b16 v[30:31], v5 offset:2336\nv_mfma_f32_16x16x32_f16 v[12:15], v[108:111], v[48:51], v[12:15]\n" \
  "ds_read_b64_tr_b16 v[72:73], v5 offset:2560\nds_read_b64_tr_b16 v[74:75], v5 offset:2592\ns_waitcnt lgkmcnt(10)\n" \
  "v_mfma_f32_16x16x32_f16 v[12:15], v[84:87], v[40:43], v[12:15]\nds_read_b64_tr_b16 v[100:101], v5 offset:2816\n" \
  "ds_read_b64_tr_b16 v[102:103], v5 offset:2848\nv_mfma_f32_16x16x32_f16 v[16:19], v[84:87], v[52:55], v[16:19]\n" \
  "ds_read_b64_tr_b16 v[68:69], v7 offset:0\nds_read_b64_tr_b16 v[70:71], v7 offset:64\n" \
  "v_mfma_f32_16x16x32_f16 v[12:15], v[44:47], v[120:123], v[12:15]\nv_mfma_f32_16x16x32_f16 v[16:19], v[44:47], v[48:51], v[16:19]\n" \
  "s_waitcnt lgkmcnt(10)\nv_mfma_f32_16x16x32_f16 v[12:15], v[80:83], v[116:119], v[12:15]\nds_read2_b32 v[96:97], v8 offset0:16 offset1:17\n" \
  "ds_read2_b32 v[98:99], v8 offset0:18 offset1:19\nv_mfma_f32_16x16x32_f16 v[16:19], v[80:83], v[40:43], v[16:19]\n" \
  "ds_read2_b32 v[64:65], v8 offset0:0 offset1:1\nds_read2_b32 v[66:67], v8 offset0:2 offset1:3\n" \
  "v_mfma_f32_16x16x32_f16 v[20:23], v[80:83], v[52:55], v[20:23]\nv_mfma_f32_16x16x32_f16 v[12:15], v[104:107], v[112:115], v[12:15]\n" \
  "v_mfma_f32_16x16x32_f16 v[16:19], v[104:107], v[120:123], v[16:19]\nv_mfma_f32_16x16x32_f16 v[20:23], v[104:107], v[48:51], v[20:23]\n" \
  "s_waitcnt lgkmcnt(10)\nv_mfma_f32_16x16x32_f16 v[16:19], v[76:79], v[116:119], v[16:19]\nds_read_b64_tr_b16 v[124:125], v7 offset:512\n" \
  "ds_read_b64_tr_b16 v[126:127], v7 offset:576\nv_mfma_f32_16x16x32_f16 v[20:23], v[76:79], v[40:43], v[20:23]\n" \
  "ds_read2_b32 v[60:61], v8 offset0:48 offset1:49\nds_read2_b32 v[62:63], v8 offset0:50 offset1:51\n" \
  "v_mfma_f32_16x16x32_f16 v[16:19], v[28:31], v[112:115], v[16:19]\nv_mfma_f32_16x16x32_f16 v[20:23], v[28:31], v[120:123], v[20:23]\n" \
  "s_waitcnt lgkmcnt(10)\nv_mfma_f32_16x16x32_f16 v[20:23], v[72:75], v[116:119], v[20:23]\nds_read_b64_tr_b16 v[92:93], v7 offset:1024\n" \
  "ds_read_b64_tr_b16 v[94:95], v7 offset:1088\nv_mfma_f32_16x16x32_f16 v[20:23], v[100:103], v[112:115], v[20:23]\n" \
  "ds_read_b64_tr_b16 v[56:57], v7 offset:1536\nds_read_b64_tr_b16 v[58:59], v7 offset:1600\ns_waitcnt lgkmcnt(10)\n" \
  "v_mfma_f32_16x16x32_f16 v[12:15], v[68:71], v[32:35], v[12:15]\nv_mfma_f32_16x16x32_f16 v[16:19], v[68:71], v[96:99], v[16:19]\n" \
  "s_waitcnt lgkmcnt(6)\nv_mfma_f32_16x16x32_f16 v[20:23], v[68:71], v[64:67], v[20:23]\ns_waitcnt lgkmcnt(2)\n" \
  "v_mfma_f32_16x16x32_f16 v[12:15], v[124:127], v[60:63], v[12:15]\nv_mfma_f32_16x16x32_f16 v[16:19], v[124:127], v[32:35], v[16:19]\n" \
  "v_mfma_f32_16x16x32_f16 v[20:23], v[124:127], v[96:99], v[20:23]\nv_mfma_f32_16x16x32_f16 v[16:19], v[92:95], v[60:63], v[16:19]\n" \
  "v_mfma_f32_16x16x32_f16 v[20:23], v[92:95], v[32:35], v[20:23]\ns_waitcnt lgkmcnt(0)\n" \
  "v_mfma_f32_16x16x32_f16 v[20:23], v[56:59], v[60:63], v[20:23]\ns_nop 1\nv_cvt_pkrtz_f16_f32 v24, v12, v13\nv_cvt_pkrtz_f16_f32 v25, v14, v15\n" \
  "v_pk_add_u16 v24, v24, 4 op_sel_hi:[1,0] clamp\nv_pk_add_u16 v25, v25, 4 op_sel_hi:[1,0] clamp\nds_write_b64 v4, v[24:25] offset:5120\n" \
  "v_cvt_pkrtz_f16_f32 v24, v16, v17\nv_cvt_pkrtz_f16_f32 v25, v18, v19\nv_pk_add_u16 v24, v24, 4 op_sel_hi:[1,0] clamp\n" \
  "v_pk_add_u16 v25, v25, 4 op_sel_hi:[1,0] clamp\nds_write_b64 v4, v[24:25] offset:6144\nv_cvt_pkrtz_f16_f32 v24, v20, v21\n" \
  "v_cvt_pkrtz_f16_f32 v25, v22, v23\nv_pk_add_u16 v24, v24, 4 op_sel_hi:[1,0] clamp\nv_pk_add_u16 v25, v25, 4 op_sel_hi:[1,0] clamp\n" \
  "ds_write_b64 v4, v[24:25] offset:7168\ns_setprio 0\ns_setpc_b64 s[90:91]\n31:\ns_movk_i32 s84, 0x4000\n33:\nds_read_b32 v27, v9 offset:32\n" \
  "s_sub_u32 s84, s84, 1\ns_cbranch_scc1 32b\ns_waitcnt lgkmcnt(0)\nv_readfirstlane_b32 s85, v27\ns_cmp_lt_u32 s85, 4\ns_cbranch_scc1 33b\n" \
  "s_branch 32b\n9:\ns_cmp_eq_u32 s89, 3\ns_cbranch_scc1 3f\ns_cmp_eq_u32 s89, 4\ns_cbranch_scc1 4f\ns_cmp_eq_u32 s89, 5\ns_cbranch_scc1 5f\n" \
  "ds_read_b64 v[124:125], v3 offset:576\nds_read_b64 v[126:127], v3 offset:584\nds_read_b64 v[120:121], v3 offset:64\n" \
  "ds_read_b64 v[122:123], v3 offset:72\nds_read_b64 v[116:117], v3 offset:640\nds_read_b64 v[118:119], v3 offset:648\n" \
  "ds_read_b64 v[112:113], v3 offset:128\nds_read_b64 v[114:115], v3 offset:136\nds_read_b64 v[108:109], v3 offset:704\n" \
  "ds_read_b64 v[110:111], v3 offset:712\nds_read_b64 v[104:105], v3 offset:192\nds_read_b64 v[106:107], v3 offset:200\n" \
  "ds_read_b64 v[100:101], v3 offset:768\nds_read_b64 v[102:103], v3 offset:776\nds_read_b64 v[96:97], v3 offset:256\n" \
  "ds_read_b64 v[98:99], v3 offset:264\nds_read_b64 v[92:93], v3 offset:832\nds_read_b64 v[94:95], v3 offset:840\n" \
  "ds_read_b64 v[88:89], v3 offset:320\nds_read_b64 v[90:91], v3 offset:328\nds_read_b64 v[84:85], v3 offset:896\n" \
  "ds_read_b64 v[86:87], v3 offset:904\nds_read_b64 v[80:81], v3 offset:384\nds_read_b64 v[82:83], v3 offset:392\n" \
  "ds_read_b64 v[76:77], v3 offset:960\nds_read_b64 v[78:79], v3 offset:968\nds_read_b64 v[72:73], v3 offset:448\n" \
  "ds_read_b64 v[74:75], v3 offset:456\nds_read_b64 v[68:69], v3 offset:1024\nds_read_b64 v[70:71], v3 offset:1032\n" \
  "ds_read_b64 v[64:65], v3 offset:512\nds_read_b64 v[66:67], v3 offset:520\ns_waitcnt lgkmcnt(0)\ns_barrier\nds_read_b128 v[24:27], v2 offset:0\n" \
  "ds_read_b128 v[28:31], v2 offset:64\nds_read_b128 v[32:35], v2 offset:128\nds_read_b128 v[36:39], v2 offset:192\ns_waitcnt lgkmcnt(3)\n" \
  "v_mfma_f32_16x16x32_f16 v[12:15], v[124:127], v[24:27], 0\ns_setprio 1\nds_read_b128 v[40:43], v2 offset:256\n" \
  "v_mfma_f32_16x16x32_f16 v[16:19], v[120:123], v[24:27], 0\ns_waitcnt lgkmcnt(3)\nv_mfma_f32_16x16x32_f16 v[12:15], v[116:119], v[28:31], v[12:15]\n" \
  "ds_read_b128 v[44:47], v2 offset:320\nv_mfma_f32_16x16x32_f16 v[16:19], v[112:115], v[28:31], v[16:19]\ns_waitcnt lgkmcnt(3)\n" \
  "v_mfma_f32_16x16x32_f16 v[12:15], v[108:111], v[32:35], v[12:15]\nds_read_b128 v[48:51], v2 offset:384\n" \
  "v_mfma_f32_16x16x32_f16 v[16:19], v[104:107], v[32:35], v[16:19]\ns_waitcnt lgkmcnt(3)\n" \
  "v_mfma_f32_16x16x32_f16 v[12:15], v[100:103], v[36:39], v[12:15]\nds_read_b128 v[52:55], v2 offset:448\n" \
  "v_mfma_f32_16x16x32_f16 v[16:19], v[96:99], v[36:39], v[16:19]\ns_waitcnt lgkmcnt(3)\n" \
  "v_mfma_f32_16x16x32_f16 v[12:15], v[92:95], v[40:43], v[12:15]\nds_read_b128 v[56:59], v2 offset:512\n" \
  "v_mfma_f32_16x16x32_f16 v[16:19], v[88:91], v[40:43], v[16:19]\nds_read_b128 v[60:63], v2 offset:576\ns_waitcnt lgkmcnt(3)\n" \
  "v_mfma_f32_16x16x32_f16 v[12:15], v[84:87], v[44:47], v[12:15]\nds_read_b128 v[120:123], v2 offset:640\n" \
  "v_mfma_f32_16x16x32_f16 v[16:19], v[80:83], v[44:47], v[16:19]\nds_read_b128 v[24:27], v2 offset:704\n" \
  "v_mfma_f32_16x16x32_f16 v[12:15], v[76:79], v[48:51], v[12:15]\nds_read_b128 v[112:115], v2 offset:768\n" \
  "v_mfma_f32_16x16x32_f16 v[16:19], v[72:75], v[48:51], v[16:19]\nds_read_b128 v[28:31], v2 offset:832\ns_waitcnt lgkmcnt(5)\n" \
  "v_mfma_f32_16x16x32_f16 v[12:15], v[68:71], v[52:55], v[12:15]\nds_read_b128 v[104:107], v2 offset:896\n" \
  "v_mfma_f32_16x16x32_f16 v[16:19], v[64:67], v[52:55], v[16:19]\nds_read_b128 v[32:35], v2 offset:960\n" \
  "v_mfma_f32_16x16x32_f16 v[16:19], v[124:127], v[56:59], v[16:19]\ns_waitcnt lgkmcnt(5)\n" \
  "v_mfma_f32_16x16x32_f16 v[16:19], v[116:119], v[60:63], v[16:19]\nv_mfma_f32_16x16x32_f16 v[16:19], v[108:111], v[120:123], v[16:19]\n" \
  "s_waitcnt lgkmcnt(3)\nv_mfma_f32_16x16x32_f16 v[16:19], v[100:103], v[24:27], v[16:19]\nv_cvt_pkrtz_f16_f32 v20, v12, v13\n" \
  "v_cvt_pkrtz_f16_f32 v21, v14, v15\nv_mfma_f32_16x16x32_f16 v[16:19], v[92:95], v[112:115], v[16:19]\n" \
  "v_pk_add_u16 v20, v20, 4 op_sel_hi:[1,0] clamp\ns_waitcnt lgkmcnt(2)\nv_mfma_f32_16x16x32_f16 v[16:19], v[84:87], v[28:31], v[16:19]\n" \
  "v_pk_add_u16 v21, v21, 4 op_sel_hi:[1,0] clamp\ns_waitcnt lgkmcnt(1)\nv_mfma_f32_16x16x32_f16 v[16:19], v[76:79], v[104:107], v[16:19]\n" \
  "ds_write_b64 v4, v[20:21] offset:0\ns_waitcnt lgkmcnt(1)\nv_mfma_f32_16x16x32_f16 v[16:19], v[68:71], v[32:35], v[16:19]\ns_nop 7\n" \
  "v_cvt_pkrtz_f16_f32 v20, v16, v17\nv_cvt_pkrtz_f16_f32 v21, v18, v19\nv_pk_add_u16 v20, v20, 4 op_sel_hi:[1,0] clamp\n" \
  "v_pk_add_u16 v21, v21, 4 op_sel_hi:[1,0] clamp\nds_write_b64 v4, v[20:21] offset:1024\ns_setprio 0\ns_setpc_b64 s[90:91]\n3:\n" \
  "ds_read_b64 v[124:125], v3 offset:0\nds_read_b64 v[126:127], v3 offset:8\nds_read_b64 v[120:121], v3 offset:64\n" \
  "ds_read_b64 v[122:123], v3 offset:72\nds_read_b64 v[116:117], v3 offset:128\nds_read_b64 v[118:119], v3 offset:136\n" \
  "ds_read_b64 v[112:113], v3 offset:192\nds_read_b64 v[114:115], v3 offset:200\nds_read_b64 v[108:109], v3 offset:256\n" \
  "ds_read_b64 v[110:111], v3 offset:264\nds_read_b64 v[104:105], v3 offset:320\nds_read_b64 v[106:107], v3 offset:328\n" \
  "ds_read_b64 v[100:101], v3 offset:384\nds_read_b64 v[102:103], v3 offset:392\nds_read_b64 v[96:97], v3 offset:448\n" \
  "ds_read_b64 v[98:99], v3 offset:456\nds_read_b64 v[92:93], v3 offset:512\nds_read_b64 v[94:95], v3 offset:520\n" \
  "ds_read_b64 v[88:89], v3 offset:576\nds_read_b64 v[90:91], v3 offset:584\nds_read_b64 v[84:85], v3 offset:640\n" \
  "ds_read_b64 v[86:87], v3 offset:648\nds_read_b64 v[80:81], v3 offset:704\nds_read_b64 v[82:83], v3 offset:712\n" \
  "ds_read_b64 v[76:77], v3 offset:768\nds_read_b64 v[78:79], v3 offset:776\nds_read_b64 v[72:73], v3 offset:832\n" \
  "ds_read_b64 v[74:75], v3 offset:840\nds_read_b64 v[68:69], v3 offset:896\nds_read_b64 v[70:71], v3 offset:904\n" \
  "ds_read_b64 v[64:65], v3 offset:960\nds_read_b64 v[66:67], v3 offset:968\nds_read_b64 v[60:61], v3 offset:1024\n" \
  "ds_read_b64 v[62:63], v3 offset:1032\nds_read_b64 v[56:57], v6 offset:256\nds_read_b64 v[58:59], v6 offset:264\n" \
  "ds_read_b64 v[52:53], v6 offset:128\nds_read_b64 v[54:55], v6 offset:136\nds_read_b64 v[48:49], v6 offset:320\n" \
  "ds_read_b64 v[50:51], v6 offset:328\nds_read_b64 v[44:45], v6 offset:192\nds_read_b64 v[46:47], v6 offset:200\ns_waitcnt lgkmcnt(0)\ns_barrier\n" \
  "ds_read_b128 v[24:27], v2 offset:448\nds_read_b128 v[28:31], v2 offset:512\nds_read_b128 v[32:35], v2 offset:576\n" \
  "ds_read_b128 v[36:39], v2 offset:640\nds_read_b128 v[40:43], v2 offset:704\ns_waitcnt lgkmcnt(3)\n" \
  "v_mfma_f32_16x16x32_f16 v[12:15], v[124:127], v[24:27], 0\ns_setprio 1\nds_read_b128 v[24:27], v2 offset:768\n" \
  "v_mfma_f32_16x16x32_f16 v[12:15], v[120:123], v[28:31], v[12:15]\nds_read_b128 v[28:31], v2 offset:832\ns_waitcnt lgkmcnt(3)\n" \
  "v_mfma_f32_16x16x32_f16 v[12:15], v[116:119], v[32:35], v[12:15]\nds_read_b128 v[32:35], v2 offset:896\n" \
  "v_mfma_f32_16x16x32_f16 v[12:15], v[112:115], v[36:39], v[12:15]\nds_read_b128 v[36:39], v2 offset:960\ns_waitcnt lgkmcnt(3)\n" \
  "v_mfma_f32_16x16x32_f16 v[12:15], v[108:111], v[40:43], v[12:15]\nds_read_b128 v[40:43], v2 offset:1024\n" \
  "v_mfma_f32_16x16x32_f16 v[12:15], v[104:107], v[24:27], v[12:15]\nds_read_b128 v[24:27], v2 offset:1088\ns_waitcnt lgkmcnt(3)\n" \
  "v_mfma_f32_16x16x32_f16 v[12:15], v[100:103], v[28:31], v[12:15]\nds_read_b128 v[28:31], v2 offset:1152\n" \
  "v_mfma_f32_16x16x32_f16 v[12:15], v[96:99], v[32:35], v[12:15]\ns_waitcnt lgkmcnt(3)\n" \
  "v_mfma_f32_16x16x32_f16 v[12:15], v[92:95], v[36:39], v[12:15]\nds_read_b128 v[32:35], v2 offset:1216\n" \
  "v_mfma_f32_16x16x32_f16 v[16:19], v[124:127], v[36:39], 0\ns_waitcnt lgkmcnt(3)\nv_mfma_f32_16x16x32_f16 v[12:15], v[88:91], v[40:43], v[12:15]\n" \
  "ds_read_b128 v[124:127], v2 offset:1280\nv_mfma_f32_16x16x32_f16 v[16:19], v[120:123], v[40:43], v[16:19]\ns_waitcnt lgkmcnt(3)\n" \
  "v_mfma_f32_16x16x32_f16 v[12:15], v[84:87], v[24:27], v[12:15]\nds_read_b128 v[36:39], v2 offset:1344\n" \
  "v_mfma_f32_16x16x32_f16 v[16:19], v[116:119], v[24:27], v[16:19]\ns_waitcnt lgkmcnt(3)\n" \
  "v_mfma_f32_16x16x32_f16 v[12:15], v[80:83], v[28:31], v[12:15]\nds_read_b128 v[120:123], v2 offset:1408\n" \
  "v_mfma_f32_16x16x32_f16 v[16:19], v[112:115], v[28:31], v[16:19]\ns_waitcnt lgkmcnt(3)\n" \
  "v_mfma_f32_16x16x32_f16 v[12:15], v[76:79], v[32:35], v[12:15]\nds_read_b128 v[40:43], v2 offset:1472\n" \
  "v_mfma_f32_16x16x32_f16 v[16:19], v[108:111], v[32:35], v[16:19]\ns_waitcnt lgkmcnt(3)\n" \
  "v_mfma_f32_16x16x32_f16 v[12:15], v[72:75], v[124:127], v[12:15]\nds_read_b128 v[116:119], v2 offset:1536\n" \
  "v_mfma_f32_16x16x32_f16 v[16:19], v[104:107], v[124:127], v[16:19]\nds_read_b128 v[24:27], v2 offset:1600\ns_waitcnt lgkmcnt(3)\n" \
  "v_mfma_f32_16x16x32_f16 v[12:15], v[68:71], v[36:39], v[12:15]\nds_read_b128 v[112:115], v2 offset:1664\nds_read_b32 v23, v9 offset:32\n" \
  "v_mfma_f32_16x16x32_f16 v[16:19], v[100:103], v[36:39], v[16:19]\nds_read_b128 v[28:31], v2 offset:1728\n" \
  "v_mfma_f32_16x16x32_f16 v[12:15], v[64:67], v[120:123], v[12:15]\nds_read_b128 v[108:111], v2 offset:1792\n" \
  "v_mfma_f32_16x16x32_f16 v[16:19], v[96:99], v[120:123], v[16:19]\nds_read_b128 v[32:35], v2 offset:1856\ns_waitcnt lgkmcnt(6)\n" \
  "v_mfma_f32_16x16x32_f16 v[12:15], v[60:63], v[40:43], v[12:15]\nds_read_b128 v[104:107], v2 offset:1920\n" \
  "v_mfma_f32_16x16x32_f16 v[16:19], v[92:95], v[40:43], v[16:19]\nds_read_b128 v[124:127], v2 offset:1984\ns_waitcnt lgkmcnt(5)\n" \
  "v_readfirstlane_b32 s85, v23\ns_cmp_lt_u32 s85, 3\ns_cbranch_scc1 51f\n52:\nv_mfma_f32_16x16x32_f16 v[16:19], v[88:91], v[116:119], v[16:19]\n" \
  "ds_read_b64_tr_b16 v[100:101], v5 offset:0\nds_read_b64_tr_b16 v[102:103], v5 offset:32\n" \
  "v_mfma_f32_16x16x32_f16 v[16:19], v[84:87], v[24:27], v[16:19]\nds_read_b64_tr_b16 v[36:37], v5 offset:256\n" \
  "ds_read_b64_tr_b16 v[38:39], v5 offset:288\nv_mfma_f32_16x16x32_f16 v[16:19], v[80:83], v[112:115], v[16:19]\n" \
  "ds_read_b64_tr_b16 v[96:97], v5 offset:512\nds_read_b64_tr_b16 v[98:99], v5 offset:544\ns_waitcnt lgkmcnt(9)\n" \
  "v_mfma_f32_16x16x32_f16 v[16:19], v[76:79], v[28:31], v[16:19]\nds_read_b64_tr_b16 v[120:121], v5 offset:768\n" \
  "ds_read_b64_tr_b16 v[122:123], v5 offset:800\nv_mfma_f32_16x16x32_f16 v[16:19], v[72:75], v[108:111], v[16:19]\ns_waitcnt lgkmcnt(9)\n" \
  "v_mfma_f32_16x16x32_f16 v[16:19], v[68:71], v[32:35], v[16:19]\nv_mfma_f32_16x16x32_f16 v[16:19], v[64:67], v[104:107], v[16:19]\n" \
  "s_waitcnt lgkmcnt(6)\nv_mfma_f32_16x16x32_f16 v[16:19], v[60:63], v[124:127], v[16:19]\n" \
  "v_mfma_f32_16x16x32_f16 v[12:15], v[100:103], v[56:59], v[12:15]\nv_mfma_f32_16x16x32_f16 v[16:19], v[100:103], v[52:55], v[16:19]\n" \
  "s_waitcnt lgkmcnt(2)\nv_mfma_f32_16x16x32_f16 v[12:15], v[36:39], v[48:51], v[12:15]\n" \
  "v_mfma_f32_16x16x32_f16 v[16:19], v[36:39], v[44:47], v[16:19]\nv_mfma_f32_16x16x32_f16 v[16:19], v[96:99], v[56:59], v[16:19]\n" \
  "s_waitcnt lgkmcnt(0)\nv_mfma_f32_16x16x32_f16 v[16:19], v[120:123], v[48:51], v[16:19]\ns_nop 3\nv_cvt_pkrtz_f16_f32 v20, v12, v13\n" \
  "v_cvt_pkrtz_f16_f32 v21, v14, v15\nv_pk_add_u16 v20, v20, 4 op_sel_hi:[1,0] clamp\nv_pk_add_u16 v21, v21, 4 op_sel_hi:[1,0] clamp\n" \
  "ds_write_b64 v4, v[20:21] offset:2048\nv_cvt_pkrtz_f16_f32 v20, v16, v17\nv_cvt_pkrtz_f16_f32 v21, v18, v19\n" \
  "v_pk_add_u16 v20, v20, 4 op_sel_hi:[1,0] clamp\nv_pk_add_u16 v21, v21, 4 op_sel_hi:[1,0] clamp\nds_write_b64 v4, v[20:21] offset:3072\n" \
  "s_setprio 0\ns_setpc_b64 s[90:91]\n51:\ns_movk_i32 s84, 0x4000\n53:\nds_read_b32 v23, v9 offset:32\ns_sub_u32 s84, s84, 1\ns_cbranch_scc1 52b\n" \
  "s_waitcnt lgkmcnt(0)\nv_readfirstlane_b32 s85, v23\ns_cmp_lt_u32 s85, 3\ns_cbranch_scc1 53b\ns_branch 52b\n4:\n" \
  "ds_read_b64 v[124:125], v3 offset:1088\nds_read_b64 v[126:127], v3 offset:1096\nds_read_b64 v[120:121], v3 offset:0\n" \
  "ds_read_b64 v[122:123], v3 offset:8\nds_read_b64 v[116:117], v3 offset:64\nds_read_b64 v[118:119], v3 offset:72\n" \
  "ds_read_b64 v[112:113], v3 offset:128\nds_read_b64 v[114:115], v3 offset:136\nds_read_b64 v[108:109], v3 offset:192\n" \
  "ds_read_b64 v[110:111], v3 offset:200\nds_read_b64 v[104:105], v3 offset:256\nds_read_b64 v[106:107], v3 offset:264\n" \
  "ds_read_b64 v[100:101], v3 offset:320\nds_read_b64 v[102:103], v3 offset:328\nds_read_b64 v[96:97], v3 offset:384\n" \
  "ds_read_b64 v[98:99], v3 offset:392\nds_read_b64 v[92:93], v3 offset:448\nds_read_b64 v[94:95], v3 offset:456\n" \
  "ds_read_b64 v[88:89], v3 offset:512\nds_read_b64 v[90:91], v3 offset:520\nds_read_b64 v[84:85], v3 offset:576\n" \
  "ds_read_b64 v[86:87], v3 offset:584\nds_read_b64 v[80:81], v3 offset:640\nds_read_b64 v[82:83], v3 offset:648\n" \
  "ds_read_b64 v[76:77], v3 offset:704\nds_read_b64 v[78:79], v3 offset:712\nds_read_b64 v[72:73], v3 offset:768\n" \
  "ds_read_b64 v[74:75], v3 offset:776\nds_read_b64 v[68:69], v3 offset:832\nds_read_b64 v[70:71], v3 offset:840\n" \
  "ds_read_b64 v[64:65], v3 offset:896\nds_read_b64 v[66:67], v3 offset:904\nds_read_b64 v[60:61], v3 offset:960\n" \
  "ds_read_b64 v[62:63], v3 offset:968\nds_read_b64 v[56:57], v3 offset:1024\nds_read_b64 v[58:59], v3 offset:1032\n" \
  "ds_read_b64 v[52:53], v6 offset:0\nds_read_b64 v[54:55], v6 offset:8\nds_read_b64 v[48:49], v6 offset:64\nds_read_b64 v[50:51], v6 offset:72\n" \
  "ds_read_b64 v[44:45], v6 offset:128\nds_read_b64 v[46:47], v6 offset:136\nds_read_b64 v[40:41], v6 offset:192\n" \
  "ds_read_b64 v[42:43], v6 offset:200\ns_waitcnt lgkmcnt(0)\ns_barrier\nds_read_b128 v[24:27], v2 offset:448\nds_read_b128 v[28:31], v2 offset:960\n" \
  "ds_read_b128 v[32:35], v2 offset:1472\nds_read_b128 v[36:39], v2 offset:1536\ns_waitcnt lgkmcnt(3)\n" \
  "v_mfma_f32_16x16x32_f16 v[12:15], v[124:127], v[24:27], 0\ns_setprio 1\nds_read_b128 v[24:27], v2 offset:1600\ns_waitcnt lgkmcnt(3)\n" \
  "v_mfma_f32_16x16x32_f16 v[16:19], v[124:127], v[28:31], 0\nds_read_b128 v[124:127], v2 offset:1664\ns_waitcnt lgkmcnt(3)\n" \
  "v_mfma_f32_16x16x32_f16 v[12:15], v[120:123], v[32:35], v[12:15]\nds_read_b128 v[28:31], v2 offset:1728\ns_waitcnt lgkmcnt(3)\n" \
  "v_mfma_f32_16x16x32_f16 v[12:15], v[116:119], v[36:39], v[12:15]\nds_read_b128 v[32:35], v2 offset:1792\ns_waitcnt lgkmcnt(3)\n" \
  "v_mfma_f32_16x16x32_f16 v[12:15], v[112:115], v[24:27], v[12:15]\nds_read_b128 v[36:39], v2 offset:1856\ns_waitcnt lgkmcnt(3)\n" \
  "v_mfma_f32_16x16x32_f16 v[12:15], v[108:111], v[124:127], v[12:15]\nds_read_b128 v[24:27], v2 offset:1920\ns_waitcnt lgkmcnt(3)\n" \
  "v_mfma_f32_16x16x32_f16 v[12:15], v[104:107], v[28:31], v[12:15]\nds_read_b128 v[124:127], v2 offset:1984\ns_waitcnt lgkmcnt(3)\n" \
  "v_mfma_f32_16x16x32_f16 v[12:15], v[100:103], v[32:35], v[12:15]\nds_read_b128 v[28:31], v2 offset:2048\ns_waitcnt lgkmcnt(3)\n" \
  "v_mfma_f32_16x16x32_f16 v[12:15], v[96:99], v[36:39], v[12:15]\nds_read_b128 v[32:35], v2 offset:2112\ns_waitcnt lgkmcnt(3)\n" \
  "v_mfma_f32_16x16x32_f16 v[12:15], v[92:95], v[24:27], v[12:15]\nds_read_b128 v[36:39], v2 offset:2176\ns_waitcnt lgkmcnt(3)\n" \
  "v_mfma_f32_16x16x32_f16 v[12:15], v[88:91], v[124:127], v[12:15]\nds_read_b128 v[24:27], v2 offset:2240\n" \
  "v_mfma_f32_16x16x32_f16 v[16:19], v[120:123], v[124:127], v[16:19]\nds_read_b64 v[120:121], v6 offset:256\nds_read_b64 v[122:123], v6 offset:264\n" \
  "s_waitcnt lgkmcnt(4)\nv_mfma_f32_16x16x32_f16 v[12:15], v[84:87], v[28:31], v[12:15]\nds_read_b128 v[124:127], v2 offset:2304\n" \
  "v_mfma_f32_16x16x32_f16 v[16:19], v[116:119], v[28:31], v[16:19]\nds_read_b64 v[116:117], v6 offset:320\nds_read_b64 v[118:119], v6 offset:328\n" \
  "v_mfma_f32_16x16x32_f16 v[12:15], v[80:83], v[32:35], v[12:15]\nds_read_b128 v[28:31], v2 offset:2368\n" \
  "v_mfma_f32_16x16x32_f16 v[16:19], v[112:115], v[32:35], v[16:19]\ns_waitcnt lgkmcnt(6)\n" \
  "v_mfma_f32_16x16x32_f16 v[12:15], v[76:79], v[36:39], v[12:15]\nds_read_b128 v[112:115], v2 offset:2432\n" \
  "v_mfma_f32_16x16x32_f16 v[16:19], v[108:111], v[36:39], v[16:19]\nds_read2_b32 v[32:33], v8 offset0:48 offset1:49\n" \
  "ds_read2_b32 v[34:35], v8 offset0:50 offset1:51\nv_mfma_f32_16x16x32_f16 v[12:15], v[72:75], v[24:27], v[12:15]\n" \
  "ds_read_b128 v[108:111], v2 offset:2496\nv_mfma_f32_16x16x32_f16 v[16:19], v[104:107], v[24:27], v[16:19]\n" \
  "ds_read2_b32 v[36:37], v8 offset0:32 offset1:33\nds_read2_b32 v[38:39], v8 offset0:34 offset1:35\ns_waitcnt lgkmcnt(6)\n" \
  "v_mfma_f32_16x16x32_f16 v[12:15], v[68:71], v[124:127], v[12:15]\nds_read_b128 v[104:107], v2 offset:2560\n" \
  "v_mfma_f32_16x16x32_f16 v[16:19], v[100:103], v[124:127], v[16:19]\nds_read_b128 v[24:27], v2 offset:2624\n" \
  "v_mfma_f32_16x16x32_f16 v[12:15], v[64:67], v[28:31], v[12:15]\nds_read_b128 v[100:103], v2 offset:2688\nds_read_b32 v23, v9 offset:32\n" \
  "v_mfma_f32_16x16x32_f16 v[16:19], v[96:99], v[28:31], v[16:19]\nds_read_b128 v[124:127], v2 offset:2752\ns_waitcnt lgkmcnt(7)\n" \
  "v_mfma_f32_16x16x32_f16 v[12:15], v[60:63], v[112:115], v[12:15]\nds_read_b128 v[96:99], v2 offset:2816\n" \
  "v_mfma_f32_16x16x32_f16 v[16:19], v[92:95], v[112:115], v[16:19]\nds_read_b128 v[28:31], v2 offset:2880\n" \
  "v_mfma_f32_16x16x32_f16 v[12:15], v[56:59], v[108:111], v[12:15]\nds_read_b128 v[92:95], v2 offset:2944\n" \
  "v_mfma_f32_16x16x32_f16 v[16:19], v[88:91], v[108:111], v[16:19]\nds_read_b128 v[112:115], v2 offset:3008\ns_waitcnt lgkmcnt(5)\n" \
  "v_readfirstlane_b32 s85, v23\ns_cmp_lt_u32 s85, 3\ns_cbranch_scc1 61f\n62:\nv_mfma_f32_16x16x32_f16 v[16:19], v[84:87], v[104:107], v[16:19]\n" \
  "ds_read_b64_tr_b16 v[88:89], v5 offset:0\nds_read_b64_tr_b16 v[90:91], v5 offset:32\n" \
  "v_mfma_f32_16x16x32_f16 v[16:19], v[80:83], v[24:27], v[16:19]\nds_read_b64_tr_b16 v[108:109], v5 offset:256\n" \
  "ds_read_b64_tr_b16 v[110:111], v5 offset:288\nv_mfma_f32_16x16x32_f16 v[16:19], v[76:79], v[100:103], v[16:19]\n" \
  "ds_read_b64_tr_b16 v[84:85], v5 offset:512\nds_read_b64_tr_b16 v[86:87], v5 offset:544\ns_waitcnt lgkmcnt(9)\n" \
  "v_mfma_f32_16x16x32_f16 v[16:19], v[72:75], v[124:127], v[16:19]\nds_read_b64_tr_b16 v[104:105], v5 offset:768\n" \
  "ds_read_b64_tr_b16 v[106:107], v5 offset:800\nv_mfma_f32_16x16x32_f16 v[16:19], v[68:71], v[96:99], v[16:19]\n" \
  "ds_read_b64_tr_b16 v[80:81], v5 offset:1024\nds_read_b64_tr_b16 v[82:83], v5 offset:1056\ns_waitcnt lgkmcnt(11)\n" \
  "v_mfma_f32_16x16x32_f16 v[16:19], v[64:67], v[28:31], v[16:19]\nds_read_b64_tr_b16 v[24:25], v5 offset:1280\n" \
  "ds_read_b64_tr_b16 v[26:27], v5 offset:1312\nv_mfma_f32_16x16x32_f16 v[16:19], v[60:63], v[92:95], v[16:19]\n" \
  "ds_read_b64_tr_b16 v[76:77], v5 offset:1536\nds_read_b64_tr_b16 v[78:79], v5 offset:1568\ns_waitcnt lgkmcnt(12)\n" \
  "v_mfma_f32_16x16x32_f16 v[16:19], v[56:59], v[112:115], v[16:19]\nds_read_b64_tr_b16 v[100:101], v5 offset:1792\n" \
  "ds_read_b64_tr_b16 v[102:103], v5 offset:1824\nv_mfma_f32_16x16x32_f16 v[12:15], v[88:91], v[52:55], v[12:15]\ns_waitcnt lgkmcnt(10)\n" \
  "v_mfma_f32_16x16x32_f16 v[12:15], v[108:111], v[48:51], v[12:15]\nds_read_b64_tr_b16 v[72:73], v7 offset:0\n" \
  "ds_read_b64_tr_b16 v[74:75], v7 offset:64\nv_mfma_f32_16x16x32_f16 v[12:15], v[84:87], v[44:47], v[12:15]\n" \
  "ds_read_b64_tr_b16 v[124:125], v7 offset:512\nds_read_b64_tr_b16 v[126:127], v7 offset:576\n" \
  "v_mfma_f32_16x16x32_f16 v[16:19], v[84:87], v[52:55], v[16:19]\ns_waitcnt lgkmcnt(10)\n" \
  "v_mfma_f32_16x16x32_f16 v[12:15], v[104:107], v[40:43], v[12:15]\nv_mfma_f32_16x16x32_f16 v[16:19], v[104:107], v[48:51], v[16:19]\n" \
  "v_mfma_f32_16x16x32_f16 v[12:15], v[80:83], v[120:123], v[12:15]\nv_mfma_f32_16x16x32_f16 v[16:19], v[80:83], v[44:47], v[16:19]\n" \
  "s_waitcnt lgkmcnt(6)\nv_mfma_f32_16x16x32_f16 v[12:15], v[24:27], v[116:119], v[12:15]\n" \
  "v_mfma_f32_16x16x32_f16 v[16:19], v[24:27], v[40:43], v[16:19]\nv_mfma_f32_16x16x32_f16 v[16:19], v[76:79], v[120:123], v[16:19]\n" \
  "s_waitcnt lgkmcnt(2)\nv_mfma_f32_16x16x32_f16 v[16:19], v[100:103], v[116:119], v[16:19]\n" \
  "v_mfma_f32_16x16x32_f16 v[12:15], v[72:75], v[32:35], v[12:15]\nv_mfma_f32_16x16x32_f16 v[16:19], v[72:75], v[36:39], v[16:19]\n" \
  "s_waitcnt lgkmcnt(0)\nv_mfma_f32_16x16x32_f16 v[16:19], v[124:127], v[32:35], v[16:19]\ns_nop 4\nv_cvt_pkrtz_f16_f32 v20, v12, v13\n" \
  "v_cvt_pkrtz_f16_f32 v21, v14, v15\nv_pk_add_u16 v20, v20, 4 op_sel_hi:[1,0] clamp\nv_pk_add_u16 v21, v21, 4 op_sel_hi:[1,0] clamp\n" \
  "ds_write_b64 v4, v[20:21] offset:4096\nv_cvt_pkrtz_f16_f32 v20, v16, v17\nv_cvt_pkrtz_f16_f32 v21, v18, v19\n" \
  "v_pk_add_u16 v20, v20, 4 op_sel_hi:[1,0] clamp\nv_pk_add_u16 v21, v21, 4 op_sel_hi:[1,0] clamp\nds_write_b64 v4, v[20:21] offset:5120\n" \
  "s_setprio 0\ns_setpc_b64 s[90:91]\n61:\ns_movk_i32 s84, 0x4000\n63:\nds_read_b32 v23, v9 offset:32\ns_sub_u32 s84, s84, 1\ns_cbranch_scc1 62b\n" \
  "s_waitcnt lgkmcnt(0)\nv_readfirstlane_b32 s85, v23\ns_cmp_lt_u32 s85, 3\ns_cbranch_scc1 63b\ns_branch 62b\n5:\n" \
  "ds_read_b64 v[124:125], v3 offset:1088\nds_read_b64 v[126:127], v3 offset:1096\nds_read_b64 v[120:121], v3 offset:0\n" \
  "ds_read_b64 v[122:123], v3 offset:8\nds_read_b64 v[116:117], v3 offset:64\nds_read_b64 v[118:119], v3 offset:72\n" \
  "ds_read_b64 v[112:113], v3 offset:128\nds_read_b64 v[114:115], v3 offset:136\nds_read_b64 v[108:109], v3 offset:192\n" \
  "ds_read_b64 v[110:111], v3 offset:200\nds_read_b64 v[104:105], v3 offset:256\nds_read_b64 v[106:107], v3 offset:264\n" \
  "ds_read_b64 v[100:101], v3 offset:320\nds_read_b64 v[102:103], v3 offset:328\nds_read_b64 v[96:97], v3 offset:384\n" \
  "ds_read_b64 v[98:99], v3 offset:392\nds_read_b64 v[92:93], v3 offset:448\nds_read_b64 v[94:95], v3 offset:456\n" \
  "ds_read_b64 v[88:89], v3 offset:512\nds_read_b64 v[90:91], v3 offset:520\nds_read_b64 v[84:85], v3 offset:576\n" \
  "ds_read_b64 v[86:87], v3 offset:584\nds_read_b64 v[80:81], v3 offset:640\nds_read_b64 v[82:83], v3 offset:648\n" \
  "ds_read_b64 v[76:77], v3 offset:704\nds_read_b64 v[78:79], v3 offset:712\nds_read_b64 v[72:73], v3 offset:768\n" \
  "ds_read_b64 v[74:75], v3 offset:776\nds_read_b64 v[68:69], v3 offset:832\nds_read_b64 v[70:71], v3 offset:840\n" \
  "ds_read_b64 v[64:65], v3 offset:896\nds_read_b64 v[66:67], v3 offset:904\nds_read_b64 v[60:61], v3 offset:960\n" \
  "ds_read_b64 v[62:63], v3 offset:968\nds_read_b64 v[56:57], v3 offset:1024\nds_read_b64 v[58:59], v3 offset:1032\n" \
  "ds_read_b64 v[52:53], v6 offset:0\nds_read_b64 v[54:55], v6 offset:8\nds_read_b64 v[48:49], v6 offset:64\nds_read_b64 v[50:51], v6 offset:72\n" \
  "ds_read_b64 v[44:45], v6 offset:128\nds_read_b64 v[46:47], v6 offset:136\nds_read_b64 v[40:41], v6 offset:192\n" \
  "ds_read_b64 v[42:43], v6 offset:200\ns_waitcnt lgkmcnt(0)\ns_barrier\nds_read_b128 v[24:27], v2 offset:1472\n" \
  "ds_read_b128 v[28:31], v2 offset:1984\nds_read_b128 v[32:35], v2 offset:2496\nds_read_b128 v[36:39], v2 offset:2560\ns_waitcnt lgkmcnt(3)\n" \
  "v_mfma_f32_16x16x32_f16 v[12:15], v[124:127], v[24:27], 0\ns_setprio 1\nds_read_b128 v[24:27], v2 offset:2624\ns_waitcnt lgkmcnt(3)\n" \
  "v_mfma_f32_16x16x32_f16 v[16:19], v[124:127], v[28:31], 0\nds_read_b128 v[124:127], v2 offset:2688\ns_waitcnt lgkmcnt(3)\n" \
  "v_mfma_f32_16x16x32_f16 v[12:15], v[120:123], v[32:35], v[12:15]\nds_read_b128 v[28:31], v2 offset:2752\ns_waitcnt lgkmcnt(3)\n" \
  "v_mfma_f32_16x16x32_f16 v[12:15], v[116:119], v[36:39], v[12:15]\nds_read_b128 v[32:35], v2 offset:2816\ns_waitcnt lgkmcnt(3)\n" \
  "v_mfma_f32_16x16x32_f16 v[12:15], v[112:115], v[24:27], v[12:15]\nds_read_b128 v[36:39], v2 offset:2880\ns_waitcnt lgkmcnt(3)\n" \
  "v_mfma_f32_16x16x32_f16 v[12:15], v[108:111], v[124:127], v[12:15]\nds_read_b128 v[24:27], v2 offset:2944\ns_waitcnt lgkmcnt(3)\n" \
  "v_mfma_f32_16x16x32_f16 v[12:15], v[104:107], v[28:31], v[12:15]\nds_read_b128 v[124:127], v2 offset:3008\ns_waitcnt lgkmcnt(3)\n" \
  "v_mfma_f32_16x16x32_f16 v[12:15], v[100:103], v[32:35], v[12:15]\nds_read_b128 v[28:31], v2 offset:3072\ns_waitcnt lgkmcnt(3)\n" \
  "v_mfma_f32_16x16x32_f16 v[12:15], v[96:99], v[36:39], v[12:15]\nds_read_b128 v[32:35], v2 offset:3136\ns_waitcnt lgkmcnt(3)\n" \
  "v_mfma_f32_16x16x32_f16 v[12:15], v[92:95], v[24:27], v[12:15]\nds_read_b128 v[36:39], v2 offset:3200\ns_waitcnt lgkmcnt(3)\n" \
  "v_mfma_f32_16x16x32_f16 v[12:15], v[88:91], v[124:127], v[12:15]\nds_read_b128 v[24:27], v2 offset:3264\n" \
  "v_mfma_f32_16x16x32_f16 v[16:19], v[120:123], v[124:127], v[16:19]\nds_read_b64 v[120:121], v6 offset:256\nds_read_b64 v[122:123], v6 offset:264\n" \
  "s_waitcnt lgkmcnt(4)\nv_mfma_f32_16x16x32_f16 v[12:15], v[84:87], v[28:31], v[12:15]\nds_read_b128 v[124:127], v2 offset:3328\n" \
  "v_mfma_f32_16x16x32_f16 v[16:19], v[116:119], v[28:31], v[16:19]\nds_read_b64 v[116:117], v6 offset:320\nds_read_b64 v[118:119], v6 offset:328\n" \
  "v_mfma_f32_16x16x32_f16 v[12:15], v[80:83], v[32:35], v[12:15]\nds_read_b128 v[28:31], v2 offset:3392\n" \
  "v_mfma_f32_16x16x32_f16 v[16:19], v[112:115], v[32:35], v[16:19]\ns_waitcnt lgkmcnt(6)\n" \
  "v_mfma_f32_16x16x32_f16 v[12:15], v[76:79], v[36:39], v[12:15]\nds_read_b128 v[112:115], v2 offset:3456\n" \
  "v_mfma_f32_16x16x32_f16 v[16:19], v[108:111], v[36:39], v[16:19]\nds_read2_b32 v[32:33], v8 offset0:16 offset1:17\n" \
  "ds_read2_b32 v[34:35], v8 offset0:18 offset1:19\nv_mfma_f32_16x16x32_f16 v[12:15], v[72:75], v[24:27], v[12:15]\n" \
  "ds_read_b128 v[108:111], v2 offset:3520\nv_mfma_f32_16x16x32_f16 v[16:19], v[104:107], v[24:27], v[16:19]\n" \
  "ds_read2_b32 v[36:37], v8 offset0:0 offset1:1\nds_read2_b32 v[38:39], v8 offset0:2 offset1:3\ns_waitcnt lgkmcnt(6)\n" \
  "v_mfma_f32_16x16x32_f16 v[12:15], v[68:71], v[124:127], v[12:15]\nds_read_b128 v[104:107], v2 offset:3584\n" \
  "v_mfma_f32_16x16x32_f16 v[16:19], v[100:103], v[124:127], v[16:19]\nds_read_b128 v[24:27], v2 offset:3648\n" \
  "ds_read2_b32 v[100:101], v8 offset0:32 offset1:33\nds_read2_b32 v[102:103], v8 offset0:34 offset1:35\n" \
  "v_mfma_f32_16x16x32_f16 v[12:15], v[64:67], v[28:31], v[12:15]\nds_read_b128 v[124:127], v2 offset:3712\nds_read_b32 v23, v9 offset:32\n" \
  "v_mfma_f32_16x16x32_f16 v[16:19], v[96:99], v[28:31], v[16:19]\nds_read_b128 v[96:99], v2 offset:3776\ns_waitcnt lgkmcnt(9)\n" \
  "v_mfma_f32_16x16x32_f16 v[12:15], v[60:63], v[112:115], v[12:15]\nds_read_b128 v[28:31], v2 offset:3840\n" \
  "v_mfma_f32_16x16x32_f16 v[16:19], v[92:95], v[112:115], v[16:19]\nds_read_b128 v[92:95], v2 offset:3904\n" \
  "v_mfma_f32_16x16x32_f16 v[12:15], v[56:59], v[108:111], v[12:15]\nds_read_b128 v[112:115], v2 offset:3968\n" \
  "v_mfma_f32_16x16x32_f16 v[16:19], v[88:91], v[108:111], v[16:19]\nds_read_b128 v[88:91], v2 offset:4032\ns_waitcnt lgkmcnt(5)\n" \
  "v_readfirstlane_b32 s85, v23\ns_cmp_lt_u32 s85, 3\ns_cbranch_scc1 71f\n72:\nv_mfma_f32_16x16x32_f16 v[16:19], v[84:87], v[104:107], v[16:19]\n" \
  "ds_read_b64_tr_b16 v[108:109], v5 offset:1024\nds_read_b64_tr_b16 v[110:111], v5 offset:1056\n" \
  "v_mfma_f32_16x16x32_f16 v[16:19], v[80:83], v[24:27], v[16:19]\nds_read_b64_tr_b16 v[84:85], v5 offset:1280\n" \
  "ds_read_b64_tr_b16 v[86:87], v5 offset:1312\nv_mfma_f32_16x16x32_f16 v[16:19], v[76:79], v[124:127], v[16:19]\n" \
  "ds_read_b64_tr_b16 v[104:105], v5 offset:1536\nds_read_b64_tr_b16 v[106:107], v5 offset:1568\ns_waitcnt lgkmcnt(9)\n" \
  "v_mfma_f32_16x16x32_f16 v[16:19], v[72:75], v[96:99], v[16:19]\nds_read_b64_tr_b16 v[80:81], v5 offset:1792\n" \
  "ds_read_b64_tr_b16 v[82:83], v5 offset:1824\nv_mfma_f32_16x16x32_f16 v[16:19], v[68:71], v[28:31], v[16:19]\n" \
  "ds_read_b64_tr_b16 v[24:25], v5 offset:2048\nds_read_b64_tr_b16 v[26:27], v5 offset:2080\ns_waitcnt lgkmcnt(11)\n" \
  "v_mfma_f32_16x16x32_f16 v[16:19], v[64:67], v[92:95], v[16:19]\nds_read_b64_tr_b16 v[76:77], v5 offset:2304\n" \
  "ds_read_b64_tr_b16 v[78:79], v5 offset:2336\nv_mfma_f32_16x16x32_f16 v[16:19], v[60:63], v[112:115], v[16:19]\n" \
  "ds_read_b64_tr_b16 v[124:125], v5 offset:2560\nds_read_b64_tr_b16 v[126:127], v5 offset:2592\ns_waitcnt lgkmcnt(12)\n" \
  "v_mfma_f32_16x16x32_f16 v[16:19], v[56:59], v[88:91], v[16:19]\nds_read_b64_tr_b16 v[72:73], v5 offset:2816\n" \
  "ds_read_b64_tr_b16 v[74:75], v5 offset:2848\nv_mfma_f32_16x16x32_f16 v[12:15], v[108:111], v[52:55], v[12:15]\ns_waitcnt lgkmcnt(10)\n" \
  "v_mfma_f32_16x16x32_f16 v[12:15], v[84:87], v[48:51], v[12:15]\nds_read_b64_tr_b16 v[96:97], v7 offset:0\n" \
  "ds_read_b64_tr_b16 v[98:99], v7 offset:64\nv_mfma_f32_16x16x32_f16 v[12:15], v[104:107], v[44:47], v[12:15]\n" \
  "ds_read_b64_tr_b16 v[68:69], v7 offset:512\nds_read_b64_tr_b16 v[70:71], v7 offset:576\n" \
  "v_mfma_f32_16x16x32_f16 v[16:19], v[104:107], v[52:55], v[16:19]\ns_waitcnt lgkmcnt(10)\n" \
  "v_mfma_f32_16x16x32_f16 v[12:15], v[80:83], v[40:43], v[12:15]\nds_read_b64_tr_b16 v[28:29], v7 offset:1024\n" \
  "ds_read_b64_tr_b16 v[30:31], v7 offset:1088\nv_mfma_f32_16x16x32_f16 v[16:19], v[80:83], v[48:51], v[16:19]\n" \
  "ds_read2_b32 v[64:65], v8 offset0:48 offset1:49\nds_read2_b32 v[66:67], v8 offset0:50 offset1:51\n" \
  "v_mfma_f32_16x16x32_f16 v[12:15], v[24:27], v[120:123], v[12:15]\nv_mfma_f32_16x16x32_f16 v[16:19], v[24:27], v[44:47], v[16:19]\n" \
  "s_waitcnt lgkmcnt(10)\nv_mfma_f32_16x16x32_f16 v[12:15], v[76:79], v[116:119], v[12:15]\nds_read_b64_tr_b16 v[92:93], v7 offset:1536\n" \
  "ds_read_b64_tr_b16 v[94:95], v7 offset:1600\nv_mfma_f32_16x16x32_f16 v[16:19], v[76:79], v[40:43], v[16:19]\n" \
  "v_mfma_f32_16x16x32_f16 v[16:19], v[124:127], v[120:123], v[16:19]\ns_waitcnt lgkmcnt(8)\n" \
  "v_mfma_f32_16x16x32_f16 v[16:19], v[72:75], v[116:119], v[16:19]\nv_mfma_f32_16x16x32_f16 v[12:15], v[96:99], v[32:35], v[12:15]\n" \
  "v_mfma_f32_16x16x32_f16 v[16:19], v[96:99], v[36:39], v[16:19]\ns_waitcnt lgkmcnt(4)\n" \
  "v_mfma_f32_16x16x32_f16 v[12:15], v[68:71], v[100:103], v[12:15]\nv_mfma_f32_16x16x32_f16 v[16:19], v[68:71], v[32:35], v[16:19]\n" \
  "s_waitcnt lgkmcnt(2)\nv_mfma_f32_16x16x32_f16 v[12:15], v[28:31], v[64:67], v[12:15]\n" \
  "v_mfma_f32_16x16x32_f16 v[16:19], v[28:31], v[100:103], v[16:19]\ns_waitcnt lgkmcnt(0)\n" \
  "v_mfma_f32_16x16x32_f16 v[16:19], v[92:95], v[64:67], v[16:19]\ns_nop 4\nv_cvt_pkrtz_f16_f32 v20, v12, v13\nv_cvt_pkrtz_f16_f32 v21, v14, v15\n" \
  "v_pk_add_u16 v20, v20, 4 op_sel_hi:[1,0] clamp\nv_pk_add_u16 v21, v21, 4 op_sel_hi:[1,0] clamp\nds_write_b64 v4, v[20:21] offset:6144\n" \
  "v_cvt_pkrtz_f16_f32 v20, v16, v17\nv_cvt_pkrtz_f16_f32 v21, v18, v19\nv_pk_add_u16 v20, v20, 4 op_sel_hi:[1,0] clamp\n" \
  "v_pk_add_u16 v21, v21, 4 op_sel_hi:[1,0] clamp\nds_write_b64 v4, v[20:21] offset:7168\ns_setprio 0\ns_setpc_b64 s[90:91]\n71:\n" \
  "s_movk_i32 s84, 0x4000\n73:\nds_read_b32 v23, v9 offset:32\ns_sub_u32 s84, s84, 1\ns_cbranch_scc1 72b\ns_waitcnt lgkmcnt(0)\n" \
  "v_readfirstlane_b32 s85, v23\ns_cmp_lt_u32 s85, 3\ns_cbranch_scc1 73b\ns_branch 72b\n"
#define CONV_CLOB "memory", "scc", "s84", "s85", "s90", "s91", "v12", "v13", "v14", "v15", "v16", "v17", "v18", "v19", "v20", "v21", "v22", "v23", "v24", "v25", "v26", "v27", "v28", "v29", "v30", "v31", "v32", "v33", "v34", "v35", "v36", "v37", "v38", "v39", "v40", "v41", "v42", "v43", "v44", "v45", "v46", "v47", "v48", "v49", "v50", "v51", "v52", "v53", "v54", "v55", "v56", "v57", "v58", "v59", "v60", "v61", "v62", "v63", "v64", "v65", "v66", "v67", "v68", "v69", "v70", "v71", "v72", "v73", "v74", "v75", "v76", "v77", "v78", "v79", "v80", "v81", "v82", "v83", "v84", "v85", "v86", "v87", "v88", "v89", "v90", "v91", "v92", "v93", "v94", "v95", "v96", "v97", "v98", "v99", "v100", "v101", "v102", "v103", "v104", "v105", "v106", "v107", "v108", "v109", "v110", "v111", "v112", "v113", "v114", "v115", "v116", "v117", "v118", "v119", "v120", "v121", "v122", "v123", "v124", "v125", "v126", "v127"
constexpr int TDP = 1216, T_3 = 4 * TDP, T_4 = T_3 + 512, TB = T_4 + 320;
constexpr int C_U = 480, UB = C_U + 4096 + 96, C_G5 = 0, C_G3 = 3200, C_G9 = 6400, C_G7 = C_G9 + 2096, GB = C_G7 + 2080 + 112;
constexpr int O_FL = 2 * TB, O_UL = O_FL + 64, O_IM = O_UL + 2 * UB, O_ST = O_IM + 2 * GB;
constexpr int X_TB = 10240, X_UL = X_TB + TB, X_IM = X_UL + UB, X_ST = X_IM + GB;
constexpr int X_WR = X_IM + 128 * IP;
#define X_RW(w) (LW + 9216 * (2 + ((w) >> 1)) + 2560 * ((w) & 1))
constexpr int RP = 1216;
constexpr int LW = 36864 + 512, LA = LW, LM = LW + 36864;
constexpr int L_SC = LM + 2048, SMEM = O_IM + 256 * IP;
static_assert(L_SC + 1152 <= SMEM && O_ST + 8192 <= SMEM && X_IM + 128 * IP <= LM && X_ST + 8192 <= LM && UB % 128 == 64 && GB % 128 == 64 && SMEM <= 79872, "");
struct TOps { uint32_t xa, xb, ya, pd, pf, pg, pe; };
__device__ __forceinline__ void table_ops(uint32_t im, uint32_t tb, int ct, int lane, TOps& t) {
  const int j = lane & 15, kq = lane >> 4, q4 = (lane >> 2) & 3, p4 = lane & 3, jj = j + 16 * ct;
  const uint32_t sa = im + (uint32_t)(IP * (lane >> 2));
  t.xa = sa + (uint32_t)(I_X + 8 * p4); t.xb = sa + (uint32_t)(I_X + 32 + 8 * (p4 & 1)); t.ya = sa + (uint32_t)(32 * ct + 8 * p4);
  t.pd = tb + (uint32_t)(2 * (541 - (8 * jj + 4 * (kq & 1) + 256 * (kq >> 1))));
  t.pf = tb + (uint32_t)((kq & 1) ? T_4 + 2 * (63 - jj) : T_3 + 2 * (127 - 2 * jj));
  t.pg = t.pf - ((kq & 1) ? 64 : 2);
  t.pe = tb + (uint32_t)(p4 * (TDP + 2) + ((lane < 12) ? 2 * (32 - q4) : 2 * (572 + (q4 & 1))));
  asm volatile("" : "+v"(t.xa), "+v"(t.xb), "+v"(t.ya), "+v"(t.pd), "+v"(t.pf), "+v"(t.pg), "+v"(t.pe));
}
typedef __attribute__((address_space(3))) char* lptr;
#define LP(T, a) ((__attribute__((address_space(3))) T*)(uintptr_t)(a))
__device__ __forceinline__ uint32_t table_tile(const TOps& t, int ct, int lane, uint32_t evp, float dsk TSP) {
  u32x4 ev;
  f32x4 ad[2], af[2];
  asm volatile(T_ASM : "=&v"(ad[0]), "=&v"(ad[1]), "=&v"(af[0]), "=&v"(af[1]), "=&v"(ev) : "v"(t.xa), "v"(t.xb), "v"(t.ya), "v"(evp) : T_CLOB);
  const int e0 = (int)(ev[0] + ev[2]), e1 = (int)(ev[1] + ev[3]);
  const int em = min(max(max(max(e0, e1), (int)((__float_as_uint(dsk) >> 23) & 0xffu) - 133), -110), 115);
  const int ts = ((uint32_t)(em + 12) <= 8u) ? 1 : -3 - em;
  float s0 = __builtin_amdgcn_ldexpf(1.f, e0 - 29 + ts), s1 = __builtin_amdgcn_ldexpf(1.f, e1 - 29 + ts);
  float d0 = (ct == 0 && lane == 0) ? __builtin_amdgcn_ldexpf(dsk, ts) : 0.f;
  asm volatile("s_nop 1" : "+v"(ad[0]), "+v"(ad[1]), "+v"(af[0]), "+v"(af[1]), "+v"(s0), "+v"(s1), "+v"(d0));
  const f32x4 k = ad[0] * s0 + (f32x4){d0, 0.f, 0.f, 0.f} + ad[1] * s1, f = af[0] * s0 + af[1] * s1;
  auto pk = [](float a, float b) { uint32_t r; asm("v_cvt_pk_f16_f32 %0, %1, %2" : "=v"(r) : "v"(a), "v"(b)); return r; };
  asm volatile("s_nop 0" : : "v"(k), "v"(f));
  const uint32_t p32 = pk(k[3], k[2]), p10 = pk(k[1], k[0]), p21 = pk(k[2], k[1]), p01 = pk(f[0], f[1]), p23 = pk(f[2], f[3]);
  *LP(u32x2, t.pd + 3 * TDP + 6) = (u32x2){p32, p10};
  *LP(uint32_t, t.pd + TDP + 2) = p32; *LP(uint32_t, t.pd + TDP + 6) = p10;
  *LP(uint32_t, t.pd + 2) = p21; *LP(uint32_t, t.pd + 2 * TDP + 6) = p21;
  *LP(u16, t.pd) = (u16)p32; *LP(u16, t.pd + 2 * TDP + 4) = (u16)p32;
  asm volatile("ds_write_b16_d16_hi %0, %1 offset:6\n\tds_write_b16_d16_hi %0, %1 offset:%2" : : "v"(t.pd), "v"(p10), "n"(2 * TDP + 10) : "memory");
  if (ct) {
    const uint32_t va = (uint32_t)__builtin_amdgcn_readlane((int)p32, 63), vb = (uint32_t)__builtin_amdgcn_readlane((int)p23, 15) >> 16;
    *LP(u16, t.pe) = (u16)((lane < 12) ? va : vb);
  }
  if (lane < 32) {
    *LP(u16, t.pf + 128) = (u16)p01; *LP(u16, t.pg + 128) = (u16)(p01 >> 16);
    *LP(u16, t.pf) = (u16)p23; *LP(u16, t.pg) = (u16)(p23 >> 16);
  }
  return (uint32_t)(127 + 5 - ts) << 23;
}
__device__ __forceinline__ uint32_t hadd(uint32_t a, uint32_t b) { return __builtin_bit_cast(uint32_t, __builtin_bit_cast(f16x2, a) + __builtin_bit_cast(f16x2, b)); }
template <bool FULL> __device__ __forceinline__ void sums(char* sm, const u32x4 p, const u32x4 c) {
  const uint32_t D[8] = {p[0], p[1], p[2], p[3], c[0], c[1], c[2], c[3]};
  uint32_t O[7];
#pragma unroll
  for (int j = 0; j < 7; ++j) O[j] = __builtin_amdgcn_alignbit(D[j + 1], D[j], 16);
  u32x4 a, b;
#pragma unroll
  for (int j = 0; j < 4; ++j) a[j] = hadd(D[j + 2], hadd(O[j + 1], O[j + 2]));
  *(u32x4*)(sm + C_G3 + 32) = a;
#pragma unroll
  for (int j = 0; j < 4; ++j) b[j] = hadd(a[j], hadd(D[j + 1], D[j + 3]));
  *(u32x4*)(sm + C_G5 + 32) = b;
  if (FULL) {
#pragma unroll
    for (int j = 0; j < 4; ++j) a[j] = hadd(b[j], hadd(O[j], O[j + 3]));
    *(u32x4*)(sm + C_G7 + 32) = a;
#pragma unroll
    for (int j = 0; j < 4; ++j) b[j] = hadd(a[j], hadd(D[j], D[j + 4]));
    *(u32x4*)(sm + C_G9 + 32) = b;
  }
}
__device__ __forceinline__ void count_up(uint32_t a) {
  uint32_t t;
  asm volatile("s_mov_b64 exec, 1\n\tv_mov_b32 %0, 1\n\tds_add_u32 %1, %0\n\ts_mov_b64 exec, -1" : "=&v"(t) : "v"(a) : "memory");
}
__device__ __forceinline__ int pad_at(int r) { return 16 * (r & 1) + 3200 * (r >> 1) - ((r >= 6) ? 3200 - 2096 : 0); }
struct COps { uint32_t b, a, o, g3, k3, g4, k4, f; };
__device__ __forceinline__ void conv_ops(uint32_t tb, uint32_t ub, uint32_t cb, int lane, COps& e) {
  const uint32_t j = lane & 15, kq = lane >> 4, q4 = (lane >> 2) & 3, p4 = lane & 3;
  e.b = ub + 32u * j + 16u * kq;
  e.a = tb + p4 * TDP + 32u - 8u * q4 + 16u * kq;
  e.g3 = cb + C_G5 + 8 + 64u * kq + 8u * (q4 + p4) + (q4 & 1u) * 3200u;
  e.g4 = cb + C_G9 + 8 + 128u * kq + 16u * q4 + 8u * p4 + (q4 & 1u) * 2096u;
  e.k3 = tb + T_3 + 120 - 8u * j + 16u * kq;
  e.k4 = tb + T_4 + 60 - 4u * j + 16u * kq;
  asm volatile("" : "+v"(e.b), "+v"(e.a), "+v"(e.g3), "+v"(e.g4), "+v"(e.k3), "+v"(e.k4));
}
#define CONV_RUN(prog, e, ...) do { int prog_ = (prog); \
    asm volatile("s_call_b64 s[90:91], kf_conv" : "+{s89}"(prog_), "+{v2}"(e.b), "+{v3}"(e.a), "+{v4}"(e.o), "+{v5}"(e.g3), "+{v6}"(e.k3), "+{v7}"(e.g4), \
                 "+{v8}"(e.k4), "+{v9}"(e.f) __VA_ARGS__ : : CONV_CLOB); } while (0)

template <bool E>
__device__ __forceinline__ void ln_stats(const uint32_t* v, int lane, float sc, f32x2* t, f32x4 z, f32x2& rc, f32x2& nc, float& e) {
  float s1a = z[0], s2a = z[1], s1b = z[2], s2b = z[3];
#pragma unroll
  for (int k = 0; k < 4; ++k) {
    s1a = dot2(v[k], 0x3f803f80u, s1a); s2a = dot2(v[k], v[k], s2a);
    s1b = dot2(v[4 + k], 0x3f803f80u, s1b); s2b = dot2(v[4 + k], v[4 + k], s2b);
  }
#pragma unroll
  for (int k = 0; k < 9; ++k) t[k] = (f32x2){bflo(v[k]), bfhi(v[k])};
  const float rs = row_sum(fold16(dot2(v[8], 0x3f803f80u, fold32(s1a, s1b)), dot2(v[8], v[8], fold32(s2a, s2b))));
  if (E) { e = row_sum(e); e += __int_as_float(__builtin_amdgcn_update_dpp(0, __float_as_int(e), 0x142, 0xa, 0xf, false)); }
  const f32x2 mean = (f32x2){rdl(rs, 15), rdl(rs, 47)} * (1.f / D_), m2 = mean * mean;
  f32x2 var = (f32x2){rdl(rs, 31), rdl(rs, 63)} * (1.f / D_) - m2;
  asm volatile("" : "+v"(t[0]), "+v"(t[1]), "+v"(t[2]), "+v"(t[3]), "+v"(t[4]), "+v"(t[5]), "+v"(t[6]), "+v"(t[7]), "+v"(t[8]));
  if (__builtin_expect(!(m2[0] <= 16.f * var[0] && m2[1] <= 16.f * var[1]), 0)) {
    float q[3] = {0.f, 0.f, 0.f};
#pragma unroll
    for (int k = 0; k < 9; ++k) {
      const f32x2 a = t[k] - ((k < 4 || (k == 8 && lane < 32)) ? mean[0] : mean[1]);
      q[k >> 2] += a[0] * a[0] + a[1] * a[1];
    }
    float qa = q[0] + ((lane < 32) ? q[2] : 0.f), qb = q[1] + ((lane < 32) ? 0.f : q[2]);
    wave_sum2(qa, qb);
    var = (f32x2){qa, qb} * (1.f / D_);
  }
  const uint32_t mp = pack2(mean[0], mean[1]), vp = pack2(var[0], var[1]);
  const f32x2 ve = (f32x2){bflo(vp), bfhi(vp)} + 1e-5f;
  const uint32_t e2 = pack2(ve[0], ve[1]);
  const uint32_t sp = pack2(__builtin_amdgcn_sqrtf(bflo(e2)), __builtin_amdgcn_sqrtf(bfhi(e2)));
  rc = (f32x2){__builtin_amdgcn_rcpf(bflo(sp)), __builtin_amdgcn_rcpf(bfhi(sp))} * sc;
  nc = -(f32x2){bflo(mp), bfhi(mp)} * rc;
}
template <bool H>
__device__ __forceinline__ void ln_apply(const f32x2* t, int lane, f32x2 rc, f32x2 nc, uint32_t* c) {
  const float nm[3] = {nc[0], nc[1], (lane < 32) ? nc[0] : nc[1]}, rr[3] = {rc[0], rc[1], (lane < 32) ? rc[0] : rc[1]};
  f32x2 dd[9];
#pragma unroll
  for (int k = 0; k < 9; ++k) dd[k] = __builtin_elementwise_fma(t[k], (f32x2){rr[k >> 2], rr[k >> 2]}, (f32x2){nm[k >> 2], nm[k >> 2]});
  asm volatile("" : "+v"(dd[0]), "+v"(dd[1]), "+v"(dd[2]), "+v"(dd[3]), "+v"(dd[4]), "+v"(dd[5]), "+v"(dd[6]), "+v"(dd[7]), "+v"(dd[8]));
#pragma unroll
  for (int k = 0; k < 9; ++k) {
    const f32x2 d = dd[k];
    typedef __attribute__((ext_vector_type(2))) uint16_t u16x2;
    if (H) c[k] = __builtin_bit_cast(uint32_t, __builtin_elementwise_add_sat(__builtin_bit_cast(u16x2, __builtin_amdgcn_cvt_pkrtz(d[0], d[1])), (u16x2){4, 4}));
    else c[k] = pack2(d[0], d[1]);
  }
}
__device__ __forceinline__ uint32_t med3u(uint32_t a, uint32_t b, uint32_t c) { return max(min(a, b), min(max(a, b), c)); }
__device__ __forceinline__ uint32_t oct_maxu(uint32_t v) {
  v = max(v, (uint32_t)DPP_I((int)v, 0xb1, 0xf));
  v = max(v, (uint32_t)DPP_I((int)v, 0x4e, 0xf));
  v = max(v, (uint32_t)DPP_I((int)v, 0x141, 0xf));
  return v;
}

#define KARGS const u16* __restrict__ x, const u16* __restrict__ Are, const u16* __restrict__ Aim, const u16* __restrict__ Bre, \
    const u16* __restrict__ Bim, const u16* __restrict__ Cre, const u16* __restrict__ Cim, const u16* __restrict__ logdt, \
    const u16* __restrict__ Dskip, char* __restrict__ ws, const u16* __restrict__ wr, const u16* __restrict__ wg, \
    const u16* __restrict__ wu, const u16* __restrict__ wd, u16* __restrict__ out
#define KPASS x, Are, Aim, Bre, Bim, Cre, Cim, logdt, Dskip, ws, wr, wg, wu, wd, out
template <int ROLE, int FFN = -1>
__device__ __forceinline__ void role_body(char* smem, KARGS) {
  const int tid = threadIdx.x, lane = tid & 63;
  const int w = __builtin_amdgcn_readfirstlane(tid >> 6), wl = w & 3;
  const int b = blockIdx.x & 255, bx = b & 7, bj = b >> 3;
  constexpr bool isl = ROLE == 1 || ROLE == 2;
  const int tg = isl ? 32 * (bj >> 2) + 4 * bx + (bj & 3) : 32 * bx + bj;
  u32x4 xa, xc;
  uint32_t ep, r0;
#define P_WAIT(n, ...) asm volatile("s_waitcnt vmcnt(" #n ")\n\tv_lshlrev_b32 %0, 16, v100\n\tv_lshlrev_b32 %1, 16, v101\n\tv_lshlrev_b32 %2, 16, v102" \
    : "=v"(ldt), "=v"(are), "=v"(aim) : : "v100", "v101", "v102" __VA_ARGS__)
#define P_MORE() asm volatile("v_lshlrev_b32 %0, 16, v103\n\tv_lshlrev_b32 %1, 16, v104\n\tv_lshlrev_b32 %2, 16, v105\n\tv_lshlrev_b32 %3, 16, v106" \
    : "=v"(bre), "=v"(bim), "=v"(cre), "=v"(cim) : : "v103", "v104", "v105", "v106")
  TSD;
  TS(0, 0);
  const uint32_t lb0 = (uint32_t)(uintptr_t)(__attribute__((address_space(3))) char*)smem;
  const int i = lane & 15, kq = lane >> 4;
  const u32x4 ones = {0xffffffffu, 0xffffffffu, 0xffffffffu, 0xffffffffu}, zero4 = {0u, 0u, 0u, 0u};
  auto u_again = [&](u32x4& uw, const u32x4& ut, uint32_t uo) {
#pragma unroll 1
    for (int spin = 0; spin < SPIN_CAP; ++spin) {
      const bool na = ((uw[0] | uw[1] | uw[2] | uw[3]) & UMASK) != 0u;
      if (__builtin_expect(!__any(na), 1)) break;
      TSC(19);
      SLEEP(USLEEP);
      if (na) { LD16W(uw, uo); uw ^= ut; }
    }
  };
  constexpr uint32_t S16 = 0x41800000u, LMASK = 0x00070007u;
  auto v_put = [&](u32x4 v, const u32x4& ut, bool other, uint32_t vo) {
    const bool second = __any(other || ((v[0] | v[1] | v[2] | v[3]) & 0x40004000u) != 0u);
    if (__builtin_expect(second, 0)) v = (v & ~LMASK) | (~ut & LMASK); else v = (v & ~LMASK) | ut;
    ST16(vo, v);
    return second;
  };
  auto v_end = [&](uint32_t T, int n) { DRAIN(); if (lane == 0) ST4(O_DN + 16u * n, T); };
  if constexpr (!isl) {
    const int ch = wl >> 1, tl = tid & 255;
    int* evp = (int*)(smem + O_FL + 16 * ch);
    float ldt, are, aim, dsk;
    if constexpr (ROLE == 0) {
      __builtin_amdgcn_s_setprio(KPRIO);
      COps e;
      conv_ops(lb0 + (uint32_t)(ch * TB), lb0 + (uint32_t)(O_UL + ch * UB), lb0 + (uint32_t)(O_IM + ch * GB), lane, e);
      e.o = lb0 + O_ST + (uint32_t)(16 * ((4 * i + 2 * (kq >> 1) + ch) ^ (i >> 1)) + 8 * (kq & 1));
      e.f = lb0 + O_FL;
      TOps t;
      table_ops(lb0 + (uint32_t)(O_IM + 128 * IP * ch), lb0 + (uint32_t)(TB * ch), wl & 1, lane, t);
      uint32_t pz = lb0 + O_IM + (uint32_t)(((lane >> 3) & 1) * GB + pad_at(lane & 7)), im = lb0 + (uint32_t)(O_IM + IP * tl);
      asm volatile("" : "+v"(e.o), "+v"(e.f), "+v"(pz), "+v"(im));
      float bre, bim, cre, cim;
      P_WAIT(0, , "v103", "v104", "v105", "v106", "v107", "v109");
      asm volatile("" : "={v109}"(ep));
      const uint32_t T = ((uint32_t)__builtin_amdgcn_readfirstlane(ep) + 1u) * 0x9E3779B1u;
      if (w == 3 && lane < 8) ST16(O_KEY + 128u * b + 16u * lane, ones);
      P_MORE();
      asm volatile("v_lshlrev_b32 %0, 16, v107" : "=v"(dsk) : : "v107");
      TS(0, 1);
      evp[wl & 1] = gen_y(ldt, are, aim, bre, bim, cre, cim, im, [] {} TSA);
      TS(0, 8);
      BAR();
      TS(0, 2);
      const uint32_t cs = table_tile(t, wl & 1, lane, lb0 + O_FL + 16 * ch, dsk TSA);
      *(uint32_t*)(smem + O_FL + 40 + 4 * ch) = cs;
      DRAIN();
      if ((wl & 1) && lane == 0) asm volatile("global_store_dwordx2 %0, %1, %2 sc1" : : "v"(O_SC + 16u * (2 * tg + ch)), "v"((u32x2){T, cs}), "s"(ws) : "memory");
      TS(0, 9);
      gsync0(lb0 + O_FL + 36, 4);
      TS(0, 3);
      *(u32x4*)(smem + O_ST + 16 * tl) = ones;
      *(u32x4*)(smem + O_ST + 4096 + 16 * tl) = ones;
      if (w == 0) *LP(u32x4, pz) = zero4;
      CONV_RUN(wl & 1, e);
      TS(0, 5);
    } else {
      __builtin_amdgcn_s_setprio(UPRIO);
#pragma unroll
      for (int k = 0; k < 3; ++k) *(u32x4*)(smem + ((16 * (tl + 256 * k) < O_FL) ? 16 * (tl + 256 * k) : O_FL + 32)) = zero4;
      if (w == 7) *(u32x4*)(smem + O_UL + (lane >> 5) * UB + 16 * min(lane & 31, 29)) = zero4;
      uint32_t uo = u_at(tl >> 1, 2 * tg + (tid & 1)), go = O_SU + (uint32_t)(256 * bx + 32 * wl + (lane & 31) + 4 * (lane & 32)) * 128u;
      uint32_t ul = (uint32_t)(O_UL + (tid & 1) * UB + C_U - 16 + 16 * (tl >> 1)), ug = (uint32_t)(O_IM + (tid & 1) * GB + 16 * (tl >> 1)), so = lb0 + O_ST + 16u * (uint32_t)(lane ^ (lane >> 3));
      uint32_t vo = O_V + (uint32_t)tg * VP + 16u * lane;
      asm volatile("" : "+v"(uo), "+v"(go), "+v"(ul), "+v"(ug), "+v"(so), "+v"(vo));
      P_WAIT(1, , "v109");
      TS(0, 1);
      evp[2 + (wl & 1)] = gen_x(ldt, are, aim, smem + O_IM + IP * tl);
      asm volatile("s_waitcnt vmcnt(0)" : "={v109}"(ep));
      const uint32_t T = ((uint32_t)__builtin_amdgcn_readfirstlane(ep) + 1u) * 0x9E3779B1u;
      TS(0, 8);
      BAR();
      TS(0, 2);
      u32x4 ut = {utag(T, 0), utag(T, 1), utag(T, 2), utag(T, 3)};
      asm volatile("" : "+v"(ut));
      SLEEP(GSLEEP);
      u32x2 pv;
      pv[1] = gate(ws, go, T);
      TS(0, 3);
      until(latest(pv[1]) + UD);
      TS(0, 7);
      u32x4 uw, ux;
      asm volatile("global_load_dwordx4 %0, %2, %4 sc1\n\tglobal_load_dwordx4 %1, %3, %4 sc1"
                   : "=&v"(uw), "=&v"(ux) : "v"(uo), "v"(uo + 64 * UP * 32), "s"(ws) : "memory");
      __builtin_amdgcn_s_setprio(FPR);
      asm volatile("s_waitcnt vmcnt(1)" : "+v"(uw));
      uw ^= ut;
      *(u32x4*)(smem + ul + 16) = uw;
      asm volatile("s_waitcnt vmcnt(0)" : "+v"(ux));
      TS(0, 11);
      ux ^= ut;
      *(u32x4*)(smem + ul + 2048 + 16) = ux;
      if (__builtin_expect(__any(((uw[0] | uw[1] | uw[2] | uw[3] | ux[0] | ux[1] | ux[2] | ux[3]) & UMASK) != 0u), 0)) {
        u_again(uw, ut, uo);
        u_again(ux, ut, uo + 64 * UP * 32);
        *(u32x4*)(smem + ul + 16) = uw;
        *(u32x4*)(smem + ul + 2048 + 16) = ux;
      }
      BAR();
      TS(0, 4);
      const u32x4 pw = *(const u32x4*)(smem + ul);
      if (wl < 2) {
        const u32x4 px = *(const u32x4*)(smem + ul + 2048);
        sums<true>(smem + ug, pw, uw);
        sums<false>(smem + ug + 2048, px, ux);
      } else sums<true>(smem + ug, pw, uw);
      count_up(lb0 + O_FL + 32);
      __builtin_amdgcn_s_setprio(SPR);
      TS(0, 5);
      const bool other = *(const uint32_t*)(smem + O_FL + 40 + 4 * (lane & 1)) != S16;
      bool second = false;
#pragma unroll
      for (int k = 0; k < 2; ++k) {
        const uint32_t o = ((k ? 0x3247u : 0x1065u) >> (4 * wl) & 7u) * 1024u;
        u32x4 v;
#pragma unroll 1
        for (int spin = 0; spin < SPIN_CAP; ++spin) {
          asm volatile("ds_read_b128 %0, %1\n\ts_waitcnt lgkmcnt(0)" : "=&v"(v) : "v"(so + o) : "memory");
          if (!__any(max4(v) == 0xffffffffu)) break;
          __builtin_amdgcn_s_sleep(VPS);
        }
        TS(0, 12 + k);
        second |= v_put(v, ut, other, vo + o);
        TS(0, 10 - 4 * k);
      }
      if (__builtin_expect(second, 0)) v_end(T, 4 * tg + wl);
    }
    TSE();
  } else {
  uint32_t* cand = (uint32_t*)(smem + LM);
  uint32_t* sel = cand + 128;
  uint32_t* kl = sel + 48;
  uint32_t* cnt = sel + 64;
  const uint32_t ca = lb0 + LM + 4 * (128 + 64);
  u16* hm = (u16*)(cnt + 12);
  uint32_t T;
  const bool ffn = (FFN < 0) ? bj < 18 : FFN, isx = FFN != 1 && bj >= 24;
  const int fb = 18 * bx + bj;
  constexpr bool ln2 = ROLE == 2;
  const int g0 = tg, g2 = tg;
  u32x4 bw[18];
#pragma unroll
  for (int s = 0; s < 9; ++s) bw[s] = (u32x4){0u, 0u, 0u, 0u};
  int tid2 = threadIdx.x;
  uint32_t ts1 = 0u;
  auto rest = [&] {
    if (ts1 && (int)((uint32_t)__builtin_amdgcn_s_memrealtime() - ts1) > 0) { TS(0, 8); until(ts1 + TSTOP + RD); if (RW) gwait(ca, 4); ts1 = 0u; }
  };
  auto gu_dma = [&](int q0, int q1, auto depth) {
    const char* gs = (const char*)((w < 2) ? wg : wu) + (size_t)((144 * (w & 1)) * (F_ * 2) + fb * 64);
    const uint32_t gv = (uint32_t)((lane >> 2) * (F_ * 2) + 16 * ((lane & 3) ^ (lane >> 5) ^ (w & 2)));
#pragma unroll 1
    for (int q = q0; q < q1; ++q) {
      const int k = (int)((((bj & 1) ? ORD1 : ((unsigned __int128)0x876543210u << 36 | 0x876543210ull)) >> (4 * (q - (q >= 9 ? 9 : 0)) + (q >= 9 ? 36 : 0))) & 15u), h = q >= 9;
      if (q >= RQ) rest();
      dma16(gs + (size_t)(288 * h + 16 * k) * (F_ * 2), gv, lb0 + LW * h + 9216 * w + 1024 * k);
      asm volatile("s_waitcnt vmcnt(%0)" : : "n"(depth.value) : "memory");
    }
  };
  auto down_dma = [&](int k0, int k1, int lane) {
#pragma unroll 1
    for (int k = k0; k < k1; ++k) {
      const int o = 9216 * wl + 2304 * k;
      const char* ds = (const char*)wd + (size_t)(fb * 36864 + o);
      const uint32_t dl = lb0 + o + 32 * ((4 * wl + k + 1) >> 1);
      if (ROLE == 1) rest();
      dma16d(ds, 16u * lane, dl);
      dma16d(ds + 1024, 16u * lane, dl + 1024);
      uint32_t keep;
      asm volatile("s_mov_b32 %0, m0\n\ts_mov_b32 m0, %3\n\ts_mov_b64 exec, 0xffff\n\tglobal_load_lds_dwordx4 %1, %2" DPOL2 "\n\ts_mov_b64 exec, -1\n\ts_mov_b32 m0, %0\n\ts_waitcnt vmcnt(%4)"
                   : "=&s"(keep) : "v"(16u * lane), "s"(ds + 2048), "s"(dl + 2048), "n"(ROLE == 1 ? DDEPTH : 40) : "memory");
    }
  };
  auto gu_read = [&](int h) {
    const uint32_t ra = lb0 + LW * h + (uint32_t)(((lane >> 1) & 1) * 18432 + (8 * kq + ((lane >> 2) & 3)) * 64 + ((w ^ (kq & 1) ^ (lane & 2)) * 16) + 8 * (lane & 1));
#pragma unroll
    for (int s = 0; s < 9; ++s) {
      const u32x2 h0 = trd(ra + 2048 * s), h1 = trd(ra + 2048 * s + 256);
      bw[9 * h + s] = (u32x4){h0[0], h0[1], h1[0], h1[1]};
    }
#pragma unroll
    for (int s = 0; s < 9; ++s) asm volatile("" : "+v"(bw[9 * h + s]));
  };
  uint32_t tv = 0u, tk = 0u;
  const uint32_t vearly = (uint32_t)((VEARLY >> (8 * (g0 >> 5))) & 0xffull);
  auto w_line = [&](int n) {
    const int q = min(lane, 62) / 9, r = min(7 * (n >> 2) + q, 287), h = r >= 144;
    return (const char*)((n & 1) ? wu : wg) + (uint32_t)((r + 144 * (h + ((n >> 1) & 1))) * (F_ * 2) + bx * 1152 + (min(lane, 62) - 9 * q) * 128);
  };
#define TOUCH(pa) asm volatile("global_load_dword v126, %0, off" DPOL4 : : "v"(pa) : "memory", "v126")
  uint32_t wv[5], xv[9];
  auto rows_xw = [&](int lane) {
    u32x4 wa, xr, xs;
    const uint32_t xo = (uint32_t)((8 * g2 + 2 * wl) * (D_ * 2));
    asm volatile("global_load_dwordx4 %0, %5, %9\n\tglobal_load_dword %1, %6, %9 offset:1024\n\t"
                 "global_load_dwordx4 %2, %7, %10\n\tglobal_load_dwordx4 %3, %7, %10 offset:1152\n\tglobal_load_dword %4, %8, %10 offset:1024\n\ts_waitcnt vmcnt(0)"
                 : "=&v"(wa), "=&v"(wv[4]), "=&v"(xr), "=&v"(xs), "=&v"(xv[8])
                 : "v"(16u * lane), "v"(4u * (lane & 31)), "v"(xo + 16u * lane), "v"(xo + 4u * lane + 32u * (lane & 32)), "s"(wr), "s"(x) : "memory");
#pragma unroll
    for (int k = 0; k < 4; ++k) { wv[k] = wa[k]; xv[k] = xr[k]; xv[4 + k] = xs[k]; }
  };
  auto ln1 = [&](int g, int o, uint32_t c0) {
    uint32_t uo = u_at(g, 64 * wl + lane);
    uint32_t ta = lb0 + (uint32_t)(o + ((lane >> 2) & 3) * RP + (64 * wl + 16 * kq + 4 * (lane & 3)) * 2);
    uint32_t la = (uint32_t)(o + 2 * wl * RP + 16 * lane), lt = (uint32_t)(o + (2 * wl + (lane >> 5)) * RP + 1024 + 4 * (lane & 31));
    uint32_t sg = O_SU + (uint32_t)(256 * (lane >> 3) + g) * 128u + 16u * (lane & 7);
    asm volatile("" : "+v"(uo), "+v"(ta), "+v"(la), "+v"(lt), "+v"(sg));
    f32x4 z4 = {0.f, 0.f, 0.f, 0.f};
    asm volatile("" : "+v"(z4));
    asm volatile("s_waitcnt vmcnt(0)" : "={v[100:103]}"(xa), "={v[104:107]}"(xc), "={v108}"(r0), "={v109}"(ep));
    TS(0, 1);
    const uint32_t v[9] = {xa[0], xa[1], xa[2], xa[3], xc[0], xc[1], xc[2], xc[3], r0};
    uint32_t c[9];
    f32x2 tf[9];
    T = ((uint32_t)__builtin_amdgcn_readfirstlane(ep) + 1u) * 0x9E3779B1u;
    if (wl == 3) { const uint32_t now = (uint32_t)__builtin_amdgcn_s_memrealtime() + SD; ST16(sg, ((u32x4){T, now, T, now})); }
    const uint32_t tj = ((T >> (3 * wl)) & 0x00070007u) | (((T >> (12 + wl)) & 0x00010001u) << 14);
    uint32_t tw[3] = {(tj & 0xffffu) * 0x10001u, (tj >> 16) * 0x10001u, 0u};
    tw[2] = (lane < 32) ? tw[0] : tw[1];
    asm volatile("" : "+v"(tw[0]), "+v"(tw[1]), "+v"(tw[2]));
    f32x2 rc, nc;
    float e_;
    ln_stats<false>(v, lane, 0.03125f, tf, z4, rc, nc, e_);
    ln_apply<true>(tf, lane, rc, nc, c);
#pragma unroll
    for (int k = 0; k < 9; ++k) c[k] = (c[k] & ~UMASK) | tw[k >> 2];
    *(u32x4*)(smem + la) = (u32x4){c[0], c[1], c[2], c[3]};
    *(u32x4*)(smem + la + RP) = (u32x4){c[4], c[5], c[6], c[7]};
    *(uint32_t*)(smem + lt) = c[8];
    u32x4 ha, hb;
    {
      uint32_t t, cc, n = SPIN_CAP;
      asm volatile("s_mov_b64 exec, 1\n\tv_mov_b32 %2, 1\n\tds_add_u32 %5, %2\n\ts_mov_b64 exec, -1\n"
                   "1:\n\tds_read_b32 %2, %5\n\tds_read_b64_tr_b16 v[40:41], %6\n\tds_read_b64_tr_b16 v[42:43], %6 offset:%7\n\tds_read_b64_tr_b16 v[44:45], %6 offset:512\n\t"
                   "ds_read_b64_tr_b16 v[46:47], %6 offset:%8\n\ts_sub_u32 %4, %4, 1\n\ts_cbranch_scc1 2f\n\ts_waitcnt lgkmcnt(4)\n\tv_readfirstlane_b32 %3, %2\n\t"
                   "s_cmp_lt_u32 %3, 4\n\ts_cbranch_scc1 1b\n2:\n\ts_waitcnt lgkmcnt(0)"
                   : "=&{v[40:43]}"(ha), "=&{v[44:47]}"(hb), "=&v"(t), "=&s"(cc), "+s"(n) : "v"(c0), "v"(ta), "n"(4 * RP), "n"(512 + 4 * RP) : "memory", "scc");
    }
    ST16(uo, ha);
    ST16(uo + 64 * 128, hb);
    if (wl == 0) {
      const u32x2 h4 = trd(ta + 1024), h5 = trd(ta + 1024 + 4 * RP);
      ST16(uo + 128 * 128, ((u32x4){h4[0], h4[1], h5[0], h5[1]}));
    }
  };
  const int dX = 512 - 24 + 8 * bx + bj;
  if constexpr (ROLE == 2) {
    const int tl = tid - 256;
    if (XOFF && (uint32_t)(bj - 4) < 8u) {
      __builtin_amdgcn_s_setprio(3);
      ln1(tg + 160, 10240, ca + 32);
      TS(0, 2);
    }
    if (!isx && T0) {
      SLEEP(T0SLEEP);
      if (wl < T0) TOUCH(w_line(24 * wl + bj));
      if (T1) { asm volatile("s_waitcnt vmcnt(0)" ::: "memory"); SLEEP(T1SLEEP); if (wl < T1) TOUCH(w_line(24 * (T0 + wl) + bj)); }
    }
    if (isx) {
      __builtin_amdgcn_s_setprio(QPRIO0);
      *(u32x4*)(smem + X_TB + 16 * tl) = zero4;
      *(u32x4*)(smem + X_TB + 16 * min(256 + tl, (TB + C_U) / 16 - 1)) = zero4;
      const int pr = (wl + XROT) & 3;
      COps e;
      conv_ops(lb0 + X_TB, lb0 + X_UL, lb0 + X_IM, lane, e);
      TOps t;
      table_ops(lb0 + X_IM, lb0 + X_TB, wl & 1, lane, t);
      e.o = lb0 + X_ST + (uint32_t)(16 * ((2 * i + (kq >> 1)) ^ ((i >> 2) & 1)) + 8 * (kq & 1));
      e.f = ca + 28 - 32;
      asm volatile("" : "+v"(e.o), "+v"(e.f));
      float ldt, are, aim, bre, bim, cre, cim, dsk;
      P_WAIT(0, , "v103", "v104", "v105", "v106", "v107", "v109");
      asm volatile("" : "={v109}"(ep));
      T = ((uint32_t)__builtin_amdgcn_readfirstlane(ep) + 1u) * 0x9E3779B1u;
      P_MORE();
      asm volatile("v_lshlrev_b32 %0, 16, v107" : "=v"(dsk) : : "v107");
      TS(0, 1);
      __builtin_amdgcn_s_setprio(QPRIO);
      int* evp = (int*)(sel + 44);
      char* im = smem + X_IM + IP * (tl & 127);
      evp[wl] = (wl < 2) ? gen_y(ldt, are, aim, bre, bim, cre, cim, lb0 + X_IM + IP * tl, [] {} TSA) : gen_x(ldt, are, aim, im);
      TS(0, 2);
      __builtin_amdgcn_s_setprio(QPRIO2);
      gsync(ca + 8, 4);
      if (wl < 2) evp[-4 + wl] = (int)table_tile(t, wl & 1, lane, lb0 + LM + 4 * (128 + 44), dsk TSA);
      if (wl < 2) gsync0(ca + 8, 8); else { count_up(ca + 8); SLEEP(XSL); gwait(ca + 8, 8); }
      const uint32_t cs = (uint32_t)__builtin_amdgcn_readfirstlane(evp[-4]);
      if (wl == 2 && lane == 0) asm volatile("global_store_dwordx2 %0, %1, %2 sc1" : : "v"(O_SC + 16u * dX), "v"((u32x2){T, cs}), "s"(ws) : "memory");
      if (wl == 3) *(u32x4*)(smem + X_IM + pad_at(lane & 7)) = zero4;
      TS(0, 3);
      CONV_RUN(2 + pr, e, , "+{v11}"(tid2));
      {
        const int lane = tid2 & 63;
        const u32x4 v = *(const u32x4*)(smem + X_ST + 2048 * pr + 1024 * (lane >> 5) + 16 * ((lane & 31) ^ ((lane >> 3) & 1)));
        if (__builtin_expect(v_put(v, (u32x4){utag(T, 0), utag(T, 1), utag(T, 2), utag(T, 3)}, cs != S16, v_at(dX, 64 * pr + lane)), 0)) {
          DRAIN();
          if (lane == 0) ST4(O_DN + 16u * (4 * (dX - 256) + pr), T);
        }
      }
      TS(0, 6);
    }
    __builtin_amdgcn_s_setprio(PPRIO);
    asm volatile("" : "+v"(tid2));
    if (isx) {
      const int lane = tid2 & 63;
      const u32x4 ra = *(const u32x4*)(smem + X_RW(wl) + 16 * lane), rb = *(const u32x4*)(smem + X_RW(wl) + 1024 + 16 * lane), rw = *(const u32x4*)(smem + X_WR + 16 * lane);
      const u32x2 rc = *(const u32x2*)(smem + X_RW(wl) + 2048 + 8 * lane);
#pragma unroll
      for (int k = 0; k < 4; ++k) { xv[k] = ra[k]; xv[4 + k] = rb[k]; wv[k] = rw[k]; }
      xv[8] = rc[0]; wv[4] = rc[1];
    } else rows_xw(tid2 & 63);
    if (!isx && !ffn) {
      SLEEP(RSLEEP);
#pragma unroll
      for (int k = 0; k < 7; ++k) ST16(O_SL + (uint32_t)((6 * bx + bj - 18) * 1792 + 256 * k - 256 + tid2) * 16u, ones);
      DRAIN();
    }
    if (!isx) BAR();
    const uint32_t tp = (uint32_t)__builtin_amdgcn_readfirstlane(cnt[4]);
    if (!isx) T = (uint32_t)__builtin_amdgcn_readfirstlane(sel[43]);
    tv = tp + UD + VD2 - vearly;
  } else {
    __builtin_amdgcn_s_setprio(3);
    if (tid < 12) cnt[tid] = 0u;
    BAR();
    if (tid < 20) ((uint32_t*)(hm + 320))[tid] = 0u;
    if (tid >= 184) *(u32x4*)(smem + L_SC - 16 * 184 + 16 * tid) = (u32x4){0x41804180u, 0x41804180u, 0x41804180u, 0x41804180u};
    uint32_t sink = 0;
    if (XOFF && isx) {
      asm volatile("s_waitcnt vmcnt(0)" : "={v109}"(ep));
      T = ((uint32_t)__builtin_amdgcn_readfirstlane(ep) + 1u) * 0x9E3779B1u;
    } else ln1(tg, 0, ca + 12);
    TS(0, 2);
    __builtin_amdgcn_s_setprio(NPRIO);
    if (!isx && 3 - w < TB1) TOUCH(w_line(24 * (3 - w) + 23 - bj));
    asm volatile("s_waitcnt vmcnt(0)" : "+v"(sink) : : "memory");
    TS(0, 13);
    if (w == 0) {
      const uint32_t xr = O_XC + (uint32_t)((511 - 2 * (g0 + w)) * 144 + lane) * 16u;
      ST16(xr, ones);
      ST16(xr + 1024u, ones);
      if (lane < 16) ST16(xr + 2048u, ones);
    }
    if (w == 2 && b < 72 && lane < 8) ST16(O_FB + (uint32_t)(8 * b + lane) * 16u, ones);
    if (isx) {
      rows_xw(lane);
      char* sp = smem + X_RW(w) + 16 * lane;
      *(u32x4*)sp = (u32x4){xv[0], xv[1], xv[2], xv[3]};
      *(u32x4*)(sp + 1024) = (u32x4){xv[4], xv[5], xv[6], xv[7]};
      *(u32x2*)(smem + X_RW(w) + 2048 + 8 * lane) = (u32x2){xv[8], wv[4]};
      if (w == 0) *(u32x4*)(smem + X_WR + 16 * lane) = (u32x4){wv[0], wv[1], wv[2], wv[3]};
    }
    SLEEP(ESLEEP);
    uint32_t tu = latest(gate(ws, O_SU + (uint32_t)(256 * bx + 64 * w + 4 * i + (bj & 3)) * 128u, T));
    asm volatile("s_mov_b64 exec, 1\n\tds_max_u32 %0, %1\n\ts_mov_b64 exec, -1" : : "v"(ca + 16), "v"(tu) : "memory");
    if (w == 0) sel[43] = T;
    if (isx) {
      const uint32_t xo = u_at(tid, dX);
      char* sm = smem + X_UL + C_U - 16 + 16 * tid;
      const u32x4 ut = {utag(T, 0), utag(T, 1), utag(T, 2), utag(T, 3)};
      __builtin_amdgcn_s_setprio(3);
      until(tu + UD + XD);
      u32x4 uw;
      asm volatile("global_load_dwordx4 %0, %1, %2 sc1" : "=&v"(uw) : "v"(xo), "s"(ws) : "memory");
      asm volatile("s_waitcnt vmcnt(0)" : "+v"(uw));
      uw ^= ut;
      u_again(uw, ut, xo);
      *(u32x4*)(sm + 16) = uw;
      if (XF) { gsync0(ca + 12, XOFF ? 4 : 8); gwait(ca + 8, 8); } else BAR();
      if (w < 3) {
        const u32x4 pw = *(const u32x4*)sm;
        if (w < 2) sums<true>(smem + X_IM + 16 * tid, pw, uw); else sums<false>(smem + X_IM + 16 * tid, pw, uw);
        count_up(ca + 28);
      }
      if (XF) BAR();
    } else {
      until(tu + UD + WD);
      BAR();
    }
    tu = (uint32_t)__builtin_amdgcn_readfirstlane(cnt[4]);
    TS(0, 3);
    __builtin_amdgcn_s_setprio(DPRIO);
    if (ffn) {
      ts1 = (tu + UD + VD2 - TSTOP) | 1u;
      gu_dma(0, N0, IC<40>{});
      gu_dma(N0, NA, IC<WDEPTH>{});
      asm volatile("s_waitcnt vmcnt(%0)" : : "n"(NA - 9) : "memory");
      gsync0(ca + 4, 4);
      gu_read(0);
      asm volatile("s_waitcnt lgkmcnt(0)" ::: "memory");
      count_up(ca + 36);
      TS(0, 4);
      gu_dma(NA, 18, IC<WDEPTH2>{});
      gwait(ca + 36, 4);
      down_dma(0, KN, lane);
      asm volatile("s_waitcnt vmcnt(%0)" : : "n"(3 * KN) : "memory");
      count_up(ca + 4);
      TS(0, 7);
    } else {
      if (!isx && C3) {
        const int n = 6 * min(tid, 191) + bj - 18, r = n >> 2;
        const char* wb = (const char*)((n & 1) ? wu : wg) + bx * 1152;
        const int c0 = (3 - (int)((uintptr_t)wb >> 7)) & 7;
        wb += (uint32_t)((r + 144 * ((r >= 144) + ((n >> 1) & 1))) * (F_ * 2) + 128 * c0);
        TOUCH(wb);
        if (c0 == 0) TOUCH(wb + 1024);
      }
      auto d_line = [&](int k) {
        const int n = min((k * 14 + bj - 18) * 64 + lane, 5183), q = n / 1296, r = n - 1296 * q;
        return (const char*)wd + (uint32_t)(bx * 5184 + (r / 18) * 72 + 18 * (HREV ? 3 - q : q) + r % 18) * 128u;
      };
      int k = w;
      if (w < HW) {
#pragma unroll 1
        for (; k < 6 && (int)((uint32_t)__builtin_amdgcn_s_memrealtime() - (tu + UD + VD2 - HSTOP)) < 0; k += HW) { TOUCH(d_line(k)); DRAIN(); }
      }
      TS(0, 4);
      until(tu + UD + VD2 + RD2);
      if (RW) gwait(ca, 4);
      if (w < HW) {
#pragma unroll 1
        for (; k < 6; k += HW) { TOUCH(d_line(k)); DRAIN(); }
      }
      TS(0, 7);
    }
    tv = tu + UD + VD2 - vearly;
    tk = tu + UD + VD2 + KD;
  }
  {
  const int tid = tid2, lane = tid & 63, i = lane & 15, kq = lane >> 4;
  __builtin_amdgcn_s_setprio(3);
  const int PL = ffn ? 6400 : LW + 6656;
  f32x2 xf[9];
  f32x4 za = {0.f, 0.f, 0.f, 0.f}, zb = za;
  uint32_t al = O_XC + 64u * (i >> 3) + 16u * kq + 128u * w;
  uint32_t ha = (ROLE == 1) ? (uint32_t)((4 * (kq & 1) + 2 * (kq >> 1) + (i >> 3)) * 80 + 16 * w + 2 * (i & 7)) : 0u;
  uint32_t h0a = (uint32_t)(min(i, 8) * 80 + 16 * kq), h1a = (uint32_t)(min(i ^ 8, 8) * 80 + 16 * kq);
  uint32_t so = O_SL + (uint32_t)fb * SP + ((ROLE == 2) ? 16u * tid - 4096u : 8192u + 16u * lane);
  uint32_t da = lb0 + (uint32_t)((8 * kq + ((lane >> 2) & 3)) * 1152 + 32 * ((8 * kq + ((lane >> 2) & 3) + 2) >> 2) + 8 * (lane & 3) + ((ROLE == 2) ? 128 * wl : 1024));
  if (ROLE == 2) asm volatile("" : "+v"(h0a), "+v"(h1a), "+v"(so), "+v"(da)); else asm volatile("" : "+v"(ha), "+v"(al), "+v"(da));
  uint32_t hv[9] = {}, nv[9] = {}, keya = 0u, keyb = 0u;
  int rka = 0, rkb = 0;
  auto put_row = [&](const uint32_t* r, int j, const void* base, uint32_t ro) {
    const u32x4 tl = {r[8], (uint32_t)DPP_I((int)r[8], 0x55, 0xf), (uint32_t)DPP_I((int)r[8], 0xaa, 0xf), (uint32_t)DPP_I((int)r[8], 0xff, 0xf)};
    asm volatile("global_store_dwordx4 %0, %1, %2 sc1\n\ts_nop 1" : : "v"(ro + 16u * lane), "v"((u32x4){r[4 * j], r[4 * j + 1], r[4 * j + 2], r[4 * j + 3]}), "s"(base) : "memory");
    if ((lane & 35) == 32 * j) asm volatile("global_store_dwordx4 %0, %1, %2 offset:1024 sc1\n\ts_nop 1" : : "v"(ro + 4u * (lane & 31)), "v"(tl), "s"(base) : "memory");
  };
  if (ln2) {
    const int po = PL - 2560 * (ROLE == 1);
    uint32_t pa = (uint32_t)(po + 9216 * wl + 32 * lane), pb = (uint32_t)(po + 9216 * wl + 2048 + 8 * (lane & 31)), ps = (lane < 32) ? 0u : 16u;
    uint32_t kid = (uint32_t)(((255 - g2) << 5) | (7 - 2 * wl - (lane >> 5)));
    const int tl = tid - 256, t2 = tl + 128, d1 = 64 * (tl / 48) + tl % 48, d3 = 64 * (t2 / 48) + t2 % 48;
    const int d2 = (tl < 128) ? 64 * (tl >> 4) + 48 + (tl & 15) : min(384 + tl, 575);
    uint32_t vo = v_at(d1, g0), vo2 = v_at(d2, g0), vo3 = v_at(d3, g0), px = (uint32_t)(PL + 4 * d1), py = (uint32_t)(PL + 4 * d2), pz = (uint32_t)(PL + 4 * d3);
#pragma unroll
    for (int k = 0; k < 9; ++k) xf[k] = (f32x2){bflo(xv[k]), bfhi(xv[k])};
    asm volatile("" : "+v"(xf[0]), "+v"(xf[1]), "+v"(xf[2]), "+v"(xf[3]), "+v"(xf[4]), "+v"(xf[5]), "+v"(xf[6]), "+v"(xf[7]), "+v"(xf[8]));
    asm volatile("" : "+v"(za), "+v"(zb), "+v"(pa), "+v"(pb), "+v"(ps), "+v"(kid), "+v"(vo), "+v"(vo2), "+v"(vo3), "+v"(px), "+v"(py), "+v"(pz));
    u32x4 ut = {utag(T, 0), utag(T, 1), utag(T, 2), utag(T, 3)};
    float sv[10], ws_ = dot2(wv[4], 0x3f803f80u, 0.f) * 0.5f;
#pragma unroll
    for (int k = 0; k < 4; ++k) ws_ = dot2(wv[k], 0x3f803f80u, ws_);
    ws_ = wave_sum(ws_);
#pragma unroll
    for (int k = 0; k < 10; ++k) sv[k] = 16.f;
    uint32_t ka_ = lb0 + LM + 4u * (128 + 48 + 2 * wl + (lane >> 5)), kb_ = lb0 + LM + 4u * (128 + 48);
    uint32_t ko_ = O_KEY + (uint32_t)(256 * (lane & 7) + 8 * (g2 & 31) + (g2 >> 5)) * 16u;
    asm volatile("" : "+v"(ut), "+v"(ws_), "+v"(sv[0]), "+v"(sv[1]), "+v"(sv[2]), "+v"(sv[3]), "+v"(sv[4]), "+v"(sv[5]), "+v"(sv[6]), "+v"(sv[7]), "+v"(sv[8]), "+v"(sv[9]),
                 "+v"(ka_), "+v"(kb_), "+v"(ko_));
    if (ffn) gwait(ca + 36, 4);
    TS(0, 7);
    until(tv);
    TS(0, 8);
    u32x4 va, vc = ut, vb = ut;
    asm volatile("global_load_dwordx4 %0, %2, %4 sc1\n\ts_cmp_lt_u32 %5, 2\n\ts_cbranch_scc1 1f\n\tglobal_load_dwordx4 %1, %3, %4 sc1\n1:"
                 : "=&v"(va), "+v"(vc) : "v"(vo), "v"(vo3), "s"(ws), "s"(wl) : "memory", "scc");
    until(tv + V2D);
    asm volatile("s_cmp_eq_u32 %5, 3\n\ts_cbranch_scc1 1f\n\tglobal_load_dwordx4 %2, %3, %4 sc1\n\ts_waitcnt vmcnt(1)\n\ts_branch 2f\n1:\n\ts_waitcnt vmcnt(0)\n2:"
                 : "+v"(va), "+v"(vc), "+v"(vb) : "v"(vo2), "s"(ws), "s"(wl) : "memory", "scc");
    auto turn1 = [&] {
#pragma unroll
      for (int e = 0; e < 4; ++e) *(uint32_t*)(smem + px + 9216 * e) = va[e];
      if (wl >= 2) {
#pragma unroll
        for (int e = 0; e < 4; ++e) *(uint32_t*)(smem + pz + 9216 * e) = vc[e];
      }
    };
    auto turn = [&] {
      if (wl < 3) {
#pragma unroll
        for (int e = 0; e < 4; ++e) *(uint32_t*)(smem + py + 9216 * e) = vb[e];
      }
    };
    va ^= ut; vc ^= ut;
    turn1();
    asm volatile("s_waitcnt vmcnt(0)" : "+v"(vb) : : "memory");
    vb ^= ut;
    turn();
    const u32x4 vs = va | vb | vc;
    if (__builtin_expect(__any(((vs[0] | vs[1] | vs[2] | vs[3]) & UMASK) != 0u), 0)) {
#pragma unroll 1
      for (int spin = 0; spin < 4; ++spin) {
        const bool na = ((va[0] | va[1] | va[2] | va[3]) & UMASK) != 0u, nb = ((vb[0] | vb[1] | vb[2] | vb[3]) & UMASK) != 0u, nc = ((vc[0] | vc[1] | vc[2] | vc[3]) & UMASK) != 0u;
        if (!__any(na || nb || nc)) break;
        TSC(16);
        if (na) LD16(va, vo);
        if (nb) LD16(vb, vo2);
        if (nc) LD16(vc, vo3);
        asm volatile("s_waitcnt vmcnt(0)" : "+v"(va), "+v"(vb), "+v"(vc) : : "memory");
        if (na) va ^= ut;
        if (nb) vb ^= ut;
        if (nc) vc ^= ut;
      }
      auto again = [&](u32x4& v, uint32_t o, int d) {
        const int J = g0 >> 5;
        const uint32_t dn = O_DN + 16u * (uint32_t)((d < 512) ? 4 * (d >> 1) + ((0x01013232 >> (4 * J)) & 3) : 4 * (d - 256) + (J >> 1));
        bool pend = ((v[0] | v[1] | v[2] | v[3]) & UMASK) != 0u;
#pragma unroll 1
        for (int spin = 0; spin < SPIN_CAP && __any(pend); ++spin) {
          TSC(16);
          SLEEP(PSLEEP);
          if (pend) {
            uint32_t fin = ~T;
            if ((v[0] & v[1] & v[2] & v[3] & LMASK) == LMASK)
              asm volatile("global_load_dword %0, %1, %2 sc1\n\ts_waitcnt vmcnt(0)" : "=&v"(fin) : "v"(dn), "s"(ws) : "memory");
            LD16W(v, o);
            v ^= ut;
            pend = ((v[0] | v[1] | v[2] | v[3]) & UMASK) != 0u;
            if (pend && fin == T) {
              u32x2 sc;
              for (int k = 0; k < SPIN_CAP; ++k) {
                asm volatile("global_load_dwordx2 %0, %1, %2 sc1\n\ts_waitcnt vmcnt(0)" : "=&v"(sc) : "v"(O_SC + 16u * d), "s"(ws) : "memory");
                if (sc[0] == T) break;
              }
              *(u16*)(smem + L_SC + 2 * d) = (u16)(sc[1] >> 16);
              cnt[10] = 1u;
              v = (v ^ ut) & ~LMASK;
              pend = false;
            }
          }
        }
      };
      again(va, vo, d1); again(vc, vo3, d3); again(vb, vo2, d2);
      turn1();
      turn();
    }
    u32x4 a0, a1;
    u32x2 a2, cf;
    {
      uint32_t cc, n = SPIN_CAP;
      asm volatile("s_mov_b64 exec, 1\n\tv_mov_b32 v90, 1\n\tds_add_u32 %6, v90\n\ts_mov_b64 exec, -1\n"
                   "1:\n\tds_read2_b32 %3, %6 offset1:10\n\tds_read_b128 %0, %7\n\tds_read_b128 %1, %7 offset:16\n\tds_read_b64 %2, %8\n\t"
                   "s_sub_u32 %5, %5, 1\n\ts_cbranch_scc1 2f\n\ts_waitcnt lgkmcnt(3)\n\tv_readfirstlane_b32 %4, v90\n\t"
                   "s_cmp_lt_u32 %4, 4\n\ts_cbranch_scc1 1b\n2:\n\ts_waitcnt lgkmcnt(0)"
                   : "=&v"(a0), "=&v"(a1), "=&v"(a2), "=&{v[90:91]}"(cf), "=&s"(cc), "+s"(n) : "v"(ca), "v"(lb0 + pa), "v"(lb0 + pb) : "memory", "scc");
    }
    TS(0, 9);
    if (__builtin_expect(__builtin_amdgcn_readfirstlane(cf[1]) != 0, 0)) {
      const u32x4 q = *(const u32x4*)(smem + L_SC + 16 * lane);
      const uint32_t r = *(const uint32_t*)(smem + L_SC + 1024 + 4 * (lane & 31));
#pragma unroll
      for (int k = 0; k < 4; ++k) { sv[2 * k] = bflo(q[k]); sv[2 * k + 1] = bfhi(q[k]); }
      sv[8] = bflo(r); sv[9] = bfhi(r);
    }
    const uint32_t av[10] = {a0[0], a0[1], a0[2], a0[3], a1[0], a1[1], a1[2], a1[3], a2[0] >> ps, a2[1] >> ps};
    f32x2 h[9];
#pragma unroll
    for (int k = 0; k < 10; ++k) {
      asm("v_fma_mix_f32 %0, %1, %2, %3 op_sel_hi:[1,0,0]" : "=v"(h[(k < 8) ? k >> 1 : 8][k & 1]) : "v"(av[k]), "v"(sv[k]), "v"(xf[(k < 8) ? k >> 1 : 8][k & 1]));
      if (k < 8) asm("v_fma_mix_f32 %0, %1, %2, %3 op_sel:[1,0,0] op_sel_hi:[1,0,0]" : "=v"(h[4 + (k >> 1)][k & 1]) : "v"(av[k]), "v"(sv[k]), "v"(xf[4 + (k >> 1)][k & 1]));
    }
#pragma unroll
    for (int k = 0; k < 9; ++k) hv[k] = pack2(h[k][0], h[k][1]);
    const float da = dot2(hv[2], wv[2], dot2(hv[0], wv[0], zb[0])) + dot2(hv[3], wv[3], dot2(hv[1], wv[1], zb[1]));
    const float db = dot2(hv[6], wv[2], dot2(hv[4], wv[0], zb[2])) + dot2(hv[7], wv[3], dot2(hv[5], wv[1], zb[3]));
    float dt = dot2(hv[8], wv[4], fold32(da, db));
    f32x2 rc, nc;
    ln_stats<true>(hv, lane, 1.f, xf, za, rc, nc, dt);
    dt = (lane < 32) ? dt * rc[0] + ws_ * nc[0] : dt * rc[1] + ws_ * nc[1];
    const uint32_t bb = pack2(dt, dt) & 0xffff0000u;
    const uint32_t kk = (bb ^ (((uint32_t)((int)bb >> 31) & 0x7fff0000u) | 0x80000000u)) | kid;
    u32x4 ka, kb;
    uint32_t old;
    asm volatile("s_mov_b64 exec, %5\n\tds_write_b32 %3, %4\n\ts_mov_b64 exec, 1\n\tv_mov_b32 %2, 1\n\tds_add_rtn_u32 %2, %7, %2\n\ts_mov_b64 exec, -1\n\t"
                 "ds_read_b128 %0, %6\n\tds_read_b128 %1, %6 offset:16\n\ts_waitcnt lgkmcnt(0)"
                 : "=&v"(ka), "=&v"(kb), "=&v"(old) : "v"(ka_), "v"(kk), "s"(0x8000000080000000ull), "v"(kb_), "v"(ca) : "memory");
    if (__builtin_amdgcn_readfirstlane(old) == 7) {
      uint32_t m1 = max(max(ka[0], ka[1]), ka[2]), m2 = med3u(ka[0], ka[1], ka[2]), m3 = min(min(ka[0], ka[1]), ka[2]);
#pragma unroll
      for (int e = 0; e < 5; ++e) {
        const uint32_t kx = e ? kb[e - 1] : ka[3];
        m3 = med3u(m2, m3, kx); m2 = med3u(m1, m2, kx); m1 = max(m1, kx);
      }
      if (lane < 8) ST16(ko_, ((u32x4){m1 | 24u, m2 | 16u, m3 | 8u, T & ~1u}));
    }
    TS(0, 10);
    keya = (uint32_t)__builtin_amdgcn_readlane(kk, 31); keyb = (uint32_t)__builtin_amdgcn_readlane(kk, 63);
    ln_apply<false>(xf, lane, rc, nc, nv);
    gwait(ca, 8);
    if (wl == 1) {
      const u32x4 kv = *(const u32x4*)&kl[4 * (lane & 1)];
      if (lane < 2) ST16(O_KF + (uint32_t)(2 * g2 + lane) * 16u, ((u32x4){__builtin_amdgcn_perm(kv[1], kv[0], 0x07060302u), T, __builtin_amdgcn_perm(kv[3], kv[2], 0x07060302u), T}));
      if (ROLE == 2 && b == 0 && lane == 0) ST4(O_EP, T * 0x0E8B2F51u);
    }
    const uint32_t ko = kl[lane & 7];
    rka = FORCEFB + __popcll(__ballot(ko > keya) & 0xffull); rkb = FORCEFB + __popcll(__ballot(ko > keyb) & 0xffull);
#pragma unroll
    for (int k = 0; k < 9; ++k) {
      const f32x2 o = xf[k] + (f32x2){bflo(nv[k]), bfhi(nv[k])};
      hv[k] = pack2(o[0], o[1]);
      nv[k] = min(nv[k], 0xfffffffeu);
    }
    if (rka < 2) put_row(nv, 0, ws, O_XC + (uint32_t)(1023 - 4 * g2 - rka) * 1152u);
    if (rkb < 2) put_row(nv, 1, ws, O_XC + (uint32_t)(1023 - 4 * g2 - rkb) * 1152u);
    if (!LPOS && ffn) down_dma(KN, 4, lane);
  }
  uint32_t k3rd = 0u, k4th = 0u;
  if constexpr (ROLE == 1) {
    u32x4 key;
    const uint32_t kof = O_KEY + (uint32_t)(256 * bx + tid) * 16u;
    until(tk);
    if (!ffn) SLEEP(NSLEEP);
    LD16(key, kof);
    if (ffn) {
      gwait0(ca + 4, 8);
      gu_read(1);
    }
    asm volatile("s_waitcnt vmcnt(0)" : "+v"(key) : : "memory");
    if (__builtin_expect(__any(key[3] != (T & ~1u)), 0)) {
#pragma unroll 1
      for (int spin = 0; spin < SPIN_CAP; ++spin) {
        const bool need = key[3] != (T & ~1u);
        if (!__any(need)) break;
        TSC(15);
        SLEEP(KBACK);
        if (need) LD16W(key, kof);
      }
    }
    TS(0, 11);
    const uint32_t m1 = oct_maxu(key[0]);
    const bool hit = key[0] == m1;
    const uint32_t k0 = hit ? key[1] : key[0], k1 = hit ? key[2] : key[1];
    const uint32_t m2 = oct_maxu(k0);
    if ((lane & 7) == 0) *(u32x2*)&cand[tid >> 2] = (u32x2){m1, m2};
    k3rd = (k0 == m2) ? k1 : k0;
    k4th = (k0 == m2) ? (hit ? 0u : key[2]) : k1;
    if (RPF == 1 && !ffn && w == (bj & 3)) {
      const uint32_t r1 = O_XC + ((m1 >> 3) & 0x3ffu) * 1152u + 128u * (lane & 7), r2 = O_XC + ((m2 >> 3) & 0x3ffu) * 1152u + 128u * (lane & 7);
      asm volatile("global_load_dword v126, %0, %3 sc1\n\tglobal_load_dword v126, %1, %3 sc1\n\tglobal_load_dword v126, %2, %3 offset:1024 sc1"
                   : : "v"(r1), "v"(r2), "v"(((lane & 1) ? r1 : r2) - 128u * (lane & 7)), "s"(ws) : "memory", "v126");
    }
  }
  auto pick = [&](const uint32_t* cd, uint32_t* sl, bool pf = false) {
    BAR();
    if (RPF == 2 && ROLE == 2 && pf && ffn && w == 7) {
      const uint32_t r1 = O_XC + ((cd[16 * (bj & 3) + (lane >> 2)] >> 3) & 0x3ffu) * 1152u + 128u * (lane & 3);
      asm volatile("global_load_dword v126, %0, %1 sc1\n\tglobal_load_dword v126, %0, %1 offset:512 sc1\n\tglobal_load_dword v126, %0, %1 offset:640 sc1"
                   : : "v"(r1), "s"(ws) : "memory", "v126");
    }
    const uint32_t c = cd[lane], own = cd[8 * w + (lane & 7)];
    const u32x4 la = *(const u32x4*)&cd[8 * w], lb = *(const u32x4*)&cd[8 * w + 4];
    uint32_t r = C_;
    asm("v_cmp_gt_u32 s[40:41], %1, %2\n\tv_cmp_gt_u32 s[42:43], %1, %3\n\tv_cmp_gt_u32 s[44:45], %1, %4\n\tv_cmp_gt_u32 s[46:47], %1, %5\n\t"
        "v_cmp_gt_u32 s[48:49], %1, %6\n\tv_cmp_gt_u32 s[50:51], %1, %7\n\tv_cmp_gt_u32 s[52:53], %1, %8\n\tv_cmp_gt_u32 s[54:55], %1, %9\n\t"
        "s_bcnt1_i32_b64 s40, s[40:41]\n\ts_bcnt1_i32_b64 s42, s[42:43]\n\tv_writelane_b32 %0, s40, 0\n\ts_bcnt1_i32_b64 s44, s[44:45]\n\t"
        "v_writelane_b32 %0, s42, 1\n\ts_bcnt1_i32_b64 s46, s[46:47]\n\tv_writelane_b32 %0, s44, 2\n\ts_bcnt1_i32_b64 s48, s[48:49]\n\t"
        "v_writelane_b32 %0, s46, 3\n\ts_bcnt1_i32_b64 s50, s[50:51]\n\tv_writelane_b32 %0, s48, 4\n\ts_bcnt1_i32_b64 s52, s[52:53]\n\t"
        "v_writelane_b32 %0, s50, 5\n\ts_bcnt1_i32_b64 s54, s[54:55]\n\tv_writelane_b32 %0, s52, 6\n\tv_writelane_b32 %0, s54, 7"
        : "+v"(r) : "v"(c), "v"(la[0]), "v"(la[1]), "v"(la[2]), "v"(la[3]), "v"(lb[0]), "v"(lb[1]), "v"(lb[2]), "v"(lb[3])
        : "scc", "s40", "s41", "s42", "s43", "s44", "s45", "s46", "s47", "s48", "s49", "s50", "s51", "s52", "s53", "s54", "s55");
    sl[min(r, (uint32_t)C_)] = own;
    BAR();
  };
  auto rows = [&](uint32_t ao, auto&& meanwhile) {
    u32x4 aa, ab, ac = __builtin_bit_cast(u32x4, zb);
    asm volatile("global_load_dwordx4 %0, %3, %4 sc1\n\tglobal_load_dwordx4 %1, %3, %4 offset:512 sc1\n\t"
                 "s_cmp_lg_u32 %5, 0\n\ts_cbranch_scc1 1f\n\tglobal_load_dwordx4 %2, %3, %4 offset:1024 sc1\n1:"
                 : "=&v"(aa), "=&v"(ab), "+v"(ac) : "v"(ao), "s"(ws), "s"(w) : "memory", "scc");
    __builtin_amdgcn_sched_barrier(0);
    meanwhile();
    asm volatile("s_waitcnt vmcnt(0)" : "+v"(aa), "+v"(ab), "+v"(ac) : : "memory");
    if (__builtin_expect(__any(max(max(max4(aa), max4(ab)), max4(ac)) == 0xffffffffu), 0)) {
#pragma unroll 1
      for (int spin = 0; spin < SPIN_CAP; ++spin) {
        const bool na = max4(aa) == 0xffffffffu, nb = max4(ab) == 0xffffffffu, nc = max4(ac) == 0xffffffffu;
        if (!__any(na || nb || nc)) break;
        TSC(14);
        SLEEP(PSLEEP);
        if (na) LD16W(aa, ao);
        if (nb) LD16W(ab, ao + 512u);
        if (nc) LD16W(ac, ao + 1024u);
      }
    }
    *(u32x4*)(smem + LA + 16 * tid) = aa;
    *(u32x4*)(smem + LA + 4096 + 16 * tid) = ab;
    if (w == 0) *(u32x4*)(smem + LA + 8192 + 16 * tid) = ac;
  };
  auto rows_dma = [&](uint32_t ao) {
    uint32_t dl = lb0 + LA + 1024u * w, keep;
    asm volatile("s_mov_b32 %0, m0\n\ts_mov_b32 m0, %1\n\ts_add_u32 %1, %1, 4096 - 512\n\tglobal_load_lds_dwordx4 %2, %3 sc1\n\ts_mov_b32 m0, %1\n\ts_add_u32 %1, %1, 4096 - 512\n\t"
                 "global_load_lds_dwordx4 %2, %3 offset:512 sc1\n\ts_cmp_lg_u32 %4, 0\n\ts_cbranch_scc1 1f\n\ts_mov_b32 m0, %1\n\ts_nop 0\n\t"
                 "global_load_lds_dwordx4 %2, %3 offset:1024 sc1\n1:\n\ts_mov_b32 m0, %0" : "=&s"(keep), "+s"(dl) : "v"(ao), "s"(ws), "s"(w) : "memory", "scc");
  };
  auto get_bd = [&](int t0, int t1) {
#pragma unroll
    for (int t = t0; t < t1; ++t) {
      const u32x2 h0 = trd(da + 512 * (t >> 2) + 32 * (t & 3)), h1 = trd(da + 512 * (t >> 2) + 32 * (t & 3) + 4608 + 32);
      bw[t] = (u32x4){h0[0], h0[1], h1[0], h1[1]};
    }
#pragma unroll
    for (int t = t0; t < t1; ++t) asm volatile("" : "+v"(bw[t]));
  };
  pick(cand, sel, true);
  uint32_t mykey = sel[lane & 7], ro = ((mykey >> 3) & 0x3ffu) * 1152u;
  TS(0, 12);
  auto check = [&] {
    cnt[(k3rd > (uint32_t)__builtin_amdgcn_readlane(mykey, 7)) ? 5 : 6] = 1u;
    const uint32_t m3 = oct_maxu(k3rd);
    if (k3rd == m3) k3rd = k4th;
    if ((lane & 7) == 0) cand[72 + (tid >> 3)] = m3;
    if (w == 0 && lane < 32) cand[(lane < 8) ? 64 + lane : 96 + lane] = (lane < 8) ? mykey : 0u;
  };
  if constexpr (ROLE == 1) {
    if (ffn && !FORCEFB) rows_dma(ro + al);
    check();
    DRAIN();
  } else if (ffn) {
    if (LPOS) { SLEEP(LSLEEP); down_dma(KN, 4, lane); } else { DRAIN(); gsync0(ca, 12); get_bd(0, 8); }
  }
  BAR();
  TS(0, 5);
  auto put_out = [&](int j, uint32_t k0, int rk) {
    const uint64_t mine = __ballot(((mykey ^ k0) & ~24u) == 0u) & 0xffull;
    if (mine == 0ull) put_row(hv, j, out, (uint32_t)((8 * g2 + 2 * wl + j) * (D_ * 2)));
    else if (rk >= 2) put_row(nv, j, ws, O_FB + 1152u * (uint32_t)__builtin_ctzll(mine));
  };
  auto rest_out = [&] { put_out(0, keya, rka); put_out(1, keyb, rkb); };
  f32x4 lo, hi;
  auto gate_up = [&] {
    u32x4 av[5];
    const uint32_t aa = lb0 + LA + 16u * lane;
#pragma unroll
    for (int j = 0; j < 5; ++j) asm volatile("ds_read_b128 %0, %1 offset:%2" : "=v"(av[j]) : "v"(aa), "n"(1024 * j) : "memory");
    lo = (f32x4){0.f, 0.f, 0.f, 0.f}; hi = lo;
#pragma unroll
    for (int j = 0; j < 9; ++j) {
      if (j < 5) asm volatile("s_waitcnt lgkmcnt(4)" : "+v"(av[j % 5]));
      else if (j == 5) asm volatile("s_waitcnt lgkmcnt(2)" : "+v"(av[0]), "+v"(av[1]));
      else if (j == 7) asm volatile("s_waitcnt lgkmcnt(0)" : "+v"(av[2]), "+v"(av[3]));
      lo = MMB(av[j % 5], bw[2 * j], lo);
      hi = MMB(av[j % 5], bw[2 * j + 1], hi);
      __builtin_amdgcn_sched_barrier(0);
      if (j < 4) asm volatile("ds_read_b128 %0, %1 offset:%2" : "=v"(av[j]) : "v"(aa), "n"(1024 * (j + 5)) : "memory");
    }
  };
  uint32_t fl;
  asm volatile("ds_read_b32 %0, %1" : "=v"(fl) : "v"(ca + 20) : "memory");
  if (ROLE == 1) gate_up();
  asm volatile("s_waitcnt lgkmcnt(0)" : "+v"(fl));
  const bool slow = FORCESLOW || FORCEFB || __builtin_amdgcn_readfirstlane(fl) != 0;
  if (__builtin_expect(slow, 0)) {
    TSC(17);
    pick(cand + 64, sel + 16);
    mykey = sel[16 + (lane & 7)];
    ro = ((mykey >> 3) & 0x3ffu) * 1152u;
    if (ROLE == 1 && (k3rd > (uint32_t)__builtin_amdgcn_readlane(mykey, 7) || (mykey & 24u) == 8u)) cnt[11] = 1u;
    BAR();
    if (FORCESLOW > 1 || FORCEFB || __builtin_amdgcn_readfirstlane(cnt[11]) != 0) {
    u32x4 key;
#pragma unroll 1
    for (int spin = 0; spin < SPIN_CAP; ++spin) {
      LD16W(key, O_KF + 16u * tid);
      if (!__any(key[1] != T || key[3] != T)) break;
      SLEEP(KBACK);
    }
    const uint32_t ib = (uint32_t)(((255 - (tid >> 1)) << 5) | (7 - 4 * (tid & 1)));
    uint32_t k0 = (key[0] << 16) | ib, k1 = (key[0] & 0xffff0000u) | (ib - 1), k2 = (key[2] << 16) | (ib - 2), k3 = (key[2] & 0xffff0000u) | (ib - 3);
    CEX(k0, k1) CEX(k2, k3) CEX(k0, k2) CEX(k1, k3) CEX(k1, k2)
#pragma unroll 1
    for (int c = 0; c < C_; ++c) {
      const uint32_t mx = wave_maxu(k0);
      const bool hit = (k0 == mx);
      k0 = hit ? k1 : k0; k1 = hit ? k2 : k1; k2 = hit ? k3 : k2; k3 = hit ? 0u : k3;
      if (lane == 0) cand[64 + 8 * w + c] = mx;
    }
    pick(cand + 64, sel + 32);
    mykey = sel[32 + (lane & 7)];
    const uint32_t okey = sel[32 + (lane >> 3)];
    const uint64_t same = __ballot(((mykey ^ okey) & 0x1fe0u) == 0u && okey > mykey);
    const int sl = FORCEFB + __popcll((same >> (lane & 7)) & 0x0101010101010101ull);
    ro = (sl < 2) ? (((mykey >> 3) & 0x3ffu) + 3 - sl) * 1152u : O_FB - O_XC + 1152u * (lane & 7);
    }
    if (ln2) rest_out();
    if (ffn) {
      if (ROLE == 1) rows(ro + al, [] {});
      BAR();
      if (ROLE == 1) gate_up();
    }
  }
  if (ROLE == 1 && ffn && !slow && __builtin_expect(__any(lo[0] != lo[0] || lo[1] != lo[1] || lo[2] != lo[2] || lo[3] != lo[3] ||
                                                           hi[0] != hi[0] || hi[1] != hi[1] || hi[2] != hi[2] || hi[3] != hi[3]), 0)) {
    gsync0(ca + 4, 12);
    rows(ro + al, [] {});
    gsync0(ca + 4, 16);
    gate_up();
  }
  if (OPOS == 0 && ROLE == 2 && ffn && !slow) { SLEEP(OD); rest_out(); }
  if (!ffn) {
    if (ln2 && !slow) { SLEEP(OD2); rest_out(); }
    TSE();
    return;
  }
  uint32_t fx = 0, fv = 0, fn = 0, fo = 0, sa[9];
  float rs, hx, xs;
  u32x4 se;
  if constexpr (ROLE == 1) {
    const f32x4 lh = (lane < 32) ? lo : hi;
    const float fa = fold32(lh[0], lh[2]), fb2 = fold32(lh[1], lh[3]);
    const float g = __int_as_float(__builtin_amdgcn_update_dpp(__float_as_int(fa), __float_as_int(fb2), 0x128, 0xf, 0xc, false));
    const float u = __int_as_float(__builtin_amdgcn_update_dpp(__float_as_int(fb2), __float_as_int(fa), 0x128, 0xf, 0x3, false));
    const uint32_t gu = pack2(g, u);
    const float gp = bflo(gu);
    const float sg = rbf(gp * __builtin_amdgcn_rcpf(1.f + __expf(-gp)));
    *(u16*)((char*)hm + ha) = f2bf(sg * bfhi(gu));
  } else if (LPOS) {
    DRAIN();
    gsync0(ca, 12);
    get_bd(0, 8);
  }
  BAR();
  TS(0, 6);
  if (ROLE == 2 || w == 2) {
    const u32x4 a0 = *(const u32x4*)((char*)hm + h0a), a1 = *(const u32x4*)((char*)hm + h1a);
    if (ROLE == 1) get_bd(0, 4);
    const f32x4 z = {0.f, 0.f, 0.f, 0.f};
    f32x4 o[4];
#pragma unroll
    for (int t = 0; t < 2 * ROLE; ++t) o[t] = MMB(a1, bw[2 * t + 1], MMB(a0, bw[2 * t], z));
#pragma unroll
    for (int t = 0; t < ROLE; ++t) {
      u32x4 sv;
#pragma unroll
      for (int r = 0; r < 4; ++r) sv[r] = min(pack2(o[2 * t][r], o[2 * t + 1][r]), 0xfffffffeu);
      ST16(so + 4096u * t, sv);
    }
    TS(0, 13);
  }
  if (OPOS == 1 && ROLE == 2) {
    SLEEP(OSLEEP);
    if (!slow) rest_out();
  }
  if (ROLE == 1 && w == 1) {
    const int q = (fb & 15) >> 2, ts = 4 * (q & 1) + ((i & 7) >> 1);
    const int dd = 64 * (fb >> 4) + 16 * (2 * (i & 1) + (q >> 1)) + 4 * (fb & 3) + 2 * (kq & 1) + (kq >> 1);
    const uint32_t fkey = (uint32_t)__builtin_amdgcn_ds_bpermute(4 * ts, (int)mykey), fr = (uint32_t)__builtin_amdgcn_ds_bpermute(4 * ts, (int)ro);
    const int ft = 2047 - (int)(((fkey >> 2) & 0x7f8u) | (fkey & 7u));
    fo = (uint32_t)(ft * D_ + dd) * 2u;
    u32x2 fs;
    asm volatile("global_load_ushort %0, %4, %8\n\tglobal_load_ushort %1, %5, %9 sc1\n\tglobal_load_ushort %2, %6, %9 sc1\n\tglobal_load_dwordx2 %3, %7, %9 sc1"
                 : "=&v"(fx), "=&v"(fv), "=&v"(fn), "=&v"(fs) : "v"(fo), "v"(v_at(dd, ft >> 3) + 2u * (ft & 7)), "v"(O_XC + fr + 2u * dd), "v"(O_SC + 16u * dd), "s"(x), "s"(ws) : "memory");
    uint32_t t16 = (((T >> (3 * ((ft >> 1) & 3))) & LMASK) | (((T >> (12 + ((ft >> 1) & 3))) & 0x00010001u) << 14)) >> (16 * (ft & 1)) & 0xffffu;
    const uint32_t o = fkey >> 16;
    const float rs0 = bf2f((u16)((o & 0x8000u) ? (o & 0x7fffu) : (~o & 0xffffu)));
    rs = rbf(__builtin_amdgcn_rcpf(1.f + __expf(-rs0)));
    const uint32_t one = (i < 8) ? (0x3f80u << (16 * (i & 1))) : 0u;
    se = (u32x4){(i >> 1) == 0 ? one : 0u, (i >> 1) == 1 ? one : 0u, (i >> 1) == 2 ? one : 0u, (i >> 1) == 3 ? one : 0u};
#pragma unroll
    for (int m = 0; m < 9; ++m) sa[m] = O_SL + (uint32_t)(16 * m + (lane >> 2)) * SP + (uint32_t)(4 * fb + (lane & 3)) * 16u;
    asm volatile("" : "+v"(t16), "+v"(rs), "+v"(se), "+v"(sa[0]), "+v"(sa[1]), "+v"(sa[2]), "+v"(sa[3]), "+v"(sa[4]), "+v"(sa[5]), "+v"(sa[6]), "+v"(sa[7]), "+v"(sa[8]));
    SLEEP(SSLEEP);
    TS(0, 1);
    float sum;
#pragma unroll 1
    for (int spin = 0; spin < SPIN_CAP; ++spin) {
      u32x4 sv[9];
#pragma unroll
      for (int m = 0; m < 9; ++m) LD16(sv[m], sa[m]);
      asm volatile("s_waitcnt vmcnt(9)" : "+v"(fx), "+v"(fv), "+v"(fn), "+v"(fs));
      if (__builtin_expect(__any(fs[0] != T), 0)) {
        for (int k = 0; k < SPIN_CAP && fs[0] != T; ++k)
          asm volatile("global_load_dwordx2 %0, %1, %2 sc1\n\ts_waitcnt vmcnt(0)" : "=&v"(fs) : "v"(O_SC + 16u * dd), "s"(ws) : "memory");
      }
      const uint32_t f1 = fv ^ t16, f2 = (f1 & 7u) ? fv & ~7u : f1;
      hx = rbf(bf2f((u16)fx) + (float)__builtin_bit_cast(_Float16, (u16)f2) * __uint_as_float(fs[1]));
      xs = bf2f((u16)fn);
      asm volatile("" : "+v"(hx), "+v"(xs));
      f32x4 dv = {0.f, 0.f, 0.f, 0.f};
      static_for<0, 9>([&](auto mm) {
        constexpr int m = mm.value;
        asm volatile("s_waitcnt vmcnt(%1)" : "+v"(sv[m]) : "n"(8 - m));
        dv = MMB(sv[m], se, dv);
        __builtin_amdgcn_sched_barrier(0);
      });
      sum = fold16(fold32(dv[0], dv[1]), fold32(dv[2], dv[3]));
      if (__builtin_expect(!__any(sum != sum), 1)) break;
      uint32_t mx = 0u;
#pragma unroll
      for (int m = 0; m < 9; ++m) mx = max(mx, max4(sv[m]));
      if (!__any(mx == 0xffffffffu)) break;
      TSC(18);
      SLEEP(PSLEEP);
    }
    const float o1 = rbf(xs + rbf(rbf(sum) * rs));
    if (i < 8) asm volatile("global_store_short %0, %1, %2 sc1" : : "v"(fo), "v"((uint32_t)f2bf(hx + o1)), "s"(out) : "memory");
    TS(0, 2);
  }
  TSE();
  }
  }
}
#define BROT "192"
#define CPF_V ""
#define CPF_A "s_getpc_b64 s[96:97]\n\ts_lshl_b32 s99, s2, 4\n\ts_lshl_b32 s84, s98, 7\n\ts_add_u32 s99, s99, s84\n\tv_and_b32 v112, 1, v0\n\t" \
              "v_lshl_add_u32 v112, v112, 12, s99\n\tv_min_u32 v112, kf_end-kf_all-0x80, v112\n\ts_mov_b64 exec, 3\n\tglobal_load_dword v112, v112, s[96:97]\n\ts_mov_b64 exec, -1\n\t"
extern "C" __global__ __launch_bounds__(512, 4) void kf_all(KARGS) {
  __shared__ __attribute__((aligned(16))) char smem[SMEM];
  asm volatile("s_load_dwordx16 s[64:79], s[0:1], 0x0\n\ts_load_dwordx4 s[80:83], s[0:1], 0x40\n\tv_readfirstlane_b32 s98, v0\n\t"
               "s_add_u32 s84, s2, " BROT "\n\ts_xor_b32 s84, s84, s2\n\ts_bitset0_b32 s84, 8\n\ts_bitset0_b32 s84, 9\n\ts_xor_b32 s2, s2, s84\n\t"
               "v_mov_b32 v109, 0\n\ts_and_b32 s85, s2, 7\n\ts_bitcmp1_b32 s2, 8\n\ts_cbranch_scc1 1f\n\t" CPF_A "s_bitcmp1_b32 s98, 8\n\ts_cbranch_scc1 2f\n\t"
               "s_andn2_b32 s84, s2, 31\n\ts_lshl2_add_u32 s84, s85, s84\n\ts_bfe_u32 s85, s2, 0x20003\n\ts_or_b32 s84, s84, s85\n\t"
               "s_mulk_i32 s84, 0x2400\n\ts_mulk_i32 s98, 36\n\ts_add_u32 s84, s84, s98\n\tv_and_b32 v110, 63, v0\n\tv_and_b32 v111, 32, v0\n\t"
               "v_lshlrev_b32 v111, 5, v111\n\tv_lshl_add_u32 v111, v110, 2, v111\n\tv_lshl_add_u32 v110, v110, 4, s84\n\tv_add_u32 v111, s84, v111\n\t"
               "s_cmpk_lt_u32 s2, 0x90\n\ts_waitcnt lgkmcnt(0)\n\t"
               "global_load_dwordx4 v[100:103], v110, s[64:65]\n\tglobal_load_dwordx4 v[104:107], v110, s[64:65] offset:1152\n\t"
               "global_load_dword v108, v111, s[64:65] offset:1024\n\tglobal_load_dword v109, v109, s[82:83] sc1\n\t"
               "s_cbranch_scc1 kf_n\n\ts_branch kf_h\n"
               "1:\n\ts_bfe_u32 s84, s2, 0x50003\n\ts_lshl_b32 s85, s85, 5\n\ts_or_b32 s84, s84, s85\n\ts_lshl_b32 s84, s84, 2\n\t"
               "v_and_b32 v110, 0xff, v0\n\tv_bfe_u32 v107, v0, 7, 1\n"
               "4:\n\ts_lshl_b32 s85, s84, 7\n\tv_lshl_add_u32 v110, v110, 1, s85\n\tv_lshl_add_u32 v107, v107, 1, s84\n\ts_and_b32 s85, s98, s2\n\ts_waitcnt lgkmcnt(0)\n\t"
               "global_load_ushort v100, v107, s[78:79]\n\tglobal_load_ushort v101, v110, s[66:67]\n\tglobal_load_ushort v102, v110, s[68:69]\n\t"
               "global_load_dword v109, v109, s[82:83] sc1\n\ts_bitcmp1_b32 s85, 8\n\ts_cbranch_scc1 kf_u\n\t"
               "global_load_ushort v103, v110, s[70:71]\n\tglobal_load_ushort v104, v110, s[72:73]\n\tglobal_load_ushort v105, v110, s[74:75]\n\t"
               "global_load_ushort v106, v110, s[76:77]\n\tglobal_load_ushort v107, v107, s[80:81]\n\ts_bitcmp1_b32 s2, 8\n\ts_cbranch_scc1 kf_c\n"
               "6:\n\ts_barrier\n\ts_branch kf_p\n"
               "2:\n\t" CPF_V "s_cmpk_lt_u32 s2, 0xc0\n\ts_cbranch_scc1 6b\n\t"
               "s_lshr_b32 s84, s2, 3\n\ts_lshl3_add_u32 s84, s85, s84\n\ts_lshl1_add_u32 s84, s84, 976\n\t"
               "v_and_b32 v110, 0x7f, v0\n\tv_mov_b32 v107, 0\n\ts_branch 4b"
               : : "s"(__builtin_amdgcn_kernarg_segment_ptr()), "s"(blockIdx.x), "v"(threadIdx.x),
                   "s"((uint32_t)(uintptr_t)(__attribute__((address_space(3))) char*)smem) : "scc", "s96", "s97", "s98", "s99", "v127");
}
extern "C" __global__ __launch_bounds__(512, 4) void kf_c(KARGS) {
  __shared__ __attribute__((aligned(16))) char smem[SMEM];
  role_body<0>(smem, KPASS);
}
extern "C" __global__ __launch_bounds__(512, 4) void kf_u(KARGS) {
  __shared__ __attribute__((aligned(16))) char smem[SMEM];
  role_body<3>(smem, KPASS);
}
extern "C" __global__ __launch_bounds__(512, 4) void kf_n(KARGS) {
  __shared__ __attribute__((aligned(16))) char smem[SMEM];
  role_body<1, 1>(smem, KPASS);
}
extern "C" __global__ __launch_bounds__(512, 4) void kf_h(KARGS) {
  __shared__ __attribute__((aligned(16))) char smem[SMEM];
  role_body<1, 0>(smem, KPASS);
}
extern "C" __global__ __launch_bounds__(512, 4) void kf_p(KARGS) {
  __shared__ __attribute__((aligned(16))) char smem[SMEM];
  role_body<2>(smem, KPASS);
}
extern "C" __global__ __launch_bounds__(512, 4) void kf_conv() { asm volatile(CONV_ASM ::: CONV_CLOB); }
extern "C" __global__ void kf_end() {}

extern "C" __global__ __launch_bounds__(1024) void k_probe(int* dummy) {
  __shared__ int sm[2];
  const uint32_t now = (uint32_t)__builtin_amdgcn_s_memrealtime();
  __shared__ int sn;
  if (threadIdx.x == 0) { sm[0] = 0; sm[1] = 2 == 1 ? 0x7fffffff : 0; sn = 0; }
  __syncthreads();
  for (int k = threadIdx.x; k < 4096; k += 1024) {
    const uint32_t a = (uint32_t)g_prb[k], b = (uint32_t)(g_prb[k] >> 32);
    if (a) atomicMax(&sm[0], (int)(now - a));
    if (b) { if (2 == 1) atomicMin(&sm[1], (int)(now - b)); else if (2 == 0) atomicMax(&sm[1], (int)(now - b)); else { atomicAdd(&sm[1], (int)(now - b)); atomicAdd(&sn, 1); } }
  }
  __syncthreads();
  const int q = min(max(sm[0] * 10 - (2 == 2 ? sm[1] * 10 / max(sn, 1) : sm[1] * 10), 0), 30000);
  if (threadIdx.x == 0) while ((int)((uint32_t)__builtin_amdgcn_s_memrealtime() - now) < q) __builtin_amdgcn_s_sleep(2);
}
extern "C" void kernel_launch(void* const* d_in, const int* in_sizes, int n_in,
                              void* d_out, int out_size, void* d_ws, size_t ws_size,
                              hipStream_t stream) {
  kf_all<<<512, 512, 0, stream>>>((const u16*)d_in[0], (const u16*)d_in[1], (const u16*)d_in[2], (const u16*)d_in[3], (const u16*)d_in[4],
                                  (const u16*)d_in[5], (const u16*)d_in[6], (const u16*)d_in[7], (const u16*)d_in[8], (char*)d_ws + (size_t)WSOFF * 1048576,
                                  (const u16*)d_in[10], (const u16*)d_in[11], (const u16*)d_in[12], (const u16*)d_in[13], (u16*)d_out);
  k_probe<<<1, 1024, 0, stream>>>((int*)d_ws);
}
```

```cpp
#include <hip/hip_runtime.h>
#include <hip/hip_bf16.h>
#include <stdint.h>
#define LPOS 1
#define LSLEEP 1
#define VD2 390
#define V2D 22
#define KD 178

typedef __attribute__((ext_vector_type(8))) short bf16x8;
typedef __attribute__((ext_vector_type(4))) float f32x4;
typedef uint16_t u16;
typedef __attribute__((ext_vector_type(4))) uint32_t u32x4;
typedef __attribute__((ext_vector_type(2))) uint32_t u32x2;
#define TSD uint32_t pr0_ = 0u, pr1_ = 0u
__device__ unsigned long long g_prb[4096];
#define TS(k, i) do { if ((i) == 0) pr0_ = (uint32_t)__builtin_amdgcn_s_memrealtime() | 1u; \
    if (ROLE == 1 && (i) == 2 && (1 < 0 || w == 1) && (blockIdx.x & 255) < 144) pr1_ = (uint32_t)__builtin_amdgcn_s_memrealtime() | 1u; } while (0)
#define TSC(i)
#define TSE() do { if (__builtin_amdgcn_mbcnt_hi(-1, __builtin_amdgcn_mbcnt_lo(-1, 0)) == 0) \
    __hip_atomic_store(&g_prb[blockIdx.x * 8 + w], ((unsigned long long)pr1_ << 32) | pr0_, __ATOMIC_RELAXED, __HIP_MEMORY_SCOPE_AGENT); } while (0)
#define TSP
#define TSA

constexpr int L_ = 2048, D_ = 576, N_ = 128, F_ = 4608, C_ = 8;
constexpr int NF = 144;
constexpr uint32_t O_EP = 0;
constexpr uint32_t O_KF = 4096;
constexpr uint32_t O_KEY = O_KF + 8192;
constexpr uint32_t O_SC = O_KEY + 32768;
constexpr uint32_t O_DN = O_SC + 9216;
constexpr uint32_t O_SU = O_DN + 20480;
constexpr uint32_t O_FB = O_SU + 262144;
constexpr uint32_t O_XC = O_FB + 8 * 1152;
constexpr uint32_t SP = 9216 + 128;
constexpr uint32_t O_SL = O_XC + 1024 * 1152;
constexpr uint32_t SLU = 336;
constexpr uint32_t O_U = O_SL + 256 * SLU * 16;
constexpr int UP = 584;
constexpr uint32_t VP = 8192 + 128, VPP = 4096 + 128;
constexpr uint32_t O_V = O_U + 256 * UP * 16, O_VP = O_V + 256 * VP;
static_assert(256 * SLU * 16 >= NF * SP && O_SL % 128 == 0 && O_U % 128 == 0 && O_V % 128 == 0, "");
__host__ __device__ __forceinline__ uint32_t u_at(int tg, int d) { return O_U + 16u * (uint32_t)((tg >> 1) * (UP * 2) + (d >> 2) * 8 + (tg & 1) * 4 + (d & 3)); }
__host__ __device__ __forceinline__ uint32_t v_at(int d, int tg) {
  return (d < 512) ? O_V + (uint32_t)(d >> 1) * VP + 16u * (uint32_t)(2 * tg + (d & 1)) : O_VP + (uint32_t)(d - 512) * VPP + 16u * (uint32_t)tg;
}
#define HAVE_VAT
#define HAVE_VTAG
__device__ __forceinline__ float bf2f(u16 v) { return __uint_as_float((uint32_t)v << 16); }
__device__ __forceinline__ float bflo(uint32_t v) { return __uint_as_float(v << 16); }
__device__ __forceinline__ float bfhi(uint32_t v) { return __uint_as_float(v & 0xffff0000u); }
__device__ __forceinline__ u16 f2bf(float f) { return __builtin_bit_cast(u16, (__bf16)f); }
__device__ __forceinline__ float rbf(float f) { return bf2f(f2bf(f)); }
typedef __attribute__((ext_vector_type(2))) float f32x2;
typedef __attribute__((ext_vector_type(2))) __bf16 bf16x2v;
__device__ __forceinline__ uint32_t pack2(float a, float b) {
  return __builtin_bit_cast(uint32_t, __builtin_convertvector((f32x2){a, b}, bf16x2v));
}

#define DPP_I(v, ctrl, rmask) __builtin_amdgcn_update_dpp(0, (v), (ctrl), (rmask), 0xf, true)
__device__ __forceinline__ float row_sum(float v) {
  v += __int_as_float(DPP_I(__float_as_int(v), 0x111, 0xf));
  v += __int_as_float(DPP_I(__float_as_int(v), 0x112, 0xf));
  v += __int_as_float(DPP_I(__float_as_int(v), 0x114, 0xf));
  v += __int_as_float(DPP_I(__float_as_int(v), 0x118, 0xf));
  return v;
}
__device__ __forceinline__ float rdl(float v, int l) { return __int_as_float(__builtin_amdgcn_readlane(__float_as_int(v), l)); }
__device__ __forceinline__ float wave_sum(float v) {
  v = row_sum(v);
  asm("s_nop 1\n\tv_add_f32_dpp %0, %0, %0 row_bcast:15 row_mask:0xa bank_mask:0xf\n\t"
      "s_nop 1\n\tv_add_f32_dpp %0, %0, %0 row_bcast:31 row_mask:0xc bank_mask:0xf\n\ts_nop 0" : "+v"(v));
  return rdl(v, 63);
}
__device__ __forceinline__ float fold32(float a, float b) {
  const auto r = __builtin_amdgcn_permlane32_swap(__float_as_uint(a), __float_as_uint(b), false, false);
  return __uint_as_float(r[0]) + __uint_as_float(r[1]);
}
__device__ __forceinline__ float fold16(float a, float b) {
  const auto r = __builtin_amdgcn_permlane16_swap(__float_as_uint(a), __float_as_uint(b), false, false);
  return __uint_as_float(r[0]) + __uint_as_float(r[1]);
}
__device__ __forceinline__ void wave_sum2(float& a, float& b) {
  float v = row_sum(fold32(a, b));
  asm("s_nop 1\n\tv_add_f32_dpp %0, %0, %0 row_bcast:15 row_mask:0xa bank_mask:0xf\n\ts_nop 0" : "+v"(v));
  a = rdl(v, 31); b = rdl(v, 63);
}
__device__ __forceinline__ float dot2(uint32_t a, uint32_t b, float c) {
  return __builtin_amdgcn_fdot2_f32_bf16(__builtin_bit_cast(bf16x2v, a), __builtin_bit_cast(bf16x2v, b), c, false);
}
__device__ __forceinline__ uint32_t row_maxu(uint32_t v) {
  v = max(v, (uint32_t)DPP_I((int)v, 0xb1, 0xf));
  v = max(v, (uint32_t)DPP_I((int)v, 0x4e, 0xf));
  v = max(v, (uint32_t)DPP_I((int)v, 0x141, 0xf));
  v = max(v, (uint32_t)DPP_I((int)v, 0x140, 0xf));
  return v;
}
__device__ __forceinline__ uint32_t wave_maxu(uint32_t v) {
  v = max(v, (uint32_t)DPP_I((int)v, 0x111, 0xf));
  v = max(v, (uint32_t)DPP_I((int)v, 0x112, 0xf));
  v = max(v, (uint32_t)DPP_I((int)v, 0x114, 0xf));
  v = max(v, (uint32_t)DPP_I((int)v, 0x118, 0xf));
  v = max(v, (uint32_t)DPP_I((int)v, 0x142, 0xa));
  v = max(v, (uint32_t)DPP_I((int)v, 0x143, 0xc));
  return (uint32_t)__builtin_amdgcn_readlane((int)v, 63);
}

typedef __attribute__((ext_vector_type(8))) _Float16 f16x8;
typedef __attribute__((ext_vector_type(2))) _Float16 f16x2;
__device__ __forceinline__ uint32_t packh(f32x2 v) { return __builtin_bit_cast(uint32_t, __builtin_convertvector(v, f16x2)); }
__device__ __forceinline__ uint32_t packh(float a, float b) { return packh((f32x2){a, b}); }
constexpr int IP = 224, I_X = 128;
__device__ __forceinline__ f32x2 cmul(f32x2 z, f32x2 w) {
  f32x2 r;
  asm("v_pk_mul_f32 %0, %1, %2 op_sel_hi:[0,1]\n\tv_pk_fma_f32 %0, %1, %2, %0 op_sel:[1,1,0] op_sel_hi:[1,0,1] neg_lo:[0,1,0]" : "=&v"(r) : "v"(z), "v"(w));
  return r;
}
__device__ __forceinline__ f32x2 cexp2(float ar2, float air, float l) {
  const float t = air * l;
  return (f32x2){__builtin_amdgcn_cosf(t), __builtin_amdgcn_sinf(t)} * __builtin_amdgcn_exp2f(ar2 * l);
}
__device__ __forceinline__ float state_a(float ldt, float are, float aim, float& ar2, float& air, float& g) {
  const float dt = rbf(__expf(ldt));
  ar2 = dt * are * 1.4426950408889634f;
  air = dt * aim * 0.15915494309189535f;
  air -= __builtin_rintf(air);
  g = 0.f;
  if (__builtin_expect(__any(ar2 > 0.f), 0)) g = fminf(__uint_as_float(wave_maxu(__float_as_uint(fmaxf(ar2, 0.f)))), 1.f);
  return dt;
}
template <int N> __device__ __forceinline__ void put_im(char* p, int im, const f32x2* z) {
  *(u32x4*)p = (u32x4){packh(z[0][0], z[1][0]), packh(z[2][0], z[3][0]), packh(z[4][0], z[5][0]), packh(z[6][0], z[7][0])};
  *(u32x4*)(p + im) = (u32x4){packh(z[0][1], z[1][1]), packh(z[2][1], z[3][1]), packh(z[4][1], z[5][1]), packh(z[6][1], z[7][1])};
}
#define Y_ASM \
  "v_mov_b64 v[40:41], %0\nv_pk_mul_f32 v[42:43], v[40:41], %1 op_sel_hi:[0,1]\nv_pk_mul_f32 v[44:45], %0, %2 op_sel_hi:[0,1]\n" \
  "v_pk_fma_f32 v[42:43], v[40:41], %1, v[42:43] op_sel:[1,1,0] op_sel_hi:[1,0,1] neg_lo:[0,1,0]\n" \
  "v_pk_fma_f32 v[44:45], %0, %2, v[44:45] op_sel:[1,1,0] op_sel_hi:[1,0,1] neg_lo:[0,1,0]\nv_cvt_pk_f16_f32 v56, v40, v42\n" \
  "v_cvt_pk_f16_f32 v60, v41, v43\nv_pk_mul_f32 v[40:41], v[42:43], %1 op_sel_hi:[0,1]\nv_pk_mul_f32 v[48:49], v[44:45], %2 op_sel_hi:[0,1]\n" \
  "v_pk_fma_f32 v[40:41], v[42:43], %1, v[40:41] op_sel:[1,1,0] op_sel_hi:[1,0,1] neg_lo:[0,1,0]\n" \
  "v_pk_fma_f32 v[48:49], v[44:45], %2, v[48:49] op_sel:[1,1,0] op_sel_hi:[1,0,1] neg_lo:[0,1,0]\n" \
  "v_pk_mul_f32 v[42:43], v[40:41], %1 op_sel_hi:[0,1]\nv_pk_mul_f32 v[52:53], v[48:49], %2 op_sel_hi:[0,1]\n" \
  "v_pk_fma_f32 v[42:43], v[40:41], %1, v[42:43] op_sel:[1,1,0] op_sel_hi:[1,0,1] neg_lo:[0,1,0]\n" \
  "v_pk_fma_f32 v[52:53], v[48:49], %2, v[52:53] op_sel:[1,1,0] op_sel_hi:[1,0,1] neg_lo:[0,1,0]\nv_cvt_pk_f16_f32 v57, v40, v42\n" \
  "v_cvt_pk_f16_f32 v61, v41, v43\nv_pk_mul_f32 v[40:41], v[42:43], %1 op_sel_hi:[0,1]\n" \
  "v_pk_fma_f32 v[40:41], v[42:43], %1, v[40:41] op_sel:[1,1,0] op_sel_hi:[1,0,1] neg_lo:[0,1,0]\n" \
  "v_pk_mul_f32 v[42:43], v[40:41], %1 op_sel_hi:[0,1]\nv_pk_mul_f32 v[46:47], v[44:45], %1 op_sel_hi:[0,1]\n" \
  "v_pk_fma_f32 v[42:43], v[40:41], %1, v[42:43] op_sel:[1,1,0] op_sel_hi:[1,0,1] neg_lo:[0,1,0]\n" \
  "v_pk_fma_f32 v[46:47], v[44:45], %1, v[46:47] op_sel:[1,1,0] op_sel_hi:[1,0,1] neg_lo:[0,1,0]\nv_cvt_pk_f16_f32 v58, v40, v42\n" \
  "v_cvt_pk_f16_f32 v62, v41, v43\nv_cvt_pk_f16_f32 v64, v44, v46\nv_cvt_pk_f16_f32 v68, v45, v47\n" \
  "v_pk_mul_f32 v[40:41], v[42:43], %1 op_sel_hi:[0,1]\nv_pk_mul_f32 v[44:45], v[46:47], %1 op_sel_hi:[0,1]\n" \
  "v_pk_fma_f32 v[40:41], v[42:43], %1, v[40:41] op_sel:[1,1,0] op_sel_hi:[1,0,1] neg_lo:[0,1,0]\n" \
  "v_pk_fma_f32 v[44:45], v[46:47], %1, v[44:45] op_sel:[1,1,0] op_sel_hi:[1,0,1] neg_lo:[0,1,0]\n" \
  "v_pk_mul_f32 v[42:43], v[40:41], %1 op_sel_hi:[0,1]\nv_pk_mul_f32 v[46:47], v[44:45], %1 op_sel_hi:[0,1]\n" \
  "v_pk_fma_f32 v[42:43], v[40:41], %1, v[42:43] op_sel:[1,1,0] op_sel_hi:[1,0,1] neg_lo:[0,1,0]\n" \
  "v_pk_fma_f32 v[46:47], v[44:45], %1, v[46:47] op_sel:[1,1,0] op_sel_hi:[1,0,1] neg_lo:[0,1,0]\nv_cvt_pk_f16_f32 v59, v40, v42\n" \
  "v_cvt_pk_f16_f32 v63, v41, v43\nv_cvt_pk_f16_f32 v65, v44, v46\nv_cvt_pk_f16_f32 v69, v45, v47\n" \
  "v_pk_mul_f32 v[44:45], v[46:47], %1 op_sel_hi:[0,1]\nds_write_b128 %3, v[56:59] offset:0\n" \
  "v_pk_fma_f32 v[44:45], v[46:47], %1, v[44:45] op_sel:[1,1,0] op_sel_hi:[1,0,1] neg_lo:[0,1,0]\nds_write_b128 %3, v[60:63] offset:64\n" \
  "v_pk_mul_f32 v[46:47], v[44:45], %1 op_sel_hi:[0,1]\nv_pk_mul_f32 v[50:51], v[48:49], %1 op_sel_hi:[0,1]\n" \
  "v_pk_fma_f32 v[46:47], v[44:45], %1, v[46:47] op_sel:[1,1,0] op_sel_hi:[1,0,1] neg_lo:[0,1,0]\n" \
  "v_pk_fma_f32 v[50:51], v[48:49], %1, v[50:51] op_sel:[1,1,0] op_sel_hi:[1,0,1] neg_lo:[0,1,0]\nv_cvt_pk_f16_f32 v66, v44, v46\n" \
  "v_cvt_pk_f16_f32 v70, v45, v47\nv_cvt_pk_f16_f32 v56, v48, v50\nv_cvt_pk_f16_f32 v60, v49, v51\n" \
  "v_pk_mul_f32 v[44:45], v[46:47], %1 op_sel_hi:[0,1]\nv_pk_mul_f32 v[48:49], v[50:51], %1 op_sel_hi:[0,1]\n" \
  "v_pk_fma_f32 v[44:45], v[46:47], %1, v[44:45] op_sel:[1,1,0] op_sel_hi:[1,0,1] neg_lo:[0,1,0]\n" \
  "v_pk_fma_f32 v[48:49], v[50:51], %1, v[48:49] op_sel:[1,1,0] op_sel_hi:[1,0,1] neg_lo:[0,1,0]\n" \
  "v_pk_mul_f32 v[46:47], v[44:45], %1 op_sel_hi:[0,1]\nv_pk_mul_f32 v[50:51], v[48:49], %1 op_sel_hi:[0,1]\n" \
  "v_pk_fma_f32 v[46:47], v[44:45], %1, v[46:47] op_sel:[1,1,0] op_sel_hi:[1,0,1] neg_lo:[0,1,0]\n" \
  "v_pk_fma_f32 v[50:51], v[48:49], %1, v[50:51] op_sel:[1,1,0] op_sel_hi:[1,0,1] neg_lo:[0,1,0]\nv_cvt_pk_f16_f32 v67, v44, v46\n" \
  "v_cvt_pk_f16_f32 v71, v45, v47\nv_cvt_pk_f16_f32 v57, v48, v50\nv_cvt_pk_f16_f32 v61, v49, v51\n" \
  "v_pk_mul_f32 v[48:49], v[50:51], %1 op_sel_hi:[0,1]\nds_write_b128 %3, v[64:67] offset:16\n" \
  "v_pk_fma_f32 v[48:49], v[50:51], %1, v[48:49] op_sel:[1,1,0] op_sel_hi:[1,0,1] neg_lo:[0,1,0]\nds_write_b128 %3, v[68:71] offset:80\n" \
  "v_pk_mul_f32 v[50:51], v[48:49], %1 op_sel_hi:[0,1]\nv_pk_mul_f32 v[54:55], v[52:53], %1 op_sel_hi:[0,1]\n" \
  "v_pk_fma_f32 v[50:51], v[48:49], %1, v[50:51] op_sel:[1,1,0] op_sel_hi:[1,0,1] neg_lo:[0,1,0]\n" \
  "v_pk_fma_f32 v[54:55], v[52:53], %1, v[54:55] op_sel:[1,1,0] op_sel_hi:[1,0,1] neg_lo:[0,1,0]\nv_cvt_pk_f16_f32 v58, v48, v50\n" \
  "v_cvt_pk_f16_f32 v62, v49, v51\nv_cvt_pk_f16_f32 v64, v52, v54\nv_cvt_pk_f16_f32 v68, v53, v55\n" \
  "v_pk_mul_f32 v[48:49], v[50:51], %1 op_sel_hi:[0,1]\nv_pk_mul_f32 v[52:53], v[54:55], %1 op_sel_hi:[0,1]\n" \
  "v_pk_fma_f32 v[48:49], v[50:51], %1, v[48:49] op_sel:[1,1,0] op_sel_hi:[1,0,1] neg_lo:[0,1,0]\n" \
  "v_pk_fma_f32 v[52:53], v[54:55], %1, v[52:53] op_sel:[1,1,0] op_sel_hi:[1,0,1] neg_lo:[0,1,0]\n" \
  "v_pk_mul_f32 v[50:51], v[48:49], %1 op_sel_hi:[0,1]\nv_pk_mul_f32 v[54:55], v[52:53], %1 op_sel_hi:[0,1]\n" \
  "v_pk_fma_f32 v[50:51], v[48:49], %1, v[50:51] op_sel:[1,1,0] op_sel_hi:[1,0,1] neg_lo:[0,1,0]\n" \
  "v_pk_fma_f32 v[54:55], v[52:53], %1, v[54:55] op_sel:[1,1,0] op_sel_hi:[1,0,1] neg_lo:[0,1,0]\nv_cvt_pk_f16_f32 v59, v48, v50\n" \
  "v_cvt_pk_f16_f32 v63, v49, v51\nv_cvt_pk_f16_f32 v65, v52, v54\nv_cvt_pk_f16_f32 v69, v53, v55\n" \
  "v_pk_mul_f32 v[52:53], v[54:55], %1 op_sel_hi:[0,1]\nds_write_b128 %3, v[56:59] offset:32\n" \
  "v_pk_fma_f32 v[52:53], v[54:55], %1, v[52:53] op_sel:[1,1,0] op_sel_hi:[1,0,1] neg_lo:[0,1,0]\nds_write_b128 %3, v[60:63] offset:96\n" \
  "v_pk_mul_f32 v[54:55], v[52:53], %1 op_sel_hi:[0,1]\n" \
  "v_pk_fma_f32 v[54:55], v[52:53], %1, v[54:55] op_sel:[1,1,0] op_sel_hi:[1,0,1] neg_lo:[0,1,0]\nv_cvt_pk_f16_f32 v66, v52, v54\n" \
  "v_cvt_pk_f16_f32 v70, v53, v55\nv_pk_mul_f32 v[52:53], v[54:55], %1 op_sel_hi:[0,1]\n" \
  "v_pk_fma_f32 v[52:53], v[54:55], %1, v[52:53] op_sel:[1,1,0] op_sel_hi:[1,0,1] neg_lo:[0,1,0]\n" \
  "v_pk_mul_f32 v[54:55], v[52:53], %1 op_sel_hi:[0,1]\n" \
  "v_pk_fma_f32 v[54:55], v[52:53], %1, v[54:55] op_sel:[1,1,0] op_sel_hi:[1,0,1] neg_lo:[0,1,0]\nv_cvt_pk_f16_f32 v67, v52, v54\n" \
  "v_cvt_pk_f16_f32 v71, v53, v55\nds_write_b128 %3, v[64:67] offset:48\nds_write_b128 %3, v[68:71] offset:112\n"
#define Y_CLOB "memory", "v40", "v41", "v42", "v43", "v44", "v45", "v46", "v47", "v48", "v49", "v50", "v51", "v52", "v53", "v54", "v55", "v56", "v57", "v58", "v59", "v60", "v61", "v62", "v63", "v64", "v65", "v66", "v67", "v68", "v69", "v70", "v71"
#define T_ASM \
  "ds_read_b128 %4, %8\nds_read_b64_tr_b16 v[80:81], %7 offset:0\nds_read_b64_tr_b16 v[82:83], %7 offset:64\n" \
  "ds_read_b64_tr_b16 v[84:85], %5 offset:0\nds_read_b64_tr_b16 v[86:87], %5 offset:48\nds_read_b64_tr_b16 v[88:89], %6 offset:0\n" \
  "ds_read_b64_tr_b16 v[90:91], %6 offset:48\nds_read_b64_tr_b16 v[92:93], %7 offset:3584\nds_read_b64_tr_b16 v[94:95], %7 offset:3648\n" \
  "ds_read_b64_tr_b16 v[96:97], %5 offset:3584\nds_read_b64_tr_b16 v[98:99], %5 offset:3632\nds_read_b64_tr_b16 v[100:101], %6 offset:3584\n" \
  "ds_read_b64_tr_b16 v[102:103], %6 offset:3632\nds_read_b64_tr_b16 v[104:105], %7 offset:7168\nds_read_b64_tr_b16 v[106:107], %7 offset:7232\n" \
  "s_waitcnt lgkmcnt(8)\nv_mfma_f32_16x16x32_f16 %0, v[84:87], v[80:83], 0\nds_read_b64_tr_b16 v[108:109], %5 offset:7168\n" \
  "ds_read_b64_tr_b16 v[110:111], %5 offset:7216\nds_read_b64_tr_b16 v[112:113], %6 offset:7168\nv_mfma_f32_16x16x32_f16 %2, v[88:91], v[80:83], 0\n" \
  "ds_read_b64_tr_b16 v[114:115], %6 offset:7216\nds_read_b64_tr_b16 v[116:117], %7 offset:10752\nds_read_b64_tr_b16 v[118:119], %7 offset:10816\n" \
  "s_waitcnt lgkmcnt(8)\nv_mfma_f32_16x16x32_f16 %0, v[96:99], v[92:95], %0\nds_read_b64_tr_b16 v[120:121], %5 offset:10752\n" \
  "ds_read_b64_tr_b16 v[122:123], %5 offset:10800\nds_read_b64_tr_b16 v[124:125], %6 offset:10752\n" \
  "v_mfma_f32_16x16x32_f16 %2, v[100:103], v[92:95], %2\nds_read_b64_tr_b16 v[126:127], %6 offset:10800\n" \
  "ds_read_b64_tr_b16 v[80:81], %7 offset:14336\nds_read_b64_tr_b16 v[82:83], %7 offset:14400\ns_waitcnt lgkmcnt(8)\n" \
  "v_mfma_f32_16x16x32_f16 %0, v[108:111], v[104:107], %0\nds_read_b64_tr_b16 v[84:85], %5 offset:14336\n" \
  "ds_read_b64_tr_b16 v[86:87], %5 offset:14384\nds_read_b64_tr_b16 v[88:89], %6 offset:14336\n" \
  "v_mfma_f32_16x16x32_f16 %2, v[112:115], v[104:107], %2\nds_read_b64_tr_b16 v[90:91], %6 offset:14384\n" \
  "ds_read_b64_tr_b16 v[92:93], %7 offset:17920\nds_read_b64_tr_b16 v[94:95], %7 offset:17984\ns_waitcnt lgkmcnt(8)\n" \
  "v_mfma_f32_16x16x32_f16 %0, v[120:123], v[116:119], %0\nds_read_b64_tr_b16 v[96:97], %5 offset:17920\n" \
  "ds_read_b64_tr_b16 v[98:99], %5 offset:17968\nds_read_b64_tr_b16 v[100:101], %6 offset:17920\n" \
  "v_mfma_f32_16x16x32_f16 %2, v[124:127], v[116:119], %2\nds_read_b64_tr_b16 v[102:103], %6 offset:17968\n" \
  "ds_read_b64_tr_b16 v[104:105], %7 offset:21504\nds_read_b64_tr_b16 v[106:107], %7 offset:21568\ns_waitcnt lgkmcnt(8)\n" \
  "v_mfma_f32_16x16x32_f16 %1, v[84:87], v[80:83], 0\nds_read_b64_tr_b16 v[108:109], %5 offset:21504\nds_read_b64_tr_b16 v[110:111], %5 offset:21552\n" \
  "ds_read_b64_tr_b16 v[112:113], %6 offset:21504\nv_mfma_f32_16x16x32_f16 %3, v[88:91], v[80:83], 0\nds_read_b64_tr_b16 v[114:115], %6 offset:21552\n" \
  "ds_read_b64_tr_b16 v[116:117], %7 offset:25088\nds_read_b64_tr_b16 v[118:119], %7 offset:25152\ns_waitcnt lgkmcnt(8)\n" \
  "v_mfma_f32_16x16x32_f16 %1, v[96:99], v[92:95], %1\nds_read_b64_tr_b16 v[120:121], %5 offset:25088\n" \
  "ds_read_b64_tr_b16 v[122:123], %5 offset:25136\nds_read_b64_tr_b16 v[124:125], %6 offset:25088\n" \
  "v_mfma_f32_16x16x32_f16 %3, v[100:103], v[92:95], %3\nds_read_b64_tr_b16 v[126:127], %6 offset:25136\ns_waitcnt lgkmcnt(6)\n" \
  "v_mfma_f32_16x16x32_f16 %1, v[108:111], v[104:107], %1\nv_mfma_f32_16x16x32_f16 %3, v[112:115], v[104:107], %3\ns_waitcnt lgkmcnt(0)\n" \
  "v_mfma_f32_16x16x32_f16 %1, v[120:123], v[116:119], %1\nv_mfma_f32_16x16x32_f16 %3, v[124:127], v[116:119], %3\n"
#define T_CLOB "memory", "v80", "v81", "v82", "v83", "v84", "v85", "v86", "v87", "v88", "v89", "v90", "v91", "v92", "v93", "v94", "v95", "v96", "v97", "v98", "v99", "v100", "v101", "v102", "v103", "v104", "v105", "v106", "v107", "v108", "v109", "v110", "v111", "v112", "v113", "v114", "v115", "v116", "v117", "v118", "v119", "v120", "v121", "v122", "v123", "v124", "v125", "v126", "v127"
template <class F> __device__ __forceinline__ int gen_y(float ldt, float are, float aim, float bre, float bim, float cre, float cim, uint32_t im, F&& mid TSP) {
  float ar2, air, g;
  const float dt = state_a(ldt, are, aim, ar2, air, g);
  const float dbr = dt * bre, dbi = dt * bim, pr = cre * dbr - cim * dbi, pi = cre * dbi + cim * dbr;
  const f32x2 w8 = cexp2(ar2, -air, 8.f), w64 = cexp2(ar2, -air, 64.f);
  const float pm = fmaxf(fabsf(pr), fabsf(pi));
  int e = -4;
  if (__builtin_expect(__any(!(pm < 0x1p-4f) || ar2 > 0.f) || !__any(pm > 0x1p-12f), 0)) e = (int)(wave_maxu(__float_as_uint(pm)) >> 23) - 126 + (int)ceilf(g * 250.f);
  const f32x2 y0 = {__builtin_amdgcn_ldexpf(pr, 14 - e), __builtin_amdgcn_ldexpf(-pi, 14 - e)};
  mid();
  asm volatile(Y_ASM : : "v"(y0), "v"(w8), "v"(w64), "v"(im) : Y_CLOB);
  return e;
}
__device__ __forceinline__ int gen_x(float ldt, float are, float aim, char* im) {
  float ar2, air, g;
  state_a(ldt, are, aim, ar2, air, g);
  const int e = min((int)ceilf(g * 1810.f), 300);
  const float s = __builtin_amdgcn_ldexpf(1.f, 15 - e);
  const f32x2 w1 = cexp2(ar2, air, 1.f), w256 = cexp2(ar2, air, 256.f), w2 = cmul(w1, w1);
  f32x2 a[8], b[8], c[8];
  a[0] = (f32x2){s, 0.f}; a[1] = w1 * s; a[2] = w2 * s;
  b[0] = b[1] = w256 * s;
  b[2] = cmul(b[0], w2); a[3] = cmul(a[1], w2); a[4] = cmul(a[2], w2);
  b[3] = b[4] = b[5] = cmul(b[2], w2); a[5] = cmul(a[3], w2); a[6] = cmul(a[4], w2);
  b[6] = cmul(b[3], w2); a[7] = cmul(a[5], w2); c[0] = cmul(b[3], w256);
  b[7] = cmul(b[6], w2); c[2] = cmul(c[0], w256);
  put_im<0>(im + I_X, 48, a);
  c[1] = cmul(b[7], w256); c[3] = cmul(c[1], w256);
  put_im<0>(im + I_X + 16, 48, b);
#pragma unroll
  for (int k = 4; k < 8; ++k) c[k] = cmul(c[k - 1], w256);
  put_im<0>(im + I_X + 32, 48, c);
  return e;
}

template <int I> struct IC { static constexpr int value = I; };
template <int B, int E, class F> __device__ __forceinline__ void static_for(F&& f) {
  if constexpr (B < E) { f(IC<B>{}); static_for<B + 1, E>(f); }
}
typedef __attribute__((ext_vector_type(4))) short s16x4;
__device__ __forceinline__ u32x2 trd(uint32_t a) {
  return __builtin_bit_cast(u32x2, __builtin_amdgcn_ds_read_tr16_b64_v4i16((__attribute__((address_space(3))) s16x4*)(uintptr_t)a));
}
#define BAR() asm volatile("s_waitcnt lgkmcnt(0)\n\ts_barrier" ::: "memory")
#define ST16(off, v) asm volatile("global_store_dwordx4 %0, %1, %2 sc1\n\ts_nop 1" : : "v"(off), "v"(v), "s"(ws) : "memory")
#define ST4(off, v) asm volatile("global_store_dword %0, %1, %2 sc1" : : "v"(off), "v"(v), "s"(ws) : "memory")
#define LD16(r, off) asm volatile("global_load_dwordx4 %0, %1, %2 sc1" : "=v"(r) : "v"(off), "s"(ws) : "memory")
#define LD16W(r, off) asm volatile("global_load_dwordx4 %0, %1, %2 sc1\n\ts_waitcnt vmcnt(0)" : "=&v"(r) : "v"(off), "s"(ws) : "memory")
#define DRAIN() asm volatile("s_waitcnt vmcnt(0)" ::: "memory")
#define CEX(a, b) { const uint32_t hi_ = max(a, b), lo_ = min(a, b); a = hi_; b = lo_; }
#define MMB(a_, b_, c_) __builtin_amdgcn_mfma_f32_16x16x32_bf16(__builtin_bit_cast(bf16x8, a_), __builtin_bit_cast(bf16x8, b_), c_, 0, 0, 0)
#define NTM 0
#define DPOL ""
#define DPOL2 ""
#define DPOL4 ""
constexpr int SPIN_CAP = 1 << 18;
constexpr uint32_t UMASK = 0x40074007u;
__device__ __forceinline__ uint32_t utag(uint32_t T, int j) {
  return ((T >> (3 * j)) & 0x00070007u) | (((j <= 2) ? (T << (2 - j)) : (T >> (j - 2))) & 0x40004000u);
}
__device__ __forceinline__ uint32_t max4(const u32x4& a) { return max(max(a[0], a[1]), max(a[2], a[3])); }
__device__ __forceinline__ void until(uint32_t t) {
#pragma unroll 1
  for (int spin = 0; spin < 600 && (int)((uint32_t)__builtin_amdgcn_s_memrealtime() - t) < 0; ++spin) __builtin_amdgcn_s_sleep(1);
}
__device__ __forceinline__ uint32_t latest(uint32_t t) {
  const uint32_t t0 = (uint32_t)__builtin_amdgcn_readfirstlane(t) - 0x40000000u;
  return wave_maxu(t - t0) + t0;
}
__device__ __forceinline__ uint32_t gate(const char* ws, uint32_t po, uint32_t T) {
  u32x2 pv;
#pragma unroll 1
  for (int spin = 0; spin < SPIN_CAP; ++spin) {
    asm volatile("global_load_dwordx2 %0, %1, %2 sc1\n\ts_waitcnt vmcnt(0)" : "=&v"(pv) : "v"(po), "s"(ws) : "memory");
    if (!__any(pv[0] != T)) break;
    __builtin_amdgcn_s_sleep(2);
  }
  return pv[1];
}
__device__ __forceinline__ uint32_t gate2(const char* ws, uint32_t oa, uint32_t da, uint32_t ob, uint32_t T) {
  u32x2 pa, pb;
#pragma unroll 1
  for (int spin = 0; spin < SPIN_CAP; ++spin) {
    asm volatile("global_load_dwordx2 %0, %2, %4 sc1\n\tglobal_load_dwordx2 %1, %3, %4 sc1\n\ts_waitcnt vmcnt(0)" : "=&v"(pa), "=&v"(pb) : "v"(oa), "v"(ob), "s"(ws) : "memory");
    if (!__any(pa[0] != T || pb[0] != T)) break;
    __builtin_amdgcn_s_sleep(4);
  }
  pa[1] += da;
  return latest(((int)(pa[1] - pb[1]) > 0) ? pa[1] : pb[1]);
}
__device__ __forceinline__ void gsync(uint32_t a, uint32_t target) {
  uint32_t t, c, n = SPIN_CAP;
  uint64_t ex;
  asm volatile("s_waitcnt lgkmcnt(0)\n\ts_mov_b64 %3, exec\n\ts_mov_b64 exec, 1\n\tv_mov_b32 %0, 1\n\tds_add_u32 %4, %0\n\ts_mov_b64 exec, %3\n"
               "1:\n\ts_sleep 1\n\tds_read_b32 %0, %4\n\ts_sub_u32 %2, %2, 1\n\ts_cbranch_scc1 2f\n\ts_waitcnt lgkmcnt(0)\n\tv_readfirstlane_b32 %1, %0\n\t"
               "s_cmp_lt_u32 %1, %5\n\ts_cbranch_scc1 1b\n2:\n\ts_waitcnt lgkmcnt(0)"
               : "=&v"(t), "=&s"(c), "+s"(n), "=&s"(ex) : "v"(a), "n"(target) : "memory", "scc");
}
__device__ __forceinline__ void gsync0(uint32_t a, uint32_t target) {
  uint32_t t, c, n = SPIN_CAP;
  asm volatile("s_waitcnt lgkmcnt(0)\n\ts_mov_b64 exec, 1\n\tv_mov_b32 %0, 1\n\tds_add_u32 %3, %0\n\ts_mov_b64 exec, -1\n"
               "1:\n\tds_read_b32 %0, %3\n\ts_sub_u32 %2, %2, 1\n\ts_cbranch_scc1 2f\n\ts_waitcnt lgkmcnt(0)\n\tv_readfirstlane_b32 %1, %0\n\t"
               "s_cmp_lt_u32 %1, %4\n\ts_cbranch_scc1 1b\n2:\n\ts_waitcnt lgkmcnt(0)"
               : "=&v"(t), "=&s"(c), "+s"(n) : "v"(a), "n"(target) : "memory", "scc");
}
__device__ __forceinline__ void gwait(uint32_t a, uint32_t target) {
  uint32_t t, c, n = SPIN_CAP;
  asm volatile("1:\n\ts_sleep 2\n\tds_read_b32 %0, %3\n\ts_sub_u32 %2, %2, 1\n\ts_cbranch_scc1 2f\n\ts_waitcnt lgkmcnt(0)\n\tv_readfirstlane_b32 %1, %0\n\t"
               "s_cmp_lt_u32 %1, %4\n\ts_cbranch_scc1 1b\n2:\n\ts_waitcnt lgkmcnt(0)"
               : "=&v"(t), "=&s"(c), "+s"(n) : "v"(a), "n"(target) : "memory", "scc");
}
__device__ __forceinline__ void gwait0(uint32_t a, uint32_t target) {
  uint32_t t, c, n = SPIN_CAP;
  asm volatile("1:\n\tds_read_b32 %0, %3\n\ts_sub_u32 %2, %2, 1\n\ts_cbranch_scc1 2f\n\ts_waitcnt lgkmcnt(0)\n\tv_readfirstlane_b32 %1, %0\n\t"
               "s_cmp_lt_u32 %1, %4\n\ts_cbranch_scc1 1b\n2:\n\ts_waitcnt lgkmcnt(0)"
               : "=&v"(t), "=&s"(c), "+s"(n) : "v"(a), "n"(target) : "memory", "scc");
}
__device__ __forceinline__ void dma16(const void* base, uint32_t voff, uint32_t lds) {
  uint32_t keep;
  asm volatile("s_mov_b32 %0, m0\n\ts_mov_b32 m0, %3\n\ts_nop 0\n\tglobal_load_lds_dwordx4 %1, %2" DPOL "\n\ts_mov_b32 m0, %0" : "=&s"(keep) : "v"(voff), "s"(base), "s"(lds) : "memory");
}
__device__ __forceinline__ void dma16d(const void* base, uint32_t voff, uint32_t lds) {
  uint32_t keep;
  asm volatile("s_mov_b32 %0, m0\n\ts_mov_b32 m0, %3\n\ts_nop 0\n\tglobal_load_lds_dwordx4 %1, %2" DPOL2 "\n\ts_mov_b32 m0, %0" : "=&s"(keep) : "v"(voff), "s"(base), "s"(lds) : "memory");
}
__device__ __forceinline__ void nap(int n) {
  for (; n > 15; n -= 15) __builtin_amdgcn_s_sleep(15);
  switch (n) {
    case 1: __builtin_amdgcn_s_sleep(1); break; case 2: __builtin_amdgcn_s_sleep(2); break; case 3: __builtin_amdgcn_s_sleep(3); break;
    case 4: __builtin_amdgcn_s_sleep(4); break; case 5: __builtin_amdgcn_s_sleep(5); break; case 6: __builtin_amdgcn_s_sleep(6); break;
    case 7: __builtin_amdgcn_s_sleep(7); break; case 8: __builtin_amdgcn_s_sleep(8); break; case 9: __builtin_amdgcn_s_sleep(9); break;
    case 10: __builtin_amdgcn_s_sleep(10); break; case 11: __builtin_amdgcn_s_sleep(11); break; case 12: __builtin_amdgcn_s_sleep(12); break;
    case 13: __builtin_amdgcn_s_sleep(13); break; case 14: __builtin_amdgcn_s_sleep(14); break; case 15: __builtin_amdgcn_s_sleep(15); break;
    default: break;
  }
}
#define SLEEP(n) do { if ((n) > 0) __builtin_amdgcn_s_sleep(n); } while (0)
#define USLEEP 3
#define SSLEEP 11
#define PSLEEP 2
#define PPRIO 0
#define LFIRST 1
#define WD 40
#define OSLEEP 12
#define QPRIO 1
#define KPRIO 2
#define GSLEEP 0
#define NSLEEP 0
#define UD 40
#define XD 0
#define RSLEEP 0
#define UPRIO 2
#define WDEPTH 4
#define ESLEEP 0
#define SD 58
#define OD 0
#define KBACK 6
#define DDEPTH 6
#define C3 1
#define XOFF 0
#define XROT 2
#define WSOFF 0
#define T0 0
#define TB1 0
#define T0SLEEP 0
#define KN 2
#define RPF 1
#define TSTOP 70
#define HSTOP 110
#define VEARLY 0ull
#define FPR 3
#define SPR 3
#define DPRIO 0
#define T1 0
#define T1SLEEP 0
#define QPRIO2 3
#define HW 2
#define RD 62
#define OPOS 1
#define OD2 0
#define NA 12
#define RD2 88
#define ORD1 ((unsigned __int128)0x321087654u << 36 | 0x321087654ull)
#define WDEPTH2 4
#define RQ NA
#define HREV 1
#define QPRIO0 3
#define VPS 1
#define RW 1
#define NPRIO 0
#define XSL 10
#define XF 0
#define N0 8
#define FORCESLOW 0
#define FORCEFB 0
#define CONV_ASM \
  "s_cmp_eq_u32 s89, 1\ns_cbranch_scc1 1f\ns_cmp_gt_u32 s89, 1\ns_cbranch_scc1 9f\nds_read_b64 v[124:125], v3 offset:576\n" \
  "ds_read_b64 v[126:127], v3 offset:584\nds_read_b64 v[120:121], v3 offset:64\nds_read_b64 v[122:123], v3 offset:72\n" \
  "ds_read_b64 v[116:117], v3 offset:640\nds_read_b64 v[118:119], v3 offset:648\nds_read_b64 v[112:113], v3 offset:128\n" \
  "ds_read_b64 v[114:115], v3 offset:136\nds_read_b64 v[108:109], v3 offset:704\nds_read_b64 v[110:111], v3 offset:712\n" \
  "ds_read_b64 v[104:105], v3 offset:192\nds_read_b64 v[106:107], v3 offset:200\nds_read_b64 v[100:101], v3 offset:768\n" \
  "ds_read_b64 v[102:103], v3 offset:776\nds_read_b64 v[96:97], v3 offset:256\nds_read_b64 v[98:99], v3 offset:264\n" \
  "ds_read_b64 v[92:93], v3 offset:832\nds_read_b64 v[94:95], v3 offset:840\nds_read_b64 v[88:89], v3 offset:320\n" \
  "ds_read_b64 v[90:91], v3 offset:328\nds_read_b64 v[84:85], v3 offset:896\nds_read_b64 v[86:87], v3 offset:904\n" \
  "ds_read_b64 v[80:81], v3 offset:384\nds_read_b64 v[82:83], v3 offset:392\nds_read_b64 v[76:77], v3 offset:960\n" \
  "ds_read_b64 v[78:79], v3 offset:968\nds_read_b64 v[72:73], v3 offset:448\nds_read_b64 v[74:75], v3 offset:456\n" \
  "ds_read_b64 v[68:69], v3 offset:1024\nds_read_b64 v[70:71], v3 offset:1032\nds_read_b64 v[64:65], v3 offset:512\n" \
  "ds_read_b64 v[66:67], v3 offset:520\nds_read_b64 v[60:61], v3 offset:0\nds_read_b64 v[62:63], v3 offset:8\nds_read_b64 v[56:57], v3 offset:1088\n" \
  "ds_read_b64 v[58:59], v3 offset:1096\ns_waitcnt lgkmcnt(0)\ns_barrier\nds_read_b128 v[36:39], v2 offset:0\nds_read_b128 v[40:43], v2 offset:64\n" \
  "ds_read_b128 v[44:47], v2 offset:128\nds_read_b128 v[48:51], v2 offset:192\ns_waitcnt lgkmcnt(3)\n" \
  "v_mfma_f32_16x16x32_f16 v[12:15], v[124:127], v[36:39], 0\ns_setprio 1\nds_read_b128 v[52:55], v2 offset:256\n" \
  "v_mfma_f32_16x16x32_f16 v[16:19], v[120:123], v[36:39], 0\ns_waitcnt lgkmcnt(3)\nv_mfma_f32_16x16x32_f16 v[12:15], v[116:119], v[40:43], v[12:15]\n" \
  "ds_read_b128 v[36:39], v2 offset:320\nv_mfma_f32_16x16x32_f16 v[16:19], v[112:115], v[40:43], v[16:19]\ns_waitcnt lgkmcnt(3)\n" \
  "v_mfma_f32_16x16x32_f16 v[12:15], v[108:111], v[44:47], v[12:15]\nds_read_b128 v[40:43], v2 offset:384\n" \
  "v_mfma_f32_16x16x32_f16 v[16:19], v[104:107], v[44:47], v[16:19]\ns_waitcnt lgkmcnt(3)\n" \
  "v_mfma_f32_16x16x32_f16 v[12:15], v[100:103], v[48:51], v[12:15]\nds_read_b128 v[44:47], v2 offset:448\n" \
  "v_mfma_f32_16x16x32_f16 v[16:19], v[96:99], v[48:51], v[16:19]\ns_waitcnt lgkmcnt(3)\n" \
  "v_mfma_f32_16x16x32_f16 v[12:15], v[92:95], v[52:55], v[12:15]\nv_mfma_f32_16x16x32_f16 v[16:19], v[88:91], v[52:55], v[16:19]\n" \
  "s_waitcnt lgkmcnt(2)\nv_mfma_f32_16x16x32_f16 v[12:15], v[84:87], v[36:39], v[12:15]\nds_read_b128 v[48:51], v2 offset:512\n" \
  "v_mfma_f32_16x16x32_f16 v[16:19], v[80:83], v[36:39], v[16:19]\ns_waitcnt lgkmcnt(2)\n" \
  "v_mfma_f32_16x16x32_f16 v[12:15], v[76:79], v[40:43], v[12:15]\nds_read_b128 v[52:55], v2 offset:576\n" \
  "v_mfma_f32_16x16x32_f16 v[16:19], v[72:75], v[40:43], v[16:19]\ns_waitcnt lgkmcnt(2)\n" \
  "v_mfma_f32_16x16x32_f16 v[12:15], v[68:71], v[44:47], v[12:15]\nds_read_b128 v[36:39], v2 offset:640\n" \
  "v_mfma_f32_16x16x32_f16 v[16:19], v[64:67], v[44:47], v[16:19]\nv_mfma_f32_16x16x32_f16 v[20:23], v[60:63], v[44:47], 0\n" \
  "ds_read_b128 v[40:43], v2 offset:704\nv_mfma_f32_16x16x32_f16 v[28:31], v[56:59], v[44:47], 0\ns_waitcnt lgkmcnt(3)\n" \
  "v_mfma_f32_16x16x32_f16 v[16:19], v[124:127], v[48:51], v[16:19]\nds_read_b128 v[56:59], v2 offset:768\nv_cvt_pkrtz_f16_f32 v32, v12, v13\n" \
  "v_mfma_f32_16x16x32_f16 v[20:23], v[120:123], v[48:51], v[20:23]\nv_cvt_pkrtz_f16_f32 v33, v14, v15\ns_waitcnt lgkmcnt(3)\n" \
  "v_mfma_f32_16x16x32_f16 v[16:19], v[116:119], v[52:55], v[16:19]\nds_read_b128 v[44:47], v2 offset:832\n" \
  "v_pk_add_u16 v32, v32, 4 op_sel_hi:[1,0] clamp\nv_mfma_f32_16x16x32_f16 v[20:23], v[112:115], v[52:55], v[20:23]\n" \
  "v_pk_add_u16 v33, v33, 4 op_sel_hi:[1,0] clamp\ns_waitcnt lgkmcnt(3)\nv_mfma_f32_16x16x32_f16 v[16:19], v[108:111], v[36:39], v[16:19]\n" \
  "ds_read_b128 v[48:51], v2 offset:896\nds_write_b64 v4, v[32:33] offset:0\nv_mfma_f32_16x16x32_f16 v[20:23], v[104:107], v[36:39], v[20:23]\n" \
  "s_waitcnt lgkmcnt(3)\nv_mfma_f32_16x16x32_f16 v[16:19], v[100:103], v[40:43], v[16:19]\nds_read_b128 v[52:55], v2 offset:960\n" \
  "v_mfma_f32_16x16x32_f16 v[20:23], v[96:99], v[40:43], v[20:23]\nv_mfma_f32_16x16x32_f16 v[16:19], v[92:95], v[56:59], v[16:19]\n" \
  "v_mfma_f32_16x16x32_f16 v[20:23], v[88:91], v[56:59], v[20:23]\nds_read_b128 v[36:39], v2 offset:1024\ns_waitcnt lgkmcnt(3)\n" \
  "v_mfma_f32_16x16x32_f16 v[16:19], v[84:87], v[44:47], v[16:19]\nv_mfma_f32_16x16x32_f16 v[20:23], v[80:83], v[44:47], v[20:23]\n" \
  "ds_read_b128 v[40:43], v2 offset:1088\nds_read_b64 v[56:57], v6 offset:256\nds_read_b64 v[58:59], v6 offset:264\n" \
  "v_mfma_f32_16x16x32_f16 v[16:19], v[76:79], v[48:51], v[16:19]\nv_mfma_f32_16x16x32_f16 v[20:23], v[72:75], v[48:51], v[20:23]\n" \
  "ds_read_b128 v[44:47], v2 offset:1152\ns_waitcnt lgkmcnt(4)\nv_mfma_f32_16x16x32_f16 v[16:19], v[68:71], v[52:55], v[16:19]\n" \
  "v_mfma_f32_16x16x32_f16 v[20:23], v[64:67], v[52:55], v[20:23]\nds_read_b128 v[48:51], v2 offset:1216\n" \
  "v_mfma_f32_16x16x32_f16 v[24:27], v[60:63], v[52:55], 0\nv_mfma_f32_16x16x32_f16 v[20:23], v[124:127], v[36:39], v[20:23]\n" \
  "ds_read_b128 v[52:55], v2 offset:1280\nv_mfma_f32_16x16x32_f16 v[24:27], v[120:123], v[36:39], v[24:27]\ns_waitcnt lgkmcnt(5)\n" \
  "v_mfma_f32_16x16x32_f16 v[20:23], v[116:119], v[40:43], v[20:23]\nds_read_b128 v[36:39], v2 offset:1344\nv_cvt_pkrtz_f16_f32 v32, v16, v17\n" \
  "v_mfma_f32_16x16x32_f16 v[24:27], v[112:115], v[40:43], v[24:27]\nv_cvt_pkrtz_f16_f32 v33, v18, v19\ns_waitcnt lgkmcnt(3)\n" \
  "v_mfma_f32_16x16x32_f16 v[20:23], v[108:111], v[44:47], v[20:23]\nds_read_b128 v[40:43], v2 offset:1408\n" \
  "v_pk_add_u16 v32, v32, 4 op_sel_hi:[1,0] clamp\nds_read_b32 v35, v9 offset:32\nv_mfma_f32_16x16x32_f16 v[24:27], v[104:107], v[44:47], v[24:27]\n" \
  "v_pk_add_u16 v33, v33, 4 op_sel_hi:[1,0] clamp\ns_waitcnt lgkmcnt(3)\nv_mfma_f32_16x16x32_f16 v[20:23], v[100:103], v[48:51], v[20:23]\n" \
  "ds_read_b128 v[44:47], v2 offset:1472\nds_write_b64 v4, v[32:33] offset:1024\nv_mfma_f32_16x16x32_f16 v[24:27], v[96:99], v[48:51], v[24:27]\n" \
  "v_mfma_f32_16x16x32_f16 v[20:23], v[92:95], v[52:55], v[20:23]\nv_mfma_f32_16x16x32_f16 v[24:27], v[88:91], v[52:55], v[24:27]\n" \
  "ds_read_b128 v[48:51], v2 offset:1536\ns_waitcnt lgkmcnt(3)\nv_readfirstlane_b32 s85, v35\ns_cmp_lt_u32 s85, 4\ns_cbranch_scc1 21f\n22:\n" \
  "v_mfma_f32_16x16x32_f16 v[20:23], v[84:87], v[36:39], v[20:23]\nv_mfma_f32_16x16x32_f16 v[24:27], v[80:83], v[36:39], v[24:27]\n" \
  "ds_read_b128 v[52:55], v2 offset:1600\nv_mfma_f32_16x16x32_f16 v[20:23], v[76:79], v[40:43], v[20:23]\n" \
  "v_mfma_f32_16x16x32_f16 v[24:27], v[72:75], v[40:43], v[24:27]\nds_read_b128 v[36:39], v2 offset:1664\ns_waitcnt lgkmcnt(4)\n" \
  "v_mfma_f32_16x16x32_f16 v[20:23], v[68:71], v[44:47], v[20:23]\nv_mfma_f32_16x16x32_f16 v[24:27], v[64:67], v[44:47], v[24:27]\n" \
  "ds_read_b128 v[40:43], v2 offset:1728\nv_mfma_f32_16x16x32_f16 v[28:31], v[60:63], v[44:47], v[28:31]\nds_read_b64_tr_b16 v[60:61], v5 offset:0\n" \
  "ds_read_b64_tr_b16 v[62:63], v5 offset:32\ns_waitcnt lgkmcnt(4)\nv_mfma_f32_16x16x32_f16 v[24:27], v[124:127], v[48:51], v[24:27]\n" \
  "v_mfma_f32_16x16x32_f16 v[28:31], v[120:123], v[48:51], v[28:31]\nds_read_b64_tr_b16 v[44:45], v5 offset:256\n" \
  "ds_read_b64_tr_b16 v[46:47], v5 offset:288\nv_mfma_f32_16x16x32_f16 v[24:27], v[116:119], v[52:55], v[24:27]\n" \
  "ds_read_b64 v[120:121], v6 offset:320\nds_read_b64 v[122:123], v6 offset:328\nv_mfma_f32_16x16x32_f16 v[28:31], v[112:115], v[52:55], v[28:31]\n" \
  "ds_read_b128 v[48:51], v2 offset:1792\ns_waitcnt lgkmcnt(7)\nv_mfma_f32_16x16x32_f16 v[24:27], v[108:111], v[36:39], v[24:27]\n" \
  "ds_read_b128 v[112:115], v2 offset:1856\nv_mfma_f32_16x16x32_f16 v[28:31], v[104:107], v[36:39], v[28:31]\nds_read_b64 v[52:53], v6 offset:128\n" \
  "ds_read_b64 v[54:55], v6 offset:136\nv_mfma_f32_16x16x32_f16 v[24:27], v[100:103], v[40:43], v[24:27]\nds_read_b128 v[104:107], v2 offset:1920\n" \
  "v_mfma_f32_16x16x32_f16 v[28:31], v[96:99], v[40:43], v[28:31]\nds_read_b64 v[36:37], v6 offset:192\nds_read_b64 v[38:39], v6 offset:200\n" \
  "s_waitcnt lgkmcnt(9)\nv_mfma_f32_16x16x32_f16 v[20:23], v[60:63], v[56:59], v[20:23]\nds_read_b128 v[96:99], v2 offset:1984\ns_waitcnt lgkmcnt(7)\n" \
  "v_mfma_f32_16x16x32_f16 v[20:23], v[44:47], v[120:123], v[20:23]\nv_mfma_f32_16x16x32_f16 v[24:27], v[92:95], v[48:51], v[24:27]\n" \
  "ds_read_b128 v[40:43], v2 offset:2048\nv_mfma_f32_16x16x32_f16 v[28:31], v[88:91], v[48:51], v[28:31]\nds_read_b64_tr_b16 v[88:89], v5 offset:512\n" \
  "ds_read_b64_tr_b16 v[90:91], v5 offset:544\ns_waitcnt lgkmcnt(6)\nv_mfma_f32_16x16x32_f16 v[24:27], v[84:87], v[112:115], v[24:27]\n" \
  "v_mfma_f32_16x16x32_f16 v[28:31], v[80:83], v[112:115], v[28:31]\nv_cvt_pkrtz_f16_f32 v32, v20, v21\nv_cvt_pkrtz_f16_f32 v33, v22, v23\n" \
  "v_mfma_f32_16x16x32_f16 v[24:27], v[76:79], v[104:107], v[24:27]\nds_read_b64_tr_b16 v[48:49], v5 offset:768\n" \
  "ds_read_b64_tr_b16 v[50:51], v5 offset:800\nv_mfma_f32_16x16x32_f16 v[28:31], v[72:75], v[104:107], v[28:31]\n" \
  "ds_read_b128 v[80:83], v2 offset:2112\ns_waitcnt lgkmcnt(5)\nv_mfma_f32_16x16x32_f16 v[24:27], v[68:71], v[96:99], v[24:27]\n" \
  "ds_read_b128 v[112:115], v2 offset:2176\nv_pk_add_u16 v32, v32, 4 op_sel_hi:[1,0] clamp\n" \
  "v_mfma_f32_16x16x32_f16 v[28:31], v[64:67], v[96:99], v[28:31]\nds_read_b128 v[72:75], v2 offset:2240\n" \
  "v_pk_add_u16 v33, v33, 4 op_sel_hi:[1,0] clamp\nv_mfma_f32_16x16x32_f16 v[28:31], v[124:127], v[40:43], v[28:31]\n" \
  "ds_read_b128 v[104:107], v2 offset:2304\nds_write_b64 v4, v[32:33] offset:2048\nv_mfma_f32_16x16x32_f16 v[24:27], v[60:63], v[52:55], v[24:27]\n" \
  "ds_read_b128 v[64:67], v2 offset:2368\nds_read_b64 v[96:97], v6 offset:0\nds_read_b64 v[98:99], v6 offset:8\n" \
  "v_mfma_f32_16x16x32_f16 v[24:27], v[44:47], v[36:39], v[24:27]\nds_read_b128 v[124:127], v2 offset:2432\ns_waitcnt lgkmcnt(9)\n" \
  "v_mfma_f32_16x16x32_f16 v[24:27], v[88:91], v[56:59], v[24:27]\nds_read_b128 v[40:43], v2 offset:2496\n" \
  "v_mfma_f32_16x16x32_f16 v[24:27], v[48:51], v[120:123], v[24:27]\ns_waitcnt lgkmcnt(8)\n" \
  "v_mfma_f32_16x16x32_f16 v[28:31], v[116:119], v[80:83], v[28:31]\nds_read_b64 v[116:117], v6 offset:64\nds_read_b64 v[118:119], v6 offset:72\n" \
  "v_mfma_f32_16x16x32_f16 v[28:31], v[108:111], v[112:115], v[28:31]\nds_read_b64_tr_b16 v[80:81], v5 offset:1024\n" \
  "ds_read_b64_tr_b16 v[82:83], v5 offset:1056\ns_waitcnt lgkmcnt(10)\nv_mfma_f32_16x16x32_f16 v[28:31], v[100:103], v[72:75], v[28:31]\n" \
  "v_cvt_pkrtz_f16_f32 v32, v24, v25\nv_cvt_pkrtz_f16_f32 v33, v26, v27\nv_mfma_f32_16x16x32_f16 v[28:31], v[92:95], v[104:107], v[28:31]\n" \
  "v_pk_add_u16 v32, v32, 4 op_sel_hi:[1,0] clamp\ns_waitcnt lgkmcnt(5)\nv_mfma_f32_16x16x32_f16 v[28:31], v[84:87], v[64:67], v[28:31]\n" \
  "ds_read_b64_tr_b16 v[108:109], v5 offset:1280\nds_read_b64_tr_b16 v[110:111], v5 offset:1312\n" \
  "v_mfma_f32_16x16x32_f16 v[28:31], v[76:79], v[124:127], v[28:31]\nds_read_b64_tr_b16 v[112:113], v7 offset:0\n" \
  "ds_read_b64_tr_b16 v[114:115], v7 offset:64\ns_waitcnt lgkmcnt(6)\nv_mfma_f32_16x16x32_f16 v[28:31], v[68:71], v[40:43], v[28:31]\n" \
  "ds_read2_b32 v[100:101], v8 offset0:48 offset1:49\nds_read2_b32 v[102:103], v8 offset0:50 offset1:51\n" \
  "v_mfma_f32_16x16x32_f16 v[28:31], v[60:63], v[96:99], v[28:31]\nv_pk_add_u16 v33, v33, 4 op_sel_hi:[1,0] clamp\n" \
  "ds_write_b64 v4, v[32:33] offset:3072\nv_mfma_f32_16x16x32_f16 v[28:31], v[44:47], v[116:119], v[28:31]\n" \
  "v_mfma_f32_16x16x32_f16 v[28:31], v[88:91], v[52:55], v[28:31]\nv_mfma_f32_16x16x32_f16 v[28:31], v[48:51], v[36:39], v[28:31]\n" \
  "s_waitcnt lgkmcnt(5)\nv_mfma_f32_16x16x32_f16 v[28:31], v[80:83], v[56:59], v[28:31]\n" \
  "v_mfma_f32_16x16x32_f16 v[28:31], v[108:111], v[120:123], v[28:31]\ns_waitcnt lgkmcnt(1)\n" \
  "v_mfma_f32_16x16x32_f16 v[28:31], v[112:115], v[100:103], v[28:31]\ns_nop 7\nv_cvt_pkrtz_f16_f32 v32, v28, v29\nv_cvt_pkrtz_f16_f32 v33, v30, v31\n" \
  "v_pk_add_u16 v32, v32, 4 op_sel_hi:[1,0] clamp\nv_pk_add_u16 v33, v33, 4 op_sel_hi:[1,0] clamp\nds_write_b64 v4, v[32:33] offset:4096\n" \
  "s_setprio 0\ns_setpc_b64 s[90:91]\n21:\ns_movk_i32 s84, 0x4000\n23:\nds_read_b32 v35, v9 offset:32\ns_sub_u32 s84, s84, 1\ns_cbranch_scc1 22b\n" \
  "s_waitcnt lgkmcnt(0)\nv_readfirstlane_b32 s85, v35\ns_cmp_lt_u32 s85, 4\ns_cbranch_scc1 23b\ns_branch 22b\n1:\n" \
  "ds_read_b64 v[124:125], v3 offset:1088\nds_read_b64 v[126:127], v3 offset:1096\nds_read_b64 v[120:121], v3 offset:0\n" \
  "ds_read_b64 v[122:123], v3 offset:8\nds_read_b64 v[116:117], v3 offset:64\nds_read_b64 v[118:119], v3 offset:72\n" \
  "ds_read_b64 v[112:113], v3 offset:128\nds_read_b64 v[114:115], v3 offset:136\nds_read_b64 v[108:109], v3 offset:192\n" \
  "ds_read_b64 v[110:111], v3 offset:200\nds_read_b64 v[104:105], v3 offset:256\nds_read_b64 v[106:107], v3 offset:264\n" \
  "ds_read_b64 v[100:101], v3 offset:320\nds_read_b64 v[102:103], v3 offset:328\nds_read_b64 v[96:97], v3 offset:384\n" \
  "ds_read_b64 v[98:99], v3 offset:392\nds_read_b64 v[92:93], v3 offset:448\nds_read_b64 v[94:95], v3 offset:456\n" \
  "ds_read_b64 v[88:89], v3 offset:512\nds_read_b64 v[90:91], v3 offset:520\nds_read_b64 v[84:85], v3 offset:576\n" \
  "ds_read_b64 v[86:87], v3 offset:584\nds_read_b64 v[80:81], v3 offset:640\nds_read_b64 v[82:83], v3 offset:648\n" \
  "ds_read_b64 v[76:77], v3 offset:704\nds_read_b64 v[78:79], v3 offset:712\nds_read_b64 v[72:73], v3 offset:768\n" \
  "ds_read_b64 v[74:75], v3 offset:776\nds_read_b64 v[68:69], v3 offset:832\nds_read_b64 v[70:71], v3 offset:840\n" \
  "ds_read_b64 v[64:65], v3 offset:896\nds_read_b64 v[66:67], v3 offset:904\nds_read_b64 v[60:61], v3 offset:960\n" \
  "ds_read_b64 v[62:63], v3 offset:968\nds_read_b64 v[56:57], v3 offset:1024\nds_read_b64 v[58:59], v3 offset:1032\n" \
  "ds_read_b64 v[52:53], v6 offset:0\nds_read_b64 v[54:55], v6 offset:8\nds_read_b64 v[48:49], v6 offset:64\nds_read_b64 v[50:51], v6 offset:72\n" \
  "s_waitcnt lgkmcnt(0)\ns_barrier\nds_read_b128 v[28:31], v2 offset:960\nds_read_b128 v[32:35], v2 offset:1472\n" \
  "ds_read_b128 v[36:39], v2 offset:1984\nds_read_b128 v[40:43], v2 offset:2048\nds_read_b128 v[44:47], v2 offset:2112\ns_waitcnt lgkmcnt(3)\n" \
  "v_mfma_f32_16x16x32_f16 v[12:15], v[124:127], v[28:31], 0\ns_setprio 1\nds_read_b128 v[28:31], v2 offset:2176\n" \
  "v_mfma_f32_16x16x32_f16 v[16:19], v[124:127], v[32:35], 0\nds_read_b128 v[32:35], v2 offset:2240\ns_waitcnt lgkmcnt(3)\n" \
  "v_mfma_f32_16x16x32_f16 v[12:15], v[120:123], v[36:39], v[12:15]\nv_mfma_f32_16x16x32_f16 v[20:23], v[124:127], v[36:39], 0\n" \
  "ds_read_b128 v[124:127], v2 offset:2304\nds_read_b128 v[36:39], v2 offset:2368\nv_mfma_f32_16x16x32_f16 v[12:15], v[116:119], v[40:43], v[12:15]\n" \
  "ds_read_b128 v[40:43], v2 offset:2432\ns_waitcnt lgkmcnt(4)\nv_mfma_f32_16x16x32_f16 v[12:15], v[112:115], v[44:47], v[12:15]\n" \
  "ds_read_b128 v[44:47], v2 offset:2496\nv_mfma_f32_16x16x32_f16 v[12:15], v[108:111], v[28:31], v[12:15]\nds_read_b128 v[28:31], v2 offset:2560\n" \
  "s_waitcnt lgkmcnt(4)\nv_mfma_f32_16x16x32_f16 v[12:15], v[104:107], v[32:35], v[12:15]\nds_read_b128 v[32:35], v2 offset:2624\n" \
  "v_mfma_f32_16x16x32_f16 v[12:15], v[100:103], v[124:127], v[12:15]\ns_waitcnt lgkmcnt(3)\n" \
  "v_mfma_f32_16x16x32_f16 v[12:15], v[96:99], v[36:39], v[12:15]\nds_read_b128 v[124:127], v2 offset:2688\n" \
  "v_mfma_f32_16x16x32_f16 v[12:15], v[92:95], v[40:43], v[12:15]\ns_waitcnt lgkmcnt(3)\n" \
  "v_mfma_f32_16x16x32_f16 v[12:15], v[88:91], v[44:47], v[12:15]\nds_read_b128 v[36:39], v2 offset:2752\n" \
  "v_mfma_f32_16x16x32_f16 v[16:19], v[120:123], v[44:47], v[16:19]\ns_waitcnt lgkmcnt(3)\n" \
  "v_mfma_f32_16x16x32_f16 v[12:15], v[84:87], v[28:31], v[12:15]\nds_read_b128 v[40:43], v2 offset:2816\n" \
  "v_mfma_f32_16x16x32_f16 v[16:19], v[116:119], v[28:31], v[16:19]\ns_waitcnt lgkmcnt(3)\n" \
  "v_mfma_f32_16x16x32_f16 v[12:15], v[80:83], v[32:35], v[12:15]\nds_read_b128 v[44:47], v2 offset:2880\n" \
  "v_mfma_f32_16x16x32_f16 v[16:19], v[112:115], v[32:35], v[16:19]\ns_waitcnt lgkmcnt(3)\n" \
  "v_mfma_f32_16x16x32_f16 v[12:15], v[76:79], v[124:127], v[12:15]\nds_read_b128 v[28:31], v2 offset:2944\n" \
  "v_mfma_f32_16x16x32_f16 v[16:19], v[108:111], v[124:127], v[16:19]\ns_waitcnt lgkmcnt(3)\n" \
  "v_mfma_f32_16x16x32_f16 v[12:15], v[72:75], v[36:39], v[12:15]\nds_read_b128 v[32:35], v2 offset:3008\n" \
  "v_mfma_f32_16x16x32_f16 v[16:19], v[104:107], v[36:39], v[16:19]\ns_waitcnt lgkmcnt(3)\n" \
  "v_mfma_f32_16x16x32_f16 v[12:15], v[68:71], v[40:43], v[12:15]\nv_mfma_f32_16x16x32_f16 v[16:19], v[100:103], v[40:43], v[16:19]\n" \
  "ds_read_b128 v[124:127], v2 offset:3072\ns_waitcnt lgkmcnt(3)\nv_mfma_f32_16x16x32_f16 v[12:15], v[64:67], v[44:47], v[12:15]\n" \
  "v_mfma_f32_16x16x32_f16 v[16:19], v[96:99], v[44:47], v[16:19]\nds_read_b128 v[36:39], v2 offset:3136\ns_waitcnt lgkmcnt(3)\n" \
  "v_mfma_f32_16x16x32_f16 v[12:15], v[60:63], v[28:31], v[12:15]\nds_read_b64 v[40:41], v6 offset:128\nds_read_b64 v[42:43], v6 offset:136\n" \
  "v_mfma_f32_16x16x32_f16 v[16:19], v[92:95], v[28:31], v[16:19]\nds_read_b128 v[44:47], v2 offset:3200\ns_waitcnt lgkmcnt(4)\n" \
  "v_mfma_f32_16x16x32_f16 v[12:15], v[56:59], v[32:35], v[12:15]\nv_mfma_f32_16x16x32_f16 v[16:19], v[88:91], v[32:35], v[16:19]\n" \
  "ds_read_b128 v[28:31], v2 offset:3264\nv_mfma_f32_16x16x32_f16 v[20:23], v[120:123], v[32:35], v[20:23]\nds_read_b64 v[120:121], v6 offset:192\n" \
  "ds_read_b64 v[122:123], v6 offset:200\nv_mfma_f32_16x16x32_f16 v[16:19], v[84:87], v[124:127], v[16:19]\nds_read_b128 v[32:35], v2 offset:3328\n" \
  "v_mfma_f32_16x16x32_f16 v[20:23], v[116:119], v[124:127], v[20:23]\nds_read_b64 v[116:117], v6 offset:256\nds_read_b64 v[118:119], v6 offset:264\n" \
  "s_waitcnt lgkmcnt(6)\nv_mfma_f32_16x16x32_f16 v[16:19], v[80:83], v[36:39], v[16:19]\nds_read_b128 v[124:127], v2 offset:3392\n" \
  "v_mfma_f32_16x16x32_f16 v[20:23], v[112:115], v[36:39], v[20:23]\nds_read_b64 v[112:113], v6 offset:320\nds_read_b64 v[114:115], v6 offset:328\n" \
  "v_mfma_f32_16x16x32_f16 v[16:19], v[76:79], v[44:47], v[16:19]\nds_read_b128 v[36:39], v2 offset:3456\n" \
  "v_mfma_f32_16x16x32_f16 v[20:23], v[108:111], v[44:47], v[20:23]\ns_waitcnt lgkmcnt(6)\n" \
  "v_mfma_f32_16x16x32_f16 v[16:19], v[72:75], v[28:31], v[16:19]\nds_read_b128 v[108:111], v2 offset:3520\n" \
  "v_mfma_f32_16x16x32_f16 v[20:23], v[104:107], v[28:31], v[20:23]\nv_mfma_f32_16x16x32_f16 v[16:19], v[68:71], v[32:35], v[16:19]\n" \
  "ds_read_b128 v[44:47], v2 offset:3584\nv_mfma_f32_16x16x32_f16 v[20:23], v[100:103], v[32:35], v[20:23]\nds_read_b128 v[104:107], v2 offset:3648\n" \
  "ds_read_b32 v27, v9 offset:32\ns_waitcnt lgkmcnt(4)\nv_mfma_f32_16x16x32_f16 v[16:19], v[64:67], v[124:127], v[16:19]\n" \
  "ds_read_b128 v[28:31], v2 offset:3712\nv_mfma_f32_16x16x32_f16 v[20:23], v[96:99], v[124:127], v[20:23]\nds_read_b128 v[100:103], v2 offset:3776\n" \
  "ds_read2_b32 v[32:33], v8 offset0:32 offset1:33\nds_read2_b32 v[34:35], v8 offset0:34 offset1:35\n" \
  "v_mfma_f32_16x16x32_f16 v[16:19], v[60:63], v[36:39], v[16:19]\nds_read_b128 v[96:99], v2 offset:3840\n" \
  "v_mfma_f32_16x16x32_f16 v[20:23], v[92:95], v[36:39], v[20:23]\nds_read_b128 v[124:127], v2 offset:3904\ns_waitcnt lgkmcnt(8)\n" \
  "v_mfma_f32_16x16x32_f16 v[16:19], v[56:59], v[108:111], v[16:19]\nds_read_b128 v[92:95], v2 offset:3968\ns_waitcnt lgkmcnt(7)\n" \
  "v_readfirstlane_b32 s85, v27\ns_cmp_lt_u32 s85, 4\ns_cbranch_scc1 31f\n32:\nv_mfma_f32_16x16x32_f16 v[20:23], v[88:91], v[108:111], v[20:23]\n" \
  "ds_read_b128 v[36:39], v2 offset:4032\nds_read_b64_tr_b16 v[88:89], v5 offset:512\nds_read_b64_tr_b16 v[90:91], v5 offset:544\n" \
  "v_mfma_f32_16x16x32_f16 v[20:23], v[84:87], v[44:47], v[20:23]\nds_read_b64_tr_b16 v[108:109], v5 offset:768\n" \
  "ds_read_b64_tr_b16 v[110:111], v5 offset:800\nv_mfma_f32_16x16x32_f16 v[20:23], v[80:83], v[104:107], v[20:23]\n" \
  "ds_read_b64_tr_b16 v[84:85], v5 offset:1024\nds_read_b64_tr_b16 v[86:87], v5 offset:1056\ns_waitcnt lgkmcnt(12)\n" \
  "v_mfma_f32_16x16x32_f16 v[20:23], v[76:79], v[28:31], v[20:23]\nds_read_b64_tr_b16 v[44:45], v5 offset:1280\n" \
  "ds_read_b64_tr_b16 v[46:47], v5 offset:1312\nv_mfma_f32_16x16x32_f16 v[20:23], v[72:75], v[100:103], v[20:23]\ns_waitcnt lgkmcnt(10)\n" \
  "v_mfma_f32_16x16x32_f16 v[20:23], v[68:71], v[96:99], v[20:23]\nds_read_b64_tr_b16 v[80:81], v5 offset:1536\n" \
  "ds_read_b64_tr_b16 v[82:83], v5 offset:1568\nv_mfma_f32_16x16x32_f16 v[20:23], v[64:67], v[124:127], v[20:23]\n" \
  "ds_read_b64_tr_b16 v[104:105], v5 offset:1792\nds_read_b64_tr_b16 v[106:107], v5 offset:1824\ns_waitcnt lgkmcnt(12)\n" \
  "v_mfma_f32_16x16x32_f16 v[20:23], v[60:63], v[92:95], v[20:23]\nds_read_b64_tr_b16 v[76:77], v5 offset:2048\n" \
  "ds_read_b64_tr_b16 v[78:79], v5 offset:2080\nv_mfma_f32_16x16x32_f16 v[20:23], v[56:59], v[36:39], v[20:23]\ns_waitcnt lgkmcnt(10)\n" \
  "v_mfma_f32_16x16x32_f16 v[12:15], v[88:91], v[52:55], v[12:15]\nds_read_b64_tr_b16 v[28:29], v5 offset:2304\n" \
  "ds_read_b64_tr_b16 v[30:31], v5 offset:2336\nv_mfma_f32_16x16x32_f16 v[12:15], v[108:111], v[48:51], v[12:15]\n" \
  "ds_read_b64_tr_b16 v[72:73], v5 offset:2560\nds_read_b64_tr_b16 v[74:75], v5 offset:2592\ns_waitcnt lgkmcnt(10)\n" \
  "v_mfma_f32_16x16x32_f16 v[12:15], v[84:87], v[40:43], v[12:15]\nds_read_b64_tr_b16 v[100:101], v5 offset:2816\n" \
  "ds_read_b64_tr_b16 v[102:103], v5 offset:2848\nv_mfma_f32_16x16x32_f16 v[16:19], v[84:87], v[52:55], v[16:19]\n" \
  "ds_read_b64_tr_b16 v[68:69], v7 offset:0\nds_read_b64_tr_b16 v[70:71], v7 offset:64\n" \
  "v_mfma_f32_16x16x32_f16 v[12:15], v[44:47], v[120:123], v[12:15]\nv_mfma_f32_16x16x32_f16 v[16:19], v[44:47], v[48:51], v[16:19]\n" \
  "s_waitcnt lgkmcnt(10)\nv_mfma_f32_16x16x32_f16 v[12:15], v[80:83], v[116:119], v[12:15]\nds_read2_b32 v[96:97], v8 offset0:16 offset1:17\n" \
  "ds_read2_b32 v[98:99], v8 offset0:18 offset1:19\nv_mfma_f32_16x16x32_f16 v[16:19], v[80:83], v[40:43], v[16:19]\n" \
  "ds_read2_b32 v[64:65], v8 offset0:0 offset1:1\nds_read2_b32 v[66:67], v8 offset0:2 offset1:3\n" \
  "v_mfma_f32_16x16x32_f16 v[20:23], v[80:83], v[52:55], v[20:23]\nv_mfma_f32_16x16x32_f16 v[12:15], v[104:107], v[112:115], v[12:15]\n" \
  "v_mfma_f32_16x16x32_f16 v[16:19], v[104:107], v[120:123], v[16:19]\nv_mfma_f32_16x16x32_f16 v[20:23], v[104:107], v[48:51], v[20:23]\n" \
  "s_waitcnt lgkmcnt(10)\nv_mfma_f32_16x16x32_f16 v[16:19], v[76:79], v[116:119], v[16:19]\nds_read_b64_tr_b16 v[124:125], v7 offset:512\n" \
  "ds_read_b64_tr_b16 v[126:127], v7 offset:576\nv_mfma_f32_16x16x32_f16 v[20:23], v[76:79], v[40:43], v[20:23]\n" \
  "ds_read2_b32 v[60:61], v8 offset0:48 offset1:49\nds_read2_b32 v[62:63], v8 offset0:50 offset1:51\n" \
  "v_mfma_f32_16x16x32_f16 v[16:19], v[28:31], v[112:115], v[16:19]\nv_mfma_f32_16x16x32_f16 v[20:23], v[28:31], v[120:123], v[20:23]\n" \
  "s_waitcnt lgkmcnt(10)\nv_mfma_f32_16x16x32_f16 v[20:23], v[72:75], v[116:119], v[20:23]\nds_read_b64_tr_b16 v[92:93], v7 offset:1024\n" \
  "ds_read_b64_tr_b16 v[94:95], v7 offset:1088\nv_mfma_f32_16x16x32_f16 v[20:23], v[100:103], v[112:115], v[20:23]\n" \
  "ds_read_b64_tr_b16 v[56:57], v7 offset:1536\nds_read_b64_tr_b16 v[58:59], v7 offset:1600\ns_waitcnt lgkmcnt(10)\n" \
  "v_mfma_f32_16x16x32_f16 v[12:15], v[68:71], v[32:35], v[12:15]\nv_mfma_f32_16x16x32_f16 v[16:19], v[68:71], v[96:99], v[16:19]\n" \
  "s_waitcnt lgkmcnt(6)\nv_mfma_f32_16x16x32_f16 v[20:23], v[68:71], v[64:67], v[20:23]\ns_waitcnt lgkmcnt(2)\n" \
  "v_mfma_f32_16x16x32_f16 v[12:15], v[124:127], v[60:63], v[12:15]\nv_mfma_f32_16x16x32_f16 v[16:19], v[124:127], v[32:35], v[16:19]\n" \
  "v_mfma_f32_16x16x32_f16 v[20:23], v[124:127], v[96:99], v[20:23]\nv_mfma_f32_16x16x32_f16 v[16:19], v[92:95], v[60:63], v[16:19]\n" \
  "v_mfma_f32_16x16x32_f16 v[20:23], v[92:95], v[32:35], v[20:23]\ns_waitcnt lgkmcnt(0)\n" \
  "v_mfma_f32_16x16x32_f16 v[20:23], v[56:59], v[60:63], v[20:23]\ns_nop 1\nv_cvt_pkrtz_f16_f32 v24, v12, v13\nv_cvt_pkrtz_f16_f32 v25, v14, v15\n" \
  "v_pk_add_u16 v24, v24, 4 op_sel_hi:[1,0] clamp\nv_pk_add_u16 v25, v25, 4 op_sel_hi:[1,0] clamp\nds_write_b64 v4, v[24:25] offset:5120\n" \
  "v_cvt_pkrtz_f16_f32 v24, v16, v17\nv_cvt_pkrtz_f16_f32 v25, v18, v19\nv_pk_add_u16 v24, v24, 4 op_sel_hi:[1,0] clamp\n" \
  "v_pk_add_u16 v25, v25, 4 op_sel_hi:[1,0] clamp\nds_write_b64 v4, v[24:25] offset:6144\nv_cvt_pkrtz_f16_f32 v24, v20, v21\n" \
  "v_cvt_pkrtz_f16_f32 v25, v22, v23\nv_pk_add_u16 v24, v24, 4 op_sel_hi:[1,0] clamp\nv_pk_add_u16 v25, v25, 4 op_sel_hi:[1,0] clamp\n" \
  "ds_write_b64 v4, v[24:25] offset:7168\ns_setprio 0\ns_setpc_b64 s[90:91]\n31:\ns_movk_i32 s84, 0x4000\n33:\nds_read_b32 v27, v9 offset:32\n" \
  "s_sub_u32 s84, s84, 1\ns_cbranch_scc1 32b\ns_waitcnt lgkmcnt(0)\nv_readfirstlane_b32 s85, v27\ns_cmp_lt_u32 s85, 4\ns_cbranch_scc1 33b\n" \
  "s_branch 32b\n9:\ns_cmp_eq_u32 s89, 3\ns_cbranch_scc1 3f\ns_cmp_eq_u32 s89, 4\ns_cbranch_scc1 4f\ns_cmp_eq_u32 s89, 5\ns_cbranch_scc1 5f\n" \
  "ds_read_b64 v[124:125], v3 offset:576\nds_read_b64 v[126:127], v3 offset:584\nds_read_b64 v[120:121], v3 offset:64\n" \
  "ds_read_b64 v[122:123], v3 offset:72\nds_read_b64 v[116:117], v3 offset:640\nds_read_b64 v[118:119], v3 offset:648\n" \
  "ds_read_b64 v[112:113], v3 offset:128\nds_read_b64 v[114:115], v3 offset:136\nds_read_b64 v[108:109], v3 offset:704\n" \
  "ds_read_b64 v[110:111], v3 offset:712\nds_read_b64 v[104:105], v3 offset:192\nds_read_b64 v[106:107], v3 offset:200\n" \
  "ds_read_b64 v[100:101], v3 offset:768\nds_read_b64 v[102:103], v3 offset:776\nds_read_b64 v[96:97], v3 offset:256\n" \
  "ds_read_b64 v[98:99], v3 offset:264\nds_read_b64 v[92:93], v3 offset:832\nds_read_b64 v[94:95], v3 offset:840\n" \
  "ds_read_b64 v[88:89], v3 offset:320\nds_read_b64 v[90:91], v3 offset:328\nds_read_b64 v[84:85], v3 offset:896\n" \
  "ds_read_b64 v[86:87], v3 offset:904\nds_read_b64 v[80:81], v3 offset:384\nds_read_b64 v[82:83], v3 offset:392\n" \
  "ds_read_b64 v[76:77], v3 offset:960\nds_read_b64 v[78:79], v3 offset:968\nds_read_b64 v[72:73], v3 offset:448\n" \
  "ds_read_b64 v[74:75], v3 offset:456\nds_read_b64 v[68:69], v3 offset:1024\nds_read_b64 v[70:71], v3 offset:1032\n" \
  "ds_read_b64 v[64:65], v3 offset:512\nds_read_b64 v[66:67], v3 offset:520\ns_waitcnt lgkmcnt(0)\ns_barrier\nds_read_b128 v[24:27], v2 offset:0\n" \
  "ds_read_b128 v[28:31], v2 offset:64\nds_read_b128 v[32:35], v2 offset:128\nds_read_b128 v[36:39], v2 offset:192\ns_waitcnt lgkmcnt(3)\n" \
  "v_mfma_f32_16x16x32_f16 v[12:15], v[124:127], v[24:27], 0\ns_setprio 1\nds_read_b128 v[40:43], v2 offset:256\n" \
  "v_mfma_f32_16x16x32_f16 v[16:19], v[120:123], v[24:27], 0\ns_waitcnt lgkmcnt(3)\nv_mfma_f32_16x16x32_f16 v[12:15], v[116:119], v[28:31], v[12:15]\n" \
  "ds_read_b128 v[44:47], v2 offset:320\nv_mfma_f32_16x16x32_f16 v[16:19], v[112:115], v[28:31], v[16:19]\ns_waitcnt lgkmcnt(3)\n" \
  "v_mfma_f32_16x16x32_f16 v[12:15], v[108:111], v[32:35], v[12:15]\nds_read_b128 v[48:51], v2 offset:384\n" \
  "v_mfma_f32_16x16x32_f16 v[16:19], v[104:107], v[32:35], v[16:19]\ns_waitcnt lgkmcnt(3)\n" \
  "v_mfma_f32_16x16x32_f16 v[12:15], v[100:103], v[36:39], v[12:15]\nds_read_b128 v[52:55], v2 offset:448\n" \
  "v_mfma_f32_16x16x32_f16 v[16:19], v[96:99], v[36:39], v[16:19]\ns_waitcnt lgkmcnt(3)\n" \
  "v_mfma_f32_16x16x32_f16 v[12:15], v[92:95], v[40:43], v[12:15]\nds_read_b128 v[56:59], v2 offset:512\n" \
  "v_mfma_f32_16x16x32_f16 v[16:19], v[88:91], v[40:43], v[16:19]\nds_read_b128 v[60:63], v2 offset:576\ns_waitcnt lgkmcnt(3)\n" \
  "v_mfma_f32_16x16x32_f16 v[12:15], v[84:87], v[44:47], v[12:15]\nds_read_b128 v[120:123], v2 offset:640\n" \
  "v_mfma_f32_16x16x32_f16 v[16:19], v[80:83], v[44:47], v[16:19]\nds_read_b128 v[24:27], v2 offset:704\n" \
  "v_mfma_f32_16x16x32_f16 v[12:15], v[76:79], v[48:51], v[12:15]\nds_read_b128 v[112:115], v2 offset:768\n" \
  "v_mfma_f32_16x16x32_f16 v[16:19], v[72:75], v[48:51], v[16:19]\nds_read_b128 v[28:31], v2 offset:832\ns_waitcnt lgkmcnt(5)\n" \
  "v_mfma_f32_16x16x32_f16 v[12:15], v[68:71], v[52:55], v[12:15]\nds_read_b128 v[104:107], v2 offset:896\n" \
  "v_mfma_f32_16x16x32_f16 v[16:19], v[64:67], v[52:55], v[16:19]\nds_read_b128 v[32:35], v2 offset:960\n" \
  "v_mfma_f32_16x16x32_f16 v[16:19], v[124:127], v[56:59], v[16:19]\ns_waitcnt lgkmcnt(5)\n" \
  "v_mfma_f32_16x16x32_f16 v[16:19], v[116:119], v[60:63], v[16:19]\nv_mfma_f32_16x16x32_f16 v[16:19], v[108:111], v[120:123], v[16:19]\n" \
  "s_waitcnt lgkmcnt(3)\nv_mfma_f32_16x16x32_f16 v[16:19], v[100:103], v[24:27], v[16:19]\nv_cvt_pkrtz_f16_f32 v20, v12, v13\n" \
  "v_cvt_pkrtz_f16_f32 v21, v14, v15\nv_mfma_f32_16x16x32_f16 v[16:19], v[92:95], v[112:115], v[16:19]\n" \
  "v_pk_add_u16 v20, v20, 4 op_sel_hi:[1,0] clamp\ns_waitcnt lgkmcnt(2)\nv_mfma_f32_16x16x32_f16 v[16:19], v[84:87], v[28:31], v[16:19]\n" \
  "v_pk_add_u16 v21, v21, 4 op_sel_hi:[1,0] clamp\ns_waitcnt lgkmcnt(1)\nv_mfma_f32_16x16x32_f16 v[16:19], v[76:79], v[104:107], v[16:19]\n" \
  "ds_write_b64 v4, v[20:21] offset:0\ns_waitcnt lgkmcnt(1)\nv_mfma_f32_16x16x32_f16 v[16:19], v[68:71], v[32:35], v[16:19]\ns_nop 7\n" \
  "v_cvt_pkrtz_f16_f32 v20, v16, v17\nv_cvt_pkrtz_f16_f32 v21, v18, v19\nv_pk_add_u16 v20, v20, 4 op_sel_hi:[1,0] clamp\n" \
  "v_pk_add_u16 v21, v21, 4 op_sel_hi:[1,0] clamp\nds_write_b64 v4, v[20:21] offset:1024\ns_setprio 0\ns_setpc_b64 s[90:91]\n3:\n" \
  "ds_read_b64 v[124:125], v3 offset:0\nds_read_b64 v[126:127], v3 offset:8\nds_read_b64 v[120:121], v3 offset:64\n" \
  "ds_read_b64 v[122:123], v3 offset:72\nds_read_b64 v[116:117], v3 offset:128\nds_read_b64 v[118:119], v3 offset:136\n" \
  "ds_read_b64 v[112:113], v3 offset:192\nds_read_b64 v[114:115], v3 offset:200\nds_read_b64 v[108:109], v3 offset:256\n" \
  "ds_read_b64 v[110:111], v3 offset:264\nds_read_b64 v[104:105], v3 offset:320\nds_read_b64 v[106:107], v3 offset:328\n" \
  "ds_read_b64 v[100:101], v3 offset:384\nds_read_b64 v[102:103], v3 offset:392\nds_read_b64 v[96:97], v3 offset:448\n" \
  "ds_read_b64 v[98:99], v3 offset:456\nds_read_b64 v[92:93], v3 offset:512\nds_read_b64 v[94:95], v3 offset:520\n" \
  "ds_read_b64 v[88:89], v3 offset:576\nds_read_b64 v[90:91], v3 offset:584\nds_read_b64 v[84:85], v3 offset:640\n" \
  "ds_read_b64 v[86:87], v3 offset:648\nds_read_b64 v[80:81], v3 offset:704\nds_read_b64 v[82:83], v3 offset:712\n" \
  "ds_read_b64 v[76:77], v3 offset:768\nds_read_b64 v[78:79], v3 offset:776\nds_read_b64 v[72:73], v3 offset:832\n" \
  "ds_read_b64 v[74:75], v3 offset:840\nds_read_b64 v[68:69], v3 offset:896\nds_read_b64 v[70:71], v3 offset:904\n" \
  "ds_read_b64 v[64:65], v3 offset:960\nds_read_b64 v[66:67], v3 offset:968\nds_read_b64 v[60:61], v3 offset:1024\n" \
  "ds_read_b64 v[62:63], v3 offset:1032\nds_read_b64 v[56:57], v6 offset:256\nds_read_b64 v[58:59], v6 offset:264\n" \
  "ds_read_b64 v[52:53], v6 offset:128\nds_read_b64 v[54:55], v6 offset:136\nds_read_b64 v[48:49], v6 offset:320\n" \
  "ds_read_b64 v[50:51], v6 offset:328\nds_read_b64 v[44:45], v6 offset:192\nds_read_b64 v[46:47], v6 offset:200\ns_waitcnt lgkmcnt(0)\ns_barrier\n" \
  "ds_read_b128 v[24:27], v2 offset:448\nds_read_b128 v[28:31], v2 offset:512\nds_read_b128 v[32:35], v2 offset:576\n" \
  "ds_read_b128 v[36:39], v2 offset:640\nds_read_b128 v[40:43], v2 offset:704\ns_waitcnt lgkmcnt(3)\n" \
  "v_mfma_f32_16x16x32_f16 v[12:15], v[124:127], v[24:27], 0\ns_setprio 1\nds_read_b128 v[24:27], v2 offset:768\n" \
  "v_mfma_f32_16x16x32_f16 v[12:15], v[120:123], v[28:31], v[12:15]\nds_read_b128 v[28:31], v2 offset:832\ns_waitcnt lgkmcnt(3)\n" \
  "v_mfma_f32_16x16x32_f16 v[12:15], v[116:119], v[32:35], v[12:15]\nds_read_b128 v[32:35], v2 offset:896\n" \
  "v_mfma_f32_16x16x32_f16 v[12:15], v[112:115], v[36:39], v[12:15]\nds_read_b128 v[36:39], v2 offset:960\ns_waitcnt lgkmcnt(3)\n" \
  "v_mfma_f32_16x16x32_f16 v[12:15], v[108:111], v[40:43], v[12:15]\nds_read_b128 v[40:43], v2 offset:1024\n" \
  "v_mfma_f32_16x16x32_f16 v[12:15], v[104:107], v[24:27], v[12:15]\nds_read_b128 v[24:27], v2 offset:1088\ns_waitcnt lgkmcnt(3)\n" \
  "v_mfma_f32_16x16x32_f16 v[12:15], v[100:103], v[28:31], v[12:15]\nds_read_b128 v[28:31], v2 offset:1152\n" \
  "v_mfma_f32_16x16x32_f16 v[12:15], v[96:99], v[32:35], v[12:15]\ns_waitcnt lgkmcnt(3)\n" \
  "v_mfma_f32_16x16x32_f16 v[12:15], v[92:95], v[36:39], v[12:15]\nds_read_b128 v[32:35], v2 offset:1216\n" \
  "v_mfma_f32_16x16x32_f16 v[16:19], v[124:127], v[36:39], 0\ns_waitcnt lgkmcnt(3)\nv_mfma_f32_16x16x32_f16 v[12:15], v[88:91], v[40:43], v[12:15]\n" \
  "ds_read_b128 v[124:127], v2 offset:1280\nv_mfma_f32_16x16x32_f16 v[16:19], v[120:123], v[40:43], v[16:19]\ns_waitcnt lgkmcnt(3)\n" \
  "v_mfma_f32_16x16x32_f16 v[12:15], v[84:87], v[24:27], v[12:15]\nds_read_b128 v[36:39], v2 offset:1344\n" \
  "v_mfma_f32_16x16x32_f16 v[16:19], v[116:119], v[24:27], v[16:19]\ns_waitcnt lgkmcnt(3)\n" \
  "v_mfma_f32_16x16x32_f16 v[12:15], v[80:83], v[28:31], v[12:15]\nds_read_b128 v[120:123], v2 offset:1408\n" \
  "v_mfma_f32_16x16x32_f16 v[16:19], v[112:115], v[28:31], v[16:19]\ns_waitcnt lgkmcnt(3)\n" \
  "v_mfma_f32_16x16x32_f16 v[12:15], v[76:79], v[32:35], v[12:15]\nds_read_b128 v[40:43], v2 offset:1472\n" \
  "v_mfma_f32_16x16x32_f16 v[16:19], v[108:111], v[32:35], v[16:19]\ns_waitcnt lgkmcnt(3)\n" \
  "v_mfma_f32_16x16x32_f16 v[12:15], v[72:75], v[124:127], v[12:15]\nds_read_b128 v[116:119], v2 offset:1536\n" \
  "v_mfma_f32_16x16x32_f16 v[16:19], v[104:107], v[124:127], v[16:19]\nds_read_b128 v[24:27], v2 offset:1600\ns_waitcnt lgkmcnt(3)\n" \
  "v_mfma_f32_16x16x32_f16 v[12:15], v[68:71], v[36:39], v[12:15]\nds_read_b128 v[112:115], v2 offset:1664\nds_read_b32 v23, v9 offset:32\n" \
  "v_mfma_f32_16x16x32_f16 v[16:19], v[100:103], v[36:39], v[16:19]\nds_read_b128 v[28:31], v2 offset:1728\n" \
  "v_mfma_f32_16x16x32_f16 v[12:15], v[64:67], v[120:123], v[12:15]\nds_read_b128 v[108:111], v2 offset:1792\n" \
  "v_mfma_f32_16x16x32_f16 v[16:19], v[96:99], v[120:123], v[16:19]\nds_read_b128 v[32:35], v2 offset:1856\ns_waitcnt lgkmcnt(6)\n" \
  "v_mfma_f32_16x16x32_f16 v[12:15], v[60:63], v[40:43], v[12:15]\nds_read_b128 v[104:107], v2 offset:1920\n" \
  "v_mfma_f32_16x16x32_f16 v[16:19], v[92:95], v[40:43], v[16:19]\nds_read_b128 v[124:127], v2 offset:1984\ns_waitcnt lgkmcnt(5)\n" \
  "v_readfirstlane_b32 s85, v23\ns_cmp_lt_u32 s85, 3\ns_cbranch_scc1 51f\n52:\nv_mfma_f32_16x16x32_f16 v[16:19], v[88:91], v[116:119], v[16:19]\n" \
  "ds_read_b64_tr_b16 v[100:101], v5 offset:0\nds_read_b64_tr_b16 v[102:103], v5 offset:32\n" \
  "v_mfma_f32_16x16x32_f16 v[16:19], v[84:87], v[24:27], v[16:19]\nds_read_b64_tr_b16 v[36:37], v5 offset:256\n" \
  "ds_read_b64_tr_b16 v[38:39], v5 offset:288\nv_mfma_f32_16x16x32_f16 v[16:19], v[80:83], v[112:115], v[16:19]\n" \
  "ds_read_b64_tr_b16 v[96:97], v5 offset:512\nds_read_b64_tr_b16 v[98:99], v5 offset:544\ns_waitcnt lgkmcnt(9)\n" \
  "v_mfma_f32_16x16x32_f16 v[16:19], v[76:79], v[28:31], v[16:19]\nds_read_b64_tr_b16 v[120:121], v5 offset:768\n" \
  "ds_read_b64_tr_b16 v[122:123], v5 offset:800\nv_mfma_f32_16x16x32_f16 v[16:19], v[72:75], v[108:111], v[16:19]\ns_waitcnt lgkmcnt(9)\n" \
  "v_mfma_f32_16x16x32_f16 v[16:19], v[68:71], v[32:35], v[16:19]\nv_mfma_f32_16x16x32_f16 v[16:19], v[64:67], v[104:107], v[16:19]\n" \
  "s_waitcnt lgkmcnt(6)\nv_mfma_f32_16x16x32_f16 v[16:19], v[60:63], v[124:127], v[16:19]\n" \
  "v_mfma_f32_16x16x32_f16 v[12:15], v[100:103], v[56:59], v[12:15]\nv_mfma_f32_16x16x32_f16 v[16:19], v[100:103], v[52:55], v[16:19]\n" \
  "s_waitcnt lgkmcnt(2)\nv_mfma_f32_16x16x32_f16 v[12:15], v[36:39], v[48:51], v[12:15]\n" \
  "v_mfma_f32_16x16x32_f16 v[16:19], v[36:39], v[44:47], v[16:19]\nv_mfma_f32_16x16x32_f16 v[16:19], v[96:99], v[56:59], v[16:19]\n" \
  "s_waitcnt lgkmcnt(0)\nv_mfma_f32_16x16x32_f16 v[16:19], v[120:123], v[48:51], v[16:19]\ns_nop 3\nv_cvt_pkrtz_f16_f32 v20, v12, v13\n" \
  "v_cvt_pkrtz_f16_f32 v21, v14, v15\nv_pk_add_u16 v20, v20, 4 op_sel_hi:[1,0] clamp\nv_pk_add_u16 v21, v21, 4 op_sel_hi:[1,0] clamp\n" \
  "ds_write_b64 v4, v[20:21] offset:2048\nv_cvt_pkrtz_f16_f32 v20, v16, v17\nv_cvt_pkrtz_f16_f32 v21, v18, v19\n" \
  "v_pk_add_u16 v20, v20, 4 op_sel_hi:[1,0] clamp\nv_pk_add_u16 v21, v21, 4 op_sel_hi:[1,0] clamp\nds_write_b64 v4, v[20:21] offset:3072\n" \
  "s_setprio 0\ns_setpc_b64 s[90:91]\n51:\ns_movk_i32 s84, 0x4000\n53:\nds_read_b32 v23, v9 offset:32\ns_sub_u32 s84, s84, 1\ns_cbranch_scc1 52b\n" \
  "s_waitcnt lgkmcnt(0)\nv_readfirstlane_b32 s85, v23\ns_cmp_lt_u32 s85, 3\ns_cbranch_scc1 53b\ns_branch 52b\n4:\n" \
  "ds_read_b64 v[124:125], v3 offset:1088\nds_read_b64 v[126:127], v3 offset:1096\nds_read_b64 v[120:121], v3 offset:0\n" \
  "ds_read_b64 v[122:123], v3 offset:8\nds_read_b64 v[116:117], v3 offset:64\nds_read_b64 v[118:119], v3 offset:72\n" \
  "ds_read_b64 v[112:113], v3 offset:128\nds_read_b64 v[114:115], v3 offset:136\nds_read_b64 v[108:109], v3 offset:192\n" \
  "ds_read_b64 v[110:111], v3 offset:200\nds_read_b64 v[104:105], v3 offset:256\nds_read_b64 v[106:107], v3 offset:264\n" \
  "ds_read_b64 v[100:101], v3 offset:320\nds_read_b64 v[102:103], v3 offset:328\nds_read_b64 v[96:97], v3 offset:384\n" \
  "ds_read_b64 v[98:99], v3 offset:392\nds_read_b64 v[92:93], v3 offset:448\nds_read_b64 v[94:95], v3 offset:456\n" \
  "ds_read_b64 v[88:89], v3 offset:512\nds_read_b64 v[90:91], v3 offset:520\nds_read_b64 v[84:85], v3 offset:576\n" \
  "ds_read_b64 v[86:87], v3 offset:584\nds_read_b64 v[80:81], v3 offset:640\nds_read_b64 v[82:83], v3 offset:648\n" \
  "ds_read_b64 v[76:77], v3 offset:704\nds_read_b64 v[78:79], v3 offset:712\nds_read_b64 v[72:73], v3 offset:768\n" \
  "ds_read_b64 v[74:75], v3 offset:776\nds_read_b64 v[68:69], v3 offset:832\nds_read_b64 v[70:71], v3 offset:840\n" \
  "ds_read_b64 v[64:65], v3 offset:896\nds_read_b64 v[66:67], v3 offset:904\nds_read_b64 v[60:61], v3 offset:960\n" \
  "ds_read_b64 v[62:63], v3 offset:968\nds_read_b64 v[56:57], v3 offset:1024\nds_read_b64 v[58:59], v3 offset:1032\n" \
  "ds_read_b64 v[52:53], v6 offset:0\nds_read_b64 v[54:55], v6 offset:8\nds_read_b64 v[48:49], v6 offset:64\nds_read_b64 v[50:51], v6 offset:72\n" \
  "ds_read_b64 v[44:45], v6 offset:128\nds_read_b64 v[46:47], v6 offset:136\nds_read_b64 v[40:41], v6 offset:192\n" \
  "ds_read_b64 v[42:43], v6 offset:200\ns_waitcnt lgkmcnt(0)\ns_barrier\nds_read_b128 v[24:27], v2 offset:448\nds_read_b128 v[28:31], v2 offset:960\n" \
  "ds_read_b128 v[32:35], v2 offset:1472\nds_read_b128 v[36:39], v2 offset:1536\ns_waitcnt lgkmcnt(3)\n" \
  "v_mfma_f32_16x16x32_f16 v[12:15], v[124:127], v[24:27], 0\ns_setprio 1\nds_read_b128 v[24:27], v2 offset:1600\ns_waitcnt lgkmcnt(3)\n" \
  "v_mfma_f32_16x16x32_f16 v[16:19], v[124:127], v[28:31], 0\nds_read_b128 v[124:127], v2 offset:1664\ns_waitcnt lgkmcnt(3)\n" \
  "v_mfma_f32_16x16x32_f16 v[12:15], v[120:123], v[32:35], v[12:15]\nds_read_b128 v[28:31], v2 offset:1728\ns_waitcnt lgkmcnt(3)\n" \
  "v_mfma_f32_16x16x32_f16 v[12:15], v[116:119], v[36:39], v[12:15]\nds_read_b128 v[32:35], v2 offset:1792\ns_waitcnt lgkmcnt(3)\n" \
  "v_mfma_f32_16x16x32_f16 v[12:15], v[112:115], v[24:27], v[12:15]\nds_read_b128 v[36:39], v2 offset:1856\ns_waitcnt lgkmcnt(3)\n" \
  "v_mfma_f32_16x16x32_f16 v[12:15], v[108:111], v[124:127], v[12:15]\nds_read_b128 v[24:27], v2 offset:1920\ns_waitcnt lgkmcnt(3)\n" \
  "v_mfma_f32_16x16x32_f16 v[12:15], v[104:107], v[28:31], v[12:15]\nds_read_b128 v[124:127], v2 offset:1984\ns_waitcnt lgkmcnt(3)\n" \
  "v_mfma_f32_16x16x32_f16 v[12:15], v[100:103], v[32:35], v[12:15]\nds_read_b128 v[28:31], v2 offset:2048\ns_waitcnt lgkmcnt(3)\n" \
  "v_mfma_f32_16x16x32_f16 v[12:15], v[96:99], v[36:39], v[12:15]\nds_read_b128 v[32:35], v2 offset:2112\ns_waitcnt lgkmcnt(3)\n" \
  "v_mfma_f32_16x16x32_f16 v[12:15], v[92:95], v[24:27], v[12:15]\nds_read_b128 v[36:39], v2 offset:2176\ns_waitcnt lgkmcnt(3)\n" \
  "v_mfma_f32_16x16x32_f16 v[12:15], v[88:91], v[124:127], v[12:15]\nds_read_b128 v[24:27], v2 offset:2240\n" \
  "v_mfma_f32_16x16x32_f16 v[16:19], v[120:123], v[124:127], v[16:19]\nds_read_b64 v[120:121], v6 offset:256\nds_read_b64 v[122:123], v6 offset:264\n" \
  "s_waitcnt lgkmcnt(4)\nv_mfma_f32_16x16x32_f16 v[12:15], v[84:87], v[28:31], v[12:15]\nds_read_b128 v[124:127], v2 offset:2304\n" \
  "v_mfma_f32_16x16x32_f16 v[16:19], v[116:119], v[28:31], v[16:19]\nds_read_b64 v[116:117], v6 offset:320\nds_read_b64 v[118:119], v6 offset:328\n" \
  "v_mfma_f32_16x16x32_f16 v[12:15], v[80:83], v[32:35], v[12:15]\nds_read_b128 v[28:31], v2 offset:2368\n" \
  "v_mfma_f32_16x16x32_f16 v[16:19], v[112:115], v[32:35], v[16:19]\ns_waitcnt lgkmcnt(6)\n" \
  "v_mfma_f32_16x16x32_f16 v[12:15], v[76:79], v[36:39], v[12:15]\nds_read_b128 v[112:115], v2 offset:2432\n" \
  "v_mfma_f32_16x16x32_f16 v[16:19], v[108:111], v[36:39], v[16:19]\nds_read2_b32 v[32:33], v8 offset0:48 offset1:49\n" \
  "ds_read2_b32 v[34:35], v8 offset0:50 offset1:51\nv_mfma_f32_16x16x32_f16 v[12:15], v[72:75], v[24:27], v[12:15]\n" \
  "ds_read_b128 v[108:111], v2 offset:2496\nv_mfma_f32_16x16x32_f16 v[16:19], v[104:107], v[24:27], v[16:19]\n" \
  "ds_read2_b32 v[36:37], v8 offset0:32 offset1:33\nds_read2_b32 v[38:39], v8 offset0:34 offset1:35\ns_waitcnt lgkmcnt(6)\n" \
  "v_mfma_f32_16x16x32_f16 v[12:15], v[68:71], v[124:127], v[12:15]\nds_read_b128 v[104:107], v2 offset:2560\n" \
  "v_mfma_f32_16x16x32_f16 v[16:19], v[100:103], v[124:127], v[16:19]\nds_read_b128 v[24:27], v2 offset:2624\n" \
  "v_mfma_f32_16x16x32_f16 v[12:15], v[64:67], v[28:31], v[12:15]\nds_read_b128 v[100:103], v2 offset:2688\nds_read_b32 v23, v9 offset:32\n" \
  "v_mfma_f32_16x16x32_f16 v[16:19], v[96:99], v[28:31], v[16:19]\nds_read_b128 v[124:127], v2 offset:2752\ns_waitcnt lgkmcnt(7)\n" \
  "v_mfma_f32_16x16x32_f16 v[12:15], v[60:63], v[112:115], v[12:15]\nds_read_b128 v[96:99], v2 offset:2816\n" \
  "v_mfma_f32_16x16x32_f16 v[16:19], v[92:95], v[112:115], v[16:19]\nds_read_b128 v[28:31], v2 offset:2880\n" \
  "v_mfma_f32_16x16x32_f16 v[12:15], v[56:59], v[108:111], v[12:15]\nds_read_b128 v[92:95], v2 offset:2944\n" \
  "v_mfma_f32_16x16x32_f16 v[16:19], v[88:91], v[108:111], v[16:19]\nds_read_b128 v[112:115], v2 offset:3008\ns_waitcnt lgkmcnt(5)\n" \
  "v_readfirstlane_b32 s85, v23\ns_cmp_lt_u32 s85, 3\ns_cbranch_scc1 61f\n62:\nv_mfma_f32_16x16x32_f16 v[16:19], v[84:87], v[104:107], v[16:19]\n" \
  "ds_read_b64_tr_b16 v[88:89], v5 offset:0\nds_read_b64_tr_b16 v[90:91], v5 offset:32\n" \
  "v_mfma_f32_16x16x32_f16 v[16:19], v[80:83], v[24:27], v[16:19]\nds_read_b64_tr_b16 v[108:109], v5 offset:256\n" \
  "ds_read_b64_tr_b16 v[110:111], v5 offset:288\nv_mfma_f32_16x16x32_f16 v[16:19], v[76:79], v[100:103], v[16:19]\n" \
  "ds_read_b64_tr_b16 v[84:85], v5 offset:512\nds_read_b64_tr_b16 v[86:87], v5 offset:544\ns_waitcnt lgkmcnt(9)\n" \
  "v_mfma_f32_16x16x32_f16 v[16:19], v[72:75], v[124:127], v[16:19]\nds_read_b64_tr_b16 v[104:105], v5 offset:768\n" \
  "ds_read_b64_tr_b16 v[106:107], v5 offset:800\nv_mfma_f32_16x16x32_f16 v[16:19], v[68:71], v[96:99], v[16:19]\n" \
  "ds_read_b64_tr_b16 v[80:81], v5 offset:1024\nds_read_b64_tr_b16 v[82:83], v5 offset:1056\ns_waitcnt lgkmcnt(11)\n" \
  "v_mfma_f32_16x16x32_f16 v[16:19], v[64:67], v[28:31], v[16:19]\nds_read_b64_tr_b16 v[24:25], v5 offset:1280\n" \
  "ds_read_b64_tr_b16 v[26:27], v5 offset:1312\nv_mfma_f32_16x16x32_f16 v[16:19], v[60:63], v[92:95], v[16:19]\n" \
  "ds_read_b64_tr_b16 v[76:77], v5 offset:1536\nds_read_b64_tr_b16 v[78:79], v5 offset:1568\ns_waitcnt lgkmcnt(12)\n" \
  "v_mfma_f32_16x16x32_f16 v[16:19], v[56:59], v[112:115], v[16:19]\nds_read_b64_tr_b16 v[100:101], v5 offset:1792\n" \
  "ds_read_b64_tr_b16 v[102:103], v5 offset:1824\nv_mfma_f32_16x16x32_f16 v[12:15], v[88:91], v[52:55], v[12:15]\ns_waitcnt lgkmcnt(10)\n" \
  "v_mfma_f32_16x16x32_f16 v[12:15], v[108:111], v[48:51], v[12:15]\nds_read_b64_tr_b16 v[72:73], v7 offset:0\n" \
  "ds_read_b64_tr_b16 v[74:75], v7 offset:64\nv_mfma_f32_16x16x32_f16 v[12:15], v[84:87], v[44:47], v[12:15]\n" \
  "ds_read_b64_tr_b16 v[124:125], v7 offset:512\nds_read_b64_tr_b16 v[126:127], v7 offset:576\n" \
  "v_mfma_f32_16x16x32_f16 v[16:19], v[84:87], v[52:55], v[16:19]\ns_waitcnt lgkmcnt(10)\n" \
  "v_mfma_f32_16x16x32_f16 v[12:15], v[104:107], v[40:43], v[12:15]\nv_mfma_f32_16x16x32_f16 v[16:19], v[104:107], v[48:51], v[16:19]\n" \
  "v_mfma_f32_16x16x32_f16 v[12:15], v[80:83], v[120:123], v[12:15]\nv_mfma_f32_16x16x32_f16 v[16:19], v[80:83], v[44:47], v[16:19]\n" \
  "s_waitcnt lgkmcnt(6)\nv_mfma_f32_16x16x32_f16 v[12:15], v[24:27], v[116:119], v[12:15]\n" \
  "v_mfma_f32_16x16x32_f16 v[16:19], v[24:27], v[40:43], v[16:19]\nv_mfma_f32_16x16x32_f16 v[16:19], v[76:79], v[120:123], v[16:19]\n" \
  "s_waitcnt lgkmcnt(2)\nv_mfma_f32_16x16x32_f16 v[16:19], v[100:103], v[116:119], v[16:19]\n" \
  "v_mfma_f32_16x16x32_f16 v[12:15], v[72:75], v[32:35], v[12:15]\nv_mfma_f32_16x16x32_f16 v[16:19], v[72:75], v[36:39], v[16:19]\n" \
  "s_waitcnt lgkmcnt(0)\nv_mfma_f32_16x16x32_f16 v[16:19], v[124:127], v[32:35], v[16:19]\ns_nop 4\nv_cvt_pkrtz_f16_f32 v20, v12, v13\n" \
  "v_cvt_pkrtz_f16_f32 v21, v14, v15\nv_pk_add_u16 v20, v20, 4 op_sel_hi:[1,0] clamp\nv_pk_add_u16 v21, v21, 4 op_sel_hi:[1,0] clamp\n" \
  "ds_write_b64 v4, v[20:21] offset:4096\nv_cvt_pkrtz_f16_f32 v20, v16, v17\nv_cvt_pkrtz_f16_f32 v21, v18, v19\n" \
  "v_pk_add_u16 v20, v20, 4 op_sel_hi:[1,0] clamp\nv_pk_add_u16 v21, v21, 4 op_sel_hi:[1,0] clamp\nds_write_b64 v4, v[20:21] offset:5120\n" \
  "s_setprio 0\ns_setpc_b64 s[90:91]\n61:\ns_movk_i32 s84, 0x4000\n63:\nds_read_b32 v23, v9 offset:32\ns_sub_u32 s84, s84, 1\ns_cbranch_scc1 62b\n" \
  "s_waitcnt lgkmcnt(0)\nv_readfirstlane_b32 s85, v23\ns_cmp_lt_u32 s85, 3\ns_cbranch_scc1 63b\ns_branch 62b\n5:\n" \
  "ds_read_b64 v[124:125], v3 offset:1088\nds_read_b64 v[126:127], v3 offset:1096\nds_read_b64 v[120:121], v3 offset:0\n" \
  "ds_read_b64 v[122:123], v3 offset:8\nds_read_b64 v[116:117], v3 offset:64\nds_read_b64 v[118:119], v3 offset:72\n" \
  "ds_read_b64 v[112:113], v3 offset:128\nds_read_b64 v[114:115], v3 offset:136\nds_read_b64 v[108:109], v3 offset:192\n" \
  "ds_read_b64 v[110:111], v3 offset:200\nds_read_b64 v[104:105], v3 offset:256\nds_read_b64 v[106:107], v3 offset:264\n" \
  "ds_read_b64 v[100:101], v3 offset:320\nds_read_b64 v[102:103], v3 offset:328\nds_read_b64 v[96:97], v3 offset:384\n" \
  "ds_read_b64 v[98:99], v3 offset:392\nds_read_b64 v[92:93], v3 offset:448\nds_read_b64 v[94:95], v3 offset:456\n" \
  "ds_read_b64 v[88:89], v3 offset:512\nds_read_b64 v[90:91], v3 offset:520\nds_read_b64 v[84:85], v3 offset:576\n" \
  "ds_read_b64 v[86:87], v3 offset:584\nds_read_b64 v[80:81], v3 offset:640\nds_read_b64 v[82:83], v3 offset:648\n" \
  "ds_read_b64 v[76:77], v3 offset:704\nds_read_b64 v[78:79], v3 offset:712\nds_read_b64 v[72:73], v3 offset:768\n" \
  "ds_read_b64 v[74:75], v3 offset:776\nds_read_b64 v[68:69], v3 offset:832\nds_read_b64 v[70:71], v3 offset:840\n" \
  "ds_read_b64 v[64:65], v3 offset:896\nds_read_b64 v[66:67], v3 offset:904\nds_read_b64 v[60:61], v3 offset:960\n" \
  "ds_read_b64 v[62:63], v3 offset:968\nds_read_b64 v[56:57], v3 offset:1024\nds_read_b64 v[58:59], v3 offset:1032\n" \
  "ds_read_b64 v[52:53], v6 offset:0\nds_read_b64 v[54:55], v6 offset:8\nds_read_b64 v[48:49], v6 offset:64\nds_read_b64 v[50:51], v6 offset:72\n" \
  "ds_read_b64 v[44:45], v6 offset:128\nds_read_b64 v[46:47], v6 offset:136\nds_read_b64 v[40:41], v6 offset:192\n" \
  "ds_read_b64 v[42:43], v6 offset:200\ns_waitcnt lgkmcnt(0)\ns_barrier\nds_read_b128 v[24:27], v2 offset:1472\n" \
  "ds_read_b128 v[28:31], v2 offset:1984\nds_read_b128 v[32:35], v2 offset:2496\nds_read_b128 v[36:39], v2 offset:2560\ns_waitcnt lgkmcnt(3)\n" \
  "v_mfma_f32_16x16x32_f16 v[12:15], v[124:127], v[24:27], 0\ns_setprio 1\nds_read_b128 v[24:27], v2 offset:2624\ns_waitcnt lgkmcnt(3)\n" \
  "v_mfma_f32_16x16x32_f16 v[16:19], v[124:127], v[28:31], 0\nds_read_b128 v[124:127], v2 offset:2688\ns_waitcnt lgkmcnt(3)\n" \
  "v_mfma_f32_16x16x32_f16 v[12:15], v[120:123], v[32:35], v[12:15]\nds_read_b128 v[28:31], v2 offset:2752\ns_waitcnt lgkmcnt(3)\n" \
  "v_mfma_f32_16x16x32_f16 v[12:15], v[116:119], v[36:39], v[12:15]\nds_read_b128 v[32:35], v2 offset:2816\ns_waitcnt lgkmcnt(3)\n" \
  "v_mfma_f32_16x16x32_f16 v[12:15], v[112:115], v[24:27], v[12:15]\nds_read_b128 v[36:39], v2 offset:2880\ns_waitcnt lgkmcnt(3)\n" \
  "v_mfma_f32_16x16x32_f16 v[12:15], v[108:111], v[124:127], v[12:15]\nds_read_b128 v[24:27], v2 offset:2944\ns_waitcnt lgkmcnt(3)\n" \
  "v_mfma_f32_16x16x32_f16 v[12:15], v[104:107], v[28:31], v[12:15]\nds_read_b128 v[124:127], v2 offset:3008\ns_waitcnt lgkmcnt(3)\n" \
  "v_mfma_f32_16x16x32_f16 v[12:15], v[100:103], v[32:35], v[12:15]\nds_read_b128 v[28:31], v2 offset:3072\ns_waitcnt lgkmcnt(3)\n" \
  "v_mfma_f32_16x16x32_f16 v[12:15], v[96:99], v[36:39], v[12:15]\nds_read_b128 v[32:35], v2 offset:3136\ns_waitcnt lgkmcnt(3)\n" \
  "v_mfma_f32_16x16x32_f16 v[12:15], v[92:95], v[24:27], v[12:15]\nds_read_b128 v[36:39], v2 offset:3200\ns_waitcnt lgkmcnt(3)\n" \
  "v_mfma_f32_16x16x32_f16 v[12:15], v[88:91], v[124:127], v[12:15]\nds_read_b128 v[24:27], v2 offset:3264\n" \
  "v_mfma_f32_16x16x32_f16 v[16:19], v[120:123], v[124:127], v[16:19]\nds_read_b64 v[120:121], v6 offset:256\nds_read_b64 v[122:123], v6 offset:264\n" \
  "s_waitcnt lgkmcnt(4)\nv_mfma_f32_16x16x32_f16 v[12:15], v[84:87], v[28:31], v[12:15]\nds_read_b128 v[124:127], v2 offset:3328\n" \
  "v_mfma_f32_16x16x32_f16 v[16:19], v[116:119], v[28:31], v[16:19]\nds_read_b64 v[116:117], v6 offset:320\nds_read_b64 v[118:119], v6 offset:328\n" \
  "v_mfma_f32_16x16x32_f16 v[12:15], v[80:83], v[32:35], v[12:15]\nds_read_b128 v[28:31], v2 offset:3392\n" \
  "v_mfma_f32_16x16x32_f16 v[16:19], v[112:115], v[32:35], v[16:19]\ns_waitcnt lgkmcnt(6)\n" \
  "v_mfma_f32_16x16x32_f16 v[12:15], v[76:79], v[36:39], v[12:15]\nds_read_b128 v[112:115], v2 offset:3456\n" \
  "v_mfma_f32_16x16x32_f16 v[16:19], v[108:111], v[36:39], v[16:19]\nds_read2_b32 v[32:33], v8 offset0:16 offset1:17\n" \
  "ds_read2_b32 v[34:35], v8 offset0:18 offset1:19\nv_mfma_f32_16x16x32_f16 v[12:15], v[72:75], v[24:27], v[12:15]\n" \
  "ds_read_b128 v[108:111], v2 offset:3520\nv_mfma_f32_16x16x32_f16 v[16:19], v[104:107], v[24:27], v[16:19]\n" \
  "ds_read2_b32 v[36:37], v8 offset0:0 offset1:1\nds_read2_b32 v[38:39], v8 offset0:2 offset1:3\ns_waitcnt lgkmcnt(6)\n" \
  "v_mfma_f32_16x16x32_f16 v[12:15], v[68:71], v[124:127], v[12:15]\nds_read_b128 v[104:107], v2 offset:3584\n" \
  "v_mfma_f32_16x16x32_f16 v[16:19], v[100:103], v[124:127], v[16:19]\nds_read_b128 v[24:27], v2 offset:3648\n" \
  "ds_read2_b32 v[100:101], v8 offset0:32 offset1:33\nds_read2_b32 v[102:103], v8 offset0:34 offset1:35\n" \
  "v_mfma_f32_16x16x32_f16 v[12:15], v[64:67], v[28:31], v[12:15]\nds_read_b128 v[124:127], v2 offset:3712\nds_read_b32 v23, v9 offset:32\n" \
  "v_mfma_f32_16x16x32_f16 v[16:19], v[96:99], v[28:31], v[16:19]\nds_read_b128 v[96:99], v2 offset:3776\ns_waitcnt lgkmcnt(9)\n" \
  "v_mfma_f32_16x16x32_f16 v[12:15], v[60:63], v[112:115], v[12:15]\nds_read_b128 v[28:31], v2 offset:3840\n" \
  "v_mfma_f32_16x16x32_f16 v[16:19], v[92:95], v[112:115], v[16:19]\nds_read_b128 v[92:95], v2 offset:3904\n" \
  "v_mfma_f32_16x16x32_f16 v[12:15], v[56:59], v[108:111], v[12:15]\nds_read_b128 v[112:115], v2 offset:3968\n" \
  "v_mfma_f32_16x16x32_f16 v[16:19], v[88:91], v[108:111], v[16:19]\nds_read_b128 v[88:91], v2 offset:4032\ns_waitcnt lgkmcnt(5)\n" \
  "v_readfirstlane_b32 s85, v23\ns_cmp_lt_u32 s85, 3\ns_cbranch_scc1 71f\n72:\nv_mfma_f32_16x16x32_f16 v[16:19], v[84:87], v[104:107], v[16:19]\n" \
  "ds_read_b64_tr_b16 v[108:109], v5 offset:1024\nds_read_b64_tr_b16 v[110:111], v5 offset:1056\n" \
  "v_mfma_f32_16x16x32_f16 v[16:19], v[80:83], v[24:27], v[16:19]\nds_read_b64_tr_b16 v[84:85], v5 offset:1280\n" \
  "ds_read_b64_tr_b16 v[86:87], v5 offset:1312\nv_mfma_f32_16x16x32_f16 v[16:19], v[76:79], v[124:127], v[16:19]\n" \
  "ds_read_b64_tr_b16 v[104:105], v5 offset:1536\nds_read_b64_tr_b16 v[106:107], v5 offset:1568\ns_waitcnt lgkmcnt(9)\n" \
  "v_mfma_f32_16x16x32_f16 v[16:19], v[72:75], v[96:99], v[16:19]\nds_read_b64_tr_b16 v[80:81], v5 offset:1792\n" \
  "ds_read_b64_tr_b16 v[82:83], v5 offset:1824\nv_mfma_f32_16x16x32_f16 v[16:19], v[68:71], v[28:31], v[16:19]\n" \
  "ds_read_b64_tr_b16 v[24:25], v5 offset:2048\nds_read_b64_tr_b16 v[26:27], v5 offset:2080\ns_waitcnt lgkmcnt(11)\n" \
  "v_mfma_f32_16x16x32_f16 v[16:19], v[64:67], v[92:95], v[16:19]\nds_read_b64_tr_b16 v[76:77], v5 offset:2304\n" \
  "ds_read_b64_tr_b16 v[78:79], v5 offset:2336\nv_mfma_f32_16x16x32_f16 v[16:19], v[60:63], v[112:115], v[16:19]\n" \
  "ds_read_b64_tr_b16 v[124:125], v5 offset:2560\nds_read_b64_tr_b16 v[126:127], v5 offset:2592\ns_waitcnt lgkmcnt(12)\n" \
  "v_mfma_f32_16x16x32_f16 v[16:19], v[56:59], v[88:91], v[16:19]\nds_read_b64_tr_b16 v[72:73], v5 offset:2816\n" \
  "ds_read_b64_tr_b16 v[74:75], v5 offset:2848\nv_mfma_f32_16x16x32_f16 v[12:15], v[108:111], v[52:55], v[12:15]\ns_waitcnt lgkmcnt(10)\n" \
  "v_mfma_f32_16x16x32_f16 v[12:15], v[84:87], v[48:51], v[12:15]\nds_read_b64_tr_b16 v[96:97], v7 offset:0\n" \
  "ds_read_b64_tr_b16 v[98:99], v7 offset:64\nv_mfma_f32_16x16x32_f16 v[12:15], v[104:107], v[44:47], v[12:15]\n" \
  "ds_read_b64_tr_b16 v[68:69], v7 offset:512\nds_read_b64_tr_b16 v[70:71], v7 offset:576\n" \
  "v_mfma_f32_16x16x32_f16 v[16:19], v[104:107], v[52:55], v[16:19]\ns_waitcnt lgkmcnt(10)\n" \
  "v_mfma_f32_16x16x32_f16 v[12:15], v[80:83], v[40:43], v[12:15]\nds_read_b64_tr_b16 v[28:29], v7 offset:1024\n" \
  "ds_read_b64_tr_b16 v[30:31], v7 offset:1088\nv_mfma_f32_16x16x32_f16 v[16:19], v[80:83], v[48:51], v[16:19]\n" \
  "ds_read2_b32 v[64:65], v8 offset0:48 offset1:49\nds_read2_b32 v[66:67], v8 offset0:50 offset1:51\n" \
  "v_mfma_f32_16x16x32_f16 v[12:15], v[24:27], v[120:123], v[12:15]\nv_mfma_f32_16x16x32_f16 v[16:19], v[24:27], v[44:47], v[16:19]\n" \
  "s_waitcnt lgkmcnt(10)\nv_mfma_f32_16x16x32_f16 v[12:15], v[76:79], v[116:119], v[12:15]\nds_read_b64_tr_b16 v[92:93], v7 offset:1536\n" \
  "ds_read_b64_tr_b16 v[94:95], v7 offset:1600\nv_mfma_f32_16x16x32_f16 v[16:19], v[76:79], v[40:43], v[16:19]\n" \
  "v_mfma_f32_16x16x32_f16 v[16:19], v[124:127], v[120:123], v[16:19]\ns_waitcnt lgkmcnt(8)\n" \
  "v_mfma_f32_16x16x32_f16 v[16:19], v[72:75], v[116:119], v[16:19]\nv_mfma_f32_16x16x32_f16 v[12:15], v[96:99], v[32:35], v[12:15]\n" \
  "v_mfma_f32_16x16x32_f16 v[16:19], v[96:99], v[36:39], v[16:19]\ns_waitcnt lgkmcnt(4)\n" \
  "v_mfma_f32_16x16x32_f16 v[12:15], v[68:71], v[100:103], v[12:15]\nv_mfma_f32_16x16x32_f16 v[16:19], v[68:71], v[32:35], v[16:19]\n" \
  "s_waitcnt lgkmcnt(2)\nv_mfma_f32_16x16x32_f16 v[12:15], v[28:31], v[64:67], v[12:15]\n" \
  "v_mfma_f32_16x16x32_f16 v[16:19], v[28:31], v[100:103], v[16:19]\ns_waitcnt lgkmcnt(0)\n" \
  "v_mfma_f32_16x16x32_f16 v[16:19], v[92:95], v[64:67], v[16:19]\ns_nop 4\nv_cvt_pkrtz_f16_f32 v20, v12, v13\nv_cvt_pkrtz_f16_f32 v21, v14, v15\n" \
  "v_pk_add_u16 v20, v20, 4 op_sel_hi:[1,0] clamp\nv_pk_add_u16 v21, v21, 4 op_sel_hi:[1,0] clamp\nds_write_b64 v4, v[20:21] offset:6144\n" \
  "v_cvt_pkrtz_f16_f32 v20, v16, v17\nv_cvt_pkrtz_f16_f32 v21, v18, v19\nv_pk_add_u16 v20, v20, 4 op_sel_hi:[1,0] clamp\n" \
  "v_pk_add_u16 v21, v21, 4 op_sel_hi:[1,0] clamp\nds_write_b64 v4, v[20:21] offset:7168\ns_setprio 0\ns_setpc_b64 s[90:91]\n71:\n" \
  "s_movk_i32 s84, 0x4000\n73:\nds_read_b32 v23, v9 offset:32\ns_sub_u32 s84, s84, 1\ns_cbranch_scc1 72b\ns_waitcnt lgkmcnt(0)\n" \
  "v_readfirstlane_b32 s85, v23\ns_cmp_lt_u32 s85, 3\ns_cbranch_scc1 73b\ns_branch 72b\n"
#define CONV_CLOB "memory", "scc", "s84", "s85", "s90", "s91", "v12", "v13", "v14", "v15", "v16", "v17", "v18", "v19", "v20", "v21", "v22", "v23", "v24", "v25", "v26", "v27", "v28", "v29", "v30", "v31", "v32", "v33", "v34", "v35", "v36", "v37", "v38", "v39", "v40", "v41", "v42", "v43", "v44", "v45", "v46", "v47", "v48", "v49", "v50", "v51", "v52", "v53", "v54", "v55", "v56", "v57", "v58", "v59", "v60", "v61", "v62", "v63", "v64", "v65", "v66", "v67", "v68", "v69", "v70", "v71", "v72", "v73", "v74", "v75", "v76", "v77", "v78", "v79", "v80", "v81", "v82", "v83", "v84", "v85", "v86", "v87", "v88", "v89", "v90", "v91", "v92", "v93", "v94", "v95", "v96", "v97", "v98", "v99", "v100", "v101", "v102", "v103", "v104", "v105", "v106", "v107", "v108", "v109", "v110", "v111", "v112", "v113", "v114", "v115", "v116", "v117", "v118", "v119", "v120", "v121", "v122", "v123", "v124", "v125", "v126", "v127"
constexpr int TDP = 1216, T_3 = 4 * TDP, T_4 = T_3 + 512, TB = T_4 + 320;
constexpr int C_U = 480, UB = C_U + 4096 + 96, C_G5 = 0, C_G3 = 3200, C_G9 = 6400, C_G7 = C_G9 + 2096, GB = C_G7 + 2080 + 112;
constexpr int O_FL = 2 * TB, O_UL = O_FL + 64, O_IM = O_UL + 2 * UB, O_ST = O_IM + 2 * GB;
constexpr int X_TB = 10240, X_UL = X_TB + TB, X_IM = X_UL + UB, X_ST = X_IM + GB;
constexpr int X_WR = X_IM + 128 * IP;
#define X_RW(w) (LW + 9216 * (2 + ((w) >> 1)) + 2560 * ((w) & 1))
constexpr int RP = 1216;
constexpr int LW = 36864 + 512, LA = LW, LM = LW + 36864;
constexpr int L_SC = LM + 2048, SMEM = O_IM + 256 * IP;
static_assert(L_SC + 1152 <= SMEM && O_ST + 8192 <= SMEM && X_IM + 128 * IP <= LM && X_ST + 8192 <= LM && UB % 128 == 64 && GB % 128 == 64 && SMEM <= 79872, "");
struct TOps { uint32_t xa, xb, ya, pd, pf, pg, pe; };
__device__ __forceinline__ void table_ops(uint32_t im, uint32_t tb, int ct, int lane, TOps& t) {
  const int j = lane & 15, kq = lane >> 4, q4 = (lane >> 2) & 3, p4 = lane & 3, jj = j + 16 * ct;
  const uint32_t sa = im + (uint32_t)(IP * (lane >> 2));
  t.xa = sa + (uint32_t)(I_X + 8 * p4); t.xb = sa + (uint32_t)(I_X + 32 + 8 * (p4 & 1)); t.ya = sa + (uint32_t)(32 * ct + 8 * p4);
  t.pd = tb + (uint32_t)(2 * (541 - (8 * jj + 4 * (kq & 1) + 256 * (kq >> 1))));
  t.pf = tb + (uint32_t)((kq & 1) ? T_4 + 2 * (63 - jj) : T_3 + 2 * (127 - 2 * jj));
  t.pg = t.pf - ((kq & 1) ? 64 : 2);
  t.pe = tb + (uint32_t)(p4 * (TDP + 2) + ((lane < 12) ? 2 * (32 - q4) : 2 * (572 + (q4 & 1))));
  asm volatile("" : "+v"(t.xa), "+v"(t.xb), "+v"(t.ya), "+v"(t.pd), "+v"(t.pf), "+v"(t.pg), "+v"(t.pe));
}
typedef __attribute__((address_space(3))) char* lptr;
#define LP(T, a) ((__attribute__((address_space(3))) T*)(uintptr_t)(a))
__device__ __forceinline__ uint32_t table_tile(const TOps& t, int ct, int lane, uint32_t evp, float dsk TSP) {
  u32x4 ev;
  f32x4 ad[2], af[2];
  asm volatile(T_ASM : "=&v"(ad[0]), "=&v"(ad[1]), "=&v"(af[0]), "=&v"(af[1]), "=&v"(ev) : "v"(t.xa), "v"(t.xb), "v"(t.ya), "v"(evp) : T_CLOB);
  const int e0 = (int)(ev[0] + ev[2]), e1 = (int)(ev[1] + ev[3]);
  const int em = min(max(max(max(e0, e1), (int)((__float_as_uint(dsk) >> 23) & 0xffu) - 133), -110), 115);
  const int ts = ((uint32_t)(em + 12) <= 8u) ? 1 : -3 - em;
  float s0 = __builtin_amdgcn_ldexpf(1.f, e0 - 29 + ts), s1 = __builtin_amdgcn_ldexpf(1.f, e1 - 29 + ts);
  float d0 = (ct == 0 && lane == 0) ? __builtin_amdgcn_ldexpf(dsk, ts) : 0.f;
  asm volatile("s_nop 1" : "+v"(ad[0]), "+v"(ad[1]), "+v"(af[0]), "+v"(af[1]), "+v"(s0), "+v"(s1), "+v"(d0));
  const f32x4 k = ad[0] * s0 + (f32x4){d0, 0.f, 0.f, 0.f} + ad[1] * s1, f = af[0] * s0 + af[1] * s1;
  auto pk = [](float a, float b) { uint32_t r; asm("v_cvt_pk_f16_f32 %0, %1, %2" : "=v"(r) : "v"(a), "v"(b)); return r; };
  asm volatile("s_nop 0" : : "v"(k), "v"(f));
  const uint32_t p32 = pk(k[3], k[2]), p10 = pk(k[1], k[0]), p21 = pk(k[2], k[1]), p01 = pk(f[0], f[1]), p23 = pk(f[2], f[3]);
  *LP(u32x2, t.pd + 3 * TDP + 6) = (u32x2){p32, p10};
  *LP(uint32_t, t.pd + TDP + 2) = p32; *LP(uint32_t, t.pd + TDP + 6) = p10;
  *LP(uint32_t, t.pd + 2) = p21; *LP(uint32_t, t.pd + 2 * TDP + 6) = p21;
  *LP(u16, t.pd) = (u16)p32; *LP(u16, t.pd + 2 * TDP + 4) = (u16)p32;
  asm volatile("ds_write_b16_d16_hi %0, %1 offset:6\n\tds_write_b16_d16_hi %0, %1 offset:%2" : : "v"(t.pd), "v"(p10), "n"(2 * TDP + 10) : "memory");
  if (ct) {
    const uint32_t va = (uint32_t)__builtin_amdgcn_readlane((int)p32, 63), vb = (uint32_t)__builtin_amdgcn_readlane((int)p23, 15) >> 16;
    *LP(u16, t.pe) = (u16)((lane < 12) ? va : vb);
  }
  if (lane < 32) {
    *LP(u16, t.pf + 128) = (u16)p01; *LP(u16, t.pg + 128) = (u16)(p01 >> 16);
    *LP(u16, t.pf) = (u16)p23; *LP(u16, t.pg) = (u16)(p23 >> 16);
  }
  return (uint32_t)(127 + 5 - ts) << 23;
}
__device__ __forceinline__ uint32_t hadd(uint32_t a, uint32_t b) { return __builtin_bit_cast(uint32_t, __builtin_bit_cast(f16x2, a) + __builtin_bit_cast(f16x2, b)); }
template <bool FULL> __device__ __forceinline__ void sums(char* sm, const u32x4 p, const u32x4 c) {
  const uint32_t D[8] = {p[0], p[1], p[2], p[3], c[0], c[1], c[2], c[3]};
  uint32_t O[7];
#pragma unroll
  for (int j = 0; j < 7; ++j) O[j] = __builtin_amdgcn_alignbit(D[j + 1], D[j], 16);
  u32x4 a, b;
#pragma unroll
  for (int j = 0; j < 4; ++j) a[j] = hadd(D[j + 2], hadd(O[j + 1], O[j + 2]));
  *(u32x4*)(sm + C_G3 + 32) = a;
#pragma unroll
  for (int j = 0; j < 4; ++j) b[j] = hadd(a[j], hadd(D[j + 1], D[j + 3]));
  *(u32x4*)(sm + C_G5 + 32) = b;
  if (FULL) {
#pragma unroll
    for (int j = 0; j < 4; ++j) a[j] = hadd(b[j], hadd(O[j], O[j + 3]));
    *(u32x4*)(sm + C_G7 + 32) = a;
#pragma unroll
    for (int j = 0; j < 4; ++j) b[j] = hadd(a[j], hadd(D[j], D[j + 4]));
    *(u32x4*)(sm + C_G9 + 32) = b;
  }
}
__device__ __forceinline__ void count_up(uint32_t a) {
  uint32_t t;
  asm volatile("s_mov_b64 exec, 1\n\tv_mov_b32 %0, 1\n\tds_add_u32 %1, %0\n\ts_mov_b64 exec, -1" : "=&v"(t) : "v"(a) : "memory");
}
__device__ __forceinline__ int pad_at(int r) { return 16 * (r & 1) + 3200 * (r >> 1) - ((r >= 6) ? 3200 - 2096 : 0); }
struct COps { uint32_t b, a, o, g3, k3, g4, k4, f; };
__device__ __forceinline__ void conv_ops(uint32_t tb, uint32_t ub, uint32_t cb, int lane, COps& e) {
  const uint32_t j = lane & 15, kq = lane >> 4, q4 = (lane >> 2) & 3, p4 = lane & 3;
  e.b = ub + 32u * j + 16u * kq;
  e.a = tb + p4 * TDP + 32u - 8u * q4 + 16u * kq;
  e.g3 = cb + C_G5 + 8 + 64u * kq + 8u * (q4 + p4) + (q4 & 1u) * 3200u;
  e.g4 = cb + C_G9 + 8 + 128u * kq + 16u * q4 + 8u * p4 + (q4 & 1u) * 2096u;
  e.k3 = tb + T_3 + 120 - 8u * j + 16u * kq;
  e.k4 = tb + T_4 + 60 - 4u * j + 16u * kq;
  asm volatile("" : "+v"(e.b), "+v"(e.a), "+v"(e.g3), "+v"(e.g4), "+v"(e.k3), "+v"(e.k4));
}
#define CONV_RUN(prog, e, ...) do { int prog_ = (prog); \
    asm volatile("s_call_b64 s[90:91], kf_conv" : "+{s89}"(prog_), "+{v2}"(e.b), "+{v3}"(e.a), "+{v4}"(e.o), "+{v5}"(e.g3), "+{v6}"(e.k3), "+{v7}"(e.g4), \
                 "+{v8}"(e.k4), "+{v9}"(e.f) __VA_ARGS__ : : CONV_CLOB); } while (0)

template <bool E>
__device__ __forceinline__ void ln_stats(const uint32_t* v, int lane, float sc, f32x2* t, f32x4 z, f32x2& rc, f32x2& nc, float& e) {
  float s1a = z[0], s2a = z[1], s1b = z[2], s2b = z[3];
#pragma unroll
  for (int k = 0; k < 4; ++k) {
    s1a = dot2(v[k], 0x3f803f80u, s1a); s2a = dot2(v[k], v[k], s2a);
    s1b = dot2(v[4 + k], 0x3f803f80u, s1b); s2b = dot2(v[4 + k], v[4 + k], s2b);
  }
#pragma unroll
  for (int k = 0; k < 9; ++k) t[k] = (f32x2){bflo(v[k]), bfhi(v[k])};
  const float rs = row_sum(fold16(dot2(v[8], 0x3f803f80u, fold32(s1a, s1b)), dot2(v[8], v[8], fold32(s2a, s2b))));
  if (E) { e = row_sum(e); e += __int_as_float(__builtin_amdgcn_update_dpp(0, __float_as_int(e), 0x142, 0xa, 0xf, false)); }
  const f32x2 mean = (f32x2){rdl(rs, 15), rdl(rs, 47)} * (1.f / D_), m2 = mean * mean;
  f32x2 var = (f32x2){rdl(rs, 31), rdl(rs, 63)} * (1.f / D_) - m2;
  asm volatile("" : "+v"(t[0]), "+v"(t[1]), "+v"(t[2]), "+v"(t[3]), "+v"(t[4]), "+v"(t[5]), "+v"(t[6]), "+v"(t[7]), "+v"(t[8]));
  if (__builtin_expect(!(m2[0] <= 16.f * var[0] && m2[1] <= 16.f * var[1]), 0)) {
    float q[3] = {0.f, 0.f, 0.f};
#pragma unroll
    for (int k = 0; k < 9; ++k) {
      const f32x2 a = t[k] - ((k < 4 || (k == 8 && lane < 32)) ? mean[0] : mean[1]);
      q[k >> 2] += a[0] * a[0] + a[1] * a[1];
    }
    float qa = q[0] + ((lane < 32) ? q[2] : 0.f), qb = q[1] + ((lane < 32) ? 0.f : q[2]);
    wave_sum2(qa, qb);
    var = (f32x2){qa, qb} * (1.f / D_);
  }
  const uint32_t mp = pack2(mean[0], mean[1]), vp = pack2(var[0], var[1]);
  const f32x2 ve = (f32x2){bflo(vp), bfhi(vp)} + 1e-5f;
  const uint32_t e2 = pack2(ve[0], ve[1]);
  const uint32_t sp = pack2(__builtin_amdgcn_sqrtf(bflo(e2)), __builtin_amdgcn_sqrtf(bfhi(e2)));
  rc = (f32x2){__builtin_amdgcn_rcpf(bflo(sp)), __builtin_amdgcn_rcpf(bfhi(sp))} * sc;
  nc = -(f32x2){bflo(mp), bfhi(mp)} * rc;
}
template <bool H>
__device__ __forceinline__ void ln_apply(const f32x2* t, int lane, f32x2 rc, f32x2 nc, uint32_t* c) {
  const float nm[3] = {nc[0], nc[1], (lane < 32) ? nc[0] : nc[1]}, rr[3] = {rc[0], rc[1], (lane < 32) ? rc[0] : rc[1]};
  f32x2 dd[9];
#pragma unroll
  for (int k = 0; k < 9; ++k) dd[k] = __builtin_elementwise_fma(t[k], (f32x2){rr[k >> 2], rr[k >> 2]}, (f32x2){nm[k >> 2], nm[k >> 2]});
  asm volatile("" : "+v"(dd[0]), "+v"(dd[1]), "+v"(dd[2]), "+v"(dd[3]), "+v"(dd[4]), "+v"(dd[5]), "+v"(dd[6]), "+v"(dd[7]), "+v"(dd[8]));
#pragma unroll
  for (int k = 0; k < 9; ++k) {
    const f32x2 d = dd[k];
    typedef __attribute__((ext_vector_type(2))) uint16_t u16x2;
    if (H) c[k] = __builtin_bit_cast(uint32_t, __builtin_elementwise_add_sat(__builtin_bit_cast(u16x2, __builtin_amdgcn_cvt_pkrtz(d[0], d[1])), (u16x2){4, 4}));
    else c[k] = pack2(d[0], d[1]);
  }
}
__device__ __forceinline__ uint32_t med3u(uint32_t a, uint32_t b, uint32_t c) { return max(min(a, b), min(max(a, b), c)); }
__device__ __forceinline__ uint32_t oct_maxu(uint32_t v) {
  v = max(v, (uint32_t)DPP_I((int)v, 0xb1, 0xf));
  v = max(v, (uint32_t)DPP_I((int)v, 0x4e, 0xf));
  v = max(v, (uint32_t)DPP_I((int)v, 0x141, 0xf));
  return v;
}

#define KARGS const u16* __restrict__ x, const u16* __restrict__ Are, const u16* __restrict__ Aim, const u16* __restrict__ Bre, \
    const u16* __restrict__ Bim, const u16* __restrict__ Cre, const u16* __restrict__ Cim, const u16* __restrict__ logdt, \
    const u16* __restrict__ Dskip, char* __restrict__ ws, const u16* __restrict__ wr, const u16* __restrict__ wg, \
    const u16* __restrict__ wu, const u16* __restrict__ wd, u16* __restrict__ out
#define KPASS x, Are, Aim, Bre, Bim, Cre, Cim, logdt, Dskip, ws, wr, wg, wu, wd, out
template <int ROLE, int FFN = -1>
__device__ __forceinline__ void role_body(char* smem, KARGS) {
  const int tid = threadIdx.x, lane = tid & 63;
  const int w = __builtin_amdgcn_readfirstlane(tid >> 6), wl = w & 3;
  const int b = blockIdx.x & 255, bx = b & 7, bj = b >> 3;
  constexpr bool isl = ROLE == 1 || ROLE == 2;
  const int tg = isl ? 32 * (bj >> 2) + 4 * bx + (bj & 3) : 32 * bx + bj;
  u32x4 xa, xc;
  uint32_t ep, r0;
#define P_WAIT(n, ...) asm volatile("s_waitcnt vmcnt(" #n ")\n\tv_lshlrev_b32 %0, 16, v100\n\tv_lshlrev_b32 %1, 16, v101\n\tv_lshlrev_b32 %2, 16, v102" \
    : "=v"(ldt), "=v"(are), "=v"(aim) : : "v100", "v101", "v102" __VA_ARGS__)
#define P_MORE() asm volatile("v_lshlrev_b32 %0, 16, v103\n\tv_lshlrev_b32 %1, 16, v104\n\tv_lshlrev_b32 %2, 16, v105\n\tv_lshlrev_b32 %3, 16, v106" \
    : "=v"(bre), "=v"(bim), "=v"(cre), "=v"(cim) : : "v103", "v104", "v105", "v106")
  TSD;
  TS(0, 0);
  const uint32_t lb0 = (uint32_t)(uintptr_t)(__attribute__((address_space(3))) char*)smem;
  const int i = lane & 15, kq = lane >> 4;
  const u32x4 ones = {0xffffffffu, 0xffffffffu, 0xffffffffu, 0xffffffffu}, zero4 = {0u, 0u, 0u, 0u};
  auto u_again = [&](u32x4& uw, const u32x4& ut, uint32_t uo) {
#pragma unroll 1
    for (int spin = 0; spin < SPIN_CAP; ++spin) {
      const bool na = ((uw[0] | uw[1] | uw[2] | uw[3]) & UMASK) != 0u;
      if (__builtin_expect(!__any(na), 1)) break;
      TSC(19);
      SLEEP(USLEEP);
      if (na) { LD16W(uw, uo); uw ^= ut; }
    }
  };
  constexpr uint32_t S16 = 0x41800000u, LMASK = 0x00070007u;
  auto v_put = [&](u32x4 v, const u32x4& ut, bool other, uint32_t vo) {
    const bool second = __any(other || ((v[0] | v[1] | v[2] | v[3]) & 0x40004000u) != 0u);
    if (__builtin_expect(second, 0)) v = (v & ~LMASK) | (~ut & LMASK); else v = (v & ~LMASK) | ut;
    ST16(vo, v);
    return second;
  };
  auto v_end = [&](uint32_t T, int n) { DRAIN(); if (lane == 0) ST4(O_DN + 16u * n, T); };
  if constexpr (!isl) {
    const int ch = wl >> 1, tl = tid & 255;
    int* evp = (int*)(smem + O_FL + 16 * ch);
    float ldt, are, aim, dsk;
    if constexpr (ROLE == 0) {
      __builtin_amdgcn_s_setprio(KPRIO);
      COps e;
      conv_ops(lb0 + (uint32_t)(ch * TB), lb0 + (uint32_t)(O_UL + ch * UB), lb0 + (uint32_t)(O_IM + ch * GB), lane, e);
      e.o = lb0 + O_ST + (uint32_t)(16 * ((4 * i + 2 * (kq >> 1) + ch) ^ (i >> 1)) + 8 * (kq & 1));
      e.f = lb0 + O_FL;
      TOps t;
      table_ops(lb0 + (uint32_t)(O_IM + 128 * IP * ch), lb0 + (uint32_t)(TB * ch), wl & 1, lane, t);
      uint32_t pz = lb0 + O_IM + (uint32_t)(((lane >> 3) & 1) * GB + pad_at(lane & 7)), im = lb0 + (uint32_t)(O_IM + IP * tl);
      asm volatile("" : "+v"(e.o), "+v"(e.f), "+v"(pz), "+v"(im));
      float bre, bim, cre, cim;
      P_WAIT(0, , "v103", "v104", "v105", "v106", "v107", "v109");
      asm volatile("" : "={v109}"(ep));
      const uint32_t T = ((uint32_t)__builtin_amdgcn_readfirstlane(ep) + 1u) * 0x9E3779B1u;
      if (w == 3 && lane < 8) ST16(O_KEY + 128u * b + 16u * lane, ones);
      P_MORE();
      asm volatile("v_lshlrev_b32 %0, 16, v107" : "=v"(dsk) : : "v107");
      TS(0, 1);
      evp[wl & 1] = gen_y(ldt, are, aim, bre, bim, cre, cim, im, [] {} TSA);
      TS(0, 8);
      BAR();
      TS(0, 2);
      const uint32_t cs = table_tile(t, wl & 1, lane, lb0 + O_FL + 16 * ch, dsk TSA);
      *(uint32_t*)(smem + O_FL + 40 + 4 * ch) = cs;
      DRAIN();
      if ((wl & 1) && lane == 0) asm volatile("global_store_dwordx2 %0, %1, %2 sc1" : : "v"(O_SC + 16u * (2 * tg + ch)), "v"((u32x2){T, cs}), "s"(ws) : "memory");
      TS(0, 9);
      gsync0(lb0 + O_FL + 36, 4);
      TS(0, 3);
      *(u32x4*)(smem + O_ST + 16 * tl) = ones;
      *(u32x4*)(smem + O_ST + 4096 + 16 * tl) = ones;
      if (w == 0) *LP(u32x4, pz) = zero4;
      CONV_RUN(wl & 1, e);
      TS(0, 5);
    } else {
      __builtin_amdgcn_s_setprio(UPRIO);
#pragma unroll
      for (int k = 0; k < 3; ++k) *(u32x4*)(smem + ((16 * (tl + 256 * k) < O_FL) ? 16 * (tl + 256 * k) : O_FL + 32)) = zero4;
      if (w == 7) *(u32x4*)(smem + O_UL + (lane >> 5) * UB + 16 * min(lane & 31, 29)) = zero4;
      uint32_t uo = u_at(tl >> 1, 2 * tg + (tid & 1)), go = O_SU + (uint32_t)(256 * bx + 32 * wl + (lane & 31) + 4 * (lane & 32)) * 128u;
      uint32_t ul = (uint32_t)(O_UL + (tid & 1) * UB + C_U - 16 + 16 * (tl >> 1)), ug = (uint32_t)(O_IM + (tid & 1) * GB + 16 * (tl >> 1)), so = lb0 + O_ST + 16u * (uint32_t)(lane ^ (lane >> 3));
      uint32_t vo = O_V + (uint32_t)tg * VP + 16u * lane;
      asm volatile("" : "+v"(uo), "+v"(go), "+v"(ul), "+v"(ug), "+v"(so), "+v"(vo));
      P_WAIT(1, , "v109");
      TS(0, 1);
      evp[2 + (wl & 1)] = gen_x(ldt, are, aim, smem + O_IM + IP * tl);
      asm volatile("s_waitcnt vmcnt(0)" : "={v109}"(ep));
      const uint32_t T = ((uint32_t)__builtin_amdgcn_readfirstlane(ep) + 1u) * 0x9E3779B1u;
      TS(0, 8);
      BAR();
      TS(0, 2);
      u32x4 ut = {utag(T, 0), utag(T, 1), utag(T, 2), utag(T, 3)};
      asm volatile("" : "+v"(ut));
      SLEEP(GSLEEP);
      u32x2 pv;
      pv[1] = gate(ws, go, T);
      TS(0, 3);
      until(latest(pv[1]) + UD);
      TS(0, 7);
      u32x4 uw, ux;
      asm volatile("global_load_dwordx4 %0, %2, %4 sc1\n\tglobal_load_dwordx4 %1, %3, %4 sc1"
                   : "=&v"(uw), "=&v"(ux) : "v"(uo), "v"(uo + 64 * UP * 32), "s"(ws) : "memory");
      __builtin_amdgcn_s_setprio(FPR);
      asm volatile("s_waitcnt vmcnt(1)" : "+v"(uw));
      uw ^= ut;
      *(u32x4*)(smem + ul + 16) = uw;
      asm volatile("s_waitcnt vmcnt(0)" : "+v"(ux));
      TS(0, 11);
      ux ^= ut;
      *(u32x4*)(smem + ul + 2048 + 16) = ux;
      if (__builtin_expect(__any(((uw[0] | uw[1] | uw[2] | uw[3] | ux[0] | ux[1] | ux[2] | ux[3]) & UMASK) != 0u), 0)) {
        u_again(uw, ut, uo);
        u_again(ux, ut, uo + 64 * UP * 32);
        *(u32x4*)(smem + ul + 16) = uw;
        *(u32x4*)(smem + ul + 2048 + 16) = ux;
      }
      BAR();
      TS(0, 4);
      const u32x4 pw = *(const u32x4*)(smem + ul);
      if (wl < 2) {
        const u32x4 px = *(const u32x4*)(smem + ul + 2048);
        sums<true>(smem + ug, pw, uw);
        sums<false>(smem + ug + 2048, px, ux);
      } else sums<true>(smem + ug, pw, uw);
      count_up(lb0 + O_FL + 32);
      __builtin_amdgcn_s_setprio(SPR);
      TS(0, 5);
      const bool other = *(const uint32_t*)(smem + O_FL + 40 + 4 * (lane & 1)) != S16;
      bool second = false;
#pragma unroll
      for (int k = 0; k < 2; ++k) {
        const uint32_t o = ((k ? 0x3247u : 0x1065u) >> (4 * wl) & 7u) * 1024u;
        u32x4 v;
#pragma unroll 1
        for (int spin = 0; spin < SPIN_CAP; ++spin) {
          asm volatile("ds_read_b128 %0, %1\n\ts_waitcnt lgkmcnt(0)" : "=&v"(v) : "v"(so + o) : "memory");
          if (!__any(max4(v) == 0xffffffffu)) break;
          __builtin_amdgcn_s_sleep(VPS);
        }
        TS(0, 12 + k);
        second |= v_put(v, ut, other, vo + o);
        TS(0, 10 - 4 * k);
      }
      if (__builtin_expect(second, 0)) v_end(T, 4 * tg + wl);
    }
    TSE();
  } else {
  uint32_t* cand = (uint32_t*)(smem + LM);
  uint32_t* sel = cand + 128;
  uint32_t* kl = sel + 48;
  uint32_t* cnt = sel + 64;
  const uint32_t ca = lb0 + LM + 4 * (128 + 64);
  u16* hm = (u16*)(cnt + 12);
  uint32_t T;
  const bool ffn = (FFN < 0) ? bj < 18 : FFN, isx = FFN != 1 && bj >= 24;
  const int fb = 18 * bx + bj;
  constexpr bool ln2 = ROLE == 2;
  const int g0 = tg, g2 = tg;
  u32x4 bw[18];
#pragma unroll
  for (int s = 0; s < 9; ++s) bw[s] = (u32x4){0u, 0u, 0u, 0u};
  int tid2 = threadIdx.x;
  uint32_t ts1 = 0u;
  auto rest = [&] {
    if (ts1 && (int)((uint32_t)__builtin_amdgcn_s_memrealtime() - ts1) > 0) { TS(0, 8); until(ts1 + TSTOP + RD); if (RW) gwait(ca, 4); ts1 = 0u; }
  };
  auto gu_dma = [&](int q0, int q1, auto depth) {
    const char* gs = (const char*)((w < 2) ? wg : wu) + (size_t)((144 * (w & 1)) * (F_ * 2) + fb * 64);
    const uint32_t gv = (uint32_t)((lane >> 2) * (F_ * 2) + 16 * ((lane & 3) ^ (lane >> 5) ^ (w & 2)));
#pragma unroll 1
    for (int q = q0; q < q1; ++q) {
      const int k = (int)((((bj & 1) ? ORD1 : ((unsigned __int128)0x876543210u << 36 | 0x876543210ull)) >> (4 * (q - (q >= 9 ? 9 : 0)) + (q >= 9 ? 36 : 0))) & 15u), h = q >= 9;
      if (q >= RQ) rest();
      dma16(gs + (size_t)(288 * h + 16 * k) * (F_ * 2), gv, lb0 + LW * h + 9216 * w + 1024 * k);
      asm volatile("s_waitcnt vmcnt(%0)" : : "n"(depth.value) : "memory");
    }
  };
  auto down_dma = [&](int k0, int k1, int lane) {
#pragma unroll 1
    for (int k = k0; k < k1; ++k) {
      const int o = 9216 * wl + 2304 * k;
      const char* ds = (const char*)wd + (size_t)(fb * 36864 + o);
      const uint32_t dl = lb0 + o + 32 * ((4 * wl + k + 1) >> 1);
      if (ROLE == 1) rest();
      dma16d(ds, 16u * lane, dl);
      dma16d(ds + 1024, 16u * lane, dl + 1024);
      uint32_t keep;
      asm volatile("s_mov_b32 %0, m0\n\ts_mov_b32 m0, %3\n\ts_mov_b64 exec, 0xffff\n\tglobal_load_lds_dwordx4 %1, %2" DPOL2 "\n\ts_mov_b64 exec, -1\n\ts_mov_b32 m0, %0\n\ts_waitcnt vmcnt(%4)"
                   : "=&s"(keep) : "v"(16u * lane), "s"(ds + 2048), "s"(dl + 2048), "n"(ROLE == 1 ? DDEPTH : 40) : "memory");
    }
  };
  auto gu_read = [&](int h) {
    const uint32_t ra = lb0 + LW * h + (uint32_t)(((lane >> 1) & 1) * 18432 + (8 * kq + ((lane >> 2) & 3)) * 64 + ((w ^ (kq & 1) ^ (lane & 2)) * 16) + 8 * (lane & 1));
#pragma unroll
    for (int s = 0; s < 9; ++s) {
      const u32x2 h0 = trd(ra + 2048 * s), h1 = trd(ra + 2048 * s + 256);
      bw[9 * h + s] = (u32x4){h0[0], h0[1], h1[0], h1[1]};
    }
#pragma unroll
    for (int s = 0; s < 9; ++s) asm volatile("" : "+v"(bw[9 * h + s]));
  };
  uint32_t tv = 0u, tk = 0u;
  const uint32_t vearly = (uint32_t)((VEARLY >> (8 * (g0 >> 5))) & 0xffull);
  auto w_line = [&](int n) {
    const int q = min(lane, 62) / 9, r = min(7 * (n >> 2) + q, 287), h = r >= 144;
    return (const char*)((n & 1) ? wu : wg) + (uint32_t)((r + 144 * (h + ((n >> 1) & 1))) * (F_ * 2) + bx * 1152 + (min(lane, 62) - 9 * q) * 128);
  };
#define TOUCH(pa) asm volatile("global_load_dword v126, %0, off" DPOL4 : : "v"(pa) : "memory", "v126")
  uint32_t wv[5], xv[9];
  auto rows_xw = [&](int lane) {
    u32x4 wa, xr, xs;
    const uint32_t xo = (uint32_t)((8 * g2 + 2 * wl) * (D_ * 2));
    asm volatile("global_load_dwordx4 %0, %5, %9\n\tglobal_load_dword %1, %6, %9 offset:1024\n\t"
                 "global_load_dwordx4 %2, %7, %10\n\tglobal_load_dwordx4 %3, %7, %10 offset:1152\n\tglobal_load_dword %4, %8, %10 offset:1024\n\ts_waitcnt vmcnt(0)"
                 : "=&v"(wa), "=&v"(wv[4]), "=&v"(xr), "=&v"(xs), "=&v"(xv[8])
                 : "v"(16u * lane), "v"(4u * (lane & 31)), "v"(xo + 16u * lane), "v"(xo + 4u * lane + 32u * (lane & 32)), "s"(wr), "s"(x) : "memory");
#pragma unroll
    for (int k = 0; k < 4; ++k) { wv[k] = wa[k]; xv[k] = xr[k]; xv[4 + k] = xs[k]; }
  };
  auto ln1 = [&](int g, int o, uint32_t c0) {
    uint32_t uo = u_at(g, 64 * wl + lane);
    uint32_t ta = lb0 + (uint32_t)(o + ((lane >> 2) & 3) * RP + (64 * wl + 16 * kq + 4 * (lane & 3)) * 2);
    uint32_t la = (uint32_t)(o + 2 * wl * RP + 16 * lane), lt = (uint32_t)(o + (2 * wl + (lane >> 5)) * RP + 1024 + 4 * (lane & 31));
    uint32_t sg = O_SU + (uint32_t)(256 * (lane >> 3) + g) * 128u + 16u * (lane & 7);
    asm volatile("" : "+v"(uo), "+v"(ta), "+v"(la), "+v"(lt), "+v"(sg));
    f32x4 z4 = {0.f, 0.f, 0.f, 0.f};
    asm volatile("" : "+v"(z4));
    asm volatile("s_waitcnt vmcnt(0)" : "={v[100:103]}"(xa), "={v[104:107]}"(xc), "={v108}"(r0), "={v109}"(ep));
    TS(0, 1);
    const uint32_t v[9] = {xa[0], xa[1], xa[2], xa[3], xc[0], xc[1], xc[2], xc[3], r0};
    uint32_t c[9];
    f32x2 tf[9];
    T = ((uint32_t)__builtin_amdgcn_readfirstlane(ep) + 1u) * 0x9E3779B1u;
    if (wl == 3) { const uint32_t now = (uint32_t)__builtin_amdgcn_s_memrealtime() + SD; ST16(sg, ((u32x4){T, now, T, now})); }
    const uint32_t tj = ((T >> (3 * wl)) & 0x00070007u) | (((T >> (12 + wl)) & 0x00010001u) << 14);
    uint32_t tw[3] = {(tj & 0xffffu) * 0x10001u, (tj >> 16) * 0x10001u, 0u};
    tw[2] = (lane < 32) ? tw[0] : tw[1];
    asm volatile("" : "+v"(tw[0]), "+v"(tw[1]), "+v"(tw[2]));
    f32x2 rc, nc;
    float e_;
    ln_stats<false>(v, lane, 0.03125f, tf, z4, rc, nc, e_);
    ln_apply<true>(tf, lane, rc, nc, c);
#pragma unroll
    for (int k = 0; k < 9; ++k) c[k] = (c[k] & ~UMASK) | tw[k >> 2];
    *(u32x4*)(smem + la) = (u32x4){c[0], c[1], c[2], c[3]};
    *(u32x4*)(smem + la + RP) = (u32x4){c[4], c[5], c[6], c[7]};
    *(uint32_t*)(smem + lt) = c[8];
    u32x4 ha, hb;
    {
      uint32_t t, cc, n = SPIN_CAP;
      asm volatile("s_mov_b64 exec, 1\n\tv_mov_b32 %2, 1\n\tds_add_u32 %5, %2\n\ts_mov_b64 exec, -1\n"
                   "1:\n\tds_read_b32 %2, %5\n\tds_read_b64_tr_b16 v[40:41], %6\n\tds_read_b64_tr_b16 v[42:43], %6 offset:%7\n\tds_read_b64_tr_b16 v[44:45], %6 offset:512\n\t"
                   "ds_read_b64_tr_b16 v[46:47], %6 offset:%8\n\ts_sub_u32 %4, %4, 1\n\ts_cbranch_scc1 2f\n\ts_waitcnt lgkmcnt(4)\n\tv_readfirstlane_b32 %3, %2\n\t"
                   "s_cmp_lt_u32 %3, 4\n\ts_cbranch_scc1 1b\n2:\n\ts_waitcnt lgkmcnt(0)"
                   : "=&{v[40:43]}"(ha), "=&{v[44:47]}"(hb), "=&v"(t), "=&s"(cc), "+s"(n) : "v"(c0), "v"(ta), "n"(4 * RP), "n"(512 + 4 * RP) : "memory", "scc");
    }
    ST16(uo, ha);
    ST16(uo + 64 * 128, hb);
    if (wl == 0) {
      const u32x2 h4 = trd(ta + 1024), h5 = trd(ta + 1024 + 4 * RP);
      ST16(uo + 128 * 128, ((u32x4){h4[0], h4[1], h5[0], h5[1]}));
    }
  };
  const int dX = 512 - 24 + 8 * bx + bj;
  if constexpr (ROLE == 2) {
    const int tl = tid - 256;
    if (XOFF && (uint32_t)(bj - 4) < 8u) {
      __builtin_amdgcn_s_setprio(3);
      ln1(tg + 160, 10240, ca + 32);
      TS(0, 2);
    }
    if (!isx && T0) {
      SLEEP(T0SLEEP);
      if (wl < T0) TOUCH(w_line(24 * wl + bj));
      if (T1) { asm volatile("s_waitcnt vmcnt(0)" ::: "memory"); SLEEP(T1SLEEP); if (wl < T1) TOUCH(w_line(24 * (T0 + wl) + bj)); }
    }
    if (isx) {
      __builtin_amdgcn_s_setprio(QPRIO0);
      *(u32x4*)(smem + X_TB + 16 * tl) = zero4;
      *(u32x4*)(smem + X_TB + 16 * min(256 + tl, (TB + C_U) / 16 - 1)) = zero4;
      const int pr = (wl + XROT) & 3;
      COps e;
      conv_ops(lb0 + X_TB, lb0 + X_UL, lb0 + X_IM, lane, e);
      TOps t;
      table_ops(lb0 + X_IM, lb0 + X_TB, wl & 1, lane, t);
      e.o = lb0 + X_ST + (uint32_t)(16 * ((2 * i + (kq >> 1)) ^ ((i >> 2) & 1)) + 8 * (kq & 1));
      e.f = ca + 28 - 32;
      asm volatile("" : "+v"(e.o), "+v"(e.f));
      float ldt, are, aim, bre, bim, cre, cim, dsk;
      P_WAIT(0, , "v103", "v104", "v105", "v106", "v107", "v109");
      asm volatile("" : "={v109}"(ep));
      T = ((uint32_t)__builtin_amdgcn_readfirstlane(ep) + 1u) * 0x9E3779B1u;
      P_MORE();
      asm volatile("v_lshlrev_b32 %0, 16, v107" : "=v"(dsk) : : "v107");
      TS(0, 1);
      __builtin_amdgcn_s_setprio(QPRIO);
      int* evp = (int*)(sel + 44);
      char* im = smem + X_IM + IP * (tl & 127);
      evp[wl] = (wl < 2) ? gen_y(ldt, are, aim, bre, bim, cre, cim, lb0 + X_IM + IP * tl, [] {} TSA) : gen_x(ldt, are, aim, im);
      TS(0, 2);
      __builtin_amdgcn_s_setprio(QPRIO2);
      gsync(ca + 8, 4);
      if (wl < 2) evp[-4 + wl] = (int)table_tile(t, wl & 1, lane, lb0 + LM + 4 * (128 + 44), dsk TSA);
      if (wl < 2) gsync0(ca + 8, 8); else { count_up(ca + 8); SLEEP(XSL); gwait(ca + 8, 8); }
      const uint32_t cs = (uint32_t)__builtin_amdgcn_readfirstlane(evp[-4]);
      if (wl == 2 && lane == 0) asm volatile("global_store_dwordx2 %0, %1, %2 sc1" : : "v"(O_SC + 16u * dX), "v"((u32x2){T, cs}), "s"(ws) : "memory");
      if (wl == 3) *(u32x4*)(smem + X_IM + pad_at(lane & 7)) = zero4;
      TS(0, 3);
      CONV_RUN(2 + pr, e, , "+{v11}"(tid2));
      {
        const int lane = tid2 & 63;
        const u32x4 v = *(const u32x4*)(smem + X_ST + 2048 * pr + 1024 * (lane >> 5) + 16 * ((lane & 31) ^ ((lane >> 3) & 1)));
        if (__builtin_expect(v_put(v, (u32x4){utag(T, 0), utag(T, 1), utag(T, 2), utag(T, 3)}, cs != S16, v_at(dX, 64 * pr + lane)), 0)) {
          DRAIN();
          if (lane == 0) ST4(O_DN + 16u * (4 * (dX - 256) + pr), T);
        }
      }
      TS(0, 6);
    }
    __builtin_amdgcn_s_setprio(PPRIO);
    asm volatile("" : "+v"(tid2));
    if (isx) {
      const int lane = tid2 & 63;
      const u32x4 ra = *(const u32x4*)(smem + X_RW(wl) + 16 * lane), rb = *(const u32x4*)(smem + X_RW(wl) + 1024 + 16 * lane), rw = *(const u32x4*)(smem + X_WR + 16 * lane);
      const u32x2 rc = *(const u32x2*)(smem + X_RW(wl) + 2048 + 8 * lane);
#pragma unroll
      for (int k = 0; k < 4; ++k) { xv[k] = ra[k]; xv[4 + k] = rb[k]; wv[k] = rw[k]; }
      xv[8] = rc[0]; wv[4] = rc[1];
    } else rows_xw(tid2 & 63);
    if (!isx && !ffn) {
      SLEEP(RSLEEP);
#pragma unroll
      for (int k = 0; k < 7; ++k) ST16(O_SL + (uint32_t)((6 * bx + bj - 18) * 1792 + 256 * k - 256 + tid2) * 16u, ones);
      DRAIN();
    }
    if (!isx) BAR();
    const uint32_t tp = (uint32_t)__builtin_amdgcn_readfirstlane(cnt[4]);
    if (!isx) T = (uint32_t)__builtin_amdgcn_readfirstlane(sel[43]);
    tv = tp + UD + VD2 - vearly;
  } else {
    __builtin_amdgcn_s_setprio(3);
    if (tid < 12) cnt[tid] = 0u;
    BAR();
    if (tid < 20) ((uint32_t*)(hm + 320))[tid] = 0u;
    if (tid >= 184) *(u32x4*)(smem + L_SC - 16 * 184 + 16 * tid) = (u32x4){0x41804180u, 0x41804180u, 0x41804180u, 0x41804180u};
    uint32_t sink = 0;
    if (XOFF && isx) {
      asm volatile("s_waitcnt vmcnt(0)" : "={v109}"(ep));
      T = ((uint32_t)__builtin_amdgcn_readfirstlane(ep) + 1u) * 0x9E3779B1u;
    } else ln1(tg, 0, ca + 12);
    TS(0, 2);
    __builtin_amdgcn_s_setprio(NPRIO);
    if (!isx && 3 - w < TB1) TOUCH(w_line(24 * (3 - w) + 23 - bj));
    asm volatile("s_waitcnt vmcnt(0)" : "+v"(sink) : : "memory");
    TS(0, 13);
    if (w == 0) {
      const uint32_t xr = O_XC + (uint32_t)((511 - 2 * (g0 + w)) * 144 + lane) * 16u;
      ST16(xr, ones);
      ST16(xr + 1024u, ones);
      if (lane < 16) ST16(xr + 2048u, ones);
    }
    if (w == 2 && b < 72 && lane < 8) ST16(O_FB + (uint32_t)(8 * b + lane) * 16u, ones);
    if (isx) {
      rows_xw(lane);
      char* sp = smem + X_RW(w) + 16 * lane;
      *(u32x4*)sp = (u32x4){xv[0], xv[1], xv[2], xv[3]};
      *(u32x4*)(sp + 1024) = (u32x4){xv[4], xv[5], xv[6], xv[7]};
      *(u32x2*)(smem + X_RW(w) + 2048 + 8 * lane) = (u32x2){xv[8], wv[4]};
      if (w == 0) *(u32x4*)(smem + X_WR + 16 * lane) = (u32x4){wv[0], wv[1], wv[2], wv[3]};
    }
    SLEEP(ESLEEP);
    uint32_t tu = latest(gate(ws, O_SU + (uint32_t)(256 * bx + 64 * w + 4 * i + (bj & 3)) * 128u, T));
    asm volatile("s_mov_b64 exec, 1\n\tds_max_u32 %0, %1\n\ts_mov_b64 exec, -1" : : "v"(ca + 16), "v"(tu) : "memory");
    if (w == 0) sel[43] = T;
    if (isx) {
      const uint32_t xo = u_at(tid, dX);
      char* sm = smem + X_UL + C_U - 16 + 16 * tid;
      const u32x4 ut = {utag(T, 0), utag(T, 1), utag(T, 2), utag(T, 3)};
      __builtin_amdgcn_s_setprio(3);
      until(tu + UD + XD);
      u32x4 uw;
      asm volatile("global_load_dwordx4 %0, %1, %2 sc1" : "=&v"(uw) : "v"(xo), "s"(ws) : "memory");
      asm volatile("s_waitcnt vmcnt(0)" : "+v"(uw));
      uw ^= ut;
      u_again(uw, ut, xo);
      *(u32x4*)(sm + 16) = uw;
      if (XF) { gsync0(ca + 12, XOFF ? 4 : 8); gwait(ca + 8, 8); } else BAR();
      if (w < 3) {
        const u32x4 pw = *(const u32x4*)sm;
        if (w < 2) sums<true>(smem + X_IM + 16 * tid, pw, uw); else sums<false>(smem + X_IM + 16 * tid, pw, uw);
        count_up(ca + 28);
      }
      if (XF) BAR();
    } else {
      until(tu + UD + WD);
      BAR();
    }
    tu = (uint32_t)__builtin_amdgcn_readfirstlane(cnt[4]);
    TS(0, 3);
    __builtin_amdgcn_s_setprio(DPRIO);
    if (ffn) {
      ts1 = (tu + UD + VD2 - TSTOP) | 1u;
      gu_dma(0, N0, IC<40>{});
      gu_dma(N0, NA, IC<WDEPTH>{});
      asm volatile("s_waitcnt vmcnt(%0)" : : "n"(NA - 9) : "memory");
      gsync0(ca + 4, 4);
      gu_read(0);
      asm volatile("s_waitcnt lgkmcnt(0)" ::: "memory");
      count_up(ca + 36);
      TS(0, 4);
      gu_dma(NA, 18, IC<WDEPTH2>{});
      gwait(ca + 36, 4);
      down_dma(0, KN, lane);
      asm volatile("s_waitcnt vmcnt(%0)" : : "n"(3 * KN) : "memory");
      count_up(ca + 4);
      TS(0, 7);
    } else {
      if (!isx && C3) {
        const int n = 6 * min(tid, 191) + bj - 18, r = n >> 2;
        const char* wb = (const char*)((n & 1) ? wu : wg) + bx * 1152;
        const int c0 = (3 - (int)((uintptr_t)wb >> 7)) & 7;
        wb += (uint32_t)((r + 144 * ((r >= 144) + ((n >> 1) & 1))) * (F_ * 2) + 128 * c0);
        TOUCH(wb);
        if (c0 == 0) TOUCH(wb + 1024);
      }
      auto d_line = [&](int k) {
        const int n = min((k * 14 + bj - 18) * 64 + lane, 5183), q = n / 1296, r = n - 1296 * q;
        return (const char*)wd + (uint32_t)(bx * 5184 + (r / 18) * 72 + 18 * (HREV ? 3 - q : q) + r % 18) * 128u;
      };
      int k = w;
      if (w < HW) {
#pragma unroll 1
        for (; k < 6 && (int)((uint32_t)__builtin_amdgcn_s_memrealtime() - (tu + UD + VD2 - HSTOP)) < 0; k += HW) { TOUCH(d_line(k)); DRAIN(); }
      }
      TS(0, 4);
      until(tu + UD + VD2 + RD2);
      if (RW) gwait(ca, 4);
      if (w < HW) {
#pragma unroll 1
        for (; k < 6; k += HW) { TOUCH(d_line(k)); DRAIN(); }
      }
      TS(0, 7);
    }
    tv = tu + UD + VD2 - vearly;
    tk = tu + UD + VD2 + KD;
  }
  {
  const int tid = tid2, lane = tid & 63, i = lane & 15, kq = lane >> 4;
  __builtin_amdgcn_s_setprio(3);
  const int PL = ffn ? 6400 : LW + 6656;
  f32x2 xf[9];
  f32x4 za = {0.f, 0.f, 0.f, 0.f}, zb = za;
  uint32_t al = O_XC + 64u * (i >> 3) + 16u * kq + 128u * w;
  uint32_t ha = (ROLE == 1) ? (uint32_t)((4 * (kq & 1) + 2 * (kq >> 1) + (i >> 3)) * 80 + 16 * w + 2 * (i & 7)) : 0u;
  uint32_t h0a = (uint32_t)(min(i, 8) * 80 + 16 * kq), h1a = (uint32_t)(min(i ^ 8, 8) * 80 + 16 * kq);
  uint32_t so = O_SL + (uint32_t)fb * SP + ((ROLE == 2) ? 16u * tid - 4096u : 8192u + 16u * lane);
  uint32_t da = lb0 + (uint32_t)((8 * kq + ((lane >> 2) & 3)) * 1152 + 32 * ((8 * kq + ((lane >> 2) & 3) + 2) >> 2) + 8 * (lane & 3) + ((ROLE == 2) ? 128 * wl : 1024));
  if (ROLE == 2) asm volatile("" : "+v"(h0a), "+v"(h1a), "+v"(so), "+v"(da)); else asm volatile("" : "+v"(ha), "+v"(al), "+v"(da));
  uint32_t hv[9] = {}, nv[9] = {}, keya = 0u, keyb = 0u;
  int rka = 0, rkb = 0;
  auto put_row = [&](const uint32_t* r, int j, const void* base, uint32_t ro) {
    const u32x4 tl = {r[8], (uint32_t)DPP_I((int)r[8], 0x55, 0xf), (uint32_t)DPP_I((int)r[8], 0xaa, 0xf), (uint32_t)DPP_I((int)r[8], 0xff, 0xf)};
    asm volatile("global_store_dwordx4 %0, %1, %2 sc1\n\ts_nop 1" : : "v"(ro + 16u * lane), "v"((u32x4){r[4 * j], r[4 * j + 1], r[4 * j + 2], r[4 * j + 3]}), "s"(base) : "memory");
    if ((lane & 35) == 32 * j) asm volatile("global_store_dwordx4 %0, %1, %2 offset:1024 sc1\n\ts_nop 1" : : "v"(ro + 4u * (lane & 31)), "v"(tl), "s"(base) : "memory");
  };
  if (ln2) {
    const int po = PL - 2560 * (ROLE == 1);
    uint32_t pa = (uint32_t)(po + 9216 * wl + 32 * lane), pb = (uint32_t)(po + 9216 * wl + 2048 + 8 * (lane & 31)), ps = (lane < 32) ? 0u : 16u;
    uint32_t kid = (uint32_t)(((255 - g2) << 5) | (7 - 2 * wl - (lane >> 5)));
    const int tl = tid - 256, t2 = tl + 128, d1 = 64 * (tl / 48) + tl % 48, d3 = 64 * (t2 / 48) + t2 % 48;
    const int d2 = (tl < 128) ? 64 * (tl >> 4) + 48 + (tl & 15) : min(384 + tl, 575);
    uint32_t vo = v_at(d1, g0), vo2 = v_at(d2, g0), vo3 = v_at(d3, g0), px = (uint32_t)(PL + 4 * d1), py = (uint32_t)(PL + 4 * d2), pz = (uint32_t)(PL + 4 * d3);
#pragma unroll
    for (int k = 0; k < 9; ++k) xf[k] = (f32x2){bflo(xv[k]), bfhi(xv[k])};
    asm volatile("" : "+v"(xf[0]), "+v"(xf[1]), "+v"(xf[2]), "+v"(xf[3]), "+v"(xf[4]), "+v"(xf[5]), "+v"(xf[6]), "+v"(xf[7]), "+v"(xf[8]));
    asm volatile("" : "+v"(za), "+v"(zb), "+v"(pa), "+v"(pb), "+v"(ps), "+v"(kid), "+v"(vo), "+v"(vo2), "+v"(vo3), "+v"(px), "+v"(py), "+v"(pz));
    u32x4 ut = {utag(T, 0), utag(T, 1), utag(T, 2), utag(T, 3)};
    float sv[10], ws_ = dot2(wv[4], 0x3f803f80u, 0.f) * 0.5f;
#pragma unroll
    for (int k = 0; k < 4; ++k) ws_ = dot2(wv[k], 0x3f803f80u, ws_);
    ws_ = wave_sum(ws_);
#pragma unroll
    for (int k = 0; k < 10; ++k) sv[k] = 16.f;
    uint32_t ka_ = lb0 + LM + 4u * (128 + 48 + 2 * wl + (lane >> 5)), kb_ = lb0 + LM + 4u * (128 + 48);
    uint32_t ko_ = O_KEY + (uint32_t)(256 * (lane & 7) + 8 * (g2 & 31) + (g2 >> 5)) * 16u;
    asm volatile("" : "+v"(ut), "+v"(ws_), "+v"(sv[0]), "+v"(sv[1]), "+v"(sv[2]), "+v"(sv[3]), "+v"(sv[4]), "+v"(sv[5]), "+v"(sv[6]), "+v"(sv[7]), "+v"(sv[8]), "+v"(sv[9]),
                 "+v"(ka_), "+v"(kb_), "+v"(ko_));
    if (ffn) gwait(ca + 36, 4);
    TS(0, 7);
    until(tv);
    TS(0, 8);
    u32x4 va, vc = ut, vb = ut;
    asm volatile("global_load_dwordx4 %0, %2, %4 sc1\n\ts_cmp_lt_u32 %5, 2\n\ts_cbranch_scc1 1f\n\tglobal_load_dwordx4 %1, %3, %4 sc1\n1:"
                 : "=&v"(va), "+v"(vc) : "v"(vo), "v"(vo3), "s"(ws), "s"(wl) : "memory", "scc");
    until(tv + V2D);
    asm volatile("s_cmp_eq_u32 %5, 3\n\ts_cbranch_scc1 1f\n\tglobal_load_dwordx4 %2, %3, %4 sc1\n\ts_waitcnt vmcnt(1)\n\ts_branch 2f\n1:\n\ts_waitcnt vmcnt(0)\n2:"
                 : "+v"(va), "+v"(vc), "+v"(vb) : "v"(vo2), "s"(ws), "s"(wl) : "memory", "scc");
    auto turn1 = [&] {
#pragma unroll
      for (int e = 0; e < 4; ++e) *(uint32_t*)(smem + px + 9216 * e) = va[e];
      if (wl >= 2) {
#pragma unroll
        for (int e = 0; e < 4; ++e) *(uint32_t*)(smem + pz + 9216 * e) = vc[e];
      }
    };
    auto turn = [&] {
      if (wl < 3) {
#pragma unroll
        for (int e = 0; e < 4; ++e) *(uint32_t*)(smem + py + 9216 * e) = vb[e];
      }
    };
    va ^= ut; vc ^= ut;
    turn1();
    asm volatile("s_waitcnt vmcnt(0)" : "+v"(vb) : : "memory");
    vb ^= ut;
    turn();
    const u32x4 vs = va | vb | vc;
    if (__builtin_expect(__any(((vs[0] | vs[1] | vs[2] | vs[3]) & UMASK) != 0u), 0)) {
#pragma unroll 1
      for (int spin = 0; spin < 4; ++spin) {
        const bool na = ((va[0] | va[1] | va[2] | va[3]) & UMASK) != 0u, nb = ((vb[0] | vb[1] | vb[2] | vb[3]) & UMASK) != 0u, nc = ((vc[0] | vc[1] | vc[2] | vc[3]) & UMASK) != 0u;
        if (!__any(na || nb || nc)) break;
        TSC(16);
        if (na) LD16(va, vo);
        if (nb) LD16(vb, vo2);
        if (nc) LD16(vc, vo3);
        asm volatile("s_waitcnt vmcnt(0)" : "+v"(va), "+v"(vb), "+v"(vc) : : "memory");
        if (na) va ^= ut;
        if (nb) vb ^= ut;
        if (nc) vc ^= ut;
      }
      auto again = [&](u32x4& v, uint32_t o, int d) {
        const int J = g0 >> 5;
        const uint32_t dn = O_DN + 16u * (uint32_t)((d < 512) ? 4 * (d >> 1) + ((0x01013232 >> (4 * J)) & 3) : 4 * (d - 256) + (J >> 1));
        bool pend = ((v[0] | v[1] | v[2] | v[3]) & UMASK) != 0u;
#pragma unroll 1
        for (int spin = 0; spin < SPIN_CAP && __any(pend); ++spin) {
          TSC(16);
          SLEEP(PSLEEP);
          if (pend) {
            uint32_t fin = ~T;
            if ((v[0] & v[1] & v[2] & v[3] & LMASK) == LMASK)
              asm volatile("global_load_dword %0, %1, %2 sc1\n\ts_waitcnt vmcnt(0)" : "=&v"(fin) : "v"(dn), "s"(ws) : "memory");
            LD16W(v, o);
            v ^= ut;
            pend = ((v[0] | v[1] | v[2] | v[3]) & UMASK) != 0u;
            if (pend && fin == T) {
              u32x2 sc;
              for (int k = 0; k < SPIN_CAP; ++k) {
                asm volatile("global_load_dwordx2 %0, %1, %2 sc1\n\ts_waitcnt vmcnt(0)" : "=&v"(sc) : "v"(O_SC + 16u * d), "s"(ws) : "memory");
                if (sc[0] == T) break;
              }
              *(u16*)(smem + L_SC + 2 * d) = (u16)(sc[1] >> 16);
              cnt[10] = 1u;
              v = (v ^ ut) & ~LMASK;
              pend = false;
            }
          }
        }
      };
      again(va, vo, d1); again(vc, vo3, d3); again(vb, vo2, d2);
      turn1();
      turn();
    }
    u32x4 a0, a1;
    u32x2 a2, cf;
    {
      uint32_t cc, n = SPIN_CAP;
      asm volatile("s_mov_b64 exec, 1\n\tv_mov_b32 v90, 1\n\tds_add_u32 %6, v90\n\ts_mov_b64 exec, -1\n"
                   "1:\n\tds_read2_b32 %3, %6 offset1:10\n\tds_read_b128 %0, %7\n\tds_read_b128 %1, %7 offset:16\n\tds_read_b64 %2, %8\n\t"
                   "s_sub_u32 %5, %5, 1\n\ts_cbranch_scc1 2f\n\ts_waitcnt lgkmcnt(3)\n\tv_readfirstlane_b32 %4, v90\n\t"
                   "s_cmp_lt_u32 %4, 4\n\ts_cbranch_scc1 1b\n2:\n\ts_waitcnt lgkmcnt(0)"
                   : "=&v"(a0), "=&v"(a1), "=&v"(a2), "=&{v[90:91]}"(cf), "=&s"(cc), "+s"(n) : "v"(ca), "v"(lb0 + pa), "v"(lb0 + pb) : "memory", "scc");
    }
    TS(0, 9);
    if (__builtin_expect(__builtin_amdgcn_readfirstlane(cf[1]) != 0, 0)) {
      const u32x4 q = *(const u32x4*)(smem + L_SC + 16 * lane);
      const uint32_t r = *(const uint32_t*)(smem + L_SC + 1024 + 4 * (lane & 31));
#pragma unroll
      for (int k = 0; k < 4; ++k) { sv[2 * k] = bflo(q[k]); sv[2 * k + 1] = bfhi(q[k]); }
      sv[8] = bflo(r); sv[9] = bfhi(r);
    }
    const uint32_t av[10] = {a0[0], a0[1], a0[2], a0[3], a1[0], a1[1], a1[2], a1[3], a2[0] >> ps, a2[1] >> ps};
    f32x2 h[9];
#pragma unroll
    for (int k = 0; k < 10; ++k) {
      asm("v_fma_mix_f32 %0, %1, %2, %3 op_sel_hi:[1,0,0]" : "=v"(h[(k < 8) ? k >> 1 : 8][k & 1]) : "v"(av[k]), "v"(sv[k]), "v"(xf[(k < 8) ? k >> 1 : 8][k & 1]));
      if (k < 8) asm("v_fma_mix_f32 %0, %1, %2, %3 op_sel:[1,0,0] op_sel_hi:[1,0,0]" : "=v"(h[4 + (k >> 1)][k & 1]) : "v"(av[k]), "v"(sv[k]), "v"(xf[4 + (k >> 1)][k & 1]));
    }
#pragma unroll
    for (int k = 0; k < 9; ++k) hv[k] = pack2(h[k][0], h[k][1]);
    const float da = dot2(hv[2], wv[2], dot2(hv[0], wv[0], zb[0])) + dot2(hv[3], wv[3], dot2(hv[1], wv[1], zb[1]));
    const float db = dot2(hv[6], wv[2], dot2(hv[4], wv[0], zb[2])) + dot2(hv[7], wv[3], dot2(hv[5], wv[1], zb[3]));
    float dt = dot2(hv[8], wv[4], fold32(da, db));
    f32x2 rc, nc;
    ln_stats<true>(hv, lane, 1.f, xf, za, rc, nc, dt);
    dt = (lane < 32) ? dt * rc[0] + ws_ * nc[0] : dt * rc[1] + ws_ * nc[1];
    const uint32_t bb = pack2(dt, dt) & 0xffff0000u;
    const uint32_t kk = (bb ^ (((uint32_t)((int)bb >> 31) & 0x7fff0000u) | 0x80000000u)) | kid;
    u32x4 ka, kb;
    uint32_t old;
    asm volatile("s_mov_b64 exec, %5\n\tds_write_b32 %3, %4\n\ts_mov_b64 exec, 1\n\tv_mov_b32 %2, 1\n\tds_add_rtn_u32 %2, %7, %2\n\ts_mov_b64 exec, -1\n\t"
                 "ds_read_b128 %0, %6\n\tds_read_b128 %1, %6 offset:16\n\ts_waitcnt lgkmcnt(0)"
                 : "=&v"(ka), "=&v"(kb), "=&v"(old) : "v"(ka_), "v"(kk), "s"(0x8000000080000000ull), "v"(kb_), "v"(ca) : "memory");
    if (__builtin_amdgcn_readfirstlane(old) == 7) {
      uint32_t m1 = max(max(ka[0], ka[1]), ka[2]), m2 = med3u(ka[0], ka[1], ka[2]), m3 = min(min(ka[0], ka[1]), ka[2]);
#pragma unroll
      for (int e = 0; e < 5; ++e) {
        const uint32_t kx = e ? kb[e - 1] : ka[3];
        m3 = med3u(m2, m3, kx); m2 = med3u(m1, m2, kx); m1 = max(m1, kx);
      }
      if (lane < 8) ST16(ko_, ((u32x4){m1 | 24u, m2 | 16u, m3 | 8u, T & ~1u}));
    }
    TS(0, 10);
    keya = (uint32_t)__builtin_amdgcn_readlane(kk, 31); keyb = (uint32_t)__builtin_amdgcn_readlane(kk, 63);
    ln_apply<false>(xf, lane, rc, nc, nv);
    gwait(ca, 8);
    if (wl == 1) {
      const u32x4 kv = *(const u32x4*)&kl[4 * (lane & 1)];
      if (lane < 2) ST16(O_KF + (uint32_t)(2 * g2 + lane) * 16u, ((u32x4){__builtin_amdgcn_perm(kv[1], kv[0], 0x07060302u), T, __builtin_amdgcn_perm(kv[3], kv[2], 0x07060302u), T}));
      if (ROLE == 2 && b == 0 && lane == 0) ST4(O_EP, T * 0x0E8B2F51u);
    }
    const uint32_t ko = kl[lane & 7];
    rka = FORCEFB + __popcll(__ballot(ko > keya) & 0xffull); rkb = FORCEFB + __popcll(__ballot(ko > keyb) & 0xffull);
#pragma unroll
    for (int k = 0; k < 9; ++k) {
      const f32x2 o = xf[k] + (f32x2){bflo(nv[k]), bfhi(nv[k])};
      hv[k] = pack2(o[0], o[1]);
      nv[k] = min(nv[k], 0xfffffffeu);
    }
    if (rka < 2) put_row(nv, 0, ws, O_XC + (uint32_t)(1023 - 4 * g2 - rka) * 1152u);
    if (rkb < 2) put_row(nv, 1, ws, O_XC + (uint32_t)(1023 - 4 * g2 - rkb) * 1152u);
    if (!LPOS && ffn) down_dma(KN, 4, lane);
  }
  uint32_t k3rd = 0u, k4th = 0u;
  if constexpr (ROLE == 1) {
    u32x4 key;
    const uint32_t kof = O_KEY + (uint32_t)(256 * bx + tid) * 16u;
    until(tk);
    if (!ffn) SLEEP(NSLEEP);
    LD16(key, kof);
    if (ffn) {
      gwait0(ca + 4, 8);
      gu_read(1);
    }
    asm volatile("s_waitcnt vmcnt(0)" : "+v"(key) : : "memory");
    if (__builtin_expect(__any(key[3] != (T & ~1u)), 0)) {
#pragma unroll 1
      for (int spin = 0; spin < SPIN_CAP; ++spin) {
        const bool need = key[3] != (T & ~1u);
        if (!__any(need)) break;
        TSC(15);
        SLEEP(KBACK);
        if (need) LD16W(key, kof);
      }
    }
    TS(0, 11);
    const uint32_t m1 = oct_maxu(key[0]);
    const bool hit = key[0] == m1;
    const uint32_t k0 = hit ? key[1] : key[0], k1 = hit ? key[2] : key[1];
    const uint32_t m2 = oct_maxu(k0);
    if ((lane & 7) == 0) *(u32x2*)&cand[tid >> 2] = (u32x2){m1, m2};
    k3rd = (k0 == m2) ? k1 : k0;
    k4th = (k0 == m2) ? (hit ? 0u : key[2]) : k1;
    if (RPF == 1 && !ffn && w == (bj & 3)) {
      const uint32_t r1 = O_XC + ((m1 >> 3) & 0x3ffu) * 1152u + 128u * (lane & 7), r2 = O_XC + ((m2 >> 3) & 0x3ffu) * 1152u + 128u * (lane & 7);
      asm volatile("global_load_dword v126, %0, %3 sc1\n\tglobal_load_dword v126, %1, %3 sc1\n\tglobal_load_dword v126, %2, %3 offset:1024 sc1"
                   : : "v"(r1), "v"(r2), "v"(((lane & 1) ? r1 : r2) - 128u * (lane & 7)), "s"(ws) : "memory", "v126");
    }
  }
  auto pick = [&](const uint32_t* cd, uint32_t* sl, bool pf = false) {
    BAR();
    if (RPF == 2 && ROLE == 2 && pf && ffn && w == 7) {
      const uint32_t r1 = O_XC + ((cd[16 * (bj & 3) + (lane >> 2)] >> 3) & 0x3ffu) * 1152u + 128u * (lane & 3);
      asm volatile("global_load_dword v126, %0, %1 sc1\n\tglobal_load_dword v126, %0, %1 offset:512 sc1\n\tglobal_load_dword v126, %0, %1 offset:640 sc1"
                   : : "v"(r1), "s"(ws) : "memory", "v126");
    }
    const uint32_t c = cd[lane], own = cd[8 * w + (lane & 7)];
    const u32x4 la = *(const u32x4*)&cd[8 * w], lb = *(const u32x4*)&cd[8 * w + 4];
    uint32_t r = C_;
    asm("v_cmp_gt_u32 s[40:41], %1, %2\n\tv_cmp_gt_u32 s[42:43], %1, %3\n\tv_cmp_gt_u32 s[44:45], %1, %4\n\tv_cmp_gt_u32 s[46:47], %1, %5\n\t"
        "v_cmp_gt_u32 s[48:49], %1, %6\n\tv_cmp_gt_u32 s[50:51], %1, %7\n\tv_cmp_gt_u32 s[52:53], %1, %8\n\tv_cmp_gt_u32 s[54:55], %1, %9\n\t"
        "s_bcnt1_i32_b64 s40, s[40:41]\n\ts_bcnt1_i32_b64 s42, s[42:43]\n\tv_writelane_b32 %0, s40, 0\n\ts_bcnt1_i32_b64 s44, s[44:45]\n\t"
        "v_writelane_b32 %0, s42, 1\n\ts_bcnt1_i32_b64 s46, s[46:47]\n\tv_writelane_b32 %0, s44, 2\n\ts_bcnt1_i32_b64 s48, s[48:49]\n\t"
        "v_writelane_b32 %0, s46, 3\n\ts_bcnt1_i32_b64 s50, s[50:51]\n\tv_writelane_b32 %0, s48, 4\n\ts_bcnt1_i32_b64 s52, s[52:53]\n\t"
        "v_writelane_b32 %0, s50, 5\n\ts_bcnt1_i32_b64 s54, s[54:55]\n\tv_writelane_b32 %0, s52, 6\n\tv_writelane_b32 %0, s54, 7"
        : "+v"(r) : "v"(c), "v"(la[0]), "v"(la[1]), "v"(la[2]), "v"(la[3]), "v"(lb[0]), "v"(lb[1]), "v"(lb[2]), "v"(lb[3])
        : "scc", "s40", "s41", "s42", "s43", "s44", "s45", "s46", "s47", "s48", "s49", "s50", "s51", "s52", "s53", "s54", "s55");
    sl[min(r, (uint32_t)C_)] = own;
    BAR();
  };
  auto rows = [&](uint32_t ao, auto&& meanwhile) {
    u32x4 aa, ab, ac = __builtin_bit_cast(u32x4, zb);
    asm volatile("global_load_dwordx4 %0, %3, %4 sc1\n\tglobal_load_dwordx4 %1, %3, %4 offset:512 sc1\n\t"
                 "s_cmp_lg_u32 %5, 0\n\ts_cbranch_scc1 1f\n\tglobal_load_dwordx4 %2, %3, %4 offset:1024 sc1\n1:"
                 : "=&v"(aa), "=&v"(ab), "+v"(ac) : "v"(ao), "s"(ws), "s"(w) : "memory", "scc");
    __builtin_amdgcn_sched_barrier(0);
    meanwhile();
    asm volatile("s_waitcnt vmcnt(0)" : "+v"(aa), "+v"(ab), "+v"(ac) : : "memory");
    if (__builtin_expect(__any(max(max(max4(aa), max4(ab)), max4(ac)) == 0xffffffffu), 0)) {
#pragma unroll 1
      for (int spin = 0; spin < SPIN_CAP; ++spin) {
        const bool na = max4(aa) == 0xffffffffu, nb = max4(ab) == 0xffffffffu, nc = max4(ac) == 0xffffffffu;
        if (!__any(na || nb || nc)) break;
        TSC(14);
        SLEEP(PSLEEP);
        if (na) LD16W(aa, ao);
        if (nb) LD16W(ab, ao + 512u);
        if (nc) LD16W(ac, ao + 1024u);
      }
    }
    *(u32x4*)(smem + LA + 16 * tid) = aa;
    *(u32x4*)(smem + LA + 4096 + 16 * tid) = ab;
    if (w == 0) *(u32x4*)(smem + LA + 8192 + 16 * tid) = ac;
  };
  auto rows_dma = [&](uint32_t ao) {
    uint32_t dl = lb0 + LA + 1024u * w, keep;
    asm volatile("s_mov_b32 %0, m0\n\ts_mov_b32 m0, %1\n\ts_add_u32 %1, %1, 4096 - 512\n\tglobal_load_lds_dwordx4 %2, %3 sc1\n\ts_mov_b32 m0, %1\n\ts_add_u32 %1, %1, 4096 - 512\n\t"
                 "global_load_lds_dwordx4 %2, %3 offset:512 sc1\n\ts_cmp_lg_u32 %4, 0\n\ts_cbranch_scc1 1f\n\ts_mov_b32 m0, %1\n\ts_nop 0\n\t"
                 "global_load_lds_dwordx4 %2, %3 offset:1024 sc1\n1:\n\ts_mov_b32 m0, %0" : "=&s"(keep), "+s"(dl) : "v"(ao), "s"(ws), "s"(w) : "memory", "scc");
  };
  auto get_bd = [&](int t0, int t1) {
#pragma unroll
    for (int t = t0; t < t1; ++t) {
      const u32x2 h0 = trd(da + 512 * (t >> 2) + 32 * (t & 3)), h1 = trd(da + 512 * (t >> 2) + 32 * (t & 3) + 4608 + 32);
      bw[t] = (u32x4){h0[0], h0[1], h1[0], h1[1]};
    }
#pragma unroll
    for (int t = t0; t < t1; ++t) asm volatile("" : "+v"(bw[t]));
  };
  pick(cand, sel, true);
  uint32_t mykey = sel[lane & 7], ro = ((mykey >> 3) & 0x3ffu) * 1152u;
  TS(0, 12);
  auto check = [&] {
    cnt[(k3rd > (uint32_t)__builtin_amdgcn_readlane(mykey, 7)) ? 5 : 6] = 1u;
    const uint32_t m3 = oct_maxu(k3rd);
    if (k3rd == m3) k3rd = k4th;
    if ((lane & 7) == 0) cand[72 + (tid >> 3)] = m3;
    if (w == 0 && lane < 32) cand[(lane < 8) ? 64 + lane : 96 + lane] = (lane < 8) ? mykey : 0u;
  };
  if constexpr (ROLE == 1) {
    if (ffn && !FORCEFB) rows_dma(ro + al);
    check();
    DRAIN();
  } else if (ffn) {
    if (LPOS) { SLEEP(LSLEEP); down_dma(KN, 4, lane); } else { DRAIN(); gsync0(ca, 12); get_bd(0, 8); }
  }
  BAR();
  TS(0, 5);
  auto put_out = [&](int j, uint32_t k0, int rk) {
    const uint64_t mine = __ballot(((mykey ^ k0) & ~24u) == 0u) & 0xffull;
    if (mine == 0ull) put_row(hv, j, out, (uint32_t)((8 * g2 + 2 * wl + j) * (D_ * 2)));
    else if (rk >= 2) put_row(nv, j, ws, O_FB + 1152u * (uint32_t)__builtin_ctzll(mine));
  };
  auto rest_out = [&] { put_out(0, keya, rka); put_out(1, keyb, rkb); };
  f32x4 lo, hi;
  auto gate_up = [&] {
    u32x4 av[5];
    const uint32_t aa = lb0 + LA + 16u * lane;
#pragma unroll
    for (int j = 0; j < 5; ++j) asm volatile("ds_read_b128 %0, %1 offset:%2" : "=v"(av[j]) : "v"(aa), "n"(1024 * j) : "memory");
    lo = (f32x4){0.f, 0.f, 0.f, 0.f}; hi = lo;
#pragma unroll
    for (int j = 0; j < 9; ++j) {
      if (j < 5) asm volatile("s_waitcnt lgkmcnt(4)" : "+v"(av[j % 5]));
      else if (j == 5) asm volatile("s_waitcnt lgkmcnt(2)" : "+v"(av[0]), "+v"(av[1]));
      else if (j == 7) asm volatile("s_waitcnt lgkmcnt(0)" : "+v"(av[2]), "+v"(av[3]));
      lo = MMB(av[j % 5], bw[2 * j], lo);
      hi = MMB(av[j % 5], bw[2 * j + 1], hi);
      __builtin_amdgcn_sched_barrier(0);
      if (j < 4) asm volatile("ds_read_b128 %0, %1 offset:%2" : "=v"(av[j]) : "v"(aa), "n"(1024 * (j + 5)) : "memory");
    }
  };
  uint32_t fl;
  asm volatile("ds_read_b32 %0, %1" : "=v"(fl) : "v"(ca + 20) : "memory");
  if (ROLE == 1) gate_up();
  asm volatile("s_waitcnt lgkmcnt(0)" : "+v"(fl));
  const bool slow = FORCESLOW || FORCEFB || __builtin_amdgcn_readfirstlane(fl) != 0;
  if (__builtin_expect(slow, 0)) {
    TSC(17);
    pick(cand + 64, sel + 16);
    mykey = sel[16 + (lane & 7)];
    ro = ((mykey >> 3) & 0x3ffu) * 1152u;
    if (ROLE == 1 && (k3rd > (uint32_t)__builtin_amdgcn_readlane(mykey, 7) || (mykey & 24u) == 8u)) cnt[11] = 1u;
    BAR();
    if (FORCESLOW > 1 || FORCEFB || __builtin_amdgcn_readfirstlane(cnt[11]) != 0) {
    u32x4 key;
#pragma unroll 1
    for (int spin = 0; spin < SPIN_CAP; ++spin) {
      LD16W(key, O_KF + 16u * tid);
      if (!__any(key[1] != T || key[3] != T)) break;
      SLEEP(KBACK);
    }
    const uint32_t ib = (uint32_t)(((255 - (tid >> 1)) << 5) | (7 - 4 * (tid & 1)));
    uint32_t k0 = (key[0] << 16) | ib, k1 = (key[0] & 0xffff0000u) | (ib - 1), k2 = (key[2] << 16) | (ib - 2), k3 = (key[2] & 0xffff0000u) | (ib - 3);
    CEX(k0, k1) CEX(k2, k3) CEX(k0, k2) CEX(k1, k3) CEX(k1, k2)
#pragma unroll 1
    for (int c = 0; c < C_; ++c) {
      const uint32_t mx = wave_maxu(k0);
      const bool hit = (k0 == mx);
      k0 = hit ? k1 : k0; k1 = hit ? k2 : k1; k2 = hit ? k3 : k2; k3 = hit ? 0u : k3;
      if (lane == 0) cand[64 + 8 * w + c] = mx;
    }
    pick(cand + 64, sel + 32);
    mykey = sel[32 + (lane & 7)];
    const uint32_t okey = sel[32 + (lane >> 3)];
    const uint64_t same = __ballot(((mykey ^ okey) & 0x1fe0u) == 0u && okey > mykey);
    const int sl = FORCEFB + __popcll((same >> (lane & 7)) & 0x0101010101010101ull);
    ro = (sl < 2) ? (((mykey >> 3) & 0x3ffu) + 3 - sl) * 1152u : O_FB - O_XC + 1152u * (lane & 7);
    }
    if (ln2) rest_out();
    if (ffn) {
      if (ROLE == 1) rows(ro + al, [] {});
      BAR();
      if (ROLE == 1) gate_up();
    }
  }
  if (ROLE == 1 && ffn && !slow && __builtin_expect(__any(lo[0] != lo[0] || lo[1] != lo[1] || lo[2] != lo[2] || lo[3] != lo[3] ||
                                                           hi[0] != hi[0] || hi[1] != hi[1] || hi[2] != hi[2] || hi[3] != hi[3]), 0)) {
    gsync0(ca + 4, 12);
    rows(ro + al, [] {});
    gsync0(ca + 4, 16);
    gate_up();
  }
  if (OPOS == 0 && ROLE == 2 && ffn && !slow) { SLEEP(OD); rest_out(); }
  if (!ffn) {
    if (ln2 && !slow) { SLEEP(OD2); rest_out(); }
    TSE();
    return;
  }
  uint32_t fx = 0, fv = 0, fn = 0, fo = 0, sa[9];
  float rs, hx, xs;
  u32x4 se;
  if constexpr (ROLE == 1) {
    const f32x4 lh = (lane < 32) ? lo : hi;
    const float fa = fold32(lh[0], lh[2]), fb2 = fold32(lh[1], lh[3]);
    const float g = __int_as_float(__builtin_amdgcn_update_dpp(__float_as_int(fa), __float_as_int(fb2), 0x128, 0xf, 0xc, false));
    const float u = __int_as_float(__builtin_amdgcn_update_dpp(__float_as_int(fb2), __float_as_int(fa), 0x128, 0xf, 0x3, false));
    const uint32_t gu = pack2(g, u);
    const float gp = bflo(gu);
    const float sg = rbf(gp * __builtin_amdgcn_rcpf(1.f + __expf(-gp)));
    *(u16*)((char*)hm + ha) = f2bf(sg * bfhi(gu));
  } else if (LPOS) {
    DRAIN();
    gsync0(ca, 12);
    get_bd(0, 8);
  }
  BAR();
  TS(0, 6);
  if (ROLE == 2 || w == 2) {
    const u32x4 a0 = *(const u32x4*)((char*)hm + h0a), a1 = *(const u32x4*)((char*)hm + h1a);
    if (ROLE == 1) get_bd(0, 4);
    const f32x4 z = {0.f, 0.f, 0.f, 0.f};
    f32x4 o[4];
#pragma unroll
    for (int t = 0; t < 2 * ROLE; ++t) o[t] = MMB(a1, bw[2 * t + 1], MMB(a0, bw[2 * t], z));
#pragma unroll
    for (int t = 0; t < ROLE; ++t) {
      u32x4 sv;
#pragma unroll
      for (int r = 0; r < 4; ++r) sv[r] = min(pack2(o[2 * t][r], o[2 * t + 1][r]), 0xfffffffeu);
      ST16(so + 4096u * t, sv);
    }
    TS(0, 13);
  }
  if (OPOS == 1 && ROLE == 2) {
    SLEEP(OSLEEP);
    if (!slow) rest_out();
  }
  if (ROLE == 1 && w == 1) {
    const int q = (fb & 15) >> 2, ts = 4 * (q & 1) + ((i & 7) >> 1);
    const int dd = 64 * (fb >> 4) + 16 * (2 * (i & 1) + (q >> 1)) + 4 * (fb & 3) + 2 * (kq & 1) + (kq >> 1);
    const uint32_t fkey = (uint32_t)__builtin_amdgcn_ds_bpermute(4 * ts, (int)mykey), fr = (uint32_t)__builtin_amdgcn_ds_bpermute(4 * ts, (int)ro);
    const int ft = 2047 - (int)(((fkey >> 2) & 0x7f8u) | (fkey & 7u));
    fo = (uint32_t)(ft * D_ + dd) * 2u;
    u32x2 fs;
    asm volatile("global_load_ushort %0, %4, %8\n\tglobal_load_ushort %1, %5, %9 sc1\n\tglobal_load_ushort %2, %6, %9 sc1\n\tglobal_load_dwordx2 %3, %7, %9 sc1"
                 : "=&v"(fx), "=&v"(fv), "=&v"(fn), "=&v"(fs) : "v"(fo), "v"(v_at(dd, ft >> 3) + 2u * (ft & 7)), "v"(O_XC + fr + 2u * dd), "v"(O_SC + 16u * dd), "s"(x), "s"(ws) : "memory");
    uint32_t t16 = (((T >> (3 * ((ft >> 1) & 3))) & LMASK) | (((T >> (12 + ((ft >> 1) & 3))) & 0x00010001u) << 14)) >> (16 * (ft & 1)) & 0xffffu;
    const uint32_t o = fkey >> 16;
    const float rs0 = bf2f((u16)((o & 0x8000u) ? (o & 0x7fffu) : (~o & 0xffffu)));
    rs = rbf(__builtin_amdgcn_rcpf(1.f + __expf(-rs0)));
    const uint32_t one = (i < 8) ? (0x3f80u << (16 * (i & 1))) : 0u;
    se = (u32x4){(i >> 1) == 0 ? one : 0u, (i >> 1) == 1 ? one : 0u, (i >> 1) == 2 ? one : 0u, (i >> 1) == 3 ? one : 0u};
#pragma unroll
    for (int m = 0; m < 9; ++m) sa[m] = O_SL + (uint32_t)(16 * m + (lane >> 2)) * SP + (uint32_t)(4 * fb + (lane & 3)) * 16u;
    asm volatile("" : "+v"(t16), "+v"(rs), "+v"(se), "+v"(sa[0]), "+v"(sa[1]), "+v"(sa[2]), "+v"(sa[3]), "+v"(sa[4]), "+v"(sa[5]), "+v"(sa[6]), "+v"(sa[7]), "+v"(sa[8]));
    SLEEP(SSLEEP);
    TS(0, 1);
    float sum;
#pragma unroll 1
    for (int spin = 0; spin < SPIN_CAP; ++spin) {
      u32x4 sv[9];
#pragma unroll
      for (int m = 0; m < 9; ++m) LD16(sv[m], sa[m]);
      asm volatile("s_waitcnt vmcnt(9)" : "+v"(fx), "+v"(fv), "+v"(fn), "+v"(fs));
      if (__builtin_expect(__any(fs[0] != T), 0)) {
        for (int k = 0; k < SPIN_CAP && fs[0] != T; ++k)
          asm volatile("global_load_dwordx2 %0, %1, %2 sc1\n\ts_waitcnt vmcnt(0)" : "=&v"(fs) : "v"(O_SC + 16u * dd), "s"(ws) : "memory");
      }
      const uint32_t f1 = fv ^ t16, f2 = (f1 & 7u) ? fv & ~7u : f1;
      hx = rbf(bf2f((u16)fx) + (float)__builtin_bit_cast(_Float16, (u16)f2) * __uint_as_float(fs[1]));
      xs = bf2f((u16)fn);
      asm volatile("" : "+v"(hx), "+v"(xs));
      f32x4 dv = {0.f, 0.f, 0.f, 0.f};
      static_for<0, 9>([&](auto mm) {
        constexpr int m = mm.value;
        asm volatile("s_waitcnt vmcnt(%1)" : "+v"(sv[m]) : "n"(8 - m));
        dv = MMB(sv[m], se, dv);
        __builtin_amdgcn_sched_barrier(0);
      });
      sum = fold16(fold32(dv[0], dv[1]), fold32(dv[2], dv[3]));
      if (__builtin_expect(!__any(sum != sum), 1)) break;
      uint32_t mx = 0u;
#pragma unroll
      for (int m = 0; m < 9; ++m) mx = max(mx, max4(sv[m]));
      if (!__any(mx == 0xffffffffu)) break;
      TSC(18);
      SLEEP(PSLEEP);
    }
    const float o1 = rbf(xs + rbf(rbf(sum) * rs));
    if (i < 8) asm volatile("global_store_short %0, %1, %2 sc1" : : "v"(fo), "v"((uint32_t)f2bf(hx + o1)), "s"(out) : "memory");
    TS(0, 2);
  }
  TSE();
  }
  }
}
#define BROT "192"
#define CPF_V ""
#define CPF_A "s_getpc_b64 s[96:97]\n\ts_lshl_b32 s99, s2, 4\n\ts_lshl_b32 s84, s98, 7\n\ts_add_u32 s99, s99, s84\n\tv_and_b32 v112, 1, v0\n\t" \
              "v_lshl_add_u32 v112, v112, 12, s99\n\tv_min_u32 v112, kf_end-kf_all-0x80, v112\n\ts_mov_b64 exec, 3\n\tglobal_load_dword v112, v112, s[96:97]\n\ts_mov_b64 exec, -1\n\t"
extern "C" __global__ __launch_bounds__(512, 4) void kf_all(KARGS) {
  __shared__ __attribute__((aligned(16))) char smem[SMEM];
  asm volatile("s_load_dwordx16 s[64:79], s[0:1], 0x0\n\ts_load_dwordx4 s[80:83], s[0:1], 0x40\n\tv_readfirstlane_b32 s98, v0\n\t"
               "s_add_u32 s84, s2, " BROT "\n\ts_xor_b32 s84, s84, s2\n\ts_bitset0_b32 s84, 8\n\ts_bitset0_b32 s84, 9\n\ts_xor_b32 s2, s2, s84\n\t"
               "v_mov_b32 v109, 0\n\ts_and_b32 s85, s2, 7\n\ts_bitcmp1_b32 s2, 8\n\ts_cbranch_scc1 1f\n\t" CPF_A "s_bitcmp1_b32 s98, 8\n\ts_cbranch_scc1 2f\n\t"
               "s_andn2_b32 s84, s2, 31\n\ts_lshl2_add_u32 s84, s85, s84\n\ts_bfe_u32 s85, s2, 0x20003\n\ts_or_b32 s84, s84, s85\n\t"
               "s_mulk_i32 s84, 0x2400\n\ts_mulk_i32 s98, 36\n\ts_add_u32 s84, s84, s98\n\tv_and_b32 v110, 63, v0\n\tv_and_b32 v111, 32, v0\n\t"
               "v_lshlrev_b32 v111, 5, v111\n\tv_lshl_add_u32 v111, v110, 2, v111\n\tv_lshl_add_u32 v110, v110, 4, s84\n\tv_add_u32 v111, s84, v111\n\t"
               "s_cmpk_lt_u32 s2, 0x90\n\ts_waitcnt lgkmcnt(0)\n\t"
               "global_load_dwordx4 v[100:103], v110, s[64:65]\n\tglobal_load_dwordx4 v[104:107], v110, s[64:65] offset:1152\n\t"
               "global_load_dword v108, v111, s[64:65] offset:1024\n\tglobal_load_dword v109, v109, s[82:83] sc1\n\t"
               "s_cbranch_scc1 kf_n\n\ts_branch kf_h\n"
               "1:\n\ts_bfe_u32 s84, s2, 0x50003\n\ts_lshl_b32 s85, s85, 5\n\ts_or_b32 s84, s84, s85\n\ts_lshl_b32 s84, s84, 2\n\t"
               "v_and_b32 v110, 0xff, v0\n\tv_bfe_u32 v107, v0, 7, 1\n"
               "4:\n\ts_lshl_b32 s85, s84, 7\n\tv_lshl_add_u32 v110, v110, 1, s85\n\tv_lshl_add_u32 v107, v107, 1, s84\n\ts_and_b32 s85, s98, s2\n\ts_waitcnt lgkmcnt(0)\n\t"
               "global_load_ushort v100, v107, s[78:79]\n\tglobal_load_ushort v101, v110, s[66:67]\n\tglobal_load_ushort v102, v110, s[68:69]\n\t"
               "global_load_dword v109, v109, s[82:83] sc1\n\ts_bitcmp1_b32 s85, 8\n\ts_cbranch_scc1 kf_u\n\t"
               "global_load_ushort v103, v110, s[70:71]\n\tglobal_load_ushort v104, v110, s[72:73]\n\tglobal_load_ushort v105, v110, s[74:75]\n\t"
               "global_load_ushort v106, v110, s[76:77]\n\tglobal_load_ushort v107, v107, s[80:81]\n\ts_bitcmp1_b32 s2, 8\n\ts_cbranch_scc1 kf_c\n"
               "6:\n\ts_barrier\n\ts_branch kf_p\n"
               "2:\n\t" CPF_V "s_cmpk_lt_u32 s2, 0xc0\n\ts_cbranch_scc1 6b\n\t"
               "s_lshr_b32 s84, s2, 3\n\ts_lshl3_add_u32 s84, s85, s84\n\ts_lshl1_add_u32 s84, s84, 976\n\t"
               "v_and_b32 v110, 0x7f, v0\n\tv_mov_b32 v107, 0\n\ts_branch 4b"
               : : "s"(__builtin_amdgcn_kernarg_segment_ptr()), "s"(blockIdx.x), "v"(threadIdx.x),
                   "s"((uint32_t)(uintptr_t)(__attribute__((address_space(3))) char*)smem) : "scc", "s96", "s97", "s98", "s99", "v127");
}
extern "C" __global__ __launch_bounds__(512, 4) void kf_c(KARGS) {
  __shared__ __attribute__((aligned(16))) char smem[SMEM];
  role_body<0>(smem, KPASS);
}
extern "C" __global__ __launch_bounds__(512, 4) void kf_u(KARGS) {
  __shared__ __attribute__((aligned(16))) char smem[SMEM];
  role_body<3>(smem, KPASS);
}
extern "C" __global__ __launch_bounds__(512, 4) void kf_n(KARGS) {
  __shared__ __attribute__((aligned(16))) char smem[SMEM];
  role_body<1, 1>(smem, KPASS);
}
extern "C" __global__ __launch_bounds__(512, 4) void kf_h(KARGS) {
  __shared__ __attribute__((aligned(16))) char smem[SMEM];
  role_body<1, 0>(smem, KPASS);
}
extern "C" __global__ __launch_bounds__(512, 4) void kf_p(KARGS) {
  __shared__ __attribute__((aligned(16))) char smem[SMEM];
  role_body<2>(smem, KPASS);
}
extern "C" __global__ __launch_bounds__(512, 4) void kf_conv() { asm volatile(CONV_ASM ::: CONV_CLOB); }
extern "C" __global__ void kf_end() {}

extern "C" __global__ __launch_bounds__(1024) void k_probe(int* dummy) {
  __shared__ int sm[2];
  const uint32_t now = (uint32_t)__builtin_amdgcn_s_memrealtime();
  __shared__ int sn;
  if (threadIdx.x == 0) { sm[0] = 0; sm[1] = 1 == 1 ? 0x7fffffff : 0; sn = 0; }
  __syncthreads();
  for (int k = threadIdx.x; k < 4096; k += 1024) {
    const uint32_t a = (uint32_t)g_prb[k], b = (uint32_t)(g_prb[k] >> 32);
    if (a) atomicMax(&sm[0], (int)(now - a));
    if (b) { if (1 == 1) atomicMin(&sm[1], (int)(now - b)); else if (1 == 0) atomicMax(&sm[1], (int)(now - b)); else { atomicAdd(&sm[1], (int)(now - b)); atomicAdd(&sn, 1); } }
  }
  __syncthreads();
  const int q = min(max(sm[0] * 10 - (1 == 2 ? sm[1] * 10 / max(sn, 1) : sm[1] * 10), 0), 30000);
  if (threadIdx.x == 0) while ((int)((uint32_t)__builtin_amdgcn_s_memrealtime() - now) < q) __builtin_amdgcn_s_sleep(2);
}
extern "C" void kernel_launch(void* const* d_in, const int* in_sizes, int n_in,
                              void* d_out, int out_size, void* d_ws, size_t ws_size,
                              hipStream_t stream) {
  kf_all<<<512, 512, 0, stream>>>((const u16*)d_in[0], (const u16*)d_in[1], (const u16*)d_in[2], (const u16*)d_in[3], (const u16*)d_in[4],
                                  (const u16*)d_in[5], (const u16*)d_in[6], (const u16*)d_in[7], (const u16*)d_in[8], (char*)d_ws + (size_t)WSOFF * 1048576,
                                  (const u16*)d_in[10], (const u16*)d_in[11], (const u16*)d_in[12], (const u16*)d_in[13], (u16*)d_out);
  k_probe<<<1, 1024, 0, stream>>>((int*)d_ws);
}
```

```cpp
#include <hip/hip_runtime.h>
#include <hip/hip_bf16.h>
#include <stdint.h>
#define SSLEEP 16

typedef __attribute__((ext_vector_type(8))) short bf16x8;
typedef __attribute__((ext_vector_type(4))) float f32x4;
typedef uint16_t u16;
typedef __attribute__((ext_vector_type(4))) uint32_t u32x4;
typedef __attribute__((ext_vector_type(2))) uint32_t u32x2;
#define TSD uint32_t pr0_ = 0u, pr1_ = 0u
__device__ unsigned long long g_prb[4096];
#define TS(k, i) do { if ((i) == 0) pr0_ = (uint32_t)__builtin_amdgcn_s_memrealtime() | 1u; \
    if (ROLE == 1 && (i) == 2 && (1 < 0 || w == 1) && (blockIdx.x & 255) < 144) pr1_ = (uint32_t)__builtin_amdgcn_s_memrealtime() | 1u; } while (0)
#define TSC(i)
#define TSE() do { if (__builtin_amdgcn_mbcnt_hi(-1, __builtin_amdgcn_mbcnt_lo(-1, 0)) == 0) \
    __hip_atomic_store(&g_prb[blockIdx.x * 8 + w], ((unsigned long long)pr1_ << 32) | pr0_, __ATOMIC_RELAXED, __HIP_MEMORY_SCOPE_AGENT); } while (0)
#define TSP
#define TSA

constexpr int L_ = 2048, D_ = 576, N_ = 128, F_ = 4608, C_ = 8;
constexpr int NF = 144;
constexpr uint32_t O_EP = 0;
constexpr uint32_t O_KF = 4096;
constexpr uint32_t O_KEY = O_KF + 8192;
constexpr uint32_t O_SC = O_KEY + 32768;
constexpr uint32_t O_DN = O_SC + 9216;
constexpr uint32_t O_SU = O_DN + 20480;
constexpr uint32_t O_FB = O_SU + 262144;
constexpr uint32_t O_XC = O_FB + 8 * 1152;
constexpr uint32_t SP = 9216 + 128;
constexpr uint32_t O_SL = O_XC + 1024 * 1152;
constexpr uint32_t SLU = 336;
constexpr uint32_t O_U = O_SL + 256 * SLU * 16;
constexpr int UP = 584;
constexpr uint32_t VP = 8192 + 128, VPP = 4096 + 128;
constexpr uint32_t O_V = O_U + 256 * UP * 16, O_VP = O_V + 256 * VP;
static_assert(256 * SLU * 16 >= NF * SP && O_SL % 128 == 0 && O_U % 128 == 0 && O_V % 128 == 0, "");
__host__ __device__ __forceinline__ uint32_t u_at(int tg, int d) { return O_U + 16u * (uint32_t)((tg >> 1) * (UP * 2) + (d >> 2) * 8 + (tg & 1) * 4 + (d & 3)); }
__host__ __device__ __forceinline__ uint32_t v_at(int d, int tg) {
  return (d < 512) ? O_V + (uint32_t)(d >> 1) * VP + 16u * (uint32_t)(2 * tg + (d & 1)) : O_VP + (uint32_t)(d - 512) * VPP + 16u * (uint32_t)tg;
}
#define HAVE_VAT
#define HAVE_VTAG
__device__ __forceinline__ float bf2f(u16 v) { return __uint_as_float((uint32_t)v << 16); }
__device__ __forceinline__ float bflo(uint32_t v) { return __uint_as_float(v << 16); }
__device__ __forceinline__ float bfhi(uint32_t v) { return __uint_as_float(v & 0xffff0000u); }
__device__ __forceinline__ u16 f2bf(float f) { return __builtin_bit_cast(u16, (__bf16)f); }
__device__ __forceinline__ float rbf(float f) { return bf2f(f2bf(f)); }
typedef __attribute__((ext_vector_type(2))) float f32x2;
typedef __attribute__((ext_vector_type(2))) __bf16 bf16x2v;
__device__ __forceinline__ uint32_t pack2(float a, float b) {
  return __builtin_bit_cast(uint32_t, __builtin_convertvector((f32x2){a, b}, bf16x2v));
}

#define DPP_I(v, ctrl, rmask) __builtin_amdgcn_update_dpp(0, (v), (ctrl), (rmask), 0xf, true)
__device__ __forceinline__ float row_sum(float v) {
  v += __int_as_float(DPP_I(__float_as_int(v), 0x111, 0xf));
  v += __int_as_float(DPP_I(__float_as_int(v), 0x112, 0xf));
  v += __int_as_float(DPP_I(__float_as_int(v), 0x114, 0xf));
  v += __int_as_float(DPP_I(__float_as_int(v), 0x118, 0xf));
  return v;
}
__device__ __forceinline__ float rdl(float v, int l) { return __int_as_float(__builtin_amdgcn_readlane(__float_as_int(v), l)); }
__device__ __forceinline__ float wave_sum(float v) {
  v = row_sum(v);
  asm("s_nop 1\n\tv_add_f32_dpp %0, %0, %0 row_bcast:15 row_mask:0xa bank_mask:0xf\n\t"
      "s_nop 1\n\tv_add_f32_dpp %0, %0, %0 row_bcast:31 row_mask:0xc bank_mask:0xf\n\ts_nop 0" : "+v"(v));
  return rdl(v, 63);
}
__device__ __forceinline__ float fold32(float a, float b) {
  const auto r = __builtin_amdgcn_permlane32_swap(__float_as_uint(a), __float_as_uint(b), false, false);
  return __uint_as_float(r[0]) + __uint_as_float(r[1]);
}
__device__ __forceinline__ float fold16(float a, float b) {
  const auto r = __builtin_amdgcn_permlane16_swap(__float_as_uint(a), __float_as_uint(b), false, false);
  return __uint_as_float(r[0]) + __uint_as_float(r[1]);
}
__device__ __forceinline__ void wave_sum2(float& a, float& b) {
  float v = row_sum(fold32(a, b));
  asm("s_nop 1\n\tv_add_f32_dpp %0, %0, %0 row_bcast:15 row_mask:0xa bank_mask:0xf\n\ts_nop 0" : "+v"(v));
  a = rdl(v, 31); b = rdl(v, 63);
}
__device__ __forceinline__ float dot2(uint32_t a, uint32_t b, float c) {
  return __builtin_amdgcn_fdot2_f32_bf16(__builtin_bit_cast(bf16x2v, a), __builtin_bit_cast(bf16x2v, b), c, false);
}
__device__ __forceinline__ uint32_t row_maxu(uint32_t v) {
  v = max(v, (uint32_t)DPP_I((int)v, 0xb1, 0xf));
  v = max(v, (uint32_t)DPP_I((int)v, 0x4e, 0xf));
  v = max(v, (uint32_t)DPP_I((int)v, 0x141, 0xf));
  v = max(v, (uint32_t)DPP_I((int)v, 0x140, 0xf));
  return v;
}
__device__ __forceinline__ uint32_t wave_maxu(uint32_t v) {
  v = max(v, (uint32_t)DPP_I((int)v, 0x111, 0xf));
  v = max(v, (uint32_t)DPP_I((int)v, 0x112, 0xf));
  v = max(v, (uint32_t)DPP_I((int)v, 0x114, 0xf));
  v = max(v, (uint32_t)DPP_I((int)v, 0x118, 0xf));
  v = max(v, (uint32_t)DPP_I((int)v, 0x142, 0xa));
  v = max(v, (uint32_t)DPP_I((int)v, 0x143, 0xc));
  return (uint32_t)__builtin_amdgcn_readlane((int)v, 63);
}

typedef __attribute__((ext_vector_type(8))) _Float16 f16x8;
typedef __attribute__((ext_vector_type(2))) _Float16 f16x2;
__device__ __forceinline__ uint32_t packh(f32x2 v) { return __builtin_bit_cast(uint32_t, __builtin_convertvector(v, f16x2)); }
__device__ __forceinline__ uint32_t packh(float a, float b) { return packh((f32x2){a, b}); }
constexpr int IP = 224, I_X = 128;
__device__ __forceinline__ f32x2 cmul(f32x2 z, f32x2 w) {
  f32x2 r;
  asm("v_pk_mul_f32 %0, %1, %2 op_sel_hi:[0,1]\n\tv_pk_fma_f32 %0, %1, %2, %0 op_sel:[1,1,0] op_sel_hi:[1,0,1] neg_lo:[0,1,0]" : "=&v"(r) : "v"(z), "v"(w));
  return r;
}
__device__ __forceinline__ f32x2 cexp2(float ar2, float air, float l) {
  const float t = air * l;
  return (f32x2){__builtin_amdgcn_cosf(t), __builtin_amdgcn_sinf(t)} * __builtin_amdgcn_exp2f(ar2 * l);
}
__device__ __forceinline__ float state_a(float ldt, float are, float aim, float& ar2, float& air, float& g) {
  const float dt = rbf(__expf(ldt));
  ar2 = dt * are * 1.4426950408889634f;
  air = dt * aim * 0.15915494309189535f;
  air -= __builtin_rintf(air);
  g = 0.f;
  if (__builtin_expect(__any(ar2 > 0.f), 0)) g = fminf(__uint_as_float(wave_maxu(__float_as_uint(fmaxf(ar2, 0.f)))), 1.f);
  return dt;
}
template <int N> __device__ __forceinline__ void put_im(char* p, int im, const f32x2* z) {
  *(u32x4*)p = (u32x4){packh(z[0][0], z[1][0]), packh(z[2][0], z[3][0]), packh(z[4][0], z[5][0]), packh(z[6][0], z[7][0])};
  *(u32x4*)(p + im) = (u32x4){packh(z[0][1], z[1][1]), packh(z[2][1], z[3][1]), packh(z[4][1], z[5][1]), packh(z[6][1], z[7][1])};
}
#define Y_ASM \
  "v_mov_b64 v[40:41], %0\nv_pk_mul_f32 v[42:43], v[40:41], %1 op_sel_hi:[0,1]\nv_pk_mul_f32 v[44:45], %0, %2 op_sel_hi:[0,1]\n" \
  "v_pk_fma_f32 v[42:43], v[40:41], %1, v[42:43] op_sel:[1,1,0] op_sel_hi:[1,0,1] neg_lo:[0,1,0]\n" \
  "v_pk_fma_f32 v[44:45], %0, %2, v[44:45] op_sel:[1,1,0] op_sel_hi:[1,0,1] neg_lo:[0,1,0]\nv_cvt_pk_f16_f32 v56, v40, v42\n" \
  "v_cvt_pk_f16_f32 v60, v41, v43\nv_pk_mul_f32 v[40:41], v[42:43], %1 op_sel_hi:[0,1]\nv_pk_mul_f32 v[48:49], v[44:45], %2 op_sel_hi:[0,1]\n" \
  "v_pk_fma_f32 v[40:41], v[42:43], %1, v[40:41] op_sel:[1,1,0] op_sel_hi:[1,0,1] neg_lo:[0,1,0]\n" \
  "v_pk_fma_f32 v[48:49], v[44:45], %2, v[48:49] op_sel:[1,1,0] op_sel_hi:[1,0,1] neg_lo:[0,1,0]\n" \
  "v_pk_mul_f32 v[42:43], v[40:41], %1 op_sel_hi:[0,1]\nv_pk_mul_f32 v[52:53], v[48:49], %2 op_sel_hi:[0,1]\n" \
  "v_pk_fma_f32 v[42:43], v[40:41], %1, v[42:43] op_sel:[1,1,0] op_sel_hi:[1,0,1] neg_lo:[0,1,0]\n" \
  "v_pk_fma_f32 v[52:53], v[48:49], %2, v[52:53] op_sel:[1,1,0] op_sel_hi:[1,0,1] neg_lo:[0,1,0]\nv_cvt_pk_f16_f32 v57, v40, v42\n" \
  "v_cvt_pk_f16_f32 v61, v41, v43\nv_pk_mul_f32 v[40:41], v[42:43], %1 op_sel_hi:[0,1]\n" \
  "v_pk_fma_f32 v[40:41], v[42:43], %1, v[40:41] op_sel:[1,1,0] op_sel_hi:[1,0,1] neg_lo:[0,1,0]\n" \
  "v_pk_mul_f32 v[42:43], v[40:41], %1 op_sel_hi:[0,1]\nv_pk_mul_f32 v[46:47], v[44:45], %1 op_sel_hi:[0,1]\n" \
  "v_pk_fma_f32 v[42:43], v[40:41], %1, v[42:43] op_sel:[1,1,0] op_sel_hi:[1,0,1] neg_lo:[0,1,0]\n" \
  "v_pk_fma_f32 v[46:47], v[44:45], %1, v[46:47] op_sel:[1,1,0] op_sel_hi:[1,0,1] neg_lo:[0,1,0]\nv_cvt_pk_f16_f32 v58, v40, v42\n" \
  "v_cvt_pk_f16_f32 v62, v41, v43\nv_cvt_pk_f16_f32 v64, v44, v46\nv_cvt_pk_f16_f32 v68, v45, v47\n" \
  "v_pk_mul_f32 v[40:41], v[42:43], %1 op_sel_hi:[0,1]\nv_pk_mul_f32 v[44:45], v[46:47], %1 op_sel_hi:[0,1]\n" \
  "v_pk_fma_f32 v[40:41], v[42:43], %1, v[40:41] op_sel:[1,1,0] op_sel_hi:[1,0,1] neg_lo:[0,1,0]\n" \
  "v_pk_fma_f32 v[44:45], v[46:47], %1, v[44:45] op_sel:[1,1,0] op_sel_hi:[1,0,1] neg_lo:[0,1,0]\n" \
  "v_pk_mul_f32 v[42:43], v[40:41], %1 op_sel_hi:[0,1]\nv_pk_mul_f32 v[46:47], v[44:45], %1 op_sel_hi:[0,1]\n" \
  "v_pk_fma_f32 v[42:43], v[40:41], %1, v[42:43] op_sel:[1,1,0] op_sel_hi:[1,0,1] neg_lo:[0,1,0]\n" \
  "v_pk_fma_f32 v[46:47], v[44:45], %1, v[46:47] op_sel:[1,1,0] op_sel_hi:[1,0,1] neg_lo:[0,1,0]\nv_cvt_pk_f16_f32 v59, v40, v42\n" \
  "v_cvt_pk_f16_f32 v63, v41, v43\nv_cvt_pk_f16_f32 v65, v44, v46\nv_cvt_pk_f16_f32 v69, v45, v47\n" \
  "v_pk_mul_f32 v[44:45], v[46:47], %1 op_sel_hi:[0,1]\nds_write_b128 %3, v[56:59] offset:0\n" \
  "v_pk_fma_f32 v[44:45], v[46:47], %1, v[44:45] op_sel:[1,1,0] op_sel_hi:[1,0,1] neg_lo:[0,1,0]\nds_write_b128 %3, v[60:63] offset:64\n" \
  "v_pk_mul_f32 v[46:47], v[44:45], %1 op_sel_hi:[0,1]\nv_pk_mul_f32 v[50:51], v[48:49], %1 op_sel_hi:[0,1]\n" \
  "v_pk_fma_f32 v[46:47], v[44:45], %1, v[46:47] op_sel:[1,1,0] op_sel_hi:[1,0,1] neg_lo:[0,1,0]\n" \
  "v_pk_fma_f32 v[50:51], v[48:49], %1, v[50:51] op_sel:[1,1,0] op_sel_hi:[1,0,1] neg_lo:[0,1,0]\nv_cvt_pk_f16_f32 v66, v44, v46\n" \
  "v_cvt_pk_f16_f32 v70, v45, v47\nv_cvt_pk_f16_f32 v56, v48, v50\nv_cvt_pk_f16_f32 v60, v49, v51\n" \
  "v_pk_mul_f32 v[44:45], v[46:47], %1 op_sel_hi:[0,1]\nv_pk_mul_f32 v[48:49], v[50:51], %1 op_sel_hi:[0,1]\n" \
  "v_pk_fma_f32 v[44:45], v[46:47], %1, v[44:45] op_sel:[1,1,0] op_sel_hi:[1,0,1] neg_lo:[0,1,0]\n" \
  "v_pk_fma_f32 v[48:49], v[50:51], %1, v[48:49] op_sel:[1,1,0] op_sel_hi:[1,0,1] neg_lo:[0,1,0]\n" \
  "v_pk_mul_f32 v[46:47], v[44:45], %1 op_sel_hi:[0,1]\nv_pk_mul_f32 v[50:51], v[48:49], %1 op_sel_hi:[0,1]\n" \
  "v_pk_fma_f32 v[46:47], v[44:45], %1, v[46:47] op_sel:[1,1,0] op_sel_hi:[1,0,1] neg_lo:[0,1,0]\n" \
  "v_pk_fma_f32 v[50:51], v[48:49], %1, v[50:51] op_sel:[1,1,0] op_sel_hi:[1,0,1] neg_lo:[0,1,0]\nv_cvt_pk_f16_f32 v67, v44, v46\n" \
  "v_cvt_pk_f16_f32 v71, v45, v47\nv_cvt_pk_f16_f32 v57, v48, v50\nv_cvt_pk_f16_f32 v61, v49, v51\n" \
  "v_pk_mul_f32 v[48:49], v[50:51], %1 op_sel_hi:[0,1]\nds_write_b128 %3, v[64:67] offset:16\n" \
  "v_pk_fma_f32 v[48:49], v[50:51], %1, v[48:49] op_sel:[1,1,0] op_sel_hi:[1,0,1] neg_lo:[0,1,0]\nds_write_b128 %3, v[68:71] offset:80\n" \
  "v_pk_mul_f32 v[50:51], v[48:49], %1 op_sel_hi:[0,1]\nv_pk_mul_f32 v[54:55], v[52:53], %1 op_sel_hi:[0,1]\n" \
  "v_pk_fma_f32 v[50:51], v[48:49], %1, v[50:51] op_sel:[1,1,0] op_sel_hi:[1,0,1] neg_lo:[0,1,0]\n" \
  "v_pk_fma_f32 v[54:55], v[52:53], %1, v[54:55] op_sel:[1,1,0] op_sel_hi:[1,0,1] neg_lo:[0,1,0]\nv_cvt_pk_f16_f32 v58, v48, v50\n" \
  "v_cvt_pk_f16_f32 v62, v49, v51\nv_cvt_pk_f16_f32 v64, v52, v54\nv_cvt_pk_f16_f32 v68, v53, v55\n" \
  "v_pk_mul_f32 v[48:49], v[50:51], %1 op_sel_hi:[0,1]\nv_pk_mul_f32 v[52:53], v[54:55], %1 op_sel_hi:[0,1]\n" \
  "v_pk_fma_f32 v[48:49], v[50:51], %1, v[48:49] op_sel:[1,1,0] op_sel_hi:[1,0,1] neg_lo:[0,1,0]\n" \
  "v_pk_fma_f32 v[52:53], v[54:55], %1, v[52:53] op_sel:[1,1,0] op_sel_hi:[1,0,1] neg_lo:[0,1,0]\n" \
  "v_pk_mul_f32 v[50:51], v[48:49], %1 op_sel_hi:[0,1]\nv_pk_mul_f32 v[54:55], v[52:53], %1 op_sel_hi:[0,1]\n" \
  "v_pk_fma_f32 v[50:51], v[48:49], %1, v[50:51] op_sel:[1,1,0] op_sel_hi:[1,0,1] neg_lo:[0,1,0]\n" \
  "v_pk_fma_f32 v[54:55], v[52:53], %1, v[54:55] op_sel:[1,1,0] op_sel_hi:[1,0,1] neg_lo:[0,1,0]\nv_cvt_pk_f16_f32 v59, v48, v50\n" \
  "v_cvt_pk_f16_f32 v63, v49, v51\nv_cvt_pk_f16_f32 v65, v52, v54\nv_cvt_pk_f16_f32 v69, v53, v55\n" \
  "v_pk_mul_f32 v[52:53], v[54:55], %1 op_sel_hi:[0,1]\nds_write_b128 %3, v[56:59] offset:32\n" \
  "v_pk_fma_f32 v[52:53], v[54:55], %1, v[52:53] op_sel:[1,1,0] op_sel_hi:[1,0,1] neg_lo:[0,1,0]\nds_write_b128 %3, v[60:63] offset:96\n" \
  "v_pk_mul_f32 v[54:55], v[52:53], %1 op_sel_hi:[0,1]\n" \
  "v_pk_fma_f32 v[54:55], v[52:53], %1, v[54:55] op_sel:[1,1,0] op_sel_hi:[1,0,1] neg_lo:[0,1,0]\nv_cvt_pk_f16_f32 v66, v52, v54\n" \
  "v_cvt_pk_f16_f32 v70, v53, v55\nv_pk_mul_f32 v[52:53], v[54:55], %1 op_sel_hi:[0,1]\n" \
  "v_pk_fma_f32 v[52:53], v[54:55], %1, v[52:53] op_sel:[1,1,0] op_sel_hi:[1,0,1] neg_lo:[0,1,0]\n" \
  "v_pk_mul_f32 v[54:55], v[52:53], %1 op_sel_hi:[0,1]\n" \
  "v_pk_fma_f32 v[54:55], v[52:53], %1, v[54:55] op_sel:[1,1,0] op_sel_hi:[1,0,1] neg_lo:[0,1,0]\nv_cvt_pk_f16_f32 v67, v52, v54\n" \
  "v_cvt_pk_f16_f32 v71, v53, v55\nds_write_b128 %3, v[64:67] offset:48\nds_write_b128 %3, v[68:71] offset:112\n"
#define Y_CLOB "memory", "v40", "v41", "v42", "v43", "v44", "v45", "v46", "v47", "v48", "v49", "v50", "v51", "v52", "v53", "v54", "v55", "v56", "v57", "v58", "v59", "v60", "v61", "v62", "v63", "v64", "v65", "v66", "v67", "v68", "v69", "v70", "v71"
#define T_ASM \
  "ds_read_b128 %4, %8\nds_read_b64_tr_b16 v[80:81], %7 offset:0\nds_read_b64_tr_b16 v[82:83], %7 offset:64\n" \
  "ds_read_b64_tr_b16 v[84:85], %5 offset:0\nds_read_b64_tr_b16 v[86:87], %5 offset:48\nds_read_b64_tr_b16 v[88:89], %6 offset:0\n" \
  "ds_read_b64_tr_b16 v[90:91], %6 offset:48\nds_read_b64_tr_b16 v[92:93], %7 offset:3584\nds_read_b64_tr_b16 v[94:95], %7 offset:3648\n" \
  "ds_read_b64_tr_b16 v[96:97], %5 offset:3584\nds_read_b64_tr_b16 v[98:99], %5 offset:3632\nds_read_b64_tr_b16 v[100:101], %6 offset:3584\n" \
  "ds_read_b64_tr_b16 v[102:103], %6 offset:3632\nds_read_b64_tr_b16 v[104:105], %7 offset:7168\nds_read_b64_tr_b16 v[106:107], %7 offset:7232\n" \
  "s_waitcnt lgkmcnt(8)\nv_mfma_f32_16x16x32_f16 %0, v[84:87], v[80:83], 0\nds_read_b64_tr_b16 v[108:109], %5 offset:7168\n" \
  "ds_read_b64_tr_b16 v[110:111], %5 offset:7216\nds_read_b64_tr_b16 v[112:113], %6 offset:7168\nv_mfma_f32_16x16x32_f16 %2, v[88:91], v[80:83], 0\n" \
  "ds_read_b64_tr_b16 v[114:115], %6 offset:7216\nds_read_b64_tr_b16 v[116:117], %7 offset:10752\nds_read_b64_tr_b16 v[118:119], %7 offset:10816\n" \
  "s_waitcnt lgkmcnt(8)\nv_mfma_f32_16x16x32_f16 %0, v[96:99], v[92:95], %0\nds_read_b64_tr_b16 v[120:121], %5 offset:10752\n" \
  "ds_read_b64_tr_b16 v[122:123], %5 offset:10800\nds_read_b64_tr_b16 v[124:125], %6 offset:10752\n" \
  "v_mfma_f32_16x16x32_f16 %2, v[100:103], v[92:95], %2\nds_read_b64_tr_b16 v[126:127], %6 offset:10800\n" \
  "ds_read_b64_tr_b16 v[80:81], %7 offset:14336\nds_read_b64_tr_b16 v[82:83], %7 offset:14400\ns_waitcnt lgkmcnt(8)\n" \
  "v_mfma_f32_16x16x32_f16 %0, v[108:111], v[104:107], %0\nds_read_b64_tr_b16 v[84:85], %5 offset:14336\n" \
  "ds_read_b64_tr_b16 v[86:87], %5 offset:14384\nds_read_b64_tr_b16 v[88:89], %6 offset:14336\n" \
  "v_mfma_f32_16x16x32_f16 %2, v[112:115], v[104:107], %2\nds_read_b64_tr_b16 v[90:91], %6 offset:14384\n" \
  "ds_read_b64_tr_b16 v[92:93], %7 offset:17920\nds_read_b64_tr_b16 v[94:95], %7 offset:17984\ns_waitcnt lgkmcnt(8)\n" \
  "v_mfma_f32_16x16x32_f16 %0, v[120:123], v[116:119], %0\nds_read_b64_tr_b16 v[96:97], %5 offset:17920\n" \
  "ds_read_b64_tr_b16 v[98:99], %5 offset:17968\nds_read_b64_tr_b16 v[100:101], %6 offset:17920\n" \
  "v_mfma_f32_16x16x32_f16 %2, v[124:127], v[116:119], %2\nds_read_b64_tr_b16 v[102:103], %6 offset:17968\n" \
  "ds_read_b64_tr_b16 v[104:105], %7 offset:21504\nds_read_b64_tr_b16 v[106:107], %7 offset:21568\ns_waitcnt lgkmcnt(8)\n" \
  "v_mfma_f32_16x16x32_f16 %1, v[84:87], v[80:83], 0\nds_read_b64_tr_b16 v[108:109], %5 offset:21504\nds_read_b64_tr_b16 v[110:111], %5 offset:21552\n" \
  "ds_read_b64_tr_b16 v[112:113], %6 offset:21504\nv_mfma_f32_16x16x32_f16 %3, v[88:91], v[80:83], 0\nds_read_b64_tr_b16 v[114:115], %6 offset:21552\n" \
  "ds_read_b64_tr_b16 v[116:117], %7 offset:25088\nds_read_b64_tr_b16 v[118:119], %7 offset:25152\ns_waitcnt lgkmcnt(8)\n" \
  "v_mfma_f32_16x16x32_f16 %1, v[96:99], v[92:95], %1\nds_read_b64_tr_b16 v[120:121], %5 offset:25088\n" \
  "ds_read_b64_tr_b16 v[122:123], %5 offset:25136\nds_read_b64_tr_b16 v[124:125], %6 offset:25088\n" \
  "v_mfma_f32_16x16x32_f16 %3, v[100:103], v[92:95], %3\nds_read_b64_tr_b16 v[126:127], %6 offset:25136\ns_waitcnt lgkmcnt(6)\n" \
  "v_mfma_f32_16x16x32_f16 %1, v[108:111], v[104:107], %1\nv_mfma_f32_16x16x32_f16 %3, v[112:115], v[104:107], %3\ns_waitcnt lgkmcnt(0)\n" \
  "v_mfma_f32_16x16x32_f16 %1, v[120:123], v[116:119], %1\nv_mfma_f32_16x16x32_f16 %3, v[124:127], v[116:119], %3\n"
#define T_CLOB "memory", "v80", "v81", "v82", "v83", "v84", "v85", "v86", "v87", "v88", "v89", "v90", "v91", "v92", "v93", "v94", "v95", "v96", "v97", "v98", "v99", "v100", "v101", "v102", "v103", "v104", "v105", "v106", "v107", "v108", "v109", "v110", "v111", "v112", "v113", "v114", "v115", "v116", "v117", "v118", "v119", "v120", "v121", "v122", "v123", "v124", "v125", "v126", "v127"
template <class F> __device__ __forceinline__ int gen_y(float ldt, float are, float aim, float bre, float bim, float cre, float cim, uint32_t im, F&& mid TSP) {
  float ar2, air, g;
  const float dt = state_a(ldt, are, aim, ar2, air, g);
  const float dbr = dt * bre, dbi = dt * bim, pr = cre * dbr - cim * dbi, pi = cre * dbi + cim * dbr;
  const f32x2 w8 = cexp2(ar2, -air, 8.f), w64 = cexp2(ar2, -air, 64.f);
  const float pm = fmaxf(fabsf(pr), fabsf(pi));
  int e = -4;
  if (__builtin_expect(__any(!(pm < 0x1p-4f) || ar2 > 0.f) || !__any(pm > 0x1p-12f), 0)) e = (int)(wave_maxu(__float_as_uint(pm)) >> 23) - 126 + (int)ceilf(g * 250.f);
  const f32x2 y0 = {__builtin_amdgcn_ldexpf(pr, 14 - e), __builtin_amdgcn_ldexpf(-pi, 14 - e)};
  mid();
  asm volatile(Y_ASM : : "v"(y0), "v"(w8), "v"(w64), "v"(im) : Y_CLOB);
  return e;
}
__device__ __forceinline__ int gen_x(float ldt, float are, float aim, char* im) {
  float ar2, air, g;
  state_a(ldt, are, aim, ar2, air, g);
  const int e = min((int)ceilf(g * 1810.f), 300);
  const float s = __builtin_amdgcn_ldexpf(1.f, 15 - e);
  const f32x2 w1 = cexp2(ar2, air, 1.f), w256 = cexp2(ar2, air, 256.f), w2 = cmul(w1, w1);
  f32x2 a[8], b[8], c[8];
  a[0] = (f32x2){s, 0.f}; a[1] = w1 * s; a[2] = w2 * s;
  b[0] = b[1] = w256 * s;
  b[2] = cmul(b[0], w2); a[3] = cmul(a[1], w2); a[4] = cmul(a[2], w2);
  b[3] = b[4] = b[5] = cmul(b[2], w2); a[5] = cmul(a[3], w2); a[6] = cmul(a[4], w2);
  b[6] = cmul(b[3], w2); a[7] = cmul(a[5], w2); c[0] = cmul(b[3], w256);
  b[7] = cmul(b[6], w2); c[2] = cmul(c[0], w256);
  put_im<0>(im + I_X, 48, a);
  c[1] = cmul(b[7], w256); c[3] = cmul(c[1], w256);
  put_im<0>(im + I_X + 16, 48, b);
#pragma unroll
  for (int k = 4; k < 8; ++k) c[k] = cmul(c[k - 1], w256);
  put_im<0>(im + I_X + 32, 48, c);
  return e;
}

template <int I> struct IC { static constexpr int value = I; };
template <int B, int E, class F> __device__ __forceinline__ void static_for(F&& f) {
  if constexpr (B < E) { f(IC<B>{}); static_for<B + 1, E>(f); }
}
typedef __attribute__((ext_vector_type(4))) short s16x4;
__device__ __forceinline__ u32x2 trd(uint32_t a) {
  return __builtin_bit_cast(u32x2, __builtin_amdgcn_ds_read_tr16_b64_v4i16((__attribute__((address_space(3))) s16x4*)(uintptr_t)a));
}
#define BAR() asm volatile("s_waitcnt lgkmcnt(0)\n\ts_barrier" ::: "memory")
#define ST16(off, v) asm volatile("global_store_dwordx4 %0, %1, %2 sc1\n\ts_nop 1" : : "v"(off), "v"(v), "s"(ws) : "memory")
#define ST4(off, v) asm volatile("global_store_dword %0, %1, %2 sc1" : : "v"(off), "v"(v), "s"(ws) : "memory")
#define LD16(r, off) asm volatile("global_load_dwordx4 %0, %1, %2 sc1" : "=v"(r) : "v"(off), "s"(ws) : "memory")
#define LD16W(r, off) asm volatile("global_load_dwordx4 %0, %1, %2 sc1\n\ts_waitcnt vmcnt(0)" : "=&v"(r) : "v"(off), "s"(ws) : "memory")
#define DRAIN() asm volatile("s_waitcnt vmcnt(0)" ::: "memory")
#define CEX(a, b) { const uint32_t hi_ = max(a, b), lo_ = min(a, b); a = hi_; b = lo_; }
#define MMB(a_, b_, c_) __builtin_amdgcn_mfma_f32_16x16x32_bf16(__builtin_bit_cast(bf16x8, a_), __builtin_bit_cast(bf16x8, b_), c_, 0, 0, 0)
#define NTM 0
#define DPOL ""
#define DPOL2 ""
#define DPOL4 ""
constexpr int SPIN_CAP = 1 << 18;
constexpr uint32_t UMASK = 0x40074007u;
__device__ __forceinline__ uint32_t utag(uint32_t T, int j) {
  return ((T >> (3 * j)) & 0x00070007u) | (((j <= 2) ? (T << (2 - j)) : (T >> (j - 2))) & 0x40004000u);
}
__device__ __forceinline__ uint32_t max4(const u32x4& a) { return max(max(a[0], a[1]), max(a[2], a[3])); }
#ifndef ULONG
#define ULONG 8
#endif
__device__ __forceinline__ void until(uint32_t t) {
#pragma unroll 1
  for (int spin = 0; spin < 600; ++spin) {
    const int d = (int)(t - (uint32_t)__builtin_amdgcn_s_memrealtime());
    if (d <= 0) break;
    if (ULONG && d > 4 * ULONG) __builtin_amdgcn_s_sleep(ULONG); else __builtin_amdgcn_s_sleep(1);
  }
}
__device__ __forceinline__ uint32_t latest(uint32_t t) {
  const uint32_t t0 = (uint32_t)__builtin_amdgcn_readfirstlane(t) - 0x40000000u;
  return wave_maxu(t - t0) + t0;
}
__device__ __forceinline__ uint32_t gate(const char* ws, uint32_t po, uint32_t T) {
  u32x2 pv;
#pragma unroll 1
  for (int spin = 0; spin < SPIN_CAP; ++spin) {
    asm volatile("global_load_dwordx2 %0, %1, %2 sc1\n\ts_waitcnt vmcnt(0)" : "=&v"(pv) : "v"(po), "s"(ws) : "memory");
    if (!__any(pv[0] != T)) break;
    __builtin_amdgcn_s_sleep(2);
  }
  return pv[1];
}
__device__ __forceinline__ uint32_t gate2(const char* ws, uint32_t oa, uint32_t da, uint32_t ob, uint32_t T) {
  u32x2 pa, pb;
#pragma unroll 1
  for (int spin = 0; spin < SPIN_CAP; ++spin) {
    asm volatile("global_load_dwordx2 %0, %2, %4 sc1\n\tglobal_load_dwordx2 %1, %3, %4 sc1\n\ts_waitcnt vmcnt(0)" : "=&v"(pa), "=&v"(pb) : "v"(oa), "v"(ob), "s"(ws) : "memory");
    if (!__any(pa[0] != T || pb[0] != T)) break;
    __builtin_amdgcn_s_sleep(4);
  }
  pa[1] += da;
  return latest(((int)(pa[1] - pb[1]) > 0) ? pa[1] : pb[1]);
}
__device__ __forceinline__ void gsync(uint32_t a, uint32_t target) {
  uint32_t t, c, n = SPIN_CAP;
  uint64_t ex;
  asm volatile("s_waitcnt lgkmcnt(0)\n\ts_mov_b64 %3, exec\n\ts_mov_b64 exec, 1\n\tv_mov_b32 %0, 1\n\tds_add_u32 %4, %0\n\ts_mov_b64 exec, %3\n"
               "1:\n\ts_sleep 1\n\tds_read_b32 %0, %4\n\ts_sub_u32 %2, %2, 1\n\ts_cbranch_scc1 2f\n\ts_waitcnt lgkmcnt(0)\n\tv_readfirstlane_b32 %1, %0\n\t"
               "s_cmp_lt_u32 %1, %5\n\ts_cbranch_scc1 1b\n2:\n\ts_waitcnt lgkmcnt(0)"
               : "=&v"(t), "=&s"(c), "+s"(n), "=&s"(ex) : "v"(a), "n"(target) : "memory", "scc");
}
__device__ __forceinline__ void gsync0(uint32_t a, uint32_t target) {
  uint32_t t, c, n = SPIN_CAP;
  asm volatile("s_waitcnt lgkmcnt(0)\n\ts_mov_b64 exec, 1\n\tv_mov_b32 %0, 1\n\tds_add_u32 %3, %0\n\ts_mov_b64 exec, -1\n"
               "1:\n\tds_read_b32 %0, %3\n\ts_sub_u32 %2, %2, 1\n\ts_cbranch_scc1 2f\n\ts_waitcnt lgkmcnt(0)\n\tv_readfirstlane_b32 %1, %0\n\t"
               "s_cmp_lt_u32 %1, %4\n\ts_cbranch_scc1 1b\n2:\n\ts_waitcnt lgkmcnt(0)"
               : "=&v"(t), "=&s"(c), "+s"(n) : "v"(a), "n"(target) : "memory", "scc");
}
__device__ __forceinline__ void gwait(uint32_t a, uint32_t target) {
  uint32_t t, c, n = SPIN_CAP;
  asm volatile("1:\n\ts_sleep 2\n\tds_read_b32 %0, %3\n\ts_sub_u32 %2, %2, 1\n\ts_cbranch_scc1 2f\n\ts_waitcnt lgkmcnt(0)\n\tv_readfirstlane_b32 %1, %0\n\t"
               "s_cmp_lt_u32 %1, %4\n\ts_cbranch_scc1 1b\n2:\n\ts_waitcnt lgkmcnt(0)"
               : "=&v"(t), "=&s"(c), "+s"(n) : "v"(a), "n"(target) : "memory", "scc");
}
__device__ __forceinline__ void gwait0(uint32_t a, uint32_t target) {
  uint32_t t, c, n = SPIN_CAP;
  asm volatile("1:\n\tds_read_b32 %0, %3\n\ts_sub_u32 %2, %2, 1\n\ts_cbranch_scc1 2f\n\ts_waitcnt lgkmcnt(0)\n\tv_readfirstlane_b32 %1, %0\n\t"
               "s_cmp_lt_u32 %1, %4\n\ts_cbranch_scc1 1b\n2:\n\ts_waitcnt lgkmcnt(0)"
               : "=&v"(t), "=&s"(c), "+s"(n) : "v"(a), "n"(target) : "memory", "scc");
}
__device__ __forceinline__ void dma16(const void* base, uint32_t voff, uint32_t lds) {
  uint32_t keep;
  asm volatile("s_mov_b32 %0, m0\n\ts_mov_b32 m0, %3\n\ts_nop 0\n\tglobal_load_lds_dwordx4 %1, %2" DPOL "\n\ts_mov_b32 m0, %0" : "=&s"(keep) : "v"(voff), "s"(base), "s"(lds) : "memory");
}
__device__ __forceinline__ void dma16d(const void* base, uint32_t voff, uint32_t lds) {
  uint32_t keep;
  asm volatile("s_mov_b32 %0, m0\n\ts_mov_b32 m0, %3\n\ts_nop 0\n\tglobal_load_lds_dwordx4 %1, %2" DPOL2 "\n\ts_mov_b32 m0, %0" : "=&s"(keep) : "v"(voff), "s"(base), "s"(lds) : "memory");
}
__device__ __forceinline__ void nap(int n) {
  for (; n > 15; n -= 15) __builtin_amdgcn_s_sleep(15);
  switch (n) {
    case 1: __builtin_amdgcn_s_sleep(1); break; case 2: __builtin_amdgcn_s_sleep(2); break; case 3: __builtin_amdgcn_s_sleep(3); break;
    case 4: __builtin_amdgcn_s_sleep(4); break; case 5: __builtin_amdgcn_s_sleep(5); break; case 6: __builtin_amdgcn_s_sleep(6); break;
    case 7: __builtin_amdgcn_s_sleep(7); break; case 8: __builtin_amdgcn_s_sleep(8); break; case 9: __builtin_amdgcn_s_sleep(9); break;
    case 10: __builtin_amdgcn_s_sleep(10); break; case 11: __builtin_amdgcn_s_sleep(11); break; case 12: __builtin_amdgcn_s_sleep(12); break;
    case 13: __builtin_amdgcn_s_sleep(13); break; case 14: __builtin_amdgcn_s_sleep(14); break; case 15: __builtin_amdgcn_s_sleep(15); break;
    default: break;
  }
}
#define SLEEP(n) do { if ((n) > 0) __builtin_amdgcn_s_sleep(n); } while (0)
#define USLEEP 3
#define PSLEEP 2
#define PPRIO 0
#define LFIRST 1
#define WD 40
#define OSLEEP 12
#define QPRIO 1
#define KPRIO 2
#define GSLEEP 0
#define NSLEEP 0
#define UD 40
#define XD 0
#define RSLEEP 0
#define UPRIO 2
#define VD2 385
#define WDEPTH 6
#define ESLEEP 0
#define SD 58
#define KD 180
#define OD 0
#define KBACK 6
#define DDEPTH 6
#define C3 1
#define XROT 2
#define WSOFF 0
#define KN 1
#define RPF 1
#define LSLEEP 1
#define TSTOP 78
#define HSTOP 110
#define VEARLY 0ull
#define FPR 3
#define SPR 3
#define DPRIO 0
#define QPRIO2 3
#define HW 2
#define RD 57
#define OPOS 1
#define OD2 6
#define NA 12
#define RD2 88
#define ORD1 ((unsigned __int128)0x321087654u << 36 | 0x321087654ull)
#define WDEPTH2 5
#define RQ NA
#define HREV 1
#define QPRIO0 3
#define VPS 1
#define RW 0
#define NPRIO 0
#define XSL 10
#define V2D 24
#define N0 8
#define LPOS 1
#define ALLP 1
#define FORCESLOW 0
#define FORCEFB 0
#define CONV_ASM \
  "s_cmp_eq_u32 s89, 1\ns_cbranch_scc1 1f\ns_cmp_gt_u32 s89, 1\ns_cbranch_scc1 9f\nds_read_b64 v[124:125], v3 offset:576\n" \
  "ds_read_b64 v[126:127], v3 offset:584\nds_read_b64 v[120:121], v3 offset:64\nds_read_b64 v[122:123], v3 offset:72\n" \
  "ds_read_b64 v[116:117], v3 offset:640\nds_read_b64 v[118:119], v3 offset:648\nds_read_b64 v[112:113], v3 offset:128\n" \
  "ds_read_b64 v[114:115], v3 offset:136\nds_read_b64 v[108:109], v3 offset:704\nds_read_b64 v[110:111], v3 offset:712\n" \
  "ds_read_b64 v[104:105], v3 offset:192\nds_read_b64 v[106:107], v3 offset:200\nds_read_b64 v[100:101], v3 offset:768\n" \
  "ds_read_b64 v[102:103], v3 offset:776\nds_read_b64 v[96:97], v3 offset:256\nds_read_b64 v[98:99], v3 offset:264\n" \
  "ds_read_b64 v[92:93], v3 offset:832\nds_read_b64 v[94:95], v3 offset:840\nds_read_b64 v[88:89], v3 offset:320\n" \
  "ds_read_b64 v[90:91], v3 offset:328\nds_read_b64 v[84:85], v3 offset:896\nds_read_b64 v[86:87], v3 offset:904\n" \
  "ds_read_b64 v[80:81], v3 offset:384\nds_read_b64 v[82:83], v3 offset:392\nds_read_b64 v[76:77], v3 offset:960\n" \
  "ds_read_b64 v[78:79], v3 offset:968\nds_read_b64 v[72:73], v3 offset:448\nds_read_b64 v[74:75], v3 offset:456\n" \
  "ds_read_b64 v[68:69], v3 offset:1024\nds_read_b64 v[70:71], v3 offset:1032\nds_read_b64 v[64:65], v3 offset:512\n" \
  "ds_read_b64 v[66:67], v3 offset:520\nds_read_b64 v[60:61], v3 offset:0\nds_read_b64 v[62:63], v3 offset:8\nds_read_b64 v[56:57], v3 offset:1088\n" \
  "ds_read_b64 v[58:59], v3 offset:1096\ns_waitcnt lgkmcnt(0)\ns_barrier\nds_read_b128 v[36:39], v2 offset:0\nds_read_b128 v[40:43], v2 offset:64\n" \
  "ds_read_b128 v[44:47], v2 offset:128\nds_read_b128 v[48:51], v2 offset:192\ns_waitcnt lgkmcnt(3)\n" \
  "v_mfma_f32_16x16x32_f16 v[12:15], v[124:127], v[36:39], 0\ns_setprio 1\nds_read_b128 v[52:55], v2 offset:256\n" \
  "v_mfma_f32_16x16x32_f16 v[16:19], v[120:123], v[36:39], 0\ns_waitcnt lgkmcnt(3)\nv_mfma_f32_16x16x32_f16 v[12:15], v[116:119], v[40:43], v[12:15]\n" \
  "ds_read_b128 v[36:39], v2 offset:320\nv_mfma_f32_16x16x32_f16 v[16:19], v[112:115], v[40:43], v[16:19]\ns_waitcnt lgkmcnt(3)\n" \
  "v_mfma_f32_16x16x32_f16 v[12:15], v[108:111], v[44:47], v[12:15]\nds_read_b128 v[40:43], v2 offset:384\n" \
  "v_mfma_f32_16x16x32_f16 v[16:19], v[104:107], v[44:47], v[16:19]\ns_waitcnt lgkmcnt(3)\n" \
  "v_mfma_f32_16x16x32_f16 v[12:15], v[100:103], v[48:51], v[12:15]\nds_read_b128 v[44:47], v2 offset:448\n" \
  "v_mfma_f32_16x16x32_f16 v[16:19], v[96:99], v[48:51], v[16:19]\ns_waitcnt lgkmcnt(3)\n" \
  "v_mfma_f32_16x16x32_f16 v[12:15], v[92:95], v[52:55], v[12:15]\nv_mfma_f32_16x16x32_f16 v[16:19], v[88:91], v[52:55], v[16:19]\n" \
  "s_waitcnt lgkmcnt(2)\nv_mfma_f32_16x16x32_f16 v[12:15], v[84:87], v[36:39], v[12:15]\nds_read_b128 v[48:51], v2 offset:512\n" \
  "v_mfma_f32_16x16x32_f16 v[16:19], v[80:83], v[36:39], v[16:19]\ns_waitcnt lgkmcnt(2)\n" \
  "v_mfma_f32_16x16x32_f16 v[12:15], v[76:79], v[40:43], v[12:15]\nds_read_b128 v[52:55], v2 offset:576\n" \
  "v_mfma_f32_16x16x32_f16 v[16:19], v[72:75], v[40:43], v[16:19]\ns_waitcnt lgkmcnt(2)\n" \
  "v_mfma_f32_16x16x32_f16 v[12:15], v[68:71], v[44:47], v[12:15]\nds_read_b128 v[36:39], v2 offset:640\n" \
  "v_mfma_f32_16x16x32_f16 v[16:19], v[64:67], v[44:47], v[16:19]\nv_mfma_f32_16x16x32_f16 v[20:23], v[60:63], v[44:47], 0\n" \
  "ds_read_b128 v[40:43], v2 offset:704\nv_mfma_f32_16x16x32_f16 v[28:31], v[56:59], v[44:47], 0\ns_waitcnt lgkmcnt(3)\n" \
  "v_mfma_f32_16x16x32_f16 v[16:19], v[124:127], v[48:51], v[16:19]\nds_read_b128 v[56:59], v2 offset:768\nv_cvt_pkrtz_f16_f32 v32, v12, v13\n" \
  "v_mfma_f32_16x16x32_f16 v[20:23], v[120:123], v[48:51], v[20:23]\nv_cvt_pkrtz_f16_f32 v33, v14, v15\ns_waitcnt lgkmcnt(3)\n" \
  "v_mfma_f32_16x16x32_f16 v[16:19], v[116:119], v[52:55], v[16:19]\nds_read_b128 v[44:47], v2 offset:832\n" \
  "v_pk_add_u16 v32, v32, 4 op_sel_hi:[1,0] clamp\nv_mfma_f32_16x16x32_f16 v[20:23], v[112:115], v[52:55], v[20:23]\n" \
  "v_pk_add_u16 v33, v33, 4 op_sel_hi:[1,0] clamp\ns_waitcnt lgkmcnt(3)\nv_mfma_f32_16x16x32_f16 v[16:19], v[108:111], v[36:39], v[16:19]\n" \
  "ds_read_b128 v[48:51], v2 offset:896\nds_write_b64 v4, v[32:33] offset:0\nv_mfma_f32_16x16x32_f16 v[20:23], v[104:107], v[36:39], v[20:23]\n" \
  "s_waitcnt lgkmcnt(3)\nv_mfma_f32_16x16x32_f16 v[16:19], v[100:103], v[40:43], v[16:19]\nds_read_b128 v[52:55], v2 offset:960\n" \
  "v_mfma_f32_16x16x32_f16 v[20:23], v[96:99], v[40:43], v[20:23]\nv_mfma_f32_16x16x32_f16 v[16:19], v[92:95], v[56:59], v[16:19]\n" \
  "v_mfma_f32_16x16x32_f16 v[20:23], v[88:91], v[56:59], v[20:23]\nds_read_b128 v[36:39], v2 offset:1024\ns_waitcnt lgkmcnt(3)\n" \
  "v_mfma_f32_16x16x32_f16 v[16:19], v[84:87], v[44:47], v[16:19]\nv_mfma_f32_16x16x32_f16 v[20:23], v[80:83], v[44:47], v[20:23]\n" \
  "ds_read_b128 v[40:43], v2 offset:1088\nds_read_b64 v[56:57], v6 offset:256\nds_read_b64 v[58:59], v6 offset:264\n" \
  "v_mfma_f32_16x16x32_f16 v[16:19], v[76:79], v[48:51], v[16:19]\nv_mfma_f32_16x16x32_f16 v[20:23], v[72:75], v[48:51], v[20:23]\n" \
  "ds_read_b128 v[44:47], v2 offset:1152\ns_waitcnt lgkmcnt(4)\nv_mfma_f32_16x16x32_f16 v[16:19], v[68:71], v[52:55], v[16:19]\n" \
  "v_mfma_f32_16x16x32_f16 v[20:23], v[64:67], v[52:55], v[20:23]\nds_read_b128 v[48:51], v2 offset:1216\n" \
  "v_mfma_f32_16x16x32_f16 v[24:27], v[60:63], v[52:55], 0\nv_mfma_f32_16x16x32_f16 v[20:23], v[124:127], v[36:39], v[20:23]\n" \
  "ds_read_b128 v[52:55], v2 offset:1280\nv_mfma_f32_16x16x32_f16 v[24:27], v[120:123], v[36:39], v[24:27]\ns_waitcnt lgkmcnt(5)\n" \
  "v_mfma_f32_16x16x32_f16 v[20:23], v[116:119], v[40:43], v[20:23]\nds_read_b128 v[36:39], v2 offset:1344\nv_cvt_pkrtz_f16_f32 v32, v16, v17\n" \
  "v_mfma_f32_16x16x32_f16 v[24:27], v[112:115], v[40:43], v[24:27]\nv_cvt_pkrtz_f16_f32 v33, v18, v19\ns_waitcnt lgkmcnt(3)\n" \
  "v_mfma_f32_16x16x32_f16 v[20:23], v[108:111], v[44:47], v[20:23]\nds_read_b128 v[40:43], v2 offset:1408\n" \
  "v_pk_add_u16 v32, v32, 4 op_sel_hi:[1,0] clamp\nds_read_b32 v35, v9 offset:32\nv_mfma_f32_16x16x32_f16 v[24:27], v[104:107], v[44:47], v[24:27]\n" \
  "v_pk_add_u16 v33, v33, 4 op_sel_hi:[1,0] clamp\ns_waitcnt lgkmcnt(3)\nv_mfma_f32_16x16x32_f16 v[20:23], v[100:103], v[48:51], v[20:23]\n" \
  "ds_read_b128 v[44:47], v2 offset:1472\nds_write_b64 v4, v[32:33] offset:1024\nv_mfma_f32_16x16x32_f16 v[24:27], v[96:99], v[48:51], v[24:27]\n" \
  "v_mfma_f32_16x16x32_f16 v[20:23], v[92:95], v[52:55], v[20:23]\nv_mfma_f32_16x16x32_f16 v[24:27], v[88:91], v[52:55], v[24:27]\n" \
  "ds_read_b128 v[48:51], v2 offset:1536\ns_waitcnt lgkmcnt(3)\nv_readfirstlane_b32 s85, v35\ns_cmp_lt_u32 s85, 4\ns_cbranch_scc1 21f\n22:\n" \
  "v_mfma_f32_16x16x32_f16 v[20:23], v[84:87], v[36:39], v[20:23]\nv_mfma_f32_16x16x32_f16 v[24:27], v[80:83], v[36:39], v[24:27]\n" \
  "ds_read_b128 v[52:55], v2 offset:1600\nv_mfma_f32_16x16x32_f16 v[20:23], v[76:79], v[40:43], v[20:23]\n" \
  "v_mfma_f32_16x16x32_f16 v[24:27], v[72:75], v[40:43], v[24:27]\nds_read_b128 v[36:39], v2 offset:1664\ns_waitcnt lgkmcnt(4)\n" \
  "v_mfma_f32_16x16x32_f16 v[20:23], v[68:71], v[44:47], v[20:23]\nv_mfma_f32_16x16x32_f16 v[24:27], v[64:67], v[44:47], v[24:27]\n" \
  "ds_read_b128 v[40:43], v2 offset:1728\nv_mfma_f32_16x16x32_f16 v[28:31], v[60:63], v[44:47], v[28:31]\nds_read_b64_tr_b16 v[60:61], v5 offset:0\n" \
  "ds_read_b64_tr_b16 v[62:63], v5 offset:32\ns_waitcnt lgkmcnt(4)\nv_mfma_f32_16x16x32_f16 v[24:27], v[124:127], v[48:51], v[24:27]\n" \
  "v_mfma_f32_16x16x32_f16 v[28:31], v[120:123], v[48:51], v[28:31]\nds_read_b64_tr_b16 v[44:45], v5 offset:256\n" \
  "ds_read_b64_tr_b16 v[46:47], v5 offset:288\nv_mfma_f32_16x16x32_f16 v[24:27], v[116:119], v[52:55], v[24:27]\n" \
  "ds_read_b64 v[120:121], v6 offset:320\nds_read_b64 v[122:123], v6 offset:328\nv_mfma_f32_16x16x32_f16 v[28:31], v[112:115], v[52:55], v[28:31]\n" \
  "ds_read_b128 v[48:51], v2 offset:1792\ns_waitcnt lgkmcnt(7)\nv_mfma_f32_16x16x32_f16 v[24:27], v[108:111], v[36:39], v[24:27]\n" \
  "ds_read_b128 v[112:115], v2 offset:1856\nv_mfma_f32_16x16x32_f16 v[28:31], v[104:107], v[36:39], v[28:31]\nds_read_b64 v[52:53], v6 offset:128\n" \
  "ds_read_b64 v[54:55], v6 offset:136\nv_mfma_f32_16x16x32_f16 v[24:27], v[100:103], v[40:43], v[24:27]\nds_read_b128 v[104:107], v2 offset:1920\n" \
  "v_mfma_f32_16x16x32_f16 v[28:31], v[96:99], v[40:43], v[28:31]\nds_read_b64 v[36:37], v6 offset:192\nds_read_b64 v[38:39], v6 offset:200\n" \
  "s_waitcnt lgkmcnt(9)\nv_mfma_f32_16x16x32_f16 v[20:23], v[60:63], v[56:59], v[20:23]\nds_read_b128 v[96:99], v2 offset:1984\ns_waitcnt lgkmcnt(7)\n" \
  "v_mfma_f32_16x16x32_f16 v[20:23], v[44:47], v[120:123], v[20:23]\nv_mfma_f32_16x16x32_f16 v[24:27], v[92:95], v[48:51], v[24:27]\n" \
  "ds_read_b128 v[40:43], v2 offset:2048\nv_mfma_f32_16x16x32_f16 v[28:31], v[88:91], v[48:51], v[28:31]\nds_read_b64_tr_b16 v[88:89], v5 offset:512\n" \
  "ds_read_b64_tr_b16 v[90:91], v5 offset:544\ns_waitcnt lgkmcnt(6)\nv_mfma_f32_16x16x32_f16 v[24:27], v[84:87], v[112:115], v[24:27]\n" \
  "v_mfma_f32_16x16x32_f16 v[28:31], v[80:83], v[112:115], v[28:31]\nv_cvt_pkrtz_f16_f32 v32, v20, v21\nv_cvt_pkrtz_f16_f32 v33, v22, v23\n" \
  "v_mfma_f32_16x16x32_f16 v[24:27], v[76:79], v[104:107], v[24:27]\nds_read_b64_tr_b16 v[48:49], v5 offset:768\n" \
  "ds_read_b64_tr_b16 v[50:51], v5 offset:800\nv_mfma_f32_16x16x32_f16 v[28:31], v[72:75], v[104:107], v[28:31]\n" \
  "ds_read_b128 v[80:83], v2 offset:2112\ns_waitcnt lgkmcnt(5)\nv_mfma_f32_16x16x32_f16 v[24:27], v[68:71], v[96:99], v[24:27]\n" \
  "ds_read_b128 v[112:115], v2 offset:2176\nv_pk_add_u16 v32, v32, 4 op_sel_hi:[1,0] clamp\n" \
  "v_mfma_f32_16x16x32_f16 v[28:31], v[64:67], v[96:99], v[28:31]\nds_read_b128 v[72:75], v2 offset:2240\n" \
  "v_pk_add_u16 v33, v33, 4 op_sel_hi:[1,0] clamp\nv_mfma_f32_16x16x32_f16 v[28:31], v[124:127], v[40:43], v[28:31]\n" \
  "ds_read_b128 v[104:107], v2 offset:2304\nds_write_b64 v4, v[32:33] offset:2048\nv_mfma_f32_16x16x32_f16 v[24:27], v[60:63], v[52:55], v[24:27]\n" \
  "ds_read_b128 v[64:67], v2 offset:2368\nds_read_b64 v[96:97], v6 offset:0\nds_read_b64 v[98:99], v6 offset:8\n" \
  "v_mfma_f32_16x16x32_f16 v[24:27], v[44:47], v[36:39], v[24:27]\nds_read_b128 v[124:127], v2 offset:2432\ns_waitcnt lgkmcnt(9)\n" \
  "v_mfma_f32_16x16x32_f16 v[24:27], v[88:91], v[56:59], v[24:27]\nds_read_b128 v[40:43], v2 offset:2496\n" \
  "v_mfma_f32_16x16x32_f16 v[24:27], v[48:51], v[120:123], v[24:27]\ns_waitcnt lgkmcnt(8)\n" \
  "v_mfma_f32_16x16x32_f16 v[28:31], v[116:119], v[80:83], v[28:31]\nds_read_b64 v[116:117], v6 offset:64\nds_read_b64 v[118:119], v6 offset:72\n" \
  "v_mfma_f32_16x16x32_f16 v[28:31], v[108:111], v[112:115], v[28:31]\nds_read_b64_tr_b16 v[80:81], v5 offset:1024\n" \
  "ds_read_b64_tr_b16 v[82:83], v5 offset:1056\ns_waitcnt lgkmcnt(10)\nv_mfma_f32_16x16x32_f16 v[28:31], v[100:103], v[72:75], v[28:31]\n" \
  "v_cvt_pkrtz_f16_f32 v32, v24, v25\nv_cvt_pkrtz_f16_f32 v33, v26, v27\nv_mfma_f32_16x16x32_f16 v[28:31], v[92:95], v[104:107], v[28:31]\n" \
  "v_pk_add_u16 v32, v32, 4 op_sel_hi:[1,0] clamp\ns_waitcnt lgkmcnt(5)\nv_mfma_f32_16x16x32_f16 v[28:31], v[84:87], v[64:67], v[28:31]\n" \
  "ds_read_b64_tr_b16 v[108:109], v5 offset:1280\nds_read_b64_tr_b16 v[110:111], v5 offset:1312\n" \
  "v_mfma_f32_16x16x32_f16 v[28:31], v[76:79], v[124:127], v[28:31]\nds_read_b64_tr_b16 v[112:113], v7 offset:0\n" \
  "ds_read_b64_tr_b16 v[114:115], v7 offset:64\ns_waitcnt lgkmcnt(6)\nv_mfma_f32_16x16x32_f16 v[28:31], v[68:71], v[40:43], v[28:31]\n" \
  "ds_read2_b32 v[100:101], v8 offset0:48 offset1:49\nds_read2_b32 v[102:103], v8 offset0:50 offset1:51\n" \
  "v_mfma_f32_16x16x32_f16 v[28:31], v[60:63], v[96:99], v[28:31]\nv_pk_add_u16 v33, v33, 4 op_sel_hi:[1,0] clamp\n" \
  "ds_write_b64 v4, v[32:33] offset:3072\nv_mfma_f32_16x16x32_f16 v[28:31], v[44:47], v[116:119], v[28:31]\n" \
  "v_mfma_f32_16x16x32_f16 v[28:31], v[88:91], v[52:55], v[28:31]\nv_mfma_f32_16x16x32_f16 v[28:31], v[48:51], v[36:39], v[28:31]\n" \
  "s_waitcnt lgkmcnt(5)\nv_mfma_f32_16x16x32_f16 v[28:31], v[80:83], v[56:59], v[28:31]\n" \
  "v_mfma_f32_16x16x32_f16 v[28:31], v[108:111], v[120:123], v[28:31]\ns_waitcnt lgkmcnt(1)\n" \
  "v_mfma_f32_16x16x32_f16 v[28:31], v[112:115], v[100:103], v[28:31]\ns_nop 7\nv_cvt_pkrtz_f16_f32 v32, v28, v29\nv_cvt_pkrtz_f16_f32 v33, v30, v31\n" \
  "v_pk_add_u16 v32, v32, 4 op_sel_hi:[1,0] clamp\nv_pk_add_u16 v33, v33, 4 op_sel_hi:[1,0] clamp\nds_write_b64 v4, v[32:33] offset:4096\n" \
  "s_setprio 0\ns_setpc_b64 s[90:91]\n21:\ns_movk_i32 s84, 0x4000\n23:\nds_read_b32 v35, v9 offset:32\ns_sub_u32 s84, s84, 1\ns_cbranch_scc1 22b\n" \
  "s_waitcnt lgkmcnt(0)\nv_readfirstlane_b32 s85, v35\ns_cmp_lt_u32 s85, 4\ns_cbranch_scc1 23b\ns_branch 22b\n1:\n" \
  "ds_read_b64 v[124:125], v3 offset:1088\nds_read_b64 v[126:127], v3 offset:1096\nds_read_b64 v[120:121], v3 offset:0\n" \
  "ds_read_b64 v[122:123], v3 offset:8\nds_read_b64 v[116:117], v3 offset:64\nds_read_b64 v[118:119], v3 offset:72\n" \
  "ds_read_b64 v[112:113], v3 offset:128\nds_read_b64 v[114:115], v3 offset:136\nds_read_b64 v[108:109], v3 offset:192\n" \
  "ds_read_b64 v[110:111], v3 offset:200\nds_read_b64 v[104:105], v3 offset:256\nds_read_b64 v[106:107], v3 offset:264\n" \
  "ds_read_b64 v[100:101], v3 offset:320\nds_read_b64 v[102:103], v3 offset:328\nds_read_b64 v[96:97], v3 offset:384\n" \
  "ds_read_b64 v[98:99], v3 offset:392\nds_read_b64 v[92:93], v3 offset:448\nds_read_b64 v[94:95], v3 offset:456\n" \
  "ds_read_b64 v[88:89], v3 offset:512\nds_read_b64 v[90:91], v3 offset:520\nds_read_b64 v[84:85], v3 offset:576\n" \
  "ds_read_b64 v[86:87], v3 offset:584\nds_read_b64 v[80:81], v3 offset:640\nds_read_b64 v[82:83], v3 offset:648\n" \
  "ds_read_b64 v[76:77], v3 offset:704\nds_read_b64 v[78:79], v3 offset:712\nds_read_b64 v[72:73], v3 offset:768\n" \
  "ds_read_b64 v[74:75], v3 offset:776\nds_read_b64 v[68:69], v3 offset:832\nds_read_b64 v[70:71], v3 offset:840\n" \
  "ds_read_b64 v[64:65], v3 offset:896\nds_read_b64 v[66:67], v3 offset:904\nds_read_b64 v[60:61], v3 offset:960\n" \
  "ds_read_b64 v[62:63], v3 offset:968\nds_read_b64 v[56:57], v3 offset:1024\nds_read_b64 v[58:59], v3 offset:1032\n" \
  "ds_read_b64 v[52:53], v6 offset:0\nds_read_b64 v[54:55], v6 offset:8\nds_read_b64 v[48:49], v6 offset:64\nds_read_b64 v[50:51], v6 offset:72\n" \
  "s_waitcnt lgkmcnt(0)\ns_barrier\nds_read_b128 v[28:31], v2 offset:960\nds_read_b128 v[32:35], v2 offset:1472\n" \
  "ds_read_b128 v[36:39], v2 offset:1984\nds_read_b128 v[40:43], v2 offset:2048\nds_read_b128 v[44:47], v2 offset:2112\ns_waitcnt lgkmcnt(3)\n" \
  "v_mfma_f32_16x16x32_f16 v[12:15], v[124:127], v[28:31], 0\ns_setprio 1\nds_read_b128 v[28:31], v2 offset:2176\n" \
  "v_mfma_f32_16x16x32_f16 v[16:19], v[124:127], v[32:35], 0\nds_read_b128 v[32:35], v2 offset:2240\ns_waitcnt lgkmcnt(3)\n" \
  "v_mfma_f32_16x16x32_f16 v[12:15], v[120:123], v[36:39], v[12:15]\nv_mfma_f32_16x16x32_f16 v[20:23], v[124:127], v[36:39], 0\n" \
  "ds_read_b128 v[124:127], v2 offset:2304\nds_read_b128 v[36:39], v2 offset:2368\nv_mfma_f32_16x16x32_f16 v[12:15], v[116:119], v[40:43], v[12:15]\n" \
  "ds_read_b128 v[40:43], v2 offset:2432\ns_waitcnt lgkmcnt(4)\nv_mfma_f32_16x16x32_f16 v[12:15], v[112:115], v[44:47], v[12:15]\n" \
  "ds_read_b128 v[44:47], v2 offset:2496\nv_mfma_f32_16x16x32_f16 v[12:15], v[108:111], v[28:31], v[12:15]\nds_read_b128 v[28:31], v2 offset:2560\n" \
  "s_waitcnt lgkmcnt(4)\nv_mfma_f32_16x16x32_f16 v[12:15], v[104:107], v[32:35], v[12:15]\nds_read_b128 v[32:35], v2 offset:2624\n" \
  "v_mfma_f32_16x16x32_f16 v[12:15], v[100:103], v[124:127], v[12:15]\ns_waitcnt lgkmcnt(3)\n" \
  "v_mfma_f32_16x16x32_f16 v[12:15], v[96:99], v[36:39], v[12:15]\nds_read_b128 v[124:127], v2 offset:2688\n" \
  "v_mfma_f32_16x16x32_f16 v[12:15], v[92:95], v[40:43], v[12:15]\ns_waitcnt lgkmcnt(3)\n" \
  "v_mfma_f32_16x16x32_f16 v[12:15], v[88:91], v[44:47], v[12:15]\nds_read_b128 v[36:39], v2 offset:2752\n" \
  "v_mfma_f32_16x16x32_f16 v[16:19], v[120:123], v[44:47], v[16:19]\ns_waitcnt lgkmcnt(3)\n" \
  "v_mfma_f32_16x16x32_f16 v[12:15], v[84:87], v[28:31], v[12:15]\nds_read_b128 v[40:43], v2 offset:2816\n" \
  "v_mfma_f32_16x16x32_f16 v[16:19], v[116:119], v[28:31], v[16:19]\ns_waitcnt lgkmcnt(3)\n" \
  "v_mfma_f32_16x16x32_f16 v[12:15], v[80:83], v[32:35], v[12:15]\nds_read_b128 v[44:47], v2 offset:2880\n" \
  "v_mfma_f32_16x16x32_f16 v[16:19], v[112:115], v[32:35], v[16:19]\ns_waitcnt lgkmcnt(3)\n" \
  "v_mfma_f32_16x16x32_f16 v[12:15], v[76:79], v[124:127], v[12:15]\nds_read_b128 v[28:31], v2 offset:2944\n" \
  "v_mfma_f32_16x16x32_f16 v[16:19], v[108:111], v[124:127], v[16:19]\ns_waitcnt lgkmcnt(3)\n" \
  "v_mfma_f32_16x16x32_f16 v[12:15], v[72:75], v[36:39], v[12:15]\nds_read_b128 v[32:35], v2 offset:3008\n" \
  "v_mfma_f32_16x16x32_f16 v[16:19], v[104:107], v[36:39], v[16:19]\ns_waitcnt lgkmcnt(3)\n" \
  "v_mfma_f32_16x16x32_f16 v[12:15], v[68:71], v[40:43], v[12:15]\nv_mfma_f32_16x16x32_f16 v[16:19], v[100:103], v[40:43], v[16:19]\n" \
  "ds_read_b128 v[124:127], v2 offset:3072\ns_waitcnt lgkmcnt(3)\nv_mfma_f32_16x16x32_f16 v[12:15], v[64:67], v[44:47], v[12:15]\n" \
  "v_mfma_f32_16x16x32_f16 v[16:19], v[96:99], v[44:47], v[16:19]\nds_read_b128 v[36:39], v2 offset:3136\ns_waitcnt lgkmcnt(3)\n" \
  "v_mfma_f32_16x16x32_f16 v[12:15], v[60:63], v[28:31], v[12:15]\nds_read_b64 v[40:41], v6 offset:128\nds_read_b64 v[42:43], v6 offset:136\n" \
  "v_mfma_f32_16x16x32_f16 v[16:19], v[92:95], v[28:31], v[16:19]\nds_read_b128 v[44:47], v2 offset:3200\ns_waitcnt lgkmcnt(4)\n" \
  "v_mfma_f32_16x16x32_f16 v[12:15], v[56:59], v[32:35], v[12:15]\nv_mfma_f32_16x16x32_f16 v[16:19], v[88:91], v[32:35], v[16:19]\n" \
  "ds_read_b128 v[28:31], v2 offset:3264\nv_mfma_f32_16x16x32_f16 v[20:23], v[120:123], v[32:35], v[20:23]\nds_read_b64 v[120:121], v6 offset:192\n" \
  "ds_read_b64 v[122:123], v6 offset:200\nv_mfma_f32_16x16x32_f16 v[16:19], v[84:87], v[124:127], v[16:19]\nds_read_b128 v[32:35], v2 offset:3328\n" \
  "v_mfma_f32_16x16x32_f16 v[20:23], v[116:119], v[124:127], v[20:23]\nds_read_b64 v[116:117], v6 offset:256\nds_read_b64 v[118:119], v6 offset:264\n" \
  "s_waitcnt lgkmcnt(6)\nv_mfma_f32_16x16x32_f16 v[16:19], v[80:83], v[36:39], v[16:19]\nds_read_b128 v[124:127], v2 offset:3392\n" \
  "v_mfma_f32_16x16x32_f16 v[20:23], v[112:115], v[36:39], v[20:23]\nds_read_b64 v[112:113], v6 offset:320\nds_read_b64 v[114:115], v6 offset:328\n" \
  "v_mfma_f32_16x16x32_f16 v[16:19], v[76:79], v[44:47], v[16:19]\nds_read_b128 v[36:39], v2 offset:3456\n" \
  "v_mfma_f32_16x16x32_f16 v[20:23], v[108:111], v[44:47], v[20:23]\ns_waitcnt lgkmcnt(6)\n" \
  "v_mfma_f32_16x16x32_f16 v[16:19], v[72:75], v[28:31], v[16:19]\nds_read_b128 v[108:111], v2 offset:3520\n" \
  "v_mfma_f32_16x16x32_f16 v[20:23], v[104:107], v[28:31], v[20:23]\nv_mfma_f32_16x16x32_f16 v[16:19], v[68:71], v[32:35], v[16:19]\n" \
  "ds_read_b128 v[44:47], v2 offset:3584\nv_mfma_f32_16x16x32_f16 v[20:23], v[100:103], v[32:35], v[20:23]\nds_read_b128 v[104:107], v2 offset:3648\n" \
  "ds_read_b32 v27, v9 offset:32\ns_waitcnt lgkmcnt(4)\nv_mfma_f32_16x16x32_f16 v[16:19], v[64:67], v[124:127], v[16:19]\n" \
  "ds_read_b128 v[28:31], v2 offset:3712\nv_mfma_f32_16x16x32_f16 v[20:23], v[96:99], v[124:127], v[20:23]\nds_read_b128 v[100:103], v2 offset:3776\n" \
  "ds_read2_b32 v[32:33], v8 offset0:32 offset1:33\nds_read2_b32 v[34:35], v8 offset0:34 offset1:35\n" \
  "v_mfma_f32_16x16x32_f16 v[16:19], v[60:63], v[36:39], v[16:19]\nds_read_b128 v[96:99], v2 offset:3840\n" \
  "v_mfma_f32_16x16x32_f16 v[20:23], v[92:95], v[36:39], v[20:23]\nds_read_b128 v[124:127], v2 offset:3904\ns_waitcnt lgkmcnt(8)\n" \
  "v_mfma_f32_16x16x32_f16 v[16:19], v[56:59], v[108:111], v[16:19]\nds_read_b128 v[92:95], v2 offset:3968\ns_waitcnt lgkmcnt(7)\n" \
  "v_readfirstlane_b32 s85, v27\ns_cmp_lt_u32 s85, 4\ns_cbranch_scc1 31f\n32:\nv_mfma_f32_16x16x32_f16 v[20:23], v[88:91], v[108:111], v[20:23]\n" \
  "ds_read_b128 v[36:39], v2 offset:4032\nds_read_b64_tr_b16 v[88:89], v5 offset:512\nds_read_b64_tr_b16 v[90:91], v5 offset:544\n" \
  "v_mfma_f32_16x16x32_f16 v[20:23], v[84:87], v[44:47], v[20:23]\nds_read_b64_tr_b16 v[108:109], v5 offset:768\n" \
  "ds_read_b64_tr_b16 v[110:111], v5 offset:800\nv_mfma_f32_16x16x32_f16 v[20:23], v[80:83], v[104:107], v[20:23]\n" \
  "ds_read_b64_tr_b16 v[84:85], v5 offset:1024\nds_read_b64_tr_b16 v[86:87], v5 offset:1056\ns_waitcnt lgkmcnt(12)\n" \
  "v_mfma_f32_16x16x32_f16 v[20:23], v[76:79], v[28:31], v[20:23]\nds_read_b64_tr_b16 v[44:45], v5 offset:1280\n" \
  "ds_read_b64_tr_b16 v[46:47], v5 offset:1312\nv_mfma_f32_16x16x32_f16 v[20:23], v[72:75], v[100:103], v[20:23]\ns_waitcnt lgkmcnt(10)\n" \
  "v_mfma_f32_16x16x32_f16 v[20:23], v[68:71], v[96:99], v[20:23]\nds_read_b64_tr_b16 v[80:81], v5 offset:1536\n" \
  "ds_read_b64_tr_b16 v[82:83], v5 offset:1568\nv_mfma_f32_16x16x32_f16 v[20:23], v[64:67], v[124:127], v[20:23]\n" \
  "ds_read_b64_tr_b16 v[104:105], v5 offset:1792\nds_read_b64_tr_b16 v[106:107], v5 offset:1824\ns_waitcnt lgkmcnt(12)\n" \
  "v_mfma_f32_16x16x32_f16 v[20:23], v[60:63], v[92:95], v[20:23]\nds_read_b64_tr_b16 v[76:77], v5 offset:2048\n" \
  "ds_read_b64_tr_b16 v[78:79], v5 offset:2080\nv_mfma_f32_16x16x32_f16 v[20:23], v[56:59], v[36:39], v[20:23]\ns_waitcnt lgkmcnt(10)\n" \
  "v_mfma_f32_16x16x32_f16 v[12:15], v[88:91], v[52:55], v[12:15]\nds_read_b64_tr_b16 v[28:29], v5 offset:2304\n" \
  "ds_read_b64_tr_b16 v[30:31], v5 offset:2336\nv_mfma_f32_16x16x32_f16 v[12:15], v[108:111], v[48:51], v[12:15]\n" \
  "ds_read_b64_tr_b16 v[72:73], v5 offset:2560\nds_read_b64_tr_b16 v[74:75], v5 offset:2592\ns_waitcnt lgkmcnt(10)\n" \
  "v_mfma_f32_16x16x32_f16 v[12:15], v[84:87], v[40:43], v[12:15]\nds_read_b64_tr_b16 v[100:101], v5 offset:2816\n" \
  "ds_read_b64_tr_b16 v[102:103], v5 offset:2848\nv_mfma_f32_16x16x32_f16 v[16:19], v[84:87], v[52:55], v[16:19]\n" \
  "ds_read_b64_tr_b16 v[68:69], v7 offset:0\nds_read_b64_tr_b16 v[70:71], v7 offset:64\n" \
  "v_mfma_f32_16x16x32_f16 v[12:15], v[44:47], v[120:123], v[12:15]\nv_mfma_f32_16x16x32_f16 v[16:19], v[44:47], v[48:51], v[16:19]\n" \
  "s_waitcnt lgkmcnt(10)\nv_mfma_f32_16x16x32_f16 v[12:15], v[80:83], v[116:119], v[12:15]\nds_read2_b32 v[96:97], v8 offset0:16 offset1:17\n" \
  "ds_read2_b32 v[98:99], v8 offset0:18 offset1:19\nv_mfma_f32_16x16x32_f16 v[16:19], v[80:83], v[40:43], v[16:19]\n" \
  "ds_read2_b32 v[64:65], v8 offset0:0 offset1:1\nds_read2_b32 v[66:67], v8 offset0:2 offset1:3\n" \
  "v_mfma_f32_16x16x32_f16 v[20:23], v[80:83], v[52:55], v[20:23]\nv_mfma_f32_16x16x32_f16 v[12:15], v[104:107], v[112:115], v[12:15]\n" \
  "v_mfma_f32_16x16x32_f16 v[16:19], v[104:107], v[120:123], v[16:19]\nv_mfma_f32_16x16x32_f16 v[20:23], v[104:107], v[48:51], v[20:23]\n" \
  "s_waitcnt lgkmcnt(10)\nv_mfma_f32_16x16x32_f16 v[16:19], v[76:79], v[116:119], v[16:19]\nds_read_b64_tr_b16 v[124:125], v7 offset:512\n" \
  "ds_read_b64_tr_b16 v[126:127], v7 offset:576\nv_mfma_f32_16x16x32_f16 v[20:23], v[76:79], v[40:43], v[20:23]\n" \
  "ds_read2_b32 v[60:61], v8 offset0:48 offset1:49\nds_read2_b32 v[62:63], v8 offset0:50 offset1:51\n" \
  "v_mfma_f32_16x16x32_f16 v[16:19], v[28:31], v[112:115], v[16:19]\nv_mfma_f32_16x16x32_f16 v[20:23], v[28:31], v[120:123], v[20:23]\n" \
  "s_waitcnt lgkmcnt(10)\nv_mfma_f32_16x16x32_f16 v[20:23], v[72:75], v[116:119], v[20:23]\nds_read_b64_tr_b16 v[92:93], v7 offset:1024\n" \
  "ds_read_b64_tr_b16 v[94:95], v7 offset:1088\nv_mfma_f32_16x16x32_f16 v[20:23], v[100:103], v[112:115], v[20:23]\n" \
  "ds_read_b64_tr_b16 v[56:57], v7 offset:1536\nds_read_b64_tr_b16 v[58:59], v7 offset:1600\ns_waitcnt lgkmcnt(10)\n" \
  "v_mfma_f32_16x16x32_f16 v[12:15], v[68:71], v[32:35], v[12:15]\nv_mfma_f32_16x16x32_f16 v[16:19], v[68:71], v[96:99], v[16:19]\n" \
  "s_waitcnt lgkmcnt(6)\nv_mfma_f32_16x16x32_f16 v[20:23], v[68:71], v[64:67], v[20:23]\ns_waitcnt lgkmcnt(2)\n" \
  "v_mfma_f32_16x16x32_f16 v[12:15], v[124:127], v[60:63], v[12:15]\nv_mfma_f32_16x16x32_f16 v[16:19], v[124:127], v[32:35], v[16:19]\n" \
  "v_mfma_f32_16x16x32_f16 v[20:23], v[124:127], v[96:99], v[20:23]\nv_mfma_f32_16x16x32_f16 v[16:19], v[92:95], v[60:63], v[16:19]\n" \
  "v_mfma_f32_16x16x32_f16 v[20:23], v[92:95], v[32:35], v[20:23]\ns_waitcnt lgkmcnt(0)\n" \
  "v_mfma_f32_16x16x32_f16 v[20:23], v[56:59], v[60:63], v[20:23]\ns_nop 1\nv_cvt_pkrtz_f16_f32 v24, v12, v13\nv_cvt_pkrtz_f16_f32 v25, v14, v15\n" \
  "v_pk_add_u16 v24, v24, 4 op_sel_hi:[1,0] clamp\nv_pk_add_u16 v25, v25, 4 op_sel_hi:[1,0] clamp\nds_write_b64 v4, v[24:25] offset:5120\n" \
  "v_cvt_pkrtz_f16_f32 v24, v16, v17\nv_cvt_pkrtz_f16_f32 v25, v18, v19\nv_pk_add_u16 v24, v24, 4 op_sel_hi:[1,0] clamp\n" \
  "v_pk_add_u16 v25, v25, 4 op_sel_hi:[1,0] clamp\nds_write_b64 v4, v[24:25] offset:6144\nv_cvt_pkrtz_f16_f32 v24, v20, v21\n" \
  "v_cvt_pkrtz_f16_f32 v25, v22, v23\nv_pk_add_u16 v24, v24, 4 op_sel_hi:[1,0] clamp\nv_pk_add_u16 v25, v25, 4 op_sel_hi:[1,0] clamp\n" \
  "ds_write_b64 v4, v[24:25] offset:7168\ns_setprio 0\ns_setpc_b64 s[90:91]\n31:\ns_movk_i32 s84, 0x4000\n33:\nds_read_b32 v27, v9 offset:32\n" \
  "s_sub_u32 s84, s84, 1\ns_cbranch_scc1 32b\ns_waitcnt lgkmcnt(0)\nv_readfirstlane_b32 s85, v27\ns_cmp_lt_u32 s85, 4\ns_cbranch_scc1 33b\n" \
  "s_branch 32b\n9:\ns_cmp_eq_u32 s89, 3\ns_cbranch_scc1 3f\ns_cmp_eq_u32 s89, 4\ns_cbranch_scc1 4f\ns_cmp_eq_u32 s89, 5\ns_cbranch_scc1 5f\n" \
  "ds_read_b64 v[124:125], v3 offset:576\nds_read_b64 v[126:127], v3 offset:584\nds_read_b64 v[120:121], v3 offset:64\n" \
  "ds_read_b64 v[122:123], v3 offset:72\nds_read_b64 v[116:117], v3 offset:640\nds_read_b64 v[118:119], v3 offset:648\n" \
  "ds_read_b64 v[112:113], v3 offset:128\nds_read_b64 v[114:115], v3 offset:136\nds_read_b64 v[108:109], v3 offset:704\n" \
  "ds_read_b64 v[110:111], v3 offset:712\nds_read_b64 v[104:105], v3 offset:192\nds_read_b64 v[106:107], v3 offset:200\n" \
  "ds_read_b64 v[100:101], v3 offset:768\nds_read_b64 v[102:103], v3 offset:776\nds_read_b64 v[96:97], v3 offset:256\n" \
  "ds_read_b64 v[98:99], v3 offset:264\nds_read_b64 v[92:93], v3 offset:832\nds_read_b64 v[94:95], v3 offset:840\n" \
  "ds_read_b64 v[88:89], v3 offset:320\nds_read_b64 v[90:91], v3 offset:328\nds_read_b64 v[84:85], v3 offset:896\n" \
  "ds_read_b64 v[86:87], v3 offset:904\nds_read_b64 v[80:81], v3 offset:384\nds_read_b64 v[82:83], v3 offset:392\n" \
  "ds_read_b64 v[76:77], v3 offset:960\nds_read_b64 v[78:79], v3 offset:968\nds_read_b64 v[72:73], v3 offset:448\n" \
  "ds_read_b64 v[74:75], v3 offset:456\nds_read_b64 v[68:69], v3 offset:1024\nds_read_b64 v[70:71], v3 offset:1032\n" \
  "ds_read_b64 v[64:65], v3 offset:512\nds_read_b64 v[66:67], v3 offset:520\ns_waitcnt lgkmcnt(0)\ns_barrier\nds_read_b128 v[24:27], v2 offset:0\n" \
  "ds_read_b128 v[28:31], v2 offset:64\nds_read_b128 v[32:35], v2 offset:128\nds_read_b128 v[36:39], v2 offset:192\ns_waitcnt lgkmcnt(3)\n" \
  "v_mfma_f32_16x16x32_f16 v[12:15], v[124:127], v[24:27], 0\ns_setprio 1\nds_read_b128 v[40:43], v2 offset:256\n" \
  "v_mfma_f32_16x16x32_f16 v[16:19], v[120:123], v[24:27], 0\ns_waitcnt lgkmcnt(3)\nv_mfma_f32_16x16x32_f16 v[12:15], v[116:119], v[28:31], v[12:15]\n" \
  "ds_read_b128 v[44:47], v2 offset:320\nv_mfma_f32_16x16x32_f16 v[16:19], v[112:115], v[28:31], v[16:19]\ns_waitcnt lgkmcnt(3)\n" \
  "v_mfma_f32_16x16x32_f16 v[12:15], v[108:111], v[32:35], v[12:15]\nds_read_b128 v[48:51], v2 offset:384\n" \
  "v_mfma_f32_16x16x32_f16 v[16:19], v[104:107], v[32:35], v[16:19]\ns_waitcnt lgkmcnt(3)\n" \
  "v_mfma_f32_16x16x32_f16 v[12:15], v[100:103], v[36:39], v[12:15]\nds_read_b128 v[52:55], v2 offset:448\n" \
  "v_mfma_f32_16x16x32_f16 v[16:19], v[96:99], v[36:39], v[16:19]\ns_waitcnt lgkmcnt(3)\n" \
  "v_mfma_f32_16x16x32_f16 v[12:15], v[92:95], v[40:43], v[12:15]\nds_read_b128 v[56:59], v2 offset:512\n" \
  "v_mfma_f32_16x16x32_f16 v[16:19], v[88:91], v[40:43], v[16:19]\nds_read_b128 v[60:63], v2 offset:576\ns_waitcnt lgkmcnt(3)\n" \
  "v_mfma_f32_16x16x32_f16 v[12:15], v[84:87], v[44:47], v[12:15]\nds_read_b128 v[120:123], v2 offset:640\n" \
  "v_mfma_f32_16x16x32_f16 v[16:19], v[80:83], v[44:47], v[16:19]\nds_read_b128 v[24:27], v2 offset:704\n" \
  "v_mfma_f32_16x16x32_f16 v[12:15], v[76:79], v[48:51], v[12:15]\nds_read_b128 v[112:115], v2 offset:768\n" \
  "v_mfma_f32_16x16x32_f16 v[16:19], v[72:75], v[48:51], v[16:19]\nds_read_b128 v[28:31], v2 offset:832\ns_waitcnt lgkmcnt(5)\n" \
  "v_mfma_f32_16x16x32_f16 v[12:15], v[68:71], v[52:55], v[12:15]\nds_read_b128 v[104:107], v2 offset:896\n" \
  "v_mfma_f32_16x16x32_f16 v[16:19], v[64:67], v[52:55], v[16:19]\nds_read_b128 v[32:35], v2 offset:960\n" \
  "v_mfma_f32_16x16x32_f16 v[16:19], v[124:127], v[56:59], v[16:19]\ns_waitcnt lgkmcnt(5)\n" \
  "v_mfma_f32_16x16x32_f16 v[16:19], v[116:119], v[60:63], v[16:19]\nv_mfma_f32_16x16x32_f16 v[16:19], v[108:111], v[120:123], v[16:19]\n" \
  "s_waitcnt lgkmcnt(3)\nv_mfma_f32_16x16x32_f16 v[16:19], v[100:103], v[24:27], v[16:19]\nv_cvt_pkrtz_f16_f32 v20, v12, v13\n" \
  "v_cvt_pkrtz_f16_f32 v21, v14, v15\nv_mfma_f32_16x16x32_f16 v[16:19], v[92:95], v[112:115], v[16:19]\n" \
  "v_pk_add_u16 v20, v20, 4 op_sel_hi:[1,0] clamp\ns_waitcnt lgkmcnt(2)\nv_mfma_f32_16x16x32_f16 v[16:19], v[84:87], v[28:31], v[16:19]\n" \
  "v_pk_add_u16 v21, v21, 4 op_sel_hi:[1,0] clamp\ns_waitcnt lgkmcnt(1)\nv_mfma_f32_16x16x32_f16 v[16:19], v[76:79], v[104:107], v[16:19]\n" \
  "ds_write_b64 v4, v[20:21] offset:0\ns_waitcnt lgkmcnt(1)\nv_mfma_f32_16x16x32_f16 v[16:19], v[68:71], v[32:35], v[16:19]\ns_nop 7\n" \
  "v_cvt_pkrtz_f16_f32 v20, v16, v17\nv_cvt_pkrtz_f16_f32 v21, v18, v19\nv_pk_add_u16 v20, v20, 4 op_sel_hi:[1,0] clamp\n" \
  "v_pk_add_u16 v21, v21, 4 op_sel_hi:[1,0] clamp\nds_write_b64 v4, v[20:21] offset:1024\ns_setprio 0\ns_setpc_b64 s[90:91]\n3:\n" \
  "ds_read_b64 v[124:125], v3 offset:0\nds_read_b64 v[126:127], v3 offset:8\nds_read_b64 v[120:121], v3 offset:64\n" \
  "ds_read_b64 v[122:123], v3 offset:72\nds_read_b64 v[116:117], v3 offset:128\nds_read_b64 v[118:119], v3 offset:136\n" \
  "ds_read_b64 v[112:113], v3 offset:192\nds_read_b64 v[114:115], v3 offset:200\nds_read_b64 v[108:109], v3 offset:256\n" \
  "ds_read_b64 v[110:111], v3 offset:264\nds_read_b64 v[104:105], v3 offset:320\nds_read_b64 v[106:107], v3 offset:328\n" \
  "ds_read_b64 v[100:101], v3 offset:384\nds_read_b64 v[102:103], v3 offset:392\nds_read_b64 v[96:97], v3 offset:448\n" \
  "ds_read_b64 v[98:99], v3 offset:456\nds_read_b64 v[92:93], v3 offset:512\nds_read_b64 v[94:95], v3 offset:520\n" \
  "ds_read_b64 v[88:89], v3 offset:576\nds_read_b64 v[90:91], v3 offset:584\nds_read_b64 v[84:85], v3 offset:640\n" \
  "ds_read_b64 v[86:87], v3 offset:648\nds_read_b64 v[80:81], v3 offset:704\nds_read_b64 v[82:83], v3 offset:712\n" \
  "ds_read_b64 v[76:77], v3 offset:768\nds_read_b64 v[78:79], v3 offset:776\nds_read_b64 v[72:73], v3 offset:832\n" \
  "ds_read_b64 v[74:75], v3 offset:840\nds_read_b64 v[68:69], v3 offset:896\nds_read_b64 v[70:71], v3 offset:904\n" \
  "ds_read_b64 v[64:65], v3 offset:960\nds_read_b64 v[66:67], v3 offset:968\nds_read_b64 v[60:61], v3 offset:1024\n" \
  "ds_read_b64 v[62:63], v3 offset:1032\nds_read_b64 v[56:57], v6 offset:256\nds_read_b64 v[58:59], v6 offset:264\n" \
  "ds_read_b64 v[52:53], v6 offset:128\nds_read_b64 v[54:55], v6 offset:136\nds_read_b64 v[48:49], v6 offset:320\n" \
  "ds_read_b64 v[50:51], v6 offset:328\nds_read_b64 v[44:45], v6 offset:192\nds_read_b64 v[46:47], v6 offset:200\ns_waitcnt lgkmcnt(0)\ns_barrier\n" \
  "ds_read_b128 v[24:27], v2 offset:448\nds_read_b128 v[28:31], v2 offset:512\nds_read_b128 v[32:35], v2 offset:576\n" \
  "ds_read_b128 v[36:39], v2 offset:640\nds_read_b128 v[40:43], v2 offset:704\ns_waitcnt lgkmcnt(3)\n" \
  "v_mfma_f32_16x16x32_f16 v[12:15], v[124:127], v[24:27], 0\ns_setprio 1\nds_read_b128 v[24:27], v2 offset:768\n" \
  "v_mfma_f32_16x16x32_f16 v[12:15], v[120:123], v[28:31], v[12:15]\nds_read_b128 v[28:31], v2 offset:832\ns_waitcnt lgkmcnt(3)\n" \
  "v_mfma_f32_16x16x32_f16 v[12:15], v[116:119], v[32:35], v[12:15]\nds_read_b128 v[32:35], v2 offset:896\n" \
  "v_mfma_f32_16x16x32_f16 v[12:15], v[112:115], v[36:39], v[12:15]\nds_read_b128 v[36:39], v2 offset:960\ns_waitcnt lgkmcnt(3)\n" \
  "v_mfma_f32_16x16x32_f16 v[12:15], v[108:111], v[40:43], v[12:15]\nds_read_b128 v[40:43], v2 offset:1024\n" \
  "v_mfma_f32_16x16x32_f16 v[12:15], v[104:107], v[24:27], v[12:15]\nds_read_b128 v[24:27], v2 offset:1088\ns_waitcnt lgkmcnt(3)\n" \
  "v_mfma_f32_16x16x32_f16 v[12:15], v[100:103], v[28:31], v[12:15]\nds_read_b128 v[28:31], v2 offset:1152\n" \
  "v_mfma_f32_16x16x32_f16 v[12:15], v[96:99], v[32:35], v[12:15]\ns_waitcnt lgkmcnt(3)\n" \
  "v_mfma_f32_16x16x32_f16 v[12:15], v[92:95], v[36:39], v[12:15]\nds_read_b128 v[32:35], v2 offset:1216\n" \
  "v_mfma_f32_16x16x32_f16 v[16:19], v[124:127], v[36:39], 0\ns_waitcnt lgkmcnt(3)\nv_mfma_f32_16x16x32_f16 v[12:15], v[88:91], v[40:43], v[12:15]\n" \
  "ds_read_b128 v[124:127], v2 offset:1280\nv_mfma_f32_16x16x32_f16 v[16:19], v[120:123], v[40:43], v[16:19]\ns_waitcnt lgkmcnt(3)\n" \
  "v_mfma_f32_16x16x32_f16 v[12:15], v[84:87], v[24:27], v[12:15]\nds_read_b128 v[36:39], v2 offset:1344\n" \
  "v_mfma_f32_16x16x32_f16 v[16:19], v[116:119], v[24:27], v[16:19]\ns_waitcnt lgkmcnt(3)\n" \
  "v_mfma_f32_16x16x32_f16 v[12:15], v[80:83], v[28:31], v[12:15]\nds_read_b128 v[120:123], v2 offset:1408\n" \
  "v_mfma_f32_16x16x32_f16 v[16:19], v[112:115], v[28:31], v[16:19]\ns_waitcnt lgkmcnt(3)\n" \
  "v_mfma_f32_16x16x32_f16 v[12:15], v[76:79], v[32:35], v[12:15]\nds_read_b128 v[40:43], v2 offset:1472\n" \
  "v_mfma_f32_16x16x32_f16 v[16:19], v[108:111], v[32:35], v[16:19]\ns_waitcnt lgkmcnt(3)\n" \
  "v_mfma_f32_16x16x32_f16 v[12:15], v[72:75], v[124:127], v[12:15]\nds_read_b128 v[116:119], v2 offset:1536\n" \
  "v_mfma_f32_16x16x32_f16 v[16:19], v[104:107], v[124:127], v[16:19]\nds_read_b128 v[24:27], v2 offset:1600\ns_waitcnt lgkmcnt(3)\n" \
  "v_mfma_f32_16x16x32_f16 v[12:15], v[68:71], v[36:39], v[12:15]\nds_read_b128 v[112:115], v2 offset:1664\nds_read_b32 v23, v9 offset:32\n" \
  "v_mfma_f32_16x16x32_f16 v[16:19], v[100:103], v[36:39], v[16:19]\nds_read_b128 v[28:31], v2 offset:1728\n" \
  "v_mfma_f32_16x16x32_f16 v[12:15], v[64:67], v[120:123], v[12:15]\nds_read_b128 v[108:111], v2 offset:1792\n" \
  "v_mfma_f32_16x16x32_f16 v[16:19], v[96:99], v[120:123], v[16:19]\nds_read_b128 v[32:35], v2 offset:1856\ns_waitcnt lgkmcnt(6)\n" \
  "v_mfma_f32_16x16x32_f16 v[12:15], v[60:63], v[40:43], v[12:15]\nds_read_b128 v[104:107], v2 offset:1920\n" \
  "v_mfma_f32_16x16x32_f16 v[16:19], v[92:95], v[40:43], v[16:19]\nds_read_b128 v[124:127], v2 offset:1984\ns_waitcnt lgkmcnt(5)\n" \
  "v_readfirstlane_b32 s85, v23\ns_cmp_lt_u32 s85, 3\ns_cbranch_scc1 51f\n52:\nv_mfma_f32_16x16x32_f16 v[16:19], v[88:91], v[116:119], v[16:19]\n" \
  "ds_read_b64_tr_b16 v[100:101], v5 offset:0\nds_read_b64_tr_b16 v[102:103], v5 offset:32\n" \
  "v_mfma_f32_16x16x32_f16 v[16:19], v[84:87], v[24:27], v[16:19]\nds_read_b64_tr_b16 v[36:37], v5 offset:256\n" \
  "ds_read_b64_tr_b16 v[38:39], v5 offset:288\nv_mfma_f32_16x16x32_f16 v[16:19], v[80:83], v[112:115], v[16:19]\n" \
  "ds_read_b64_tr_b16 v[96:97], v5 offset:512\nds_read_b64_tr_b16 v[98:99], v5 offset:544\ns_waitcnt lgkmcnt(9)\n" \
  "v_mfma_f32_16x16x32_f16 v[16:19], v[76:79], v[28:31], v[16:19]\nds_read_b64_tr_b16 v[120:121], v5 offset:768\n" \
  "ds_read_b64_tr_b16 v[122:123], v5 offset:800\nv_mfma_f32_16x16x32_f16 v[16:19], v[72:75], v[108:111], v[16:19]\ns_waitcnt lgkmcnt(9)\n" \
  "v_mfma_f32_16x16x32_f16 v[16:19], v[68:71], v[32:35], v[16:19]\nv_mfma_f32_16x16x32_f16 v[16:19], v[64:67], v[104:107], v[16:19]\n" \
  "s_waitcnt lgkmcnt(6)\nv_mfma_f32_16x16x32_f16 v[16:19], v[60:63], v[124:127], v[16:19]\n" \
  "v_mfma_f32_16x16x32_f16 v[12:15], v[100:103], v[56:59], v[12:15]\nv_mfma_f32_16x16x32_f16 v[16:19], v[100:103], v[52:55], v[16:19]\n" \
  "s_waitcnt lgkmcnt(2)\nv_mfma_f32_16x16x32_f16 v[12:15], v[36:39], v[48:51], v[12:15]\n" \
  "v_mfma_f32_16x16x32_f16 v[16:19], v[36:39], v[44:47], v[16:19]\nv_mfma_f32_16x16x32_f16 v[16:19], v[96:99], v[56:59], v[16:19]\n" \
  "s_waitcnt lgkmcnt(0)\nv_mfma_f32_16x16x32_f16 v[16:19], v[120:123], v[48:51], v[16:19]\ns_nop 3\nv_cvt_pkrtz_f16_f32 v20, v12, v13\n" \
  "v_cvt_pkrtz_f16_f32 v21, v14, v15\nv_pk_add_u16 v20, v20, 4 op_sel_hi:[1,0] clamp\nv_pk_add_u16 v21, v21, 4 op_sel_hi:[1,0] clamp\n" \
  "ds_write_b64 v4, v[20:21] offset:2048\nv_cvt_pkrtz_f16_f32 v20, v16, v17\nv_cvt_pkrtz_f16_f32 v21, v18, v19\n" \
  "v_pk_add_u16 v20, v20, 4 op_sel_hi:[1,0] clamp\nv_pk_add_u16 v21, v21, 4 op_sel_hi:[1,0] clamp\nds_write_b64 v4, v[20:21] offset:3072\n" \
  "s_setprio 0\ns_setpc_b64 s[90:91]\n51:\ns_movk_i32 s84, 0x4000\n53:\nds_read_b32 v23, v9 offset:32\ns_sub_u32 s84, s84, 1\ns_cbranch_scc1 52b\n" \
  "s_waitcnt lgkmcnt(0)\nv_readfirstlane_b32 s85, v23\ns_cmp_lt_u32 s85, 3\ns_cbranch_scc1 53b\ns_branch 52b\n4:\n" \
  "ds_read_b64 v[124:125], v3 offset:1088\nds_read_b64 v[126:127], v3 offset:1096\nds_read_b64 v[120:121], v3 offset:0\n" \
  "ds_read_b64 v[122:123], v3 offset:8\nds_read_b64 v[116:117], v3 offset:64\nds_read_b64 v[118:119], v3 offset:72\n" \
  "ds_read_b64 v[112:113], v3 offset:128\nds_read_b64 v[114:115], v3 offset:136\nds_read_b64 v[108:109], v3 offset:192\n" \
  "ds_read_b64 v[110:111], v3 offset:200\nds_read_b64 v[104:105], v3 offset:256\nds_read_b64 v[106:107], v3 offset:264\n" \
  "ds_read_b64 v[100:101], v3 offset:320\nds_read_b64 v[102:103], v3 offset:328\nds_read_b64 v[96:97], v3 offset:384\n" \
  "ds_read_b64 v[98:99], v3 offset:392\nds_read_b64 v[92:93], v3 offset:448\nds_read_b64 v[94:95], v3 offset:456\n" \
  "ds_read_b64 v[88:89], v3 offset:512\nds_read_b64 v[90:91], v3 offset:520\nds_read_b64 v[84:85], v3 offset:576\n" \
  "ds_read_b64 v[86:87], v3 offset:584\nds_read_b64 v[80:81], v3 offset:640\nds_read_b64 v[82:83], v3 offset:648\n" \
  "ds_read_b64 v[76:77], v3 offset:704\nds_read_b64 v[78:79], v3 offset:712\nds_read_b64 v[72:73], v3 offset:768\n" \
  "ds_read_b64 v[74:75], v3 offset:776\nds_read_b64 v[68:69], v3 offset:832\nds_read_b64 v[70:71], v3 offset:840\n" \
  "ds_read_b64 v[64:65], v3 offset:896\nds_read_b64 v[66:67], v3 offset:904\nds_read_b64 v[60:61], v3 offset:960\n" \
  "ds_read_b64 v[62:63], v3 offset:968\nds_read_b64 v[56:57], v3 offset:1024\nds_read_b64 v[58:59], v3 offset:1032\n" \
  "ds_read_b64 v[52:53], v6 offset:0\nds_read_b64 v[54:55], v6 offset:8\nds_read_b64 v[48:49], v6 offset:64\nds_read_b64 v[50:51], v6 offset:72\n" \
  "ds_read_b64 v[44:45], v6 offset:128\nds_read_b64 v[46:47], v6 offset:136\nds_read_b64 v[40:41], v6 offset:192\n" \
  "ds_read_b64 v[42:43], v6 offset:200\ns_waitcnt lgkmcnt(0)\ns_barrier\nds_read_b128 v[24:27], v2 offset:448\nds_read_b128 v[28:31], v2 offset:960\n" \
  "ds_read_b128 v[32:35], v2 offset:1472\nds_read_b128 v[36:39], v2 offset:1536\ns_waitcnt lgkmcnt(3)\n" \
  "v_mfma_f32_16x16x32_f16 v[12:15], v[124:127], v[24:27], 0\ns_setprio 1\nds_read_b128 v[24:27], v2 offset:1600\ns_waitcnt lgkmcnt(3)\n" \
  "v_mfma_f32_16x16x32_f16 v[16:19], v[124:127], v[28:31], 0\nds_read_b128 v[124:127], v2 offset:1664\ns_waitcnt lgkmcnt(3)\n" \
  "v_mfma_f32_16x16x32_f16 v[12:15], v[120:123], v[32:35], v[12:15]\nds_read_b128 v[28:31], v2 offset:1728\ns_waitcnt lgkmcnt(3)\n" \
  "v_mfma_f32_16x16x32_f16 v[12:15], v[116:119], v[36:39], v[12:15]\nds_read_b128 v[32:35], v2 offset:1792\ns_waitcnt lgkmcnt(3)\n" \
  "v_mfma_f32_16x16x32_f16 v[12:15], v[112:115], v[24:27], v[12:15]\nds_read_b128 v[36:39], v2 offset:1856\ns_waitcnt lgkmcnt(3)\n" \
  "v_mfma_f32_16x16x32_f16 v[12:15], v[108:111], v[124:127], v[12:15]\nds_read_b128 v[24:27], v2 offset:1920\ns_waitcnt lgkmcnt(3)\n" \
  "v_mfma_f32_16x16x32_f16 v[12:15], v[104:107], v[28:31], v[12:15]\nds_read_b128 v[124:127], v2 offset:1984\ns_waitcnt lgkmcnt(3)\n" \
  "v_mfma_f32_16x16x32_f16 v[12:15], v[100:103], v[32:35], v[12:15]\nds_read_b128 v[28:31], v2 offset:2048\ns_waitcnt lgkmcnt(3)\n" \
  "v_mfma_f32_16x16x32_f16 v[12:15], v[96:99], v[36:39], v[12:15]\nds_read_b128 v[32:35], v2 offset:2112\ns_waitcnt lgkmcnt(3)\n" \
  "v_mfma_f32_16x16x32_f16 v[12:15], v[92:95], v[24:27], v[12:15]\nds_read_b128 v[36:39], v2 offset:2176\ns_waitcnt lgkmcnt(3)\n" \
  "v_mfma_f32_16x16x32_f16 v[12:15], v[88:91], v[124:127], v[12:15]\nds_read_b128 v[24:27], v2 offset:2240\n" \
  "v_mfma_f32_16x16x32_f16 v[16:19], v[120:123], v[124:127], v[16:19]\nds_read_b64 v[120:121], v6 offset:256\nds_read_b64 v[122:123], v6 offset:264\n" \
  "s_waitcnt lgkmcnt(4)\nv_mfma_f32_16x16x32_f16 v[12:15], v[84:87], v[28:31], v[12:15]\nds_read_b128 v[124:127], v2 offset:2304\n" \
  "v_mfma_f32_16x16x32_f16 v[16:19], v[116:119], v[28:31], v[16:19]\nds_read_b64 v[116:117], v6 offset:320\nds_read_b64 v[118:119], v6 offset:328\n" \
  "v_mfma_f32_16x16x32_f16 v[12:15], v[80:83], v[32:35], v[12:15]\nds_read_b128 v[28:31], v2 offset:2368\n" \
  "v_mfma_f32_16x16x32_f16 v[16:19], v[112:115], v[32:35], v[16:19]\ns_waitcnt lgkmcnt(6)\n" \
  "v_mfma_f32_16x16x32_f16 v[12:15], v[76:79], v[36:39], v[12:15]\nds_read_b128 v[112:115], v2 offset:2432\n" \
  "v_mfma_f32_16x16x32_f16 v[16:19], v[108:111], v[36:39], v[16:19]\nds_read2_b32 v[32:33], v8 offset0:48 offset1:49\n" \
  "ds_read2_b32 v[34:35], v8 offset0:50 offset1:51\nv_mfma_f32_16x16x32_f16 v[12:15], v[72:75], v[24:27], v[12:15]\n" \
  "ds_read_b128 v[108:111], v2 offset:2496\nv_mfma_f32_16x16x32_f16 v[16:19], v[104:107], v[24:27], v[16:19]\n" \
  "ds_read2_b32 v[36:37], v8 offset0:32 offset1:33\nds_read2_b32 v[38:39], v8 offset0:34 offset1:35\ns_waitcnt lgkmcnt(6)\n" \
  "v_mfma_f32_16x16x32_f16 v[12:15], v[68:71], v[124:127], v[12:15]\nds_read_b128 v[104:107], v2 offset:2560\n" \
  "v_mfma_f32_16x16x32_f16 v[16:19], v[100:103], v[124:127], v[16:19]\nds_read_b128 v[24:27], v2 offset:2624\n" \
  "v_mfma_f32_16x16x32_f16 v[12:15], v[64:67], v[28:31], v[12:15]\nds_read_b128 v[100:103], v2 offset:2688\nds_read_b32 v23, v9 offset:32\n" \
  "v_mfma_f32_16x16x32_f16 v[16:19], v[96:99], v[28:31], v[16:19]\nds_read_b128 v[124:127], v2 offset:2752\ns_waitcnt lgkmcnt(7)\n" \
  "v_mfma_f32_16x16x32_f16 v[12:15], v[60:63], v[112:115], v[12:15]\nds_read_b128 v[96:99], v2 offset:2816\n" \
  "v_mfma_f32_16x16x32_f16 v[16:19], v[92:95], v[112:115], v[16:19]\nds_read_b128 v[28:31], v2 offset:2880\n" \
  "v_mfma_f32_16x16x32_f16 v[12:15], v[56:59], v[108:111], v[12:15]\nds_read_b128 v[92:95], v2 offset:2944\n" \
  "v_mfma_f32_16x16x32_f16 v[16:19], v[88:91], v[108:111], v[16:19]\nds_read_b128 v[112:115], v2 offset:3008\ns_waitcnt lgkmcnt(5)\n" \
  "v_readfirstlane_b32 s85, v23\ns_cmp_lt_u32 s85, 3\ns_cbranch_scc1 61f\n62:\nv_mfma_f32_16x16x32_f16 v[16:19], v[84:87], v[104:107], v[16:19]\n" \
  "ds_read_b64_tr_b16 v[88:89], v5 offset:0\nds_read_b64_tr_b16 v[90:91], v5 offset:32\n" \
  "v_mfma_f32_16x16x32_f16 v[16:19], v[80:83], v[24:27], v[16:19]\nds_read_b64_tr_b16 v[108:109], v5 offset:256\n" \
  "ds_read_b64_tr_b16 v[110:111], v5 offset:288\nv_mfma_f32_16x16x32_f16 v[16:19], v[76:79], v[100:103], v[16:19]\n" \
  "ds_read_b64_tr_b16 v[84:85], v5 offset:512\nds_read_b64_tr_b16 v[86:87], v5 offset:544\ns_waitcnt lgkmcnt(9)\n" \
  "v_mfma_f32_16x16x32_f16 v[16:19], v[72:75], v[124:127], v[16:19]\nds_read_b64_tr_b16 v[104:105], v5 offset:768\n" \
  "ds_read_b64_tr_b16 v[106:107], v5 offset:800\nv_mfma_f32_16x16x32_f16 v[16:19], v[68:71], v[96:99], v[16:19]\n" \
  "ds_read_b64_tr_b16 v[80:81], v5 offset:1024\nds_read_b64_tr_b16 v[82:83], v5 offset:1056\ns_waitcnt lgkmcnt(11)\n" \
  "v_mfma_f32_16x16x32_f16 v[16:19], v[64:67], v[28:31], v[16:19]\nds_read_b64_tr_b16 v[24:25], v5 offset:1280\n" \
  "ds_read_b64_tr_b16 v[26:27], v5 offset:1312\nv_mfma_f32_16x16x32_f16 v[16:19], v[60:63], v[92:95], v[16:19]\n" \
  "ds_read_b64_tr_b16 v[76:77], v5 offset:1536\nds_read_b64_tr_b16 v[78:79], v5 offset:1568\ns_waitcnt lgkmcnt(12)\n" \
  "v_mfma_f32_16x16x32_f16 v[16:19], v[56:59], v[112:115], v[16:19]\nds_read_b64_tr_b16 v[100:101], v5 offset:1792\n" \
  "ds_read_b64_tr_b16 v[102:103], v5 offset:1824\nv_mfma_f32_16x16x32_f16 v[12:15], v[88:91], v[52:55], v[12:15]\ns_waitcnt lgkmcnt(10)\n" \
  "v_mfma_f32_16x16x32_f16 v[12:15], v[108:111], v[48:51], v[12:15]\nds_read_b64_tr_b16 v[72:73], v7 offset:0\n" \
  "ds_read_b64_tr_b16 v[74:75], v7 offset:64\nv_mfma_f32_16x16x32_f16 v[12:15], v[84:87], v[44:47], v[12:15]\n" \
  "ds_read_b64_tr_b16 v[124:125], v7 offset:512\nds_read_b64_tr_b16 v[126:127], v7 offset:576\n" \
  "v_mfma_f32_16x16x32_f16 v[16:19], v[84:87], v[52:55], v[16:19]\ns_waitcnt lgkmcnt(10)\n" \
  "v_mfma_f32_16x16x32_f16 v[12:15], v[104:107], v[40:43], v[12:15]\nv_mfma_f32_16x16x32_f16 v[16:19], v[104:107], v[48:51], v[16:19]\n" \
  "v_mfma_f32_16x16x32_f16 v[12:15], v[80:83], v[120:123], v[12:15]\nv_mfma_f32_16x16x32_f16 v[16:19], v[80:83], v[44:47], v[16:19]\n" \
  "s_waitcnt lgkmcnt(6)\nv_mfma_f32_16x16x32_f16 v[12:15], v[24:27], v[116:119], v[12:15]\n" \
  "v_mfma_f32_16x16x32_f16 v[16:19], v[24:27], v[40:43], v[16:19]\nv_mfma_f32_16x16x32_f16 v[16:19], v[76:79], v[120:123], v[16:19]\n" \
  "s_waitcnt lgkmcnt(2)\nv_mfma_f32_16x16x32_f16 v[16:19], v[100:103], v[116:119], v[16:19]\n" \
  "v_mfma_f32_16x16x32_f16 v[12:15], v[72:75], v[32:35], v[12:15]\nv_mfma_f32_16x16x32_f16 v[16:19], v[72:75], v[36:39], v[16:19]\n" \
  "s_waitcnt lgkmcnt(0)\nv_mfma_f32_16x16x32_f16 v[16:19], v[124:127], v[32:35], v[16:19]\ns_nop 4\nv_cvt_pkrtz_f16_f32 v20, v12, v13\n" \
  "v_cvt_pkrtz_f16_f32 v21, v14, v15\nv_pk_add_u16 v20, v20, 4 op_sel_hi:[1,0] clamp\nv_pk_add_u16 v21, v21, 4 op_sel_hi:[1,0] clamp\n" \
  "ds_write_b64 v4, v[20:21] offset:4096\nv_cvt_pkrtz_f16_f32 v20, v16, v17\nv_cvt_pkrtz_f16_f32 v21, v18, v19\n" \
  "v_pk_add_u16 v20, v20, 4 op_sel_hi:[1,0] clamp\nv_pk_add_u16 v21, v21, 4 op_sel_hi:[1,0] clamp\nds_write_b64 v4, v[20:21] offset:5120\n" \
  "s_setprio 0\ns_setpc_b64 s[90:91]\n61:\ns_movk_i32 s84, 0x4000\n63:\nds_read_b32 v23, v9 offset:32\ns_sub_u32 s84, s84, 1\ns_cbranch_scc1 62b\n" \
  "s_waitcnt lgkmcnt(0)\nv_readfirstlane_b32 s85, v23\ns_cmp_lt_u32 s85, 3\ns_cbranch_scc1 63b\ns_branch 62b\n5:\n" \
  "ds_read_b64 v[124:125], v3 offset:1088\nds_read_b64 v[126:127], v3 offset:1096\nds_read_b64 v[120:121], v3 offset:0\n" \
  "ds_read_b64 v[122:123], v3 offset:8\nds_read_b64 v[116:117], v3 offset:64\nds_read_b64 v[118:119], v3 offset:72\n" \
  "ds_read_b64 v[112:113], v3 offset:128\nds_read_b64 v[114:115], v3 offset:136\nds_read_b64 v[108:109], v3 offset:192\n" \
  "ds_read_b64 v[110:111], v3 offset:200\nds_read_b64 v[104:105], v3 offset:256\nds_read_b64 v[106:107], v3 offset:264\n" \
  "ds_read_b64 v[100:101], v3 offset:320\nds_read_b64 v[102:103], v3 offset:328\nds_read_b64 v[96:97], v3 offset:384\n" \
  "ds_read_b64 v[98:99], v3 offset:392\nds_read_b64 v[92:93], v3 offset:448\nds_read_b64 v[94:95], v3 offset:456\n" \
  "ds_read_b64 v[88:89], v3 offset:512\nds_read_b64 v[90:91], v3 offset:520\nds_read_b64 v[84:85], v3 offset:576\n" \
  "ds_read_b64 v[86:87], v3 offset:584\nds_read_b64 v[80:81], v3 offset:640\nds_read_b64 v[82:83], v3 offset:648\n" \
  "ds_read_b64 v[76:77], v3 offset:704\nds_read_b64 v[78:79], v3 offset:712\nds_read_b64 v[72:73], v3 offset:768\n" \
  "ds_read_b64 v[74:75], v3 offset:776\nds_read_b64 v[68:69], v3 offset:832\nds_read_b64 v[70:71], v3 offset:840\n" \
  "ds_read_b64 v[64:65], v3 offset:896\nds_read_b64 v[66:67], v3 offset:904\nds_read_b64 v[60:61], v3 offset:960\n" \
  "ds_read_b64 v[62:63], v3 offset:968\nds_read_b64 v[56:57], v3 offset:1024\nds_read_b64 v[58:59], v3 offset:1032\n" \
  "ds_read_b64 v[52:53], v6 offset:0\nds_read_b64 v[54:55], v6 offset:8\nds_read_b64 v[48:49], v6 offset:64\nds_read_b64 v[50:51], v6 offset:72\n" \
  "ds_read_b64 v[44:45], v6 offset:128\nds_read_b64 v[46:47], v6 offset:136\nds_read_b64 v[40:41], v6 offset:192\n" \
  "ds_read_b64 v[42:43], v6 offset:200\ns_waitcnt lgkmcnt(0)\ns_barrier\nds_read_b128 v[24:27], v2 offset:1472\n" \
  "ds_read_b128 v[28:31], v2 offset:1984\nds_read_b128 v[32:35], v2 offset:2496\nds_read_b128 v[36:39], v2 offset:2560\ns_waitcnt lgkmcnt(3)\n" \
  "v_mfma_f32_16x16x32_f16 v[12:15], v[124:127], v[24:27], 0\ns_setprio 1\nds_read_b128 v[24:27], v2 offset:2624\ns_waitcnt lgkmcnt(3)\n" \
  "v_mfma_f32_16x16x32_f16 v[16:19], v[124:127], v[28:31], 0\nds_read_b128 v[124:127], v2 offset:2688\ns_waitcnt lgkmcnt(3)\n" \
  "v_mfma_f32_16x16x32_f16 v[12:15], v[120:123], v[32:35], v[12:15]\nds_read_b128 v[28:31], v2 offset:2752\ns_waitcnt lgkmcnt(3)\n" \
  "v_mfma_f32_16x16x32_f16 v[12:15], v[116:119], v[36:39], v[12:15]\nds_read_b128 v[32:35], v2 offset:2816\ns_waitcnt lgkmcnt(3)\n" \
  "v_mfma_f32_16x16x32_f16 v[12:15], v[112:115], v[24:27], v[12:15]\nds_read_b128 v[36:39], v2 offset:2880\ns_waitcnt lgkmcnt(3)\n" \
  "v_mfma_f32_16x16x32_f16 v[12:15], v[108:111], v[124:127], v[12:15]\nds_read_b128 v[24:27], v2 offset:2944\ns_waitcnt lgkmcnt(3)\n" \
  "v_mfma_f32_16x16x32_f16 v[12:15], v[104:107], v[28:31], v[12:15]\nds_read_b128 v[124:127], v2 offset:3008\ns_waitcnt lgkmcnt(3)\n" \
  "v_mfma_f32_16x16x32_f16 v[12:15], v[100:103], v[32:35], v[12:15]\nds_read_b128 v[28:31], v2 offset:3072\ns_waitcnt lgkmcnt(3)\n" \
  "v_mfma_f32_16x16x32_f16 v[12:15], v[96:99], v[36:39], v[12:15]\nds_read_b128 v[32:35], v2 offset:3136\ns_waitcnt lgkmcnt(3)\n" \
  "v_mfma_f32_16x16x32_f16 v[12:15], v[92:95], v[24:27], v[12:15]\nds_read_b128 v[36:39], v2 offset:3200\ns_waitcnt lgkmcnt(3)\n" \
  "v_mfma_f32_16x16x32_f16 v[12:15], v[88:91], v[124:127], v[12:15]\nds_read_b128 v[24:27], v2 offset:3264\n" \
  "v_mfma_f32_16x16x32_f16 v[16:19], v[120:123], v[124:127], v[16:19]\nds_read_b64 v[120:121], v6 offset:256\nds_read_b64 v[122:123], v6 offset:264\n" \
  "s_waitcnt lgkmcnt(4)\nv_mfma_f32_16x16x32_f16 v[12:15], v[84:87], v[28:31], v[12:15]\nds_read_b128 v[124:127], v2 offset:3328\n" \
  "v_mfma_f32_16x16x32_f16 v[16:19], v[116:119], v[28:31], v[16:19]\nds_read_b64 v[116:117], v6 offset:320\nds_read_b64 v[118:119], v6 offset:328\n" \
  "v_mfma_f32_16x16x32_f16 v[12:15], v[80:83], v[32:35], v[12:15]\nds_read_b128 v[28:31], v2 offset:3392\n" \
  "v_mfma_f32_16x16x32_f16 v[16:19], v[112:115], v[32:35], v[16:19]\ns_waitcnt lgkmcnt(6)\n" \
  "v_mfma_f32_16x16x32_f16 v[12:15], v[76:79], v[36:39], v[12:15]\nds_read_b128 v[112:115], v2 offset:3456\n" \
  "v_mfma_f32_16x16x32_f16 v[16:19], v[108:111], v[36:39], v[16:19]\nds_read2_b32 v[32:33], v8 offset0:16 offset1:17\n" \
  "ds_read2_b32 v[34:35], v8 offset0:18 offset1:19\nv_mfma_f32_16x16x32_f16 v[12:15], v[72:75], v[24:27], v[12:15]\n" \
  "ds_read_b128 v[108:111], v2 offset:3520\nv_mfma_f32_16x16x32_f16 v[16:19], v[104:107], v[24:27], v[16:19]\n" \
  "ds_read2_b32 v[36:37], v8 offset0:0 offset1:1\nds_read2_b32 v[38:39], v8 offset0:2 offset1:3\ns_waitcnt lgkmcnt(6)\n" \
  "v_mfma_f32_16x16x32_f16 v[12:15], v[68:71], v[124:127], v[12:15]\nds_read_b128 v[104:107], v2 offset:3584\n" \
  "v_mfma_f32_16x16x32_f16 v[16:19], v[100:103], v[124:127], v[16:19]\nds_read_b128 v[24:27], v2 offset:3648\n" \
  "ds_read2_b32 v[100:101], v8 offset0:32 offset1:33\nds_read2_b32 v[102:103], v8 offset0:34 offset1:35\n" \
  "v_mfma_f32_16x16x32_f16 v[12:15], v[64:67], v[28:31], v[12:15]\nds_read_b128 v[124:127], v2 offset:3712\nds_read_b32 v23, v9 offset:32\n" \
  "v_mfma_f32_16x16x32_f16 v[16:19], v[96:99], v[28:31], v[16:19]\nds_read_b128 v[96:99], v2 offset:3776\ns_waitcnt lgkmcnt(9)\n" \
  "v_mfma_f32_16x16x32_f16 v[12:15], v[60:63], v[112:115], v[12:15]\nds_read_b128 v[28:31], v2 offset:3840\n" \
  "v_mfma_f32_16x16x32_f16 v[16:19], v[92:95], v[112:115], v[16:19]\nds_read_b128 v[92:95], v2 offset:3904\n" \
  "v_mfma_f32_16x16x32_f16 v[12:15], v[56:59], v[108:111], v[12:15]\nds_read_b128 v[112:115], v2 offset:3968\n" \
  "v_mfma_f32_16x16x32_f16 v[16:19], v[88:91], v[108:111], v[16:19]\nds_read_b128 v[88:91], v2 offset:4032\ns_waitcnt lgkmcnt(5)\n" \
  "v_readfirstlane_b32 s85, v23\ns_cmp_lt_u32 s85, 3\ns_cbranch_scc1 71f\n72:\nv_mfma_f32_16x16x32_f16 v[16:19], v[84:87], v[104:107], v[16:19]\n" \
  "ds_read_b64_tr_b16 v[108:109], v5 offset:1024\nds_read_b64_tr_b16 v[110:111], v5 offset:1056\n" \
  "v_mfma_f32_16x16x32_f16 v[16:19], v[80:83], v[24:27], v[16:19]\nds_read_b64_tr_b16 v[84:85], v5 offset:1280\n" \
  "ds_read_b64_tr_b16 v[86:87], v5 offset:1312\nv_mfma_f32_16x16x32_f16 v[16:19], v[76:79], v[124:127], v[16:19]\n" \
  "ds_read_b64_tr_b16 v[104:105], v5 offset:1536\nds_read_b64_tr_b16 v[106:107], v5 offset:1568\ns_waitcnt lgkmcnt(9)\n" \
  "v_mfma_f32_16x16x32_f16 v[16:19], v[72:75], v[96:99], v[16:19]\nds_read_b64_tr_b16 v[80:81], v5 offset:1792\n" \
  "ds_read_b64_tr_b16 v[82:83], v5 offset:1824\nv_mfma_f32_16x16x32_f16 v[16:19], v[68:71], v[28:31], v[16:19]\n" \
  "ds_read_b64_tr_b16 v[24:25], v5 offset:2048\nds_read_b64_tr_b16 v[26:27], v5 offset:2080\ns_waitcnt lgkmcnt(11)\n" \
  "v_mfma_f32_16x16x32_f16 v[16:19], v[64:67], v[92:95], v[16:19]\nds_read_b64_tr_b16 v[76:77], v5 offset:2304\n" \
  "ds_read_b64_tr_b16 v[78:79], v5 offset:2336\nv_mfma_f32_16x16x32_f16 v[16:19], v[60:63], v[112:115], v[16:19]\n" \
  "ds_read_b64_tr_b16 v[124:125], v5 offset:2560\nds_read_b64_tr_b16 v[126:127], v5 offset:2592\ns_waitcnt lgkmcnt(12)\n" \
  "v_mfma_f32_16x16x32_f16 v[16:19], v[56:59], v[88:91], v[16:19]\nds_read_b64_tr_b16 v[72:73], v5 offset:2816\n" \
  "ds_read_b64_tr_b16 v[74:75], v5 offset:2848\nv_mfma_f32_16x16x32_f16 v[12:15], v[108:111], v[52:55], v[12:15]\ns_waitcnt lgkmcnt(10)\n" \
  "v_mfma_f32_16x16x32_f16 v[12:15], v[84:87], v[48:51], v[12:15]\nds_read_b64_tr_b16 v[96:97], v7 offset:0\n" \
  "ds_read_b64_tr_b16 v[98:99], v7 offset:64\nv_mfma_f32_16x16x32_f16 v[12:15], v[104:107], v[44:47], v[12:15]\n" \
  "ds_read_b64_tr_b16 v[68:69], v7 offset:512\nds_read_b64_tr_b16 v[70:71], v7 offset:576\n" \
  "v_mfma_f32_16x16x32_f16 v[16:19], v[104:107], v[52:55], v[16:19]\ns_waitcnt lgkmcnt(10)\n" \
  "v_mfma_f32_16x16x32_f16 v[12:15], v[80:83], v[40:43], v[12:15]\nds_read_b64_tr_b16 v[28:29], v7 offset:1024\n" \
  "ds_read_b64_tr_b16 v[30:31], v7 offset:1088\nv_mfma_f32_16x16x32_f16 v[16:19], v[80:83], v[48:51], v[16:19]\n" \
  "ds_read2_b32 v[64:65], v8 offset0:48 offset1:49\nds_read2_b32 v[66:67], v8 offset0:50 offset1:51\n" \
  "v_mfma_f32_16x16x32_f16 v[12:15], v[24:27], v[120:123], v[12:15]\nv_mfma_f32_16x16x32_f16 v[16:19], v[24:27], v[44:47], v[16:19]\n" \
  "s_waitcnt lgkmcnt(10)\nv_mfma_f32_16x16x32_f16 v[12:15], v[76:79], v[116:119], v[12:15]\nds_read_b64_tr_b16 v[92:93], v7 offset:1536\n" \
  "ds_read_b64_tr_b16 v[94:95], v7 offset:1600\nv_mfma_f32_16x16x32_f16 v[16:19], v[76:79], v[40:43], v[16:19]\n" \
  "v_mfma_f32_16x16x32_f16 v[16:19], v[124:127], v[120:123], v[16:19]\ns_waitcnt lgkmcnt(8)\n" \
  "v_mfma_f32_16x16x32_f16 v[16:19], v[72:75], v[116:119], v[16:19]\nv_mfma_f32_16x16x32_f16 v[12:15], v[96:99], v[32:35], v[12:15]\n" \
  "v_mfma_f32_16x16x32_f16 v[16:19], v[96:99], v[36:39], v[16:19]\ns_waitcnt lgkmcnt(4)\n" \
  "v_mfma_f32_16x16x32_f16 v[12:15], v[68:71], v[100:103], v[12:15]\nv_mfma_f32_16x16x32_f16 v[16:19], v[68:71], v[32:35], v[16:19]\n" \
  "s_waitcnt lgkmcnt(2)\nv_mfma_f32_16x16x32_f16 v[12:15], v[28:31], v[64:67], v[12:15]\n" \
  "v_mfma_f32_16x16x32_f16 v[16:19], v[28:31], v[100:103], v[16:19]\ns_waitcnt lgkmcnt(0)\n" \
  "v_mfma_f32_16x16x32_f16 v[16:19], v[92:95], v[64:67], v[16:19]\ns_nop 4\nv_cvt_pkrtz_f16_f32 v20, v12, v13\nv_cvt_pkrtz_f16_f32 v21, v14, v15\n" \
  "v_pk_add_u16 v20, v20, 4 op_sel_hi:[1,0] clamp\nv_pk_add_u16 v21, v21, 4 op_sel_hi:[1,0] clamp\nds_write_b64 v4, v[20:21] offset:6144\n" \
  "v_cvt_pkrtz_f16_f32 v20, v16, v17\nv_cvt_pkrtz_f16_f32 v21, v18, v19\nv_pk_add_u16 v20, v20, 4 op_sel_hi:[1,0] clamp\n" \
  "v_pk_add_u16 v21, v21, 4 op_sel_hi:[1,0] clamp\nds_write_b64 v4, v[20:21] offset:7168\ns_setprio 0\ns_setpc_b64 s[90:91]\n71:\n" \
  "s_movk_i32 s84, 0x4000\n73:\nds_read_b32 v23, v9 offset:32\ns_sub_u32 s84, s84, 1\ns_cbranch_scc1 72b\ns_waitcnt lgkmcnt(0)\n" \
  "v_readfirstlane_b32 s85, v23\ns_cmp_lt_u32 s85, 3\ns_cbranch_scc1 73b\ns_branch 72b\n"
#define CONV_CLOB "memory", "scc", "s84", "s85", "s90", "s91", "v12", "v13", "v14", "v15", "v16", "v17", "v18", "v19", "v20", "v21", "v22", "v23", "v24", "v25", "v26", "v27", "v28", "v29", "v30", "v31", "v32", "v33", "v34", "v35", "v36", "v37", "v38", "v39", "v40", "v41", "v42", "v43", "v44", "v45", "v46", "v47", "v48", "v49", "v50", "v51", "v52", "v53", "v54", "v55", "v56", "v57", "v58", "v59", "v60", "v61", "v62", "v63", "v64", "v65", "v66", "v67", "v68", "v69", "v70", "v71", "v72", "v73", "v74", "v75", "v76", "v77", "v78", "v79", "v80", "v81", "v82", "v83", "v84", "v85", "v86", "v87", "v88", "v89", "v90", "v91", "v92", "v93", "v94", "v95", "v96", "v97", "v98", "v99", "v100", "v101", "v102", "v103", "v104", "v105", "v106", "v107", "v108", "v109", "v110", "v111", "v112", "v113", "v114", "v115", "v116", "v117", "v118", "v119", "v120", "v121", "v122", "v123", "v124", "v125", "v126", "v127"
constexpr int TDP = 1216, T_3 = 4 * TDP, T_4 = T_3 + 512, TB = T_4 + 320;
constexpr int C_U = 480, UB = C_U + 4096 + 96, C_G5 = 0, C_G3 = 3200, C_G9 = 6400, C_G7 = C_G9 + 2096, GB = C_G7 + 2080 + 112;
constexpr int O_FL = 2 * TB, O_UL = O_FL + 64, O_IM = O_UL + 2 * UB, O_ST = O_IM + 2 * GB;
constexpr int X_TB = 10240, X_UL = X_TB + TB, X_IM = X_UL + UB, X_ST = X_IM + GB;
constexpr int X_WR = X_IM + 128 * IP;
#define X_RW(w) (LW + 9216 * (2 + ((w) >> 1)) + 2560 * ((w) & 1))
constexpr int RP = 1216;
constexpr int LW = 36864 + 512, LA = LW, LM = LW + 36864;
constexpr int L_SC = LM + 2048, SMEM = O_IM + 256 * IP;
static_assert(L_SC + 1152 <= SMEM && O_ST + 8192 <= SMEM && X_IM + 128 * IP <= LM && X_ST + 8192 <= LM && UB % 128 == 64 && GB % 128 == 64 && SMEM <= 79872, "");
struct TOps { uint32_t xa, xb, ya, pd, pf, pg, pe; };
__device__ __forceinline__ void table_ops(uint32_t im, uint32_t tb, int ct, int lane, TOps& t) {
  const int j = lane & 15, kq = lane >> 4, q4 = (lane >> 2) & 3, p4 = lane & 3, jj = j + 16 * ct;
  const uint32_t sa = im + (uint32_t)(IP * (lane >> 2));
  t.xa = sa + (uint32_t)(I_X + 8 * p4); t.xb = sa + (uint32_t)(I_X + 32 + 8 * (p4 & 1)); t.ya = sa + (uint32_t)(32 * ct + 8 * p4);
  t.pd = tb + (uint32_t)(2 * (541 - (8 * jj + 4 * (kq & 1) + 256 * (kq >> 1))));
  t.pf = tb + (uint32_t)((kq & 1) ? T_4 + 2 * (63 - jj) : T_3 + 2 * (127 - 2 * jj));
  t.pg = t.pf - ((kq & 1) ? 64 : 2);
  t.pe = tb + (uint32_t)(p4 * (TDP + 2) + ((lane < 12) ? 2 * (32 - q4) : 2 * (572 + (q4 & 1))));
  asm volatile("" : "+v"(t.xa), "+v"(t.xb), "+v"(t.ya), "+v"(t.pd), "+v"(t.pf), "+v"(t.pg), "+v"(t.pe));
}
typedef __attribute__((address_space(3))) char* lptr;
#define LP(T, a) ((__attribute__((address_space(3))) T*)(uintptr_t)(a))
__device__ __forceinline__ uint32_t table_tile(const TOps& t, int ct, int lane, uint32_t evp, float dsk TSP) {
  u32x4 ev;
  f32x4 ad[2], af[2];
  asm volatile(T_ASM : "=&v"(ad[0]), "=&v"(ad[1]), "=&v"(af[0]), "=&v"(af[1]), "=&v"(ev) : "v"(t.xa), "v"(t.xb), "v"(t.ya), "v"(evp) : T_CLOB);
  const int e0 = (int)(ev[0] + ev[2]), e1 = (int)(ev[1] + ev[3]);
  const int em = min(max(max(max(e0, e1), (int)((__float_as_uint(dsk) >> 23) & 0xffu) - 133), -110), 115);
  const int ts = ((uint32_t)(em + 12) <= 8u) ? 1 : -3 - em;
  float s0 = __builtin_amdgcn_ldexpf(1.f, e0 - 29 + ts), s1 = __builtin_amdgcn_ldexpf(1.f, e1 - 29 + ts);
  float d0 = (ct == 0 && lane == 0) ? __builtin_amdgcn_ldexpf(dsk, ts) : 0.f;
  asm volatile("s_nop 1" : "+v"(ad[0]), "+v"(ad[1]), "+v"(af[0]), "+v"(af[1]), "+v"(s0), "+v"(s1), "+v"(d0));
  const f32x4 k = ad[0] * s0 + (f32x4){d0, 0.f, 0.f, 0.f} + ad[1] * s1, f = af[0] * s0 + af[1] * s1;
  auto pk = [](float a, float b) { uint32_t r; asm("v_cvt_pk_f16_f32 %0, %1, %2" : "=v"(r) : "v"(a), "v"(b)); return r; };
  asm volatile("s_nop 0" : : "v"(k), "v"(f));
  const uint32_t p32 = pk(k[3], k[2]), p10 = pk(k[1], k[0]), p21 = pk(k[2], k[1]), p01 = pk(f[0], f[1]), p23 = pk(f[2], f[3]);
  *LP(u32x2, t.pd + 3 * TDP + 6) = (u32x2){p32, p10};
  *LP(uint32_t, t.pd + TDP + 2) = p32; *LP(uint32_t, t.pd + TDP + 6) = p10;
  *LP(uint32_t, t.pd + 2) = p21; *LP(uint32_t, t.pd + 2 * TDP + 6) = p21;
  *LP(u16, t.pd) = (u16)p32; *LP(u16, t.pd + 2 * TDP + 4) = (u16)p32;
  asm volatile("ds_write_b16_d16_hi %0, %1 offset:6\n\tds_write_b16_d16_hi %0, %1 offset:%2" : : "v"(t.pd), "v"(p10), "n"(2 * TDP + 10) : "memory");
  if (ct) {
    const uint32_t va = (uint32_t)__builtin_amdgcn_readlane((int)p32, 63), vb = (uint32_t)__builtin_amdgcn_readlane((int)p23, 15) >> 16;
    *LP(u16, t.pe) = (u16)((lane < 12) ? va : vb);
  }
  if (lane < 32) {
    *LP(u16, t.pf + 128) = (u16)p01; *LP(u16, t.pg + 128) = (u16)(p01 >> 16);
    *LP(u16, t.pf) = (u16)p23; *LP(u16, t.pg) = (u16)(p23 >> 16);
  }
  return (uint32_t)(127 + 5 - ts) << 23;
}
__device__ __forceinline__ uint32_t hadd(uint32_t a, uint32_t b) { return __builtin_bit_cast(uint32_t, __builtin_bit_cast(f16x2, a) + __builtin_bit_cast(f16x2, b)); }
template <bool FULL> __device__ __forceinline__ void sums(char* sm, const u32x4 p, const u32x4 c) {
  const uint32_t D[8] = {p[0], p[1], p[2], p[3], c[0], c[1], c[2], c[3]};
  uint32_t O[7];
#pragma unroll
  for (int j = 0; j < 7; ++j) O[j] = __builtin_amdgcn_alignbit(D[j + 1], D[j], 16);
  u32x4 a, b;
#pragma unroll
  for (int j = 0; j < 4; ++j) a[j] = hadd(D[j + 2], hadd(O[j + 1], O[j + 2]));
  *(u32x4*)(sm + C_G3 + 32) = a;
#pragma unroll
  for (int j = 0; j < 4; ++j) b[j] = hadd(a[j], hadd(D[j + 1], D[j + 3]));
  *(u32x4*)(sm + C_G5 + 32) = b;
  if (FULL) {
#pragma unroll
    for (int j = 0; j < 4; ++j) a[j] = hadd(b[j], hadd(O[j], O[j + 3]));
    *(u32x4*)(sm + C_G7 + 32) = a;
#pragma unroll
    for (int j = 0; j < 4; ++j) b[j] = hadd(a[j], hadd(D[j], D[j + 4]));
    *(u32x4*)(sm + C_G9 + 32) = b;
  }
}
__device__ __forceinline__ void count_up(uint32_t a) {
  uint32_t t;
  asm volatile("s_mov_b64 exec, 1\n\tv_mov_b32 %0, 1\n\tds_add_u32 %1, %0\n\ts_mov_b64 exec, -1" : "=&v"(t) : "v"(a) : "memory");
}
__device__ __forceinline__ int pad_at(int r) { return 16 * (r & 1) + 3200 * (r >> 1) - ((r >= 6) ? 3200 - 2096 : 0); }
struct COps { uint32_t b, a, o, g3, k3, g4, k4, f; };
__device__ __forceinline__ void conv_ops(uint32_t tb, uint32_t ub, uint32_t cb, int lane, COps& e) {
  const uint32_t j = lane & 15, kq = lane >> 4, q4 = (lane >> 2) & 3, p4 = lane & 3;
  e.b = ub + 32u * j + 16u * kq;
  e.a = tb + p4 * TDP + 32u - 8u * q4 + 16u * kq;
  e.g3 = cb + C_G5 + 8 + 64u * kq + 8u * (q4 + p4) + (q4 & 1u) * 3200u;
  e.g4 = cb + C_G9 + 8 + 128u * kq + 16u * q4 + 8u * p4 + (q4 & 1u) * 2096u;
  e.k3 = tb + T_3 + 120 - 8u * j + 16u * kq;
  e.k4 = tb + T_4 + 60 - 4u * j + 16u * kq;
  asm volatile("" : "+v"(e.b), "+v"(e.a), "+v"(e.g3), "+v"(e.g4), "+v"(e.k3), "+v"(e.k4));
}
#define CONV_RUN(prog, e, ...) do { int prog_ = (prog); \
    asm volatile("s_call_b64 s[90:91], kf_conv" : "+{s89}"(prog_), "+{v2}"(e.b), "+{v3}"(e.a), "+{v4}"(e.o), "+{v5}"(e.g3), "+{v6}"(e.k3), "+{v7}"(e.g4), \
                 "+{v8}"(e.k4), "+{v9}"(e.f) __VA_ARGS__ : : CONV_CLOB); } while (0)

template <bool E>
__device__ __forceinline__ void ln_stats(const uint32_t* v, int lane, float sc, f32x2* t, f32x4 z, f32x2& rc, f32x2& nc, float& e) {
  float s1a = z[0], s2a = z[1], s1b = z[2], s2b = z[3];
#pragma unroll
  for (int k = 0; k < 4; ++k) {
    s1a = dot2(v[k], 0x3f803f80u, s1a); s2a = dot2(v[k], v[k], s2a);
    s1b = dot2(v[4 + k], 0x3f803f80u, s1b); s2b = dot2(v[4 + k], v[4 + k], s2b);
  }
#pragma unroll
  for (int k = 0; k < 9; ++k) t[k] = (f32x2){bflo(v[k]), bfhi(v[k])};
  const float rs = row_sum(fold16(dot2(v[8], 0x3f803f80u, fold32(s1a, s1b)), dot2(v[8], v[8], fold32(s2a, s2b))));
  if (E) { e = row_sum(e); e += __int_as_float(__builtin_amdgcn_update_dpp(0, __float_as_int(e), 0x142, 0xa, 0xf, false)); }
  const f32x2 mean = (f32x2){rdl(rs, 15), rdl(rs, 47)} * (1.f / D_), m2 = mean * mean;
  f32x2 var = (f32x2){rdl(rs, 31), rdl(rs, 63)} * (1.f / D_) - m2;
  asm volatile("" : "+v"(t[0]), "+v"(t[1]), "+v"(t[2]), "+v"(t[3]), "+v"(t[4]), "+v"(t[5]), "+v"(t[6]), "+v"(t[7]), "+v"(t[8]));
  if (__builtin_expect(!(m2[0] <= 16.f * var[0] && m2[1] <= 16.f * var[1]), 0)) {
    float q[3] = {0.f, 0.f, 0.f};
#pragma unroll
    for (int k = 0; k < 9; ++k) {
      const f32x2 a = t[k] - ((k < 4 || (k == 8 && lane < 32)) ? mean[0] : mean[1]);
      q[k >> 2] += a[0] * a[0] + a[1] * a[1];
    }
    float qa = q[0] + ((lane < 32) ? q[2] : 0.f), qb = q[1] + ((lane < 32) ? 0.f : q[2]);
    wave_sum2(qa, qb);
    var = (f32x2){qa, qb} * (1.f / D_);
  }
  const uint32_t mp = pack2(mean[0], mean[1]), vp = pack2(var[0], var[1]);
  const f32x2 ve = (f32x2){bflo(vp), bfhi(vp)} + 1e-5f;
  const uint32_t e2 = pack2(ve[0], ve[1]);
  const uint32_t sp = pack2(__builtin_amdgcn_sqrtf(bflo(e2)), __builtin_amdgcn_sqrtf(bfhi(e2)));
  rc = (f32x2){__builtin_amdgcn_rcpf(bflo(sp)), __builtin_amdgcn_rcpf(bfhi(sp))} * sc;
  nc = -(f32x2){bflo(mp), bfhi(mp)} * rc;
}
template <bool H>
__device__ __forceinline__ void ln_apply(const f32x2* t, int lane, f32x2 rc, f32x2 nc, uint32_t* c) {
  const float nm[3] = {nc[0], nc[1], (lane < 32) ? nc[0] : nc[1]}, rr[3] = {rc[0], rc[1], (lane < 32) ? rc[0] : rc[1]};
  f32x2 dd[9];
#pragma unroll
  for (int k = 0; k < 9; ++k) dd[k] = __builtin_elementwise_fma(t[k], (f32x2){rr[k >> 2], rr[k >> 2]}, (f32x2){nm[k >> 2], nm[k >> 2]});
  asm volatile("" : "+v"(dd[0]), "+v"(dd[1]), "+v"(dd[2]), "+v"(dd[3]), "+v"(dd[4]), "+v"(dd[5]), "+v"(dd[6]), "+v"(dd[7]), "+v"(dd[8]));
#pragma unroll
  for (int k = 0; k < 9; ++k) {
    const f32x2 d = dd[k];
    typedef __attribute__((ext_vector_type(2))) uint16_t u16x2;
    if (H) c[k] = __builtin_bit_cast(uint32_t, __builtin_elementwise_add_sat(__builtin_bit_cast(u16x2, __builtin_amdgcn_cvt_pkrtz(d[0], d[1])), (u16x2){4, 4}));
    else c[k] = pack2(d[0], d[1]);
  }
}
__device__ __forceinline__ uint32_t med3u(uint32_t a, uint32_t b, uint32_t c) { return max(min(a, b), min(max(a, b), c)); }
__device__ __forceinline__ uint32_t oct_maxu(uint32_t v) {
  v = max(v, (uint32_t)DPP_I((int)v, 0xb1, 0xf));
  v = max(v, (uint32_t)DPP_I((int)v, 0x4e, 0xf));
  v = max(v, (uint32_t)DPP_I((int)v, 0x141, 0xf));
  return v;
}

#define KARGS const u16* __restrict__ x, const u16* __restrict__ Are, const u16* __restrict__ Aim, const u16* __restrict__ Bre, \
    const u16* __restrict__ Bim, const u16* __restrict__ Cre, const u16* __restrict__ Cim, const u16* __restrict__ logdt, \
    const u16* __restrict__ Dskip, char* __restrict__ ws, const u16* __restrict__ wr, const u16* __restrict__ wg, \
    const u16* __restrict__ wu, const u16* __restrict__ wd, u16* __restrict__ out
#define KPASS x, Are, Aim, Bre, Bim, Cre, Cim, logdt, Dskip, ws, wr, wg, wu, wd, out
template <int ROLE, int FFN = -1>
__device__ __forceinline__ void role_body(char* smem, KARGS) {
  const int tid = threadIdx.x, lane = tid & 63;
  const int w = __builtin_amdgcn_readfirstlane(tid >> 6), wl = w & 3;
  const int b = blockIdx.x & 255, bx = b & 7, bj = b >> 3;
  constexpr bool isl = ROLE == 1 || ROLE == 2;
  const int tg = isl ? 32 * (bj >> 2) + 4 * bx + (bj & 3) : 32 * bx + bj;
  u32x4 xa, xc;
  uint32_t ep, r0;
#define P_WAIT(n, ...) asm volatile("s_waitcnt vmcnt(" #n ")\n\tv_lshlrev_b32 %0, 16, v100\n\tv_lshlrev_b32 %1, 16, v101\n\tv_lshlrev_b32 %2, 16, v102" \
    : "=v"(ldt), "=v"(are), "=v"(aim) : : "v100", "v101", "v102" __VA_ARGS__)
#define P_MORE() asm volatile("v_lshlrev_b32 %0, 16, v103\n\tv_lshlrev_b32 %1, 16, v104\n\tv_lshlrev_b32 %2, 16, v105\n\tv_lshlrev_b32 %3, 16, v106" \
    : "=v"(bre), "=v"(bim), "=v"(cre), "=v"(cim) : : "v103", "v104", "v105", "v106")
  TSD;
  TS(0, 0);
  const uint32_t lb0 = (uint32_t)(uintptr_t)(__attribute__((address_space(3))) char*)smem;
  const int i = lane & 15, kq = lane >> 4;
  const u32x4 ones = {0xffffffffu, 0xffffffffu, 0xffffffffu, 0xffffffffu}, zero4 = {0u, 0u, 0u, 0u};
  auto u_again = [&](u32x4& uw, const u32x4& ut, uint32_t uo) {
#pragma unroll 1
    for (int spin = 0; spin < SPIN_CAP; ++spin) {
      const bool na = ((uw[0] | uw[1] | uw[2] | uw[3]) & UMASK) != 0u;
      if (__builtin_expect(!__any(na), 1)) break;
      TSC(19);
      SLEEP(USLEEP);
      if (na) { LD16W(uw, uo); uw ^= ut; }
    }
  };
  constexpr uint32_t S16 = 0x41800000u, LMASK = 0x00070007u;
  auto v_put = [&](u32x4 v, const u32x4& ut, bool other, uint32_t vo) {
    const bool second = __any(other || ((v[0] | v[1] | v[2] | v[3]) & 0x40004000u) != 0u);
    if (__builtin_expect(second, 0)) v = (v & ~LMASK) | (~ut & LMASK); else v = (v & ~LMASK) | ut;
    ST16(vo, v);
    return second;
  };
  auto v_end = [&](uint32_t T, int n) { DRAIN(); if (lane == 0) ST4(O_DN + 16u * n, T); };
  if constexpr (!isl) {
    const int ch = wl >> 1, tl = tid & 255;
    int* evp = (int*)(smem + O_FL + 16 * ch);
    float ldt, are, aim, dsk;
    if constexpr (ROLE == 0) {
      __builtin_amdgcn_s_setprio(KPRIO);
      COps e;
      conv_ops(lb0 + (uint32_t)(ch * TB), lb0 + (uint32_t)(O_UL + ch * UB), lb0 + (uint32_t)(O_IM + ch * GB), lane, e);
      e.o = lb0 + O_ST + (uint32_t)(16 * ((4 * i + 2 * (kq >> 1) + ch) ^ (i >> 1)) + 8 * (kq & 1));
      e.f = lb0 + O_FL;
      TOps t;
      table_ops(lb0 + (uint32_t)(O_IM + 128 * IP * ch), lb0 + (uint32_t)(TB * ch), wl & 1, lane, t);
      uint32_t pz = lb0 + O_IM + (uint32_t)(((lane >> 3) & 1) * GB + pad_at(lane & 7)), im = lb0 + (uint32_t)(O_IM + IP * tl);
      asm volatile("" : "+v"(e.o), "+v"(e.f), "+v"(pz), "+v"(im));
      float bre, bim, cre, cim;
      P_WAIT(0, , "v103", "v104", "v105", "v106", "v107", "v109");
      asm volatile("" : "={v109}"(ep));
      const uint32_t T = ((uint32_t)__builtin_amdgcn_readfirstlane(ep) + 1u) * 0x9E3779B1u;
      if (w == 3 && lane < 8) ST16(O_KEY + 128u * b + 16u * lane, ones);
      P_MORE();
      asm volatile("v_lshlrev_b32 %0, 16, v107" : "=v"(dsk) : : "v107");
      TS(0, 1);
      evp[wl & 1] = gen_y(ldt, are, aim, bre, bim, cre, cim, im, [] {} TSA);
      TS(0, 8);
      BAR();
      TS(0, 2);
      const uint32_t cv = table_tile(t, wl & 1, lane, lb0 + O_FL + 16 * ch, dsk TSA), cs = (uint32_t)__builtin_amdgcn_readfirstlane(cv);
      *(uint32_t*)(smem + O_FL + 40 + 4 * ch) = cv;
      TS(0, 9);
      gsync0(lb0 + O_FL + 36, 4);
      TS(0, 3);
      *(u32x4*)(smem + O_ST + 16 * tl) = ones;
      *(u32x4*)(smem + O_ST + 4096 + 16 * tl) = ones;
      if (w == 0) *LP(u32x4, pz) = zero4;
      DRAIN();
      CONV_RUN(wl & 1, e);
      if (wl & 1) asm volatile("s_mov_b64 exec, 1\n\tglobal_store_dwordx2 %0, %1, %2 sc1" : : "v"(O_SC + 16u * (2 * tg + ch)), "v"((u32x2){T, cs}), "s"(ws) : "memory");
      TS(0, 5);
    } else {
      __builtin_amdgcn_s_setprio(UPRIO);
#pragma unroll
      for (int k = 0; k < 3; ++k) *(u32x4*)(smem + ((16 * (tl + 256 * k) < O_FL) ? 16 * (tl + 256 * k) : O_FL + 32)) = zero4;
      if (w == 7) *(u32x4*)(smem + O_UL + (lane >> 5) * UB + 16 * min(lane & 31, 29)) = zero4;
      uint32_t uo = u_at(tl >> 1, 2 * tg + (tid & 1)), go = O_SU + (uint32_t)(256 * bx + 32 * wl + (lane & 31) + 4 * (lane & 32)) * 128u;
      uint32_t ul = (uint32_t)(O_UL + (tid & 1) * UB + C_U - 16 + 16 * (tl >> 1)), ug = (uint32_t)(O_IM + (tid & 1) * GB + 16 * (tl >> 1)), so = lb0 + O_ST + 16u * (uint32_t)(lane ^ (lane >> 3));
      uint32_t vo = O_V + (uint32_t)tg * VP + 16u * lane;
      asm volatile("" : "+v"(uo), "+v"(go), "+v"(ul), "+v"(ug), "+v"(so), "+v"(vo));
      P_WAIT(1, , "v109");
      TS(0, 1);
      evp[2 + (wl & 1)] = gen_x(ldt, are, aim, smem + O_IM + IP * tl);
      asm volatile("s_waitcnt vmcnt(0)" : "={v109}"(ep));
      const uint32_t T = ((uint32_t)__builtin_amdgcn_readfirstlane(ep) + 1u) * 0x9E3779B1u;
      TS(0, 8);
      BAR();
      TS(0, 2);
      u32x4 ut = {utag(T, 0), utag(T, 1), utag(T, 2), utag(T, 3)};
      asm volatile("" : "+v"(ut));
      SLEEP(GSLEEP);
      u32x2 pv;
      pv[1] = gate(ws, go, T);
      TS(0, 3);
      until(latest(pv[1]) + UD);
      TS(0, 7);
      u32x4 uw, ux;
      asm volatile("global_load_dwordx4 %0, %2, %4 sc1\n\tglobal_load_dwordx4 %1, %3, %4 sc1"
                   : "=&v"(uw), "=&v"(ux) : "v"(uo), "v"(uo + 64 * UP * 32), "s"(ws) : "memory");
      __builtin_amdgcn_s_setprio(FPR);
      asm volatile("s_waitcnt vmcnt(1)" : "+v"(uw));
      uw ^= ut;
      *(u32x4*)(smem + ul + 16) = uw;
      asm volatile("s_waitcnt vmcnt(0)" : "+v"(ux));
      TS(0, 11);
      ux ^= ut;
      *(u32x4*)(smem + ul + 2048 + 16) = ux;
      if (__builtin_expect(__any(((uw[0] | uw[1] | uw[2] | uw[3] | ux[0] | ux[1] | ux[2] | ux[3]) & UMASK) != 0u), 0)) {
        u_again(uw, ut, uo);
        u_again(ux, ut, uo + 64 * UP * 32);
        *(u32x4*)(smem + ul + 16) = uw;
        *(u32x4*)(smem + ul + 2048 + 16) = ux;
      }
      BAR();
      TS(0, 4);
      const u32x4 pw = *(const u32x4*)(smem + ul);
      if (wl < 2) {
        const u32x4 px = *(const u32x4*)(smem + ul + 2048);
        sums<true>(smem + ug, pw, uw);
        sums<false>(smem + ug + 2048, px, ux);
      } else sums<true>(smem + ug, pw, uw);
      count_up(lb0 + O_FL + 32);
      __builtin_amdgcn_s_setprio(SPR);
      TS(0, 5);
      const bool other = *(const uint32_t*)(smem + O_FL + 40 + 4 * (lane & 1)) != S16;
      bool second = false;
#pragma unroll
      for (int k = 0; k < 2; ++k) {
        const uint32_t o = ((k ? 0x3247u : 0x1065u) >> (4 * wl) & 7u) * 1024u;
        u32x4 v;
#pragma unroll 1
        for (int spin = 0; spin < SPIN_CAP; ++spin) {
          asm volatile("ds_read_b128 %0, %1\n\ts_waitcnt lgkmcnt(0)" : "=&v"(v) : "v"(so + o) : "memory");
          if (!__any(max4(v) == 0xffffffffu)) break;
          __builtin_amdgcn_s_sleep(VPS);
        }
        TS(0, 12 + k);
        second |= v_put(v, ut, other, vo + o);
        TS(0, 10 - 4 * k);
      }
      if (__builtin_expect(second, 0)) v_end(T, 4 * tg + wl);
    }
    TSE();
  } else {
  uint32_t* cand = (uint32_t*)(smem + LM);
  uint32_t* sel = cand + 128;
  uint32_t* kl = sel + 48;
  uint32_t* cnt = sel + 64;
  const uint32_t ca = lb0 + LM + 4 * (128 + 64);
  u16* hm = (u16*)(cnt + 12);
  uint32_t T;
  const bool ffn = (FFN < 0) ? bj < 18 : FFN, isx = FFN != 1 && bj >= 24;
  const int fb = 18 * bx + bj;
  constexpr bool ln2 = ROLE == 2;
  const int g0 = tg, g2 = tg;
  u32x4 bw[18];
#pragma unroll
  for (int s = 0; s < 9; ++s) bw[s] = (u32x4){0u, 0u, 0u, 0u};
  int tid2 = threadIdx.x;
  uint32_t ts1 = 0u;
  auto rest = [&] {
    if (ts1 && (int)((uint32_t)__builtin_amdgcn_s_memrealtime() - ts1) > 0) { TS(0, 8); until(ts1 + TSTOP + RD); if (RW) gwait(ca, 4); ts1 = 0u; }
  };
  auto gu_dma = [&](int q0, int q1, auto depth) {
    const char* gs = (const char*)((w < 2) ? wg : wu) + (size_t)((144 * (w & 1)) * (F_ * 2) + fb * 64);
    const uint32_t gv = (uint32_t)((lane >> 2) * (F_ * 2) + 16 * ((lane & 3) ^ (lane >> 5) ^ (w & 2)));
#pragma unroll 1
    for (int q = q0; q < q1; ++q) {
      const int k = (int)((((bj & 1) ? ORD1 : ((unsigned __int128)0x876543210u << 36 | 0x876543210ull)) >> (4 * (q - (q >= 9 ? 9 : 0)) + (q >= 9 ? 36 : 0))) & 15u), h = q >= 9;
      if (q >= RQ) rest();
      dma16(gs + (size_t)(288 * h + 16 * k) * (F_ * 2), gv, lb0 + LW * h + 9216 * w + 1024 * k);
      asm volatile("s_waitcnt vmcnt(%0)" : : "n"(depth.value) : "memory");
    }
  };
  auto down_dma = [&](int k0, int k1, int lane) {
#pragma unroll 1
    for (int k = k0; k < k1; ++k) {
      const int o = 9216 * wl + 2304 * k;
      const char* ds = (const char*)wd + (size_t)(fb * 36864 + o);
      const uint32_t dl = lb0 + o + 32 * ((4 * wl + k + 1) >> 1);
      if (ROLE == 1) rest();
      dma16d(ds, 16u * lane, dl);
      dma16d(ds + 1024, 16u * lane, dl + 1024);
      uint32_t keep;
      asm volatile("s_mov_b32 %0, m0\n\ts_mov_b32 m0, %3\n\ts_mov_b64 exec, 0xffff\n\tglobal_load_lds_dwordx4 %1, %2" DPOL2 "\n\ts_mov_b64 exec, -1\n\ts_mov_b32 m0, %0\n\ts_waitcnt vmcnt(%4)"
                   : "=&s"(keep) : "v"(16u * lane), "s"(ds + 2048), "s"(dl + 2048), "n"(ROLE == 1 ? DDEPTH : 40) : "memory");
    }
  };
  auto gu_read = [&](int h) {
    const uint32_t ra = lb0 + LW * h + (uint32_t)(((lane >> 1) & 1) * 18432 + (8 * kq + ((lane >> 2) & 3)) * 64 + ((w ^ (kq & 1) ^ (lane & 2)) * 16) + 8 * (lane & 1));
#pragma unroll
    for (int s = 0; s < 9; ++s) {
      const u32x2 h0 = trd(ra + 2048 * s), h1 = trd(ra + 2048 * s + 256);
      bw[9 * h + s] = (u32x4){h0[0], h0[1], h1[0], h1[1]};
    }
#pragma unroll
    for (int s = 0; s < 9; ++s) asm volatile("" : "+v"(bw[9 * h + s]));
  };
  uint32_t tv = 0u, tk = 0u;
  const uint32_t vearly = (uint32_t)((VEARLY >> (8 * (g0 >> 5))) & 0xffull);
#define TOUCH(pa) asm volatile("global_load_dword v126, %0, off" DPOL4 : : "v"(pa) : "memory", "v126")
  uint32_t wv[5], xv[9];
  auto rows_xw = [&](int lane) {
    u32x4 wa, xr, xs;
    const uint32_t xo = (uint32_t)((8 * g2 + 2 * wl) * (D_ * 2));
    asm volatile("global_load_dwordx4 %0, %5, %9\n\tglobal_load_dword %1, %6, %9 offset:1024\n\t"
                 "global_load_dwordx4 %2, %7, %10\n\tglobal_load_dwordx4 %3, %7, %10 offset:1152\n\tglobal_load_dword %4, %8, %10 offset:1024\n\ts_waitcnt vmcnt(0)"
                 : "=&v"(wa), "=&v"(wv[4]), "=&v"(xr), "=&v"(xs), "=&v"(xv[8])
                 : "v"(16u * lane), "v"(4u * (lane & 31)), "v"(xo + 16u * lane), "v"(xo + 4u * lane + 32u * (lane & 32)), "s"(wr), "s"(x) : "memory");
#pragma unroll
    for (int k = 0; k < 4; ++k) { wv[k] = wa[k]; xv[k] = xr[k]; xv[4 + k] = xs[k]; }
  };
  auto ln1 = [&](int g, int o, uint32_t c0) {
    uint32_t uo = u_at(g, 64 * wl + lane);
    uint32_t ta = lb0 + (uint32_t)(o + ((lane >> 2) & 3) * RP + (64 * wl + 16 * kq + 4 * (lane & 3)) * 2);
    uint32_t la = (uint32_t)(o + 2 * wl * RP + 16 * lane), lt = (uint32_t)(o + (2 * wl + (lane >> 5)) * RP + 1024 + 4 * (lane & 31));
    uint32_t sg = O_SU + (uint32_t)(256 * (lane >> 3) + g) * 128u + 16u * (lane & 7);
    asm volatile("" : "+v"(uo), "+v"(ta), "+v"(la), "+v"(lt), "+v"(sg));
    f32x4 z4 = {0.f, 0.f, 0.f, 0.f};
    asm volatile("" : "+v"(z4));
    asm volatile("s_waitcnt vmcnt(0)" : "={v[100:103]}"(xa), "={v[104:107]}"(xc), "={v108}"(r0), "={v109}"(ep));
    TS(0, 1);
    const uint32_t v[9] = {xa[0], xa[1], xa[2], xa[3], xc[0], xc[1], xc[2], xc[3], r0};
    uint32_t c[9];
    f32x2 tf[9];
    T = ((uint32_t)__builtin_amdgcn_readfirstlane(ep) + 1u) * 0x9E3779B1u;
    if (wl == 3) { const uint32_t now = (uint32_t)__builtin_amdgcn_s_memrealtime() + SD; ST16(sg, ((u32x4){T, now, T, now})); }
    const uint32_t tj = ((T >> (3 * wl)) & 0x00070007u) | (((T >> (12 + wl)) & 0x00010001u) << 14);
    uint32_t tw[3] = {(tj & 0xffffu) * 0x10001u, (tj >> 16) * 0x10001u, 0u};
    tw[2] = (lane < 32) ? tw[0] : tw[1];
    asm volatile("" : "+v"(tw[0]), "+v"(tw[1]), "+v"(tw[2]));
    f32x2 rc, nc;
    float e_;
    ln_stats<false>(v, lane, 0.03125f, tf, z4, rc, nc, e_);
    ln_apply<true>(tf, lane, rc, nc, c);
#pragma unroll
    for (int k = 0; k < 9; ++k) c[k] = (c[k] & ~UMASK) | tw[k >> 2];
    *(u32x4*)(smem + la) = (u32x4){c[0], c[1], c[2], c[3]};
    *(u32x4*)(smem + la + RP) = (u32x4){c[4], c[5], c[6], c[7]};
    *(uint32_t*)(smem + lt) = c[8];
    u32x4 ha, hb;
    {
      uint32_t t, cc, n = SPIN_CAP;
      asm volatile("s_mov_b64 exec, 1\n\tv_mov_b32 %2, 1\n\tds_add_u32 %5, %2\n\ts_mov_b64 exec, -1\n"
                   "1:\n\tds_read_b32 %2, %5\n\tds_read_b64_tr_b16 v[40:41], %6\n\tds_read_b64_tr_b16 v[42:43], %6 offset:%7\n\tds_read_b64_tr_b16 v[44:45], %6 offset:512\n\t"
                   "ds_read_b64_tr_b16 v[46:47], %6 offset:%8\n\ts_sub_u32 %4, %4, 1\n\ts_cbranch_scc1 2f\n\ts_waitcnt lgkmcnt(4)\n\tv_readfirstlane_b32 %3, %2\n\t"
                   "s_cmp_lt_u32 %3, 4\n\ts_cbranch_scc1 1b\n2:\n\ts_waitcnt lgkmcnt(0)"
                   : "=&{v[40:43]}"(ha), "=&{v[44:47]}"(hb), "=&v"(t), "=&s"(cc), "+s"(n) : "v"(c0), "v"(ta), "n"(4 * RP), "n"(512 + 4 * RP) : "memory", "scc");
    }
    ST16(uo, ha);
    ST16(uo + 64 * 128, hb);
    if (wl == 0) {
      const u32x2 h4 = trd(ta + 1024), h5 = trd(ta + 1024 + 4 * RP);
      ST16(uo + 128 * 128, ((u32x4){h4[0], h4[1], h5[0], h5[1]}));
    }
  };
  const int dX = 512 - 24 + 8 * bx + bj;
  if constexpr (ROLE == 2) {
    const int tl = tid - 256;
    if (isx) {
      __builtin_amdgcn_s_setprio(QPRIO0);
      *(u32x4*)(smem + X_TB + 16 * tl) = zero4;
      *(u32x4*)(smem + X_TB + 16 * min(256 + tl, (TB + C_U) / 16 - 1)) = zero4;
      const int pr = (wl + XROT) & 3;
      COps e;
      conv_ops(lb0 + X_TB, lb0 + X_UL, lb0 + X_IM, lane, e);
      TOps t;
      table_ops(lb0 + X_IM, lb0 + X_TB, wl & 1, lane, t);
      e.o = lb0 + X_ST + (uint32_t)(16 * ((2 * i + (kq >> 1)) ^ ((i >> 2) & 1)) + 8 * (kq & 1));
      e.f = ca + 28 - 32;
      asm volatile("" : "+v"(e.o), "+v"(e.f));
      float ldt, are, aim, bre, bim, cre, cim, dsk;
      P_WAIT(0, , "v103", "v104", "v105", "v106", "v107", "v109");
      asm volatile("" : "={v109}"(ep));
      T = ((uint32_t)__builtin_amdgcn_readfirstlane(ep) + 1u) * 0x9E3779B1u;
      P_MORE();
      asm volatile("v_lshlrev_b32 %0, 16, v107" : "=v"(dsk) : : "v107");
      TS(0, 1);
      __builtin_amdgcn_s_setprio(QPRIO);
      int* evp = (int*)(sel + 44);
      char* im = smem + X_IM + IP * (tl & 127);
      evp[wl] = (wl < 2) ? gen_y(ldt, are, aim, bre, bim, cre, cim, lb0 + X_IM + IP * tl, [] {} TSA) : gen_x(ldt, are, aim, im);
      TS(0, 2);
      __builtin_amdgcn_s_setprio(QPRIO2);
      gsync(ca + 8, 4);
      if (wl < 2) evp[-4 + wl] = (int)table_tile(t, wl & 1, lane, lb0 + LM + 4 * (128 + 44), dsk TSA);
      if (wl < 2) gsync0(ca + 8, 8); else { count_up(ca + 8); SLEEP(XSL); gwait(ca + 8, 8); }
      const uint32_t cs = (uint32_t)__builtin_amdgcn_readfirstlane(evp[-4]);
      if (wl == 3) *(u32x4*)(smem + X_IM + pad_at(lane & 7)) = zero4;
      TS(0, 3);
      CONV_RUN(2 + pr, e, , "+{v11}"(tid2));
      __builtin_amdgcn_s_setprio(3);
      {
        const int lane = tid2 & 63;
        const u32x4 v = *(const u32x4*)(smem + X_ST + 2048 * pr + 1024 * (lane >> 5) + 16 * ((lane & 31) ^ ((lane >> 3) & 1)));
        if (__builtin_expect(v_put(v, (u32x4){utag(T, 0), utag(T, 1), utag(T, 2), utag(T, 3)}, cs != S16, v_at(dX, 64 * pr + lane)), 0)) {
          DRAIN();
          if (lane == 0) ST4(O_DN + 16u * (4 * (dX - 256) + pr), T);
        }
        if (pr == 0 && lane == 0) asm volatile("global_store_dwordx2 %0, %1, %2 sc1" : : "v"(O_SC + 16u * dX), "v"((u32x2){T, cs}), "s"(ws) : "memory");
      }
      TS(0, 6);
    }
    if (!isx) __builtin_amdgcn_s_setprio(PPRIO);
    asm volatile("" : "+v"(tid2));
    if (!isx) rows_xw(tid2 & 63);
    if (!isx && !ffn) {
      SLEEP(RSLEEP);
#pragma unroll
      for (int k = 0; k < 7; ++k) ST16(O_SL + (uint32_t)((6 * bx + bj - 18) * 1792 + 256 * k - 256 + tid2) * 16u, ones);
      DRAIN();
    }
    if (!isx) BAR();
    const uint32_t tp = (uint32_t)__builtin_amdgcn_readfirstlane(cnt[4]);
    if (!isx) T = (uint32_t)__builtin_amdgcn_readfirstlane(sel[43]);
    tv = tp + UD + VD2 - vearly;
  } else {
    __builtin_amdgcn_s_setprio(3);
    if (tid < 12) cnt[tid] = 0u;
    BAR();
    if (tid < 20) ((uint32_t*)(hm + 320))[tid] = 0u;
    if (tid >= 184) *(u32x4*)(smem + L_SC - 16 * 184 + 16 * tid) = (u32x4){0x41804180u, 0x41804180u, 0x41804180u, 0x41804180u};
    uint32_t sink = 0;
    ln1(tg, 0, ca + 12);
    TS(0, 2);
    __builtin_amdgcn_s_setprio(NPRIO);
    asm volatile("s_waitcnt vmcnt(0)" : "+v"(sink) : : "memory");
    TS(0, 13);
    if (w == 0) {
      const uint32_t xr = O_XC + (uint32_t)((511 - 2 * (g0 + w)) * 144 + lane) * 16u;
      ST16(xr, ones);
      ST16(xr + 1024u, ones);
      if (lane < 16) ST16(xr + 2048u, ones);
    }
    if (w == 2 && b < 72 && lane < 8) ST16(O_FB + (uint32_t)(8 * b + lane) * 16u, ones);
    if (isx) {
      rows_xw(lane);
      char* sp = smem + X_RW(w) + 16 * lane;
      *(u32x4*)sp = (u32x4){xv[0], xv[1], xv[2], xv[3]};
      *(u32x4*)(sp + 1024) = (u32x4){xv[4], xv[5], xv[6], xv[7]};
      *(u32x2*)(smem + X_RW(w) + 2048 + 8 * lane) = (u32x2){xv[8], wv[4]};
      if (w == 0) *(u32x4*)(smem + X_WR + 16 * lane) = (u32x4){wv[0], wv[1], wv[2], wv[3]};
    }
    SLEEP(ESLEEP);
    uint32_t tu = latest(gate(ws, O_SU + (uint32_t)(256 * bx + (ALLP ? tid : 64 * w + 4 * i + (bj & 3))) * 128u, T));
    asm volatile("s_mov_b64 exec, 1\n\tds_max_u32 %0, %1\n\ts_mov_b64 exec, -1" : : "v"(ca + 16), "v"(tu) : "memory");
    if (w == 0) sel[43] = T;
    if (isx) {
      const uint32_t xo = u_at(tid, dX);
      char* sm = smem + X_UL + C_U - 16 + 16 * tid;
      const u32x4 ut = {utag(T, 0), utag(T, 1), utag(T, 2), utag(T, 3)};
      __builtin_amdgcn_s_setprio(3);
      until(tu + UD + XD);
      u32x4 uw;
      asm volatile("global_load_dwordx4 %0, %1, %2 sc1" : "=&v"(uw) : "v"(xo), "s"(ws) : "memory");
      asm volatile("s_waitcnt vmcnt(0)" : "+v"(uw));
      uw ^= ut;
      u_again(uw, ut, xo);
      *(u32x4*)(sm + 16) = uw;
      BAR();
      if (w < 3) {
        const u32x4 pw = *(const u32x4*)sm;
        if (w < 2) sums<true>(smem + X_IM + 16 * tid, pw, uw); else sums<false>(smem + X_IM + 16 * tid, pw, uw);
        count_up(ca + 28);
      }
    } else {
      until(tu + UD + WD);
      BAR();
    }
    tu = (uint32_t)__builtin_amdgcn_readfirstlane(cnt[4]);
    TS(0, 3);
    __builtin_amdgcn_s_setprio(DPRIO);
    if (ffn) {
      ts1 = (tu + UD + VD2 - TSTOP) | 1u;
      gu_dma(0, N0, IC<40>{});
      gu_dma(N0, NA, IC<WDEPTH>{});
      asm volatile("s_waitcnt vmcnt(%0)" : : "n"(NA - 9) : "memory");
      gsync0(ca + 4, 4);
      gu_read(0);
      asm volatile("s_waitcnt lgkmcnt(0)" ::: "memory");
      count_up(ca + 36);
      TS(0, 4);
      gu_dma(NA, 18, IC<WDEPTH2>{});
      gwait(ca + 36, 4);
      down_dma(0, KN, lane);
      asm volatile("s_waitcnt vmcnt(%0)" : : "n"(3 * KN) : "memory");
      count_up(ca + 4);
      TS(0, 7);
    } else {
      if (!isx && C3) {
        const int n = 6 * min(tid, 191) + bj - 18, r = n >> 2;
        const char* wb = (const char*)((n & 1) ? wu : wg) + bx * 1152;
        const int c0 = (3 - (int)((uintptr_t)wb >> 7)) & 7;
        wb += (uint32_t)((r + 144 * ((r >= 144) + ((n >> 1) & 1))) * (F_ * 2) + 128 * c0);
        TOUCH(wb);
        if (c0 == 0) TOUCH(wb + 1024);
      }
      auto d_line = [&](int k) {
        const int n = min((k * 14 + bj - 18) * 64 + lane, 5183), q = n / 1296, r = n - 1296 * q;
        return (const char*)wd + (uint32_t)(bx * 5184 + (r / 18) * 72 + 18 * (HREV ? 3 - q : q) + r % 18) * 128u;
      };
      int k = w;
      if (w < HW) {
#pragma unroll 1
        for (; k < 6 && (int)((uint32_t)__builtin_amdgcn_s_memrealtime() - (tu + UD + VD2 - HSTOP)) < 0; k += HW) { TOUCH(d_line(k)); DRAIN(); }
      }
      TS(0, 4);
      until(tu + UD + VD2 + RD2);
      if (RW) gwait(ca, 4);
      if (w < HW) {
#pragma unroll 1
        for (; k < 6; k += HW) { TOUCH(d_line(k)); DRAIN(); }
      }
      TS(0, 7);
    }
    tv = tu + UD + VD2 - vearly;
    tk = tu + UD + VD2 + KD;
  }
  {
  const int tid = tid2, lane = tid & 63, i = lane & 15, kq = lane >> 4;
  __builtin_amdgcn_s_setprio(3);
  const int PL = ffn ? 6400 : LW + 6656;
  f32x2 xf[9];
  f32x4 za = {0.f, 0.f, 0.f, 0.f}, zb = za;
  uint32_t al, ha, h0a, h1a, so, da;
  auto ffn_terms = [&] {
    al = O_XC + 64u * (i >> 3) + 16u * kq + 128u * w;
    ha = (ROLE == 1) ? (uint32_t)((4 * (kq & 1) + 2 * (kq >> 1) + (i >> 3)) * 80 + 16 * w + 2 * (i & 7)) : 0u;
    h0a = (uint32_t)(min(i, 8) * 80 + 16 * kq); h1a = (uint32_t)(min(i ^ 8, 8) * 80 + 16 * kq);
    so = O_SL + (uint32_t)fb * SP + ((ROLE == 2) ? 16u * tid - 4096u : 8192u + 16u * lane);
    da = lb0 + (uint32_t)((8 * kq + ((lane >> 2) & 3)) * 1152 + 32 * ((8 * kq + ((lane >> 2) & 3) + 2) >> 2) + 8 * (lane & 3) + ((ROLE == 2) ? 128 * wl : 1024));
    if (ROLE == 2) asm volatile("" : "+v"(h0a), "+v"(h1a), "+v"(so), "+v"(da)); else asm volatile("" : "+v"(ha), "+v"(al), "+v"(da));
  };
  if (ROLE == 1) ffn_terms();
  uint32_t hv[9] = {}, nv[9] = {}, keya = 0u, keyb = 0u;
  int rka = 0, rkb = 0;
  auto put_row = [&](const uint32_t* r, int j, const void* base, uint32_t ro) {
    const u32x4 tl = {r[8], (uint32_t)DPP_I((int)r[8], 0x55, 0xf), (uint32_t)DPP_I((int)r[8], 0xaa, 0xf), (uint32_t)DPP_I((int)r[8], 0xff, 0xf)};
    asm volatile("global_store_dwordx4 %0, %1, %2 sc1\n\ts_nop 1" : : "v"(ro + 16u * lane), "v"((u32x4){r[4 * j], r[4 * j + 1], r[4 * j + 2], r[4 * j + 3]}), "s"(base) : "memory");
    if ((lane & 35) == 32 * j) asm volatile("global_store_dwordx4 %0, %1, %2 offset:1024 sc1\n\ts_nop 1" : : "v"(ro + 4u * (lane & 31)), "v"(tl), "s"(base) : "memory");
  };
  if (ln2) {
    const int tl = tid - 256, d1 = 64 * (tl >> 5) + (tl & 31), d3 = 64 * ((tl >> 4) & 7) + 32 + (tl & 15), d2 = (tl < 128) ? d3 + 16 : min(384 + tl, 575);
    uint32_t vo = v_at(d1, g0), vo2 = v_at(d2, g0), vo3 = v_at(d3, g0);
    u32x4 ut = {utag(T, 0), utag(T, 1), utag(T, 2), utag(T, 3)};
    u32x4 va, vc = ut, vb = ut;
    asm volatile("" : "+v"(vo), "+v"(vo2), "+v"(vo3), "+v"(ut), "+v"(vc), "+v"(vb));
    TS(0, 7);
    until(tv);
    TS(0, 8);
    asm volatile("global_load_dwordx4 %0, %2, %4 sc1\n\ts_cmp_lt_u32 %5, 2\n\ts_cbranch_scc1 1f\n\tglobal_load_dwordx4 %1, %3, %4 sc1\n1:"
                 : "=&v"(va), "+v"(vc) : "v"(vo), "v"(vo3), "s"(ws), "s"(wl) : "memory", "scc");
    until(tv + V2D);
    asm volatile("s_cmp_eq_u32 %3, 3\n\ts_cbranch_scc1 1f\n\tglobal_load_dwordx4 %0, %1, %2 sc1\n1:" : "+v"(vb) : "v"(vo2), "s"(ws), "s"(wl) : "memory", "scc");
    __builtin_amdgcn_sched_barrier(0);
    if (isx) {
      const u32x4 ra = *(const u32x4*)(smem + X_RW(wl) + 16 * lane), rb = *(const u32x4*)(smem + X_RW(wl) + 1024 + 16 * lane), rw = *(const u32x4*)(smem + X_WR + 16 * lane);
      const u32x2 rc = *(const u32x2*)(smem + X_RW(wl) + 2048 + 8 * lane);
#pragma unroll
      for (int k = 0; k < 4; ++k) { xv[k] = ra[k]; xv[4 + k] = rb[k]; wv[k] = rw[k]; }
      xv[8] = rc[0]; wv[4] = rc[1];
    }
    uint32_t pa = (uint32_t)(PL + 9216 * wl + 32 * lane), pb = (uint32_t)(PL + 9216 * wl + 2048 + 8 * (lane & 31)), ps = (lane < 32) ? 0u : 16u;
    uint32_t kid = (uint32_t)(((255 - g2) << 5) | (7 - 2 * wl - (lane >> 5)));
    uint32_t px = (uint32_t)(PL + 4 * d1), py = (uint32_t)(PL + 4 * d2), pz = (uint32_t)(PL + 4 * d3);
#pragma unroll
    for (int k = 0; k < 9; ++k) xf[k] = (f32x2){bflo(xv[k]), bfhi(xv[k])};
    asm volatile("" : "+v"(xf[0]), "+v"(xf[1]), "+v"(xf[2]), "+v"(xf[3]), "+v"(xf[4]), "+v"(xf[5]), "+v"(xf[6]), "+v"(xf[7]), "+v"(xf[8]));
    asm volatile("" : "+v"(za), "+v"(zb), "+v"(pa), "+v"(pb), "+v"(ps), "+v"(kid), "+v"(px), "+v"(py), "+v"(pz));
    float sv[10], ws_ = dot2(wv[4], 0x3f803f80u, 0.f) * 0.5f;
#pragma unroll
    for (int k = 0; k < 4; ++k) ws_ = dot2(wv[k], 0x3f803f80u, ws_);
    ws_ = wave_sum(ws_);
#pragma unroll
    for (int k = 0; k < 10; ++k) sv[k] = 16.f;
    uint32_t ka_ = lb0 + LM + 4u * (128 + 48 + 2 * wl + (lane >> 5)), kb_ = lb0 + LM + 4u * (128 + 48);
    uint32_t ko_ = O_KEY + (uint32_t)(256 * (lane & 7) + 8 * (g2 & 31) + (g2 >> 5)) * 16u;
    asm volatile("" : "+v"(ws_), "+v"(sv[0]), "+v"(sv[1]), "+v"(sv[2]), "+v"(sv[3]), "+v"(sv[4]), "+v"(sv[5]), "+v"(sv[6]), "+v"(sv[7]), "+v"(sv[8]), "+v"(sv[9]),
                 "+v"(ka_), "+v"(kb_), "+v"(ko_));
    if (ffn) gwait(ca + 36, 4);
    asm volatile("s_waitcnt vmcnt(1)\n\ts_cmp_eq_u32 %2, 3\n\ts_cbranch_scc0 1f\n\ts_waitcnt vmcnt(0)\n1:" : "+v"(va), "+v"(vc) : "s"(wl) : "memory", "scc");
    auto turn1 = [&] {
#pragma unroll
      for (int e = 0; e < 4; ++e) *(uint32_t*)(smem + px + 9216 * e) = va[e];
      if (wl >= 2) {
#pragma unroll
        for (int e = 0; e < 4; ++e) *(uint32_t*)(smem + pz + 9216 * e) = vc[e];
      }
    };
    auto turn = [&] {
      if (wl < 3) {
#pragma unroll
        for (int e = 0; e < 4; ++e) *(uint32_t*)(smem + py + 9216 * e) = vb[e];
      }
    };
    va ^= ut; vc ^= ut;
    turn1();
    asm volatile("s_waitcnt vmcnt(0)" : "+v"(vb) : : "memory");
    vb ^= ut;
    turn();
    const u32x4 vs = va | vb | vc;
    if (__builtin_expect(__any(((vs[0] | vs[1] | vs[2] | vs[3]) & UMASK) != 0u), 0)) {
#pragma unroll 1
      for (int spin = 0; spin < 4; ++spin) {
        const bool na = ((va[0] | va[1] | va[2] | va[3]) & UMASK) != 0u, nb = ((vb[0] | vb[1] | vb[2] | vb[3]) & UMASK) != 0u, nc = ((vc[0] | vc[1] | vc[2] | vc[3]) & UMASK) != 0u;
        if (!__any(na || nb || nc)) break;
        TSC(16);
        if (na) LD16(va, vo);
        if (nb) LD16(vb, vo2);
        if (nc) LD16(vc, vo3);
        asm volatile("s_waitcnt vmcnt(0)" : "+v"(va), "+v"(vb), "+v"(vc) : : "memory");
        if (na) va ^= ut;
        if (nb) vb ^= ut;
        if (nc) vc ^= ut;
      }
      auto again = [&](u32x4& v, uint32_t o, int d) {
        const int J = g0 >> 5;
        const uint32_t dn = O_DN + 16u * (uint32_t)((d < 512) ? 4 * (d >> 1) + ((0x01013232 >> (4 * J)) & 3) : 4 * (d - 256) + (J >> 1));
        bool pend = ((v[0] | v[1] | v[2] | v[3]) & UMASK) != 0u;
#pragma unroll 1
        for (int spin = 0; spin < SPIN_CAP && __any(pend); ++spin) {
          TSC(16);
          SLEEP(PSLEEP);
          if (pend) {
            uint32_t fin = ~T;
            if ((v[0] & v[1] & v[2] & v[3] & LMASK) == LMASK)
              asm volatile("global_load_dword %0, %1, %2 sc1\n\ts_waitcnt vmcnt(0)" : "=&v"(fin) : "v"(dn), "s"(ws) : "memory");
            LD16W(v, o);
            v ^= ut;
            pend = ((v[0] | v[1] | v[2] | v[3]) & UMASK) != 0u;
            if (pend && fin == T) {
              u32x2 sc;
              for (int k = 0; k < SPIN_CAP; ++k) {
                asm volatile("global_load_dwordx2 %0, %1, %2 sc1\n\ts_waitcnt vmcnt(0)" : "=&v"(sc) : "v"(O_SC + 16u * d), "s"(ws) : "memory");
                if (sc[0] == T) break;
              }
              *(u16*)(smem + L_SC + 2 * d) = (u16)(sc[1] >> 16);
              cnt[10] = 1u;
              v = (v ^ ut) & ~LMASK;
              pend = false;
            }
          }
        }
      };
      again(va, vo, d1); again(vc, vo3, d3); again(vb, vo2, d2);
      turn1();
      turn();
    }
    u32x4 a0, a1;
    u32x2 a2, cf;
    {
      uint32_t cc, n = SPIN_CAP;
      asm volatile("s_mov_b64 exec, 1\n\tv_mov_b32 v90, 1\n\tds_add_u32 %6, v90\n\ts_mov_b64 exec, -1\n"
                   "1:\n\tds_read2_b32 %3, %6 offset1:10\n\tds_read_b128 %0, %7\n\tds_read_b128 %1, %7 offset:16\n\tds_read_b64 %2, %8\n\t"
                   "s_sub_u32 %5, %5, 1\n\ts_cbranch_scc1 2f\n\ts_waitcnt lgkmcnt(3)\n\tv_readfirstlane_b32 %4, v90\n\t"
                   "s_cmp_lt_u32 %4, 4\n\ts_cbranch_scc1 1b\n2:\n\ts_waitcnt lgkmcnt(0)"
                   : "=&v"(a0), "=&v"(a1), "=&v"(a2), "=&{v[90:91]}"(cf), "=&s"(cc), "+s"(n) : "v"(ca), "v"(lb0 + pa), "v"(lb0 + pb) : "memory", "scc");
    }
    TS(0, 9);
    if (__builtin_expect(__builtin_amdgcn_readfirstlane(cf[1]) != 0, 0)) {
      const u32x4 q = *(const u32x4*)(smem + L_SC + 16 * lane);
      const uint32_t r = *(const uint32_t*)(smem + L_SC + 1024 + 4 * (lane & 31));
#pragma unroll
      for (int k = 0; k < 4; ++k) { sv[2 * k] = bflo(q[k]); sv[2 * k + 1] = bfhi(q[k]); }
      sv[8] = bflo(r); sv[9] = bfhi(r);
    }
    const uint32_t av[10] = {a0[0], a0[1], a0[2], a0[3], a1[0], a1[1], a1[2], a1[3], a2[0] >> ps, a2[1] >> ps};
    f32x2 h[9];
#pragma unroll
    for (int k = 0; k < 10; ++k) {
      asm("v_fma_mix_f32 %0, %1, %2, %3 op_sel_hi:[1,0,0]" : "=v"(h[(k < 8) ? k >> 1 : 8][k & 1]) : "v"(av[k]), "v"(sv[k]), "v"(xf[(k < 8) ? k >> 1 : 8][k & 1]));
      if (k < 8) asm("v_fma_mix_f32 %0, %1, %2, %3 op_sel:[1,0,0] op_sel_hi:[1,0,0]" : "=v"(h[4 + (k >> 1)][k & 1]) : "v"(av[k]), "v"(sv[k]), "v"(xf[4 + (k >> 1)][k & 1]));
    }
#pragma unroll
    for (int k = 0; k < 9; ++k) hv[k] = pack2(h[k][0], h[k][1]);
    const float da = dot2(hv[2], wv[2], dot2(hv[0], wv[0], zb[0])) + dot2(hv[3], wv[3], dot2(hv[1], wv[1], zb[1]));
    const float db = dot2(hv[6], wv[2], dot2(hv[4], wv[0], zb[2])) + dot2(hv[7], wv[3], dot2(hv[5], wv[1], zb[3]));
    float dt = dot2(hv[8], wv[4], fold32(da, db));
    f32x2 rc, nc;
    ln_stats<true>(hv, lane, 1.f, xf, za, rc, nc, dt);
    dt = (lane < 32) ? dt * rc[0] + ws_ * nc[0] : dt * rc[1] + ws_ * nc[1];
    const uint32_t bb = pack2(dt, dt) & 0xffff0000u;
    const uint32_t kk = (bb ^ (((uint32_t)((int)bb >> 31) & 0x7fff0000u) | 0x80000000u)) | kid;
    u32x4 ka, kb;
    uint32_t old;
    asm volatile("s_mov_b64 exec, %5\n\tds_write_b32 %3, %4\n\ts_mov_b64 exec, 1\n\tv_mov_b32 %2, 1\n\tds_add_rtn_u32 %2, %7, %2\n\ts_mov_b64 exec, -1\n\t"
                 "ds_read_b128 %0, %6\n\tds_read_b128 %1, %6 offset:16\n\ts_waitcnt lgkmcnt(0)"
                 : "=&v"(ka), "=&v"(kb), "=&v"(old) : "v"(ka_), "v"(kk), "s"(0x8000000080000000ull), "v"(kb_), "v"(ca) : "memory");
    if (__builtin_amdgcn_readfirstlane(old) == 7) {
      uint32_t m1 = max(max(ka[0], ka[1]), ka[2]), m2 = med3u(ka[0], ka[1], ka[2]), m3 = min(min(ka[0], ka[1]), ka[2]);
#pragma unroll
      for (int e = 0; e < 5; ++e) {
        const uint32_t kx = e ? kb[e - 1] : ka[3];
        m3 = med3u(m2, m3, kx); m2 = med3u(m1, m2, kx); m1 = max(m1, kx);
      }
      if (lane < 8) ST16(ko_, ((u32x4){m1 | 24u, m2 | 16u, m3 | 8u, T & ~1u}));
    }
    TS(0, 10);
    keya = (uint32_t)__builtin_amdgcn_readlane(kk, 31); keyb = (uint32_t)__builtin_amdgcn_readlane(kk, 63);
    ln_apply<false>(xf, lane, rc, nc, nv);
    gwait(ca, 8);
    if (wl == 1) {
      const u32x4 kv = *(const u32x4*)&kl[4 * (lane & 1)];
      if (lane < 2) ST16(O_KF + (uint32_t)(2 * g2 + lane) * 16u, ((u32x4){__builtin_amdgcn_perm(kv[1], kv[0], 0x07060302u), T, __builtin_amdgcn_perm(kv[3], kv[2], 0x07060302u), T}));
      if (ROLE == 2 && b == 0 && lane == 0) ST4(O_EP, T * 0x0E8B2F51u);
    }
    const uint32_t ko = kl[lane & 7];
    rka = FORCEFB + __popcll(__ballot(ko > keya) & 0xffull); rkb = FORCEFB + __popcll(__ballot(ko > keyb) & 0xffull);
#pragma unroll
    for (int k = 0; k < 9; ++k) {
      const f32x2 o = xf[k] + (f32x2){bflo(nv[k]), bfhi(nv[k])};
      hv[k] = pack2(o[0], o[1]);
      nv[k] = min(nv[k], 0xfffffffeu);
    }
    if (rka < 2) put_row(nv, 0, ws, O_XC + (uint32_t)(1023 - 4 * g2 - rka) * 1152u);
    if (rkb < 2) put_row(nv, 1, ws, O_XC + (uint32_t)(1023 - 4 * g2 - rkb) * 1152u);
    if (!LPOS && ffn) down_dma(KN, 4, lane);
    ffn_terms();
  }
  uint32_t k3rd = 0u, k4th = 0u;
  uint32_t q0 = lb0 + LM + 4u * lane, q1 = lb0 + LM + 32u * w, q2 = q1 + 4u * (lane & 7), q3 = lb0 + LM + 512u + 4u * (lane & 7), q4 = lb0 + LM + (uint32_t)(tid & ~7);
  uint32_t r8 = C_, sb = lb0 + LM + 512u;
  asm volatile("" : "+v"(q0), "+v"(q1), "+v"(q2), "+v"(q3), "+v"(q4), "+v"(r8), "+v"(sb));
  if constexpr (ROLE == 1) {
    u32x4 key;
    const uint32_t kof = O_KEY + (uint32_t)(256 * bx + tid) * 16u;
    until(tk);
    if (!ffn) SLEEP(NSLEEP);
    LD16(key, kof);
    if (ffn) {
      gwait0(ca + 4, 8);
      gu_read(1);
    }
    asm volatile("s_waitcnt vmcnt(0)" : "+v"(key) : : "memory");
    if (__builtin_expect(__any(key[3] != (T & ~1u)), 0)) {
#pragma unroll 1
      for (int spin = 0; spin < SPIN_CAP; ++spin) {
        const bool need = key[3] != (T & ~1u);
        if (!__any(need)) break;
        TSC(15);
        SLEEP(KBACK);
        if (need) LD16W(key, kof);
      }
    }
    TS(0, 11);
    const uint32_t m1 = oct_maxu(key[0]);
    const bool hit = key[0] == m1;
    const uint32_t k0 = hit ? key[1] : key[0], k1 = hit ? key[2] : key[1];
    const uint32_t m2 = oct_maxu(k0);
    *LP(u32x2, q4) = (u32x2){m1, m2};
    k3rd = (k0 == m2) ? k1 : k0;
    k4th = (k0 == m2) ? (hit ? 0u : key[2]) : k1;
    if (RPF == 1 && !ffn && w == (bj & 3)) {
      const uint32_t r1 = O_XC + ((m1 >> 3) & 0x3ffu) * 1152u + 128u * (lane & 7), r2 = O_XC + ((m2 >> 3) & 0x3ffu) * 1152u + 128u * (lane & 7);
      asm volatile("global_load_dword v126, %0, %3 sc1\n\tglobal_load_dword v126, %1, %3 sc1\n\tglobal_load_dword v126, %2, %3 offset:1024 sc1"
                   : : "v"(r1), "v"(r2), "v"(((lane & 1) ? r1 : r2) - 128u * (lane & 7)), "s"(ws) : "memory", "v126");
    }
  }
  auto pick = [&](auto co, auto so_) {
    BAR();
    const uint32_t c = *LP(uint32_t, q0 + co.value), own = *LP(uint32_t, q2 + co.value);
    const u32x4 la = *LP(u32x4, q1 + co.value), lb = *LP(u32x4, q1 + co.value + 16);
    uint32_t r = r8;
    asm("v_cmp_gt_u32 s[40:41], %1, %2\n\tv_cmp_gt_u32 s[42:43], %1, %3\n\tv_cmp_gt_u32 s[44:45], %1, %4\n\tv_cmp_gt_u32 s[46:47], %1, %5\n\t"
        "v_cmp_gt_u32 s[48:49], %1, %6\n\tv_cmp_gt_u32 s[50:51], %1, %7\n\tv_cmp_gt_u32 s[52:53], %1, %8\n\tv_cmp_gt_u32 s[54:55], %1, %9\n\t"
        "s_bcnt1_i32_b64 s40, s[40:41]\n\ts_bcnt1_i32_b64 s42, s[42:43]\n\tv_writelane_b32 %0, s40, 0\n\ts_bcnt1_i32_b64 s44, s[44:45]\n\t"
        "v_writelane_b32 %0, s42, 1\n\ts_bcnt1_i32_b64 s46, s[46:47]\n\tv_writelane_b32 %0, s44, 2\n\ts_bcnt1_i32_b64 s48, s[48:49]\n\t"
        "v_writelane_b32 %0, s46, 3\n\ts_bcnt1_i32_b64 s50, s[50:51]\n\tv_writelane_b32 %0, s48, 4\n\ts_bcnt1_i32_b64 s52, s[52:53]\n\t"
        "v_writelane_b32 %0, s50, 5\n\ts_bcnt1_i32_b64 s54, s[54:55]\n\tv_writelane_b32 %0, s52, 6\n\tv_writelane_b32 %0, s54, 7"
        : "+v"(r) : "v"(c), "v"(la[0]), "v"(la[1]), "v"(la[2]), "v"(la[3]), "v"(lb[0]), "v"(lb[1]), "v"(lb[2]), "v"(lb[3])
        : "scc", "s40", "s41", "s42", "s43", "s44", "s45", "s46", "s47", "s48", "s49", "s50", "s51", "s52", "s53", "s54", "s55");
    *LP(uint32_t, sb + 4u * min(r, (uint32_t)C_) + so_.value) = own;
    BAR();
  };
  auto rows = [&](uint32_t ao, auto&& meanwhile) {
    u32x4 aa, ab, ac = __builtin_bit_cast(u32x4, zb);
    asm volatile("global_load_dwordx4 %0, %3, %4 sc1\n\tglobal_load_dwordx4 %1, %3, %4 offset:512 sc1\n\t"
                 "s_cmp_lg_u32 %5, 0\n\ts_cbranch_scc1 1f\n\tglobal_load_dwordx4 %2, %3, %4 offset:1024 sc1\n1:"
                 : "=&v"(aa), "=&v"(ab), "+v"(ac) : "v"(ao), "s"(ws), "s"(w) : "memory", "scc");
    __builtin_amdgcn_sched_barrier(0);
    meanwhile();
    asm volatile("s_waitcnt vmcnt(0)" : "+v"(aa), "+v"(ab), "+v"(ac) : : "memory");
    if (__builtin_expect(__any(max(max(max4(aa), max4(ab)), max4(ac)) == 0xffffffffu), 0)) {
#pragma unroll 1
      for (int spin = 0; spin < SPIN_CAP; ++spin) {
        const bool na = max4(aa) == 0xffffffffu, nb = max4(ab) == 0xffffffffu, nc = max4(ac) == 0xffffffffu;
        if (!__any(na || nb || nc)) break;
        TSC(14);
        SLEEP(PSLEEP);
        if (na) LD16W(aa, ao);
        if (nb) LD16W(ab, ao + 512u);
        if (nc) LD16W(ac, ao + 1024u);
      }
    }
    *(u32x4*)(smem + LA + 16 * tid) = aa;
    *(u32x4*)(smem + LA + 4096 + 16 * tid) = ab;
    if (w == 0) *(u32x4*)(smem + LA + 8192 + 16 * tid) = ac;
  };
  auto rows_dma = [&](uint32_t ao) {
    uint32_t dl = lb0 + LA + 1024u * w, keep;
    asm volatile("s_mov_b32 %0, m0\n\ts_mov_b32 m0, %1\n\ts_add_u32 %1, %1, 4096 - 512\n\tglobal_load_lds_dwordx4 %2, %3 sc1\n\ts_mov_b32 m0, %1\n\ts_add_u32 %1, %1, 4096 - 512\n\t"
                 "global_load_lds_dwordx4 %2, %3 offset:512 sc1\n\ts_cmp_lg_u32 %4, 0\n\ts_cbranch_scc1 1f\n\ts_mov_b32 m0, %1\n\ts_nop 0\n\t"
                 "global_load_lds_dwordx4 %2, %3 offset:1024 sc1\n1:\n\ts_mov_b32 m0, %0" : "=&s"(keep), "+s"(dl) : "v"(ao), "s"(ws), "s"(w) : "memory", "scc");
  };
  auto get_bd = [&](int t0, int t1) {
#pragma unroll
    for (int t = t0; t < t1; ++t) {
      const u32x2 h0 = trd(da + 512 * (t >> 2) + 32 * (t & 3)), h1 = trd(da + 512 * (t >> 2) + 32 * (t & 3) + 4608 + 32);
      bw[t] = (u32x4){h0[0], h0[1], h1[0], h1[1]};
    }
#pragma unroll
    for (int t = t0; t < t1; ++t) asm volatile("" : "+v"(bw[t]));
  };
  pick(IC<0>{}, IC<0>{});
  uint32_t mykey = *LP(uint32_t, q3), ro = ((mykey >> 3) & 0x3ffu) * 1152u;
  TS(0, 12);
  auto check = [&] {
    cnt[(k3rd > (uint32_t)__builtin_amdgcn_readlane(mykey, 7)) ? 5 : 6] = 1u;
    const uint32_t m3 = oct_maxu(k3rd);
    if (k3rd == m3) k3rd = k4th;
    if ((lane & 7) == 0) cand[72 + (tid >> 3)] = m3;
    if (w == 0 && lane < 32) cand[(lane < 8) ? 64 + lane : 96 + lane] = (lane < 8) ? mykey : 0u;
  };
  if constexpr (ROLE == 1) {
    if (ffn && !FORCEFB) rows_dma(ro + al);
    check();
    DRAIN();
  } else if (ffn) {
    if (LPOS) { SLEEP(LSLEEP); down_dma(KN, 4, lane); } else { DRAIN(); gsync0(ca, 12); get_bd(0, 8); }
  }
  BAR();
  TS(0, 5);
  auto put_out = [&](int j, uint32_t k0, int rk) {
    const uint64_t mine = __ballot(((mykey ^ k0) & ~24u) == 0u) & 0xffull;
    if (mine == 0ull) put_row(hv, j, out, (uint32_t)((8 * g2 + 2 * wl + j) * (D_ * 2)));
    else if (rk >= 2) put_row(nv, j, ws, O_FB + 1152u * (uint32_t)__builtin_ctzll(mine));
  };
  auto rest_out = [&] { put_out(0, keya, rka); put_out(1, keyb, rkb); };
  f32x4 lo, hi;
  auto gate_up = [&] {
    u32x4 av[5];
    const uint32_t aa = lb0 + LA + 16u * lane;
#pragma unroll
    for (int j = 0; j < 5; ++j) asm volatile("ds_read_b128 %0, %1 offset:%2" : "=v"(av[j]) : "v"(aa), "n"(1024 * j) : "memory");
    lo = (f32x4){0.f, 0.f, 0.f, 0.f}; hi = lo;
#pragma unroll
    for (int j = 0; j < 9; ++j) {
      if (j < 5) asm volatile("s_waitcnt lgkmcnt(4)" : "+v"(av[j % 5]));
      else if (j == 5) asm volatile("s_waitcnt lgkmcnt(2)" : "+v"(av[0]), "+v"(av[1]));
      else if (j == 7) asm volatile("s_waitcnt lgkmcnt(0)" : "+v"(av[2]), "+v"(av[3]));
      lo = MMB(av[j % 5], bw[2 * j], lo);
      hi = MMB(av[j % 5], bw[2 * j + 1], hi);
      __builtin_amdgcn_sched_barrier(0);
      if (j < 4) asm volatile("ds_read_b128 %0, %1 offset:%2" : "=v"(av[j]) : "v"(aa), "n"(1024 * (j + 5)) : "memory");
    }
  };
  uint32_t fl;
  asm volatile("ds_read_b32 %0, %1" : "=v"(fl) : "v"(ca + 20) : "memory");
  if (ROLE == 1) gate_up();
  asm volatile("s_waitcnt lgkmcnt(0)" : "+v"(fl));
  const bool slow = FORCESLOW || FORCEFB || __builtin_amdgcn_readfirstlane(fl) != 0;
  if (__builtin_expect(slow, 0)) {
    TSC(17);
    pick(IC<256>{}, IC<64>{});
    mykey = sel[16 + (lane & 7)];
    ro = ((mykey >> 3) & 0x3ffu) * 1152u;
    if (ROLE == 1 && (k3rd > (uint32_t)__builtin_amdgcn_readlane(mykey, 7) || (mykey & 24u) == 8u)) cnt[11] = 1u;
    BAR();
    if (FORCESLOW > 1 || FORCEFB || __builtin_amdgcn_readfirstlane(cnt[11]) != 0) {
    u32x4 key;
#pragma unroll 1
    for (int spin = 0; spin < SPIN_CAP; ++spin) {
      LD16W(key, O_KF + 16u * tid);
      if (!__any(key[1] != T || key[3] != T)) break;
      SLEEP(KBACK);
    }
    const uint32_t ib = (uint32_t)(((255 - (tid >> 1)) << 5) | (7 - 4 * (tid & 1)));
    uint32_t k0 = (key[0] << 16) | ib, k1 = (key[0] & 0xffff0000u) | (ib - 1), k2 = (key[2] << 16) | (ib - 2), k3 = (key[2] & 0xffff0000u) | (ib - 3);
    CEX(k0, k1) CEX(k2, k3) CEX(k0, k2) CEX(k1, k3) CEX(k1, k2)
#pragma unroll 1
    for (int c = 0; c < C_; ++c) {
      const uint32_t mx = wave_maxu(k0);
      const bool hit = (k0 == mx);
      k0 = hit ? k1 : k0; k1 = hit ? k2 : k1; k2 = hit ? k3 : k2; k3 = hit ? 0u : k3;
      if (lane == 0) cand[64 + 8 * w + c] = mx;
    }
    pick(IC<256>{}, IC<128>{});
    mykey = sel[32 + (lane & 7)];
    const uint32_t okey = sel[32 + (lane >> 3)];
    const uint64_t same = __ballot(((mykey ^ okey) & 0x1fe0u) == 0u && okey > mykey);
    const int sl = FORCEFB + __popcll((same >> (lane & 7)) & 0x0101010101010101ull);
    ro = (sl < 2) ? (((mykey >> 3) & 0x3ffu) + 3 - sl) * 1152u : O_FB - O_XC + 1152u * (lane & 7);
    }
    if (ln2) rest_out();
    if (ffn) {
      if (ROLE == 1) rows(ro + al, [] {});
      BAR();
      if (ROLE == 1) gate_up();
    }
  }
  if (OPOS == 0 && ROLE == 2 && ffn && !slow) { SLEEP(OD); rest_out(); }
  if (!ffn) {
    if (ln2 && !slow) { SLEEP(OD2); rest_out(); }
    TSE();
    return;
  }
  uint32_t fx = 0, fv = 0, fn = 0, fo = 0, sa[9];
  float rs, hx, xs;
  u32x4 se;
  if constexpr (ROLE == 1) {
    f32x4 lh = (lane < 32) ? lo : hi;
    float fa = fold32(lh[0], lh[2]), fb2 = fold32(lh[1], lh[3]);
    if (__builtin_expect(__any(__builtin_isunordered(fa, fb2)) && !slow, 0)) {
      gsync0(ca + 4, 12);
      rows(ro + al, [] {});
      gsync0(ca + 4, 16);
      gate_up();
      lh = (lane < 32) ? lo : hi;
      fa = fold32(lh[0], lh[2]); fb2 = fold32(lh[1], lh[3]);
    }
    const float g = __int_as_float(__builtin_amdgcn_update_dpp(__float_as_int(fa), __float_as_int(fb2), 0x128, 0xf, 0xc, false));
    const float u = __int_as_float(__builtin_amdgcn_update_dpp(__float_as_int(fb2), __float_as_int(fa), 0x128, 0xf, 0x3, false));
    const uint32_t gu = pack2(g, u);
    const float gp = bflo(gu);
    const float sg = rbf(gp * __builtin_amdgcn_rcpf(1.f + __expf(-gp)));
    *(u16*)((char*)hm + ha) = f2bf(sg * bfhi(gu));
  } else if (LPOS) {
    DRAIN();
    gsync0(ca, 12);
    get_bd(0, 8);
  }
  BAR();
  TS(0, 6);
  if (ROLE == 2 || w == 2) {
    const u32x4 a0 = *(const u32x4*)((char*)hm + h0a), a1 = *(const u32x4*)((char*)hm + h1a);
    if (ROLE == 1) get_bd(0, 4);
    const f32x4 z = {0.f, 0.f, 0.f, 0.f};
    f32x4 o[4];
#pragma unroll
    for (int t = 0; t < 2 * ROLE; ++t) o[t] = MMB(a1, bw[2 * t + 1], MMB(a0, bw[2 * t], z));
#pragma unroll
    for (int t = 0; t < ROLE; ++t) {
      u32x4 sv;
#pragma unroll
      for (int r = 0; r < 4; ++r) sv[r] = min(pack2(o[2 * t][r], o[2 * t + 1][r]), 0xfffffffeu);
      ST16(so + 4096u * t, sv);
    }
    TS(0, 13);
  }
  if (OPOS == 1 && ROLE == 2) {
    SLEEP(OSLEEP);
    if (!slow) rest_out();
  }
  if (ROLE == 1 && w == 1) {
    const int q = (fb & 15) >> 2, ts = 4 * (q & 1) + ((i & 7) >> 1);
    const int dd = 64 * (fb >> 4) + 16 * (2 * (i & 1) + (q >> 1)) + 4 * (fb & 3) + 2 * (kq & 1) + (kq >> 1);
    const uint32_t fkey = (uint32_t)__builtin_amdgcn_ds_bpermute(4 * ts, (int)mykey), fr = (uint32_t)__builtin_amdgcn_ds_bpermute(4 * ts, (int)ro);
    const int ft = 2047 - (int)(((fkey >> 2) & 0x7f8u) | (fkey & 7u));
    fo = (uint32_t)(ft * D_ + dd) * 2u;
    u32x2 fs;
    asm volatile("global_load_ushort %0, %4, %8\n\tglobal_load_ushort %1, %5, %9 sc1\n\tglobal_load_ushort %2, %6, %9 sc1\n\tglobal_load_dwordx2 %3, %7, %9 sc1"
                 : "=&v"(fx), "=&v"(fv), "=&v"(fn), "=&v"(fs) : "v"(fo), "v"(v_at(dd, ft >> 3) + 2u * (ft & 7)), "v"(O_XC + fr + 2u * dd), "v"(O_SC + 16u * dd), "s"(x), "s"(ws) : "memory");
    uint32_t t16 = (((T >> (3 * ((ft >> 1) & 3))) & LMASK) | (((T >> (12 + ((ft >> 1) & 3))) & 0x00010001u) << 14)) >> (16 * (ft & 1)) & 0xffffu;
    const uint32_t o = fkey >> 16;
    const float rs0 = bf2f((u16)((o & 0x8000u) ? (o & 0x7fffu) : (~o & 0xffffu)));
    rs = rbf(__builtin_amdgcn_rcpf(1.f + __expf(-rs0)));
    const uint32_t one = (i < 8) ? (0x3f80u << (16 * (i & 1))) : 0u;
    se = (u32x4){(i >> 1) == 0 ? one : 0u, (i >> 1) == 1 ? one : 0u, (i >> 1) == 2 ? one : 0u, (i >> 1) == 3 ? one : 0u};
#pragma unroll
    for (int m = 0; m < 9; ++m) sa[m] = O_SL + (uint32_t)(16 * m + (lane >> 2)) * SP + (uint32_t)(4 * fb + (lane & 3)) * 16u;
    asm volatile("" : "+v"(t16), "+v"(rs), "+v"(se), "+v"(sa[0]), "+v"(sa[1]), "+v"(sa[2]), "+v"(sa[3]), "+v"(sa[4]), "+v"(sa[5]), "+v"(sa[6]), "+v"(sa[7]), "+v"(sa[8]));
    SLEEP(SSLEEP);
    TS(0, 1);
    float sum;
#pragma unroll 1
    for (int spin = 0; spin < SPIN_CAP; ++spin) {
      u32x4 sv[9];
#pragma unroll
      for (int m = 0; m < 9; ++m) LD16(sv[m], sa[m]);
      asm volatile("s_waitcnt vmcnt(9)" : "+v"(fx), "+v"(fv), "+v"(fn), "+v"(fs));
      if (__builtin_expect(__any(fs[0] != T), 0)) {
        for (int k = 0; k < SPIN_CAP && fs[0] != T; ++k)
          asm volatile("global_load_dwordx2 %0, %1, %2 sc1\n\ts_waitcnt vmcnt(0)" : "=&v"(fs) : "v"(O_SC + 16u * dd), "s"(ws) : "memory");
      }
      const uint32_t f1 = fv ^ t16, f2 = (f1 & 7u) ? fv & ~7u : f1;
      hx = rbf(bf2f((u16)fx) + (float)__builtin_bit_cast(_Float16, (u16)f2) * __uint_as_float(fs[1]));
      xs = bf2f((u16)fn);
      asm volatile("" : "+v"(hx), "+v"(xs));
      f32x4 dv = {0.f, 0.f, 0.f, 0.f};
      static_for<0, 9>([&](auto mm) {
        constexpr int m = mm.value;
        asm volatile("s_waitcnt vmcnt(%1)" : "+v"(sv[m]) : "n"(8 - m));
        dv = MMB(sv[m], se, dv);
        __builtin_amdgcn_sched_barrier(0);
      });
      sum = fold16(fold32(dv[0], dv[1]), fold32(dv[2], dv[3]));
      if (__builtin_expect(!__any(sum != sum), 1)) break;
      uint32_t mx = 0u;
#pragma unroll
      for (int m = 0; m < 9; ++m) mx = max(mx, max4(sv[m]));
      if (!__any(mx == 0xffffffffu)) break;
      TSC(18);
      SLEEP(PSLEEP);
    }
    const float o1 = rbf(xs + rbf(rbf(sum) * rs));
    if (i < 8) asm volatile("global_store_short %0, %1, %2 sc1" : : "v"(fo), "v"((uint32_t)f2bf(hx + o1)), "s"(out) : "memory");
    TS(0, 2);
  }
  TSE();
  }
  }
}
#define BROT "192"
#define CPF_A "s_getpc_b64 s[96:97]\n\ts_lshl_b32 s99, s2, 4\n\ts_lshl_b32 s84, s98, 7\n\ts_add_u32 s99, s99, s84\n\tv_and_b32 v112, 1, v0\n\t" \
              "v_lshl_add_u32 v112, v112, 12, s99\n\tv_min_u32 v112, kf_end-kf_all-0x80, v112\n\ts_mov_b64 exec, 3\n\tglobal_load_dword v112, v112, s[96:97]\n\ts_mov_b64 exec, -1\n\t"
extern "C" __global__ __launch_bounds__(512, 4) void kf_all(KARGS) {
  __shared__ __attribute__((aligned(16))) char smem[SMEM];
  asm volatile("s_load_dwordx16 s[64:79], s[0:1], 0x0\n\ts_load_dwordx4 s[80:83], s[0:1], 0x40\n\tv_readfirstlane_b32 s98, v0\n\t"
               "s_add_u32 s84, s2, " BROT "\n\ts_xor_b32 s84, s84, s2\n\ts_bitset0_b32 s84, 8\n\ts_bitset0_b32 s84, 9\n\ts_xor_b32 s2, s2, s84\n\t"
               "v_mov_b32 v109, 0\n\ts_and_b32 s85, s2, 7\n\ts_bitcmp1_b32 s2, 8\n\ts_cbranch_scc1 1f\n\t" CPF_A "s_bitcmp1_b32 s98, 8\n\ts_cbranch_scc1 2f\n\t"
               "s_andn2_b32 s84, s2, 31\n\ts_lshl2_add_u32 s84, s85, s84\n\ts_bfe_u32 s85, s2, 0x20003\n\ts_or_b32 s84, s84, s85\n\t"
               "s_mulk_i32 s84, 0x2400\n\ts_mulk_i32 s98, 36\n\ts_add_u32 s84, s84, s98\n\tv_and_b32 v110, 63, v0\n\tv_and_b32 v111, 32, v0\n\t"
               "v_lshlrev_b32 v111, 5, v111\n\tv_lshl_add_u32 v111, v110, 2, v111\n\tv_lshl_add_u32 v110, v110, 4, s84\n\tv_add_u32 v111, s84, v111\n\t"
               "s_cmpk_lt_u32 s2, 0x90\n\ts_waitcnt lgkmcnt(0)\n\t"
               "global_load_dwordx4 v[100:103], v110, s[64:65]\n\tglobal_load_dwordx4 v[104:107], v110, s[64:65] offset:1152\n\t"
               "global_load_dword v108, v111, s[64:65] offset:1024\n\tglobal_load_dword v109, v109, s[82:83] sc1\n\t"
               "s_cbranch_scc1 kf_n\n\ts_branch kf_h\n"
               "1:\n\ts_bfe_u32 s84, s2, 0x50003\n\ts_lshl_b32 s85, s85, 5\n\ts_or_b32 s84, s84, s85\n\ts_lshl_b32 s84, s84, 2\n\t"
               "v_and_b32 v110, 0xff, v0\n\tv_bfe_u32 v107, v0, 7, 1\n"
               "4:\n\ts_lshl_b32 s85, s84, 7\n\tv_lshl_add_u32 v110, v110, 1, s85\n\tv_lshl_add_u32 v107, v107, 1, s84\n\ts_and_b32 s85, s98, s2\n\ts_waitcnt lgkmcnt(0)\n\t"
               "global_load_ushort v100, v107, s[78:79]\n\tglobal_load_ushort v101, v110, s[66:67]\n\tglobal_load_ushort v102, v110, s[68:69]\n\t"
               "global_load_dword v109, v109, s[82:83] sc1\n\ts_bitcmp1_b32 s85, 8\n\ts_cbranch_scc1 kf_u\n\t"
               "global_load_ushort v103, v110, s[70:71]\n\tglobal_load_ushort v104, v110, s[72:73]\n\tglobal_load_ushort v105, v110, s[74:75]\n\t"
               "global_load_ushort v106, v110, s[76:77]\n\tglobal_load_ushort v107, v107, s[80:81]\n\ts_bitcmp1_b32 s2, 8\n\ts_cbranch_scc1 kf_c\n"
               "6:\n\ts_barrier\n\ts_branch kf_p\n"
               "2:\n\ts_cmpk_lt_u32 s2, 0xc0\n\ts_cbranch_scc1 6b\n\t"
               "s_lshr_b32 s84, s2, 3\n\ts_lshl3_add_u32 s84, s85, s84\n\ts_lshl1_add_u32 s84, s84, 976\n\t"
               "v_and_b32 v110, 0x7f, v0\n\tv_mov_b32 v107, 0\n\ts_branch 4b"
               : : "s"(__builtin_amdgcn_kernarg_segment_ptr()), "s"(blockIdx.x), "v"(threadIdx.x),
                   "s"((uint32_t)(uintptr_t)(__attribute__((address_space(3))) char*)smem) : "scc", "s96", "s97", "s98", "s99", "v127");
}
extern "C" __global__ __launch_bounds__(512, 4) void kf_c(KARGS) {
  __shared__ __attribute__((aligned(16))) char smem[SMEM];
  role_body<0>(smem, KPASS);
}
extern "C" __global__ __launch_bounds__(512, 4) void kf_u(KARGS) {
  __shared__ __attribute__((aligned(16))) char smem[SMEM];
  role_body<3>(smem, KPASS);
}
extern "C" __global__ __launch_bounds__(512, 4) void kf_n(KARGS) {
  __shared__ __attribute__((aligned(16))) char smem[SMEM];
  role_body<1, 1>(smem, KPASS);
}
extern "C" __global__ __launch_bounds__(512, 4) void kf_h(KARGS) {
  __shared__ __attribute__((aligned(16))) char smem[SMEM];
  role_body<1, 0>(smem, KPASS);
}
extern "C" __global__ __launch_bounds__(512, 4) void kf_p(KARGS) {
  __shared__ __attribute__((aligned(16))) char smem[SMEM];
  role_body<2>(smem, KPASS);
}
extern "C" __global__ __launch_bounds__(512, 4) void kf_conv() { asm volatile(CONV_ASM ::: CONV_CLOB); }
extern "C" __global__ void kf_end() {}

extern "C" __global__ __launch_bounds__(1024) void k_probe(int* dummy) {
  __shared__ int sm[2];
  const uint32_t now = (uint32_t)__builtin_amdgcn_s_memrealtime();
  __shared__ int sn;
  if (threadIdx.x == 0) { sm[0] = 0; sm[1] = 1 == 1 ? 0x7fffffff : 0; sn = 0; }
  __syncthreads();
  for (int k = threadIdx.x; k < 4096; k += 1024) {
    const uint32_t a = (uint32_t)g_prb[k], b = (uint32_t)(g_prb[k] >> 32);
    if (a) atomicMax(&sm[0], (int)(now - a));
    if (b) { if (1 == 1) atomicMin(&sm[1], (int)(now - b)); else if (1 == 0) atomicMax(&sm[1], (int)(now - b)); else { atomicAdd(&sm[1], (int)(now - b)); atomicAdd(&sn, 1); } }
  }
  __syncthreads();
  const int q = min(max(sm[0] * 10 - (1 == 2 ? sm[1] * 10 / max(sn, 1) : sm[1] * 10), 0), 30000);
  if (threadIdx.x == 0) while ((int)((uint32_t)__builtin_amdgcn_s_memrealtime() - now) < q) __builtin_amdgcn_s_sleep(2);
}
extern "C" void kernel_launch(void* const* d_in, const int* in_sizes, int n_in,
                              void* d_out, int out_size, void* d_ws, size_t ws_size,
                              hipStream_t stream) {
  kf_all<<<512, 512, 0, stream>>>((const u16*)d_in[0], (const u16*)d_in[1], (const u16*)d_in[2], (const u16*)d_in[3], (const u16*)d_in[4],
                                  (const u16*)d_in[5], (const u16*)d_in[6], (const u16*)d_in[7], (const u16*)d_in[8], (char*)d_ws + (size_t)WSOFF * 1048576,
                                  (const u16*)d_in[10], (const u16*)d_in[11], (const u16*)d_in[12], (const u16*)d_in[13], (u16*)d_out);
  k_probe<<<1, 1024, 0, stream>>>((int*)d_ws);
}
```

```cpp
#include <hip/hip_runtime.h>
#include <hip/hip_bf16.h>
#include <stdint.h>

typedef __attribute__((ext_vector_type(8))) short bf16x8;
typedef __attribute__((ext_vector_type(4))) float f32x4;
typedef uint16_t u16;
typedef __attribute__((ext_vector_type(4))) uint32_t u32x4;
typedef __attribute__((ext_vector_type(2))) uint32_t u32x2;
#define TSD
#define TS(k, i)
#define TSC(i)
#define TSE()
#define TSP
#define TSA

constexpr int L_ = 2048, D_ = 576, N_ = 128, F_ = 4608, C_ = 8;
constexpr int NF = 144;
constexpr uint32_t O_EP = 0;
constexpr uint32_t O_KF = 4096;
constexpr uint32_t O_KEY = O_KF + 8192;
constexpr uint32_t O_SC = O_KEY + 32768;
constexpr uint32_t O_DN = O_SC + 9216;
constexpr uint32_t O_SU = O_DN + 20480;
constexpr uint32_t O_FB = O_SU + 262144;
constexpr uint32_t O_XC = O_FB + 8 * 1152;
constexpr uint32_t SP = 9216 + 128;
constexpr uint32_t O_SL = O_XC + 1024 * 1152;
constexpr uint32_t SLU = 336;
constexpr uint32_t O_U = O_SL + 256 * SLU * 16;
constexpr int UP = 584;
constexpr uint32_t VP = 8192 + 128, VPP = 4096 + 128;
constexpr uint32_t O_V = O_U + 256 * UP * 16, O_VP = O_V + 256 * VP;
static_assert(256 * SLU * 16 >= NF * SP && O_SL % 128 == 0 && O_U % 128 == 0 && O_V % 128 == 0, "");
__host__ __device__ __forceinline__ uint32_t u_at(int tg, int d) { return O_U + 16u * (uint32_t)((tg >> 1) * (UP * 2) + (d >> 2) * 8 + (tg & 1) * 4 + (d & 3)); }
__host__ __device__ __forceinline__ uint32_t v_at(int d, int tg) {
  return (d < 512) ? O_V + (uint32_t)(d >> 1) * VP + 16u * (uint32_t)(2 * tg + (d & 1)) : O_VP + (uint32_t)(d - 512) * VPP + 16u * (uint32_t)tg;
}
#define HAVE_VAT
#define HAVE_VTAG
__device__ __forceinline__ float bf2f(u16 v) { return __uint_as_float((uint32_t)v << 16); }
__device__ __forceinline__ float bflo(uint32_t v) { return __uint_as_float(v << 16); }
__device__ __forceinline__ float bfhi(uint32_t v) { return __uint_as_float(v & 0xffff0000u); }
__device__ __forceinline__ u16 f2bf(float f) { return __builtin_bit_cast(u16, (__bf16)f); }
__device__ __forceinline__ float rbf(float f) { return bf2f(f2bf(f)); }
typedef __attribute__((ext_vector_type(2))) float f32x2;
typedef __attribute__((ext_vector_type(2))) __bf16 bf16x2v;
__device__ __forceinline__ uint32_t pack2(float a, float b) {
  return __builtin_bit_cast(uint32_t, __builtin_convertvector((f32x2){a, b}, bf16x2v));
}

#define DPP_I(v, ctrl, rmask) __builtin_amdgcn_update_dpp(0, (v), (ctrl), (rmask), 0xf, true)
__device__ __forceinline__ float row_sum(float v) {
  v += __int_as_float(DPP_I(__float_as_int(v), 0x111, 0xf));
  v += __int_as_float(DPP_I(__float_as_int(v), 0x112, 0xf));
  v += __int_as_float(DPP_I(__float_as_int(v), 0x114, 0xf));
  v += __int_as_float(DPP_I(__float_as_int(v), 0x118, 0xf));
  return v;
}
__device__ __forceinline__ float rdl(float v, int l) { return __int_as_float(__builtin_amdgcn_readlane(__float_as_int(v), l)); }
__device__ __forceinline__ float wave_sum(float v) {
  v = row_sum(v);
  asm("s_nop 1\n\tv_add_f32_dpp %0, %0, %0 row_bcast:15 row_mask:0xa bank_mask:0xf\n\t"
      "s_nop 1\n\tv_add_f32_dpp %0, %0, %0 row_bcast:31 row_mask:0xc bank_mask:0xf\n\ts_nop 0" : "+v"(v));
  return rdl(v, 63);
}
__device__ __forceinline__ float fold32(float a, float b) {
  const auto r = __builtin_amdgcn_permlane32_swap(__float_as_uint(a), __float_as_uint(b), false, false);
  return __uint_as_float(r[0]) + __uint_as_float(r[1]);
}
__device__ __forceinline__ float fold16(float a, float b) {
  const auto r = __builtin_amdgcn_permlane16_swap(__float_as_uint(a), __float_as_uint(b), false, false);
  return __uint_as_float(r[0]) + __uint_as_float(r[1]);
}
__device__ __forceinline__ void wave_sum2(float& a, float& b) {
  float v = row_sum(fold32(a, b));
  asm("s_nop 1\n\tv_add_f32_dpp %0, %0, %0 row_bcast:15 row_mask:0xa bank_mask:0xf\n\ts_nop 0" : "+v"(v));
  a = rdl(v, 31); b = rdl(v, 63);
}
__device__ __forceinline__ float dot2(uint32_t a, uint32_t b, float c) {
  return __builtin_amdgcn_fdot2_f32_bf16(__builtin_bit_cast(bf16x2v, a), __builtin_bit_cast(bf16x2v, b), c, false);
}
__device__ __forceinline__ uint32_t row_maxu(uint32_t v) {
  v = max(v, (uint32_t)DPP_I((int)v, 0xb1, 0xf));
  v = max(v, (uint32_t)DPP_I((int)v, 0x4e, 0xf));
  v = max(v, (uint32_t)DPP_I((int)v, 0x141, 0xf));
  v = max(v, (uint32_t)DPP_I((int)v, 0x140, 0xf));
  return v;
}
__device__ __forceinline__ uint32_t wave_maxu(uint32_t v) {
  v = max(v, (uint32_t)DPP_I((int)v, 0x111, 0xf));
  v = max(v, (uint32_t)DPP_I((int)v, 0x112, 0xf));
  v = max(v, (uint32_t)DPP_I((int)v, 0x114, 0xf));
  v = max(v, (uint32_t)DPP_I((int)v, 0x118, 0xf));
  v = max(v, (uint32_t)DPP_I((int)v, 0x142, 0xa));
  v = max(v, (uint32_t)DPP_I((int)v, 0x143, 0xc));
  return (uint32_t)__builtin_amdgcn_readlane((int)v, 63);
}

typedef __attribute__((ext_vector_type(8))) _Float16 f16x8;
typedef __attribute__((ext_vector_type(2))) _Float16 f16x2;
__device__ __forceinline__ uint32_t packh(f32x2 v) { return __builtin_bit_cast(uint32_t, __builtin_convertvector(v, f16x2)); }
__device__ __forceinline__ uint32_t packh(float a, float b) { return packh((f32x2){a, b}); }
constexpr int IP = 224, I_X = 128;
__device__ __forceinline__ f32x2 cmul(f32x2 z, f32x2 w) {
  f32x2 r;
  asm("v_pk_mul_f32 %0, %1, %2 op_sel_hi:[0,1]\n\tv_pk_fma_f32 %0, %1, %2, %0 op_sel:[1,1,0] op_sel_hi:[1,0,1] neg_lo:[0,1,0]" : "=&v"(r) : "v"(z), "v"(w));
  return r;
}
__device__ __forceinline__ f32x2 cexp2(float ar2, float air, float l) {
  const float t = air * l;
  return (f32x2){__builtin_amdgcn_cosf(t), __builtin_amdgcn_sinf(t)} * __builtin_amdgcn_exp2f(ar2 * l);
}
__device__ __forceinline__ float state_a(float ldt, float are, float aim, float& ar2, float& air, float& g) {
  const float dt = rbf(__expf(ldt));
  ar2 = dt * are * 1.4426950408889634f;
  air = dt * aim * 0.15915494309189535f;
  air -= __builtin_rintf(air);
  g = 0.f;
  if (__builtin_expect(__any(ar2 > 0.f), 0)) g = fminf(__uint_as_float(wave_maxu(__float_as_uint(fmaxf(ar2, 0.f)))), 1.f);
  return dt;
}
template <int N> __device__ __forceinline__ void put_im(char* p, int im, const f32x2* z) {
  *(u32x4*)p = (u32x4){packh(z[0][0], z[1][0]), packh(z[2][0], z[3][0]), packh(z[4][0], z[5][0]), packh(z[6][0], z[7][0])};
  *(u32x4*)(p + im) = (u32x4){packh(z[0][1], z[1][1]), packh(z[2][1], z[3][1]), packh(z[4][1], z[5][1]), packh(z[6][1], z[7][1])};
}
#define Y_ASM \
  "v_mov_b64 v[40:41], %0\nv_pk_mul_f32 v[42:43], v[40:41], %1 op_sel_hi:[0,1]\nv_pk_mul_f32 v[44:45], %0, %2 op_sel_hi:[0,1]\n" \
  "v_pk_fma_f32 v[42:43], v[40:41], %1, v[42:43] op_sel:[1,1,0] op_sel_hi:[1,0,1] neg_lo:[0,1,0]\n" \
  "v_pk_fma_f32 v[44:45], %0, %2, v[44:45] op_sel:[1,1,0] op_sel_hi:[1,0,1] neg_lo:[0,1,0]\nv_cvt_pk_f16_f32 v56, v40, v42\n" \
  "v_cvt_pk_f16_f32 v60, v41, v43\nv_pk_mul_f32 v[40:41], v[42:43], %1 op_sel_hi:[0,1]\nv_pk_mul_f32 v[48:49], v[44:45], %2 op_sel_hi:[0,1]\n" \
  "v_pk_fma_f32 v[40:41], v[42:43], %1, v[40:41] op_sel:[1,1,0] op_sel_hi:[1,0,1] neg_lo:[0,1,0]\n" \
  "v_pk_fma_f32 v[48:49], v[44:45], %2, v[48:49] op_sel:[1,1,0] op_sel_hi:[1,0,1] neg_lo:[0,1,0]\n" \
  "v_pk_mul_f32 v[42:43], v[40:41], %1 op_sel_hi:[0,1]\nv_pk_mul_f32 v[52:53], v[48:49], %2 op_sel_hi:[0,1]\n" \
  "v_pk_fma_f32 v[42:43], v[40:41], %1, v[42:43] op_sel:[1,1,0] op_sel_hi:[1,0,1] neg_lo:[0,1,0]\n" \
  "v_pk_fma_f32 v[52:53], v[48:49], %2, v[52:53] op_sel:[1,1,0] op_sel_hi:[1,0,1] neg_lo:[0,1,0]\nv_cvt_pk_f16_f32 v57, v40, v42\n" \
  "v_cvt_pk_f16_f32 v61, v41, v43\nv_pk_mul_f32 v[40:41], v[42:43], %1 op_sel_hi:[0,1]\n" \
  "v_pk_fma_f32 v[40:41], v[42:43], %1, v[40:41] op_sel:[1,1,0] op_sel_hi:[1,0,1] neg_lo:[0,1,0]\n" \
  "v_pk_mul_f32 v[42:43], v[40:41], %1 op_sel_hi:[0,1]\nv_pk_mul_f32 v[46:47], v[44:45], %1 op_sel_hi:[0,1]\n" \
  "v_pk_fma_f32 v[42:43], v[40:41], %1, v[42:43] op_sel:[1,1,0] op_sel_hi:[1,0,1] neg_lo:[0,1,0]\n" \
  "v_pk_fma_f32 v[46:47], v[44:45], %1, v[46:47] op_sel:[1,1,0] op_sel_hi:[1,0,1] neg_lo:[0,1,0]\nv_cvt_pk_f16_f32 v58, v40, v42\n" \
  "v_cvt_pk_f16_f32 v62, v41, v43\nv_cvt_pk_f16_f32 v64, v44, v46\nv_cvt_pk_f16_f32 v68, v45, v47\n" \
  "v_pk_mul_f32 v[40:41], v[42:43], %1 op_sel_hi:[0,1]\nv_pk_mul_f32 v[44:45], v[46:47], %1 op_sel_hi:[0,1]\n" \
  "v_pk_fma_f32 v[40:41], v[42:43], %1, v[40:41] op_sel:[1,1,0] op_sel_hi:[1,0,1] neg_lo:[0,1,0]\n" \
  "v_pk_fma_f32 v[44:45], v[46:47], %1, v[44:45] op_sel:[1,1,0] op_sel_hi:[1,0,1] neg_lo:[0,1,0]\n" \
  "v_pk_mul_f32 v[42:43], v[40:41], %1 op_sel_hi:[0,1]\nv_pk_mul_f32 v[46:47], v[44:45], %1 op_sel_hi:[0,1]\n" \
  "v_pk_fma_f32 v[42:43], v[40:41], %1, v[42:43] op_sel:[1,1,0] op_sel_hi:[1,0,1] neg_lo:[0,1,0]\n" \
  "v_pk_fma_f32 v[46:47], v[44:45], %1, v[46:47] op_sel:[1,1,0] op_sel_hi:[1,0,1] neg_lo:[0,1,0]\nv_cvt_pk_f16_f32 v59, v40, v42\n" \
  "v_cvt_pk_f16_f32 v63, v41, v43\nv_cvt_pk_f16_f32 v65, v44, v46\nv_cvt_pk_f16_f32 v69, v45, v47\n" \
  "v_pk_mul_f32 v[44:45], v[46:47], %1 op_sel_hi:[0,1]\nds_write_b128 %3, v[56:59] offset:0\n" \
  "v_pk_fma_f32 v[44:45], v[46:47], %1, v[44:45] op_sel:[1,1,0] op_sel_hi:[1,0,1] neg_lo:[0,1,0]\nds_write_b128 %3, v[60:63] offset:64\n" \
  "v_pk_mul_f32 v[46:47], v[44:45], %1 op_sel_hi:[0,1]\nv_pk_mul_f32 v[50:51], v[48:49], %1 op_sel_hi:[0,1]\n" \
  "v_pk_fma_f32 v[46:47], v[44:45], %1, v[46:47] op_sel:[1,1,0] op_sel_hi:[1,0,1] neg_lo:[0,1,0]\n" \
  "v_pk_fma_f32 v[50:51], v[48:49], %1, v[50:51] op_sel:[1,1,0] op_sel_hi:[1,0,1] neg_lo:[0,1,0]\nv_cvt_pk_f16_f32 v66, v44, v46\n" \
  "v_cvt_pk_f16_f32 v70, v45, v47\nv_cvt_pk_f16_f32 v56, v48, v50\nv_cvt_pk_f16_f32 v60, v49, v51\n" \
  "v_pk_mul_f32 v[44:45], v[46:47], %1 op_sel_hi:[0,1]\nv_pk_mul_f32 v[48:49], v[50:51], %1 op_sel_hi:[0,1]\n" \
  "v_pk_fma_f32 v[44:45], v[46:47], %1, v[44:45] op_sel:[1,1,0] op_sel_hi:[1,0,1] neg_lo:[0,1,0]\n" \
  "v_pk_fma_f32 v[48:49], v[50:51], %1, v[48:49] op_sel:[1,1,0] op_sel_hi:[1,0,1] neg_lo:[0,1,0]\n" \
  "v_pk_mul_f32 v[46:47], v[44:45], %1 op_sel_hi:[0,1]\nv_pk_mul_f32 v[50:51], v[48:49], %1 op_sel_hi:[0,1]\n" \
  "v_pk_fma_f32 v[46:47], v[44:45], %1, v[46:47] op_sel:[1,1,0] op_sel_hi:[1,0,1] neg_lo:[0,1,0]\n" \
  "v_pk_fma_f32 v[50:51], v[48:49], %1, v[50:51] op_sel:[1,1,0] op_sel_hi:[1,0,1] neg_lo:[0,1,0]\nv_cvt_pk_f16_f32 v67, v44, v46\n" \
  "v_cvt_pk_f16_f32 v71, v45, v47\nv_cvt_pk_f16_f32 v57, v48, v50\nv_cvt_pk_f16_f32 v61, v49, v51\n" \
  "v_pk_mul_f32 v[48:49], v[50:51], %1 op_sel_hi:[0,1]\nds_write_b128 %3, v[64:67] offset:16\n" \
  "v_pk_fma_f32 v[48:49], v[50:51], %1, v[48:49] op_sel:[1,1,0] op_sel_hi:[1,0,1] neg_lo:[0,1,0]\nds_write_b128 %3, v[68:71] offset:80\n" \
  "v_pk_mul_f32 v[50:51], v[48:49], %1 op_sel_hi:[0,1]\nv_pk_mul_f32 v[54:55], v[52:53], %1 op_sel_hi:[0,1]\n" \
  "v_pk_fma_f32 v[50:51], v[48:49], %1, v[50:51] op_sel:[1,1,0] op_sel_hi:[1,0,1] neg_lo:[0,1,0]\n" \
  "v_pk_fma_f32 v[54:55], v[52:53], %1, v[54:55] op_sel:[1,1,0] op_sel_hi:[1,0,1] neg_lo:[0,1,0]\nv_cvt_pk_f16_f32 v58, v48, v50\n" \
  "v_cvt_pk_f16_f32 v62, v49, v51\nv_cvt_pk_f16_f32 v64, v52, v54\nv_cvt_pk_f16_f32 v68, v53, v55\n" \
  "v_pk_mul_f32 v[48:49], v[50:51], %1 op_sel_hi:[0,1]\nv_pk_mul_f32 v[52:53], v[54:55], %1 op_sel_hi:[0,1]\n" \
  "v_pk_fma_f32 v[48:49], v[50:51], %1, v[48:49] op_sel:[1,1,0] op_sel_hi:[1,0,1] neg_lo:[0,1,0]\n" \
  "v_pk_fma_f32 v[52:53], v[54:55], %1, v[52:53] op_sel:[1,1,0] op_sel_hi:[1,0,1] neg_lo:[0,1,0]\n" \
  "v_pk_mul_f32 v[50:51], v[48:49], %1 op_sel_hi:[0,1]\nv_pk_mul_f32 v[54:55], v[52:53], %1 op_sel_hi:[0,1]\n" \
  "v_pk_fma_f32 v[50:51], v[48:49], %1, v[50:51] op_sel:[1,1,0] op_sel_hi:[1,0,1] neg_lo:[0,1,0]\n" \
  "v_pk_fma_f32 v[54:55], v[52:53], %1, v[54:55] op_sel:[1,1,0] op_sel_hi:[1,0,1] neg_lo:[0,1,0]\nv_cvt_pk_f16_f32 v59, v48, v50\n" \
  "v_cvt_pk_f16_f32 v63, v49, v51\nv_cvt_pk_f16_f32 v65, v52, v54\nv_cvt_pk_f16_f32 v69, v53, v55\n" \
  "v_pk_mul_f32 v[52:53], v[54:55], %1 op_sel_hi:[0,1]\nds_write_b128 %3, v[56:59] offset:32\n" \
  "v_pk_fma_f32 v[52:53], v[54:55], %1, v[52:53] op_sel:[1,1,0] op_sel_hi:[1,0,1] neg_lo:[0,1,0]\nds_write_b128 %3, v[60:63] offset:96\n" \
  "v_pk_mul_f32 v[54:55], v[52:53], %1 op_sel_hi:[0,1]\n" \
  "v_pk_fma_f32 v[54:55], v[52:53], %1, v[54:55] op_sel:[1,1,0] op_sel_hi:[1,0,1] neg_lo:[0,1,0]\nv_cvt_pk_f16_f32 v66, v52, v54\n" \
  "v_cvt_pk_f16_f32 v70, v53, v55\nv_pk_mul_f32 v[52:53], v[54:55], %1 op_sel_hi:[0,1]\n" \
  "v_pk_fma_f32 v[52:53], v[54:55], %1, v[52:53] op_sel:[1,1,0] op_sel_hi:[1,0,1] neg_lo:[0,1,0]\n" \
  "v_pk_mul_f32 v[54:55], v[52:53], %1 op_sel_hi:[0,1]\n" \
  "v_pk_fma_f32 v[54:55], v[52:53], %1, v[54:55] op_sel:[1,1,0] op_sel_hi:[1,0,1] neg_lo:[0,1,0]\nv_cvt_pk_f16_f32 v67, v52, v54\n" \
  "v_cvt_pk_f16_f32 v71, v53, v55\nds_write_b128 %3, v[64:67] offset:48\nds_write_b128 %3, v[68:71] offset:112\n"
#define Y_CLOB "memory", "v40", "v41", "v42", "v43", "v44", "v45", "v46", "v47", "v48", "v49", "v50", "v51", "v52", "v53", "v54", "v55", "v56", "v57", "v58", "v59", "v60", "v61", "v62", "v63", "v64", "v65", "v66", "v67", "v68", "v69", "v70", "v71"
#define T_ASM \
  "ds_read_b128 %4, %8\nds_read_b64_tr_b16 v[80:81], %7 offset:0\nds_read_b64_tr_b16 v[82:83], %7 offset:64\n" \
  "ds_read_b64_tr_b16 v[84:85], %5 offset:0\nds_read_b64_tr_b16 v[86:87], %5 offset:48\nds_read_b64_tr_b16 v[88:89], %6 offset:0\n" \
  "ds_read_b64_tr_b16 v[90:91], %6 offset:48\nds_read_b64_tr_b16 v[92:93], %7 offset:3584\nds_read_b64_tr_b16 v[94:95], %7 offset:3648\n" \
  "ds_read_b64_tr_b16 v[96:97], %5 offset:3584\nds_read_b64_tr_b16 v[98:99], %5 offset:3632\nds_read_b64_tr_b16 v[100:101], %6 offset:3584\n" \
  "ds_read_b64_tr_b16 v[102:103], %6 offset:3632\nds_read_b64_tr_b16 v[104:105], %7 offset:7168\nds_read_b64_tr_b16 v[106:107], %7 offset:7232\n" \
  "s_waitcnt lgkmcnt(8)\nv_mfma_f32_16x16x32_f16 %0, v[84:87], v[80:83], 0\nds_read_b64_tr_b16 v[108:109], %5 offset:7168\n" \
  "ds_read_b64_tr_b16 v[110:111], %5 offset:7216\nds_read_b64_tr_b16 v[112:113], %6 offset:7168\nv_mfma_f32_16x16x32_f16 %2, v[88:91], v[80:83], 0\n" \
  "ds_read_b64_tr_b16 v[114:115], %6 offset:7216\nds_read_b64_tr_b16 v[116:117], %7 offset:10752\nds_read_b64_tr_b16 v[118:119], %7 offset:10816\n" \
  "s_waitcnt lgkmcnt(8)\nv_mfma_f32_16x16x32_f16 %0, v[96:99], v[92:95], %0\nds_read_b64_tr_b16 v[120:121], %5 offset:10752\n" \
  "ds_read_b64_tr_b16 v[122:123], %5 offset:10800\nds_read_b64_tr_b16 v[124:125], %6 offset:10752\n" \
  "v_mfma_f32_16x16x32_f16 %2, v[100:103], v[92:95], %2\nds_read_b64_tr_b16 v[126:127], %6 offset:10800\n" \
  "ds_read_b64_tr_b16 v[80:81], %7 offset:14336\nds_read_b64_tr_b16 v[82:83], %7 offset:14400\ns_waitcnt lgkmcnt(8)\n" \
  "v_mfma_f32_16x16x32_f16 %0, v[108:111], v[104:107], %0\nds_read_b64_tr_b16 v[84:85], %5 offset:14336\n" \
  "ds_read_b64_tr_b16 v[86:87], %5 offset:14384\nds_read_b64_tr_b16 v[88:89], %6 offset:14336\n" \
  "v_mfma_f32_16x16x32_f16 %2, v[112:115], v[104:107], %2\nds_read_b64_tr_b16 v[90:91], %6 offset:14384\n" \
  "ds_read_b64_tr_b16 v[92:93], %7 offset:17920\nds_read_b64_tr_b16 v[94:95], %7 offset:17984\ns_waitcnt lgkmcnt(8)\n" \
  "v_mfma_f32_16x16x32_f16 %0, v[120:123], v[116:119], %0\nds_read_b64_tr_b16 v[96:97], %5 offset:17920\n" \
  "ds_read_b64_tr_b16 v[98:99], %5 offset:17968\nds_read_b64_tr_b16 v[100:101], %6 offset:17920\n" \
  "v_mfma_f32_16x16x32_f16 %2, v[124:127], v[116:119], %2\nds_read_b64_tr_b16 v[102:103], %6 offset:17968\n" \
  "ds_read_b64_tr_b16 v[104:105], %7 offset:21504\nds_read_b64_tr_b16 v[106:107], %7 offset:21568\ns_waitcnt lgkmcnt(8)\n" \
  "v_mfma_f32_16x16x32_f16 %1, v[84:87], v[80:83], 0\nds_read_b64_tr_b16 v[108:109], %5 offset:21504\nds_read_b64_tr_b16 v[110:111], %5 offset:21552\n" \
  "ds_read_b64_tr_b16 v[112:113], %6 offset:21504\nv_mfma_f32_16x16x32_f16 %3, v[88:91], v[80:83], 0\nds_read_b64_tr_b16 v[114:115], %6 offset:21552\n" \
  "ds_read_b64_tr_b16 v[116:117], %7 offset:25088\nds_read_b64_tr_b16 v[118:119], %7 offset:25152\ns_waitcnt lgkmcnt(8)\n" \
  "v_mfma_f32_16x16x32_f16 %1, v[96:99], v[92:95], %1\nds_read_b64_tr_b16 v[120:121], %5 offset:25088\n" \
  "ds_read_b64_tr_b16 v[122:123], %5 offset:25136\nds_read_b64_tr_b16 v[124:125], %6 offset:25088\n" \
  "v_mfma_f32_16x16x32_f16 %3, v[100:103], v[92:95], %3\nds_read_b64_tr_b16 v[126:127], %6 offset:25136\ns_waitcnt lgkmcnt(6)\n" \
  "v_mfma_f32_16x16x32_f16 %1, v[108:111], v[104:107], %1\nv_mfma_f32_16x16x32_f16 %3, v[112:115], v[104:107], %3\ns_waitcnt lgkmcnt(0)\n" \
  "v_mfma_f32_16x16x32_f16 %1, v[120:123], v[116:119], %1\nv_mfma_f32_16x16x32_f16 %3, v[124:127], v[116:119], %3\n"
#define T_CLOB "memory", "v80", "v81", "v82", "v83", "v84", "v85", "v86", "v87", "v88", "v89", "v90", "v91", "v92", "v93", "v94", "v95", "v96", "v97", "v98", "v99", "v100", "v101", "v102", "v103", "v104", "v105", "v106", "v107", "v108", "v109", "v110", "v111", "v112", "v113", "v114", "v115", "v116", "v117", "v118", "v119", "v120", "v121", "v122", "v123", "v124", "v125", "v126", "v127"
template <class F> __device__ __forceinline__ int gen_y(float ldt, float are, float aim, float bre, float bim, float cre, float cim, uint32_t im, F&& mid TSP) {
  float ar2, air, g;
  const float dt = state_a(ldt, are, aim, ar2, air, g);
  const float dbr = dt * bre, dbi = dt * bim, pr = cre * dbr - cim * dbi, pi = cre * dbi + cim * dbr;
  const f32x2 w8 = cexp2(ar2, -air, 8.f), w64 = cexp2(ar2, -air, 64.f);
  const float pm = fmaxf(fabsf(pr), fabsf(pi));
  int e = -4;
  if (__builtin_expect(__any(!(pm < 0x1p-4f) || ar2 > 0.f) || !__any(pm > 0x1p-12f), 0)) e = (int)(wave_maxu(__float_as_uint(pm)) >> 23) - 126 + (int)ceilf(g * 250.f);
  const f32x2 y0 = {__builtin_amdgcn_ldexpf(pr, 14 - e), __builtin_amdgcn_ldexpf(-pi, 14 - e)};
  mid();
  TS(0, 7);
  asm volatile(Y_ASM : : "v"(y0), "v"(w8), "v"(w64), "v"(im) : Y_CLOB);
  return e;
}
__device__ __forceinline__ int gen_x(float ldt, float are, float aim, char* im) {
  float ar2, air, g;
  state_a(ldt, are, aim, ar2, air, g);
  const int e = min((int)ceilf(g * 1810.f), 300);
  const float s = __builtin_amdgcn_ldexpf(1.f, 15 - e);
  const f32x2 w1 = cexp2(ar2, air, 1.f), w256 = cexp2(ar2, air, 256.f), w2 = cmul(w1, w1);
  f32x2 a[8], b[8], c[8];
  a[0] = (f32x2){s, 0.f}; a[1] = w1 * s; a[2] = w2 * s;
  b[0] = b[1] = w256 * s;
  b[2] = cmul(b[0], w2); a[3] = cmul(a[1], w2); a[4] = cmul(a[2], w2);
  b[3] = b[4] = b[5] = cmul(b[2], w2); a[5] = cmul(a[3], w2); a[6] = cmul(a[4], w2);
  b[6] = cmul(b[3], w2); a[7] = cmul(a[5], w2); c[0] = cmul(b[3], w256);
  b[7] = cmul(b[6], w2); c[2] = cmul(c[0], w256);
  put_im<0>(im + I_X, 48, a);
  c[1] = cmul(b[7], w256); c[3] = cmul(c[1], w256);
  put_im<0>(im + I_X + 16, 48, b);
#pragma unroll
  for (int k = 4; k < 8; ++k) c[k] = cmul(c[k - 1], w256);
  put_im<0>(im + I_X + 32, 48, c);
  return e;
}

template <int I> struct IC { static constexpr int value = I; };
template <int B, int E, class F> __device__ __forceinline__ void static_for(F&& f) {
  if constexpr (B < E) { f(IC<B>{}); static_for<B + 1, E>(f); }
}
typedef __attribute__((ext_vector_type(4))) short s16x4;
__device__ __forceinline__ u32x2 trd(uint32_t a) {
  return __builtin_bit_cast(u32x2, __builtin_amdgcn_ds_read_tr16_b64_v4i16((__attribute__((address_space(3))) s16x4*)(uintptr_t)a));
}
#define BAR() asm volatile("s_waitcnt lgkmcnt(0)\n\ts_barrier" ::: "memory")
#define ST16(off, v) asm volatile("global_store_dwordx4 %0, %1, %2 sc1\n\ts_nop 1" : : "v"(off), "v"(v), "s"(ws) : "memory")
#define ST4(off, v) asm volatile("global_store_dword %0, %1, %2 sc1" : : "v"(off), "v"(v), "s"(ws) : "memory")
#define LD16(r, off) asm volatile("global_load_dwordx4 %0, %1, %2 sc1" : "=v"(r) : "v"(off), "s"(ws) : "memory")
#define LD16W(r, off) asm volatile("global_load_dwordx4 %0, %1, %2 sc1\n\ts_waitcnt vmcnt(0)" : "=&v"(r) : "v"(off), "s"(ws) : "memory")
#define DRAIN() asm volatile("s_waitcnt vmcnt(0)" ::: "memory")
#define CEX(a, b) { const uint32_t hi_ = max(a, b), lo_ = min(a, b); a = hi_; b = lo_; }
#define MMB(a_, b_, c_) __builtin_amdgcn_mfma_f32_16x16x32_bf16(__builtin_bit_cast(bf16x8, a_), __builtin_bit_cast(bf16x8, b_), c_, 0, 0, 0)
#define NTM 0
#define DPOL ""
#define DPOL2 ""
#define DPOL4 ""
constexpr int SPIN_CAP = 1 << 18;
constexpr uint32_t UMASK = 0x40074007u;
__device__ __forceinline__ uint32_t utag(uint32_t T, int j) {
  return ((T >> (3 * j)) & 0x00070007u) | (((j <= 2) ? (T << (2 - j)) : (T >> (j - 2))) & 0x40004000u);
}
__device__ __forceinline__ uint32_t max4(const u32x4& a) { return max(max(a[0], a[1]), max(a[2], a[3])); }
#ifndef ULONG
#define ULONG 8
#endif
__device__ __forceinline__ void until(uint32_t t) {
#pragma unroll 1
  for (int spin = 0; spin < 600; ++spin) {
    const int d = (int)(t - (uint32_t)__builtin_amdgcn_s_memrealtime());
    if (d <= 0) break;
    if (ULONG && d > 4 * ULONG) __builtin_amdgcn_s_sleep(ULONG); else __builtin_amdgcn_s_sleep(1);
  }
}
__device__ __forceinline__ uint32_t latest(uint32_t t) {
  const uint32_t t0 = (uint32_t)__builtin_amdgcn_readfirstlane(t) - 0x40000000u;
  return wave_maxu(t - t0) + t0;
}
__device__ __forceinline__ uint32_t gate(const char* ws, uint32_t po, uint32_t T) {
  u32x2 pv;
#pragma unroll 1
  for (int spin = 0; spin < SPIN_CAP; ++spin) {
    asm volatile("global_load_dwordx2 %0, %1, %2 sc1\n\ts_waitcnt vmcnt(0)" : "=&v"(pv) : "v"(po), "s"(ws) : "memory");
    if (!__any(pv[0] != T)) break;
    __builtin_amdgcn_s_sleep(2);
  }
  return pv[1];
}
__device__ __forceinline__ uint32_t gate2(const char* ws, uint32_t oa, uint32_t da, uint32_t ob, uint32_t T) {
  u32x2 pa, pb;
#pragma unroll 1
  for (int spin = 0; spin < SPIN_CAP; ++spin) {
    asm volatile("global_load_dwordx2 %0, %2, %4 sc1\n\tglobal_load_dwordx2 %1, %3, %4 sc1\n\ts_waitcnt vmcnt(0)" : "=&v"(pa), "=&v"(pb) : "v"(oa), "v"(ob), "s"(ws) : "memory");
    if (!__any(pa[0] != T || pb[0] != T)) break;
    __builtin_amdgcn_s_sleep(4);
  }
  pa[1] += da;
  return latest(((int)(pa[1] - pb[1]) > 0) ? pa[1] : pb[1]);
}
__device__ __forceinline__ void gsync(uint32_t a, uint32_t target) {
  uint32_t t, c, n = SPIN_CAP;
  uint64_t ex;
  asm volatile("s_waitcnt lgkmcnt(0)\n\ts_mov_b64 %3, exec\n\ts_mov_b64 exec, 1\n\tv_mov_b32 %0, 1\n\tds_add_u32 %4, %0\n\ts_mov_b64 exec, %3\n"
               "1:\n\ts_sleep 1\n\tds_read_b32 %0, %4\n\ts_sub_u32 %2, %2, 1\n\ts_cbranch_scc1 2f\n\ts_waitcnt lgkmcnt(0)\n\tv_readfirstlane_b32 %1, %0\n\t"
               "s_cmp_lt_u32 %1, %5\n\ts_cbranch_scc1 1b\n2:\n\ts_waitcnt lgkmcnt(0)"
               : "=&v"(t), "=&s"(c), "+s"(n), "=&s"(ex) : "v"(a), "n"(target) : "memory", "scc");
}
__device__ __forceinline__ void gsync0(uint32_t a, uint32_t target) {
  uint32_t t, c, n = SPIN_CAP;
  asm volatile("s_waitcnt lgkmcnt(0)\n\ts_mov_b64 exec, 1\n\tv_mov_b32 %0, 1\n\tds_add_u32 %3, %0\n\ts_mov_b64 exec, -1\n"
               "1:\n\tds_read_b32 %0, %3\n\ts_sub_u32 %2, %2, 1\n\ts_cbranch_scc1 2f\n\ts_waitcnt lgkmcnt(0)\n\tv_readfirstlane_b32 %1, %0\n\t"
               "s_cmp_lt_u32 %1, %4\n\ts_cbranch_scc1 1b\n2:\n\ts_waitcnt lgkmcnt(0)"
               : "=&v"(t), "=&s"(c), "+s"(n) : "v"(a), "n"(target) : "memory", "scc");
}
__device__ __forceinline__ void gwait(uint32_t a, uint32_t target) {
  uint32_t t, c, n = SPIN_CAP;
  asm volatile("1:\n\ts_sleep 2\n\tds_read_b32 %0, %3\n\ts_sub_u32 %2, %2, 1\n\ts_cbranch_scc1 2f\n\ts_waitcnt lgkmcnt(0)\n\tv_readfirstlane_b32 %1, %0\n\t"
               "s_cmp_lt_u32 %1, %4\n\ts_cbranch_scc1 1b\n2:\n\ts_waitcnt lgkmcnt(0)"
               : "=&v"(t), "=&s"(c), "+s"(n) : "v"(a), "n"(target) : "memory", "scc");
}
__device__ __forceinline__ void gwait0(uint32_t a, uint32_t target) {
  uint32_t t, c, n = SPIN_CAP;
  asm volatile("1:\n\tds_read_b32 %0, %3\n\ts_sub_u32 %2, %2, 1\n\ts_cbranch_scc1 2f\n\ts_waitcnt lgkmcnt(0)\n\tv_readfirstlane_b32 %1, %0\n\t"
               "s_cmp_lt_u32 %1, %4\n\ts_cbranch_scc1 1b\n2:\n\ts_waitcnt lgkmcnt(0)"
               : "=&v"(t), "=&s"(c), "+s"(n) : "v"(a), "n"(target) : "memory", "scc");
}
__device__ __forceinline__ void dma16(const void* base, uint32_t voff, uint32_t lds) {
  uint32_t keep;
  asm volatile("s_mov_b32 %0, m0\n\ts_mov_b32 m0, %3\n\ts_nop 0\n\tglobal_load_lds_dwordx4 %1, %2" DPOL "\n\ts_mov_b32 m0, %0" : "=&s"(keep) : "v"(voff), "s"(base), "s"(lds) : "memory");
}
__device__ __forceinline__ void dma16d(const void* base, uint32_t voff, uint32_t lds) {
  uint32_t keep;
  asm volatile("s_mov_b32 %0, m0\n\ts_mov_b32 m0, %3\n\ts_nop 0\n\tglobal_load_lds_dwordx4 %1, %2" DPOL2 "\n\ts_mov_b32 m0, %0" : "=&s"(keep) : "v"(voff), "s"(base), "s"(lds) : "memory");
}
__device__ __forceinline__ void nap(int n) {
  for (; n > 15; n -= 15) __builtin_amdgcn_s_sleep(15);
  switch (n) {
    case 1: __builtin_amdgcn_s_sleep(1); break; case 2: __builtin_amdgcn_s_sleep(2); break; case 3: __builtin_amdgcn_s_sleep(3); break;
    case 4: __builtin_amdgcn_s_sleep(4); break; case 5: __builtin_amdgcn_s_sleep(5); break; case 6: __builtin_amdgcn_s_sleep(6); break;
    case 7: __builtin_amdgcn_s_sleep(7); break; case 8: __builtin_amdgcn_s_sleep(8); break; case 9: __builtin_amdgcn_s_sleep(9); break;
    case 10: __builtin_amdgcn_s_sleep(10); break; case 11: __builtin_amdgcn_s_sleep(11); break; case 12: __builtin_amdgcn_s_sleep(12); break;
    case 13: __builtin_amdgcn_s_sleep(13); break; case 14: __builtin_amdgcn_s_sleep(14); break; case 15: __builtin_amdgcn_s_sleep(15); break;
    default: break;
  }
}
#define SLEEP(n) do { if ((n) > 0) __builtin_amdgcn_s_sleep(n); } while (0)
#define USLEEP 3
#define SSLEEP 14
#define PSLEEP 2
#define PPRIO 0
#define LFIRST 1
#define WD 40
#define OSLEEP 12
#define QPRIO 1
#define KPRIO 2
#define GSLEEP 0
#define NSLEEP 0
#define UD 40
#define XD 0
#define RSLEEP 0
#define UPRIO 2
#define VD2 385
#define WDEPTH 6
#define ESLEEP 0
#define SD 58
#define KD 180
#define OD 0
#define KBACK 6
#define DDEPTH 6
#define C3 1
#define XROT 2
#define WSOFF 0
#define KN 1
#define RPF 1
#define LSLEEP 1
#define TSTOP 78
#define HSTOP 110
#define VEARLY 0ull
#define FPR 3
#define SPR 3
#define DPRIO 0
#define QPRIO2 3
#define HW 2
#define RD 57
#define OPOS 1
#define OD2 6
#define NA 12
#define RD2 88
#define ORD1 ((unsigned __int128)0x321087654u << 36 | 0x321087654ull)
#define WDEPTH2 5
#define RQ NA
#define HREV 1
#define QPRIO0 3
#define VPS 1
#define RW 0
#define NPRIO 0
#define XSL 10
#define V2D 24
#define N0 8
#define LPOS 1
#define ALLP 1
#define FORCESLOW 0
#define FORCEFB 0
#define CONV_ASM \
  "s_cmp_eq_u32 s89, 1\ns_cbranch_scc1 1f\ns_cmp_gt_u32 s89, 1\ns_cbranch_scc1 9f\nds_read_b64 v[124:125], v3 offset:576\n" \
  "ds_read_b64 v[126:127], v3 offset:584\nds_read_b64 v[120:121], v3 offset:64\nds_read_b64 v[122:123], v3 offset:72\n" \
  "ds_read_b64 v[116:117], v3 offset:640\nds_read_b64 v[118:119], v3 offset:648\nds_read_b64 v[112:113], v3 offset:128\n" \
  "ds_read_b64 v[114:115], v3 offset:136\nds_read_b64 v[108:109], v3 offset:704\nds_read_b64 v[110:111], v3 offset:712\n" \
  "ds_read_b64 v[104:105], v3 offset:192\nds_read_b64 v[106:107], v3 offset:200\nds_read_b64 v[100:101], v3 offset:768\n" \
  "ds_read_b64 v[102:103], v3 offset:776\nds_read_b64 v[96:97], v3 offset:256\nds_read_b64 v[98:99], v3 offset:264\n" \
  "ds_read_b64 v[92:93], v3 offset:832\nds_read_b64 v[94:95], v3 offset:840\nds_read_b64 v[88:89], v3 offset:320\n" \
  "ds_read_b64 v[90:91], v3 offset:328\nds_read_b64 v[84:85], v3 offset:896\nds_read_b64 v[86:87], v3 offset:904\n" \
  "ds_read_b64 v[80:81], v3 offset:384\nds_read_b64 v[82:83], v3 offset:392\nds_read_b64 v[76:77], v3 offset:960\n" \
  "ds_read_b64 v[78:79], v3 offset:968\nds_read_b64 v[72:73], v3 offset:448\nds_read_b64 v[74:75], v3 offset:456\n" \
  "ds_read_b64 v[68:69], v3 offset:1024\nds_read_b64 v[70:71], v3 offset:1032\nds_read_b64 v[64:65], v3 offset:512\n" \
  "ds_read_b64 v[66:67], v3 offset:520\nds_read_b64 v[60:61], v3 offset:0\nds_read_b64 v[62:63], v3 offset:8\nds_read_b64 v[56:57], v3 offset:1088\n" \
  "ds_read_b64 v[58:59], v3 offset:1096\ns_waitcnt lgkmcnt(0)\ns_barrier\nds_read_b128 v[36:39], v2 offset:0\nds_read_b128 v[40:43], v2 offset:64\n" \
  "ds_read_b128 v[44:47], v2 offset:128\nds_read_b128 v[48:51], v2 offset:192\ns_waitcnt lgkmcnt(3)\n" \
  "v_mfma_f32_16x16x32_f16 v[12:15], v[124:127], v[36:39], 0\ns_setprio 1\nds_read_b128 v[52:55], v2 offset:256\n" \
  "v_mfma_f32_16x16x32_f16 v[16:19], v[120:123], v[36:39], 0\ns_waitcnt lgkmcnt(3)\nv_mfma_f32_16x16x32_f16 v[12:15], v[116:119], v[40:43], v[12:15]\n" \
  "ds_read_b128 v[36:39], v2 offset:320\nv_mfma_f32_16x16x32_f16 v[16:19], v[112:115], v[40:43], v[16:19]\ns_waitcnt lgkmcnt(3)\n" \
  "v_mfma_f32_16x16x32_f16 v[12:15], v[108:111], v[44:47], v[12:15]\nds_read_b128 v[40:43], v2 offset:384\n" \
  "v_mfma_f32_16x16x32_f16 v[16:19], v[104:107], v[44:47], v[16:19]\ns_waitcnt lgkmcnt(3)\n" \
  "v_mfma_f32_16x16x32_f16 v[12:15], v[100:103], v[48:51], v[12:15]\nds_read_b128 v[44:47], v2 offset:448\n" \
  "v_mfma_f32_16x16x32_f16 v[16:19], v[96:99], v[48:51], v[16:19]\ns_waitcnt lgkmcnt(3)\n" \
  "v_mfma_f32_16x16x32_f16 v[12:15], v[92:95], v[52:55], v[12:15]\nv_mfma_f32_16x16x32_f16 v[16:19], v[88:91], v[52:55], v[16:19]\n" \
  "s_waitcnt lgkmcnt(2)\nv_mfma_f32_16x16x32_f16 v[12:15], v[84:87], v[36:39], v[12:15]\nds_read_b128 v[48:51], v2 offset:512\n" \
  "v_mfma_f32_16x16x32_f16 v[16:19], v[80:83], v[36:39], v[16:19]\ns_waitcnt lgkmcnt(2)\n" \
  "v_mfma_f32_16x16x32_f16 v[12:15], v[76:79], v[40:43], v[12:15]\nds_read_b128 v[52:55], v2 offset:576\n" \
  "v_mfma_f32_16x16x32_f16 v[16:19], v[72:75], v[40:43], v[16:19]\ns_waitcnt lgkmcnt(2)\n" \
  "v_mfma_f32_16x16x32_f16 v[12:15], v[68:71], v[44:47], v[12:15]\nds_read_b128 v[36:39], v2 offset:640\n" \
  "v_mfma_f32_16x16x32_f16 v[16:19], v[64:67], v[44:47], v[16:19]\nv_mfma_f32_16x16x32_f16 v[20:23], v[60:63], v[44:47], 0\n" \
  "ds_read_b128 v[40:43], v2 offset:704\nv_mfma_f32_16x16x32_f16 v[28:31], v[56:59], v[44:47], 0\ns_waitcnt lgkmcnt(3)\n" \
  "v_mfma_f32_16x16x32_f16 v[16:19], v[124:127], v[48:51], v[16:19]\nds_read_b128 v[56:59], v2 offset:768\nv_cvt_pkrtz_f16_f32 v32, v12, v13\n" \
  "v_mfma_f32_16x16x32_f16 v[20:23], v[120:123], v[48:51], v[20:23]\nv_cvt_pkrtz_f16_f32 v33, v14, v15\ns_waitcnt lgkmcnt(3)\n" \
  "v_mfma_f32_16x16x32_f16 v[16:19], v[116:119], v[52:55], v[16:19]\nds_read_b128 v[44:47], v2 offset:832\n" \
  "v_pk_add_u16 v32, v32, 4 op_sel_hi:[1,0] clamp\nv_mfma_f32_16x16x32_f16 v[20:23], v[112:115], v[52:55], v[20:23]\n" \
  "v_pk_add_u16 v33, v33, 4 op_sel_hi:[1,0] clamp\ns_waitcnt lgkmcnt(3)\nv_mfma_f32_16x16x32_f16 v[16:19], v[108:111], v[36:39], v[16:19]\n" \
  "ds_read_b128 v[48:51], v2 offset:896\nds_write_b64 v4, v[32:33] offset:0\nv_mfma_f32_16x16x32_f16 v[20:23], v[104:107], v[36:39], v[20:23]\n" \
  "s_waitcnt lgkmcnt(3)\nv_mfma_f32_16x16x32_f16 v[16:19], v[100:103], v[40:43], v[16:19]\nds_read_b128 v[52:55], v2 offset:960\n" \
  "v_mfma_f32_16x16x32_f16 v[20:23], v[96:99], v[40:43], v[20:23]\nv_mfma_f32_16x16x32_f16 v[16:19], v[92:95], v[56:59], v[16:19]\n" \
  "v_mfma_f32_16x16x32_f16 v[20:23], v[88:91], v[56:59], v[20:23]\nds_read_b128 v[36:39], v2 offset:1024\ns_waitcnt lgkmcnt(3)\n" \
  "v_mfma_f32_16x16x32_f16 v[16:19], v[84:87], v[44:47], v[16:19]\nv_mfma_f32_16x16x32_f16 v[20:23], v[80:83], v[44:47], v[20:23]\n" \
  "ds_read_b128 v[40:43], v2 offset:1088\nds_read_b64 v[56:57], v6 offset:256\nds_read_b64 v[58:59], v6 offset:264\n" \
  "v_mfma_f32_16x16x32_f16 v[16:19], v[76:79], v[48:51], v[16:19]\nv_mfma_f32_16x16x32_f16 v[20:23], v[72:75], v[48:51], v[20:23]\n" \
  "ds_read_b128 v[44:47], v2 offset:1152\ns_waitcnt lgkmcnt(4)\nv_mfma_f32_16x16x32_f16 v[16:19], v[68:71], v[52:55], v[16:19]\n" \
  "v_mfma_f32_16x16x32_f16 v[20:23], v[64:67], v[52:55], v[20:23]\nds_read_b128 v[48:51], v2 offset:1216\n" \
  "v_mfma_f32_16x16x32_f16 v[24:27], v[60:63], v[52:55], 0\nv_mfma_f32_16x16x32_f16 v[20:23], v[124:127], v[36:39], v[20:23]\n" \
  "ds_read_b128 v[52:55], v2 offset:1280\nv_mfma_f32_16x16x32_f16 v[24:27], v[120:123], v[36:39], v[24:27]\ns_waitcnt lgkmcnt(5)\n" \
  "v_mfma_f32_16x16x32_f16 v[20:23], v[116:119], v[40:43], v[20:23]\nds_read_b128 v[36:39], v2 offset:1344\nv_cvt_pkrtz_f16_f32 v32, v16, v17\n" \
  "v_mfma_f32_16x16x32_f16 v[24:27], v[112:115], v[40:43], v[24:27]\nv_cvt_pkrtz_f16_f32 v33, v18, v19\ns_waitcnt lgkmcnt(3)\n" \
  "v_mfma_f32_16x16x32_f16 v[20:23], v[108:111], v[44:47], v[20:23]\nds_read_b128 v[40:43], v2 offset:1408\n" \
  "v_pk_add_u16 v32, v32, 4 op_sel_hi:[1,0] clamp\nds_read_b32 v35, v9 offset:32\nv_mfma_f32_16x16x32_f16 v[24:27], v[104:107], v[44:47], v[24:27]\n" \
  "v_pk_add_u16 v33, v33, 4 op_sel_hi:[1,0] clamp\ns_waitcnt lgkmcnt(3)\nv_mfma_f32_16x16x32_f16 v[20:23], v[100:103], v[48:51], v[20:23]\n" \
  "ds_read_b128 v[44:47], v2 offset:1472\nds_write_b64 v4, v[32:33] offset:1024\nv_mfma_f32_16x16x32_f16 v[24:27], v[96:99], v[48:51], v[24:27]\n" \
  "v_mfma_f32_16x16x32_f16 v[20:23], v[92:95], v[52:55], v[20:23]\nv_mfma_f32_16x16x32_f16 v[24:27], v[88:91], v[52:55], v[24:27]\n" \
  "ds_read_b128 v[48:51], v2 offset:1536\ns_waitcnt lgkmcnt(3)\nv_readfirstlane_b32 s85, v35\ns_cmp_lt_u32 s85, 4\ns_cbranch_scc1 21f\n22:\n" \
  "v_mfma_f32_16x16x32_f16 v[20:23], v[84:87], v[36:39], v[20:23]\nv_mfma_f32_16x16x32_f16 v[24:27], v[80:83], v[36:39], v[24:27]\n" \
  "ds_read_b128 v[52:55], v2 offset:1600\nv_mfma_f32_16x16x32_f16 v[20:23], v[76:79], v[40:43], v[20:23]\n" \
  "v_mfma_f32_16x16x32_f16 v[24:27], v[72:75], v[40:43], v[24:27]\nds_read_b128 v[36:39], v2 offset:1664\ns_waitcnt lgkmcnt(4)\n" \
  "v_mfma_f32_16x16x32_f16 v[20:23], v[68:71], v[44:47], v[20:23]\nv_mfma_f32_16x16x32_f16 v[24:27], v[64:67], v[44:47], v[24:27]\n" \
  "ds_read_b128 v[40:43], v2 offset:1728\nv_mfma_f32_16x16x32_f16 v[28:31], v[60:63], v[44:47], v[28:31]\nds_read_b64_tr_b16 v[60:61], v5 offset:0\n" \
  "ds_read_b64_tr_b16 v[62:63], v5 offset:32\ns_waitcnt lgkmcnt(4)\nv_mfma_f32_16x16x32_f16 v[24:27], v[124:127], v[48:51], v[24:27]\n" \
  "v_mfma_f32_16x16x32_f16 v[28:31], v[120:123], v[48:51], v[28:31]\nds_read_b64_tr_b16 v[44:45], v5 offset:256\n" \
  "ds_read_b64_tr_b16 v[46:47], v5 offset:288\nv_mfma_f32_16x16x32_f16 v[24:27], v[116:119], v[52:55], v[24:27]\n" \
  "ds_read_b64 v[120:121], v6 offset:320\nds_read_b64 v[122:123], v6 offset:328\nv_mfma_f32_16x16x32_f16 v[28:31], v[112:115], v[52:55], v[28:31]\n" \
  "ds_read_b128 v[48:51], v2 offset:1792\ns_waitcnt lgkmcnt(7)\nv_mfma_f32_16x16x32_f16 v[24:27], v[108:111], v[36:39], v[24:27]\n" \
  "ds_read_b128 v[112:115], v2 offset:1856\nv_mfma_f32_16x16x32_f16 v[28:31], v[104:107], v[36:39], v[28:31]\nds_read_b64 v[52:53], v6 offset:128\n" \
  "ds_read_b64 v[54:55], v6 offset:136\nv_mfma_f32_16x16x32_f16 v[24:27], v[100:103], v[40:43], v[24:27]\nds_read_b128 v[104:107], v2 offset:1920\n" \
  "v_mfma_f32_16x16x32_f16 v[28:31], v[96:99], v[40:43], v[28:31]\nds_read_b64 v[36:37], v6 offset:192\nds_read_b64 v[38:39], v6 offset:200\n" \
  "s_waitcnt lgkmcnt(9)\nv_mfma_f32_16x16x32_f16 v[20:23], v[60:63], v[56:59], v[20:23]\nds_read_b128 v[96:99], v2 offset:1984\ns_waitcnt lgkmcnt(7)\n" \
  "v_mfma_f32_16x16x32_f16 v[20:23], v[44:47], v[120:123], v[20:23]\nv_mfma_f32_16x16x32_f16 v[24:27], v[92:95], v[48:51], v[24:27]\n" \
  "ds_read_b128 v[40:43], v2 offset:2048\nv_mfma_f32_16x16x32_f16 v[28:31], v[88:91], v[48:51], v[28:31]\nds_read_b64_tr_b16 v[88:89], v5 offset:512\n" \
  "ds_read_b64_tr_b16 v[90:91], v5 offset:544\ns_waitcnt lgkmcnt(6)\nv_mfma_f32_16x16x32_f16 v[24:27], v[84:87], v[112:115], v[24:27]\n" \
  "v_mfma_f32_16x16x32_f16 v[28:31], v[80:83], v[112:115], v[28:31]\nv_cvt_pkrtz_f16_f32 v32, v20, v21\nv_cvt_pkrtz_f16_f32 v33, v22, v23\n" \
  "v_mfma_f32_16x16x32_f16 v[24:27], v[76:79], v[104:107], v[24:27]\nds_read_b64_tr_b16 v[48:49], v5 offset:768\n" \
  "ds_read_b64_tr_b16 v[50:51], v5 offset:800\nv_mfma_f32_16x16x32_f16 v[28:31], v[72:75], v[104:107], v[28:31]\n" \
  "ds_read_b128 v[80:83], v2 offset:2112\ns_waitcnt lgkmcnt(5)\nv_mfma_f32_16x16x32_f16 v[24:27], v[68:71], v[96:99], v[24:27]\n" \
  "ds_read_b128 v[112:115], v2 offset:2176\nv_pk_add_u16 v32, v32, 4 op_sel_hi:[1,0] clamp\n" \
  "v_mfma_f32_16x16x32_f16 v[28:31], v[64:67], v[96:99], v[28:31]\nds_read_b128 v[72:75], v2 offset:2240\n" \
  "v_pk_add_u16 v33, v33, 4 op_sel_hi:[1,0] clamp\nv_mfma_f32_16x16x32_f16 v[28:31], v[124:127], v[40:43], v[28:31]\n" \
  "ds_read_b128 v[104:107], v2 offset:2304\nds_write_b64 v4, v[32:33] offset:2048\nv_mfma_f32_16x16x32_f16 v[24:27], v[60:63], v[52:55], v[24:27]\n" \
  "ds_read_b128 v[64:67], v2 offset:2368\nds_read_b64 v[96:97], v6 offset:0\nds_read_b64 v[98:99], v6 offset:8\n" \
  "v_mfma_f32_16x16x32_f16 v[24:27], v[44:47], v[36:39], v[24:27]\nds_read_b128 v[124:127], v2 offset:2432\ns_waitcnt lgkmcnt(9)\n" \
  "v_mfma_f32_16x16x32_f16 v[24:27], v[88:91], v[56:59], v[24:27]\nds_read_b128 v[40:43], v2 offset:2496\n" \
  "v_mfma_f32_16x16x32_f16 v[24:27], v[48:51], v[120:123], v[24:27]\ns_waitcnt lgkmcnt(8)\n" \
  "v_mfma_f32_16x16x32_f16 v[28:31], v[116:119], v[80:83], v[28:31]\nds_read_b64 v[116:117], v6 offset:64\nds_read_b64 v[118:119], v6 offset:72\n" \
  "v_mfma_f32_16x16x32_f16 v[28:31], v[108:111], v[112:115], v[28:31]\nds_read_b64_tr_b16 v[80:81], v5 offset:1024\n" \
  "ds_read_b64_tr_b16 v[82:83], v5 offset:1056\ns_waitcnt lgkmcnt(10)\nv_mfma_f32_16x16x32_f16 v[28:31], v[100:103], v[72:75], v[28:31]\n" \
  "v_cvt_pkrtz_f16_f32 v32, v24, v25\nv_cvt_pkrtz_f16_f32 v33, v26, v27\nv_mfma_f32_16x16x32_f16 v[28:31], v[92:95], v[104:107], v[28:31]\n" \
  "v_pk_add_u16 v32, v32, 4 op_sel_hi:[1,0] clamp\ns_waitcnt lgkmcnt(5)\nv_mfma_f32_16x16x32_f16 v[28:31], v[84:87], v[64:67], v[28:31]\n" \
  "ds_read_b64_tr_b16 v[108:109], v5 offset:1280\nds_read_b64_tr_b16 v[110:111], v5 offset:1312\n" \
  "v_mfma_f32_16x16x32_f16 v[28:31], v[76:79], v[124:127], v[28:31]\nds_read_b64_tr_b16 v[112:113], v7 offset:0\n" \
  "ds_read_b64_tr_b16 v[114:115], v7 offset:64\ns_waitcnt lgkmcnt(6)\nv_mfma_f32_16x16x32_f16 v[28:31], v[68:71], v[40:43], v[28:31]\n" \
  "ds_read2_b32 v[100:101], v8 offset0:48 offset1:49\nds_read2_b32 v[102:103], v8 offset0:50 offset1:51\n" \
  "v_mfma_f32_16x16x32_f16 v[28:31], v[60:63], v[96:99], v[28:31]\nv_pk_add_u16 v33, v33, 4 op_sel_hi:[1,0] clamp\n" \
  "ds_write_b64 v4, v[32:33] offset:3072\nv_mfma_f32_16x16x32_f16 v[28:31], v[44:47], v[116:119], v[28:31]\n" \
  "v_mfma_f32_16x16x32_f16 v[28:31], v[88:91], v[52:55], v[28:31]\nv_mfma_f32_16x16x32_f16 v[28:31], v[48:51], v[36:39], v[28:31]\n" \
  "s_waitcnt lgkmcnt(5)\nv_mfma_f32_16x16x32_f16 v[28:31], v[80:83], v[56:59], v[28:31]\n" \
  "v_mfma_f32_16x16x32_f16 v[28:31], v[108:111], v[120:123], v[28:31]\ns_waitcnt lgkmcnt(1)\n" \
  "v_mfma_f32_16x16x32_f16 v[28:31], v[112:115], v[100:103], v[28:31]\ns_nop 7\nv_cvt_pkrtz_f16_f32 v32, v28, v29\nv_cvt_pkrtz_f16_f32 v33, v30, v31\n" \
  "v_pk_add_u16 v32, v32, 4 op_sel_hi:[1,0] clamp\nv_pk_add_u16 v33, v33, 4 op_sel_hi:[1,0] clamp\nds_write_b64 v4, v[32:33] offset:4096\n" \
  "s_setprio 0\ns_setpc_b64 s[90:91]\n21:\ns_movk_i32 s84, 0x4000\n23:\nds_read_b32 v35, v9 offset:32\ns_sub_u32 s84, s84, 1\ns_cbranch_scc1 22b\n" \
  "s_waitcnt lgkmcnt(0)\nv_readfirstlane_b32 s85, v35\ns_cmp_lt_u32 s85, 4\ns_cbranch_scc1 23b\ns_branch 22b\n1:\n" \
  "ds_read_b64 v[124:125], v3 offset:1088\nds_read_b64 v[126:127], v3 offset:1096\nds_read_b64 v[120:121], v3 offset:0\n" \
  "ds_read_b64 v[122:123], v3 offset:8\nds_read_b64 v[116:117], v3 offset:64\nds_read_b64 v[118:119], v3 offset:72\n" \
  "ds_read_b64 v[112:113], v3 offset:128\nds_read_b64 v[114:115], v3 offset:136\nds_read_b64 v[108:109], v3 offset:192\n" \
  "ds_read_b64 v[110:111], v3 offset:200\nds_read_b64 v[104:105], v3 offset:256\nds_read_b64 v[106:107], v3 offset:264\n" \
  "ds_read_b64 v[100:101], v3 offset:320\nds_read_b64 v[102:103], v3 offset:328\nds_read_b64 v[96:97], v3 offset:384\n" \
  "ds_read_b64 v[98:99], v3 offset:392\nds_read_b64 v[92:93], v3 offset:448\nds_read_b64 v[94:95], v3 offset:456\n" \
  "ds_read_b64 v[88:89], v3 offset:512\nds_read_b64 v[90:91], v3 offset:520\nds_read_b64 v[84:85], v3 offset:576\n" \
  "ds_read_b64 v[86:87], v3 offset:584\nds_read_b64 v[80:81], v3 offset:640\nds_read_b64 v[82:83], v3 offset:648\n" \
  "ds_read_b64 v[76:77], v3 offset:704\nds_read_b64 v[78:79], v3 offset:712\nds_read_b64 v[72:73], v3 offset:768\n" \
  "ds_read_b64 v[74:75], v3 offset:776\nds_read_b64 v[68:69], v3 offset:832\nds_read_b64 v[70:71], v3 offset:840\n" \
  "ds_read_b64 v[64:65], v3 offset:896\nds_read_b64 v[66:67], v3 offset:904\nds_read_b64 v[60:61], v3 offset:960\n" \
  "ds_read_b64 v[62:63], v3 offset:968\nds_read_b64 v[56:57], v3 offset:1024\nds_read_b64 v[58:59], v3 offset:1032\n" \
  "ds_read_b64 v[52:53], v6 offset:0\nds_read_b64 v[54:55], v6 offset:8\nds_read_b64 v[48:49], v6 offset:64\nds_read_b64 v[50:51], v6 offset:72\n" \
  "s_waitcnt lgkmcnt(0)\ns_barrier\nds_read_b128 v[28:31], v2 offset:960\nds_read_b128 v[32:35], v2 offset:1472\n" \
  "ds_read_b128 v[36:39], v2 offset:1984\nds_read_b128 v[40:43], v2 offset:2048\nds_read_b128 v[44:47], v2 offset:2112\ns_waitcnt lgkmcnt(3)\n" \
  "v_mfma_f32_16x16x32_f16 v[12:15], v[124:127], v[28:31], 0\ns_setprio 1\nds_read_b128 v[28:31], v2 offset:2176\n" \
  "v_mfma_f32_16x16x32_f16 v[16:19], v[124:127], v[32:35], 0\nds_read_b128 v[32:35], v2 offset:2240\ns_waitcnt lgkmcnt(3)\n" \
  "v_mfma_f32_16x16x32_f16 v[12:15], v[120:123], v[36:39], v[12:15]\nv_mfma_f32_16x16x32_f16 v[20:23], v[124:127], v[36:39], 0\n" \
  "ds_read_b128 v[124:127], v2 offset:2304\nds_read_b128 v[36:39], v2 offset:2368\nv_mfma_f32_16x16x32_f16 v[12:15], v[116:119], v[40:43], v[12:15]\n" \
  "ds_read_b128 v[40:43], v2 offset:2432\ns_waitcnt lgkmcnt(4)\nv_mfma_f32_16x16x32_f16 v[12:15], v[112:115], v[44:47], v[12:15]\n" \
  "ds_read_b128 v[44:47], v2 offset:2496\nv_mfma_f32_16x16x32_f16 v[12:15], v[108:111], v[28:31], v[12:15]\nds_read_b128 v[28:31], v2 offset:2560\n" \
  "s_waitcnt lgkmcnt(4)\nv_mfma_f32_16x16x32_f16 v[12:15], v[104:107], v[32:35], v[12:15]\nds_read_b128 v[32:35], v2 offset:2624\n" \
  "v_mfma_f32_16x16x32_f16 v[12:15], v[100:103], v[124:127], v[12:15]\ns_waitcnt lgkmcnt(3)\n" \
  "v_mfma_f32_16x16x32_f16 v[12:15], v[96:99], v[36:39], v[12:15]\nds_read_b128 v[124:127], v2 offset:2688\n" \
  "v_mfma_f32_16x16x32_f16 v[12:15], v[92:95], v[40:43], v[12:15]\ns_waitcnt lgkmcnt(3)\n" \
  "v_mfma_f32_16x16x32_f16 v[12:15], v[88:91], v[44:47], v[12:15]\nds_read_b128 v[36:39], v2 offset:2752\n" \
  "v_mfma_f32_16x16x32_f16 v[16:19], v[120:123], v[44:47], v[16:19]\ns_waitcnt lgkmcnt(3)\n" \
  "v_mfma_f32_16x16x32_f16 v[12:15], v[84:87], v[28:31], v[12:15]\nds_read_b128 v[40:43], v2 offset:2816\n" \
  "v_mfma_f32_16x16x32_f16 v[16:19], v[116:119], v[28:31], v[16:19]\ns_waitcnt lgkmcnt(3)\n" \
  "v_mfma_f32_16x16x32_f16 v[12:15], v[80:83], v[32:35], v[12:15]\nds_read_b128 v[44:47], v2 offset:2880\n" \
  "v_mfma_f32_16x16x32_f16 v[16:19], v[112:115], v[32:35], v[16:19]\ns_waitcnt lgkmcnt(3)\n" \
  "v_mfma_f32_16x16x32_f16 v[12:15], v[76:79], v[124:127], v[12:15]\nds_read_b128 v[28:31], v2 offset:2944\n" \
  "v_mfma_f32_16x16x32_f16 v[16:19], v[108:111], v[124:127], v[16:19]\ns_waitcnt lgkmcnt(3)\n" \
  "v_mfma_f32_16x16x32_f16 v[12:15], v[72:75], v[36:39], v[12:15]\nds_read_b128 v[32:35], v2 offset:3008\n" \
  "v_mfma_f32_16x16x32_f16 v[16:19], v[104:107], v[36:39], v[16:19]\ns_waitcnt lgkmcnt(3)\n" \
  "v_mfma_f32_16x16x32_f16 v[12:15], v[68:71], v[40:43], v[12:15]\nv_mfma_f32_16x16x32_f16 v[16:19], v[100:103], v[40:43], v[16:19]\n" \
  "ds_read_b128 v[124:127], v2 offset:3072\ns_waitcnt lgkmcnt(3)\nv_mfma_f32_16x16x32_f16 v[12:15], v[64:67], v[44:47], v[12:15]\n" \
  "v_mfma_f32_16x16x32_f16 v[16:19], v[96:99], v[44:47], v[16:19]\nds_read_b128 v[36:39], v2 offset:3136\ns_waitcnt lgkmcnt(3)\n" \
  "v_mfma_f32_16x16x32_f16 v[12:15], v[60:63], v[28:31], v[12:15]\nds_read_b64 v[40:41], v6 offset:128\nds_read_b64 v[42:43], v6 offset:136\n" \
  "v_mfma_f32_16x16x32_f16 v[16:19], v[92:95], v[28:31], v[16:19]\nds_read_b128 v[44:47], v2 offset:3200\ns_waitcnt lgkmcnt(4)\n" \
  "v_mfma_f32_16x16x32_f16 v[12:15], v[56:59], v[32:35], v[12:15]\nv_mfma_f32_16x16x32_f16 v[16:19], v[88:91], v[32:35], v[16:19]\n" \
  "ds_read_b128 v[28:31], v2 offset:3264\nv_mfma_f32_16x16x32_f16 v[20:23], v[120:123], v[32:35], v[20:23]\nds_read_b64 v[120:121], v6 offset:192\n" \
  "ds_read_b64 v[122:123], v6 offset:200\nv_mfma_f32_16x16x32_f16 v[16:19], v[84:87], v[124:127], v[16:19]\nds_read_b128 v[32:35], v2 offset:3328\n" \
  "v_mfma_f32_16x16x32_f16 v[20:23], v[116:119], v[124:127], v[20:23]\nds_read_b64 v[116:117], v6 offset:256\nds_read_b64 v[118:119], v6 offset:264\n" \
  "s_waitcnt lgkmcnt(6)\nv_mfma_f32_16x16x32_f16 v[16:19], v[80:83], v[36:39], v[16:19]\nds_read_b128 v[124:127], v2 offset:3392\n" \
  "v_mfma_f32_16x16x32_f16 v[20:23], v[112:115], v[36:39], v[20:23]\nds_read_b64 v[112:113], v6 offset:320\nds_read_b64 v[114:115], v6 offset:328\n" \
  "v_mfma_f32_16x16x32_f16 v[16:19], v[76:79], v[44:47], v[16:19]\nds_read_b128 v[36:39], v2 offset:3456\n" \
  "v_mfma_f32_16x16x32_f16 v[20:23], v[108:111], v[44:47], v[20:23]\ns_waitcnt lgkmcnt(6)\n" \
  "v_mfma_f32_16x16x32_f16 v[16:19], v[72:75], v[28:31], v[16:19]\nds_read_b128 v[108:111], v2 offset:3520\n" \
  "v_mfma_f32_16x16x32_f16 v[20:23], v[104:107], v[28:31], v[20:23]\nv_mfma_f32_16x16x32_f16 v[16:19], v[68:71], v[32:35], v[16:19]\n" \
  "ds_read_b128 v[44:47], v2 offset:3584\nv_mfma_f32_16x16x32_f16 v[20:23], v[100:103], v[32:35], v[20:23]\nds_read_b128 v[104:107], v2 offset:3648\n" \
  "ds_read_b32 v27, v9 offset:32\ns_waitcnt lgkmcnt(4)\nv_mfma_f32_16x16x32_f16 v[16:19], v[64:67], v[124:127], v[16:19]\n" \
  "ds_read_b128 v[28:31], v2 offset:3712\nv_mfma_f32_16x16x32_f16 v[20:23], v[96:99], v[124:127], v[20:23]\nds_read_b128 v[100:103], v2 offset:3776\n" \
  "ds_read2_b32 v[32:33], v8 offset0:32 offset1:33\nds_read2_b32 v[34:35], v8 offset0:34 offset1:35\n" \
  "v_mfma_f32_16x16x32_f16 v[16:19], v[60:63], v[36:39], v[16:19]\nds_read_b128 v[96:99], v2 offset:3840\n" \
  "v_mfma_f32_16x16x32_f16 v[20:23], v[92:95], v[36:39], v[20:23]\nds_read_b128 v[124:127], v2 offset:3904\ns_waitcnt lgkmcnt(8)\n" \
  "v_mfma_f32_16x16x32_f16 v[16:19], v[56:59], v[108:111], v[16:19]\nds_read_b128 v[92:95], v2 offset:3968\ns_waitcnt lgkmcnt(7)\n" \
  "v_readfirstlane_b32 s85, v27\ns_cmp_lt_u32 s85, 4\ns_cbranch_scc1 31f\n32:\nv_mfma_f32_16x16x32_f16 v[20:23], v[88:91], v[108:111], v[20:23]\n" \
  "ds_read_b128 v[36:39], v2 offset:4032\nds_read_b64_tr_b16 v[88:89], v5 offset:512\nds_read_b64_tr_b16 v[90:91], v5 offset:544\n" \
  "v_mfma_f32_16x16x32_f16 v[20:23], v[84:87], v[44:47], v[20:23]\nds_read_b64_tr_b16 v[108:109], v5 offset:768\n" \
  "ds_read_b64_tr_b16 v[110:111], v5 offset:800\nv_mfma_f32_16x16x32_f16 v[20:23], v[80:83], v[104:107], v[20:23]\n" \
  "ds_read_b64_tr_b16 v[84:85], v5 offset:1024\nds_read_b64_tr_b16 v[86:87], v5 offset:1056\ns_waitcnt lgkmcnt(12)\n" \
  "v_mfma_f32_16x16x32_f16 v[20:23], v[76:79], v[28:31], v[20:23]\nds_read_b64_tr_b16 v[44:45], v5 offset:1280\n" \
  "ds_read_b64_tr_b16 v[46:47], v5 offset:1312\nv_mfma_f32_16x16x32_f16 v[20:23], v[72:75], v[100:103], v[20:23]\ns_waitcnt lgkmcnt(10)\n" \
  "v_mfma_f32_16x16x32_f16 v[20:23], v[68:71], v[96:99], v[20:23]\nds_read_b64_tr_b16 v[80:81], v5 offset:1536\n" \
  "ds_read_b64_tr_b16 v[82:83], v5 offset:1568\nv_mfma_f32_16x16x32_f16 v[20:23], v[64:67], v[124:127], v[20:23]\n" \
  "ds_read_b64_tr_b16 v[104:105], v5 offset:1792\nds_read_b64_tr_b16 v[106:107], v5 offset:1824\ns_waitcnt lgkmcnt(12)\n" \
  "v_mfma_f32_16x16x32_f16 v[20:23], v[60:63], v[92:95], v[20:23]\nds_read_b64_tr_b16 v[76:77], v5 offset:2048\n" \
  "ds_read_b64_tr_b16 v[78:79], v5 offset:2080\nv_mfma_f32_16x16x32_f16 v[20:23], v[56:59], v[36:39], v[20:23]\ns_waitcnt lgkmcnt(10)\n" \
  "v_mfma_f32_16x16x32_f16 v[12:15], v[88:91], v[52:55], v[12:15]\nds_read_b64_tr_b16 v[28:29], v5 offset:2304\n" \
  "ds_read_b64_tr_b16 v[30:31], v5 offset:2336\nv_mfma_f32_16x16x32_f16 v[12:15], v[108:111], v[48:51], v[12:15]\n" \
  "ds_read_b64_tr_b16 v[72:73], v5 offset:2560\nds_read_b64_tr_b16 v[74:75], v5 offset:2592\ns_waitcnt lgkmcnt(10)\n" \
  "v_mfma_f32_16x16x32_f16 v[12:15], v[84:87], v[40:43], v[12:15]\nds_read_b64_tr_b16 v[100:101], v5 offset:2816\n" \
  "ds_read_b64_tr_b16 v[102:103], v5 offset:2848\nv_mfma_f32_16x16x32_f16 v[16:19], v[84:87], v[52:55], v[16:19]\n" \
  "ds_read_b64_tr_b16 v[68:69], v7 offset:0\nds_read_b64_tr_b16 v[70:71], v7 offset:64\n" \
  "v_mfma_f32_16x16x32_f16 v[12:15], v[44:47], v[120:123], v[12:15]\nv_mfma_f32_16x16x32_f16 v[16:19], v[44:47], v[48:51], v[16:19]\n" \
  "s_waitcnt lgkmcnt(10)\nv_mfma_f32_16x16x32_f16 v[12:15], v[80:83], v[116:119], v[12:15]\nds_read2_b32 v[96:97], v8 offset0:16 offset1:17\n" \
  "ds_read2_b32 v[98:99], v8 offset0:18 offset1:19\nv_mfma_f32_16x16x32_f16 v[16:19], v[80:83], v[40:43], v[16:19]\n" \
  "ds_read2_b32 v[64:65], v8 offset0:0 offset1:1\nds_read2_b32 v[66:67], v8 offset0:2 offset1:3\n" \
  "v_mfma_f32_16x16x32_f16 v[20:23], v[80:83], v[52:55], v[20:23]\nv_mfma_f32_16x16x32_f16 v[12:15], v[104:107], v[112:115], v[12:15]\n" \
  "v_mfma_f32_16x16x32_f16 v[16:19], v[104:107], v[120:123], v[16:19]\nv_mfma_f32_16x16x32_f16 v[20:23], v[104:107], v[48:51], v[20:23]\n" \
  "s_waitcnt lgkmcnt(10)\nv_mfma_f32_16x16x32_f16 v[16:19], v[76:79], v[116:119], v[16:19]\nds_read_b64_tr_b16 v[124:125], v7 offset:512\n" \
  "ds_read_b64_tr_b16 v[126:127], v7 offset:576\nv_mfma_f32_16x16x32_f16 v[20:23], v[76:79], v[40:43], v[20:23]\n" \
  "ds_read2_b32 v[60:61], v8 offset0:48 offset1:49\nds_read2_b32 v[62:63], v8 offset0:50 offset1:51\n" \
  "v_mfma_f32_16x16x32_f16 v[16:19], v[28:31], v[112:115], v[16:19]\nv_mfma_f32_16x16x32_f16 v[20:23], v[28:31], v[120:123], v[20:23]\n" \
  "s_waitcnt lgkmcnt(10)\nv_mfma_f32_16x16x32_f16 v[20:23], v[72:75], v[116:119], v[20:23]\nds_read_b64_tr_b16 v[92:93], v7 offset:1024\n" \
  "ds_read_b64_tr_b16 v[94:95], v7 offset:1088\nv_mfma_f32_16x16x32_f16 v[20:23], v[100:103], v[112:115], v[20:23]\n" \
  "ds_read_b64_tr_b16 v[56:57], v7 offset:1536\nds_read_b64_tr_b16 v[58:59], v7 offset:1600\ns_waitcnt lgkmcnt(10)\n" \
  "v_mfma_f32_16x16x32_f16 v[12:15], v[68:71], v[32:35], v[12:15]\nv_mfma_f32_16x16x32_f16 v[16:19], v[68:71], v[96:99], v[16:19]\n" \
  "s_waitcnt lgkmcnt(6)\nv_mfma_f32_16x16x32_f16 v[20:23], v[68:71], v[64:67], v[20:23]\ns_waitcnt lgkmcnt(2)\n" \
  "v_mfma_f32_16x16x32_f16 v[12:15], v[124:127], v[60:63], v[12:15]\nv_mfma_f32_16x16x32_f16 v[16:19], v[124:127], v[32:35], v[16:19]\n" \
  "v_mfma_f32_16x16x32_f16 v[20:23], v[124:127], v[96:99], v[20:23]\nv_mfma_f32_16x16x32_f16 v[16:19], v[92:95], v[60:63], v[16:19]\n" \
  "v_mfma_f32_16x16x32_f16 v[20:23], v[92:95], v[32:35], v[20:23]\ns_waitcnt lgkmcnt(0)\n" \
  "v_mfma_f32_16x16x32_f16 v[20:23], v[56:59], v[60:63], v[20:23]\ns_nop 1\nv_cvt_pkrtz_f16_f32 v24, v12, v13\nv_cvt_pkrtz_f16_f32 v25, v14, v15\n" \
  "v_pk_add_u16 v24, v24, 4 op_sel_hi:[1,0] clamp\nv_pk_add_u16 v25, v25, 4 op_sel_hi:[1,0] clamp\nds_write_b64 v4, v[24:25] offset:5120\n" \
  "v_cvt_pkrtz_f16_f32 v24, v16, v17\nv_cvt_pkrtz_f16_f32 v25, v18, v19\nv_pk_add_u16 v24, v24, 4 op_sel_hi:[1,0] clamp\n" \
  "v_pk_add_u16 v25, v25, 4 op_sel_hi:[1,0] clamp\nds_write_b64 v4, v[24:25] offset:6144\nv_cvt_pkrtz_f16_f32 v24, v20, v21\n" \
  "v_cvt_pkrtz_f16_f32 v25, v22, v23\nv_pk_add_u16 v24, v24, 4 op_sel_hi:[1,0] clamp\nv_pk_add_u16 v25, v25, 4 op_sel_hi:[1,0] clamp\n" \
  "ds_write_b64 v4, v[24:25] offset:7168\ns_setprio 0\ns_setpc_b64 s[90:91]\n31:\ns_movk_i32 s84, 0x4000\n33:\nds_read_b32 v27, v9 offset:32\n" \
  "s_sub_u32 s84, s84, 1\ns_cbranch_scc1 32b\ns_waitcnt lgkmcnt(0)\nv_readfirstlane_b32 s85, v27\ns_cmp_lt_u32 s85, 4\ns_cbranch_scc1 33b\n" \
  "s_branch 32b\n9:\ns_cmp_eq_u32 s89, 3\ns_cbranch_scc1 3f\ns_cmp_eq_u32 s89, 4\ns_cbranch_scc1 4f\ns_cmp_eq_u32 s89, 5\ns_cbranch_scc1 5f\n" \
  "ds_read_b64 v[124:125], v3 offset:576\nds_read_b64 v[126:127], v3 offset:584\nds_read_b64 v[120:121], v3 offset:64\n" \
  "ds_read_b64 v[122:123], v3 offset:72\nds_read_b64 v[116:117], v3 offset:640\nds_read_b64 v[118:119], v3 offset:648\n" \
  "ds_read_b64 v[112:113], v3 offset:128\nds_read_b64 v[114:115], v3 offset:136\nds_read_b64 v[108:109], v3 offset:704\n" \
  "ds_read_b64 v[110:111], v3 offset:712\nds_read_b64 v[104:105], v3 offset:192\nds_read_b64 v[106:107], v3 offset:200\n" \
  "ds_read_b64 v[100:101], v3 offset:768\nds_read_b64 v[102:103], v3 offset:776\nds_read_b64 v[96:97], v3 offset:256\n" \
  "ds_read_b64 v[98:99], v3 offset:264\nds_read_b64 v[92:93], v3 offset:832\nds_read_b64 v[94:95], v3 offset:840\n" \
  "ds_read_b64 v[88:89], v3 offset:320\nds_read_b64 v[90:91], v3 offset:328\nds_read_b64 v[84:85], v3 offset:896\n" \
  "ds_read_b64 v[86:87], v3 offset:904\nds_read_b64 v[80:81], v3 offset:384\nds_read_b64 v[82:83], v3 offset:392\n" \
  "ds_read_b64 v[76:77], v3 offset:960\nds_read_b64 v[78:79], v3 offset:968\nds_read_b64 v[72:73], v3 offset:448\n" \
  "ds_read_b64 v[74:75], v3 offset:456\nds_read_b64 v[68:69], v3 offset:1024\nds_read_b64 v[70:71], v3 offset:1032\n" \
  "ds_read_b64 v[64:65], v3 offset:512\nds_read_b64 v[66:67], v3 offset:520\ns_waitcnt lgkmcnt(0)\ns_barrier\nds_read_b128 v[24:27], v2 offset:0\n" \
  "ds_read_b128 v[28:31], v2 offset:64\nds_read_b128 v[32:35], v2 offset:128\nds_read_b128 v[36:39], v2 offset:192\ns_waitcnt lgkmcnt(3)\n" \
  "v_mfma_f32_16x16x32_f16 v[12:15], v[124:127], v[24:27], 0\ns_setprio 1\nds_read_b128 v[40:43], v2 offset:256\n" \
  "v_mfma_f32_16x16x32_f16 v[16:19], v[120:123], v[24:27], 0\ns_waitcnt lgkmcnt(3)\nv_mfma_f32_16x16x32_f16 v[12:15], v[116:119], v[28:31], v[12:15]\n" \
  "ds_read_b128 v[44:47], v2 offset:320\nv_mfma_f32_16x16x32_f16 v[16:19], v[112:115], v[28:31], v[16:19]\ns_waitcnt lgkmcnt(3)\n" \
  "v_mfma_f32_16x16x32_f16 v[12:15], v[108:111], v[32:35], v[12:15]\nds_read_b128 v[48:51], v2 offset:384\n" \
  "v_mfma_f32_16x16x32_f16 v[16:19], v[104:107], v[32:35], v[16:19]\ns_waitcnt lgkmcnt(3)\n" \
  "v_mfma_f32_16x16x32_f16 v[12:15], v[100:103], v[36:39], v[12:15]\nds_read_b128 v[52:55], v2 offset:448\n" \
  "v_mfma_f32_16x16x32_f16 v[16:19], v[96:99], v[36:39], v[16:19]\ns_waitcnt lgkmcnt(3)\n" \
  "v_mfma_f32_16x16x32_f16 v[12:15], v[92:95], v[40:43], v[12:15]\nds_read_b128 v[56:59], v2 offset:512\n" \
  "v_mfma_f32_16x16x32_f16 v[16:19], v[88:91], v[40:43], v[16:19]\nds_read_b128 v[60:63], v2 offset:576\ns_waitcnt lgkmcnt(3)\n" \
  "v_mfma_f32_16x16x32_f16 v[12:15], v[84:87], v[44:47], v[12:15]\nds_read_b128 v[120:123], v2 offset:640\n" \
  "v_mfma_f32_16x16x32_f16 v[16:19], v[80:83], v[44:47], v[16:19]\nds_read_b128 v[24:27], v2 offset:704\n" \
  "v_mfma_f32_16x16x32_f16 v[12:15], v[76:79], v[48:51], v[12:15]\nds_read_b128 v[112:115], v2 offset:768\n" \
  "v_mfma_f32_16x16x32_f16 v[16:19], v[72:75], v[48:51], v[16:19]\nds_read_b128 v[28:31], v2 offset:832\ns_waitcnt lgkmcnt(5)\n" \
  "v_mfma_f32_16x16x32_f16 v[12:15], v[68:71], v[52:55], v[12:15]\nds_read_b128 v[104:107], v2 offset:896\n" \
  "v_mfma_f32_16x16x32_f16 v[16:19], v[64:67], v[52:55], v[16:19]\nds_read_b128 v[32:35], v2 offset:960\n" \
  "v_mfma_f32_16x16x32_f16 v[16:19], v[124:127], v[56:59], v[16:19]\ns_waitcnt lgkmcnt(5)\n" \
  "v_mfma_f32_16x16x32_f16 v[16:19], v[116:119], v[60:63], v[16:19]\nv_mfma_f32_16x16x32_f16 v[16:19], v[108:111], v[120:123], v[16:19]\n" \
  "s_waitcnt lgkmcnt(3)\nv_mfma_f32_16x16x32_f16 v[16:19], v[100:103], v[24:27], v[16:19]\nv_cvt_pkrtz_f16_f32 v20, v12, v13\n" \
  "v_cvt_pkrtz_f16_f32 v21, v14, v15\nv_mfma_f32_16x16x32_f16 v[16:19], v[92:95], v[112:115], v[16:19]\n" \
  "v_pk_add_u16 v20, v20, 4 op_sel_hi:[1,0] clamp\ns_waitcnt lgkmcnt(2)\nv_mfma_f32_16x16x32_f16 v[16:19], v[84:87], v[28:31], v[16:19]\n" \
  "v_pk_add_u16 v21, v21, 4 op_sel_hi:[1,0] clamp\ns_waitcnt lgkmcnt(1)\nv_mfma_f32_16x16x32_f16 v[16:19], v[76:79], v[104:107], v[16:19]\n" \
  "ds_write_b64 v4, v[20:21] offset:0\ns_waitcnt lgkmcnt(1)\nv_mfma_f32_16x16x32_f16 v[16:19], v[68:71], v[32:35], v[16:19]\ns_nop 7\n" \
  "v_cvt_pkrtz_f16_f32 v20, v16, v17\nv_cvt_pkrtz_f16_f32 v21, v18, v19\nv_pk_add_u16 v20, v20, 4 op_sel_hi:[1,0] clamp\n" \
  "v_pk_add_u16 v21, v21, 4 op_sel_hi:[1,0] clamp\nds_write_b64 v4, v[20:21] offset:1024\ns_setprio 0\ns_setpc_b64 s[90:91]\n3:\n" \
  "ds_read_b64 v[124:125], v3 offset:0\nds_read_b64 v[126:127], v3 offset:8\nds_read_b64 v[120:121], v3 offset:64\n" \
  "ds_read_b64 v[122:123], v3 offset:72\nds_read_b64 v[116:117], v3 offset:128\nds_read_b64 v[118:119], v3 offset:136\n" \
  "ds_read_b64 v[112:113], v3 offset:192\nds_read_b64 v[114:115], v3 offset:200\nds_read_b64 v[108:109], v3 offset:256\n" \
  "ds_read_b64 v[110:111], v3 offset:264\nds_read_b64 v[104:105], v3 offset:320\nds_read_b64 v[106:107], v3 offset:328\n" \
  "ds_read_b64 v[100:101], v3 offset:384\nds_read_b64 v[102:103], v3 offset:392\nds_read_b64 v[96:97], v3 offset:448\n" \
  "ds_read_b64 v[98:99], v3 offset:456\nds_read_b64 v[92:93], v3 offset:512\nds_read_b64 v[94:95], v3 offset:520\n" \
  "ds_read_b64 v[88:89], v3 offset:576\nds_read_b64 v[90:91], v3 offset:584\nds_read_b64 v[84:85], v3 offset:640\n" \
  "ds_read_b64 v[86:87], v3 offset:648\nds_read_b64 v[80:81], v3 offset:704\nds_read_b64 v[82:83], v3 offset:712\n" \
  "ds_read_b64 v[76:77], v3 offset:768\nds_read_b64 v[78:79], v3 offset:776\nds_read_b64 v[72:73], v3 offset:832\n" \
  "ds_read_b64 v[74:75], v3 offset:840\nds_read_b64 v[68:69], v3 offset:896\nds_read_b64 v[70:71], v3 offset:904\n" \
  "ds_read_b64 v[64:65], v3 offset:960\nds_read_b64 v[66:67], v3 offset:968\nds_read_b64 v[60:61], v3 offset:1024\n" \
  "ds_read_b64 v[62:63], v3 offset:1032\nds_read_b64 v[56:57], v6 offset:256\nds_read_b64 v[58:59], v6 offset:264\n" \
  "ds_read_b64 v[52:53], v6 offset:128\nds_read_b64 v[54:55], v6 offset:136\nds_read_b64 v[48:49], v6 offset:320\n" \
  "ds_read_b64 v[50:51], v6 offset:328\nds_read_b64 v[44:45], v6 offset:192\nds_read_b64 v[46:47], v6 offset:200\ns_waitcnt lgkmcnt(0)\ns_barrier\n" \
  "ds_read_b128 v[24:27], v2 offset:448\nds_read_b128 v[28:31], v2 offset:512\nds_read_b128 v[32:35], v2 offset:576\n" \
  "ds_read_b128 v[36:39], v2 offset:640\nds_read_b128 v[40:43], v2 offset:704\ns_waitcnt lgkmcnt(3)\n" \
  "v_mfma_f32_16x16x32_f16 v[12:15], v[124:127], v[24:27], 0\ns_setprio 1\nds_read_b128 v[24:27], v2 offset:768\n" \
  "v_mfma_f32_16x16x32_f16 v[12:15], v[120:123], v[28:31], v[12:15]\nds_read_b128 v[28:31], v2 offset:832\ns_waitcnt lgkmcnt(3)\n" \
  "v_mfma_f32_16x16x32_f16 v[12:15], v[116:119], v[32:35], v[12:15]\nds_read_b128 v[32:35], v2 offset:896\n" \
  "v_mfma_f32_16x16x32_f16 v[12:15], v[112:115], v[36:39], v[12:15]\nds_read_b128 v[36:39], v2 offset:960\ns_waitcnt lgkmcnt(3)\n" \
  "v_mfma_f32_16x16x32_f16 v[12:15], v[108:111], v[40:43], v[12:15]\nds_read_b128 v[40:43], v2 offset:1024\n" \
  "v_mfma_f32_16x16x32_f16 v[12:15], v[104:107], v[24:27], v[12:15]\nds_read_b128 v[24:27], v2 offset:1088\ns_waitcnt lgkmcnt(3)\n" \
  "v_mfma_f32_16x16x32_f16 v[12:15], v[100:103], v[28:31], v[12:15]\nds_read_b128 v[28:31], v2 offset:1152\n" \
  "v_mfma_f32_16x16x32_f16 v[12:15], v[96:99], v[32:35], v[12:15]\ns_waitcnt lgkmcnt(3)\n" \
  "v_mfma_f32_16x16x32_f16 v[12:15], v[92:95], v[36:39], v[12:15]\nds_read_b128 v[32:35], v2 offset:1216\n" \
  "v_mfma_f32_16x16x32_f16 v[16:19], v[124:127], v[36:39], 0\ns_waitcnt lgkmcnt(3)\nv_mfma_f32_16x16x32_f16 v[12:15], v[88:91], v[40:43], v[12:15]\n" \
  "ds_read_b128 v[124:127], v2 offset:1280\nv_mfma_f32_16x16x32_f16 v[16:19], v[120:123], v[40:43], v[16:19]\ns_waitcnt lgkmcnt(3)\n" \
  "v_mfma_f32_16x16x32_f16 v[12:15], v[84:87], v[24:27], v[12:15]\nds_read_b128 v[36:39], v2 offset:1344\n" \
  "v_mfma_f32_16x16x32_f16 v[16:19], v[116:119], v[24:27], v[16:19]\ns_waitcnt lgkmcnt(3)\n" \
  "v_mfma_f32_16x16x32_f16 v[12:15], v[80:83], v[28:31], v[12:15]\nds_read_b128 v[120:123], v2 offset:1408\n" \
  "v_mfma_f32_16x16x32_f16 v[16:19], v[112:115], v[28:31], v[16:19]\ns_waitcnt lgkmcnt(3)\n" \
  "v_mfma_f32_16x16x32_f16 v[12:15], v[76:79], v[32:35], v[12:15]\nds_read_b128 v[40:43], v2 offset:1472\n" \
  "v_mfma_f32_16x16x32_f16 v[16:19], v[108:111], v[32:35], v[16:19]\ns_waitcnt lgkmcnt(3)\n" \
  "v_mfma_f32_16x16x32_f16 v[12:15], v[72:75], v[124:127], v[12:15]\nds_read_b128 v[116:119], v2 offset:1536\n" \
  "v_mfma_f32_16x16x32_f16 v[16:19], v[104:107], v[124:127], v[16:19]\nds_read_b128 v[24:27], v2 offset:1600\ns_waitcnt lgkmcnt(3)\n" \
  "v_mfma_f32_16x16x32_f16 v[12:15], v[68:71], v[36:39], v[12:15]\nds_read_b128 v[112:115], v2 offset:1664\nds_read_b32 v23, v9 offset:32\n" \
  "v_mfma_f32_16x16x32_f16 v[16:19], v[100:103], v[36:39], v[16:19]\nds_read_b128 v[28:31], v2 offset:1728\n" \
  "v_mfma_f32_16x16x32_f16 v[12:15], v[64:67], v[120:123], v[12:15]\nds_read_b128 v[108:111], v2 offset:1792\n" \
  "v_mfma_f32_16x16x32_f16 v[16:19], v[96:99], v[120:123], v[16:19]\nds_read_b128 v[32:35], v2 offset:1856\ns_waitcnt lgkmcnt(6)\n" \
  "v_mfma_f32_16x16x32_f16 v[12:15], v[60:63], v[40:43], v[12:15]\nds_read_b128 v[104:107], v2 offset:1920\n" \
  "v_mfma_f32_16x16x32_f16 v[16:19], v[92:95], v[40:43], v[16:19]\nds_read_b128 v[124:127], v2 offset:1984\ns_waitcnt lgkmcnt(5)\n" \
  "v_readfirstlane_b32 s85, v23\ns_cmp_lt_u32 s85, 3\ns_cbranch_scc1 51f\n52:\nv_mfma_f32_16x16x32_f16 v[16:19], v[88:91], v[116:119], v[16:19]\n" \
  "ds_read_b64_tr_b16 v[100:101], v5 offset:0\nds_read_b64_tr_b16 v[102:103], v5 offset:32\n" \
  "v_mfma_f32_16x16x32_f16 v[16:19], v[84:87], v[24:27], v[16:19]\nds_read_b64_tr_b16 v[36:37], v5 offset:256\n" \
  "ds_read_b64_tr_b16 v[38:39], v5 offset:288\nv_mfma_f32_16x16x32_f16 v[16:19], v[80:83], v[112:115], v[16:19]\n" \
  "ds_read_b64_tr_b16 v[96:97], v5 offset:512\nds_read_b64_tr_b16 v[98:99], v5 offset:544\ns_waitcnt lgkmcnt(9)\n" \
  "v_mfma_f32_16x16x32_f16 v[16:19], v[76:79], v[28:31], v[16:19]\nds_read_b64_tr_b16 v[120:121], v5 offset:768\n" \
  "ds_read_b64_tr_b16 v[122:123], v5 offset:800\nv_mfma_f32_16x16x32_f16 v[16:19], v[72:75], v[108:111], v[16:19]\ns_waitcnt lgkmcnt(9)\n" \
  "v_mfma_f32_16x16x32_f16 v[16:19], v[68:71], v[32:35], v[16:19]\nv_mfma_f32_16x16x32_f16 v[16:19], v[64:67], v[104:107], v[16:19]\n" \
  "s_waitcnt lgkmcnt(6)\nv_mfma_f32_16x16x32_f16 v[16:19], v[60:63], v[124:127], v[16:19]\n" \
  "v_mfma_f32_16x16x32_f16 v[12:15], v[100:103], v[56:59], v[12:15]\nv_mfma_f32_16x16x32_f16 v[16:19], v[100:103], v[52:55], v[16:19]\n" \
  "s_waitcnt lgkmcnt(2)\nv_mfma_f32_16x16x32_f16 v[12:15], v[36:39], v[48:51], v[12:15]\n" \
  "v_mfma_f32_16x16x32_f16 v[16:19], v[36:39], v[44:47], v[16:19]\nv_mfma_f32_16x16x32_f16 v[16:19], v[96:99], v[56:59], v[16:19]\n" \
  "s_waitcnt lgkmcnt(0)\nv_mfma_f32_16x16x32_f16 v[16:19], v[120:123], v[48:51], v[16:19]\ns_nop 3\nv_cvt_pkrtz_f16_f32 v20, v12, v13\n" \
  "v_cvt_pkrtz_f16_f32 v21, v14, v15\nv_pk_add_u16 v20, v20, 4 op_sel_hi:[1,0] clamp\nv_pk_add_u16 v21, v21, 4 op_sel_hi:[1,0] clamp\n" \
  "ds_write_b64 v4, v[20:21] offset:2048\nv_cvt_pkrtz_f16_f32 v20, v16, v17\nv_cvt_pkrtz_f16_f32 v21, v18, v19\n" \
  "v_pk_add_u16 v20, v20, 4 op_sel_hi:[1,0] clamp\nv_pk_add_u16 v21, v21, 4 op_sel_hi:[1,0] clamp\nds_write_b64 v4, v[20:21] offset:3072\n" \
  "s_setprio 0\ns_setpc_b64 s[90:91]\n51:\ns_movk_i32 s84, 0x4000\n53:\nds_read_b32 v23, v9 offset:32\ns_sub_u32 s84, s84, 1\ns_cbranch_scc1 52b\n" \
  "s_waitcnt lgkmcnt(0)\nv_readfirstlane_b32 s85, v23\ns_cmp_lt_u32 s85, 3\ns_cbranch_scc1 53b\ns_branch 52b\n4:\n" \
  "ds_read_b64 v[124:125], v3 offset:1088\nds_read_b64 v[126:127], v3 offset:1096\nds_read_b64 v[120:121], v3 offset:0\n" \
  "ds_read_b64 v[122:123], v3 offset:8\nds_read_b64 v[116:117], v3 offset:64\nds_read_b64 v[118:119], v3 offset:72\n" \
  "ds_read_b64 v[112:113], v3 offset:128\nds_read_b64 v[114:115], v3 offset:136\nds_read_b64 v[108:109], v3 offset:192\n" \
  "ds_read_b64 v[110:111], v3 offset:200\nds_read_b64 v[104:105], v3 offset:256\nds_read_b64 v[106:107], v3 offset:264\n" \
  "ds_read_b64 v[100:101], v3 offset:320\nds_read_b64 v[102:103], v3 offset:328\nds_read_b64 v[96:97], v3 offset:384\n" \
  "ds_read_b64 v[98:99], v3 offset:392\nds_read_b64 v[92:93], v3 offset:448\nds_read_b64 v[94:95], v3 offset:456\n" \
  "ds_read_b64 v[88:89], v3 offset:512\nds_read_b64 v[90:91], v3 offset:520\nds_read_b64 v[84:85], v3 offset:576\n" \
  "ds_read_b64 v[86:87], v3 offset:584\nds_read_b64 v[80:81], v3 offset:640\nds_read_b64 v[82:83], v3 offset:648\n" \
  "ds_read_b64 v[76:77], v3 offset:704\nds_read_b64 v[78:79], v3 offset:712\nds_read_b64 v[72:73], v3 offset:768\n" \
  "ds_read_b64 v[74:75], v3 offset:776\nds_read_b64 v[68:69], v3 offset:832\nds_read_b64 v[70:71], v3 offset:840\n" \
  "ds_read_b64 v[64:65], v3 offset:896\nds_read_b64 v[66:67], v3 offset:904\nds_read_b64 v[60:61], v3 offset:960\n" \
  "ds_read_b64 v[62:63], v3 offset:968\nds_read_b64 v[56:57], v3 offset:1024\nds_read_b64 v[58:59], v3 offset:1032\n" \
  "ds_read_b64 v[52:53], v6 offset:0\nds_read_b64 v[54:55], v6 offset:8\nds_read_b64 v[48:49], v6 offset:64\nds_read_b64 v[50:51], v6 offset:72\n" \
  "ds_read_b64 v[44:45], v6 offset:128\nds_read_b64 v[46:47], v6 offset:136\nds_read_b64 v[40:41], v6 offset:192\n" \
  "ds_read_b64 v[42:43], v6 offset:200\ns_waitcnt lgkmcnt(0)\ns_barrier\nds_read_b128 v[24:27], v2 offset:448\nds_read_b128 v[28:31], v2 offset:960\n" \
  "ds_read_b128 v[32:35], v2 offset:1472\nds_read_b128 v[36:39], v2 offset:1536\ns_waitcnt lgkmcnt(3)\n" \
  "v_mfma_f32_16x16x32_f16 v[12:15], v[124:127], v[24:27], 0\ns_setprio 1\nds_read_b128 v[24:27], v2 offset:1600\ns_waitcnt lgkmcnt(3)\n" \
  "v_mfma_f32_16x16x32_f16 v[16:19], v[124:127], v[28:31], 0\nds_read_b128 v[124:127], v2 offset:1664\ns_waitcnt lgkmcnt(3)\n" \
  "v_mfma_f32_16x16x32_f16 v[12:15], v[120:123], v[32:35], v[12:15]\nds_read_b128 v[28:31], v2 offset:1728\ns_waitcnt lgkmcnt(3)\n" \
  "v_mfma_f32_16x16x32_f16 v[12:15], v[116:119], v[36:39], v[12:15]\nds_read_b128 v[32:35], v2 offset:1792\ns_waitcnt lgkmcnt(3)\n" \
  "v_mfma_f32_16x16x32_f16 v[12:15], v[112:115], v[24:27], v[12:15]\nds_read_b128 v[36:39], v2 offset:1856\ns_waitcnt lgkmcnt(3)\n" \
  "v_mfma_f32_16x16x32_f16 v[12:15], v[108:111], v[124:127], v[12:15]\nds_read_b128 v[24:27], v2 offset:1920\ns_waitcnt lgkmcnt(3)\n" \
  "v_mfma_f32_16x16x32_f16 v[12:15], v[104:107], v[28:31], v[12:15]\nds_read_b128 v[124:127], v2 offset:1984\ns_waitcnt lgkmcnt(3)\n" \
  "v_mfma_f32_16x16x32_f16 v[12:15], v[100:103], v[32:35], v[12:15]\nds_read_b128 v[28:31], v2 offset:2048\ns_waitcnt lgkmcnt(3)\n" \
  "v_mfma_f32_16x16x32_f16 v[12:15], v[96:99], v[36:39], v[12:15]\nds_read_b128 v[32:35], v2 offset:2112\ns_waitcnt lgkmcnt(3)\n" \
  "v_mfma_f32_16x16x32_f16 v[12:15], v[92:95], v[24:27], v[12:15]\nds_read_b128 v[36:39], v2 offset:2176\ns_waitcnt lgkmcnt(3)\n" \
  "v_mfma_f32_16x16x32_f16 v[12:15], v[88:91], v[124:127], v[12:15]\nds_read_b128 v[24:27], v2 offset:2240\n" \
  "v_mfma_f32_16x16x32_f16 v[16:19], v[120:123], v[124:127], v[16:19]\nds_read_b64 v[120:121], v6 offset:256\nds_read_b64 v[122:123], v6 offset:264\n" \
  "s_waitcnt lgkmcnt(4)\nv_mfma_f32_16x16x32_f16 v[12:15], v[84:87], v[28:31], v[12:15]\nds_read_b128 v[124:127], v2 offset:2304\n" \
  "v_mfma_f32_16x16x32_f16 v[16:19], v[116:119], v[28:31], v[16:19]\nds_read_b64 v[116:117], v6 offset:320\nds_read_b64 v[118:119], v6 offset:328\n" \
  "v_mfma_f32_16x16x32_f16 v[12:15], v[80:83], v[32:35], v[12:15]\nds_read_b128 v[28:31], v2 offset:2368\n" \
  "v_mfma_f32_16x16x32_f16 v[16:19], v[112:115], v[32:35], v[16:19]\ns_waitcnt lgkmcnt(6)\n" \
  "v_mfma_f32_16x16x32_f16 v[12:15], v[76:79], v[36:39], v[12:15]\nds_read_b128 v[112:115], v2 offset:2432\n" \
  "v_mfma_f32_16x16x32_f16 v[16:19], v[108:111], v[36:39], v[16:19]\nds_read2_b32 v[32:33], v8 offset0:48 offset1:49\n" \
  "ds_read2_b32 v[34:35], v8 offset0:50 offset1:51\nv_mfma_f32_16x16x32_f16 v[12:15], v[72:75], v[24:27], v[12:15]\n" \
  "ds_read_b128 v[108:111], v2 offset:2496\nv_mfma_f32_16x16x32_f16 v[16:19], v[104:107], v[24:27], v[16:19]\n" \
  "ds_read2_b32 v[36:37], v8 offset0:32 offset1:33\nds_read2_b32 v[38:39], v8 offset0:34 offset1:35\ns_waitcnt lgkmcnt(6)\n" \
  "v_mfma_f32_16x16x32_f16 v[12:15], v[68:71], v[124:127], v[12:15]\nds_read_b128 v[104:107], v2 offset:2560\n" \
  "v_mfma_f32_16x16x32_f16 v[16:19], v[100:103], v[124:127], v[16:19]\nds_read_b128 v[24:27], v2 offset:2624\n" \
  "v_mfma_f32_16x16x32_f16 v[12:15], v[64:67], v[28:31], v[12:15]\nds_read_b128 v[100:103], v2 offset:2688\nds_read_b32 v23, v9 offset:32\n" \
  "v_mfma_f32_16x16x32_f16 v[16:19], v[96:99], v[28:31], v[16:19]\nds_read_b128 v[124:127], v2 offset:2752\ns_waitcnt lgkmcnt(7)\n" \
  "v_mfma_f32_16x16x32_f16 v[12:15], v[60:63], v[112:115], v[12:15]\nds_read_b128 v[96:99], v2 offset:2816\n" \
  "v_mfma_f32_16x16x32_f16 v[16:19], v[92:95], v[112:115], v[16:19]\nds_read_b128 v[28:31], v2 offset:2880\n" \
  "v_mfma_f32_16x16x32_f16 v[12:15], v[56:59], v[108:111], v[12:15]\nds_read_b128 v[92:95], v2 offset:2944\n" \
  "v_mfma_f32_16x16x32_f16 v[16:19], v[88:91], v[108:111], v[16:19]\nds_read_b128 v[112:115], v2 offset:3008\ns_waitcnt lgkmcnt(5)\n" \
  "v_readfirstlane_b32 s85, v23\ns_cmp_lt_u32 s85, 3\ns_cbranch_scc1 61f\n62:\nv_mfma_f32_16x16x32_f16 v[16:19], v[84:87], v[104:107], v[16:19]\n" \
  "ds_read_b64_tr_b16 v[88:89], v5 offset:0\nds_read_b64_tr_b16 v[90:91], v5 offset:32\n" \
  "v_mfma_f32_16x16x32_f16 v[16:19], v[80:83], v[24:27], v[16:19]\nds_read_b64_tr_b16 v[108:109], v5 offset:256\n" \
  "ds_read_b64_tr_b16 v[110:111], v5 offset:288\nv_mfma_f32_16x16x32_f16 v[16:19], v[76:79], v[100:103], v[16:19]\n" \
  "ds_read_b64_tr_b16 v[84:85], v5 offset:512\nds_read_b64_tr_b16 v[86:87], v5 offset:544\ns_waitcnt lgkmcnt(9)\n" \
  "v_mfma_f32_16x16x32_f16 v[16:19], v[72:75], v[124:127], v[16:19]\nds_read_b64_tr_b16 v[104:105], v5 offset:768\n" \
  "ds_read_b64_tr_b16 v[106:107], v5 offset:800\nv_mfma_f32_16x16x32_f16 v[16:19], v[68:71], v[96:99], v[16:19]\n" \
  "ds_read_b64_tr_b16 v[80:81], v5 offset:1024\nds_read_b64_tr_b16 v[82:83], v5 offset:1056\ns_waitcnt lgkmcnt(11)\n" \
  "v_mfma_f32_16x16x32_f16 v[16:19], v[64:67], v[28:31], v[16:19]\nds_read_b64_tr_b16 v[24:25], v5 offset:1280\n" \
  "ds_read_b64_tr_b16 v[26:27], v5 offset:1312\nv_mfma_f32_16x16x32_f16 v[16:19], v[60:63], v[92:95], v[16:19]\n" \
  "ds_read_b64_tr_b16 v[76:77], v5 offset:1536\nds_read_b64_tr_b16 v[78:79], v5 offset:1568\ns_waitcnt lgkmcnt(12)\n" \
  "v_mfma_f32_16x16x32_f16 v[16:19], v[56:59], v[112:115], v[16:19]\nds_read_b64_tr_b16 v[100:101], v5 offset:1792\n" \
  "ds_read_b64_tr_b16 v[102:103], v5 offset:1824\nv_mfma_f32_16x16x32_f16 v[12:15], v[88:91], v[52:55], v[12:15]\ns_waitcnt lgkmcnt(10)\n" \
  "v_mfma_f32_16x16x32_f16 v[12:15], v[108:111], v[48:51], v[12:15]\nds_read_b64_tr_b16 v[72:73], v7 offset:0\n" \
  "ds_read_b64_tr_b16 v[74:75], v7 offset:64\nv_mfma_f32_16x16x32_f16 v[12:15], v[84:87], v[44:47], v[12:15]\n" \
  "ds_read_b64_tr_b16 v[124:125], v7 offset:512\nds_read_b64_tr_b16 v[126:127], v7 offset:576\n" \
  "v_mfma_f32_16x16x32_f16 v[16:19], v[84:87], v[52:55], v[16:19]\ns_waitcnt lgkmcnt(10)\n" \
  "v_mfma_f32_16x16x32_f16 v[12:15], v[104:107], v[40:43], v[12:15]\nv_mfma_f32_16x16x32_f16 v[16:19], v[104:107], v[48:51], v[16:19]\n" \
  "v_mfma_f32_16x16x32_f16 v[12:15], v[80:83], v[120:123], v[12:15]\nv_mfma_f32_16x16x32_f16 v[16:19], v[80:83], v[44:47], v[16:19]\n" \
  "s_waitcnt lgkmcnt(6)\nv_mfma_f32_16x16x32_f16 v[12:15], v[24:27], v[116:119], v[12:15]\n" \
  "v_mfma_f32_16x16x32_f16 v[16:19], v[24:27], v[40:43], v[16:19]\nv_mfma_f32_16x16x32_f16 v[16:19], v[76:79], v[120:123], v[16:19]\n" \
  "s_waitcnt lgkmcnt(2)\nv_mfma_f32_16x16x32_f16 v[16:19], v[100:103], v[116:119], v[16:19]\n" \
  "v_mfma_f32_16x16x32_f16 v[12:15], v[72:75], v[32:35], v[12:15]\nv_mfma_f32_16x16x32_f16 v[16:19], v[72:75], v[36:39], v[16:19]\n" \
  "s_waitcnt lgkmcnt(0)\nv_mfma_f32_16x16x32_f16 v[16:19], v[124:127], v[32:35], v[16:19]\ns_nop 4\nv_cvt_pkrtz_f16_f32 v20, v12, v13\n" \
  "v_cvt_pkrtz_f16_f32 v21, v14, v15\nv_pk_add_u16 v20, v20, 4 op_sel_hi:[1,0] clamp\nv_pk_add_u16 v21, v21, 4 op_sel_hi:[1,0] clamp\n" \
  "ds_write_b64 v4, v[20:21] offset:4096\nv_cvt_pkrtz_f16_f32 v20, v16, v17\nv_cvt_pkrtz_f16_f32 v21, v18, v19\n" \
  "v_pk_add_u16 v20, v20, 4 op_sel_hi:[1,0] clamp\nv_pk_add_u16 v21, v21, 4 op_sel_hi:[1,0] clamp\nds_write_b64 v4, v[20:21] offset:5120\n" \
  "s_setprio 0\ns_setpc_b64 s[90:91]\n61:\ns_movk_i32 s84, 0x4000\n63:\nds_read_b32 v23, v9 offset:32\ns_sub_u32 s84, s84, 1\ns_cbranch_scc1 62b\n" \
  "s_waitcnt lgkmcnt(0)\nv_readfirstlane_b32 s85, v23\ns_cmp_lt_u32 s85, 3\ns_cbranch_scc1 63b\ns_branch 62b\n5:\n" \
  "ds_read_b64 v[124:125], v3 offset:1088\nds_read_b64 v[126:127], v3 offset:1096\nds_read_b64 v[120:121], v3 offset:0\n" \
  "ds_read_b64 v[122:123], v3 offset:8\nds_read_b64 v[116:117], v3 offset:64\nds_read_b64 v[118:119], v3 offset:72\n" \
  "ds_read_b64 v[112:113], v3 offset:128\nds_read_b64 v[114:115], v3 offset:136\nds_read_b64 v[108:109], v3 offset:192\n" \
  "ds_read_b64 v[110:111], v3 offset:200\nds_read_b64 v[104:105], v3 offset:256\nds_read_b64 v[106:107], v3 offset:264\n" \
  "ds_read_b64 v[100:101], v3 offset:320\nds_read_b64 v[102:103], v3 offset:328\nds_read_b64 v[96:97], v3 offset:384\n" \
  "ds_read_b64 v[98:99], v3 offset:392\nds_read_b64 v[92:93], v3 offset:448\nds_read_b64 v[94:95], v3 offset:456\n" \
  "ds_read_b64 v[88:89], v3 offset:512\nds_read_b64 v[90:91], v3 offset:520\nds_read_b64 v[84:85], v3 offset:576\n" \
  "ds_read_b64 v[86:87], v3 offset:584\nds_read_b64 v[80:81], v3 offset:640\nds_read_b64 v[82:83], v3 offset:648\n" \
  "ds_read_b64 v[76:77], v3 offset:704\nds_read_b64 v[78:79], v3 offset:712\nds_read_b64 v[72:73], v3 offset:768\n" \
  "ds_read_b64 v[74:75], v3 offset:776\nds_read_b64 v[68:69], v3 offset:832\nds_read_b64 v[70:71], v3 offset:840\n" \
  "ds_read_b64 v[64:65], v3 offset:896\nds_read_b64 v[66:67], v3 offset:904\nds_read_b64 v[60:61], v3 offset:960\n" \
  "ds_read_b64 v[62:63], v3 offset:968\nds_read_b64 v[56:57], v3 offset:1024\nds_read_b64 v[58:59], v3 offset:1032\n" \
  "ds_read_b64 v[52:53], v6 offset:0\nds_read_b64 v[54:55], v6 offset:8\nds_read_b64 v[48:49], v6 offset:64\nds_read_b64 v[50:51], v6 offset:72\n" \
  "ds_read_b64 v[44:45], v6 offset:128\nds_read_b64 v[46:47], v6 offset:136\nds_read_b64 v[40:41], v6 offset:192\n" \
  "ds_read_b64 v[42:43], v6 offset:200\ns_waitcnt lgkmcnt(0)\ns_barrier\nds_read_b128 v[24:27], v2 offset:1472\n" \
  "ds_read_b128 v[28:31], v2 offset:1984\nds_read_b128 v[32:35], v2 offset:2496\nds_read_b128 v[36:39], v2 offset:2560\ns_waitcnt lgkmcnt(3)\n" \
  "v_mfma_f32_16x16x32_f16 v[12:15], v[124:127], v[24:27], 0\ns_setprio 1\nds_read_b128 v[24:27], v2 offset:2624\ns_waitcnt lgkmcnt(3)\n" \
  "v_mfma_f32_16x16x32_f16 v[16:19], v[124:127], v[28:31], 0\nds_read_b128 v[124:127], v2 offset:2688\ns_waitcnt lgkmcnt(3)\n" \
  "v_mfma_f32_16x16x32_f16 v[12:15], v[120:123], v[32:35], v[12:15]\nds_read_b128 v[28:31], v2 offset:2752\ns_waitcnt lgkmcnt(3)\n" \
  "v_mfma_f32_16x16x32_f16 v[12:15], v[116:119], v[36:39], v[12:15]\nds_read_b128 v[32:35], v2 offset:2816\ns_waitcnt lgkmcnt(3)\n" \
  "v_mfma_f32_16x16x32_f16 v[12:15], v[112:115], v[24:27], v[12:15]\nds_read_b128 v[36:39], v2 offset:2880\ns_waitcnt lgkmcnt(3)\n" \
  "v_mfma_f32_16x16x32_f16 v[12:15], v[108:111], v[124:127], v[12:15]\nds_read_b128 v[24:27], v2 offset:2944\ns_waitcnt lgkmcnt(3)\n" \
  "v_mfma_f32_16x16x32_f16 v[12:15], v[104:107], v[28:31], v[12:15]\nds_read_b128 v[124:127], v2 offset:3008\ns_waitcnt lgkmcnt(3)\n" \
  "v_mfma_f32_16x16x32_f16 v[12:15], v[100:103], v[32:35], v[12:15]\nds_read_b128 v[28:31], v2 offset:3072\ns_waitcnt lgkmcnt(3)\n" \
  "v_mfma_f32_16x16x32_f16 v[12:15], v[96:99], v[36:39], v[12:15]\nds_read_b128 v[32:35], v2 offset:3136\ns_waitcnt lgkmcnt(3)\n" \
  "v_mfma_f32_16x16x32_f16 v[12:15], v[92:95], v[24:27], v[12:15]\nds_read_b128 v[36:39], v2 offset:3200\ns_waitcnt lgkmcnt(3)\n" \
  "v_mfma_f32_16x16x32_f16 v[12:15], v[88:91], v[124:127], v[12:15]\nds_read_b128 v[24:27], v2 offset:3264\n" \
  "v_mfma_f32_16x16x32_f16 v[16:19], v[120:123], v[124:127], v[16:19]\nds_read_b64 v[120:121], v6 offset:256\nds_read_b64 v[122:123], v6 offset:264\n" \
  "s_waitcnt lgkmcnt(4)\nv_mfma_f32_16x16x32_f16 v[12:15], v[84:87], v[28:31], v[12:15]\nds_read_b128 v[124:127], v2 offset:3328\n" \
  "v_mfma_f32_16x16x32_f16 v[16:19], v[116:119], v[28:31], v[16:19]\nds_read_b64 v[116:117], v6 offset:320\nds_read_b64 v[118:119], v6 offset:328\n" \
  "v_mfma_f32_16x16x32_f16 v[12:15], v[80:83], v[32:35], v[12:15]\nds_read_b128 v[28:31], v2 offset:3392\n" \
  "v_mfma_f32_16x16x32_f16 v[16:19], v[112:115], v[32:35], v[16:19]\ns_waitcnt lgkmcnt(6)\n" \
  "v_mfma_f32_16x16x32_f16 v[12:15], v[76:79], v[36:39], v[12:15]\nds_read_b128 v[112:115], v2 offset:3456\n" \
  "v_mfma_f32_16x16x32_f16 v[16:19], v[108:111], v[36:39], v[16:19]\nds_read2_b32 v[32:33], v8 offset0:16 offset1:17\n" \
  "ds_read2_b32 v[34:35], v8 offset0:18 offset1:19\nv_mfma_f32_16x16x32_f16 v[12:15], v[72:75], v[24:27], v[12:15]\n" \
  "ds_read_b128 v[108:111], v2 offset:3520\nv_mfma_f32_16x16x32_f16 v[16:19], v[104:107], v[24:27], v[16:19]\n" \
  "ds_read2_b32 v[36:37], v8 offset0:0 offset1:1\nds_read2_b32 v[38:39], v8 offset0:2 offset1:3\ns_waitcnt lgkmcnt(6)\n" \
  "v_mfma_f32_16x16x32_f16 v[12:15], v[68:71], v[124:127], v[12:15]\nds_read_b128 v[104:107], v2 offset:3584\n" \
  "v_mfma_f32_16x16x32_f16 v[16:19], v[100:103], v[124:127], v[16:19]\nds_read_b128 v[24:27], v2 offset:3648\n" \
  "ds_read2_b32 v[100:101], v8 offset0:32 offset1:33\nds_read2_b32 v[102:103], v8 offset0:34 offset1:35\n" \
  "v_mfma_f32_16x16x32_f16 v[12:15], v[64:67], v[28:31], v[12:15]\nds_read_b128 v[124:127], v2 offset:3712\nds_read_b32 v23, v9 offset:32\n" \
  "v_mfma_f32_16x16x32_f16 v[16:19], v[96:99], v[28:31], v[16:19]\nds_read_b128 v[96:99], v2 offset:3776\ns_waitcnt lgkmcnt(9)\n" \
  "v_mfma_f32_16x16x32_f16 v[12:15], v[60:63], v[112:115], v[12:15]\nds_read_b128 v[28:31], v2 offset:3840\n" \
  "v_mfma_f32_16x16x32_f16 v[16:19], v[92:95], v[112:115], v[16:19]\nds_read_b128 v[92:95], v2 offset:3904\n" \
  "v_mfma_f32_16x16x32_f16 v[12:15], v[56:59], v[108:111], v[12:15]\nds_read_b128 v[112:115], v2 offset:3968\n" \
  "v_mfma_f32_16x16x32_f16 v[16:19], v[88:91], v[108:111], v[16:19]\nds_read_b128 v[88:91], v2 offset:4032\ns_waitcnt lgkmcnt(5)\n" \
  "v_readfirstlane_b32 s85, v23\ns_cmp_lt_u32 s85, 3\ns_cbranch_scc1 71f\n72:\nv_mfma_f32_16x16x32_f16 v[16:19], v[84:87], v[104:107], v[16:19]\n" \
  "ds_read_b64_tr_b16 v[108:109], v5 offset:1024\nds_read_b64_tr_b16 v[110:111], v5 offset:1056\n" \
  "v_mfma_f32_16x16x32_f16 v[16:19], v[80:83], v[24:27], v[16:19]\nds_read_b64_tr_b16 v[84:85], v5 offset:1280\n" \
  "ds_read_b64_tr_b16 v[86:87], v5 offset:1312\nv_mfma_f32_16x16x32_f16 v[16:19], v[76:79], v[124:127], v[16:19]\n" \
  "ds_read_b64_tr_b16 v[104:105], v5 offset:1536\nds_read_b64_tr_b16 v[106:107], v5 offset:1568\ns_waitcnt lgkmcnt(9)\n" \
  "v_mfma_f32_16x16x32_f16 v[16:19], v[72:75], v[96:99], v[16:19]\nds_read_b64_tr_b16 v[80:81], v5 offset:1792\n" \
  "ds_read_b64_tr_b16 v[82:83], v5 offset:1824\nv_mfma_f32_16x16x32_f16 v[16:19], v[68:71], v[28:31], v[16:19]\n" \
  "ds_read_b64_tr_b16 v[24:25], v5 offset:2048\nds_read_b64_tr_b16 v[26:27], v5 offset:2080\ns_waitcnt lgkmcnt(11)\n" \
  "v_mfma_f32_16x16x32_f16 v[16:19], v[64:67], v[92:95], v[16:19]\nds_read_b64_tr_b16 v[76:77], v5 offset:2304\n" \
  "ds_read_b64_tr_b16 v[78:79], v5 offset:2336\nv_mfma_f32_16x16x32_f16 v[16:19], v[60:63], v[112:115], v[16:19]\n" \
  "ds_read_b64_tr_b16 v[124:125], v5 offset:2560\nds_read_b64_tr_b16 v[126:127], v5 offset:2592\ns_waitcnt lgkmcnt(12)\n" \
  "v_mfma_f32_16x16x32_f16 v[16:19], v[56:59], v[88:91], v[16:19]\nds_read_b64_tr_b16 v[72:73], v5 offset:2816\n" \
  "ds_read_b64_tr_b16 v[74:75], v5 offset:2848\nv_mfma_f32_16x16x32_f16 v[12:15], v[108:111], v[52:55], v[12:15]\ns_waitcnt lgkmcnt(10)\n" \
  "v_mfma_f32_16x16x32_f16 v[12:15], v[84:87], v[48:51], v[12:15]\nds_read_b64_tr_b16 v[96:97], v7 offset:0\n" \
  "ds_read_b64_tr_b16 v[98:99], v7 offset:64\nv_mfma_f32_16x16x32_f16 v[12:15], v[104:107], v[44:47], v[12:15]\n" \
  "ds_read_b64_tr_b16 v[68:69], v7 offset:512\nds_read_b64_tr_b16 v[70:71], v7 offset:576\n" \
  "v_mfma_f32_16x16x32_f16 v[16:19], v[104:107], v[52:55], v[16:19]\ns_waitcnt lgkmcnt(10)\n" \
  "v_mfma_f32_16x16x32_f16 v[12:15], v[80:83], v[40:43], v[12:15]\nds_read_b64_tr_b16 v[28:29], v7 offset:1024\n" \
  "ds_read_b64_tr_b16 v[30:31], v7 offset:1088\nv_mfma_f32_16x16x32_f16 v[16:19], v[80:83], v[48:51], v[16:19]\n" \
  "ds_read2_b32 v[64:65], v8 offset0:48 offset1:49\nds_read2_b32 v[66:67], v8 offset0:50 offset1:51\n" \
  "v_mfma_f32_16x16x32_f16 v[12:15], v[24:27], v[120:123], v[12:15]\nv_mfma_f32_16x16x32_f16 v[16:19], v[24:27], v[44:47], v[16:19]\n" \
  "s_waitcnt lgkmcnt(10)\nv_mfma_f32_16x16x32_f16 v[12:15], v[76:79], v[116:119], v[12:15]\nds_read_b64_tr_b16 v[92:93], v7 offset:1536\n" \
  "ds_read_b64_tr_b16 v[94:95], v7 offset:1600\nv_mfma_f32_16x16x32_f16 v[16:19], v[76:79], v[40:43], v[16:19]\n" \
  "v_mfma_f32_16x16x32_f16 v[16:19], v[124:127], v[120:123], v[16:19]\ns_waitcnt lgkmcnt(8)\n" \
  "v_mfma_f32_16x16x32_f16 v[16:19], v[72:75], v[116:119], v[16:19]\nv_mfma_f32_16x16x32_f16 v[12:15], v[96:99], v[32:35], v[12:15]\n" \
  "v_mfma_f32_16x16x32_f16 v[16:19], v[96:99], v[36:39], v[16:19]\ns_waitcnt lgkmcnt(4)\n" \
  "v_mfma_f32_16x16x32_f16 v[12:15], v[68:71], v[100:103], v[12:15]\nv_mfma_f32_16x16x32_f16 v[16:19], v[68:71], v[32:35], v[16:19]\n" \
  "s_waitcnt lgkmcnt(2)\nv_mfma_f32_16x16x32_f16 v[12:15], v[28:31], v[64:67], v[12:15]\n" \
  "v_mfma_f32_16x16x32_f16 v[16:19], v[28:31], v[100:103], v[16:19]\ns_waitcnt lgkmcnt(0)\n" \
  "v_mfma_f32_16x16x32_f16 v[16:19], v[92:95], v[64:67], v[16:19]\ns_nop 4\nv_cvt_pkrtz_f16_f32 v20, v12, v13\nv_cvt_pkrtz_f16_f32 v21, v14, v15\n" \
  "v_pk_add_u16 v20, v20, 4 op_sel_hi:[1,0] clamp\nv_pk_add_u16 v21, v21, 4 op_sel_hi:[1,0] clamp\nds_write_b64 v4, v[20:21] offset:6144\n" \
  "v_cvt_pkrtz_f16_f32 v20, v16, v17\nv_cvt_pkrtz_f16_f32 v21, v18, v19\nv_pk_add_u16 v20, v20, 4 op_sel_hi:[1,0] clamp\n" \
  "v_pk_add_u16 v21, v21, 4 op_sel_hi:[1,0] clamp\nds_write_b64 v4, v[20:21] offset:7168\ns_setprio 0\ns_setpc_b64 s[90:91]\n71:\n" \
  "s_movk_i32 s84, 0x4000\n73:\nds_read_b32 v23, v9 offset:32\ns_sub_u32 s84, s84, 1\ns_cbranch_scc1 72b\ns_waitcnt lgkmcnt(0)\n" \
  "v_readfirstlane_b32 s85, v23\ns_cmp_lt_u32 s85, 3\ns_cbranch_scc1 73b\ns_branch 72b\n"
#define CONV_CLOB "memory", "scc", "s84", "s85", "s90", "s91", "v12", "v13", "v14", "v15", "v16", "v17", "v18", "v19", "v20", "v21", "v22", "v23", "v24", "v25", "v26", "v27", "v28", "v29", "v30", "v31", "v32", "v33", "v34", "v35", "v36", "v37", "v38", "v39", "v40", "v41", "v42", "v43", "v44", "v45", "v46", "v47", "v48", "v49", "v50", "v51", "v52", "v53", "v54", "v55", "v56", "v57", "v58", "v59", "v60", "v61", "v62", "v63", "v64", "v65", "v66", "v67", "v68", "v69", "v70", "v71", "v72", "v73", "v74", "v75", "v76", "v77", "v78", "v79", "v80", "v81", "v82", "v83", "v84", "v85", "v86", "v87", "v88", "v89", "v90", "v91", "v92", "v93", "v94", "v95", "v96", "v97", "v98", "v99", "v100", "v101", "v102", "v103", "v104", "v105", "v106", "v107", "v108", "v109", "v110", "v111", "v112", "v113", "v114", "v115", "v116", "v117", "v118", "v119", "v120", "v121", "v122", "v123", "v124", "v125", "v126", "v127"
constexpr int TDP = 1216, T_3 = 4 * TDP, T_4 = T_3 + 512, TB = T_4 + 320;
constexpr int C_U = 480, UB = C_U + 4096 + 96, C_G5 = 0, C_G3 = 3200, C_G9 = 6400, C_G7 = C_G9 + 2096, GB = C_G7 + 2080 + 112;
constexpr int O_FL = 2 * TB, O_UL = O_FL + 64, O_IM = O_UL + 2 * UB, O_ST = O_IM + 2 * GB;
constexpr int X_TB = 10240, X_UL = X_TB + TB, X_IM = X_UL + UB, X_ST = X_IM + GB;
constexpr int X_WR = X_IM + 128 * IP;
#define X_RW(w) (LW + 9216 * (2 + ((w) >> 1)) + 2560 * ((w) & 1))
constexpr int RP = 1216;
constexpr int LW = 36864 + 512, LA = LW, LM = LW + 36864;
constexpr int L_SC = LM + 2048, SMEM = O_IM + 256 * IP;
static_assert(L_SC + 1152 <= SMEM && O_ST + 8192 <= SMEM && X_IM + 128 * IP <= LM && X_ST + 8192 <= LM && UB % 128 == 64 && GB % 128 == 64 && SMEM <= 79872, "");
struct TOps { uint32_t xa, xb, ya, pd, pf, pg, pe; };
__device__ __forceinline__ void table_ops(uint32_t im, uint32_t tb, int ct, int lane, TOps& t) {
  const int j = lane & 15, kq = lane >> 4, q4 = (lane >> 2) & 3, p4 = lane & 3, jj = j + 16 * ct;
  const uint32_t sa = im + (uint32_t)(IP * (lane >> 2));
  t.xa = sa + (uint32_t)(I_X + 8 * p4); t.xb = sa + (uint32_t)(I_X + 32 + 8 * (p4 & 1)); t.ya = sa + (uint32_t)(32 * ct + 8 * p4);
  t.pd = tb + (uint32_t)(2 * (541 - (8 * jj + 4 * (kq & 1) + 256 * (kq >> 1))));
  t.pf = tb + (uint32_t)((kq & 1) ? T_4 + 2 * (63 - jj) : T_3 + 2 * (127 - 2 * jj));
  t.pg = t.pf - ((kq & 1) ? 64 : 2);
  t.pe = tb + (uint32_t)(p4 * (TDP + 2) + ((lane < 12) ? 2 * (32 - q4) : 2 * (572 + (q4 & 1))));
  asm volatile("" : "+v"(t.xa), "+v"(t.xb), "+v"(t.ya), "+v"(t.pd), "+v"(t.pf), "+v"(t.pg), "+v"(t.pe));
}
typedef __attribute__((address_space(3))) char* lptr;
#define LP(T, a) ((__attribute__((address_space(3))) T*)(uintptr_t)(a))
__device__ __forceinline__ uint32_t table_tile(const TOps& t, int ct, int lane, uint32_t evp, float dsk TSP) {
  u32x4 ev;
  f32x4 ad[2], af[2];
  asm volatile(T_ASM : "=&v"(ad[0]), "=&v"(ad[1]), "=&v"(af[0]), "=&v"(af[1]), "=&v"(ev) : "v"(t.xa), "v"(t.xb), "v"(t.ya), "v"(evp) : T_CLOB);
  TS(0, 4);
  const int e0 = (int)(ev[0] + ev[2]), e1 = (int)(ev[1] + ev[3]);
  const int em = min(max(max(max(e0, e1), (int)((__float_as_uint(dsk) >> 23) & 0xffu) - 133), -110), 115);
  const int ts = ((uint32_t)(em + 12) <= 8u) ? 1 : -3 - em;
  float s0 = __builtin_amdgcn_ldexpf(1.f, e0 - 29 + ts), s1 = __builtin_amdgcn_ldexpf(1.f, e1 - 29 + ts);
  float d0 = (ct == 0 && lane == 0) ? __builtin_amdgcn_ldexpf(dsk, ts) : 0.f;
  asm volatile("s_nop 1" : "+v"(ad[0]), "+v"(ad[1]), "+v"(af[0]), "+v"(af[1]), "+v"(s0), "+v"(s1), "+v"(d0));
  const f32x4 k = ad[0] * s0 + (f32x4){d0, 0.f, 0.f, 0.f} + ad[1] * s1, f = af[0] * s0 + af[1] * s1;
  auto pk = [](float a, float b) { uint32_t r; asm("v_cvt_pk_f16_f32 %0, %1, %2" : "=v"(r) : "v"(a), "v"(b)); return r; };
  asm volatile("s_nop 0" : : "v"(k), "v"(f));
  const uint32_t p32 = pk(k[3], k[2]), p10 = pk(k[1], k[0]), p21 = pk(k[2], k[1]), p01 = pk(f[0], f[1]), p23 = pk(f[2], f[3]);
  *LP(u32x2, t.pd + 3 * TDP + 6) = (u32x2){p32, p10};
  *LP(uint32_t, t.pd + TDP + 2) = p32; *LP(uint32_t, t.pd + TDP + 6) = p10;
  *LP(uint32_t, t.pd + 2) = p21; *LP(uint32_t, t.pd + 2 * TDP + 6) = p21;
  *LP(u16, t.pd) = (u16)p32; *LP(u16, t.pd + 2 * TDP + 4) = (u16)p32;
  asm volatile("ds_write_b16_d16_hi %0, %1 offset:6\n\tds_write_b16_d16_hi %0, %1 offset:%2" : : "v"(t.pd), "v"(p10), "n"(2 * TDP + 10) : "memory");
  if (ct) {
    const uint32_t va = (uint32_t)__builtin_amdgcn_readlane((int)p32, 63), vb = (uint32_t)__builtin_amdgcn_readlane((int)p23, 15) >> 16;
    *LP(u16, t.pe) = (u16)((lane < 12) ? va : vb);
  }
  if (lane < 32) {
    *LP(u16, t.pf + 128) = (u16)p01; *LP(u16, t.pg + 128) = (u16)(p01 >> 16);
    *LP(u16, t.pf) = (u16)p23; *LP(u16, t.pg) = (u16)(p23 >> 16);
  }
  return (uint32_t)(127 + 5 - ts) << 23;
}
__device__ __forceinline__ uint32_t hadd(uint32_t a, uint32_t b) { return __builtin_bit_cast(uint32_t, __builtin_bit_cast(f16x2, a) + __builtin_bit_cast(f16x2, b)); }
template <bool FULL> __device__ __forceinline__ void sums(char* sm, const u32x4 p, const u32x4 c) {
  const uint32_t D[8] = {p[0], p[1], p[2], p[3], c[0], c[1], c[2], c[3]};
  uint32_t O[7];
#pragma unroll
  for (int j = 0; j < 7; ++j) O[j] = __builtin_amdgcn_alignbit(D[j + 1], D[j], 16);
  u32x4 a, b;
#pragma unroll
  for (int j = 0; j < 4; ++j) a[j] = hadd(D[j + 2], hadd(O[j + 1], O[j + 2]));
  *(u32x4*)(sm + C_G3 + 32) = a;
#pragma unroll
  for (int j = 0; j < 4; ++j) b[j] = hadd(a[j], hadd(D[j + 1], D[j + 3]));
  *(u32x4*)(sm + C_G5 + 32) = b;
  if (FULL) {
#pragma unroll
    for (int j = 0; j < 4; ++j) a[j] = hadd(b[j], hadd(O[j], O[j + 3]));
    *(u32x4*)(sm + C_G7 + 32) = a;
#pragma unroll
    for (int j = 0; j < 4; ++j) b[j] = hadd(a[j], hadd(D[j], D[j + 4]));
    *(u32x4*)(sm + C_G9 + 32) = b;
  }
}
__device__ __forceinline__ void count_up(uint32_t a) {
  uint32_t t;
  asm volatile("s_mov_b64 exec, 1\n\tv_mov_b32 %0, 1\n\tds_add_u32 %1, %0\n\ts_mov_b64 exec, -1" : "=&v"(t) : "v"(a) : "memory");
}
__device__ __forceinline__ int pad_at(int r) { return 16 * (r & 1) + 3200 * (r >> 1) - ((r >= 6) ? 3200 - 2096 : 0); }
struct COps { uint32_t b, a, o, g3, k3, g4, k4, f; };
__device__ __forceinline__ void conv_ops(uint32_t tb, uint32_t ub, uint32_t cb, int lane, COps& e) {
  const uint32_t j = lane & 15, kq = lane >> 4, q4 = (lane >> 2) & 3, p4 = lane & 3;
  e.b = ub + 32u * j + 16u * kq;
  e.a = tb + p4 * TDP + 32u - 8u * q4 + 16u * kq;
  e.g3 = cb + C_G5 + 8 + 64u * kq + 8u * (q4 + p4) + (q4 & 1u) * 3200u;
  e.g4 = cb + C_G9 + 8 + 128u * kq + 16u * q4 + 8u * p4 + (q4 & 1u) * 2096u;
  e.k3 = tb + T_3 + 120 - 8u * j + 16u * kq;
  e.k4 = tb + T_4 + 60 - 4u * j + 16u * kq;
  asm volatile("" : "+v"(e.b), "+v"(e.a), "+v"(e.g3), "+v"(e.g4), "+v"(e.k3), "+v"(e.k4));
}
#define CONV_RUN(prog, e, ...) do { int prog_ = (prog); \
    asm volatile("s_call_b64 s[90:91], kf_conv" : "+{s89}"(prog_), "+{v2}"(e.b), "+{v3}"(e.a), "+{v4}"(e.o), "+{v5}"(e.g3), "+{v6}"(e.k3), "+{v7}"(e.g4), \
                 "+{v8}"(e.k4), "+{v9}"(e.f) __VA_ARGS__ : : CONV_CLOB); } while (0)

template <bool E>
__device__ __forceinline__ void ln_stats(const uint32_t* v, int lane, float sc, f32x2* t, f32x4 z, f32x2& rc, f32x2& nc, float& e) {
  float s1a = z[0], s2a = z[1], s1b = z[2], s2b = z[3];
#pragma unroll
  for (int k = 0; k < 4; ++k) {
    s1a = dot2(v[k], 0x3f803f80u, s1a); s2a = dot2(v[k], v[k], s2a);
    s1b = dot2(v[4 + k], 0x3f803f80u, s1b); s2b = dot2(v[4 + k], v[4 + k], s2b);
  }
#pragma unroll
  for (int k = 0; k < 9; ++k) t[k] = (f32x2){bflo(v[k]), bfhi(v[k])};
  const float rs = row_sum(fold16(dot2(v[8], 0x3f803f80u, fold32(s1a, s1b)), dot2(v[8], v[8], fold32(s2a, s2b))));
  if (E) { e = row_sum(e); e += __int_as_float(__builtin_amdgcn_update_dpp(0, __float_as_int(e), 0x142, 0xa, 0xf, false)); }
  const f32x2 mean = (f32x2){rdl(rs, 15), rdl(rs, 47)} * (1.f / D_), m2 = mean * mean;
  f32x2 var = (f32x2){rdl(rs, 31), rdl(rs, 63)} * (1.f / D_) - m2;
  asm volatile("" : "+v"(t[0]), "+v"(t[1]), "+v"(t[2]), "+v"(t[3]), "+v"(t[4]), "+v"(t[5]), "+v"(t[6]), "+v"(t[7]), "+v"(t[8]));
  if (__builtin_expect(!(m2[0] <= 16.f * var[0] && m2[1] <= 16.f * var[1]), 0)) {
    float q[3] = {0.f, 0.f, 0.f};
#pragma unroll
    for (int k = 0; k < 9; ++k) {
      const f32x2 a = t[k] - ((k < 4 || (k == 8 && lane < 32)) ? mean[0] : mean[1]);
      q[k >> 2] += a[0] * a[0] + a[1] * a[1];
    }
    float qa = q[0] + ((lane < 32) ? q[2] : 0.f), qb = q[1] + ((lane < 32) ? 0.f : q[2]);
    wave_sum2(qa, qb);
    var = (f32x2){qa, qb} * (1.f / D_);
  }
  const uint32_t mp = pack2(mean[0], mean[1]), vp = pack2(var[0], var[1]);
  const f32x2 ve = (f32x2){bflo(vp), bfhi(vp)} + 1e-5f;
  const uint32_t e2 = pack2(ve[0], ve[1]);
  const uint32_t sp = pack2(__builtin_amdgcn_sqrtf(bflo(e2)), __builtin_amdgcn_sqrtf(bfhi(e2)));
  rc = (f32x2){__builtin_amdgcn_rcpf(bflo(sp)), __builtin_amdgcn_rcpf(bfhi(sp))} * sc;
  nc = -(f32x2){bflo(mp), bfhi(mp)} * rc;
}
template <bool H>
__device__ __forceinline__ void ln_apply(const f32x2* t, int lane, f32x2 rc, f32x2 nc, uint32_t* c) {
  const float nm[3] = {nc[0], nc[1], (lane < 32) ? nc[0] : nc[1]}, rr[3] = {rc[0], rc[1], (lane < 32) ? rc[0] : rc[1]};
  f32x2 dd[9];
#pragma unroll
  for (int k = 0; k < 9; ++k) dd[k] = __builtin_elementwise_fma(t[k], (f32x2){rr[k >> 2], rr[k >> 2]}, (f32x2){nm[k >> 2], nm[k >> 2]});
  asm volatile("" : "+v"(dd[0]), "+v"(dd[1]), "+v"(dd[2]), "+v"(dd[3]), "+v"(dd[4]), "+v"(dd[5]), "+v"(dd[6]), "+v"(dd[7]), "+v"(dd[8]));
#pragma unroll
  for (int k = 0; k < 9; ++k) {
    const f32x2 d = dd[k];
    typedef __attribute__((ext_vector_type(2))) uint16_t u16x2;
    if (H) c[k] = __builtin_bit_cast(uint32_t, __builtin_elementwise_add_sat(__builtin_bit_cast(u16x2, __builtin_amdgcn_cvt_pkrtz(d[0], d[1])), (u16x2){4, 4}));
    else c[k] = pack2(d[0], d[1]);
  }
}
__device__ __forceinline__ uint32_t med3u(uint32_t a, uint32_t b, uint32_t c) { return max(min(a, b), min(max(a, b), c)); }
__device__ __forceinline__ uint32_t oct_maxu(uint32_t v) {
  v = max(v, (uint32_t)DPP_I((int)v, 0xb1, 0xf));
  v = max(v, (uint32_t)DPP_I((int)v, 0x4e, 0xf));
  v = max(v, (uint32_t)DPP_I((int)v, 0x141, 0xf));
  return v;
}

#define KARGS const u16* __restrict__ x, const u16* __restrict__ Are, const u16* __restrict__ Aim, const u16* __restrict__ Bre, \
    const u16* __restrict__ Bim, const u16* __restrict__ Cre, const u16* __restrict__ Cim, const u16* __restrict__ logdt, \
    const u16* __restrict__ Dskip, char* __restrict__ ws, const u16* __restrict__ wr, const u16* __restrict__ wg, \
    const u16* __restrict__ wu, const u16* __restrict__ wd, u16* __restrict__ out
#define KPASS x, Are, Aim, Bre, Bim, Cre, Cim, logdt, Dskip, ws, wr, wg, wu, wd, out
template <int ROLE, int FFN = -1>
__device__ __forceinline__ void role_body(char* smem, KARGS) {
  const int tid = threadIdx.x, lane = tid & 63;
  const int w = __builtin_amdgcn_readfirstlane(tid >> 6), wl = w & 3;
  const int b = blockIdx.x & 255, bx = b & 7, bj = b >> 3;
  constexpr bool isl = ROLE == 1 || ROLE == 2;
  const int tg = isl ? 32 * (bj >> 2) + 4 * bx + (bj & 3) : 32 * bx + bj;
  u32x4 xa, xc;
  uint32_t ep, r0;
#define P_WAIT(n, ...) asm volatile("s_waitcnt vmcnt(" #n ")\n\tv_lshlrev_b32 %0, 16, v100\n\tv_lshlrev_b32 %1, 16, v101\n\tv_lshlrev_b32 %2, 16, v102" \
    : "=v"(ldt), "=v"(are), "=v"(aim) : : "v100", "v101", "v102" __VA_ARGS__)
#define P_MORE() asm volatile("v_lshlrev_b32 %0, 16, v103\n\tv_lshlrev_b32 %1, 16, v104\n\tv_lshlrev_b32 %2, 16, v105\n\tv_lshlrev_b32 %3, 16, v106" \
    : "=v"(bre), "=v"(bim), "=v"(cre), "=v"(cim) : : "v103", "v104", "v105", "v106")
  TSD;
  TS(0, 0);
  const uint32_t lb0 = (uint32_t)(uintptr_t)(__attribute__((address_space(3))) char*)smem;
  const int i = lane & 15, kq = lane >> 4;
  const u32x4 ones = {0xffffffffu, 0xffffffffu, 0xffffffffu, 0xffffffffu}, zero4 = {0u, 0u, 0u, 0u};
  auto u_again = [&](u32x4& uw, const u32x4& ut, uint32_t uo) {
#pragma unroll 1
    for (int spin = 0; spin < SPIN_CAP; ++spin) {
      const bool na = ((uw[0] | uw[1] | uw[2] | uw[3]) & UMASK) != 0u;
      if (__builtin_expect(!__any(na), 1)) break;
      TSC(19);
      SLEEP(USLEEP);
      if (na) { LD16W(uw, uo); uw ^= ut; }
    }
  };
  constexpr uint32_t S16 = 0x41800000u, LMASK = 0x00070007u;
  auto v_put = [&](u32x4 v, const u32x4& ut, bool other, uint32_t vo) {
    const bool second = __any(other || ((v[0] | v[1] | v[2] | v[3]) & 0x40004000u) != 0u);
    if (__builtin_expect(second, 0)) v = (v & ~LMASK) | (~ut & LMASK); else v = (v & ~LMASK) | ut;
    ST16(vo, v);
    return second;
  };
  auto v_end = [&](uint32_t T, int n) { DRAIN(); if (lane == 0) ST4(O_DN + 16u * n, T); };
  if constexpr (!isl) {
    const int ch = wl >> 1, tl = tid & 255;
    int* evp = (int*)(smem + O_FL + 16 * ch);
    float ldt, are, aim, dsk;
    if constexpr (ROLE == 0) {
      __builtin_amdgcn_s_setprio(KPRIO);
      COps e;
      conv_ops(lb0 + (uint32_t)(ch * TB), lb0 + (uint32_t)(O_UL + ch * UB), lb0 + (uint32_t)(O_IM + ch * GB), lane, e);
      e.o = lb0 + O_ST + (uint32_t)(16 * ((4 * i + 2 * (kq >> 1) + ch) ^ (i >> 1)) + 8 * (kq & 1));
      e.f = lb0 + O_FL;
      TOps t;
      table_ops(lb0 + (uint32_t)(O_IM + 128 * IP * ch), lb0 + (uint32_t)(TB * ch), wl & 1, lane, t);
      uint32_t pz = lb0 + O_IM + (uint32_t)(((lane >> 3) & 1) * GB + pad_at(lane & 7)), im = lb0 + (uint32_t)(O_IM + IP * tl);
      asm volatile("" : "+v"(e.o), "+v"(e.f), "+v"(pz), "+v"(im));
      float bre, bim, cre, cim;
      P_WAIT(0, , "v103", "v104", "v105", "v106", "v107", "v109");
      asm volatile("" : "={v109}"(ep));
      const uint32_t T = ((uint32_t)__builtin_amdgcn_readfirstlane(ep) + 1u) * 0x9E3779B1u;
      if (w == 3 && lane < 8) ST16(O_KEY + 128u * b + 16u * lane, ones);
      P_MORE();
      asm volatile("v_lshlrev_b32 %0, 16, v107" : "=v"(dsk) : : "v107");
      TS(0, 1);
      evp[wl & 1] = gen_y(ldt, are, aim, bre, bim, cre, cim, im, [] {} TSA);
      TS(0, 8);
      BAR();
      TS(0, 2);
      const uint32_t cv = table_tile(t, wl & 1, lane, lb0 + O_FL + 16 * ch, dsk TSA), cs = (uint32_t)__builtin_amdgcn_readfirstlane(cv);
      *(uint32_t*)(smem + O_FL + 40 + 4 * ch) = cv;
      TS(0, 9);
      gsync0(lb0 + O_FL + 36, 4);
      TS(0, 3);
      *(u32x4*)(smem + O_ST + 16 * tl) = ones;
      *(u32x4*)(smem + O_ST + 4096 + 16 * tl) = ones;
      if (w == 0) *LP(u32x4, pz) = zero4;
      DRAIN();
      CONV_RUN(wl & 1, e);
      if (wl & 1) asm volatile("s_mov_b64 exec, 1\n\tglobal_store_dwordx2 %0, %1, %2 sc1" : : "v"(O_SC + 16u * (2 * tg + ch)), "v"((u32x2){T, cs}), "s"(ws) : "memory");
      TS(0, 5);
    } else {
      __builtin_amdgcn_s_setprio(UPRIO);
#pragma unroll
      for (int k = 0; k < 3; ++k) *(u32x4*)(smem + ((16 * (tl + 256 * k) < O_FL) ? 16 * (tl + 256 * k) : O_FL + 32)) = zero4;
      if (w == 7) *(u32x4*)(smem + O_UL + (lane >> 5) * UB + 16 * min(lane & 31, 29)) = zero4;
      uint32_t uo = u_at(tl >> 1, 2 * tg + (tid & 1)), go = O_SU + (uint32_t)(256 * bx + 32 * wl + (lane & 31) + 4 * (lane & 32)) * 128u;
      uint32_t ul = (uint32_t)(O_UL + (tid & 1) * UB + C_U - 16 + 16 * (tl >> 1)), ug = (uint32_t)(O_IM + (tid & 1) * GB + 16 * (tl >> 1)), so = lb0 + O_ST + 16u * (uint32_t)(lane ^ (lane >> 3));
      uint32_t vo = O_V + (uint32_t)tg * VP + 16u * lane;
      asm volatile("" : "+v"(uo), "+v"(go), "+v"(ul), "+v"(ug), "+v"(so), "+v"(vo));
      P_WAIT(1, , "v109");
      TS(0, 1);
      evp[2 + (wl & 1)] = gen_x(ldt, are, aim, smem + O_IM + IP * tl);
      asm volatile("s_waitcnt vmcnt(0)" : "={v109}"(ep));
      const uint32_t T = ((uint32_t)__builtin_amdgcn_readfirstlane(ep) + 1u) * 0x9E3779B1u;
      TS(0, 8);
      BAR();
      TS(0, 2);
      u32x4 ut = {utag(T, 0), utag(T, 1), utag(T, 2), utag(T, 3)};
      asm volatile("" : "+v"(ut));
      SLEEP(GSLEEP);
      u32x2 pv;
      pv[1] = gate(ws, go, T);
      TS(0, 3);
      until(latest(pv[1]) + UD);
      TS(0, 7);
      u32x4 uw, ux;
      asm volatile("global_load_dwordx4 %0, %2, %4 sc1\n\tglobal_load_dwordx4 %1, %3, %4 sc1"
                   : "=&v"(uw), "=&v"(ux) : "v"(uo), "v"(uo + 64 * UP * 32), "s"(ws) : "memory");
      __builtin_amdgcn_s_setprio(FPR);
      asm volatile("s_waitcnt vmcnt(1)" : "+v"(uw));
      uw ^= ut;
      *(u32x4*)(smem + ul + 16) = uw;
      asm volatile("s_waitcnt vmcnt(0)" : "+v"(ux));
      TS(0, 11);
      ux ^= ut;
      *(u32x4*)(smem + ul + 2048 + 16) = ux;
      if (__builtin_expect(__any(((uw[0] | uw[1] | uw[2] | uw[3] | ux[0] | ux[1] | ux[2] | ux[3]) & UMASK) != 0u), 0)) {
        u_again(uw, ut, uo);
        u_again(ux, ut, uo + 64 * UP * 32);
        *(u32x4*)(smem + ul + 16) = uw;
        *(u32x4*)(smem + ul + 2048 + 16) = ux;
      }
      BAR();
      TS(0, 4);
      const u32x4 pw = *(const u32x4*)(smem + ul);
      if (wl < 2) {
        const u32x4 px = *(const u32x4*)(smem + ul + 2048);
        sums<true>(smem + ug, pw, uw);
        sums<false>(smem + ug + 2048, px, ux);
      } else sums<true>(smem + ug, pw, uw);
      count_up(lb0 + O_FL + 32);
      __builtin_amdgcn_s_setprio(SPR);
      TS(0, 5);
      const bool other = *(const uint32_t*)(smem + O_FL + 40 + 4 * (lane & 1)) != S16;
      bool second = false;
#pragma unroll
      for (int k = 0; k < 2; ++k) {
        const uint32_t o = ((k ? 0x3247u : 0x1065u) >> (4 * wl) & 7u) * 1024u;
        u32x4 v;
#pragma unroll 1
        for (int spin = 0; spin < SPIN_CAP; ++spin) {
          asm volatile("ds_read_b128 %0, %1\n\ts_waitcnt lgkmcnt(0)" : "=&v"(v) : "v"(so + o) : "memory");
          if (!__any(max4(v) == 0xffffffffu)) break;
          __builtin_amdgcn_s_sleep(VPS);
        }
        TS(0, 12 + k);
        second |= v_put(v, ut, other, vo + o);
        TS(0, 10 - 4 * k);
      }
      if (__builtin_expect(second, 0)) v_end(T, 4 * tg + wl);
    }
    TSE();
  } else {
  uint32_t* cand = (uint32_t*)(smem + LM);
  uint32_t* sel = cand + 128;
  uint32_t* kl = sel + 48;
  uint32_t* cnt = sel + 64;
  const uint32_t ca = lb0 + LM + 4 * (128 + 64);
  u16* hm = (u16*)(cnt + 12);
  uint32_t T;
  const bool ffn = (FFN < 0) ? bj < 18 : FFN, isx = FFN != 1 && bj >= 24;
  const int fb = 18 * bx + bj;
  constexpr bool ln2 = ROLE == 2;
  const int g0 = tg, g2 = tg;
  u32x4 bw[18];
#pragma unroll
  for (int s = 0; s < 9; ++s) bw[s] = (u32x4){0u, 0u, 0u, 0u};
  int tid2 = threadIdx.x;
  uint32_t ts1 = 0u;
  auto rest = [&] {
    if (ts1 && (int)((uint32_t)__builtin_amdgcn_s_memrealtime() - ts1) > 0) { TS(0, 8); until(ts1 + TSTOP + RD); if (RW) gwait(ca, 4); ts1 = 0u; }
  };
  auto gu_dma = [&](int q0, int q1, auto depth) {
    const char* gs = (const char*)((w < 2) ? wg : wu) + (size_t)((144 * (w & 1)) * (F_ * 2) + fb * 64);
    const uint32_t gv = (uint32_t)((lane >> 2) * (F_ * 2) + 16 * ((lane & 3) ^ (lane >> 5) ^ (w & 2)));
#pragma unroll 1
    for (int q = q0; q < q1; ++q) {
      const int k = (int)((((bj & 1) ? ORD1 : ((unsigned __int128)0x876543210u << 36 | 0x876543210ull)) >> (4 * (q - (q >= 9 ? 9 : 0)) + (q >= 9 ? 36 : 0))) & 15u), h = q >= 9;
      if (q >= RQ) rest();
      dma16(gs + (size_t)(288 * h + 16 * k) * (F_ * 2), gv, lb0 + LW * h + 9216 * w + 1024 * k);
      asm volatile("s_waitcnt vmcnt(%0)" : : "n"(depth.value) : "memory");
    }
  };
  auto down_dma = [&](int k0, int k1, int lane) {
#pragma unroll 1
    for (int k = k0; k < k1; ++k) {
      const int o = 9216 * wl + 2304 * k;
      const char* ds = (const char*)wd + (size_t)(fb * 36864 + o);
      const uint32_t dl = lb0 + o + 32 * ((4 * wl + k + 1) >> 1);
      if (ROLE == 1) rest();
      dma16d(ds, 16u * lane, dl);
      dma16d(ds + 1024, 16u * lane, dl + 1024);
      uint32_t keep;
      asm volatile("s_mov_b32 %0, m0\n\ts_mov_b32 m0, %3\n\ts_mov_b64 exec, 0xffff\n\tglobal_load_lds_dwordx4 %1, %2" DPOL2 "\n\ts_mov_b64 exec, -1\n\ts_mov_b32 m0, %0\n\ts_waitcnt vmcnt(%4)"
                   : "=&s"(keep) : "v"(16u * lane), "s"(ds + 2048), "s"(dl + 2048), "n"(ROLE == 1 ? DDEPTH : 40) : "memory");
    }
  };
  auto gu_read = [&](int h) {
    const uint32_t ra = lb0 + LW * h + (uint32_t)(((lane >> 1) & 1) * 18432 + (8 * kq + ((lane >> 2) & 3)) * 64 + ((w ^ (kq & 1) ^ (lane & 2)) * 16) + 8 * (lane & 1));
#pragma unroll
    for (int s = 0; s < 9; ++s) {
      const u32x2 h0 = trd(ra + 2048 * s), h1 = trd(ra + 2048 * s + 256);
      bw[9 * h + s] = (u32x4){h0[0], h0[1], h1[0], h1[1]};
    }
#pragma unroll
    for (int s = 0; s < 9; ++s) asm volatile("" : "+v"(bw[9 * h + s]));
  };
  uint32_t tv = 0u, tk = 0u;
  const uint32_t vearly = (uint32_t)((VEARLY >> (8 * (g0 >> 5))) & 0xffull);
#define TOUCH(pa) asm volatile("global_load_dword v126, %0, off" DPOL4 : : "v"(pa) : "memory", "v126")
  uint32_t wv[5], xv[9];
  auto rows_xw = [&](int lane) {
    u32x4 wa, xr, xs;
    const uint32_t xo = (uint32_t)((8 * g2 + 2 * wl) * (D_ * 2));
    asm volatile("global_load_dwordx4 %0, %5, %9\n\tglobal_load_dword %1, %6, %9 offset:1024\n\t"
                 "global_load_dwordx4 %2, %7, %10\n\tglobal_load_dwordx4 %3, %7, %10 offset:1152\n\tglobal_load_dword %4, %8, %10 offset:1024\n\ts_waitcnt vmcnt(0)"
                 : "=&v"(wa), "=&v"(wv[4]), "=&v"(xr), "=&v"(xs), "=&v"(xv[8])
                 : "v"(16u * lane), "v"(4u * (lane & 31)), "v"(xo + 16u * lane), "v"(xo + 4u * lane + 32u * (lane & 32)), "s"(wr), "s"(x) : "memory");
#pragma unroll
    for (int k = 0; k < 4; ++k) { wv[k] = wa[k]; xv[k] = xr[k]; xv[4 + k] = xs[k]; }
  };
  auto ln1 = [&](int g, int o, uint32_t c0) {
    uint32_t uo = u_at(g, 64 * wl + lane);
    uint32_t ta = lb0 + (uint32_t)(o + ((lane >> 2) & 3) * RP + (64 * wl + 16 * kq + 4 * (lane & 3)) * 2);
    uint32_t la = (uint32_t)(o + 2 * wl * RP + 16 * lane), lt = (uint32_t)(o + (2 * wl + (lane >> 5)) * RP + 1024 + 4 * (lane & 31));
    uint32_t sg = O_SU + (uint32_t)(256 * (lane >> 3) + g) * 128u + 16u * (lane & 7);
    asm volatile("" : "+v"(uo), "+v"(ta), "+v"(la), "+v"(lt), "+v"(sg));
    f32x4 z4 = {0.f, 0.f, 0.f, 0.f};
    asm volatile("" : "+v"(z4));
    asm volatile("s_waitcnt vmcnt(0)" : "={v[100:103]}"(xa), "={v[104:107]}"(xc), "={v108}"(r0), "={v109}"(ep));
    TS(0, 1);
    const uint32_t v[9] = {xa[0], xa[1], xa[2], xa[3], xc[0], xc[1], xc[2], xc[3], r0};
    uint32_t c[9];
    f32x2 tf[9];
    T = ((uint32_t)__builtin_amdgcn_readfirstlane(ep) + 1u) * 0x9E3779B1u;
    if (wl == 3) { const uint32_t now = (uint32_t)__builtin_amdgcn_s_memrealtime() + SD; ST16(sg, ((u32x4){T, now, T, now})); }
    const uint32_t tj = ((T >> (3 * wl)) & 0x00070007u) | (((T >> (12 + wl)) & 0x00010001u) << 14);
    uint32_t tw[3] = {(tj & 0xffffu) * 0x10001u, (tj >> 16) * 0x10001u, 0u};
    tw[2] = (lane < 32) ? tw[0] : tw[1];
    asm volatile("" : "+v"(tw[0]), "+v"(tw[1]), "+v"(tw[2]));
    f32x2 rc, nc;
    float e_;
    ln_stats<false>(v, lane, 0.03125f, tf, z4, rc, nc, e_);
    ln_apply<true>(tf, lane, rc, nc, c);
#pragma unroll
    for (int k = 0; k < 9; ++k) c[k] = (c[k] & ~UMASK) | tw[k >> 2];
    *(u32x4*)(smem + la) = (u32x4){c[0], c[1], c[2], c[3]};
    *(u32x4*)(smem + la + RP) = (u32x4){c[4], c[5], c[6], c[7]};
    *(uint32_t*)(smem + lt) = c[8];
    u32x4 ha, hb;
    {
      uint32_t t, cc, n = SPIN_CAP;
      asm volatile("s_mov_b64 exec, 1\n\tv_mov_b32 %2, 1\n\tds_add_u32 %5, %2\n\ts_mov_b64 exec, -1\n"
                   "1:\n\tds_read_b32 %2, %5\n\tds_read_b64_tr_b16 v[40:41], %6\n\tds_read_b64_tr_b16 v[42:43], %6 offset:%7\n\tds_read_b64_tr_b16 v[44:45], %6 offset:512\n\t"
                   "ds_read_b64_tr_b16 v[46:47], %6 offset:%8\n\ts_sub_u32 %4, %4, 1\n\ts_cbranch_scc1 2f\n\ts_waitcnt lgkmcnt(4)\n\tv_readfirstlane_b32 %3, %2\n\t"
                   "s_cmp_lt_u32 %3, 4\n\ts_cbranch_scc1 1b\n2:\n\ts_waitcnt lgkmcnt(0)"
                   : "=&{v[40:43]}"(ha), "=&{v[44:47]}"(hb), "=&v"(t), "=&s"(cc), "+s"(n) : "v"(c0), "v"(ta), "n"(4 * RP), "n"(512 + 4 * RP) : "memory", "scc");
    }
    ST16(uo, ha);
    ST16(uo + 64 * 128, hb);
    if (wl == 0) {
      const u32x2 h4 = trd(ta + 1024), h5 = trd(ta + 1024 + 4 * RP);
      ST16(uo + 128 * 128, ((u32x4){h4[0], h4[1], h5[0], h5[1]}));
    }
  };
  const int dX = 512 - 24 + 8 * bx + bj;
  if constexpr (ROLE == 2) {
    const int tl = tid - 256;
    if (isx) {
      __builtin_amdgcn_s_setprio(QPRIO0);
      *(u32x4*)(smem + X_TB + 16 * tl) = zero4;
      *(u32x4*)(smem + X_TB + 16 * min(256 + tl, (TB + C_U) / 16 - 1)) = zero4;
      const int pr = (wl + XROT) & 3;
      COps e;
      conv_ops(lb0 + X_TB, lb0 + X_UL, lb0 + X_IM, lane, e);
      TOps t;
      table_ops(lb0 + X_IM, lb0 + X_TB, wl & 1, lane, t);
      e.o = lb0 + X_ST + (uint32_t)(16 * ((2 * i + (kq >> 1)) ^ ((i >> 2) & 1)) + 8 * (kq & 1));
      e.f = ca + 28 - 32;
      asm volatile("" : "+v"(e.o), "+v"(e.f));
      float ldt, are, aim, bre, bim, cre, cim, dsk;
      P_WAIT(0, , "v103", "v104", "v105", "v106", "v107", "v109");
      asm volatile("" : "={v109}"(ep));
      T = ((uint32_t)__builtin_amdgcn_readfirstlane(ep) + 1u) * 0x9E3779B1u;
      P_MORE();
      asm volatile("v_lshlrev_b32 %0, 16, v107" : "=v"(dsk) : : "v107");
      TS(0, 1);
      __builtin_amdgcn_s_setprio(QPRIO);
      int* evp = (int*)(sel + 44);
      char* im = smem + X_IM + IP * (tl & 127);
      evp[wl] = (wl < 2) ? gen_y(ldt, are, aim, bre, bim, cre, cim, lb0 + X_IM + IP * tl, [] {} TSA) : gen_x(ldt, are, aim, im);
      TS(0, 2);
      __builtin_amdgcn_s_setprio(QPRIO2);
      gsync(ca + 8, 4);
      if (wl < 2) evp[-4 + wl] = (int)table_tile(t, wl & 1, lane, lb0 + LM + 4 * (128 + 44), dsk TSA);
      if (wl < 2) gsync0(ca + 8, 8); else { count_up(ca + 8); SLEEP(XSL); gwait(ca + 8, 8); }
      const uint32_t cs = (uint32_t)__builtin_amdgcn_readfirstlane(evp[-4]);
      if (wl == 3) *(u32x4*)(smem + X_IM + pad_at(lane & 7)) = zero4;
      TS(0, 3);
      CONV_RUN(2 + pr, e, , "+{v11}"(tid2));
      __builtin_amdgcn_s_setprio(3);
      {
        const int lane = tid2 & 63;
        const u32x4 v = *(const u32x4*)(smem + X_ST + 2048 * pr + 1024 * (lane >> 5) + 16 * ((lane & 31) ^ ((lane >> 3) & 1)));
        if (__builtin_expect(v_put(v, (u32x4){utag(T, 0), utag(T, 1), utag(T, 2), utag(T, 3)}, cs != S16, v_at(dX, 64 * pr + lane)), 0)) {
          DRAIN();
          if (lane == 0) ST4(O_DN + 16u * (4 * (dX - 256) + pr), T);
        }
        if (pr == 0 && lane == 0) asm volatile("global_store_dwordx2 %0, %1, %2 sc1" : : "v"(O_SC + 16u * dX), "v"((u32x2){T, cs}), "s"(ws) : "memory");
      }
      TS(0, 6);
    }
    if (!isx) __builtin_amdgcn_s_setprio(PPRIO);
    asm volatile("" : "+v"(tid2));
    if (!isx) rows_xw(tid2 & 63);
    if (!isx && !ffn) {
      SLEEP(RSLEEP);
#pragma unroll
      for (int k = 0; k < 7; ++k) ST16(O_SL + (uint32_t)((6 * bx + bj - 18) * 1792 + 256 * k - 256 + tid2) * 16u, ones);
      DRAIN();
    }
    if (!isx) BAR();
    const uint32_t tp = (uint32_t)__builtin_amdgcn_readfirstlane(cnt[4]);
    if (!isx) T = (uint32_t)__builtin_amdgcn_readfirstlane(sel[43]);
    tv = tp + UD + VD2 - vearly;
  } else {
    __builtin_amdgcn_s_setprio(3);
    if (tid < 12) cnt[tid] = 0u;
    BAR();
    if (tid < 20) ((uint32_t*)(hm + 320))[tid] = 0u;
    if (tid >= 184) *(u32x4*)(smem + L_SC - 16 * 184 + 16 * tid) = (u32x4){0x41804180u, 0x41804180u, 0x41804180u, 0x41804180u};
    uint32_t sink = 0;
    ln1(tg, 0, ca + 12);
    TS(0, 2);
    __builtin_amdgcn_s_setprio(NPRIO);
    asm volatile("s_waitcnt vmcnt(0)" : "+v"(sink) : : "memory");
    TS(0, 13);
    if (w == 0) {
      const uint32_t xr = O_XC + (uint32_t)((511 - 2 * (g0 + w)) * 144 + lane) * 16u;
      ST16(xr, ones);
      ST16(xr + 1024u, ones);
      if (lane < 16) ST16(xr + 2048u, ones);
    }
    if (w == 2 && b < 72 && lane < 8) ST16(O_FB + (uint32_t)(8 * b + lane) * 16u, ones);
    if (isx) {
      rows_xw(lane);
      char* sp = smem + X_RW(w) + 16 * lane;
      *(u32x4*)sp = (u32x4){xv[0], xv[1], xv[2], xv[3]};
      *(u32x4*)(sp + 1024) = (u32x4){xv[4], xv[5], xv[6], xv[7]};
      *(u32x2*)(smem + X_RW(w) + 2048 + 8 * lane) = (u32x2){xv[8], wv[4]};
      if (w == 0) *(u32x4*)(smem + X_WR + 16 * lane) = (u32x4){wv[0], wv[1], wv[2], wv[3]};
    }
    SLEEP(ESLEEP);
    uint32_t tu = latest(gate(ws, O_SU + (uint32_t)(256 * bx + (ALLP ? tid : 64 * w + 4 * i + (bj & 3))) * 128u, T));
    asm volatile("s_mov_b64 exec, 1\n\tds_max_u32 %0, %1\n\ts_mov_b64 exec, -1" : : "v"(ca + 16), "v"(tu) : "memory");
    if (w == 0) sel[43] = T;
    if (isx) {
      const uint32_t xo = u_at(tid, dX);
      char* sm = smem + X_UL + C_U - 16 + 16 * tid;
      const u32x4 ut = {utag(T, 0), utag(T, 1), utag(T, 2), utag(T, 3)};
      __builtin_amdgcn_s_setprio(3);
      until(tu + UD + XD);
      u32x4 uw;
      asm volatile("global_load_dwordx4 %0, %1, %2 sc1" : "=&v"(uw) : "v"(xo), "s"(ws) : "memory");
      asm volatile("s_waitcnt vmcnt(0)" : "+v"(uw));
      uw ^= ut;
      u_again(uw, ut, xo);
      *(u32x4*)(sm + 16) = uw;
      BAR();
      if (w < 3) {
        const u32x4 pw = *(const u32x4*)sm;
        if (w < 2) sums<true>(smem + X_IM + 16 * tid, pw, uw); else sums<false>(smem + X_IM + 16 * tid, pw, uw);
        count_up(ca + 28);
      }
    } else {
      until(tu + UD + WD);
      BAR();
    }
    tu = (uint32_t)__builtin_amdgcn_readfirstlane(cnt[4]);
    TS(0, 3);
    __builtin_amdgcn_s_setprio(DPRIO);
    if (ffn) {
      ts1 = (tu + UD + VD2 - TSTOP) | 1u;
      gu_dma(0, N0, IC<40>{});
      gu_dma(N0, NA, IC<WDEPTH>{});
      asm volatile("s_waitcnt vmcnt(%0)" : : "n"(NA - 9) : "memory");
      gsync0(ca + 4, 4);
      gu_read(0);
      asm volatile("s_waitcnt lgkmcnt(0)" ::: "memory");
      count_up(ca + 36);
      TS(0, 4);
      gu_dma(NA, 18, IC<WDEPTH2>{});
      gwait(ca + 36, 4);
      down_dma(0, KN, lane);
      asm volatile("s_waitcnt vmcnt(%0)" : : "n"(3 * KN) : "memory");
      count_up(ca + 4);
      TS(0, 7);
    } else {
      if (!isx && C3) {
        const int n = 6 * min(tid, 191) + bj - 18, r = n >> 2;
        const char* wb = (const char*)((n & 1) ? wu : wg) + bx * 1152;
        const int c0 = (3 - (int)((uintptr_t)wb >> 7)) & 7;
        wb += (uint32_t)((r + 144 * ((r >= 144) + ((n >> 1) & 1))) * (F_ * 2) + 128 * c0);
        TOUCH(wb);
        if (c0 == 0) TOUCH(wb + 1024);
      }
      auto d_line = [&](int k) {
        const int n = min((k * 14 + bj - 18) * 64 + lane, 5183), q = n / 1296, r = n - 1296 * q;
        return (const char*)wd + (uint32_t)(bx * 5184 + (r / 18) * 72 + 18 * (HREV ? 3 - q : q) + r % 18) * 128u;
      };
      int k = w;
      if (w < HW) {
#pragma unroll 1
        for (; k < 6 && (int)((uint32_t)__builtin_amdgcn_s_memrealtime() - (tu + UD + VD2 - HSTOP)) < 0; k += HW) { TOUCH(d_line(k)); DRAIN(); }
      }
      TS(0, 4);
      until(tu + UD + VD2 + RD2);
      if (RW) gwait(ca, 4);
      if (w < HW) {
#pragma unroll 1
        for (; k < 6; k += HW) { TOUCH(d_line(k)); DRAIN(); }
      }
      TS(0, 7);
    }
    tv = tu + UD + VD2 - vearly;
    tk = tu + UD + VD2 + KD;
  }
  {
  const int tid = tid2, lane = tid & 63, i = lane & 15, kq = lane >> 4;
  __builtin_amdgcn_s_setprio(3);
  const int PL = ffn ? 6400 : LW + 6656;
  f32x2 xf[9];
  f32x4 za = {0.f, 0.f, 0.f, 0.f}, zb = za;
  uint32_t al, ha, h0a, h1a, so, da;
  auto ffn_terms = [&] {
    al = O_XC + 64u * (i >> 3) + 16u * kq + 128u * w;
    ha = (ROLE == 1) ? (uint32_t)((4 * (kq & 1) + 2 * (kq >> 1) + (i >> 3)) * 80 + 16 * w + 2 * (i & 7)) : 0u;
    h0a = (uint32_t)(min(i, 8) * 80 + 16 * kq); h1a = (uint32_t)(min(i ^ 8, 8) * 80 + 16 * kq);
    so = O_SL + (uint32_t)fb * SP + ((ROLE == 2) ? 16u * tid - 4096u : 8192u + 16u * lane);
    da = lb0 + (uint32_t)((8 * kq + ((lane >> 2) & 3)) * 1152 + 32 * ((8 * kq + ((lane >> 2) & 3) + 2) >> 2) + 8 * (lane & 3) + ((ROLE == 2) ? 128 * wl : 1024));
    if (ROLE == 2) asm volatile("" : "+v"(h0a), "+v"(h1a), "+v"(so), "+v"(da)); else asm volatile("" : "+v"(ha), "+v"(al), "+v"(da));
  };
  if (ROLE == 1) ffn_terms();
  uint32_t hv[9] = {}, nv[9] = {}, keya = 0u, keyb = 0u;
  int rka = 0, rkb = 0;
  auto put_row = [&](const uint32_t* r, int j, const void* base, uint32_t ro) {
    const u32x4 tl = {r[8], (uint32_t)DPP_I((int)r[8], 0x55, 0xf), (uint32_t)DPP_I((int)r[8], 0xaa, 0xf), (uint32_t)DPP_I((int)r[8], 0xff, 0xf)};
    asm volatile("global_store_dwordx4 %0, %1, %2 sc1\n\ts_nop 1" : : "v"(ro + 16u * lane), "v"((u32x4){r[4 * j], r[4 * j + 1], r[4 * j + 2], r[4 * j + 3]}), "s"(base) : "memory");
    if ((lane & 35) == 32 * j) asm volatile("global_store_dwordx4 %0, %1, %2 offset:1024 sc1\n\ts_nop 1" : : "v"(ro + 4u * (lane & 31)), "v"(tl), "s"(base) : "memory");
  };
  if (ln2) {
    const int tl = tid - 256, d1 = 64 * (tl >> 5) + (tl & 31), d3 = 64 * ((tl >> 4) & 7) + 32 + (tl & 15), d2 = (tl < 128) ? d3 + 16 : min(384 + tl, 575);
    uint32_t vo = v_at(d1, g0), vo2 = v_at(d2, g0), vo3 = v_at(d3, g0);
    u32x4 ut = {utag(T, 0), utag(T, 1), utag(T, 2), utag(T, 3)};
    u32x4 va, vc = ut, vb = ut;
    asm volatile("" : "+v"(vo), "+v"(vo2), "+v"(vo3), "+v"(ut), "+v"(vc), "+v"(vb));
    TS(0, 7);
    until(tv);
    TS(0, 8);
    asm volatile("global_load_dwordx4 %0, %2, %4 sc1\n\ts_cmp_lt_u32 %5, 2\n\ts_cbranch_scc1 1f\n\tglobal_load_dwordx4 %1, %3, %4 sc1\n1:"
                 : "=&v"(va), "+v"(vc) : "v"(vo), "v"(vo3), "s"(ws), "s"(wl) : "memory", "scc");
    until(tv + V2D);
    asm volatile("s_cmp_eq_u32 %3, 3\n\ts_cbranch_scc1 1f\n\tglobal_load_dwordx4 %0, %1, %2 sc1\n1:" : "+v"(vb) : "v"(vo2), "s"(ws), "s"(wl) : "memory", "scc");
    __builtin_amdgcn_sched_barrier(0);
    if (isx) {
      const u32x4 ra = *(const u32x4*)(smem + X_RW(wl) + 16 * lane), rb = *(const u32x4*)(smem + X_RW(wl) + 1024 + 16 * lane), rw = *(const u32x4*)(smem + X_WR + 16 * lane);
      const u32x2 rc = *(const u32x2*)(smem + X_RW(wl) + 2048 + 8 * lane);
#pragma unroll
      for (int k = 0; k < 4; ++k) { xv[k] = ra[k]; xv[4 + k] = rb[k]; wv[k] = rw[k]; }
      xv[8] = rc[0]; wv[4] = rc[1];
    }
    uint32_t pa = (uint32_t)(PL + 9216 * wl + 32 * lane), pb = (uint32_t)(PL + 9216 * wl + 2048 + 8 * (lane & 31)), ps = (lane < 32) ? 0u : 16u;
    uint32_t kid = (uint32_t)(((255 - g2) << 5) | (7 - 2 * wl - (lane >> 5)));
    uint32_t px = (uint32_t)(PL + 4 * d1), py = (uint32_t)(PL + 4 * d2), pz = (uint32_t)(PL + 4 * d3);
#pragma unroll
    for (int k = 0; k < 9; ++k) xf[k] = (f32x2){bflo(xv[k]), bfhi(xv[k])};
    asm volatile("" : "+v"(xf[0]), "+v"(xf[1]), "+v"(xf[2]), "+v"(xf[3]), "+v"(xf[4]), "+v"(xf[5]), "+v"(xf[6]), "+v"(xf[7]), "+v"(xf[8]));
    asm volatile("" : "+v"(za), "+v"(zb), "+v"(pa), "+v"(pb), "+v"(ps), "+v"(kid), "+v"(px), "+v"(py), "+v"(pz));
    float sv[10], ws_ = dot2(wv[4], 0x3f803f80u, 0.f) * 0.5f;
#pragma unroll
    for (int k = 0; k < 4; ++k) ws_ = dot2(wv[k], 0x3f803f80u, ws_);
    ws_ = wave_sum(ws_);
#pragma unroll
    for (int k = 0; k < 10; ++k) sv[k] = 16.f;
    uint32_t ka_ = lb0 + LM + 4u * (128 + 48 + 2 * wl + (lane >> 5)), kb_ = lb0 + LM + 4u * (128 + 48);
    uint32_t ko_ = O_KEY + (uint32_t)(256 * (lane & 7) + 8 * (g2 & 31) + (g2 >> 5)) * 16u;
    asm volatile("" : "+v"(ws_), "+v"(sv[0]), "+v"(sv[1]), "+v"(sv[2]), "+v"(sv[3]), "+v"(sv[4]), "+v"(sv[5]), "+v"(sv[6]), "+v"(sv[7]), "+v"(sv[8]), "+v"(sv[9]),
                 "+v"(ka_), "+v"(kb_), "+v"(ko_));
    if (ffn) gwait(ca + 36, 4);
    asm volatile("s_waitcnt vmcnt(1)\n\ts_cmp_eq_u32 %2, 3\n\ts_cbranch_scc0 1f\n\ts_waitcnt vmcnt(0)\n1:" : "+v"(va), "+v"(vc) : "s"(wl) : "memory", "scc");
    auto turn1 = [&] {
#pragma unroll
      for (int e = 0; e < 4; ++e) *(uint32_t*)(smem + px + 9216 * e) = va[e];
      if (wl >= 2) {
#pragma unroll
        for (int e = 0; e < 4; ++e) *(uint32_t*)(smem + pz + 9216 * e) = vc[e];
      }
    };
    auto turn = [&] {
      if (wl < 3) {
#pragma unroll
        for (int e = 0; e < 4; ++e) *(uint32_t*)(smem + py + 9216 * e) = vb[e];
      }
    };
    va ^= ut; vc ^= ut;
    turn1();
    asm volatile("s_waitcnt vmcnt(0)" : "+v"(vb) : : "memory");
    vb ^= ut;
    turn();
    const u32x4 vs = va | vb | vc;
    if (__builtin_expect(__any(((vs[0] | vs[1] | vs[2] | vs[3]) & UMASK) != 0u), 0)) {
#pragma unroll 1
      for (int spin = 0; spin < 4; ++spin) {
        const bool na = ((va[0] | va[1] | va[2] | va[3]) & UMASK) != 0u, nb = ((vb[0] | vb[1] | vb[2] | vb[3]) & UMASK) != 0u, nc = ((vc[0] | vc[1] | vc[2] | vc[3]) & UMASK) != 0u;
        if (!__any(na || nb || nc)) break;
        TSC(16);
        if (na) LD16(va, vo);
        if (nb) LD16(vb, vo2);
        if (nc) LD16(vc, vo3);
        asm volatile("s_waitcnt vmcnt(0)" : "+v"(va), "+v"(vb), "+v"(vc) : : "memory");
        if (na) va ^= ut;
        if (nb) vb ^= ut;
        if (nc) vc ^= ut;
      }
      auto again = [&](u32x4& v, uint32_t o, int d) {
        const int J = g0 >> 5;
        const uint32_t dn = O_DN + 16u * (uint32_t)((d < 512) ? 4 * (d >> 1) + ((0x01013232 >> (4 * J)) & 3) : 4 * (d - 256) + (J >> 1));
        bool pend = ((v[0] | v[1] | v[2] | v[3]) & UMASK) != 0u;
#pragma unroll 1
        for (int spin = 0; spin < SPIN_CAP && __any(pend); ++spin) {
          TSC(16);
          SLEEP(PSLEEP);
          if (pend) {
            uint32_t fin = ~T;
            if ((v[0] & v[1] & v[2] & v[3] & LMASK) == LMASK)
              asm volatile("global_load_dword %0, %1, %2 sc1\n\ts_waitcnt vmcnt(0)" : "=&v"(fin) : "v"(dn), "s"(ws) : "memory");
            LD16W(v, o);
            v ^= ut;
            pend = ((v[0] | v[1] | v[2] | v[3]) & UMASK) != 0u;
            if (pend && fin == T) {
              u32x2 sc;
              for (int k = 0; k < SPIN_CAP; ++k) {
                asm volatile("global_load_dwordx2 %0, %1, %2 sc1\n\ts_waitcnt vmcnt(0)" : "=&v"(sc) : "v"(O_SC + 16u * d), "s"(ws) : "memory");
                if (sc[0] == T) break;
              }
              *(u16*)(smem + L_SC + 2 * d) = (u16)(sc[1] >> 16);
              cnt[10] = 1u;
              v = (v ^ ut) & ~LMASK;
              pend = false;
            }
          }
        }
      };
      again(va, vo, d1); again(vc, vo3, d3); again(vb, vo2, d2);
      turn1();
      turn();
    }
    u32x4 a0, a1;
    u32x2 a2, cf;
    {
      uint32_t cc, n = SPIN_CAP;
      asm volatile("s_mov_b64 exec, 1\n\tv_mov_b32 v90, 1\n\tds_add_u32 %6, v90\n\ts_mov_b64 exec, -1\n"
                   "1:\n\tds_read2_b32 %3, %6 offset1:10\n\tds_read_b128 %0, %7\n\tds_read_b128 %1, %7 offset:16\n\tds_read_b64 %2, %8\n\t"
                   "s_sub_u32 %5, %5, 1\n\ts_cbranch_scc1 2f\n\ts_waitcnt lgkmcnt(3)\n\tv_readfirstlane_b32 %4, v90\n\t"
                   "s_cmp_lt_u32 %4, 4\n\ts_cbranch_scc1 1b\n2:\n\ts_waitcnt lgkmcnt(0)"
                   : "=&v"(a0), "=&v"(a1), "=&v"(a2), "=&{v[90:91]}"(cf), "=&s"(cc), "+s"(n) : "v"(ca), "v"(lb0 + pa), "v"(lb0 + pb) : "memory", "scc");
    }
    TS(0, 9);
    if (__builtin_expect(__builtin_amdgcn_readfirstlane(cf[1]) != 0, 0)) {
      const u32x4 q = *(const u32x4*)(smem + L_SC + 16 * lane);
      const uint32_t r = *(const uint32_t*)(smem + L_SC + 1024 + 4 * (lane & 31));
#pragma unroll
      for (int k = 0; k < 4; ++k) { sv[2 * k] = bflo(q[k]); sv[2 * k + 1] = bfhi(q[k]); }
      sv[8] = bflo(r); sv[9] = bfhi(r);
    }
    const uint32_t av[10] = {a0[0], a0[1], a0[2], a0[3], a1[0], a1[1], a1[2], a1[3], a2[0] >> ps, a2[1] >> ps};
    f32x2 h[9];
#pragma unroll
    for (int k = 0; k < 10; ++k) {
      asm("v_fma_mix_f32 %0, %1, %2, %3 op_sel_hi:[1,0,0]" : "=v"(h[(k < 8) ? k >> 1 : 8][k & 1]) : "v"(av[k]), "v"(sv[k]), "v"(xf[(k < 8) ? k >> 1 : 8][k & 1]));
      if (k < 8) asm("v_fma_mix_f32 %0, %1, %2, %3 op_sel:[1,0,0] op_sel_hi:[1,0,0]" : "=v"(h[4 + (k >> 1)][k & 1]) : "v"(av[k]), "v"(sv[k]), "v"(xf[4 + (k >> 1)][k & 1]));
    }
#pragma unroll
    for (int k = 0; k < 9; ++k) hv[k] = pack2(h[k][0], h[k][1]);
    const float da = dot2(hv[2], wv[2], dot2(hv[0], wv[0], zb[0])) + dot2(hv[3], wv[3], dot2(hv[1], wv[1], zb[1]));
    const float db = dot2(hv[6], wv[2], dot2(hv[4], wv[0], zb[2])) + dot2(hv[7], wv[3], dot2(hv[5], wv[1], zb[3]));
    float dt = dot2(hv[8], wv[4], fold32(da, db));
    f32x2 rc, nc;
    ln_stats<true>(hv, lane, 1.f, xf, za, rc, nc, dt);
    dt = (lane < 32) ? dt * rc[0] + ws_ * nc[0] : dt * rc[1] + ws_ * nc[1];
    const uint32_t bb = pack2(dt, dt) & 0xffff0000u;
    const uint32_t kk = (bb ^ (((uint32_t)((int)bb >> 31) & 0x7fff0000u) | 0x80000000u)) | kid;
    u32x4 ka, kb;
    uint32_t old;
    asm volatile("s_mov_b64 exec, %5\n\tds_write_b32 %3, %4\n\ts_mov_b64 exec, 1\n\tv_mov_b32 %2, 1\n\tds_add_rtn_u32 %2, %7, %2\n\ts_mov_b64 exec, -1\n\t"
                 "ds_read_b128 %0, %6\n\tds_read_b128 %1, %6 offset:16\n\ts_waitcnt lgkmcnt(0)"
                 : "=&v"(ka), "=&v"(kb), "=&v"(old) : "v"(ka_), "v"(kk), "s"(0x8000000080000000ull), "v"(kb_), "v"(ca) : "memory");
    if (__builtin_amdgcn_readfirstlane(old) == 7) {
      uint32_t m1 = max(max(ka[0], ka[1]), ka[2]), m2 = med3u(ka[0], ka[1], ka[2]), m3 = min(min(ka[0], ka[1]), ka[2]);
#pragma unroll
      for (int e = 0; e < 5; ++e) {
        const uint32_t kx = e ? kb[e - 1] : ka[3];
        m3 = med3u(m2, m3, kx); m2 = med3u(m1, m2, kx); m1 = max(m1, kx);
      }
      if (lane < 8) ST16(ko_, ((u32x4){m1 | 24u, m2 | 16u, m3 | 8u, T & ~1u}));
    }
    TS(0, 10);
    keya = (uint32_t)__builtin_amdgcn_readlane(kk, 31); keyb = (uint32_t)__builtin_amdgcn_readlane(kk, 63);
    ln_apply<false>(xf, lane, rc, nc, nv);
    gwait(ca, 8);
    if (wl == 1) {
      const u32x4 kv = *(const u32x4*)&kl[4 * (lane & 1)];
      if (lane < 2) ST16(O_KF + (uint32_t)(2 * g2 + lane) * 16u, ((u32x4){__builtin_amdgcn_perm(kv[1], kv[0], 0x07060302u), T, __builtin_amdgcn_perm(kv[3], kv[2], 0x07060302u), T}));
      if (ROLE == 2 && b == 0 && lane == 0) ST4(O_EP, T * 0x0E8B2F51u);
    }
    const uint32_t ko = kl[lane & 7];
    rka = FORCEFB + __popcll(__ballot(ko > keya) & 0xffull); rkb = FORCEFB + __popcll(__ballot(ko > keyb) & 0xffull);
#pragma unroll
    for (int k = 0; k < 9; ++k) {
      const f32x2 o = xf[k] + (f32x2){bflo(nv[k]), bfhi(nv[k])};
      hv[k] = pack2(o[0], o[1]);
      nv[k] = min(nv[k], 0xfffffffeu);
    }
    if (rka < 2) put_row(nv, 0, ws, O_XC + (uint32_t)(1023 - 4 * g2 - rka) * 1152u);
    if (rkb < 2) put_row(nv, 1, ws, O_XC + (uint32_t)(1023 - 4 * g2 - rkb) * 1152u);
    if (!LPOS && ffn) down_dma(KN, 4, lane);
    ffn_terms();
  }
  uint32_t k3rd = 0u, k4th = 0u;
  uint32_t q0 = lb0 + LM + 4u * lane, q1 = lb0 + LM + 32u * w, q2 = q1 + 4u * (lane & 7), q3 = lb0 + LM + 512u + 4u * (lane & 7), q4 = lb0 + LM + (uint32_t)(tid & ~7);
  uint32_t r8 = C_, sb = lb0 + LM + 512u;
  asm volatile("" : "+v"(q0), "+v"(q1), "+v"(q2), "+v"(q3), "+v"(q4), "+v"(r8), "+v"(sb));
  if constexpr (ROLE == 1) {
    u32x4 key;
    const uint32_t kof = O_KEY + (uint32_t)(256 * bx + tid) * 16u;
    until(tk);
    if (!ffn) SLEEP(NSLEEP);
    LD16(key, kof);
    if (ffn) {
      gwait0(ca + 4, 8);
      gu_read(1);
    }
    asm volatile("s_waitcnt vmcnt(0)" : "+v"(key) : : "memory");
    if (__builtin_expect(__any(key[3] != (T & ~1u)), 0)) {
#pragma unroll 1
      for (int spin = 0; spin < SPIN_CAP; ++spin) {
        const bool need = key[3] != (T & ~1u);
        if (!__any(need)) break;
        TSC(15);
        SLEEP(KBACK);
        if (need) LD16W(key, kof);
      }
    }
    TS(0, 11);
    const uint32_t m1 = oct_maxu(key[0]);
    const bool hit = key[0] == m1;
    const uint32_t k0 = hit ? key[1] : key[0], k1 = hit ? key[2] : key[1];
    const uint32_t m2 = oct_maxu(k0);
    *LP(u32x2, q4) = (u32x2){m1, m2};
    k3rd = (k0 == m2) ? k1 : k0;
    k4th = (k0 == m2) ? (hit ? 0u : key[2]) : k1;
    if (RPF == 1 && !ffn && w == (bj & 3)) {
      const uint32_t r1 = O_XC + ((m1 >> 3) & 0x3ffu) * 1152u + 128u * (lane & 7), r2 = O_XC + ((m2 >> 3) & 0x3ffu) * 1152u + 128u * (lane & 7);
      asm volatile("global_load_dword v126, %0, %3 sc1\n\tglobal_load_dword v126, %1, %3 sc1\n\tglobal_load_dword v126, %2, %3 offset:1024 sc1"
                   : : "v"(r1), "v"(r2), "v"(((lane & 1) ? r1 : r2) - 128u * (lane & 7)), "s"(ws) : "memory", "v126");
    }
  }
  auto pick = [&](auto co, auto so_) {
    BAR();
    const uint32_t c = *LP(uint32_t, q0 + co.value), own = *LP(uint32_t, q2 + co.value);
    const u32x4 la = *LP(u32x4, q1 + co.value), lb = *LP(u32x4, q1 + co.value + 16);
    uint32_t r = r8;
    asm("v_cmp_gt_u32 s[40:41], %1, %2\n\tv_cmp_gt_u32 s[42:43], %1, %3\n\tv_cmp_gt_u32 s[44:45], %1, %4\n\tv_cmp_gt_u32 s[46:47], %1, %5\n\t"
        "v_cmp_gt_u32 s[48:49], %1, %6\n\tv_cmp_gt_u32 s[50:51], %1, %7\n\tv_cmp_gt_u32 s[52:53], %1, %8\n\tv_cmp_gt_u32 s[54:55], %1, %9\n\t"
        "s_bcnt1_i32_b64 s40, s[40:41]\n\ts_bcnt1_i32_b64 s42, s[42:43]\n\tv_writelane_b32 %0, s40, 0\n\ts_bcnt1_i32_b64 s44, s[44:45]\n\t"
        "v_writelane_b32 %0, s42, 1\n\ts_bcnt1_i32_b64 s46, s[46:47]\n\tv_writelane_b32 %0, s44, 2\n\ts_bcnt1_i32_b64 s48, s[48:49]\n\t"
        "v_writelane_b32 %0, s46, 3\n\ts_bcnt1_i32_b64 s50, s[50:51]\n\tv_writelane_b32 %0, s48, 4\n\ts_bcnt1_i32_b64 s52, s[52:53]\n\t"
        "v_writelane_b32 %0, s50, 5\n\ts_bcnt1_i32_b64 s54, s[54:55]\n\tv_writelane_b32 %0, s52, 6\n\tv_writelane_b32 %0, s54, 7"
        : "+v"(r) : "v"(c), "v"(la[0]), "v"(la[1]), "v"(la[2]), "v"(la[3]), "v"(lb[0]), "v"(lb[1]), "v"(lb[2]), "v"(lb[3])
        : "scc", "s40", "s41", "s42", "s43", "s44", "s45", "s46", "s47", "s48", "s49", "s50", "s51", "s52", "s53", "s54", "s55");
    *LP(uint32_t, sb + 4u * min(r, (uint32_t)C_) + so_.value) = own;
    BAR();
  };
  auto rows = [&](uint32_t ao, auto&& meanwhile) {
    u32x4 aa, ab, ac = __builtin_bit_cast(u32x4, zb);
    asm volatile("global_load_dwordx4 %0, %3, %4 sc1\n\tglobal_load_dwordx4 %1, %3, %4 offset:512 sc1\n\t"
                 "s_cmp_lg_u32 %5, 0\n\ts_cbranch_scc1 1f\n\tglobal_load_dwordx4 %2, %3, %4 offset:1024 sc1\n1:"
                 : "=&v"(aa), "=&v"(ab), "+v"(ac) : "v"(ao), "s"(ws), "s"(w) : "memory", "scc");
    __builtin_amdgcn_sched_barrier(0);
    meanwhile();
    asm volatile("s_waitcnt vmcnt(0)" : "+v"(aa), "+v"(ab), "+v"(ac) : : "memory");
    if (__builtin_expect(__any(max(max(max4(aa), max4(ab)), max4(ac)) == 0xffffffffu), 0)) {
#pragma unroll 1
      for (int spin = 0; spin < SPIN_CAP; ++spin) {
        const bool na = max4(aa) == 0xffffffffu, nb = max4(ab) == 0xffffffffu, nc = max4(ac) == 0xffffffffu;
        if (!__any(na || nb || nc)) break;
        TSC(14);
        SLEEP(PSLEEP);
        if (na) LD16W(aa, ao);
        if (nb) LD16W(ab, ao + 512u);
        if (nc) LD16W(ac, ao + 1024u);
      }
    }
    *(u32x4*)(smem + LA + 16 * tid) = aa;
    *(u32x4*)(smem + LA + 4096 + 16 * tid) = ab;
    if (w == 0) *(u32x4*)(smem + LA + 8192 + 16 * tid) = ac;
  };
  auto rows_dma = [&](uint32_t ao) {
    uint32_t dl = lb0 + LA + 1024u * w, keep;
    asm volatile("s_mov_b32 %0, m0\n\ts_mov_b32 m0, %1\n\ts_add_u32 %1, %1, 4096 - 512\n\tglobal_load_lds_dwordx4 %2, %3 sc1\n\ts_mov_b32 m0, %1\n\ts_add_u32 %1, %1, 4096 - 512\n\t"
                 "global_load_lds_dwordx4 %2, %3 offset:512 sc1\n\ts_cmp_lg_u32 %4, 0\n\ts_cbranch_scc1 1f\n\ts_mov_b32 m0, %1\n\ts_nop 0\n\t"
                 "global_load_lds_dwordx4 %2, %3 offset:1024 sc1\n1:\n\ts_mov_b32 m0, %0" : "=&s"(keep), "+s"(dl) : "v"(ao), "s"(ws), "s"(w) : "memory", "scc");
  };
  auto get_bd = [&](int t0, int t1) {
#pragma unroll
    for (int t = t0; t < t1; ++t) {
      const u32x2 h0 = trd(da + 512 * (t >> 2) + 32 * (t & 3)), h1 = trd(da + 512 * (t >> 2) + 32 * (t & 3) + 4608 + 32);
      bw[t] = (u32x4){h0[0], h0[1], h1[0], h1[1]};
    }
#pragma unroll
    for (int t = t0; t < t1; ++t) asm volatile("" : "+v"(bw[t]));
  };
  pick(IC<0>{}, IC<0>{});
  uint32_t mykey = *LP(uint32_t, q3), ro = ((mykey >> 3) & 0x3ffu) * 1152u;
  TS(0, 12);
  auto check = [&] {
    cnt[(k3rd > (uint32_t)__builtin_amdgcn_readlane(mykey, 7)) ? 5 : 6] = 1u;
    const uint32_t m3 = oct_maxu(k3rd);
    if (k3rd == m3) k3rd = k4th;
    if ((lane & 7) == 0) cand[72 + (tid >> 3)] = m3;
    if (w == 0 && lane < 32) cand[(lane < 8) ? 64 + lane : 96 + lane] = (lane < 8) ? mykey : 0u;
  };
  if constexpr (ROLE == 1) {
    if (ffn && !FORCEFB) rows_dma(ro + al);
    check();
    DRAIN();
  } else if (ffn) {
    if (LPOS) { SLEEP(LSLEEP); down_dma(KN, 4, lane); } else { DRAIN(); gsync0(ca, 12); get_bd(0, 8); }
  }
  BAR();
  TS(0, 5);
  auto put_out = [&](int j, uint32_t k0, int rk) {
    const uint64_t mine = __ballot(((mykey ^ k0) & ~24u) == 0u) & 0xffull;
    if (mine == 0ull) put_row(hv, j, out, (uint32_t)((8 * g2 + 2 * wl + j) * (D_ * 2)));
    else if (rk >= 2) put_row(nv, j, ws, O_FB + 1152u * (uint32_t)__builtin_ctzll(mine));
  };
  auto rest_out = [&] { put_out(0, keya, rka); put_out(1, keyb, rkb); };
  f32x4 lo, hi;
  auto gate_up = [&] {
    u32x4 av[5];
    const uint32_t aa = lb0 + LA + 16u * lane;
#pragma unroll
    for (int j = 0; j < 5; ++j) asm volatile("ds_read_b128 %0, %1 offset:%2" : "=v"(av[j]) : "v"(aa), "n"(1024 * j) : "memory");
    lo = (f32x4){0.f, 0.f, 0.f, 0.f}; hi = lo;
#pragma unroll
    for (int j = 0; j < 9; ++j) {
      if (j < 5) asm volatile("s_waitcnt lgkmcnt(4)" : "+v"(av[j % 5]));
      else if (j == 5) asm volatile("s_waitcnt lgkmcnt(2)" : "+v"(av[0]), "+v"(av[1]));
      else if (j == 7) asm volatile("s_waitcnt lgkmcnt(0)" : "+v"(av[2]), "+v"(av[3]));
      lo = MMB(av[j % 5], bw[2 * j], lo);
      hi = MMB(av[j % 5], bw[2 * j + 1], hi);
      __builtin_amdgcn_sched_barrier(0);
      if (j < 4) asm volatile("ds_read_b128 %0, %1 offset:%2" : "=v"(av[j]) : "v"(aa), "n"(1024 * (j + 5)) : "memory");
    }
  };
  uint32_t fl;
  asm volatile("ds_read_b32 %0, %1" : "=v"(fl) : "v"(ca + 20) : "memory");
  if (ROLE == 1) gate_up();
  asm volatile("s_waitcnt lgkmcnt(0)" : "+v"(fl));
  const bool slow = FORCESLOW || FORCEFB || __builtin_amdgcn_readfirstlane(fl) != 0;
  if (__builtin_expect(slow, 0)) {
    TSC(17);
    pick(IC<256>{}, IC<64>{});
    mykey = sel[16 + (lane & 7)];
    ro = ((mykey >> 3) & 0x3ffu) * 1152u;
    if (ROLE == 1 && (k3rd > (uint32_t)__builtin_amdgcn_readlane(mykey, 7) || (mykey & 24u) == 8u)) cnt[11] = 1u;
    BAR();
    if (FORCESLOW > 1 || FORCEFB || __builtin_amdgcn_readfirstlane(cnt[11]) != 0) {
    u32x4 key;
#pragma unroll 1
    for (int spin = 0; spin < SPIN_CAP; ++spin) {
      LD16W(key, O_KF + 16u * tid);
      if (!__any(key[1] != T || key[3] != T)) break;
      SLEEP(KBACK);
    }
    const uint32_t ib = (uint32_t)(((255 - (tid >> 1)) << 5) | (7 - 4 * (tid & 1)));
    uint32_t k0 = (key[0] << 16) | ib, k1 = (key[0] & 0xffff0000u) | (ib - 1), k2 = (key[2] << 16) | (ib - 2), k3 = (key[2] & 0xffff0000u) | (ib - 3);
    CEX(k0, k1) CEX(k2, k3) CEX(k0, k2) CEX(k1, k3) CEX(k1, k2)
#pragma unroll 1
    for (int c = 0; c < C_; ++c) {
      const uint32_t mx = wave_maxu(k0);
      const bool hit = (k0 == mx);
      k0 = hit ? k1 : k0; k1 = hit ? k2 : k1; k2 = hit ? k3 : k2; k3 = hit ? 0u : k3;
      if (lane == 0) cand[64 + 8 * w + c] = mx;
    }
    pick(IC<256>{}, IC<128>{});
    mykey = sel[32 + (lane & 7)];
    const uint32_t okey = sel[32 + (lane >> 3)];
    const uint64_t same = __ballot(((mykey ^ okey) & 0x1fe0u) == 0u && okey > mykey);
    const int sl = FORCEFB + __popcll((same >> (lane & 7)) & 0x0101010101010101ull);
    ro = (sl < 2) ? (((mykey >> 3) & 0x3ffu) + 3 - sl) * 1152u : O_FB - O_XC + 1152u * (lane & 7);
    }
    if (ln2) rest_out();
    if (ffn) {
      if (ROLE == 1) rows(ro + al, [] {});
      BAR();
      if (ROLE == 1) gate_up();
    }
  }
  if (OPOS == 0 && ROLE == 2 && ffn && !slow) { SLEEP(OD); rest_out(); }
  if (!ffn) {
    if (ln2 && !slow) { SLEEP(OD2); rest_out(); }
    TSE();
    return;
  }
  uint32_t fx = 0, fv = 0, fn = 0, fo = 0, sa[9];
  float rs, hx, xs;
  u32x4 se;
  if constexpr (ROLE == 1) {
    f32x4 lh = (lane < 32) ? lo : hi;
    float fa = fold32(lh[0], lh[2]), fb2 = fold32(lh[1], lh[3]);
    if (__builtin_expect(__any(__builtin_isunordered(fa, fb2)) && !slow, 0)) {
      gsync0(ca + 4, 12);
      rows(ro + al, [] {});
      gsync0(ca + 4, 16);
      gate_up();
      lh = (lane < 32) ? lo : hi;
      fa = fold32(lh[0], lh[2]); fb2 = fold32(lh[1], lh[3]);
    }
    const float g = __int_as_float(__builtin_amdgcn_update_dpp(__float_as_int(fa), __float_as_int(fb2), 0x128, 0xf, 0xc, false));
    const float u = __int_as_float(__builtin_amdgcn_update_dpp(__float_as_int(fb2), __float_as_int(fa), 0x128, 0xf, 0x3, false));
    const uint32_t gu = pack2(g, u);
    const float gp = bflo(gu);
    const float sg = rbf(gp * __builtin_amdgcn_rcpf(1.f + __expf(-gp)));
    *(u16*)((char*)hm + ha) = f2bf(sg * bfhi(gu));
  } else if (LPOS) {
    DRAIN();
    gsync0(ca, 12);
    get_bd(0, 8);
  }
  BAR();
  TS(0, 6);
  if (ROLE == 2 || w == 2) {
    const u32x4 a0 = *(const u32x4*)((char*)hm + h0a), a1 = *(const u32x4*)((char*)hm + h1a);
    if (ROLE == 1) get_bd(0, 4);
    const f32x4 z = {0.f, 0.f, 0.f, 0.f};
    f32x4 o[4];
#pragma unroll
    for (int t = 0; t < 2 * ROLE; ++t) o[t] = MMB(a1, bw[2 * t + 1], MMB(a0, bw[2 * t], z));
#pragma unroll
    for (int t = 0; t < ROLE; ++t) {
      u32x4 sv;
#pragma unroll
      for (int r = 0; r < 4; ++r) sv[r] = min(pack2(o[2 * t][r], o[2 * t + 1][r]), 0xfffffffeu);
      ST16(so + 4096u * t, sv);
    }
    TS(0, 13);
  }
  if (OPOS == 1 && ROLE == 2) {
    SLEEP(OSLEEP);
    if (!slow) rest_out();
  }
  if (ROLE == 1 && w == 1) {
    const int q = (fb & 15) >> 2, ts = 4 * (q & 1) + ((i & 7) >> 1);
    const int dd = 64 * (fb >> 4) + 16 * (2 * (i & 1) + (q >> 1)) + 4 * (fb & 3) + 2 * (kq & 1) + (kq >> 1);
    const uint32_t fkey = (uint32_t)__builtin_amdgcn_ds_bpermute(4 * ts, (int)mykey), fr = (uint32_t)__builtin_amdgcn_ds_bpermute(4 * ts, (int)ro);
    const int ft = 2047 - (int)(((fkey >> 2) & 0x7f8u) | (fkey & 7u));
    fo = (uint32_t)(ft * D_ + dd) * 2u;
    u32x2 fs;
    asm volatile("global_load_ushort %0, %4, %8\n\tglobal_load_ushort %1, %5, %9 sc1\n\tglobal_load_ushort %2, %6, %9 sc1\n\tglobal_load_dwordx2 %3, %7, %9 sc1"
                 : "=&v"(fx), "=&v"(fv), "=&v"(fn), "=&v"(fs) : "v"(fo), "v"(v_at(dd, ft >> 3) + 2u * (ft & 7)), "v"(O_XC + fr + 2u * dd), "v"(O_SC + 16u * dd), "s"(x), "s"(ws) : "memory");
    uint32_t t16 = (((T >> (3 * ((ft >> 1) & 3))) & LMASK) | (((T >> (12 + ((ft >> 1) & 3))) & 0x00010001u) << 14)) >> (16 * (ft & 1)) & 0xffffu;
    const uint32_t o = fkey >> 16;
    const float rs0 = bf2f((u16)((o & 0x8000u) ? (o & 0x7fffu) : (~o & 0xffffu)));
    rs = rbf(__builtin_amdgcn_rcpf(1.f + __expf(-rs0)));
    const uint32_t one = (i < 8) ? (0x3f80u << (16 * (i & 1))) : 0u;
    se = (u32x4){(i >> 1) == 0 ? one : 0u, (i >> 1) == 1 ? one : 0u, (i >> 1) == 2 ? one : 0u, (i >> 1) == 3 ? one : 0u};
#pragma unroll
    for (int m = 0; m < 9; ++m) sa[m] = O_SL + (uint32_t)(16 * m + (lane >> 2)) * SP + (uint32_t)(4 * fb + (lane & 3)) * 16u;
    asm volatile("" : "+v"(t16), "+v"(rs), "+v"(se), "+v"(sa[0]), "+v"(sa[1]), "+v"(sa[2]), "+v"(sa[3]), "+v"(sa[4]), "+v"(sa[5]), "+v"(sa[6]), "+v"(sa[7]), "+v"(sa[8]));
    SLEEP(SSLEEP);
    TS(0, 1);
    float sum;
#pragma unroll 1
    for (int spin = 0; spin < SPIN_CAP; ++spin) {
      u32x4 sv[9];
#pragma unroll
      for (int m = 0; m < 9; ++m) LD16(sv[m], sa[m]);
      asm volatile("s_waitcnt vmcnt(9)" : "+v"(fx), "+v"(fv), "+v"(fn), "+v"(fs));
      if (__builtin_expect(__any(fs[0] != T), 0)) {
        for (int k = 0; k < SPIN_CAP && fs[0] != T; ++k)
          asm volatile("global_load_dwordx2 %0, %1, %2 sc1\n\ts_waitcnt vmcnt(0)" : "=&v"(fs) : "v"(O_SC + 16u * dd), "s"(ws) : "memory");
      }
      const uint32_t f1 = fv ^ t16, f2 = (f1 & 7u) ? fv & ~7u : f1;
      hx = rbf(bf2f((u16)fx) + (float)__builtin_bit_cast(_Float16, (u16)f2) * __uint_as_float(fs[1]));
      xs = bf2f((u16)fn);
      asm volatile("" : "+v"(hx), "+v"(xs));
      f32x4 dv = {0.f, 0.f, 0.f, 0.f};
      static_for<0, 9>([&](auto mm) {
        constexpr int m = mm.value;
        asm volatile("s_waitcnt vmcnt(%1)" : "+v"(sv[m]) : "n"(8 - m));
        dv = MMB(sv[m], se, dv);
        __builtin_amdgcn_sched_barrier(0);
      });
      sum = fold16(fold32(dv[0], dv[1]), fold32(dv[2], dv[3]));
      if (__builtin_expect(!__any(sum != sum), 1)) break;
      uint32_t mx = 0u;
#pragma unroll
      for (int m = 0; m < 9; ++m) mx = max(mx, max4(sv[m]));
      if (!__any(mx == 0xffffffffu)) break;
      TSC(18);
      SLEEP(PSLEEP);
    }
    const float o1 = rbf(xs + rbf(rbf(sum) * rs));
    if (i < 8) asm volatile("global_store_short %0, %1, %2 sc1" : : "v"(fo), "v"((uint32_t)f2bf(hx + o1)), "s"(out) : "memory");
    TS(0, 2);
  }
  TSE();
  }
  }
}
#define BROT "192"
#define CPF_A "s_getpc_b64 s[96:97]\n\ts_lshl_b32 s99, s2, 4\n\ts_lshl_b32 s84, s98, 7\n\ts_add_u32 s99, s99, s84\n\tv_and_b32 v112, 1, v0\n\t" \
              "v_lshl_add_u32 v112, v112, 12, s99\n\tv_min_u32 v112, kf_end-kf_all-0x80, v112\n\ts_mov_b64 exec, 3\n\tglobal_load_dword v112, v112, s[96:97]\n\ts_mov_b64 exec, -1\n\t"
extern "C" __global__ __launch_bounds__(512, 4) void kf_all(KARGS) {
  __shared__ __attribute__((aligned(16))) char smem[SMEM];
  asm volatile("s_load_dwordx16 s[64:79], s[0:1], 0x0\n\ts_load_dwordx4 s[80:83], s[0:1], 0x40\n\tv_readfirstlane_b32 s98, v0\n\t"
               "s_add_u32 s84, s2, " BROT "\n\ts_xor_b32 s84, s84, s2\n\ts_bitset0_b32 s84, 8\n\ts_bitset0_b32 s84, 9\n\ts_xor_b32 s2, s2, s84\n\t"
               "v_mov_b32 v109, 0\n\ts_and_b32 s85, s2, 7\n\ts_bitcmp1_b32 s2, 8\n\ts_cbranch_scc1 1f\n\t" CPF_A "s_bitcmp1_b32 s98, 8\n\ts_cbranch_scc1 2f\n\t"
               "s_andn2_b32 s84, s2, 31\n\ts_lshl2_add_u32 s84, s85, s84\n\ts_bfe_u32 s85, s2, 0x20003\n\ts_or_b32 s84, s84, s85\n\t"
               "s_mulk_i32 s84, 0x2400\n\ts_mulk_i32 s98, 36\n\ts_add_u32 s84, s84, s98\n\tv_and_b32 v110, 63, v0\n\tv_and_b32 v111, 32, v0\n\t"
               "v_lshlrev_b32 v111, 5, v111\n\tv_lshl_add_u32 v111, v110, 2, v111\n\tv_lshl_add_u32 v110, v110, 4, s84\n\tv_add_u32 v111, s84, v111\n\t"
               "s_cmpk_lt_u32 s2, 0x90\n\ts_waitcnt lgkmcnt(0)\n\t"
               "global_load_dwordx4 v[100:103], v110, s[64:65]\n\tglobal_load_dwordx4 v[104:107], v110, s[64:65] offset:1152\n\t"
               "global_load_dword v108, v111, s[64:65] offset:1024\n\tglobal_load_dword v109, v109, s[82:83] sc1\n\t"
               "s_cbranch_scc1 kf_n\n\ts_branch kf_h\n"
               "1:\n\ts_bfe_u32 s84, s2, 0x50003\n\ts_lshl_b32 s85, s85, 5\n\ts_or_b32 s84, s84, s85\n\ts_lshl_b32 s84, s84, 2\n\t"
               "v_and_b32 v110, 0xff, v0\n\tv_bfe_u32 v107, v0, 7, 1\n"
               "4:\n\ts_lshl_b32 s85, s84, 7\n\tv_lshl_add_u32 v110, v110, 1, s85\n\tv_lshl_add_u32 v107, v107, 1, s84\n\ts_and_b32 s85, s98, s2\n\ts_waitcnt lgkmcnt(0)\n\t"
               "global_load_ushort v100, v107, s[78:79]\n\tglobal_load_ushort v101, v110, s[66:67]\n\tglobal_load_ushort v102, v110, s[68:69]\n\t"
               "global_load_dword v109, v109, s[82:83] sc1\n\ts_bitcmp1_b32 s85, 8\n\ts_cbranch_scc1 kf_u\n\t"
               "global_load_ushort v103, v110, s[70:71]\n\tglobal_load_ushort v104, v110, s[72:73]\n\tglobal_load_ushort v105, v110, s[74:75]\n\t"
               "global_load_ushort v106, v110, s[76:77]\n\tglobal_load_ushort v107, v107, s[80:81]\n\ts_bitcmp1_b32 s2, 8\n\ts_cbranch_scc1 kf_c\n"
               "6:\n\ts_barrier\n\ts_branch kf_p\n"
               "2:\n\ts_cmpk_lt_u32 s2, 0xc0\n\ts_cbranch_scc1 6b\n\t"
               "s_lshr_b32 s84, s2, 3\n\ts_lshl3_add_u32 s84, s85, s84\n\ts_lshl1_add_u32 s84, s84, 976\n\t"
               "v_and_b32 v110, 0x7f, v0\n\tv_mov_b32 v107, 0\n\ts_branch 4b"
               : : "s"(__builtin_amdgcn_kernarg_segment_ptr()), "s"(blockIdx.x), "v"(threadIdx.x),
                   "s"((uint32_t)(uintptr_t)(__attribute__((address_space(3))) char*)smem) : "scc", "s96", "s97", "s98", "s99", "v127");
}
extern "C" __global__ __launch_bounds__(512, 4) void kf_c(KARGS) {
  __shared__ __attribute__((aligned(16))) char smem[SMEM];
  role_body<0>(smem, KPASS);
}
extern "C" __global__ __launch_bounds__(512, 4) void kf_u(KARGS) {
  __shared__ __attribute__((aligned(16))) char smem[SMEM];
  role_body<3>(smem, KPASS);
}
extern "C" __global__ __launch_bounds__(512, 4) void kf_n(KARGS) {
  __shared__ __attribute__((aligned(16))) char smem[SMEM];
  role_body<1, 1>(smem, KPASS);
}
extern "C" __global__ __launch_bounds__(512, 4) void kf_h(KARGS) {
  __shared__ __attribute__((aligned(16))) char smem[SMEM];
  role_body<1, 0>(smem, KPASS);
}
extern "C" __global__ __launch_bounds__(512, 4) void kf_p(KARGS) {
  __shared__ __attribute__((aligned(16))) char smem[SMEM];
  role_body<2>(smem, KPASS);
}
extern "C" __global__ __launch_bounds__(512, 4) void kf_conv() { asm volatile(CONV_ASM ::: CONV_CLOB); }
extern "C" __global__ void kf_end() {}

extern "C" void kernel_launch(void* const* d_in, const int* in_sizes, int n_in,
                              void* d_out, int out_size, void* d_ws, size_t ws_size,
                              hipStream_t stream) {
  kf_all<<<512, 512, 0, stream>>>((const u16*)d_in[0], (const u16*)d_in[1], (const u16*)d_in[2], (const u16*)d_in[3], (const u16*)d_in[4],
                                  (const u16*)d_in[5], (const u16*)d_in[6], (const u16*)d_in[7], (const u16*)d_in[8], (char*)d_ws + (size_t)WSOFF * 1048576,
                                  (const u16*)d_in[10], (const u16*)d_in[11], (const u16*)d_in[12], (const u16*)d_in[13], (u16*)d_out);
}
```

```cpp
#include <hip/hip_runtime.h>
#include <hip/hip_bf16.h>
#include <stdint.h>
#define DDEPTH 9
#define NA 13

typedef __attribute__((ext_vector_type(8))) short bf16x8;
typedef __attribute__((ext_vector_type(4))) float f32x4;
typedef uint16_t u16;
typedef __attribute__((ext_vector_type(4))) uint32_t u32x4;
typedef __attribute__((ext_vector_type(2))) uint32_t u32x2;
#define TSD uint32_t pr0_ = 0u, pr1_ = 0u
__device__ unsigned long long g_prb[4096];
#define TS(k, i) do { if ((i) == 0) pr0_ = (uint32_t)__builtin_amdgcn_s_memrealtime() | 1u; \
    if (ROLE == 1 && (i) == 2 && (1 < 0 || w == 1) && (blockIdx.x & 255) < 144) pr1_ = (uint32_t)__builtin_amdgcn_s_memrealtime() | 1u; } while (0)
#define TSC(i)
#define TSE() do { if (__builtin_amdgcn_mbcnt_hi(-1, __builtin_amdgcn_mbcnt_lo(-1, 0)) == 0) \
    __hip_atomic_store(&g_prb[blockIdx.x * 8 + w], ((unsigned long long)pr1_ << 32) | pr0_, __ATOMIC_RELAXED, __HIP_MEMORY_SCOPE_AGENT); } while (0)
#define TSP
#define TSA

constexpr int L_ = 2048, D_ = 576, N_ = 128, F_ = 4608, C_ = 8;
constexpr int NF = 144;
constexpr uint32_t O_EP = 0;
constexpr uint32_t O_KF = 4096;
constexpr uint32_t O_KEY = O_KF + 8192;
constexpr uint32_t O_SC = O_KEY + 32768;
constexpr uint32_t O_DN = O_SC + 9216;
constexpr uint32_t O_SU = O_DN + 20480;
constexpr uint32_t O_FB = O_SU + 262144;
constexpr uint32_t O_XC = O_FB + 8 * 1152;
constexpr uint32_t SP = 9216 + 128;
constexpr uint32_t O_SL = O_XC + 1024 * 1152;
constexpr uint32_t SLU = 336;
constexpr uint32_t O_U = O_SL + 256 * SLU * 16;
constexpr int UP = 584;
constexpr uint32_t VP = 8192 + 128, VPP = 4096 + 128;
constexpr uint32_t O_V = O_U + 256 * UP * 16, O_VP = O_V + 256 * VP;
static_assert(256 * SLU * 16 >= NF * SP && O_SL % 128 == 0 && O_U % 128 == 0 && O_V % 128 == 0, "");
__host__ __device__ __forceinline__ uint32_t u_at(int tg, int d) { return O_U + 16u * (uint32_t)((tg >> 1) * (UP * 2) + (d >> 2) * 8 + (tg & 1) * 4 + (d & 3)); }
__host__ __device__ __forceinline__ uint32_t v_at(int d, int tg) {
  return (d < 512) ? O_V + (uint32_t)(d >> 1) * VP + 16u * (uint32_t)(2 * tg + (d & 1)) : O_VP + (uint32_t)(d - 512) * VPP + 16u * (uint32_t)tg;
}
#define HAVE_VAT
#define HAVE_VTAG
__device__ __forceinline__ float bf2f(u16 v) { return __uint_as_float((uint32_t)v << 16); }
__device__ __forceinline__ float bflo(uint32_t v) { return __uint_as_float(v << 16); }
__device__ __forceinline__ float bfhi(uint32_t v) { return __uint_as_float(v & 0xffff0000u); }
__device__ __forceinline__ u16 f2bf(float f) { return __builtin_bit_cast(u16, (__bf16)f); }
__device__ __forceinline__ float rbf(float f) { return bf2f(f2bf(f)); }
typedef __attribute__((ext_vector_type(2))) float f32x2;
typedef __attribute__((ext_vector_type(2))) __bf16 bf16x2v;
__device__ __forceinline__ uint32_t pack2(float a, float b) {
  return __builtin_bit_cast(uint32_t, __builtin_convertvector((f32x2){a, b}, bf16x2v));
}

#define DPP_I(v, ctrl, rmask) __builtin_amdgcn_update_dpp(0, (v), (ctrl), (rmask), 0xf, true)
__device__ __forceinline__ float row_sum(float v) {
  v += __int_as_float(DPP_I(__float_as_int(v), 0x111, 0xf));
  v += __int_as_float(DPP_I(__float_as_int(v), 0x112, 0xf));
  v += __int_as_float(DPP_I(__float_as_int(v), 0x114, 0xf));
  v += __int_as_float(DPP_I(__float_as_int(v), 0x118, 0xf));
  return v;
}
__device__ __forceinline__ float rdl(float v, int l) { return __int_as_float(__builtin_amdgcn_readlane(__float_as_int(v), l)); }
__device__ __forceinline__ float wave_sum(float v) {
  v = row_sum(v);
  asm("s_nop 1\n\tv_add_f32_dpp %0, %0, %0 row_bcast:15 row_mask:0xa bank_mask:0xf\n\t"
      "s_nop 1\n\tv_add_f32_dpp %0, %0, %0 row_bcast:31 row_mask:0xc bank_mask:0xf\n\ts_nop 0" : "+v"(v));
  return rdl(v, 63);
}
__device__ __forceinline__ float fold32(float a, float b) {
  const auto r = __builtin_amdgcn_permlane32_swap(__float_as_uint(a), __float_as_uint(b), false, false);
  return __uint_as_float(r[0]) + __uint_as_float(r[1]);
}
__device__ __forceinline__ float fold16(float a, float b) {
  const auto r = __builtin_amdgcn_permlane16_swap(__float_as_uint(a), __float_as_uint(b), false, false);
  return __uint_as_float(r[0]) + __uint_as_float(r[1]);
}
__device__ __forceinline__ void wave_sum2(float& a, float& b) {
  float v = row_sum(fold32(a, b));
  asm("s_nop 1\n\tv_add_f32_dpp %0, %0, %0 row_bcast:15 row_mask:0xa bank_mask:0xf\n\ts_nop 0" : "+v"(v));
  a = rdl(v, 31); b = rdl(v, 63);
}
__device__ __forceinline__ float dot2(uint32_t a, uint32_t b, float c) {
  return __builtin_amdgcn_fdot2_f32_bf16(__builtin_bit_cast(bf16x2v, a), __builtin_bit_cast(bf16x2v, b), c, false);
}
__device__ __forceinline__ uint32_t row_maxu(uint32_t v) {
  v = max(v, (uint32_t)DPP_I((int)v, 0xb1, 0xf));
  v = max(v, (uint32_t)DPP_I((int)v, 0x4e, 0xf));
  v = max(v, (uint32_t)DPP_I((int)v, 0x141, 0xf));
  v = max(v, (uint32_t)DPP_I((int)v, 0x140, 0xf));
  return v;
}
__device__ __forceinline__ uint32_t wave_maxu(uint32_t v) {
  v = max(v, (uint32_t)DPP_I((int)v, 0x111, 0xf));
  v = max(v, (uint32_t)DPP_I((int)v, 0x112, 0xf));
  v = max(v, (uint32_t)DPP_I((int)v, 0x114, 0xf));
  v = max(v, (uint32_t)DPP_I((int)v, 0x118, 0xf));
  v = max(v, (uint32_t)DPP_I((int)v, 0x142, 0xa));
  v = max(v, (uint32_t)DPP_I((int)v, 0x143, 0xc));
  return (uint32_t)__builtin_amdgcn_readlane((int)v, 63);
}

typedef __attribute__((ext_vector_type(8))) _Float16 f16x8;
typedef __attribute__((ext_vector_type(2))) _Float16 f16x2;
__device__ __forceinline__ uint32_t packh(f32x2 v) { return __builtin_bit_cast(uint32_t, __builtin_convertvector(v, f16x2)); }
__device__ __forceinline__ uint32_t packh(float a, float b) { return packh((f32x2){a, b}); }
constexpr int IP = 224, I_X = 128;
__device__ __forceinline__ f32x2 cmul(f32x2 z, f32x2 w) {
  f32x2 r;
  asm("v_pk_mul_f32 %0, %1, %2 op_sel_hi:[0,1]\n\tv_pk_fma_f32 %0, %1, %2, %0 op_sel:[1,1,0] op_sel_hi:[1,0,1] neg_lo:[0,1,0]" : "=&v"(r) : "v"(z), "v"(w));
  return r;
}
__device__ __forceinline__ f32x2 cexp2(float ar2, float air, float l) {
  const float t = air * l;
  return (f32x2){__builtin_amdgcn_cosf(t), __builtin_amdgcn_sinf(t)} * __builtin_amdgcn_exp2f(ar2 * l);
}
__device__ __forceinline__ float state_a(float ldt, float are, float aim, float& ar2, float& air, float& g) {
  const float dt = rbf(__expf(ldt));
  ar2 = dt * are * 1.4426950408889634f;
  air = dt * aim * 0.15915494309189535f;
  air -= __builtin_rintf(air);
  g = 0.f;
  if (__builtin_expect(__any(ar2 > 0.f), 0)) g = fminf(__uint_as_float(wave_maxu(__float_as_uint(fmaxf(ar2, 0.f)))), 1.f);
  return dt;
}
template <int N> __device__ __forceinline__ void put_im(char* p, int im, const f32x2* z) {
  *(u32x4*)p = (u32x4){packh(z[0][0], z[1][0]), packh(z[2][0], z[3][0]), packh(z[4][0], z[5][0]), packh(z[6][0], z[7][0])};
  *(u32x4*)(p + im) = (u32x4){packh(z[0][1], z[1][1]), packh(z[2][1], z[3][1]), packh(z[4][1], z[5][1]), packh(z[6][1], z[7][1])};
}
#define Y_ASM \
  "v_mov_b64 v[40:41], %0\nv_pk_mul_f32 v[42:43], v[40:41], %1 op_sel_hi:[0,1]\nv_pk_mul_f32 v[44:45], %0, %2 op_sel_hi:[0,1]\n" \
  "v_pk_fma_f32 v[42:43], v[40:41], %1, v[42:43] op_sel:[1,1,0] op_sel_hi:[1,0,1] neg_lo:[0,1,0]\n" \
  "v_pk_fma_f32 v[44:45], %0, %2, v[44:45] op_sel:[1,1,0] op_sel_hi:[1,0,1] neg_lo:[0,1,0]\nv_cvt_pk_f16_f32 v56, v40, v42\n" \
  "v_cvt_pk_f16_f32 v60, v41, v43\nv_pk_mul_f32 v[40:41], v[42:43], %1 op_sel_hi:[0,1]\nv_pk_mul_f32 v[48:49], v[44:45], %2 op_sel_hi:[0,1]\n" \
  "v_pk_fma_f32 v[40:41], v[42:43], %1, v[40:41] op_sel:[1,1,0] op_sel_hi:[1,0,1] neg_lo:[0,1,0]\n" \
  "v_pk_fma_f32 v[48:49], v[44:45], %2, v[48:49] op_sel:[1,1,0] op_sel_hi:[1,0,1] neg_lo:[0,1,0]\n" \
  "v_pk_mul_f32 v[42:43], v[40:41], %1 op_sel_hi:[0,1]\nv_pk_mul_f32 v[52:53], v[48:49], %2 op_sel_hi:[0,1]\n" \
  "v_pk_fma_f32 v[42:43], v[40:41], %1, v[42:43] op_sel:[1,1,0] op_sel_hi:[1,0,1] neg_lo:[0,1,0]\n" \
  "v_pk_fma_f32 v[52:53], v[48:49], %2, v[52:53] op_sel:[1,1,0] op_sel_hi:[1,0,1] neg_lo:[0,1,0]\nv_cvt_pk_f16_f32 v57, v40, v42\n" \
  "v_cvt_pk_f16_f32 v61, v41, v43\nv_pk_mul_f32 v[40:41], v[42:43], %1 op_sel_hi:[0,1]\n" \
  "v_pk_fma_f32 v[40:41], v[42:43], %1, v[40:41] op_sel:[1,1,0] op_sel_hi:[1,0,1] neg_lo:[0,1,0]\n" \
  "v_pk_mul_f32 v[42:43], v[40:41], %1 op_sel_hi:[0,1]\nv_pk_mul_f32 v[46:47], v[44:45], %1 op_sel_hi:[0,1]\n" \
  "v_pk_fma_f32 v[42:43], v[40:41], %1, v[42:43] op_sel:[1,1,0] op_sel_hi:[1,0,1] neg_lo:[0,1,0]\n" \
  "v_pk_fma_f32 v[46:47], v[44:45], %1, v[46:47] op_sel:[1,1,0] op_sel_hi:[1,0,1] neg_lo:[0,1,0]\nv_cvt_pk_f16_f32 v58, v40, v42\n" \
  "v_cvt_pk_f16_f32 v62, v41, v43\nv_cvt_pk_f16_f32 v64, v44, v46\nv_cvt_pk_f16_f32 v68, v45, v47\n" \
  "v_pk_mul_f32 v[40:41], v[42:43], %1 op_sel_hi:[0,1]\nv_pk_mul_f32 v[44:45], v[46:47], %1 op_sel_hi:[0,1]\n" \
  "v_pk_fma_f32 v[40:41], v[42:43], %1, v[40:41] op_sel:[1,1,0] op_sel_hi:[1,0,1] neg_lo:[0,1,0]\n" \
  "v_pk_fma_f32 v[44:45], v[46:47], %1, v[44:45] op_sel:[1,1,0] op_sel_hi:[1,0,1] neg_lo:[0,1,0]\n" \
  "v_pk_mul_f32 v[42:43], v[40:41], %1 op_sel_hi:[0,1]\nv_pk_mul_f32 v[46:47], v[44:45], %1 op_sel_hi:[0,1]\n" \
  "v_pk_fma_f32 v[42:43], v[40:41], %1, v[42:43] op_sel:[1,1,0] op_sel_hi:[1,0,1] neg_lo:[0,1,0]\n" \
  "v_pk_fma_f32 v[46:47], v[44:45], %1, v[46:47] op_sel:[1,1,0] op_sel_hi:[1,0,1] neg_lo:[0,1,0]\nv_cvt_pk_f16_f32 v59, v40, v42\n" \
  "v_cvt_pk_f16_f32 v63, v41, v43\nv_cvt_pk_f16_f32 v65, v44, v46\nv_cvt_pk_f16_f32 v69, v45, v47\n" \
  "v_pk_mul_f32 v[44:45], v[46:47], %1 op_sel_hi:[0,1]\nds_write_b128 %3, v[56:59] offset:0\n" \
  "v_pk_fma_f32 v[44:45], v[46:47], %1, v[44:45] op_sel:[1,1,0] op_sel_hi:[1,0,1] neg_lo:[0,1,0]\nds_write_b128 %3, v[60:63] offset:64\n" \
  "v_pk_mul_f32 v[46:47], v[44:45], %1 op_sel_hi:[0,1]\nv_pk_mul_f32 v[50:51], v[48:49], %1 op_sel_hi:[0,1]\n" \
  "v_pk_fma_f32 v[46:47], v[44:45], %1, v[46:47] op_sel:[1,1,0] op_sel_hi:[1,0,1] neg_lo:[0,1,0]\n" \
  "v_pk_fma_f32 v[50:51], v[48:49], %1, v[50:51] op_sel:[1,1,0] op_sel_hi:[1,0,1] neg_lo:[0,1,0]\nv_cvt_pk_f16_f32 v66, v44, v46\n" \
  "v_cvt_pk_f16_f32 v70, v45, v47\nv_cvt_pk_f16_f32 v56, v48, v50\nv_cvt_pk_f16_f32 v60, v49, v51\n" \
  "v_pk_mul_f32 v[44:45], v[46:47], %1 op_sel_hi:[0,1]\nv_pk_mul_f32 v[48:49], v[50:51], %1 op_sel_hi:[0,1]\n" \
  "v_pk_fma_f32 v[44:45], v[46:47], %1, v[44:45] op_sel:[1,1,0] op_sel_hi:[1,0,1] neg_lo:[0,1,0]\n" \
  "v_pk_fma_f32 v[48:49], v[50:51], %1, v[48:49] op_sel:[1,1,0] op_sel_hi:[1,0,1] neg_lo:[0,1,0]\n" \
  "v_pk_mul_f32 v[46:47], v[44:45], %1 op_sel_hi:[0,1]\nv_pk_mul_f32 v[50:51], v[48:49], %1 op_sel_hi:[0,1]\n" \
  "v_pk_fma_f32 v[46:47], v[44:45], %1, v[46:47] op_sel:[1,1,0] op_sel_hi:[1,0,1] neg_lo:[0,1,0]\n" \
  "v_pk_fma_f32 v[50:51], v[48:49], %1, v[50:51] op_sel:[1,1,0] op_sel_hi:[1,0,1] neg_lo:[0,1,0]\nv_cvt_pk_f16_f32 v67, v44, v46\n" \
  "v_cvt_pk_f16_f32 v71, v45, v47\nv_cvt_pk_f16_f32 v57, v48, v50\nv_cvt_pk_f16_f32 v61, v49, v51\n" \
  "v_pk_mul_f32 v[48:49], v[50:51], %1 op_sel_hi:[0,1]\nds_write_b128 %3, v[64:67] offset:16\n" \
  "v_pk_fma_f32 v[48:49], v[50:51], %1, v[48:49] op_sel:[1,1,0] op_sel_hi:[1,0,1] neg_lo:[0,1,0]\nds_write_b128 %3, v[68:71] offset:80\n" \
  "v_pk_mul_f32 v[50:51], v[48:49], %1 op_sel_hi:[0,1]\nv_pk_mul_f32 v[54:55], v[52:53], %1 op_sel_hi:[0,1]\n" \
  "v_pk_fma_f32 v[50:51], v[48:49], %1, v[50:51] op_sel:[1,1,0] op_sel_hi:[1,0,1] neg_lo:[0,1,0]\n" \
  "v_pk_fma_f32 v[54:55], v[52:53], %1, v[54:55] op_sel:[1,1,0] op_sel_hi:[1,0,1] neg_lo:[0,1,0]\nv_cvt_pk_f16_f32 v58, v48, v50\n" \
  "v_cvt_pk_f16_f32 v62, v49, v51\nv_cvt_pk_f16_f32 v64, v52, v54\nv_cvt_pk_f16_f32 v68, v53, v55\n" \
  "v_pk_mul_f32 v[48:49], v[50:51], %1 op_sel_hi:[0,1]\nv_pk_mul_f32 v[52:53], v[54:55], %1 op_sel_hi:[0,1]\n" \
  "v_pk_fma_f32 v[48:49], v[50:51], %1, v[48:49] op_sel:[1,1,0] op_sel_hi:[1,0,1] neg_lo:[0,1,0]\n" \
  "v_pk_fma_f32 v[52:53], v[54:55], %1, v[52:53] op_sel:[1,1,0] op_sel_hi:[1,0,1] neg_lo:[0,1,0]\n" \
  "v_pk_mul_f32 v[50:51], v[48:49], %1 op_sel_hi:[0,1]\nv_pk_mul_f32 v[54:55], v[52:53], %1 op_sel_hi:[0,1]\n" \
  "v_pk_fma_f32 v[50:51], v[48:49], %1, v[50:51] op_sel:[1,1,0] op_sel_hi:[1,0,1] neg_lo:[0,1,0]\n" \
  "v_pk_fma_f32 v[54:55], v[52:53], %1, v[54:55] op_sel:[1,1,0] op_sel_hi:[1,0,1] neg_lo:[0,1,0]\nv_cvt_pk_f16_f32 v59, v48, v50\n" \
  "v_cvt_pk_f16_f32 v63, v49, v51\nv_cvt_pk_f16_f32 v65, v52, v54\nv_cvt_pk_f16_f32 v69, v53, v55\n" \
  "v_pk_mul_f32 v[52:53], v[54:55], %1 op_sel_hi:[0,1]\nds_write_b128 %3, v[56:59] offset:32\n" \
  "v_pk_fma_f32 v[52:53], v[54:55], %1, v[52:53] op_sel:[1,1,0] op_sel_hi:[1,0,1] neg_lo:[0,1,0]\nds_write_b128 %3, v[60:63] offset:96\n" \
  "v_pk_mul_f32 v[54:55], v[52:53], %1 op_sel_hi:[0,1]\n" \
  "v_pk_fma_f32 v[54:55], v[52:53], %1, v[54:55] op_sel:[1,1,0] op_sel_hi:[1,0,1] neg_lo:[0,1,0]\nv_cvt_pk_f16_f32 v66, v52, v54\n" \
  "v_cvt_pk_f16_f32 v70, v53, v55\nv_pk_mul_f32 v[52:53], v[54:55], %1 op_sel_hi:[0,1]\n" \
  "v_pk_fma_f32 v[52:53], v[54:55], %1, v[52:53] op_sel:[1,1,0] op_sel_hi:[1,0,1] neg_lo:[0,1,0]\n" \
  "v_pk_mul_f32 v[54:55], v[52:53], %1 op_sel_hi:[0,1]\n" \
  "v_pk_fma_f32 v[54:55], v[52:53], %1, v[54:55] op_sel:[1,1,0] op_sel_hi:[1,0,1] neg_lo:[0,1,0]\nv_cvt_pk_f16_f32 v67, v52, v54\n" \
  "v_cvt_pk_f16_f32 v71, v53, v55\nds_write_b128 %3, v[64:67] offset:48\nds_write_b128 %3, v[68:71] offset:112\n"
#define Y_CLOB "memory", "v40", "v41", "v42", "v43", "v44", "v45", "v46", "v47", "v48", "v49", "v50", "v51", "v52", "v53", "v54", "v55", "v56", "v57", "v58", "v59", "v60", "v61", "v62", "v63", "v64", "v65", "v66", "v67", "v68", "v69", "v70", "v71"
#define T_ASM \
  "ds_read_b128 %4, %8\nds_read_b64_tr_b16 v[80:81], %7 offset:0\nds_read_b64_tr_b16 v[82:83], %7 offset:64\n" \
  "ds_read_b64_tr_b16 v[84:85], %5 offset:0\nds_read_b64_tr_b16 v[86:87], %5 offset:48\nds_read_b64_tr_b16 v[88:89], %6 offset:0\n" \
  "ds_read_b64_tr_b16 v[90:91], %6 offset:48\nds_read_b64_tr_b16 v[92:93], %7 offset:3584\nds_read_b64_tr_b16 v[94:95], %7 offset:3648\n" \
  "ds_read_b64_tr_b16 v[96:97], %5 offset:3584\nds_read_b64_tr_b16 v[98:99], %5 offset:3632\nds_read_b64_tr_b16 v[100:101], %6 offset:3584\n" \
  "ds_read_b64_tr_b16 v[102:103], %6 offset:3632\nds_read_b64_tr_b16 v[104:105], %7 offset:7168\nds_read_b64_tr_b16 v[106:107], %7 offset:7232\n" \
  "s_waitcnt lgkmcnt(8)\nv_mfma_f32_16x16x32_f16 %0, v[84:87], v[80:83], 0\nds_read_b64_tr_b16 v[108:109], %5 offset:7168\n" \
  "ds_read_b64_tr_b16 v[110:111], %5 offset:7216\nds_read_b64_tr_b16 v[112:113], %6 offset:7168\nv_mfma_f32_16x16x32_f16 %2, v[88:91], v[80:83], 0\n" \
  "ds_read_b64_tr_b16 v[114:115], %6 offset:7216\nds_read_b64_tr_b16 v[116:117], %7 offset:10752\nds_read_b64_tr_b16 v[118:119], %7 offset:10816\n" \
  "s_waitcnt lgkmcnt(8)\nv_mfma_f32_16x16x32_f16 %0, v[96:99], v[92:95], %0\nds_read_b64_tr_b16 v[120:121], %5 offset:10752\n" \
  "ds_read_b64_tr_b16 v[122:123], %5 offset:10800\nds_read_b64_tr_b16 v[124:125], %6 offset:10752\n" \
  "v_mfma_f32_16x16x32_f16 %2, v[100:103], v[92:95], %2\nds_read_b64_tr_b16 v[126:127], %6 offset:10800\n" \
  "ds_read_b64_tr_b16 v[80:81], %7 offset:14336\nds_read_b64_tr_b16 v[82:83], %7 offset:14400\ns_waitcnt lgkmcnt(8)\n" \
  "v_mfma_f32_16x16x32_f16 %0, v[108:111], v[104:107], %0\nds_read_b64_tr_b16 v[84:85], %5 offset:14336\n" \
  "ds_read_b64_tr_b16 v[86:87], %5 offset:14384\nds_read_b64_tr_b16 v[88:89], %6 offset:14336\n" \
  "v_mfma_f32_16x16x32_f16 %2, v[112:115], v[104:107], %2\nds_read_b64_tr_b16 v[90:91], %6 offset:14384\n" \
  "ds_read_b64_tr_b16 v[92:93], %7 offset:17920\nds_read_b64_tr_b16 v[94:95], %7 offset:17984\ns_waitcnt lgkmcnt(8)\n" \
  "v_mfma_f32_16x16x32_f16 %0, v[120:123], v[116:119], %0\nds_read_b64_tr_b16 v[96:97], %5 offset:17920\n" \
  "ds_read_b64_tr_b16 v[98:99], %5 offset:17968\nds_read_b64_tr_b16 v[100:101], %6 offset:17920\n" \
  "v_mfma_f32_16x16x32_f16 %2, v[124:127], v[116:119], %2\nds_read_b64_tr_b16 v[102:103], %6 offset:17968\n" \
  "ds_read_b64_tr_b16 v[104:105], %7 offset:21504\nds_read_b64_tr_b16 v[106:107], %7 offset:21568\ns_waitcnt lgkmcnt(8)\n" \
  "v_mfma_f32_16x16x32_f16 %1, v[84:87], v[80:83], 0\nds_read_b64_tr_b16 v[108:109], %5 offset:21504\nds_read_b64_tr_b16 v[110:111], %5 offset:21552\n" \
  "ds_read_b64_tr_b16 v[112:113], %6 offset:21504\nv_mfma_f32_16x16x32_f16 %3, v[88:91], v[80:83], 0\nds_read_b64_tr_b16 v[114:115], %6 offset:21552\n" \
  "ds_read_b64_tr_b16 v[116:117], %7 offset:25088\nds_read_b64_tr_b16 v[118:119], %7 offset:25152\ns_waitcnt lgkmcnt(8)\n" \
  "v_mfma_f32_16x16x32_f16 %1, v[96:99], v[92:95], %1\nds_read_b64_tr_b16 v[120:121], %5 offset:25088\n" \
  "ds_read_b64_tr_b16 v[122:123], %5 offset:25136\nds_read_b64_tr_b16 v[124:125], %6 offset:25088\n" \
  "v_mfma_f32_16x16x32_f16 %3, v[100:103], v[92:95], %3\nds_read_b64_tr_b16 v[126:127], %6 offset:25136\ns_waitcnt lgkmcnt(6)\n" \
  "v_mfma_f32_16x16x32_f16 %1, v[108:111], v[104:107], %1\nv_mfma_f32_16x16x32_f16 %3, v[112:115], v[104:107], %3\ns_waitcnt lgkmcnt(0)\n" \
  "v_mfma_f32_16x16x32_f16 %1, v[120:123], v[116:119], %1\nv_mfma_f32_16x16x32_f16 %3, v[124:127], v[116:119], %3\n"
#define T_CLOB "memory", "v80", "v81", "v82", "v83", "v84", "v85", "v86", "v87", "v88", "v89", "v90", "v91", "v92", "v93", "v94", "v95", "v96", "v97", "v98", "v99", "v100", "v101", "v102", "v103", "v104", "v105", "v106", "v107", "v108", "v109", "v110", "v111", "v112", "v113", "v114", "v115", "v116", "v117", "v118", "v119", "v120", "v121", "v122", "v123", "v124", "v125", "v126", "v127"
template <class F> __device__ __forceinline__ int gen_y(float ldt, float are, float aim, float bre, float bim, float cre, float cim, uint32_t im, F&& mid TSP) {
  float ar2, air, g;
  const float dt = state_a(ldt, are, aim, ar2, air, g);
  const float dbr = dt * bre, dbi = dt * bim, pr = cre * dbr - cim * dbi, pi = cre * dbi + cim * dbr;
  const f32x2 w8 = cexp2(ar2, -air, 8.f), w64 = cexp2(ar2, -air, 64.f);
  const float pm = fmaxf(fabsf(pr), fabsf(pi));
  int e = -4;
  if (__builtin_expect(__any(!(pm < 0x1p-4f) || ar2 > 0.f) || !__any(pm > 0x1p-12f), 0)) e = (int)(wave_maxu(__float_as_uint(pm)) >> 23) - 126 + (int)ceilf(g * 250.f);
  const f32x2 y0 = {__builtin_amdgcn_ldexpf(pr, 14 - e), __builtin_amdgcn_ldexpf(-pi, 14 - e)};
  mid();
  asm volatile(Y_ASM : : "v"(y0), "v"(w8), "v"(w64), "v"(im) : Y_CLOB);
  return e;
}
__device__ __forceinline__ int gen_x(float ldt, float are, float aim, char* im) {
  float ar2, air, g;
  state_a(ldt, are, aim, ar2, air, g);
  const int e = min((int)ceilf(g * 1810.f), 300);
  const float s = __builtin_amdgcn_ldexpf(1.f, 15 - e);
  const f32x2 w1 = cexp2(ar2, air, 1.f), w256 = cexp2(ar2, air, 256.f), w2 = cmul(w1, w1);
  f32x2 a[8], b[8], c[8];
  a[0] = (f32x2){s, 0.f}; a[1] = w1 * s; a[2] = w2 * s;
  b[0] = b[1] = w256 * s;
  b[2] = cmul(b[0], w2); a[3] = cmul(a[1], w2); a[4] = cmul(a[2], w2);
  b[3] = b[4] = b[5] = cmul(b[2], w2); a[5] = cmul(a[3], w2); a[6] = cmul(a[4], w2);
  b[6] = cmul(b[3], w2); a[7] = cmul(a[5], w2); c[0] = cmul(b[3], w256);
  b[7] = cmul(b[6], w2); c[2] = cmul(c[0], w256);
  put_im<0>(im + I_X, 48, a);
  c[1] = cmul(b[7], w256); c[3] = cmul(c[1], w256);
  put_im<0>(im + I_X + 16, 48, b);
#pragma unroll
  for (int k = 4; k < 8; ++k) c[k] = cmul(c[k - 1], w256);
  put_im<0>(im + I_X + 32, 48, c);
  return e;
}

template <int I> struct IC { static constexpr int value = I; };
template <int B, int E, class F> __device__ __forceinline__ void static_for(F&& f) {
  if constexpr (B < E) { f(IC<B>{}); static_for<B + 1, E>(f); }
}
typedef __attribute__((ext_vector_type(4))) short s16x4;
__device__ __forceinline__ u32x2 trd(uint32_t a) {
  return __builtin_bit_cast(u32x2, __builtin_amdgcn_ds_read_tr16_b64_v4i16((__attribute__((address_space(3))) s16x4*)(uintptr_t)a));
}
#define BAR() asm volatile("s_waitcnt lgkmcnt(0)\n\ts_barrier" ::: "memory")
#define ST16(off, v) asm volatile("global_store_dwordx4 %0, %1, %2 sc1\n\ts_nop 1" : : "v"(off), "v"(v), "s"(ws) : "memory")
#define ST4(off, v) asm volatile("global_store_dword %0, %1, %2 sc1" : : "v"(off), "v"(v), "s"(ws) : "memory")
#define LD16(r, off) asm volatile("global_load_dwordx4 %0, %1, %2 sc1" : "=v"(r) : "v"(off), "s"(ws) : "memory")
#define LD16W(r, off) asm volatile("global_load_dwordx4 %0, %1, %2 sc1\n\ts_waitcnt vmcnt(0)" : "=&v"(r) : "v"(off), "s"(ws) : "memory")
#define DRAIN() asm volatile("s_waitcnt vmcnt(0)" ::: "memory")
#define CEX(a, b) { const uint32_t hi_ = max(a, b), lo_ = min(a, b); a = hi_; b = lo_; }
#define MMB(a_, b_, c_) __builtin_amdgcn_mfma_f32_16x16x32_bf16(__builtin_bit_cast(bf16x8, a_), __builtin_bit_cast(bf16x8, b_), c_, 0, 0, 0)
#define NTM 0
#define DPOL ""
#define DPOL2 ""
#define DPOL4 ""
constexpr int SPIN_CAP = 1 << 18;
constexpr uint32_t UMASK = 0x40074007u;
__device__ __forceinline__ uint32_t utag(uint32_t T, int j) {
  return ((T >> (3 * j)) & 0x00070007u) | (((j <= 2) ? (T << (2 - j)) : (T >> (j - 2))) & 0x40004000u);
}
__device__ __forceinline__ uint32_t max4(const u32x4& a) { return max(max(a[0], a[1]), max(a[2], a[3])); }
#ifndef ULONG
#define ULONG 8
#endif
__device__ __forceinline__ void until(uint32_t t) {
#pragma unroll 1
  for (int spin = 0; spin < 600; ++spin) {
    const int d = (int)(t - (uint32_t)__builtin_amdgcn_s_memrealtime());
    if (d <= 0) break;
    if (ULONG && d > 4 * ULONG) __builtin_amdgcn_s_sleep(ULONG); else __builtin_amdgcn_s_sleep(1);
  }
}
__device__ __forceinline__ uint32_t latest(uint32_t t) {
  const uint32_t t0 = (uint32_t)__builtin_amdgcn_readfirstlane(t) - 0x40000000u;
  return wave_maxu(t - t0) + t0;
}
__device__ __forceinline__ uint32_t gate(const char* ws, uint32_t po, uint32_t T) {
  u32x2 pv;
#pragma unroll 1
  for (int spin = 0; spin < SPIN_CAP; ++spin) {
    asm volatile("global_load_dwordx2 %0, %1, %2 sc1\n\ts_waitcnt vmcnt(0)" : "=&v"(pv) : "v"(po), "s"(ws) : "memory");
    if (!__any(pv[0] != T)) break;
    __builtin_amdgcn_s_sleep(2);
  }
  return pv[1];
}
__device__ __forceinline__ uint32_t gate2(const char* ws, uint32_t oa, uint32_t da, uint32_t ob, uint32_t T) {
  u32x2 pa, pb;
#pragma unroll 1
  for (int spin = 0; spin < SPIN_CAP; ++spin) {
    asm volatile("global_load_dwordx2 %0, %2, %4 sc1\n\tglobal_load_dwordx2 %1, %3, %4 sc1\n\ts_waitcnt vmcnt(0)" : "=&v"(pa), "=&v"(pb) : "v"(oa), "v"(ob), "s"(ws) : "memory");
    if (!__any(pa[0] != T || pb[0] != T)) break;
    __builtin_amdgcn_s_sleep(4);
  }
  pa[1] += da;
  return latest(((int)(pa[1] - pb[1]) > 0) ? pa[1] : pb[1]);
}
__device__ __forceinline__ void gsync(uint32_t a, uint32_t target) {
  uint32_t t, c, n = SPIN_CAP;
  uint64_t ex;
  asm volatile("s_waitcnt lgkmcnt(0)\n\ts_mov_b64 %3, exec\n\ts_mov_b64 exec, 1\n\tv_mov_b32 %0, 1\n\tds_add_u32 %4, %0\n\ts_mov_b64 exec, %3\n"
               "1:\n\ts_sleep 1\n\tds_read_b32 %0, %4\n\ts_sub_u32 %2, %2, 1\n\ts_cbranch_scc1 2f\n\ts_waitcnt lgkmcnt(0)\n\tv_readfirstlane_b32 %1, %0\n\t"
               "s_cmp_lt_u32 %1, %5\n\ts_cbranch_scc1 1b\n2:\n\ts_waitcnt lgkmcnt(0)"
               : "=&v"(t), "=&s"(c), "+s"(n), "=&s"(ex) : "v"(a), "n"(target) : "memory", "scc");
}
__device__ __forceinline__ void gsync0(uint32_t a, uint32_t target) {
  uint32_t t, c, n = SPIN_CAP;
  asm volatile("s_waitcnt lgkmcnt(0)\n\ts_mov_b64 exec, 1\n\tv_mov_b32 %0, 1\n\tds_add_u32 %3, %0\n\ts_mov_b64 exec, -1\n"
               "1:\n\tds_read_b32 %0, %3\n\ts_sub_u32 %2, %2, 1\n\ts_cbranch_scc1 2f\n\ts_waitcnt lgkmcnt(0)\n\tv_readfirstlane_b32 %1, %0\n\t"
               "s_cmp_lt_u32 %1, %4\n\ts_cbranch_scc1 1b\n2:\n\ts_waitcnt lgkmcnt(0)"
               : "=&v"(t), "=&s"(c), "+s"(n) : "v"(a), "n"(target) : "memory", "scc");
}
__device__ __forceinline__ void gwait(uint32_t a, uint32_t target) {
  uint32_t t, c, n = SPIN_CAP;
  asm volatile("1:\n\ts_sleep 2\n\tds_read_b32 %0, %3\n\ts_sub_u32 %2, %2, 1\n\ts_cbranch_scc1 2f\n\ts_waitcnt lgkmcnt(0)\n\tv_readfirstlane_b32 %1, %0\n\t"
               "s_cmp_lt_u32 %1, %4\n\ts_cbranch_scc1 1b\n2:\n\ts_waitcnt lgkmcnt(0)"
               : "=&v"(t), "=&s"(c), "+s"(n) : "v"(a), "n"(target) : "memory", "scc");
}
__device__ __forceinline__ void gwait0(uint32_t a, uint32_t target) {
  uint32_t t, c, n = SPIN_CAP;
  asm volatile("1:\n\tds_read_b32 %0, %3\n\ts_sub_u32 %2, %2, 1\n\ts_cbranch_scc1 2f\n\ts_waitcnt lgkmcnt(0)\n\tv_readfirstlane_b32 %1, %0\n\t"
               "s_cmp_lt_u32 %1, %4\n\ts_cbranch_scc1 1b\n2:\n\ts_waitcnt lgkmcnt(0)"
               : "=&v"(t), "=&s"(c), "+s"(n) : "v"(a), "n"(target) : "memory", "scc");
}
__device__ __forceinline__ void dma16(const void* base, uint32_t voff, uint32_t lds) {
  uint32_t keep;
  asm volatile("s_mov_b32 %0, m0\n\ts_mov_b32 m0, %3\n\ts_nop 0\n\tglobal_load_lds_dwordx4 %1, %2" DPOL "\n\ts_mov_b32 m0, %0" : "=&s"(keep) : "v"(voff), "s"(base), "s"(lds) : "memory");
}
__device__ __forceinline__ void dma16d(const void* base, uint32_t voff, uint32_t lds) {
  uint32_t keep;
  asm volatile("s_mov_b32 %0, m0\n\ts_mov_b32 m0, %3\n\ts_nop 0\n\tglobal_load_lds_dwordx4 %1, %2" DPOL2 "\n\ts_mov_b32 m0, %0" : "=&s"(keep) : "v"(voff), "s"(base), "s"(lds) : "memory");
}
__device__ __forceinline__ void nap(int n) {
  for (; n > 15; n -= 15) __builtin_amdgcn_s_sleep(15);
  switch (n) {
    case 1: __builtin_amdgcn_s_sleep(1); break; case 2: __builtin_amdgcn_s_sleep(2); break; case 3: __builtin_amdgcn_s_sleep(3); break;
    case 4: __builtin_amdgcn_s_sleep(4); break; case 5: __builtin_amdgcn_s_sleep(5); break; case 6: __builtin_amdgcn_s_sleep(6); break;
    case 7: __builtin_amdgcn_s_sleep(7); break; case 8: __builtin_amdgcn_s_sleep(8); break; case 9: __builtin_amdgcn_s_sleep(9); break;
    case 10: __builtin_amdgcn_s_sleep(10); break; case 11: __builtin_amdgcn_s_sleep(11); break; case 12: __builtin_amdgcn_s_sleep(12); break;
    case 13: __builtin_amdgcn_s_sleep(13); break; case 14: __builtin_amdgcn_s_sleep(14); break; case 15: __builtin_amdgcn_s_sleep(15); break;
    default: break;
  }
}
#define SLEEP(n) do { if ((n) > 0) __builtin_amdgcn_s_sleep(n); } while (0)
#define USLEEP 3
#define SSLEEP 14
#define PSLEEP 2
#define PPRIO 0
#define LFIRST 1
#define WD 40
#define OSLEEP 12
#define QPRIO 1
#define KPRIO 2
#define GSLEEP 0
#define NSLEEP 0
#define UD 40
#define XD 0
#define RSLEEP 0
#define UPRIO 2
#define VD2 385
#define WDEPTH 6
#define ESLEEP 0
#define SD 58
#define KD 180
#define OD 0
#define KBACK 6
#define C3 1
#define XROT 2
#define WSOFF 0
#define KN 1
#define RPF 1
#define LSLEEP 1
#define TSTOP 78
#define HSTOP 110
#define VEARLY 0ull
#define FPR 3
#define SPR 3
#define DPRIO 0
#define QPRIO2 3
#define HW 2
#define RD 57
#define OPOS 1
#define OD2 6
#define RD2 88
#define ORD1 ((unsigned __int128)0x321087654u << 36 | 0x321087654ull)
#define WDEPTH2 5
#define RQ NA
#define HREV 1
#define QPRIO0 3
#define VPS 1
#define RW 0
#define NPRIO 0
#define XSL 10
#define V2D 24
#define N0 8
#define LPOS 1
#define ALLP 1
#define FORCESLOW 0
#define FORCEFB 0
#define CONV_ASM \
  "s_cmp_eq_u32 s89, 1\ns_cbranch_scc1 1f\ns_cmp_gt_u32 s89, 1\ns_cbranch_scc1 9f\nds_read_b64 v[124:125], v3 offset:576\n" \
  "ds_read_b64 v[126:127], v3 offset:584\nds_read_b64 v[120:121], v3 offset:64\nds_read_b64 v[122:123], v3 offset:72\n" \
  "ds_read_b64 v[116:117], v3 offset:640\nds_read_b64 v[118:119], v3 offset:648\nds_read_b64 v[112:113], v3 offset:128\n" \
  "ds_read_b64 v[114:115], v3 offset:136\nds_read_b64 v[108:109], v3 offset:704\nds_read_b64 v[110:111], v3 offset:712\n" \
  "ds_read_b64 v[104:105], v3 offset:192\nds_read_b64 v[106:107], v3 offset:200\nds_read_b64 v[100:101], v3 offset:768\n" \
  "ds_read_b64 v[102:103], v3 offset:776\nds_read_b64 v[96:97], v3 offset:256\nds_read_b64 v[98:99], v3 offset:264\n" \
  "ds_read_b64 v[92:93], v3 offset:832\nds_read_b64 v[94:95], v3 offset:840\nds_read_b64 v[88:89], v3 offset:320\n" \
  "ds_read_b64 v[90:91], v3 offset:328\nds_read_b64 v[84:85], v3 offset:896\nds_read_b64 v[86:87], v3 offset:904\n" \
  "ds_read_b64 v[80:81], v3 offset:384\nds_read_b64 v[82:83], v3 offset:392\nds_read_b64 v[76:77], v3 offset:960\n" \
  "ds_read_b64 v[78:79], v3 offset:968\nds_read_b64 v[72:73], v3 offset:448\nds_read_b64 v[74:75], v3 offset:456\n" \
  "ds_read_b64 v[68:69], v3 offset:1024\nds_read_b64 v[70:71], v3 offset:1032\nds_read_b64 v[64:65], v3 offset:512\n" \
  "ds_read_b64 v[66:67], v3 offset:520\nds_read_b64 v[60:61], v3 offset:0\nds_read_b64 v[62:63], v3 offset:8\nds_read_b64 v[56:57], v3 offset:1088\n" \
  "ds_read_b64 v[58:59], v3 offset:1096\ns_waitcnt lgkmcnt(0)\ns_barrier\nds_read_b128 v[36:39], v2 offset:0\nds_read_b128 v[40:43], v2 offset:64\n" \
  "ds_read_b128 v[44:47], v2 offset:128\nds_read_b128 v[48:51], v2 offset:192\ns_waitcnt lgkmcnt(3)\n" \
  "v_mfma_f32_16x16x32_f16 v[12:15], v[124:127], v[36:39], 0\ns_setprio 1\nds_read_b128 v[52:55], v2 offset:256\n" \
  "v_mfma_f32_16x16x32_f16 v[16:19], v[120:123], v[36:39], 0\ns_waitcnt lgkmcnt(3)\nv_mfma_f32_16x16x32_f16 v[12:15], v[116:119], v[40:43], v[12:15]\n" \
  "ds_read_b128 v[36:39], v2 offset:320\nv_mfma_f32_16x16x32_f16 v[16:19], v[112:115], v[40:43], v[16:19]\ns_waitcnt lgkmcnt(3)\n" \
  "v_mfma_f32_16x16x32_f16 v[12:15], v[108:111], v[44:47], v[12:15]\nds_read_b128 v[40:43], v2 offset:384\n" \
  "v_mfma_f32_16x16x32_f16 v[16:19], v[104:107], v[44:47], v[16:19]\ns_waitcnt lgkmcnt(3)\n" \
  "v_mfma_f32_16x16x32_f16 v[12:15], v[100:103], v[48:51], v[12:15]\nds_read_b128 v[44:47], v2 offset:448\n" \
  "v_mfma_f32_16x16x32_f16 v[16:19], v[96:99], v[48:51], v[16:19]\ns_waitcnt lgkmcnt(3)\n" \
  "v_mfma_f32_16x16x32_f16 v[12:15], v[92:95], v[52:55], v[12:15]\nv_mfma_f32_16x16x32_f16 v[16:19], v[88:91], v[52:55], v[16:19]\n" \
  "s_waitcnt lgkmcnt(2)\nv_mfma_f32_16x16x32_f16 v[12:15], v[84:87], v[36:39], v[12:15]\nds_read_b128 v[48:51], v2 offset:512\n" \
  "v_mfma_f32_16x16x32_f16 v[16:19], v[80:83], v[36:39], v[16:19]\ns_waitcnt lgkmcnt(2)\n" \
  "v_mfma_f32_16x16x32_f16 v[12:15], v[76:79], v[40:43], v[12:15]\nds_read_b128 v[52:55], v2 offset:576\n" \
  "v_mfma_f32_16x16x32_f16 v[16:19], v[72:75], v[40:43], v[16:19]\ns_waitcnt lgkmcnt(2)\n" \
  "v_mfma_f32_16x16x32_f16 v[12:15], v[68:71], v[44:47], v[12:15]\nds_read_b128 v[36:39], v2 offset:640\n" \
  "v_mfma_f32_16x16x32_f16 v[16:19], v[64:67], v[44:47], v[16:19]\nv_mfma_f32_16x16x32_f16 v[20:23], v[60:63], v[44:47], 0\n" \
  "ds_read_b128 v[40:43], v2 offset:704\nv_mfma_f32_16x16x32_f16 v[28:31], v[56:59], v[44:47], 0\ns_waitcnt lgkmcnt(3)\n" \
  "v_mfma_f32_16x16x32_f16 v[16:19], v[124:127], v[48:51], v[16:19]\nds_read_b128 v[56:59], v2 offset:768\nv_cvt_pkrtz_f16_f32 v32, v12, v13\n" \
  "v_mfma_f32_16x16x32_f16 v[20:23], v[120:123], v[48:51], v[20:23]\nv_cvt_pkrtz_f16_f32 v33, v14, v15\ns_waitcnt lgkmcnt(3)\n" \
  "v_mfma_f32_16x16x32_f16 v[16:19], v[116:119], v[52:55], v[16:19]\nds_read_b128 v[44:47], v2 offset:832\n" \
  "v_pk_add_u16 v32, v32, 4 op_sel_hi:[1,0] clamp\nv_mfma_f32_16x16x32_f16 v[20:23], v[112:115], v[52:55], v[20:23]\n" \
  "v_pk_add_u16 v33, v33, 4 op_sel_hi:[1,0] clamp\ns_waitcnt lgkmcnt(3)\nv_mfma_f32_16x16x32_f16 v[16:19], v[108:111], v[36:39], v[16:19]\n" \
  "ds_read_b128 v[48:51], v2 offset:896\nds_write_b64 v4, v[32:33] offset:0\nv_mfma_f32_16x16x32_f16 v[20:23], v[104:107], v[36:39], v[20:23]\n" \
  "s_waitcnt lgkmcnt(3)\nv_mfma_f32_16x16x32_f16 v[16:19], v[100:103], v[40:43], v[16:19]\nds_read_b128 v[52:55], v2 offset:960\n" \
  "v_mfma_f32_16x16x32_f16 v[20:23], v[96:99], v[40:43], v[20:23]\nv_mfma_f32_16x16x32_f16 v[16:19], v[92:95], v[56:59], v[16:19]\n" \
  "v_mfma_f32_16x16x32_f16 v[20:23], v[88:91], v[56:59], v[20:23]\nds_read_b128 v[36:39], v2 offset:1024\ns_waitcnt lgkmcnt(3)\n" \
  "v_mfma_f32_16x16x32_f16 v[16:19], v[84:87], v[44:47], v[16:19]\nv_mfma_f32_16x16x32_f16 v[20:23], v[80:83], v[44:47], v[20:23]\n" \
  "ds_read_b128 v[40:43], v2 offset:1088\nds_read_b64 v[56:57], v6 offset:256\nds_read_b64 v[58:59], v6 offset:264\n" \
  "v_mfma_f32_16x16x32_f16 v[16:19], v[76:79], v[48:51], v[16:19]\nv_mfma_f32_16x16x32_f16 v[20:23], v[72:75], v[48:51], v[20:23]\n" \
  "ds_read_b128 v[44:47], v2 offset:1152\ns_waitcnt lgkmcnt(4)\nv_mfma_f32_16x16x32_f16 v[16:19], v[68:71], v[52:55], v[16:19]\n" \
  "v_mfma_f32_16x16x32_f16 v[20:23], v[64:67], v[52:55], v[20:23]\nds_read_b128 v[48:51], v2 offset:1216\n" \
  "v_mfma_f32_16x16x32_f16 v[24:27], v[60:63], v[52:55], 0\nv_mfma_f32_16x16x32_f16 v[20:23], v[124:127], v[36:39], v[20:23]\n" \
  "ds_read_b128 v[52:55], v2 offset:1280\nv_mfma_f32_16x16x32_f16 v[24:27], v[120:123], v[36:39], v[24:27]\ns_waitcnt lgkmcnt(5)\n" \
  "v_mfma_f32_16x16x32_f16 v[20:23], v[116:119], v[40:43], v[20:23]\nds_read_b128 v[36:39], v2 offset:1344\nv_cvt_pkrtz_f16_f32 v32, v16, v17\n" \
  "v_mfma_f32_16x16x32_f16 v[24:27], v[112:115], v[40:43], v[24:27]\nv_cvt_pkrtz_f16_f32 v33, v18, v19\ns_waitcnt lgkmcnt(3)\n" \
  "v_mfma_f32_16x16x32_f16 v[20:23], v[108:111], v[44:47], v[20:23]\nds_read_b128 v[40:43], v2 offset:1408\n" \
  "v_pk_add_u16 v32, v32, 4 op_sel_hi:[1,0] clamp\nds_read_b32 v35, v9 offset:32\nv_mfma_f32_16x16x32_f16 v[24:27], v[104:107], v[44:47], v[24:27]\n" \
  "v_pk_add_u16 v33, v33, 4 op_sel_hi:[1,0] clamp\ns_waitcnt lgkmcnt(3)\nv_mfma_f32_16x16x32_f16 v[20:23], v[100:103], v[48:51], v[20:23]\n" \
  "ds_read_b128 v[44:47], v2 offset:1472\nds_write_b64 v4, v[32:33] offset:1024\nv_mfma_f32_16x16x32_f16 v[24:27], v[96:99], v[48:51], v[24:27]\n" \
  "v_mfma_f32_16x16x32_f16 v[20:23], v[92:95], v[52:55], v[20:23]\nv_mfma_f32_16x16x32_f16 v[24:27], v[88:91], v[52:55], v[24:27]\n" \
  "ds_read_b128 v[48:51], v2 offset:1536\ns_waitcnt lgkmcnt(3)\nv_readfirstlane_b32 s85, v35\ns_cmp_lt_u32 s85, 4\ns_cbranch_scc1 21f\n22:\n" \
  "v_mfma_f32_16x16x32_f16 v[20:23], v[84:87], v[36:39], v[20:23]\nv_mfma_f32_16x16x32_f16 v[24:27], v[80:83], v[36:39], v[24:27]\n" \
  "ds_read_b128 v[52:55], v2 offset:1600\nv_mfma_f32_16x16x32_f16 v[20:23], v[76:79], v[40:43], v[20:23]\n" \
  "v_mfma_f32_16x16x32_f16 v[24:27], v[72:75], v[40:43], v[24:27]\nds_read_b128 v[36:39], v2 offset:1664\ns_waitcnt lgkmcnt(4)\n" \
  "v_mfma_f32_16x16x32_f16 v[20:23], v[68:71], v[44:47], v[20:23]\nv_mfma_f32_16x16x32_f16 v[24:27], v[64:67], v[44:47], v[24:27]\n" \
  "ds_read_b128 v[40:43], v2 offset:1728\nv_mfma_f32_16x16x32_f16 v[28:31], v[60:63], v[44:47], v[28:31]\nds_read_b64_tr_b16 v[60:61], v5 offset:0\n" \
  "ds_read_b64_tr_b16 v[62:63], v5 offset:32\ns_waitcnt lgkmcnt(4)\nv_mfma_f32_16x16x32_f16 v[24:27], v[124:127], v[48:51], v[24:27]\n" \
  "v_mfma_f32_16x16x32_f16 v[28:31], v[120:123], v[48:51], v[28:31]\nds_read_b64_tr_b16 v[44:45], v5 offset:256\n" \
  "ds_read_b64_tr_b16 v[46:47], v5 offset:288\nv_mfma_f32_16x16x32_f16 v[24:27], v[116:119], v[52:55], v[24:27]\n" \
  "ds_read_b64 v[120:121], v6 offset:320\nds_read_b64 v[122:123], v6 offset:328\nv_mfma_f32_16x16x32_f16 v[28:31], v[112:115], v[52:55], v[28:31]\n" \
  "ds_read_b128 v[48:51], v2 offset:1792\ns_waitcnt lgkmcnt(7)\nv_mfma_f32_16x16x32_f16 v[24:27], v[108:111], v[36:39], v[24:27]\n" \
  "ds_read_b128 v[112:115], v2 offset:1856\nv_mfma_f32_16x16x32_f16 v[28:31], v[104:107], v[36:39], v[28:31]\nds_read_b64 v[52:53], v6 offset:128\n" \
  "ds_read_b64 v[54:55], v6 offset:136\nv_mfma_f32_16x16x32_f16 v[24:27], v[100:103], v[40:43], v[24:27]\nds_read_b128 v[104:107], v2 offset:1920\n" \
  "v_mfma_f32_16x16x32_f16 v[28:31], v[96:99], v[40:43], v[28:31]\nds_read_b64 v[36:37], v6 offset:192\nds_read_b64 v[38:39], v6 offset:200\n" \
  "s_waitcnt lgkmcnt(9)\nv_mfma_f32_16x16x32_f16 v[20:23], v[60:63], v[56:59], v[20:23]\nds_read_b128 v[96:99], v2 offset:1984\ns_waitcnt lgkmcnt(7)\n" \
  "v_mfma_f32_16x16x32_f16 v[20:23], v[44:47], v[120:123], v[20:23]\nv_mfma_f32_16x16x32_f16 v[24:27], v[92:95], v[48:51], v[24:27]\n" \
  "ds_read_b128 v[40:43], v2 offset:2048\nv_mfma_f32_16x16x32_f16 v[28:31], v[88:91], v[48:51], v[28:31]\nds_read_b64_tr_b16 v[88:89], v5 offset:512\n" \
  "ds_read_b64_tr_b16 v[90:91], v5 offset:544\ns_waitcnt lgkmcnt(6)\nv_mfma_f32_16x16x32_f16 v[24:27], v[84:87], v[112:115], v[24:27]\n" \
  "v_mfma_f32_16x16x32_f16 v[28:31], v[80:83], v[112:115], v[28:31]\nv_cvt_pkrtz_f16_f32 v32, v20, v21\nv_cvt_pkrtz_f16_f32 v33, v22, v23\n" \
  "v_mfma_f32_16x16x32_f16 v[24:27], v[76:79], v[104:107], v[24:27]\nds_read_b64_tr_b16 v[48:49], v5 offset:768\n" \
  "ds_read_b64_tr_b16 v[50:51], v5 offset:800\nv_mfma_f32_16x16x32_f16 v[28:31], v[72:75], v[104:107], v[28:31]\n" \
  "ds_read_b128 v[80:83], v2 offset:2112\ns_waitcnt lgkmcnt(5)\nv_mfma_f32_16x16x32_f16 v[24:27], v[68:71], v[96:99], v[24:27]\n" \
  "ds_read_b128 v[112:115], v2 offset:2176\nv_pk_add_u16 v32, v32, 4 op_sel_hi:[1,0] clamp\n" \
  "v_mfma_f32_16x16x32_f16 v[28:31], v[64:67], v[96:99], v[28:31]\nds_read_b128 v[72:75], v2 offset:2240\n" \
  "v_pk_add_u16 v33, v33, 4 op_sel_hi:[1,0] clamp\nv_mfma_f32_16x16x32_f16 v[28:31], v[124:127], v[40:43], v[28:31]\n" \
  "ds_read_b128 v[104:107], v2 offset:2304\nds_write_b64 v4, v[32:33] offset:2048\nv_mfma_f32_16x16x32_f16 v[24:27], v[60:63], v[52:55], v[24:27]\n" \
  "ds_read_b128 v[64:67], v2 offset:2368\nds_read_b64 v[96:97], v6 offset:0\nds_read_b64 v[98:99], v6 offset:8\n" \
  "v_mfma_f32_16x16x32_f16 v[24:27], v[44:47], v[36:39], v[24:27]\nds_read_b128 v[124:127], v2 offset:2432\ns_waitcnt lgkmcnt(9)\n" \
  "v_mfma_f32_16x16x32_f16 v[24:27], v[88:91], v[56:59], v[24:27]\nds_read_b128 v[40:43], v2 offset:2496\n" \
  "v_mfma_f32_16x16x32_f16 v[24:27], v[48:51], v[120:123], v[24:27]\ns_waitcnt lgkmcnt(8)\n" \
  "v_mfma_f32_16x16x32_f16 v[28:31], v[116:119], v[80:83], v[28:31]\nds_read_b64 v[116:117], v6 offset:64\nds_read_b64 v[118:119], v6 offset:72\n" \
  "v_mfma_f32_16x16x32_f16 v[28:31], v[108:111], v[112:115], v[28:31]\nds_read_b64_tr_b16 v[80:81], v5 offset:1024\n" \
  "ds_read_b64_tr_b16 v[82:83], v5 offset:1056\ns_waitcnt lgkmcnt(10)\nv_mfma_f32_16x16x32_f16 v[28:31], v[100:103], v[72:75], v[28:31]\n" \
  "v_cvt_pkrtz_f16_f32 v32, v24, v25\nv_cvt_pkrtz_f16_f32 v33, v26, v27\nv_mfma_f32_16x16x32_f16 v[28:31], v[92:95], v[104:107], v[28:31]\n" \
  "v_pk_add_u16 v32, v32, 4 op_sel_hi:[1,0] clamp\ns_waitcnt lgkmcnt(5)\nv_mfma_f32_16x16x32_f16 v[28:31], v[84:87], v[64:67], v[28:31]\n" \
  "ds_read_b64_tr_b16 v[108:109], v5 offset:1280\nds_read_b64_tr_b16 v[110:111], v5 offset:1312\n" \
  "v_mfma_f32_16x16x32_f16 v[28:31], v[76:79], v[124:127], v[28:31]\nds_read_b64_tr_b16 v[112:113], v7 offset:0\n" \
  "ds_read_b64_tr_b16 v[114:115], v7 offset:64\ns_waitcnt lgkmcnt(6)\nv_mfma_f32_16x16x32_f16 v[28:31], v[68:71], v[40:43], v[28:31]\n" \
  "ds_read2_b32 v[100:101], v8 offset0:48 offset1:49\nds_read2_b32 v[102:103], v8 offset0:50 offset1:51\n" \
  "v_mfma_f32_16x16x32_f16 v[28:31], v[60:63], v[96:99], v[28:31]\nv_pk_add_u16 v33, v33, 4 op_sel_hi:[1,0] clamp\n" \
  "ds_write_b64 v4, v[32:33] offset:3072\nv_mfma_f32_16x16x32_f16 v[28:31], v[44:47], v[116:119], v[28:31]\n" \
  "v_mfma_f32_16x16x32_f16 v[28:31], v[88:91], v[52:55], v[28:31]\nv_mfma_f32_16x16x32_f16 v[28:31], v[48:51], v[36:39], v[28:31]\n" \
  "s_waitcnt lgkmcnt(5)\nv_mfma_f32_16x16x32_f16 v[28:31], v[80:83], v[56:59], v[28:31]\n" \
  "v_mfma_f32_16x16x32_f16 v[28:31], v[108:111], v[120:123], v[28:31]\ns_waitcnt lgkmcnt(1)\n" \
  "v_mfma_f32_16x16x32_f16 v[28:31], v[112:115], v[100:103], v[28:31]\ns_nop 7\nv_cvt_pkrtz_f16_f32 v32, v28, v29\nv_cvt_pkrtz_f16_f32 v33, v30, v31\n" \
  "v_pk_add_u16 v32, v32, 4 op_sel_hi:[1,0] clamp\nv_pk_add_u16 v33, v33, 4 op_sel_hi:[1,0] clamp\nds_write_b64 v4, v[32:33] offset:4096\n" \
  "s_setprio 0\ns_setpc_b64 s[90:91]\n21:\ns_movk_i32 s84, 0x4000\n23:\nds_read_b32 v35, v9 offset:32\ns_sub_u32 s84, s84, 1\ns_cbranch_scc1 22b\n" \
  "s_waitcnt lgkmcnt(0)\nv_readfirstlane_b32 s85, v35\ns_cmp_lt_u32 s85, 4\ns_cbranch_scc1 23b\ns_branch 22b\n1:\n" \
  "ds_read_b64 v[124:125], v3 offset:1088\nds_read_b64 v[126:127], v3 offset:1096\nds_read_b64 v[120:121], v3 offset:0\n" \
  "ds_read_b64 v[122:123], v3 offset:8\nds_read_b64 v[116:117], v3 offset:64\nds_read_b64 v[118:119], v3 offset:72\n" \
  "ds_read_b64 v[112:113], v3 offset:128\nds_read_b64 v[114:115], v3 offset:136\nds_read_b64 v[108:109], v3 offset:192\n" \
  "ds_read_b64 v[110:111], v3 offset:200\nds_read_b64 v[104:105], v3 offset:256\nds_read_b64 v[106:107], v3 offset:264\n" \
  "ds_read_b64 v[100:101], v3 offset:320\nds_read_b64 v[102:103], v3 offset:328\nds_read_b64 v[96:97], v3 offset:384\n" \
  "ds_read_b64 v[98:99], v3 offset:392\nds_read_b64 v[92:93], v3 offset:448\nds_read_b64 v[94:95], v3 offset:456\n" \
  "ds_read_b64 v[88:89], v3 offset:512\nds_read_b64 v[90:91], v3 offset:520\nds_read_b64 v[84:85], v3 offset:576\n" \
  "ds_read_b64 v[86:87], v3 offset:584\nds_read_b64 v[80:81], v3 offset:640\nds_read_b64 v[82:83], v3 offset:648\n" \
  "ds_read_b64 v[76:77], v3 offset:704\nds_read_b64 v[78:79], v3 offset:712\nds_read_b64 v[72:73], v3 offset:768\n" \
  "ds_read_b64 v[74:75], v3 offset:776\nds_read_b64 v[68:69], v3 offset:832\nds_read_b64 v[70:71], v3 offset:840\n" \
  "ds_read_b64 v[64:65], v3 offset:896\nds_read_b64 v[66:67], v3 offset:904\nds_read_b64 v[60:61], v3 offset:960\n" \
  "ds_read_b64 v[62:63], v3 offset:968\nds_read_b64 v[56:57], v3 offset:1024\nds_read_b64 v[58:59], v3 offset:1032\n" \
  "ds_read_b64 v[52:53], v6 offset:0\nds_read_b64 v[54:55], v6 offset:8\nds_read_b64 v[48:49], v6 offset:64\nds_read_b64 v[50:51], v6 offset:72\n" \
  "s_waitcnt lgkmcnt(0)\ns_barrier\nds_read_b128 v[28:31], v2 offset:960\nds_read_b128 v[32:35], v2 offset:1472\n" \
  "ds_read_b128 v[36:39], v2 offset:1984\nds_read_b128 v[40:43], v2 offset:2048\nds_read_b128 v[44:47], v2 offset:2112\ns_waitcnt lgkmcnt(3)\n" \
  "v_mfma_f32_16x16x32_f16 v[12:15], v[124:127], v[28:31], 0\ns_setprio 1\nds_read_b128 v[28:31], v2 offset:2176\n" \
  "v_mfma_f32_16x16x32_f16 v[16:19], v[124:127], v[32:35], 0\nds_read_b128 v[32:35], v2 offset:2240\ns_waitcnt lgkmcnt(3)\n" \
  "v_mfma_f32_16x16x32_f16 v[12:15], v[120:123], v[36:39], v[12:15]\nv_mfma_f32_16x16x32_f16 v[20:23], v[124:127], v[36:39], 0\n" \
  "ds_read_b128 v[124:127], v2 offset:2304\nds_read_b128 v[36:39], v2 offset:2368\nv_mfma_f32_16x16x32_f16 v[12:15], v[116:119], v[40:43], v[12:15]\n" \
  "ds_read_b128 v[40:43], v2 offset:2432\ns_waitcnt lgkmcnt(4)\nv_mfma_f32_16x16x32_f16 v[12:15], v[112:115], v[44:47], v[12:15]\n" \
  "ds_read_b128 v[44:47], v2 offset:2496\nv_mfma_f32_16x16x32_f16 v[12:15], v[108:111], v[28:31], v[12:15]\nds_read_b128 v[28:31], v2 offset:2560\n" \
  "s_waitcnt lgkmcnt(4)\nv_mfma_f32_16x16x32_f16 v[12:15], v[104:107], v[32:35], v[12:15]\nds_read_b128 v[32:35], v2 offset:2624\n" \
  "v_mfma_f32_16x16x32_f16 v[12:15], v[100:103], v[124:127], v[12:15]\ns_waitcnt lgkmcnt(3)\n" \
  "v_mfma_f32_16x16x32_f16 v[12:15], v[96:99], v[36:39], v[12:15]\nds_read_b128 v[124:127], v2 offset:2688\n" \
  "v_mfma_f32_16x16x32_f16 v[12:15], v[92:95], v[40:43], v[12:15]\ns_waitcnt lgkmcnt(3)\n" \
  "v_mfma_f32_16x16x32_f16 v[12:15], v[88:91], v[44:47], v[12:15]\nds_read_b128 v[36:39], v2 offset:2752\n" \
  "v_mfma_f32_16x16x32_f16 v[16:19], v[120:123], v[44:47], v[16:19]\ns_waitcnt lgkmcnt(3)\n" \
  "v_mfma_f32_16x16x32_f16 v[12:15], v[84:87], v[28:31], v[12:15]\nds_read_b128 v[40:43], v2 offset:2816\n" \
  "v_mfma_f32_16x16x32_f16 v[16:19], v[116:119], v[28:31], v[16:19]\ns_waitcnt lgkmcnt(3)\n" \
  "v_mfma_f32_16x16x32_f16 v[12:15], v[80:83], v[32:35], v[12:15]\nds_read_b128 v[44:47], v2 offset:2880\n" \
  "v_mfma_f32_16x16x32_f16 v[16:19], v[112:115], v[32:35], v[16:19]\ns_waitcnt lgkmcnt(3)\n" \
  "v_mfma_f32_16x16x32_f16 v[12:15], v[76:79], v[124:127], v[12:15]\nds_read_b128 v[28:31], v2 offset:2944\n" \
  "v_mfma_f32_16x16x32_f16 v[16:19], v[108:111], v[124:127], v[16:19]\ns_waitcnt lgkmcnt(3)\n" \
  "v_mfma_f32_16x16x32_f16 v[12:15], v[72:75], v[36:39], v[12:15]\nds_read_b128 v[32:35], v2 offset:3008\n" \
  "v_mfma_f32_16x16x32_f16 v[16:19], v[104:107], v[36:39], v[16:19]\ns_waitcnt lgkmcnt(3)\n" \
  "v_mfma_f32_16x16x32_f16 v[12:15], v[68:71], v[40:43], v[12:15]\nv_mfma_f32_16x16x32_f16 v[16:19], v[100:103], v[40:43], v[16:19]\n" \
  "ds_read_b128 v[124:127], v2 offset:3072\ns_waitcnt lgkmcnt(3)\nv_mfma_f32_16x16x32_f16 v[12:15], v[64:67], v[44:47], v[12:15]\n" \
  "v_mfma_f32_16x16x32_f16 v[16:19], v[96:99], v[44:47], v[16:19]\nds_read_b128 v[36:39], v2 offset:3136\ns_waitcnt lgkmcnt(3)\n" \
  "v_mfma_f32_16x16x32_f16 v[12:15], v[60:63], v[28:31], v[12:15]\nds_read_b64 v[40:41], v6 offset:128\nds_read_b64 v[42:43], v6 offset:136\n" \
  "v_mfma_f32_16x16x32_f16 v[16:19], v[92:95], v[28:31], v[16:19]\nds_read_b128 v[44:47], v2 offset:3200\ns_waitcnt lgkmcnt(4)\n" \
  "v_mfma_f32_16x16x32_f16 v[12:15], v[56:59], v[32:35], v[12:15]\nv_mfma_f32_16x16x32_f16 v[16:19], v[88:91], v[32:35], v[16:19]\n" \
  "ds_read_b128 v[28:31], v2 offset:3264\nv_mfma_f32_16x16x32_f16 v[20:23], v[120:123], v[32:35], v[20:23]\nds_read_b64 v[120:121], v6 offset:192\n" \
  "ds_read_b64 v[122:123], v6 offset:200\nv_mfma_f32_16x16x32_f16 v[16:19], v[84:87], v[124:127], v[16:19]\nds_read_b128 v[32:35], v2 offset:3328\n" \
  "v_mfma_f32_16x16x32_f16 v[20:23], v[116:119], v[124:127], v[20:23]\nds_read_b64 v[116:117], v6 offset:256\nds_read_b64 v[118:119], v6 offset:264\n" \
  "s_waitcnt lgkmcnt(6)\nv_mfma_f32_16x16x32_f16 v[16:19], v[80:83], v[36:39], v[16:19]\nds_read_b128 v[124:127], v2 offset:3392\n" \
  "v_mfma_f32_16x16x32_f16 v[20:23], v[112:115], v[36:39], v[20:23]\nds_read_b64 v[112:113], v6 offset:320\nds_read_b64 v[114:115], v6 offset:328\n" \
  "v_mfma_f32_16x16x32_f16 v[16:19], v[76:79], v[44:47], v[16:19]\nds_read_b128 v[36:39], v2 offset:3456\n" \
  "v_mfma_f32_16x16x32_f16 v[20:23], v[108:111], v[44:47], v[20:23]\ns_waitcnt lgkmcnt(6)\n" \
  "v_mfma_f32_16x16x32_f16 v[16:19], v[72:75], v[28:31], v[16:19]\nds_read_b128 v[108:111], v2 offset:3520\n" \
  "v_mfma_f32_16x16x32_f16 v[20:23], v[104:107], v[28:31], v[20:23]\nv_mfma_f32_16x16x32_f16 v[16:19], v[68:71], v[32:35], v[16:19]\n" \
  "ds_read_b128 v[44:47], v2 offset:3584\nv_mfma_f32_16x16x32_f16 v[20:23], v[100:103], v[32:35], v[20:23]\nds_read_b128 v[104:107], v2 offset:3648\n" \
  "ds_read_b32 v27, v9 offset:32\ns_waitcnt lgkmcnt(4)\nv_mfma_f32_16x16x32_f16 v[16:19], v[64:67], v[124:127], v[16:19]\n" \
  "ds_read_b128 v[28:31], v2 offset:3712\nv_mfma_f32_16x16x32_f16 v[20:23], v[96:99], v[124:127], v[20:23]\nds_read_b128 v[100:103], v2 offset:3776\n" \
  "ds_read2_b32 v[32:33], v8 offset0:32 offset1:33\nds_read2_b32 v[34:35], v8 offset0:34 offset1:35\n" \
  "v_mfma_f32_16x16x32_f16 v[16:19], v[60:63], v[36:39], v[16:19]\nds_read_b128 v[96:99], v2 offset:3840\n" \
  "v_mfma_f32_16x16x32_f16 v[20:23], v[92:95], v[36:39], v[20:23]\nds_read_b128 v[124:127], v2 offset:3904\ns_waitcnt lgkmcnt(8)\n" \
  "v_mfma_f32_16x16x32_f16 v[16:19], v[56:59], v[108:111], v[16:19]\nds_read_b128 v[92:95], v2 offset:3968\ns_waitcnt lgkmcnt(7)\n" \
  "v_readfirstlane_b32 s85, v27\ns_cmp_lt_u32 s85, 4\ns_cbranch_scc1 31f\n32:\nv_mfma_f32_16x16x32_f16 v[20:23], v[88:91], v[108:111], v[20:23]\n" \
  "ds_read_b128 v[36:39], v2 offset:4032\nds_read_b64_tr_b16 v[88:89], v5 offset:512\nds_read_b64_tr_b16 v[90:91], v5 offset:544\n" \
  "v_mfma_f32_16x16x32_f16 v[20:23], v[84:87], v[44:47], v[20:23]\nds_read_b64_tr_b16 v[108:109], v5 offset:768\n" \
  "ds_read_b64_tr_b16 v[110:111], v5 offset:800\nv_mfma_f32_16x16x32_f16 v[20:23], v[80:83], v[104:107], v[20:23]\n" \
  "ds_read_b64_tr_b16 v[84:85], v5 offset:1024\nds_read_b64_tr_b16 v[86:87], v5 offset:1056\ns_waitcnt lgkmcnt(12)\n" \
  "v_mfma_f32_16x16x32_f16 v[20:23], v[76:79], v[28:31], v[20:23]\nds_read_b64_tr_b16 v[44:45], v5 offset:1280\n" \
  "ds_read_b64_tr_b16 v[46:47], v5 offset:1312\nv_mfma_f32_16x16x32_f16 v[20:23], v[72:75], v[100:103], v[20:23]\ns_waitcnt lgkmcnt(10)\n" \
  "v_mfma_f32_16x16x32_f16 v[20:23], v[68:71], v[96:99], v[20:23]\nds_read_b64_tr_b16 v[80:81], v5 offset:1536\n" \
  "ds_read_b64_tr_b16 v[82:83], v5 offset:1568\nv_mfma_f32_16x16x32_f16 v[20:23], v[64:67], v[124:127], v[20:23]\n" \
  "ds_read_b64_tr_b16 v[104:105], v5 offset:1792\nds_read_b64_tr_b16 v[106:107], v5 offset:1824\ns_waitcnt lgkmcnt(12)\n" \
  "v_mfma_f32_16x16x32_f16 v[20:23], v[60:63], v[92:95], v[20:23]\nds_read_b64_tr_b16 v[76:77], v5 offset:2048\n" \
  "ds_read_b64_tr_b16 v[78:79], v5 offset:2080\nv_mfma_f32_16x16x32_f16 v[20:23], v[56:59], v[36:39], v[20:23]\ns_waitcnt lgkmcnt(10)\n" \
  "v_mfma_f32_16x16x32_f16 v[12:15], v[88:91], v[52:55], v[12:15]\nds_read_b64_tr_b16 v[28:29], v5 offset:2304\n" \
  "ds_read_b64_tr_b16 v[30:31], v5 offset:2336\nv_mfma_f32_16x16x32_f16 v[12:15], v[108:111], v[48:51], v[12:15]\n" \
  "ds_read_b64_tr_b16 v[72:73], v5 offset:2560\nds_read_b64_tr_b16 v[74:75], v5 offset:2592\ns_waitcnt lgkmcnt(10)\n" \
  "v_mfma_f32_16x16x32_f16 v[12:15], v[84:87], v[40:43], v[12:15]\nds_read_b64_tr_b16 v[100:101], v5 offset:2816\n" \
  "ds_read_b64_tr_b16 v[102:103], v5 offset:2848\nv_mfma_f32_16x16x32_f16 v[16:19], v[84:87], v[52:55], v[16:19]\n" \
  "ds_read_b64_tr_b16 v[68:69], v7 offset:0\nds_read_b64_tr_b16 v[70:71], v7 offset:64\n" \
  "v_mfma_f32_16x16x32_f16 v[12:15], v[44:47], v[120:123], v[12:15]\nv_mfma_f32_16x16x32_f16 v[16:19], v[44:47], v[48:51], v[16:19]\n" \
  "s_waitcnt lgkmcnt(10)\nv_mfma_f32_16x16x32_f16 v[12:15], v[80:83], v[116:119], v[12:15]\nds_read2_b32 v[96:97], v8 offset0:16 offset1:17\n" \
  "ds_read2_b32 v[98:99], v8 offset0:18 offset1:19\nv_mfma_f32_16x16x32_f16 v[16:19], v[80:83], v[40:43], v[16:19]\n" \
  "ds_read2_b32 v[64:65], v8 offset0:0 offset1:1\nds_read2_b32 v[66:67], v8 offset0:2 offset1:3\n" \
  "v_mfma_f32_16x16x32_f16 v[20:23], v[80:83], v[52:55], v[20:23]\nv_mfma_f32_16x16x32_f16 v[12:15], v[104:107], v[112:115], v[12:15]\n" \
  "v_mfma_f32_16x16x32_f16 v[16:19], v[104:107], v[120:123], v[16:19]\nv_mfma_f32_16x16x32_f16 v[20:23], v[104:107], v[48:51], v[20:23]\n" \
  "s_waitcnt lgkmcnt(10)\nv_mfma_f32_16x16x32_f16 v[16:19], v[76:79], v[116:119], v[16:19]\nds_read_b64_tr_b16 v[124:125], v7 offset:512\n" \
  "ds_read_b64_tr_b16 v[126:127], v7 offset:576\nv_mfma_f32_16x16x32_f16 v[20:23], v[76:79], v[40:43], v[20:23]\n" \
  "ds_read2_b32 v[60:61], v8 offset0:48 offset1:49\nds_read2_b32 v[62:63], v8 offset0:50 offset1:51\n" \
  "v_mfma_f32_16x16x32_f16 v[16:19], v[28:31], v[112:115], v[16:19]\nv_mfma_f32_16x16x32_f16 v[20:23], v[28:31], v[120:123], v[20:23]\n" \
  "s_waitcnt lgkmcnt(10)\nv_mfma_f32_16x16x32_f16 v[20:23], v[72:75], v[116:119], v[20:23]\nds_read_b64_tr_b16 v[92:93], v7 offset:1024\n" \
  "ds_read_b64_tr_b16 v[94:95], v7 offset:1088\nv_mfma_f32_16x16x32_f16 v[20:23], v[100:103], v[112:115], v[20:23]\n" \
  "ds_read_b64_tr_b16 v[56:57], v7 offset:1536\nds_read_b64_tr_b16 v[58:59], v7 offset:1600\ns_waitcnt lgkmcnt(10)\n" \
  "v_mfma_f32_16x16x32_f16 v[12:15], v[68:71], v[32:35], v[12:15]\nv_mfma_f32_16x16x32_f16 v[16:19], v[68:71], v[96:99], v[16:19]\n" \
  "s_waitcnt lgkmcnt(6)\nv_mfma_f32_16x16x32_f16 v[20:23], v[68:71], v[64:67], v[20:23]\ns_waitcnt lgkmcnt(2)\n" \
  "v_mfma_f32_16x16x32_f16 v[12:15], v[124:127], v[60:63], v[12:15]\nv_mfma_f32_16x16x32_f16 v[16:19], v[124:127], v[32:35], v[16:19]\n" \
  "v_mfma_f32_16x16x32_f16 v[20:23], v[124:127], v[96:99], v[20:23]\nv_mfma_f32_16x16x32_f16 v[16:19], v[92:95], v[60:63], v[16:19]\n" \
  "v_mfma_f32_16x16x32_f16 v[20:23], v[92:95], v[32:35], v[20:23]\ns_waitcnt lgkmcnt(0)\n" \
  "v_mfma_f32_16x16x32_f16 v[20:23], v[56:59], v[60:63], v[20:23]\ns_nop 1\nv_cvt_pkrtz_f16_f32 v24, v12, v13\nv_cvt_pkrtz_f16_f32 v25, v14, v15\n" \
  "v_pk_add_u16 v24, v24, 4 op_sel_hi:[1,0] clamp\nv_pk_add_u16 v25, v25, 4 op_sel_hi:[1,0] clamp\nds_write_b64 v4, v[24:25] offset:5120\n" \
  "v_cvt_pkrtz_f16_f32 v24, v16, v17\nv_cvt_pkrtz_f16_f32 v25, v18, v19\nv_pk_add_u16 v24, v24, 4 op_sel_hi:[1,0] clamp\n" \
  "v_pk_add_u16 v25, v25, 4 op_sel_hi:[1,0] clamp\nds_write_b64 v4, v[24:25] offset:6144\nv_cvt_pkrtz_f16_f32 v24, v20, v21\n" \
  "v_cvt_pkrtz_f16_f32 v25, v22, v23\nv_pk_add_u16 v24, v24, 4 op_sel_hi:[1,0] clamp\nv_pk_add_u16 v25, v25, 4 op_sel_hi:[1,0] clamp\n" \
  "ds_write_b64 v4, v[24:25] offset:7168\ns_setprio 0\ns_setpc_b64 s[90:91]\n31:\ns_movk_i32 s84, 0x4000\n33:\nds_read_b32 v27, v9 offset:32\n" \
  "s_sub_u32 s84, s84, 1\ns_cbranch_scc1 32b\ns_waitcnt lgkmcnt(0)\nv_readfirstlane_b32 s85, v27\ns_cmp_lt_u32 s85, 4\ns_cbranch_scc1 33b\n" \
  "s_branch 32b\n9:\ns_cmp_eq_u32 s89, 3\ns_cbranch_scc1 3f\ns_cmp_eq_u32 s89, 4\ns_cbranch_scc1 4f\ns_cmp_eq_u32 s89, 5\ns_cbranch_scc1 5f\n" \
  "ds_read_b64 v[124:125], v3 offset:576\nds_read_b64 v[126:127], v3 offset:584\nds_read_b64 v[120:121], v3 offset:64\n" \
  "ds_read_b64 v[122:123], v3 offset:72\nds_read_b64 v[116:117], v3 offset:640\nds_read_b64 v[118:119], v3 offset:648\n" \
  "ds_read_b64 v[112:113], v3 offset:128\nds_read_b64 v[114:115], v3 offset:136\nds_read_b64 v[108:109], v3 offset:704\n" \
  "ds_read_b64 v[110:111], v3 offset:712\nds_read_b64 v[104:105], v3 offset:192\nds_read_b64 v[106:107], v3 offset:200\n" \
  "ds_read_b64 v[100:101], v3 offset:768\nds_read_b64 v[102:103], v3 offset:776\nds_read_b64 v[96:97], v3 offset:256\n" \
  "ds_read_b64 v[98:99], v3 offset:264\nds_read_b64 v[92:93], v3 offset:832\nds_read_b64 v[94:95], v3 offset:840\n" \
  "ds_read_b64 v[88:89], v3 offset:320\nds_read_b64 v[90:91], v3 offset:328\nds_read_b64 v[84:85], v3 offset:896\n" \
  "ds_read_b64 v[86:87], v3 offset:904\nds_read_b64 v[80:81], v3 offset:384\nds_read_b64 v[82:83], v3 offset:392\n" \
  "ds_read_b64 v[76:77], v3 offset:960\nds_read_b64 v[78:79], v3 offset:968\nds_read_b64 v[72:73], v3 offset:448\n" \
  "ds_read_b64 v[74:75], v3 offset:456\nds_read_b64 v[68:69], v3 offset:1024\nds_read_b64 v[70:71], v3 offset:1032\n" \
  "ds_read_b64 v[64:65], v3 offset:512\nds_read_b64 v[66:67], v3 offset:520\ns_waitcnt lgkmcnt(0)\ns_barrier\nds_read_b128 v[24:27], v2 offset:0\n" \
  "ds_read_b128 v[28:31], v2 offset:64\nds_read_b128 v[32:35], v2 offset:128\nds_read_b128 v[36:39], v2 offset:192\ns_waitcnt lgkmcnt(3)\n" \
  "v_mfma_f32_16x16x32_f16 v[12:15], v[124:127], v[24:27], 0\ns_setprio 1\nds_read_b128 v[40:43], v2 offset:256\n" \
  "v_mfma_f32_16x16x32_f16 v[16:19], v[120:123], v[24:27], 0\ns_waitcnt lgkmcnt(3)\nv_mfma_f32_16x16x32_f16 v[12:15], v[116:119], v[28:31], v[12:15]\n" \
  "ds_read_b128 v[44:47], v2 offset:320\nv_mfma_f32_16x16x32_f16 v[16:19], v[112:115], v[28:31], v[16:19]\ns_waitcnt lgkmcnt(3)\n" \
  "v_mfma_f32_16x16x32_f16 v[12:15], v[108:111], v[32:35], v[12:15]\nds_read_b128 v[48:51], v2 offset:384\n" \
  "v_mfma_f32_16x16x32_f16 v[16:19], v[104:107], v[32:35], v[16:19]\ns_waitcnt lgkmcnt(3)\n" \
  "v_mfma_f32_16x16x32_f16 v[12:15], v[100:103], v[36:39], v[12:15]\nds_read_b128 v[52:55], v2 offset:448\n" \
  "v_mfma_f32_16x16x32_f16 v[16:19], v[96:99], v[36:39], v[16:19]\ns_waitcnt lgkmcnt(3)\n" \
  "v_mfma_f32_16x16x32_f16 v[12:15], v[92:95], v[40:43], v[12:15]\nds_read_b128 v[56:59], v2 offset:512\n" \
  "v_mfma_f32_16x16x32_f16 v[16:19], v[88:91], v[40:43], v[16:19]\nds_read_b128 v[60:63], v2 offset:576\ns_waitcnt lgkmcnt(3)\n" \
  "v_mfma_f32_16x16x32_f16 v[12:15], v[84:87], v[44:47], v[12:15]\nds_read_b128 v[120:123], v2 offset:640\n" \
  "v_mfma_f32_16x16x32_f16 v[16:19], v[80:83], v[44:47], v[16:19]\nds_read_b128 v[24:27], v2 offset:704\n" \
  "v_mfma_f32_16x16x32_f16 v[12:15], v[76:79], v[48:51], v[12:15]\nds_read_b128 v[112:115], v2 offset:768\n" \
  "v_mfma_f32_16x16x32_f16 v[16:19], v[72:75], v[48:51], v[16:19]\nds_read_b128 v[28:31], v2 offset:832\ns_waitcnt lgkmcnt(5)\n" \
  "v_mfma_f32_16x16x32_f16 v[12:15], v[68:71], v[52:55], v[12:15]\nds_read_b128 v[104:107], v2 offset:896\n" \
  "v_mfma_f32_16x16x32_f16 v[16:19], v[64:67], v[52:55], v[16:19]\nds_read_b128 v[32:35], v2 offset:960\n" \
  "v_mfma_f32_16x16x32_f16 v[16:19], v[124:127], v[56:59], v[16:19]\ns_waitcnt lgkmcnt(5)\n" \
  "v_mfma_f32_16x16x32_f16 v[16:19], v[116:119], v[60:63], v[16:19]\nv_mfma_f32_16x16x32_f16 v[16:19], v[108:111], v[120:123], v[16:19]\n" \
  "s_waitcnt lgkmcnt(3)\nv_mfma_f32_16x16x32_f16 v[16:19], v[100:103], v[24:27], v[16:19]\nv_cvt_pkrtz_f16_f32 v20, v12, v13\n" \
  "v_cvt_pkrtz_f16_f32 v21, v14, v15\nv_mfma_f32_16x16x32_f16 v[16:19], v[92:95], v[112:115], v[16:19]\n" \
  "v_pk_add_u16 v20, v20, 4 op_sel_hi:[1,0] clamp\ns_waitcnt lgkmcnt(2)\nv_mfma_f32_16x16x32_f16 v[16:19], v[84:87], v[28:31], v[16:19]\n" \
  "v_pk_add_u16 v21, v21, 4 op_sel_hi:[1,0] clamp\ns_waitcnt lgkmcnt(1)\nv_mfma_f32_16x16x32_f16 v[16:19], v[76:79], v[104:107], v[16:19]\n" \
  "ds_write_b64 v4, v[20:21] offset:0\ns_waitcnt lgkmcnt(1)\nv_mfma_f32_16x16x32_f16 v[16:19], v[68:71], v[32:35], v[16:19]\ns_nop 7\n" \
  "v_cvt_pkrtz_f16_f32 v20, v16, v17\nv_cvt_pkrtz_f16_f32 v21, v18, v19\nv_pk_add_u16 v20, v20, 4 op_sel_hi:[1,0] clamp\n" \
  "v_pk_add_u16 v21, v21, 4 op_sel_hi:[1,0] clamp\nds_write_b64 v4, v[20:21] offset:1024\ns_setprio 0\ns_setpc_b64 s[90:91]\n3:\n" \
  "ds_read_b64 v[124:125], v3 offset:0\nds_read_b64 v[126:127], v3 offset:8\nds_read_b64 v[120:121], v3 offset:64\n" \
  "ds_read_b64 v[122:123], v3 offset:72\nds_read_b64 v[116:117], v3 offset:128\nds_read_b64 v[118:119], v3 offset:136\n" \
  "ds_read_b64 v[112:113], v3 offset:192\nds_read_b64 v[114:115], v3 offset:200\nds_read_b64 v[108:109], v3 offset:256\n" \
  "ds_read_b64 v[110:111], v3 offset:264\nds_read_b64 v[104:105], v3 offset:320\nds_read_b64 v[106:107], v3 offset:328\n" \
  "ds_read_b64 v[100:101], v3 offset:384\nds_read_b64 v[102:103], v3 offset:392\nds_read_b64 v[96:97], v3 offset:448\n" \
  "ds_read_b64 v[98:99], v3 offset:456\nds_read_b64 v[92:93], v3 offset:512\nds_read_b64 v[94:95], v3 offset:520\n" \
  "ds_read_b64 v[88:89], v3 offset:576\nds_read_b64 v[90:91], v3 offset:584\nds_read_b64 v[84:85], v3 offset:640\n" \
  "ds_read_b64 v[86:87], v3 offset:648\nds_read_b64 v[80:81], v3 offset:704\nds_read_b64 v[82:83], v3 offset:712\n" \
  "ds_read_b64 v[76:77], v3 offset:768\nds_read_b64 v[78:79], v3 offset:776\nds_read_b64 v[72:73], v3 offset:832\n" \
  "ds_read_b64 v[74:75], v3 offset:840\nds_read_b64 v[68:69], v3 offset:896\nds_read_b64 v[70:71], v3 offset:904\n" \
  "ds_read_b64 v[64:65], v3 offset:960\nds_read_b64 v[66:67], v3 offset:968\nds_read_b64 v[60:61], v3 offset:1024\n" \
  "ds_read_b64 v[62:63], v3 offset:1032\nds_read_b64 v[56:57], v6 offset:256\nds_read_b64 v[58:59], v6 offset:264\n" \
  "ds_read_b64 v[52:53], v6 offset:128\nds_read_b64 v[54:55], v6 offset:136\nds_read_b64 v[48:49], v6 offset:320\n" \
  "ds_read_b64 v[50:51], v6 offset:328\nds_read_b64 v[44:45], v6 offset:192\nds_read_b64 v[46:47], v6 offset:200\ns_waitcnt lgkmcnt(0)\ns_barrier\n" \
  "ds_read_b128 v[24:27], v2 offset:448\nds_read_b128 v[28:31], v2 offset:512\nds_read_b128 v[32:35], v2 offset:576\n" \
  "ds_read_b128 v[36:39], v2 offset:640\nds_read_b128 v[40:43], v2 offset:704\ns_waitcnt lgkmcnt(3)\n" \
  "v_mfma_f32_16x16x32_f16 v[12:15], v[124:127], v[24:27], 0\ns_setprio 1\nds_read_b128 v[24:27], v2 offset:768\n" \
  "v_mfma_f32_16x16x32_f16 v[12:15], v[120:123], v[28:31], v[12:15]\nds_read_b128 v[28:31], v2 offset:832\ns_waitcnt lgkmcnt(3)\n" \
  "v_mfma_f32_16x16x32_f16 v[12:15], v[116:119], v[32:35], v[12:15]\nds_read_b128 v[32:35], v2 offset:896\n" \
  "v_mfma_f32_16x16x32_f16 v[12:15], v[112:115], v[36:39], v[12:15]\nds_read_b128 v[36:39], v2 offset:960\ns_waitcnt lgkmcnt(3)\n" \
  "v_mfma_f32_16x16x32_f16 v[12:15], v[108:111], v[40:43], v[12:15]\nds_read_b128 v[40:43], v2 offset:1024\n" \
  "v_mfma_f32_16x16x32_f16 v[12:15], v[104:107], v[24:27], v[12:15]\nds_read_b128 v[24:27], v2 offset:1088\ns_waitcnt lgkmcnt(3)\n" \
  "v_mfma_f32_16x16x32_f16 v[12:15], v[100:103], v[28:31], v[12:15]\nds_read_b128 v[28:31], v2 offset:1152\n" \
  "v_mfma_f32_16x16x32_f16 v[12:15], v[96:99], v[32:35], v[12:15]\ns_waitcnt lgkmcnt(3)\n" \
  "v_mfma_f32_16x16x32_f16 v[12:15], v[92:95], v[36:39], v[12:15]\nds_read_b128 v[32:35], v2 offset:1216\n" \
  "v_mfma_f32_16x16x32_f16 v[16:19], v[124:127], v[36:39], 0\ns_waitcnt lgkmcnt(3)\nv_mfma_f32_16x16x32_f16 v[12:15], v[88:91], v[40:43], v[12:15]\n" \
  "ds_read_b128 v[124:127], v2 offset:1280\nv_mfma_f32_16x16x32_f16 v[16:19], v[120:123], v[40:43], v[16:19]\ns_waitcnt lgkmcnt(3)\n" \
  "v_mfma_f32_16x16x32_f16 v[12:15], v[84:87], v[24:27], v[12:15]\nds_read_b128 v[36:39], v2 offset:1344\n" \
  "v_mfma_f32_16x16x32_f16 v[16:19], v[116:119], v[24:27], v[16:19]\ns_waitcnt lgkmcnt(3)\n" \
  "v_mfma_f32_16x16x32_f16 v[12:15], v[80:83], v[28:31], v[12:15]\nds_read_b128 v[120:123], v2 offset:1408\n" \
  "v_mfma_f32_16x16x32_f16 v[16:19], v[112:115], v[28:31], v[16:19]\ns_waitcnt lgkmcnt(3)\n" \
  "v_mfma_f32_16x16x32_f16 v[12:15], v[76:79], v[32:35], v[12:15]\nds_read_b128 v[40:43], v2 offset:1472\n" \
  "v_mfma_f32_16x16x32_f16 v[16:19], v[108:111], v[32:35], v[16:19]\ns_waitcnt lgkmcnt(3)\n" \
  "v_mfma_f32_16x16x32_f16 v[12:15], v[72:75], v[124:127], v[12:15]\nds_read_b128 v[116:119], v2 offset:1536\n" \
  "v_mfma_f32_16x16x32_f16 v[16:19], v[104:107], v[124:127], v[16:19]\nds_read_b128 v[24:27], v2 offset:1600\ns_waitcnt lgkmcnt(3)\n" \
  "v_mfma_f32_16x16x32_f16 v[12:15], v[68:71], v[36:39], v[12:15]\nds_read_b128 v[112:115], v2 offset:1664\nds_read_b32 v23, v9 offset:32\n" \
  "v_mfma_f32_16x16x32_f16 v[16:19], v[100:103], v[36:39], v[16:19]\nds_read_b128 v[28:31], v2 offset:1728\n" \
  "v_mfma_f32_16x16x32_f16 v[12:15], v[64:67], v[120:123], v[12:15]\nds_read_b128 v[108:111], v2 offset:1792\n" \
  "v_mfma_f32_16x16x32_f16 v[16:19], v[96:99], v[120:123], v[16:19]\nds_read_b128 v[32:35], v2 offset:1856\ns_waitcnt lgkmcnt(6)\n" \
  "v_mfma_f32_16x16x32_f16 v[12:15], v[60:63], v[40:43], v[12:15]\nds_read_b128 v[104:107], v2 offset:1920\n" \
  "v_mfma_f32_16x16x32_f16 v[16:19], v[92:95], v[40:43], v[16:19]\nds_read_b128 v[124:127], v2 offset:1984\ns_waitcnt lgkmcnt(5)\n" \
  "v_readfirstlane_b32 s85, v23\ns_cmp_lt_u32 s85, 3\ns_cbranch_scc1 51f\n52:\nv_mfma_f32_16x16x32_f16 v[16:19], v[88:91], v[116:119], v[16:19]\n" \
  "ds_read_b64_tr_b16 v[100:101], v5 offset:0\nds_read_b64_tr_b16 v[102:103], v5 offset:32\n" \
  "v_mfma_f32_16x16x32_f16 v[16:19], v[84:87], v[24:27], v[16:19]\nds_read_b64_tr_b16 v[36:37], v5 offset:256\n" \
  "ds_read_b64_tr_b16 v[38:39], v5 offset:288\nv_mfma_f32_16x16x32_f16 v[16:19], v[80:83], v[112:115], v[16:19]\n" \
  "ds_read_b64_tr_b16 v[96:97], v5 offset:512\nds_read_b64_tr_b16 v[98:99], v5 offset:544\ns_waitcnt lgkmcnt(9)\n" \
  "v_mfma_f32_16x16x32_f16 v[16:19], v[76:79], v[28:31], v[16:19]\nds_read_b64_tr_b16 v[120:121], v5 offset:768\n" \
  "ds_read_b64_tr_b16 v[122:123], v5 offset:800\nv_mfma_f32_16x16x32_f16 v[16:19], v[72:75], v[108:111], v[16:19]\ns_waitcnt lgkmcnt(9)\n" \
  "v_mfma_f32_16x16x32_f16 v[16:19], v[68:71], v[32:35], v[16:19]\nv_mfma_f32_16x16x32_f16 v[16:19], v[64:67], v[104:107], v[16:19]\n" \
  "s_waitcnt lgkmcnt(6)\nv_mfma_f32_16x16x32_f16 v[16:19], v[60:63], v[124:127], v[16:19]\n" \
  "v_mfma_f32_16x16x32_f16 v[12:15], v[100:103], v[56:59], v[12:15]\nv_mfma_f32_16x16x32_f16 v[16:19], v[100:103], v[52:55], v[16:19]\n" \
  "s_waitcnt lgkmcnt(2)\nv_mfma_f32_16x16x32_f16 v[12:15], v[36:39], v[48:51], v[12:15]\n" \
  "v_mfma_f32_16x16x32_f16 v[16:19], v[36:39], v[44:47], v[16:19]\nv_mfma_f32_16x16x32_f16 v[16:19], v[96:99], v[56:59], v[16:19]\n" \
  "s_waitcnt lgkmcnt(0)\nv_mfma_f32_16x16x32_f16 v[16:19], v[120:123], v[48:51], v[16:19]\ns_nop 3\nv_cvt_pkrtz_f16_f32 v20, v12, v13\n" \
  "v_cvt_pkrtz_f16_f32 v21, v14, v15\nv_pk_add_u16 v20, v20, 4 op_sel_hi:[1,0] clamp\nv_pk_add_u16 v21, v21, 4 op_sel_hi:[1,0] clamp\n" \
  "ds_write_b64 v4, v[20:21] offset:2048\nv_cvt_pkrtz_f16_f32 v20, v16, v17\nv_cvt_pkrtz_f16_f32 v21, v18, v19\n" \
  "v_pk_add_u16 v20, v20, 4 op_sel_hi:[1,0] clamp\nv_pk_add_u16 v21, v21, 4 op_sel_hi:[1,0] clamp\nds_write_b64 v4, v[20:21] offset:3072\n" \
  "s_setprio 0\ns_setpc_b64 s[90:91]\n51:\ns_movk_i32 s84, 0x4000\n53:\nds_read_b32 v23, v9 offset:32\ns_sub_u32 s84, s84, 1\ns_cbranch_scc1 52b\n" \
  "s_waitcnt lgkmcnt(0)\nv_readfirstlane_b32 s85, v23\ns_cmp_lt_u32 s85, 3\ns_cbranch_scc1 53b\ns_branch 52b\n4:\n" \
  "ds_read_b64 v[124:125], v3 offset:1088\nds_read_b64 v[126:127], v3 offset:1096\nds_read_b64 v[120:121], v3 offset:0\n" \
  "ds_read_b64 v[122:123], v3 offset:8\nds_read_b64 v[116:117], v3 offset:64\nds_read_b64 v[118:119], v3 offset:72\n" \
  "ds_read_b64 v[112:113], v3 offset:128\nds_read_b64 v[114:115], v3 offset:136\nds_read_b64 v[108:109], v3 offset:192\n" \
  "ds_read_b64 v[110:111], v3 offset:200\nds_read_b64 v[104:105], v3 offset:256\nds_read_b64 v[106:107], v3 offset:264\n" \
  "ds_read_b64 v[100:101], v3 offset:320\nds_read_b64 v[102:103], v3 offset:328\nds_read_b64 v[96:97], v3 offset:384\n" \
  "ds_read_b64 v[98:99], v3 offset:392\nds_read_b64 v[92:93], v3 offset:448\nds_read_b64 v[94:95], v3 offset:456\n" \
  "ds_read_b64 v[88:89], v3 offset:512\nds_read_b64 v[90:91], v3 offset:520\nds_read_b64 v[84:85], v3 offset:576\n" \
  "ds_read_b64 v[86:87], v3 offset:584\nds_read_b64 v[80:81], v3 offset:640\nds_read_b64 v[82:83], v3 offset:648\n" \
  "ds_read_b64 v[76:77], v3 offset:704\nds_read_b64 v[78:79], v3 offset:712\nds_read_b64 v[72:73], v3 offset:768\n" \
  "ds_read_b64 v[74:75], v3 offset:776\nds_read_b64 v[68:69], v3 offset:832\nds_read_b64 v[70:71], v3 offset:840\n" \
  "ds_read_b64 v[64:65], v3 offset:896\nds_read_b64 v[66:67], v3 offset:904\nds_read_b64 v[60:61], v3 offset:960\n" \
  "ds_read_b64 v[62:63], v3 offset:968\nds_read_b64 v[56:57], v3 offset:1024\nds_read_b64 v[58:59], v3 offset:1032\n" \
  "ds_read_b64 v[52:53], v6 offset:0\nds_read_b64 v[54:55], v6 offset:8\nds_read_b64 v[48:49], v6 offset:64\nds_read_b64 v[50:51], v6 offset:72\n" \
  "ds_read_b64 v[44:45], v6 offset:128\nds_read_b64 v[46:47], v6 offset:136\nds_read_b64 v[40:41], v6 offset:192\n" \
  "ds_read_b64 v[42:43], v6 offset:200\ns_waitcnt lgkmcnt(0)\ns_barrier\nds_read_b128 v[24:27], v2 offset:448\nds_read_b128 v[28:31], v2 offset:960\n" \
  "ds_read_b128 v[32:35], v2 offset:1472\nds_read_b128 v[36:39], v2 offset:1536\ns_waitcnt lgkmcnt(3)\n" \
  "v_mfma_f32_16x16x32_f16 v[12:15], v[124:127], v[24:27], 0\ns_setprio 1\nds_read_b128 v[24:27], v2 offset:1600\ns_waitcnt lgkmcnt(3)\n" \
  "v_mfma_f32_16x16x32_f16 v[16:19], v[124:127], v[28:31], 0\nds_read_b128 v[124:127], v2 offset:1664\ns_waitcnt lgkmcnt(3)\n" \
  "v_mfma_f32_16x16x32_f16 v[12:15], v[120:123], v[32:35], v[12:15]\nds_read_b128 v[28:31], v2 offset:1728\ns_waitcnt lgkmcnt(3)\n" \
  "v_mfma_f32_16x16x32_f16 v[12:15], v[116:119], v[36:39], v[12:15]\nds_read_b128 v[32:35], v2 offset:1792\ns_waitcnt lgkmcnt(3)\n" \
  "v_mfma_f32_16x16x32_f16 v[12:15], v[112:115], v[24:27], v[12:15]\nds_read_b128 v[36:39], v2 offset:1856\ns_waitcnt lgkmcnt(3)\n" \
  "v_mfma_f32_16x16x32_f16 v[12:15], v[108:111], v[124:127], v[12:15]\nds_read_b128 v[24:27], v2 offset:1920\ns_waitcnt lgkmcnt(3)\n" \
  "v_mfma_f32_16x16x32_f16 v[12:15], v[104:107], v[28:31], v[12:15]\nds_read_b128 v[124:127], v2 offset:1984\ns_waitcnt lgkmcnt(3)\n" \
  "v_mfma_f32_16x16x32_f16 v[12:15], v[100:103], v[32:35], v[12:15]\nds_read_b128 v[28:31], v2 offset:2048\ns_waitcnt lgkmcnt(3)\n" \
  "v_mfma_f32_16x16x32_f16 v[12:15], v[96:99], v[36:39], v[12:15]\nds_read_b128 v[32:35], v2 offset:2112\ns_waitcnt lgkmcnt(3)\n" \
  "v_mfma_f32_16x16x32_f16 v[12:15], v[92:95], v[24:27], v[12:15]\nds_read_b128 v[36:39], v2 offset:2176\ns_waitcnt lgkmcnt(3)\n" \
  "v_mfma_f32_16x16x32_f16 v[12:15], v[88:91], v[124:127], v[12:15]\nds_read_b128 v[24:27], v2 offset:2240\n" \
  "v_mfma_f32_16x16x32_f16 v[16:19], v[120:123], v[124:127], v[16:19]\nds_read_b64 v[120:121], v6 offset:256\nds_read_b64 v[122:123], v6 offset:264\n" \
  "s_waitcnt lgkmcnt(4)\nv_mfma_f32_16x16x32_f16 v[12:15], v[84:87], v[28:31], v[12:15]\nds_read_b128 v[124:127], v2 offset:2304\n" \
  "v_mfma_f32_16x16x32_f16 v[16:19], v[116:119], v[28:31], v[16:19]\nds_read_b64 v[116:117], v6 offset:320\nds_read_b64 v[118:119], v6 offset:328\n" \
  "v_mfma_f32_16x16x32_f16 v[12:15], v[80:83], v[32:35], v[12:15]\nds_read_b128 v[28:31], v2 offset:2368\n" \
  "v_mfma_f32_16x16x32_f16 v[16:19], v[112:115], v[32:35], v[16:19]\ns_waitcnt lgkmcnt(6)\n" \
  "v_mfma_f32_16x16x32_f16 v[12:15], v[76:79], v[36:39], v[12:15]\nds_read_b128 v[112:115], v2 offset:2432\n" \
  "v_mfma_f32_16x16x32_f16 v[16:19], v[108:111], v[36:39], v[16:19]\nds_read2_b32 v[32:33], v8 offset0:48 offset1:49\n" \
  "ds_read2_b32 v[34:35], v8 offset0:50 offset1:51\nv_mfma_f32_16x16x32_f16 v[12:15], v[72:75], v[24:27], v[12:15]\n" \
  "ds_read_b128 v[108:111], v2 offset:2496\nv_mfma_f32_16x16x32_f16 v[16:19], v[104:107], v[24:27], v[16:19]\n" \
  "ds_read2_b32 v[36:37], v8 offset0:32 offset1:33\nds_read2_b32 v[38:39], v8 offset0:34 offset1:35\ns_waitcnt lgkmcnt(6)\n" \
  "v_mfma_f32_16x16x32_f16 v[12:15], v[68:71], v[124:127], v[12:15]\nds_read_b128 v[104:107], v2 offset:2560\n" \
  "v_mfma_f32_16x16x32_f16 v[16:19], v[100:103], v[124:127], v[16:19]\nds_read_b128 v[24:27], v2 offset:2624\n" \
  "v_mfma_f32_16x16x32_f16 v[12:15], v[64:67], v[28:31], v[12:15]\nds_read_b128 v[100:103], v2 offset:2688\nds_read_b32 v23, v9 offset:32\n" \
  "v_mfma_f32_16x16x32_f16 v[16:19], v[96:99], v[28:31], v[16:19]\nds_read_b128 v[124:127], v2 offset:2752\ns_waitcnt lgkmcnt(7)\n" \
  "v_mfma_f32_16x16x32_f16 v[12:15], v[60:63], v[112:115], v[12:15]\nds_read_b128 v[96:99], v2 offset:2816\n" \
  "v_mfma_f32_16x16x32_f16 v[16:19], v[92:95], v[112:115], v[16:19]\nds_read_b128 v[28:31], v2 offset:2880\n" \
  "v_mfma_f32_16x16x32_f16 v[12:15], v[56:59], v[108:111], v[12:15]\nds_read_b128 v[92:95], v2 offset:2944\n" \
  "v_mfma_f32_16x16x32_f16 v[16:19], v[88:91], v[108:111], v[16:19]\nds_read_b128 v[112:115], v2 offset:3008\ns_waitcnt lgkmcnt(5)\n" \
  "v_readfirstlane_b32 s85, v23\ns_cmp_lt_u32 s85, 3\ns_cbranch_scc1 61f\n62:\nv_mfma_f32_16x16x32_f16 v[16:19], v[84:87], v[104:107], v[16:19]\n" \
  "ds_read_b64_tr_b16 v[88:89], v5 offset:0\nds_read_b64_tr_b16 v[90:91], v5 offset:32\n" \
  "v_mfma_f32_16x16x32_f16 v[16:19], v[80:83], v[24:27], v[16:19]\nds_read_b64_tr_b16 v[108:109], v5 offset:256\n" \
  "ds_read_b64_tr_b16 v[110:111], v5 offset:288\nv_mfma_f32_16x16x32_f16 v[16:19], v[76:79], v[100:103], v[16:19]\n" \
  "ds_read_b64_tr_b16 v[84:85], v5 offset:512\nds_read_b64_tr_b16 v[86:87], v5 offset:544\ns_waitcnt lgkmcnt(9)\n" \
  "v_mfma_f32_16x16x32_f16 v[16:19], v[72:75], v[124:127], v[16:19]\nds_read_b64_tr_b16 v[104:105], v5 offset:768\n" \
  "ds_read_b64_tr_b16 v[106:107], v5 offset:800\nv_mfma_f32_16x16x32_f16 v[16:19], v[68:71], v[96:99], v[16:19]\n" \
  "ds_read_b64_tr_b16 v[80:81], v5 offset:1024\nds_read_b64_tr_b16 v[82:83], v5 offset:1056\ns_waitcnt lgkmcnt(11)\n" \
  "v_mfma_f32_16x16x32_f16 v[16:19], v[64:67], v[28:31], v[16:19]\nds_read_b64_tr_b16 v[24:25], v5 offset:1280\n" \
  "ds_read_b64_tr_b16 v[26:27], v5 offset:1312\nv_mfma_f32_16x16x32_f16 v[16:19], v[60:63], v[92:95], v[16:19]\n" \
  "ds_read_b64_tr_b16 v[76:77], v5 offset:1536\nds_read_b64_tr_b16 v[78:79], v5 offset:1568\ns_waitcnt lgkmcnt(12)\n" \
  "v_mfma_f32_16x16x32_f16 v[16:19], v[56:59], v[112:115], v[16:19]\nds_read_b64_tr_b16 v[100:101], v5 offset:1792\n" \
  "ds_read_b64_tr_b16 v[102:103], v5 offset:1824\nv_mfma_f32_16x16x32_f16 v[12:15], v[88:91], v[52:55], v[12:15]\ns_waitcnt lgkmcnt(10)\n" \
  "v_mfma_f32_16x16x32_f16 v[12:15], v[108:111], v[48:51], v[12:15]\nds_read_b64_tr_b16 v[72:73], v7 offset:0\n" \
  "ds_read_b64_tr_b16 v[74:75], v7 offset:64\nv_mfma_f32_16x16x32_f16 v[12:15], v[84:87], v[44:47], v[12:15]\n" \
  "ds_read_b64_tr_b16 v[124:125], v7 offset:512\nds_read_b64_tr_b16 v[126:127], v7 offset:576\n" \
  "v_mfma_f32_16x16x32_f16 v[16:19], v[84:87], v[52:55], v[16:19]\ns_waitcnt lgkmcnt(10)\n" \
  "v_mfma_f32_16x16x32_f16 v[12:15], v[104:107], v[40:43], v[12:15]\nv_mfma_f32_16x16x32_f16 v[16:19], v[104:107], v[48:51], v[16:19]\n" \
  "v_mfma_f32_16x16x32_f16 v[12:15], v[80:83], v[120:123], v[12:15]\nv_mfma_f32_16x16x32_f16 v[16:19], v[80:83], v[44:47], v[16:19]\n" \
  "s_waitcnt lgkmcnt(6)\nv_mfma_f32_16x16x32_f16 v[12:15], v[24:27], v[116:119], v[12:15]\n" \
  "v_mfma_f32_16x16x32_f16 v[16:19], v[24:27], v[40:43], v[16:19]\nv_mfma_f32_16x16x32_f16 v[16:19], v[76:79], v[120:123], v[16:19]\n" \
  "s_waitcnt lgkmcnt(2)\nv_mfma_f32_16x16x32_f16 v[16:19], v[100:103], v[116:119], v[16:19]\n" \
  "v_mfma_f32_16x16x32_f16 v[12:15], v[72:75], v[32:35], v[12:15]\nv_mfma_f32_16x16x32_f16 v[16:19], v[72:75], v[36:39], v[16:19]\n" \
  "s_waitcnt lgkmcnt(0)\nv_mfma_f32_16x16x32_f16 v[16:19], v[124:127], v[32:35], v[16:19]\ns_nop 4\nv_cvt_pkrtz_f16_f32 v20, v12, v13\n" \
  "v_cvt_pkrtz_f16_f32 v21, v14, v15\nv_pk_add_u16 v20, v20, 4 op_sel_hi:[1,0] clamp\nv_pk_add_u16 v21, v21, 4 op_sel_hi:[1,0] clamp\n" \
  "ds_write_b64 v4, v[20:21] offset:4096\nv_cvt_pkrtz_f16_f32 v20, v16, v17\nv_cvt_pkrtz_f16_f32 v21, v18, v19\n" \
  "v_pk_add_u16 v20, v20, 4 op_sel_hi:[1,0] clamp\nv_pk_add_u16 v21, v21, 4 op_sel_hi:[1,0] clamp\nds_write_b64 v4, v[20:21] offset:5120\n" \
  "s_setprio 0\ns_setpc_b64 s[90:91]\n61:\ns_movk_i32 s84, 0x4000\n63:\nds_read_b32 v23, v9 offset:32\ns_sub_u32 s84, s84, 1\ns_cbranch_scc1 62b\n" \
  "s_waitcnt lgkmcnt(0)\nv_readfirstlane_b32 s85, v23\ns_cmp_lt_u32 s85, 3\ns_cbranch_scc1 63b\ns_branch 62b\n5:\n" \
  "ds_read_b64 v[124:125], v3 offset:1088\nds_read_b64 v[126:127], v3 offset:1096\nds_read_b64 v[120:121], v3 offset:0\n" \
  "ds_read_b64 v[122:123], v3 offset:8\nds_read_b64 v[116:117], v3 offset:64\nds_read_b64 v[118:119], v3 offset:72\n" \
  "ds_read_b64 v[112:113], v3 offset:128\nds_read_b64 v[114:115], v3 offset:136\nds_read_b64 v[108:109], v3 offset:192\n" \
  "ds_read_b64 v[110:111], v3 offset:200\nds_read_b64 v[104:105], v3 offset:256\nds_read_b64 v[106:107], v3 offset:264\n" \
  "ds_read_b64 v[100:101], v3 offset:320\nds_read_b64 v[102:103], v3 offset:328\nds_read_b64 v[96:97], v3 offset:384\n" \
  "ds_read_b64 v[98:99], v3 offset:392\nds_read_b64 v[92:93], v3 offset:448\nds_read_b64 v[94:95], v3 offset:456\n" \
  "ds_read_b64 v[88:89], v3 offset:512\nds_read_b64 v[90:91], v3 offset:520\nds_read_b64 v[84:85], v3 offset:576\n" \
  "ds_read_b64 v[86:87], v3 offset:584\nds_read_b64 v[80:81], v3 offset:640\nds_read_b64 v[82:83], v3 offset:648\n" \
  "ds_read_b64 v[76:77], v3 offset:704\nds_read_b64 v[78:79], v3 offset:712\nds_read_b64 v[72:73], v3 offset:768\n" \
  "ds_read_b64 v[74:75], v3 offset:776\nds_read_b64 v[68:69], v3 offset:832\nds_read_b64 v[70:71], v3 offset:840\n" \
  "ds_read_b64 v[64:65], v3 offset:896\nds_read_b64 v[66:67], v3 offset:904\nds_read_b64 v[60:61], v3 offset:960\n" \
  "ds_read_b64 v[62:63], v3 offset:968\nds_read_b64 v[56:57], v3 offset:1024\nds_read_b64 v[58:59], v3 offset:1032\n" \
  "ds_read_b64 v[52:53], v6 offset:0\nds_read_b64 v[54:55], v6 offset:8\nds_read_b64 v[48:49], v6 offset:64\nds_read_b64 v[50:51], v6 offset:72\n" \
  "ds_read_b64 v[44:45], v6 offset:128\nds_read_b64 v[46:47], v6 offset:136\nds_read_b64 v[40:41], v6 offset:192\n" \
  "ds_read_b64 v[42:43], v6 offset:200\ns_waitcnt lgkmcnt(0)\ns_barrier\nds_read_b128 v[24:27], v2 offset:1472\n" \
  "ds_read_b128 v[28:31], v2 offset:1984\nds_read_b128 v[32:35], v2 offset:2496\nds_read_b128 v[36:39], v2 offset:2560\ns_waitcnt lgkmcnt(3)\n" \
  "v_mfma_f32_16x16x32_f16 v[12:15], v[124:127], v[24:27], 0\ns_setprio 1\nds_read_b128 v[24:27], v2 offset:2624\ns_waitcnt lgkmcnt(3)\n" \
  "v_mfma_f32_16x16x32_f16 v[16:19], v[124:127], v[28:31], 0\nds_read_b128 v[124:127], v2 offset:2688\ns_waitcnt lgkmcnt(3)\n" \
  "v_mfma_f32_16x16x32_f16 v[12:15], v[120:123], v[32:35], v[12:15]\nds_read_b128 v[28:31], v2 offset:2752\ns_waitcnt lgkmcnt(3)\n" \
  "v_mfma_f32_16x16x32_f16 v[12:15], v[116:119], v[36:39], v[12:15]\nds_read_b128 v[32:35], v2 offset:2816\ns_waitcnt lgkmcnt(3)\n" \
  "v_mfma_f32_16x16x32_f16 v[12:15], v[112:115], v[24:27], v[12:15]\nds_read_b128 v[36:39], v2 offset:2880\ns_waitcnt lgkmcnt(3)\n" \
  "v_mfma_f32_16x16x32_f16 v[12:15], v[108:111], v[124:127], v[12:15]\nds_read_b128 v[24:27], v2 offset:2944\ns_waitcnt lgkmcnt(3)\n" \
  "v_mfma_f32_16x16x32_f16 v[12:15], v[104:107], v[28:31], v[12:15]\nds_read_b128 v[124:127], v2 offset:3008\ns_waitcnt lgkmcnt(3)\n" \
  "v_mfma_f32_16x16x32_f16 v[12:15], v[100:103], v[32:35], v[12:15]\nds_read_b128 v[28:31], v2 offset:3072\ns_waitcnt lgkmcnt(3)\n" \
  "v_mfma_f32_16x16x32_f16 v[12:15], v[96:99], v[36:39], v[12:15]\nds_read_b128 v[32:35], v2 offset:3136\ns_waitcnt lgkmcnt(3)\n" \
  "v_mfma_f32_16x16x32_f16 v[12:15], v[92:95], v[24:27], v[12:15]\nds_read_b128 v[36:39], v2 offset:3200\ns_waitcnt lgkmcnt(3)\n" \
  "v_mfma_f32_16x16x32_f16 v[12:15], v[88:91], v[124:127], v[12:15]\nds_read_b128 v[24:27], v2 offset:3264\n" \
  "v_mfma_f32_16x16x32_f16 v[16:19], v[120:123], v[124:127], v[16:19]\nds_read_b64 v[120:121], v6 offset:256\nds_read_b64 v[122:123], v6 offset:264\n" \
  "s_waitcnt lgkmcnt(4)\nv_mfma_f32_16x16x32_f16 v[12:15], v[84:87], v[28:31], v[12:15]\nds_read_b128 v[124:127], v2 offset:3328\n" \
  "v_mfma_f32_16x16x32_f16 v[16:19], v[116:119], v[28:31], v[16:19]\nds_read_b64 v[116:117], v6 offset:320\nds_read_b64 v[118:119], v6 offset:328\n" \
  "v_mfma_f32_16x16x32_f16 v[12:15], v[80:83], v[32:35], v[12:15]\nds_read_b128 v[28:31], v2 offset:3392\n" \
  "v_mfma_f32_16x16x32_f16 v[16:19], v[112:115], v[32:35], v[16:19]\ns_waitcnt lgkmcnt(6)\n" \
  "v_mfma_f32_16x16x32_f16 v[12:15], v[76:79], v[36:39], v[12:15]\nds_read_b128 v[112:115], v2 offset:3456\n" \
  "v_mfma_f32_16x16x32_f16 v[16:19], v[108:111], v[36:39], v[16:19]\nds_read2_b32 v[32:33], v8 offset0:16 offset1:17\n" \
  "ds_read2_b32 v[34:35], v8 offset0:18 offset1:19\nv_mfma_f32_16x16x32_f16 v[12:15], v[72:75], v[24:27], v[12:15]\n" \
  "ds_read_b128 v[108:111], v2 offset:3520\nv_mfma_f32_16x16x32_f16 v[16:19], v[104:107], v[24:27], v[16:19]\n" \
  "ds_read2_b32 v[36:37], v8 offset0:0 offset1:1\nds_read2_b32 v[38:39], v8 offset0:2 offset1:3\ns_waitcnt lgkmcnt(6)\n" \
  "v_mfma_f32_16x16x32_f16 v[12:15], v[68:71], v[124:127], v[12:15]\nds_read_b128 v[104:107], v2 offset:3584\n" \
  "v_mfma_f32_16x16x32_f16 v[16:19], v[100:103], v[124:127], v[16:19]\nds_read_b128 v[24:27], v2 offset:3648\n" \
  "ds_read2_b32 v[100:101], v8 offset0:32 offset1:33\nds_read2_b32 v[102:103], v8 offset0:34 offset1:35\n" \
  "v_mfma_f32_16x16x32_f16 v[12:15], v[64:67], v[28:31], v[12:15]\nds_read_b128 v[124:127], v2 offset:3712\nds_read_b32 v23, v9 offset:32\n" \
  "v_mfma_f32_16x16x32_f16 v[16:19], v[96:99], v[28:31], v[16:19]\nds_read_b128 v[96:99], v2 offset:3776\ns_waitcnt lgkmcnt(9)\n" \
  "v_mfma_f32_16x16x32_f16 v[12:15], v[60:63], v[112:115], v[12:15]\nds_read_b128 v[28:31], v2 offset:3840\n" \
  "v_mfma_f32_16x16x32_f16 v[16:19], v[92:95], v[112:115], v[16:19]\nds_read_b128 v[92:95], v2 offset:3904\n" \
  "v_mfma_f32_16x16x32_f16 v[12:15], v[56:59], v[108:111], v[12:15]\nds_read_b128 v[112:115], v2 offset:3968\n" \
  "v_mfma_f32_16x16x32_f16 v[16:19], v[88:91], v[108:111], v[16:19]\nds_read_b128 v[88:91], v2 offset:4032\ns_waitcnt lgkmcnt(5)\n" \
  "v_readfirstlane_b32 s85, v23\ns_cmp_lt_u32 s85, 3\ns_cbranch_scc1 71f\n72:\nv_mfma_f32_16x16x32_f16 v[16:19], v[84:87], v[104:107], v[16:19]\n" \
  "ds_read_b64_tr_b16 v[108:109], v5 offset:1024\nds_read_b64_tr_b16 v[110:111], v5 offset:1056\n" \
  "v_mfma_f32_16x16x32_f16 v[16:19], v[80:83], v[24:27], v[16:19]\nds_read_b64_tr_b16 v[84:85], v5 offset:1280\n" \
  "ds_read_b64_tr_b16 v[86:87], v5 offset:1312\nv_mfma_f32_16x16x32_f16 v[16:19], v[76:79], v[124:127], v[16:19]\n" \
  "ds_read_b64_tr_b16 v[104:105], v5 offset:1536\nds_read_b64_tr_b16 v[106:107], v5 offset:1568\ns_waitcnt lgkmcnt(9)\n" \
  "v_mfma_f32_16x16x32_f16 v[16:19], v[72:75], v[96:99], v[16:19]\nds_read_b64_tr_b16 v[80:81], v5 offset:1792\n" \
  "ds_read_b64_tr_b16 v[82:83], v5 offset:1824\nv_mfma_f32_16x16x32_f16 v[16:19], v[68:71], v[28:31], v[16:19]\n" \
  "ds_read_b64_tr_b16 v[24:25], v5 offset:2048\nds_read_b64_tr_b16 v[26:27], v5 offset:2080\ns_waitcnt lgkmcnt(11)\n" \
  "v_mfma_f32_16x16x32_f16 v[16:19], v[64:67], v[92:95], v[16:19]\nds_read_b64_tr_b16 v[76:77], v5 offset:2304\n" \
  "ds_read_b64_tr_b16 v[78:79], v5 offset:2336\nv_mfma_f32_16x16x32_f16 v[16:19], v[60:63], v[112:115], v[16:19]\n" \
  "ds_read_b64_tr_b16 v[124:125], v5 offset:2560\nds_read_b64_tr_b16 v[126:127], v5 offset:2592\ns_waitcnt lgkmcnt(12)\n" \
  "v_mfma_f32_16x16x32_f16 v[16:19], v[56:59], v[88:91], v[16:19]\nds_read_b64_tr_b16 v[72:73], v5 offset:2816\n" \
  "ds_read_b64_tr_b16 v[74:75], v5 offset:2848\nv_mfma_f32_16x16x32_f16 v[12:15], v[108:111], v[52:55], v[12:15]\ns_waitcnt lgkmcnt(10)\n" \
  "v_mfma_f32_16x16x32_f16 v[12:15], v[84:87], v[48:51], v[12:15]\nds_read_b64_tr_b16 v[96:97], v7 offset:0\n" \
  "ds_read_b64_tr_b16 v[98:99], v7 offset:64\nv_mfma_f32_16x16x32_f16 v[12:15], v[104:107], v[44:47], v[12:15]\n" \
  "ds_read_b64_tr_b16 v[68:69], v7 offset:512\nds_read_b64_tr_b16 v[70:71], v7 offset:576\n" \
  "v_mfma_f32_16x16x32_f16 v[16:19], v[104:107], v[52:55], v[16:19]\ns_waitcnt lgkmcnt(10)\n" \
  "v_mfma_f32_16x16x32_f16 v[12:15], v[80:83], v[40:43], v[12:15]\nds_read_b64_tr_b16 v[28:29], v7 offset:1024\n" \
  "ds_read_b64_tr_b16 v[30:31], v7 offset:1088\nv_mfma_f32_16x16x32_f16 v[16:19], v[80:83], v[48:51], v[16:19]\n" \
  "ds_read2_b32 v[64:65], v8 offset0:48 offset1:49\nds_read2_b32 v[66:67], v8 offset0:50 offset1:51\n" \
  "v_mfma_f32_16x16x32_f16 v[12:15], v[24:27], v[120:123], v[12:15]\nv_mfma_f32_16x16x32_f16 v[16:19], v[24:27], v[44:47], v[16:19]\n" \
  "s_waitcnt lgkmcnt(10)\nv_mfma_f32_16x16x32_f16 v[12:15], v[76:79], v[116:119], v[12:15]\nds_read_b64_tr_b16 v[92:93], v7 offset:1536\n" \
  "ds_read_b64_tr_b16 v[94:95], v7 offset:1600\nv_mfma_f32_16x16x32_f16 v[16:19], v[76:79], v[40:43], v[16:19]\n" \
  "v_mfma_f32_16x16x32_f16 v[16:19], v[124:127], v[120:123], v[16:19]\ns_waitcnt lgkmcnt(8)\n" \
  "v_mfma_f32_16x16x32_f16 v[16:19], v[72:75], v[116:119], v[16:19]\nv_mfma_f32_16x16x32_f16 v[12:15], v[96:99], v[32:35], v[12:15]\n" \
  "v_mfma_f32_16x16x32_f16 v[16:19], v[96:99], v[36:39], v[16:19]\ns_waitcnt lgkmcnt(4)\n" \
  "v_mfma_f32_16x16x32_f16 v[12:15], v[68:71], v[100:103], v[12:15]\nv_mfma_f32_16x16x32_f16 v[16:19], v[68:71], v[32:35], v[16:19]\n" \
  "s_waitcnt lgkmcnt(2)\nv_mfma_f32_16x16x32_f16 v[12:15], v[28:31], v[64:67], v[12:15]\n" \
  "v_mfma_f32_16x16x32_f16 v[16:19], v[28:31], v[100:103], v[16:19]\ns_waitcnt lgkmcnt(0)\n" \
  "v_mfma_f32_16x16x32_f16 v[16:19], v[92:95], v[64:67], v[16:19]\ns_nop 4\nv_cvt_pkrtz_f16_f32 v20, v12, v13\nv_cvt_pkrtz_f16_f32 v21, v14, v15\n" \
  "v_pk_add_u16 v20, v20, 4 op_sel_hi:[1,0] clamp\nv_pk_add_u16 v21, v21, 4 op_sel_hi:[1,0] clamp\nds_write_b64 v4, v[20:21] offset:6144\n" \
  "v_cvt_pkrtz_f16_f32 v20, v16, v17\nv_cvt_pkrtz_f16_f32 v21, v18, v19\nv_pk_add_u16 v20, v20, 4 op_sel_hi:[1,0] clamp\n" \
  "v_pk_add_u16 v21, v21, 4 op_sel_hi:[1,0] clamp\nds_write_b64 v4, v[20:21] offset:7168\ns_setprio 0\ns_setpc_b64 s[90:91]\n71:\n" \
  "s_movk_i32 s84, 0x4000\n73:\nds_read_b32 v23, v9 offset:32\ns_sub_u32 s84, s84, 1\ns_cbranch_scc1 72b\ns_waitcnt lgkmcnt(0)\n" \
  "v_readfirstlane_b32 s85, v23\ns_cmp_lt_u32 s85, 3\ns_cbranch_scc1 73b\ns_branch 72b\n"
#define CONV_CLOB "memory", "scc", "s84", "s85", "s90", "s91", "v12", "v13", "v14", "v15", "v16", "v17", "v18", "v19", "v20", "v21", "v22", "v23", "v24", "v25", "v26", "v27", "v28", "v29", "v30", "v31", "v32", "v33", "v34", "v35", "v36", "v37", "v38", "v39", "v40", "v41", "v42", "v43", "v44", "v45", "v46", "v47", "v48", "v49", "v50", "v51", "v52", "v53", "v54", "v55", "v56", "v57", "v58", "v59", "v60", "v61", "v62", "v63", "v64", "v65", "v66", "v67", "v68", "v69", "v70", "v71", "v72", "v73", "v74", "v75", "v76", "v77", "v78", "v79", "v80", "v81", "v82", "v83", "v84", "v85", "v86", "v87", "v88", "v89", "v90", "v91", "v92", "v93", "v94", "v95", "v96", "v97", "v98", "v99", "v100", "v101", "v102", "v103", "v104", "v105", "v106", "v107", "v108", "v109", "v110", "v111", "v112", "v113", "v114", "v115", "v116", "v117", "v118", "v119", "v120", "v121", "v122", "v123", "v124", "v125", "v126", "v127"
constexpr int TDP = 1216, T_3 = 4 * TDP, T_4 = T_3 + 512, TB = T_4 + 320;
constexpr int C_U = 480, UB = C_U + 4096 + 96, C_G5 = 0, C_G3 = 3200, C_G9 = 6400, C_G7 = C_G9 + 2096, GB = C_G7 + 2080 + 112;
constexpr int O_FL = 2 * TB, O_UL = O_FL + 64, O_IM = O_UL + 2 * UB, O_ST = O_IM + 2 * GB;
constexpr int X_TB = 10240, X_UL = X_TB + TB, X_IM = X_UL + UB, X_ST = X_IM + GB;
constexpr int X_WR = X_IM + 128 * IP;
#define X_RW(w) (LW + 9216 * (2 + ((w) >> 1)) + 2560 * ((w) & 1))
constexpr int RP = 1216;
constexpr int LW = 36864 + 512, LA = LW, LM = LW + 36864;
constexpr int L_SC = LM + 2048, SMEM = O_IM + 256 * IP;
static_assert(L_SC + 1152 <= SMEM && O_ST + 8192 <= SMEM && X_IM + 128 * IP <= LM && X_ST + 8192 <= LM && UB % 128 == 64 && GB % 128 == 64 && SMEM <= 79872, "");
struct TOps { uint32_t xa, xb, ya, pd, pf, pg, pe; };
__device__ __forceinline__ void table_ops(uint32_t im, uint32_t tb, int ct, int lane, TOps& t) {
  const int j = lane & 15, kq = lane >> 4, q4 = (lane >> 2) & 3, p4 = lane & 3, jj = j + 16 * ct;
  const uint32_t sa = im + (uint32_t)(IP * (lane >> 2));
  t.xa = sa + (uint32_t)(I_X + 8 * p4); t.xb = sa + (uint32_t)(I_X + 32 + 8 * (p4 & 1)); t.ya = sa + (uint32_t)(32 * ct + 8 * p4);
  t.pd = tb + (uint32_t)(2 * (541 - (8 * jj + 4 * (kq & 1) + 256 * (kq >> 1))));
  t.pf = tb + (uint32_t)((kq & 1) ? T_4 + 2 * (63 - jj) : T_3 + 2 * (127 - 2 * jj));
  t.pg = t.pf - ((kq & 1) ? 64 : 2);
  t.pe = tb + (uint32_t)(p4 * (TDP + 2) + ((lane < 12) ? 2 * (32 - q4) : 2 * (572 + (q4 & 1))));
  asm volatile("" : "+v"(t.xa), "+v"(t.xb), "+v"(t.ya), "+v"(t.pd), "+v"(t.pf), "+v"(t.pg), "+v"(t.pe));
}
typedef __attribute__((address_space(3))) char* lptr;
#define LP(T, a) ((__attribute__((address_space(3))) T*)(uintptr_t)(a))
__device__ __forceinline__ uint32_t table_tile(const TOps& t, int ct, int lane, uint32_t evp, float dsk TSP) {
  u32x4 ev;
  f32x4 ad[2], af[2];
  asm volatile(T_ASM : "=&v"(ad[0]), "=&v"(ad[1]), "=&v"(af[0]), "=&v"(af[1]), "=&v"(ev) : "v"(t.xa), "v"(t.xb), "v"(t.ya), "v"(evp) : T_CLOB);
  const int e0 = (int)(ev[0] + ev[2]), e1 = (int)(ev[1] + ev[3]);
  const int em = min(max(max(max(e0, e1), (int)((__float_as_uint(dsk) >> 23) & 0xffu) - 133), -110), 115);
  const int ts = ((uint32_t)(em + 12) <= 8u) ? 1 : -3 - em;
  float s0 = __builtin_amdgcn_ldexpf(1.f, e0 - 29 + ts), s1 = __builtin_amdgcn_ldexpf(1.f, e1 - 29 + ts);
  float d0 = (ct == 0 && lane == 0) ? __builtin_amdgcn_ldexpf(dsk, ts) : 0.f;
  asm volatile("s_nop 1" : "+v"(ad[0]), "+v"(ad[1]), "+v"(af[0]), "+v"(af[1]), "+v"(s0), "+v"(s1), "+v"(d0));
  const f32x4 k = ad[0] * s0 + (f32x4){d0, 0.f, 0.f, 0.f} + ad[1] * s1, f = af[0] * s0 + af[1] * s1;
  auto pk = [](float a, float b) { uint32_t r; asm("v_cvt_pk_f16_f32 %0, %1, %2" : "=v"(r) : "v"(a), "v"(b)); return r; };
  asm volatile("s_nop 0" : : "v"(k), "v"(f));
  const uint32_t p32 = pk(k[3], k[2]), p10 = pk(k[1], k[0]), p21 = pk(k[2], k[1]), p01 = pk(f[0], f[1]), p23 = pk(f[2], f[3]);
  *LP(u32x2, t.pd + 3 * TDP + 6) = (u32x2){p32, p10};
  *LP(uint32_t, t.pd + TDP + 2) = p32; *LP(uint32_t, t.pd + TDP + 6) = p10;
  *LP(uint32_t, t.pd + 2) = p21; *LP(uint32_t, t.pd + 2 * TDP + 6) = p21;
  *LP(u16, t.pd) = (u16)p32; *LP(u16, t.pd + 2 * TDP + 4) = (u16)p32;
  asm volatile("ds_write_b16_d16_hi %0, %1 offset:6\n\tds_write_b16_d16_hi %0, %1 offset:%2" : : "v"(t.pd), "v"(p10), "n"(2 * TDP + 10) : "memory");
  if (ct) {
    const uint32_t va = (uint32_t)__builtin_amdgcn_readlane((int)p32, 63), vb = (uint32_t)__builtin_amdgcn_readlane((int)p23, 15) >> 16;
    *LP(u16, t.pe) = (u16)((lane < 12) ? va : vb);
  }
  if (lane < 32) {
    *LP(u16, t.pf + 128) = (u16)p01; *LP(u16, t.pg + 128) = (u16)(p01 >> 16);
    *LP(u16, t.pf) = (u16)p23; *LP(u16, t.pg) = (u16)(p23 >> 16);
  }
  return (uint32_t)(127 + 5 - ts) << 23;
}
__device__ __forceinline__ uint32_t hadd(uint32_t a, uint32_t b) { return __builtin_bit_cast(uint32_t, __builtin_bit_cast(f16x2, a) + __builtin_bit_cast(f16x2, b)); }
template <bool FULL> __device__ __forceinline__ void sums(char* sm, const u32x4 p, const u32x4 c) {
  const uint32_t D[8] = {p[0], p[1], p[2], p[3], c[0], c[1], c[2], c[3]};
  uint32_t O[7];
#pragma unroll
  for (int j = 0; j < 7; ++j) O[j] = __builtin_amdgcn_alignbit(D[j + 1], D[j], 16);
  u32x4 a, b;
#pragma unroll
  for (int j = 0; j < 4; ++j) a[j] = hadd(D[j + 2], hadd(O[j + 1], O[j + 2]));
  *(u32x4*)(sm + C_G3 + 32) = a;
#pragma unroll
  for (int j = 0; j < 4; ++j) b[j] = hadd(a[j], hadd(D[j + 1], D[j + 3]));
  *(u32x4*)(sm + C_G5 + 32) = b;
  if (FULL) {
#pragma unroll
    for (int j = 0; j < 4; ++j) a[j] = hadd(b[j], hadd(O[j], O[j + 3]));
    *(u32x4*)(sm + C_G7 + 32) = a;
#pragma unroll
    for (int j = 0; j < 4; ++j) b[j] = hadd(a[j], hadd(D[j], D[j + 4]));
    *(u32x4*)(sm + C_G9 + 32) = b;
  }
}
__device__ __forceinline__ void count_up(uint32_t a) {
  uint32_t t;
  asm volatile("s_mov_b64 exec, 1\n\tv_mov_b32 %0, 1\n\tds_add_u32 %1, %0\n\ts_mov_b64 exec, -1" : "=&v"(t) : "v"(a) : "memory");
}
__device__ __forceinline__ int pad_at(int r) { return 16 * (r & 1) + 3200 * (r >> 1) - ((r >= 6) ? 3200 - 2096 : 0); }
struct COps { uint32_t b, a, o, g3, k3, g4, k4, f; };
__device__ __forceinline__ void conv_ops(uint32_t tb, uint32_t ub, uint32_t cb, int lane, COps& e) {
  const uint32_t j = lane & 15, kq = lane >> 4, q4 = (lane >> 2) & 3, p4 = lane & 3;
  e.b = ub + 32u * j + 16u * kq;
  e.a = tb + p4 * TDP + 32u - 8u * q4 + 16u * kq;
  e.g3 = cb + C_G5 + 8 + 64u * kq + 8u * (q4 + p4) + (q4 & 1u) * 3200u;
  e.g4 = cb + C_G9 + 8 + 128u * kq + 16u * q4 + 8u * p4 + (q4 & 1u) * 2096u;
  e.k3 = tb + T_3 + 120 - 8u * j + 16u * kq;
  e.k4 = tb + T_4 + 60 - 4u * j + 16u * kq;
  asm volatile("" : "+v"(e.b), "+v"(e.a), "+v"(e.g3), "+v"(e.g4), "+v"(e.k3), "+v"(e.k4));
}
#define CONV_RUN(prog, e, ...) do { int prog_ = (prog); \
    asm volatile("s_call_b64 s[90:91], kf_conv" : "+{s89}"(prog_), "+{v2}"(e.b), "+{v3}"(e.a), "+{v4}"(e.o), "+{v5}"(e.g3), "+{v6}"(e.k3), "+{v7}"(e.g4), \
                 "+{v8}"(e.k4), "+{v9}"(e.f) __VA_ARGS__ : : CONV_CLOB); } while (0)

template <bool E>
__device__ __forceinline__ void ln_stats(const uint32_t* v, int lane, float sc, f32x2* t, f32x4 z, f32x2& rc, f32x2& nc, float& e) {
  float s1a = z[0], s2a = z[1], s1b = z[2], s2b = z[3];
#pragma unroll
  for (int k = 0; k < 4; ++k) {
    s1a = dot2(v[k], 0x3f803f80u, s1a); s2a = dot2(v[k], v[k], s2a);
    s1b = dot2(v[4 + k], 0x3f803f80u, s1b); s2b = dot2(v[4 + k], v[4 + k], s2b);
  }
#pragma unroll
  for (int k = 0; k < 9; ++k) t[k] = (f32x2){bflo(v[k]), bfhi(v[k])};
  const float rs = row_sum(fold16(dot2(v[8], 0x3f803f80u, fold32(s1a, s1b)), dot2(v[8], v[8], fold32(s2a, s2b))));
  if (E) { e = row_sum(e); e += __int_as_float(__builtin_amdgcn_update_dpp(0, __float_as_int(e), 0x142, 0xa, 0xf, false)); }
  const f32x2 mean = (f32x2){rdl(rs, 15), rdl(rs, 47)} * (1.f / D_), m2 = mean * mean;
  f32x2 var = (f32x2){rdl(rs, 31), rdl(rs, 63)} * (1.f / D_) - m2;
  asm volatile("" : "+v"(t[0]), "+v"(t[1]), "+v"(t[2]), "+v"(t[3]), "+v"(t[4]), "+v"(t[5]), "+v"(t[6]), "+v"(t[7]), "+v"(t[8]));
  if (__builtin_expect(!(m2[0] <= 16.f * var[0] && m2[1] <= 16.f * var[1]), 0)) {
    float q[3] = {0.f, 0.f, 0.f};
#pragma unroll
    for (int k = 0; k < 9; ++k) {
      const f32x2 a = t[k] - ((k < 4 || (k == 8 && lane < 32)) ? mean[0] : mean[1]);
      q[k >> 2] += a[0] * a[0] + a[1] * a[1];
    }
    float qa = q[0] + ((lane < 32) ? q[2] : 0.f), qb = q[1] + ((lane < 32) ? 0.f : q[2]);
    wave_sum2(qa, qb);
    var = (f32x2){qa, qb} * (1.f / D_);
  }
  const uint32_t mp = pack2(mean[0], mean[1]), vp = pack2(var[0], var[1]);
  const f32x2 ve = (f32x2){bflo(vp), bfhi(vp)} + 1e-5f;
  const uint32_t e2 = pack2(ve[0], ve[1]);
  const uint32_t sp = pack2(__builtin_amdgcn_sqrtf(bflo(e2)), __builtin_amdgcn_sqrtf(bfhi(e2)));
  rc = (f32x2){__builtin_amdgcn_rcpf(bflo(sp)), __builtin_amdgcn_rcpf(bfhi(sp))} * sc;
  nc = -(f32x2){bflo(mp), bfhi(mp)} * rc;
}
template <bool H>
__device__ __forceinline__ void ln_apply(const f32x2* t, int lane, f32x2 rc, f32x2 nc, uint32_t* c) {
  const float nm[3] = {nc[0], nc[1], (lane < 32) ? nc[0] : nc[1]}, rr[3] = {rc[0], rc[1], (lane < 32) ? rc[0] : rc[1]};
  f32x2 dd[9];
#pragma unroll
  for (int k = 0; k < 9; ++k) dd[k] = __builtin_elementwise_fma(t[k], (f32x2){rr[k >> 2], rr[k >> 2]}, (f32x2){nm[k >> 2], nm[k >> 2]});
  asm volatile("" : "+v"(dd[0]), "+v"(dd[1]), "+v"(dd[2]), "+v"(dd[3]), "+v"(dd[4]), "+v"(dd[5]), "+v"(dd[6]), "+v"(dd[7]), "+v"(dd[8]));
#pragma unroll
  for (int k = 0; k < 9; ++k) {
    const f32x2 d = dd[k];
    typedef __attribute__((ext_vector_type(2))) uint16_t u16x2;
    if (H) c[k] = __builtin_bit_cast(uint32_t, __builtin_elementwise_add_sat(__builtin_bit_cast(u16x2, __builtin_amdgcn_cvt_pkrtz(d[0], d[1])), (u16x2){4, 4}));
    else c[k] = pack2(d[0], d[1]);
  }
}
__device__ __forceinline__ uint32_t med3u(uint32_t a, uint32_t b, uint32_t c) { return max(min(a, b), min(max(a, b), c)); }
__device__ __forceinline__ uint32_t oct_maxu(uint32_t v) {
  v = max(v, (uint32_t)DPP_I((int)v, 0xb1, 0xf));
  v = max(v, (uint32_t)DPP_I((int)v, 0x4e, 0xf));
  v = max(v, (uint32_t)DPP_I((int)v, 0x141, 0xf));
  return v;
}

#define KARGS const u16* __restrict__ x, const u16* __restrict__ Are, const u16* __restrict__ Aim, const u16* __restrict__ Bre, \
    const u16* __restrict__ Bim, const u16* __restrict__ Cre, const u16* __restrict__ Cim, const u16* __restrict__ logdt, \
    const u16* __restrict__ Dskip, char* __restrict__ ws, const u16* __restrict__ wr, const u16* __restrict__ wg, \
    const u16* __restrict__ wu, const u16* __restrict__ wd, u16* __restrict__ out
#define KPASS x, Are, Aim, Bre, Bim, Cre, Cim, logdt, Dskip, ws, wr, wg, wu, wd, out
template <int ROLE, int FFN = -1>
__device__ __forceinline__ void role_body(char* smem, KARGS) {
  const int tid = threadIdx.x, lane = tid & 63;
  const int w = __builtin_amdgcn_readfirstlane(tid >> 6), wl = w & 3;
  const int b = blockIdx.x & 255, bx = b & 7, bj = b >> 3;
  constexpr bool isl = ROLE == 1 || ROLE == 2;
  const int tg = isl ? 32 * (bj >> 2) + 4 * bx + (bj & 3) : 32 * bx + bj;
  u32x4 xa, xc;
  uint32_t ep, r0;
#define P_WAIT(n, ...) asm volatile("s_waitcnt vmcnt(" #n ")\n\tv_lshlrev_b32 %0, 16, v100\n\tv_lshlrev_b32 %1, 16, v101\n\tv_lshlrev_b32 %2, 16, v102" \
    : "=v"(ldt), "=v"(are), "=v"(aim) : : "v100", "v101", "v102" __VA_ARGS__)
#define P_MORE() asm volatile("v_lshlrev_b32 %0, 16, v103\n\tv_lshlrev_b32 %1, 16, v104\n\tv_lshlrev_b32 %2, 16, v105\n\tv_lshlrev_b32 %3, 16, v106" \
    : "=v"(bre), "=v"(bim), "=v"(cre), "=v"(cim) : : "v103", "v104", "v105", "v106")
  TSD;
  TS(0, 0);
  const uint32_t lb0 = (uint32_t)(uintptr_t)(__attribute__((address_space(3))) char*)smem;
  const int i = lane & 15, kq = lane >> 4;
  const u32x4 ones = {0xffffffffu, 0xffffffffu, 0xffffffffu, 0xffffffffu}, zero4 = {0u, 0u, 0u, 0u};
  auto u_again = [&](u32x4& uw, const u32x4& ut, uint32_t uo) {
#pragma unroll 1
    for (int spin = 0; spin < SPIN_CAP; ++spin) {
      const bool na = ((uw[0] | uw[1] | uw[2] | uw[3]) & UMASK) != 0u;
      if (__builtin_expect(!__any(na), 1)) break;
      TSC(19);
      SLEEP(USLEEP);
      if (na) { LD16W(uw, uo); uw ^= ut; }
    }
  };
  constexpr uint32_t S16 = 0x41800000u, LMASK = 0x00070007u;
  auto v_put = [&](u32x4 v, const u32x4& ut, bool other, uint32_t vo) {
    const bool second = __any(other || ((v[0] | v[1] | v[2] | v[3]) & 0x40004000u) != 0u);
    if (__builtin_expect(second, 0)) v = (v & ~LMASK) | (~ut & LMASK); else v = (v & ~LMASK) | ut;
    ST16(vo, v);
    return second;
  };
  auto v_end = [&](uint32_t T, int n) { DRAIN(); if (lane == 0) ST4(O_DN + 16u * n, T); };
  if constexpr (!isl) {
    const int ch = wl >> 1, tl = tid & 255;
    int* evp = (int*)(smem + O_FL + 16 * ch);
    float ldt, are, aim, dsk;
    if constexpr (ROLE == 0) {
      __builtin_amdgcn_s_setprio(KPRIO);
      COps e;
      conv_ops(lb0 + (uint32_t)(ch * TB), lb0 + (uint32_t)(O_UL + ch * UB), lb0 + (uint32_t)(O_IM + ch * GB), lane, e);
      e.o = lb0 + O_ST + (uint32_t)(16 * ((4 * i + 2 * (kq >> 1) + ch) ^ (i >> 1)) + 8 * (kq & 1));
      e.f = lb0 + O_FL;
      TOps t;
      table_ops(lb0 + (uint32_t)(O_IM + 128 * IP * ch), lb0 + (uint32_t)(TB * ch), wl & 1, lane, t);
      uint32_t pz = lb0 + O_IM + (uint32_t)(((lane >> 3) & 1) * GB + pad_at(lane & 7)), im = lb0 + (uint32_t)(O_IM + IP * tl);
      asm volatile("" : "+v"(e.o), "+v"(e.f), "+v"(pz), "+v"(im));
      float bre, bim, cre, cim;
      P_WAIT(0, , "v103", "v104", "v105", "v106", "v107", "v109");
      asm volatile("" : "={v109}"(ep));
      const uint32_t T = ((uint32_t)__builtin_amdgcn_readfirstlane(ep) + 1u) * 0x9E3779B1u;
      if (w == 3 && lane < 8) ST16(O_KEY + 128u * b + 16u * lane, ones);
      P_MORE();
      asm volatile("v_lshlrev_b32 %0, 16, v107" : "=v"(dsk) : : "v107");
      TS(0, 1);
      evp[wl & 1] = gen_y(ldt, are, aim, bre, bim, cre, cim, im, [] {} TSA);
      TS(0, 8);
      BAR();
      TS(0, 2);
      const uint32_t cv = table_tile(t, wl & 1, lane, lb0 + O_FL + 16 * ch, dsk TSA), cs = (uint32_t)__builtin_amdgcn_readfirstlane(cv);
      *(uint32_t*)(smem + O_FL + 40 + 4 * ch) = cv;
      TS(0, 9);
      gsync0(lb0 + O_FL + 36, 4);
      TS(0, 3);
      *(u32x4*)(smem + O_ST + 16 * tl) = ones;
      *(u32x4*)(smem + O_ST + 4096 + 16 * tl) = ones;
      if (w == 0) *LP(u32x4, pz) = zero4;
      DRAIN();
      CONV_RUN(wl & 1, e);
      if (wl & 1) asm volatile("s_mov_b64 exec, 1\n\tglobal_store_dwordx2 %0, %1, %2 sc1" : : "v"(O_SC + 16u * (2 * tg + ch)), "v"((u32x2){T, cs}), "s"(ws) : "memory");
      TS(0, 5);
    } else {
      __builtin_amdgcn_s_setprio(UPRIO);
#pragma unroll
      for (int k = 0; k < 3; ++k) *(u32x4*)(smem + ((16 * (tl + 256 * k) < O_FL) ? 16 * (tl + 256 * k) : O_FL + 32)) = zero4;
      if (w == 7) *(u32x4*)(smem + O_UL + (lane >> 5) * UB + 16 * min(lane & 31, 29)) = zero4;
      uint32_t uo = u_at(tl >> 1, 2 * tg + (tid & 1)), go = O_SU + (uint32_t)(256 * bx + 32 * wl + (lane & 31) + 4 * (lane & 32)) * 128u;
      uint32_t ul = (uint32_t)(O_UL + (tid & 1) * UB + C_U - 16 + 16 * (tl >> 1)), ug = (uint32_t)(O_IM + (tid & 1) * GB + 16 * (tl >> 1)), so = lb0 + O_ST + 16u * (uint32_t)(lane ^ (lane >> 3));
      uint32_t vo = O_V + (uint32_t)tg * VP + 16u * lane;
      asm volatile("" : "+v"(uo), "+v"(go), "+v"(ul), "+v"(ug), "+v"(so), "+v"(vo));
      P_WAIT(1, , "v109");
      TS(0, 1);
      evp[2 + (wl & 1)] = gen_x(ldt, are, aim, smem + O_IM + IP * tl);
      asm volatile("s_waitcnt vmcnt(0)" : "={v109}"(ep));
      const uint32_t T = ((uint32_t)__builtin_amdgcn_readfirstlane(ep) + 1u) * 0x9E3779B1u;
      TS(0, 8);
      BAR();
      TS(0, 2);
      u32x4 ut = {utag(T, 0), utag(T, 1), utag(T, 2), utag(T, 3)};
      asm volatile("" : "+v"(ut));
      SLEEP(GSLEEP);
      u32x2 pv;
      pv[1] = gate(ws, go, T);
      TS(0, 3);
      until(latest(pv[1]) + UD);
      TS(0, 7);
      u32x4 uw, ux;
      asm volatile("global_load_dwordx4 %0, %2, %4 sc1\n\tglobal_load_dwordx4 %1, %3, %4 sc1"
                   : "=&v"(uw), "=&v"(ux) : "v"(uo), "v"(uo + 64 * UP * 32), "s"(ws) : "memory");
      __builtin_amdgcn_s_setprio(FPR);
      asm volatile("s_waitcnt vmcnt(1)" : "+v"(uw));
      uw ^= ut;
      *(u32x4*)(smem + ul + 16) = uw;
      asm volatile("s_waitcnt vmcnt(0)" : "+v"(ux));
      TS(0, 11);
      ux ^= ut;
      *(u32x4*)(smem + ul + 2048 + 16) = ux;
      if (__builtin_expect(__any(((uw[0] | uw[1] | uw[2] | uw[3] | ux[0] | ux[1] | ux[2] | ux[3]) & UMASK) != 0u), 0)) {
        u_again(uw, ut, uo);
        u_again(ux, ut, uo + 64 * UP * 32);
        *(u32x4*)(smem + ul + 16) = uw;
        *(u32x4*)(smem + ul + 2048 + 16) = ux;
      }
      BAR();
      TS(0, 4);
      const u32x4 pw = *(const u32x4*)(smem + ul);
      if (wl < 2) {
        const u32x4 px = *(const u32x4*)(smem + ul + 2048);
        sums<true>(smem + ug, pw, uw);
        sums<false>(smem + ug + 2048, px, ux);
      } else sums<true>(smem + ug, pw, uw);
      count_up(lb0 + O_FL + 32);
      __builtin_amdgcn_s_setprio(SPR);
      TS(0, 5);
      const bool other = *(const uint32_t*)(smem + O_FL + 40 + 4 * (lane & 1)) != S16;
      bool second = false;
#pragma unroll
      for (int k = 0; k < 2; ++k) {
        const uint32_t o = ((k ? 0x3247u : 0x1065u) >> (4 * wl) & 7u) * 1024u;
        u32x4 v;
#pragma unroll 1
        for (int spin = 0; spin < SPIN_CAP; ++spin) {
          asm volatile("ds_read_b128 %0, %1\n\ts_waitcnt lgkmcnt(0)" : "=&v"(v) : "v"(so + o) : "memory");
          if (!__any(max4(v) == 0xffffffffu)) break;
          __builtin_amdgcn_s_sleep(VPS);
        }
        TS(0, 12 + k);
        second |= v_put(v, ut, other, vo + o);
        TS(0, 10 - 4 * k);
      }
      if (__builtin_expect(second, 0)) v_end(T, 4 * tg + wl);
    }
    TSE();
  } else {
  uint32_t* cand = (uint32_t*)(smem + LM);
  uint32_t* sel = cand + 128;
  uint32_t* kl = sel + 48;
  uint32_t* cnt = sel + 64;
  const uint32_t ca = lb0 + LM + 4 * (128 + 64);
  u16* hm = (u16*)(cnt + 12);
  uint32_t T;
  const bool ffn = (FFN < 0) ? bj < 18 : FFN, isx = FFN != 1 && bj >= 24;
  const int fb = 18 * bx + bj;
  constexpr bool ln2 = ROLE == 2;
  const int g0 = tg, g2 = tg;
  u32x4 bw[18];
#pragma unroll
  for (int s = 0; s < 9; ++s) bw[s] = (u32x4){0u, 0u, 0u, 0u};
  int tid2 = threadIdx.x;
  uint32_t ts1 = 0u;
  auto rest = [&] {
    if (ts1 && (int)((uint32_t)__builtin_amdgcn_s_memrealtime() - ts1) > 0) { TS(0, 8); until(ts1 + TSTOP + RD); if (RW) gwait(ca, 4); ts1 = 0u; }
  };
  auto gu_dma = [&](int q0, int q1, auto depth) {
    const char* gs = (const char*)((w < 2) ? wg : wu) + (size_t)((144 * (w & 1)) * (F_ * 2) + fb * 64);
    const uint32_t gv = (uint32_t)((lane >> 2) * (F_ * 2) + 16 * ((lane & 3) ^ (lane >> 5) ^ (w & 2)));
#pragma unroll 1
    for (int q = q0; q < q1; ++q) {
      const int k = (int)((((bj & 1) ? ORD1 : ((unsigned __int128)0x876543210u << 36 | 0x876543210ull)) >> (4 * (q - (q >= 9 ? 9 : 0)) + (q >= 9 ? 36 : 0))) & 15u), h = q >= 9;
      if (q >= RQ) rest();
      dma16(gs + (size_t)(288 * h + 16 * k) * (F_ * 2), gv, lb0 + LW * h + 9216 * w + 1024 * k);
      asm volatile("s_waitcnt vmcnt(%0)" : : "n"(depth.value) : "memory");
    }
  };
  auto down_dma = [&](int k0, int k1, int lane) {
#pragma unroll 1
    for (int k = k0; k < k1; ++k) {
      const int o = 9216 * wl + 2304 * k;
      const char* ds = (const char*)wd + (size_t)(fb * 36864 + o);
      const uint32_t dl = lb0 + o + 32 * ((4 * wl + k + 1) >> 1);
      if (ROLE == 1) rest();
      dma16d(ds, 16u * lane, dl);
      dma16d(ds + 1024, 16u * lane, dl + 1024);
      uint32_t keep;
      asm volatile("s_mov_b32 %0, m0\n\ts_mov_b32 m0, %3\n\ts_mov_b64 exec, 0xffff\n\tglobal_load_lds_dwordx4 %1, %2" DPOL2 "\n\ts_mov_b64 exec, -1\n\ts_mov_b32 m0, %0\n\ts_waitcnt vmcnt(%4)"
                   : "=&s"(keep) : "v"(16u * lane), "s"(ds + 2048), "s"(dl + 2048), "n"(ROLE == 1 ? DDEPTH : 40) : "memory");
    }
  };
  auto gu_read = [&](int h) {
    const uint32_t ra = lb0 + LW * h + (uint32_t)(((lane >> 1) & 1) * 18432 + (8 * kq + ((lane >> 2) & 3)) * 64 + ((w ^ (kq & 1) ^ (lane & 2)) * 16) + 8 * (lane & 1));
#pragma unroll
    for (int s = 0; s < 9; ++s) {
      const u32x2 h0 = trd(ra + 2048 * s), h1 = trd(ra + 2048 * s + 256);
      bw[9 * h + s] = (u32x4){h0[0], h0[1], h1[0], h1[1]};
    }
#pragma unroll
    for (int s = 0; s < 9; ++s) asm volatile("" : "+v"(bw[9 * h + s]));
  };
  uint32_t tv = 0u, tk = 0u;
  const uint32_t vearly = (uint32_t)((VEARLY >> (8 * (g0 >> 5))) & 0xffull);
#define TOUCH(pa) asm volatile("global_load_dword v126, %0, off" DPOL4 : : "v"(pa) : "memory", "v126")
  uint32_t wv[5], xv[9];
  auto rows_xw = [&](int lane) {
    u32x4 wa, xr, xs;
    const uint32_t xo = (uint32_t)((8 * g2 + 2 * wl) * (D_ * 2));
    asm volatile("global_load_dwordx4 %0, %5, %9\n\tglobal_load_dword %1, %6, %9 offset:1024\n\t"
                 "global_load_dwordx4 %2, %7, %10\n\tglobal_load_dwordx4 %3, %7, %10 offset:1152\n\tglobal_load_dword %4, %8, %10 offset:1024\n\ts_waitcnt vmcnt(0)"
                 : "=&v"(wa), "=&v"(wv[4]), "=&v"(xr), "=&v"(xs), "=&v"(xv[8])
                 : "v"(16u * lane), "v"(4u * (lane & 31)), "v"(xo + 16u * lane), "v"(xo + 4u * lane + 32u * (lane & 32)), "s"(wr), "s"(x) : "memory");
#pragma unroll
    for (int k = 0; k < 4; ++k) { wv[k] = wa[k]; xv[k] = xr[k]; xv[4 + k] = xs[k]; }
  };
  auto ln1 = [&](int g, int o, uint32_t c0) {
    uint32_t uo = u_at(g, 64 * wl + lane);
    uint32_t ta = lb0 + (uint32_t)(o + ((lane >> 2) & 3) * RP + (64 * wl + 16 * kq + 4 * (lane & 3)) * 2);
    uint32_t la = (uint32_t)(o + 2 * wl * RP + 16 * lane), lt = (uint32_t)(o + (2 * wl + (lane >> 5)) * RP + 1024 + 4 * (lane & 31));
    uint32_t sg = O_SU + (uint32_t)(256 * (lane >> 3) + g) * 128u + 16u * (lane & 7);
    asm volatile("" : "+v"(uo), "+v"(ta), "+v"(la), "+v"(lt), "+v"(sg));
    f32x4 z4 = {0.f, 0.f, 0.f, 0.f};
    asm volatile("" : "+v"(z4));
    asm volatile("s_waitcnt vmcnt(0)" : "={v[100:103]}"(xa), "={v[104:107]}"(xc), "={v108}"(r0), "={v109}"(ep));
    TS(0, 1);
    const uint32_t v[9] = {xa[0], xa[1], xa[2], xa[3], xc[0], xc[1], xc[2], xc[3], r0};
    uint32_t c[9];
    f32x2 tf[9];
    T = ((uint32_t)__builtin_amdgcn_readfirstlane(ep) + 1u) * 0x9E3779B1u;
    if (wl == 3) { const uint32_t now = (uint32_t)__builtin_amdgcn_s_memrealtime() + SD; ST16(sg, ((u32x4){T, now, T, now})); }
    const uint32_t tj = ((T >> (3 * wl)) & 0x00070007u) | (((T >> (12 + wl)) & 0x00010001u) << 14);
    uint32_t tw[3] = {(tj & 0xffffu) * 0x10001u, (tj >> 16) * 0x10001u, 0u};
    tw[2] = (lane < 32) ? tw[0] : tw[1];
    asm volatile("" : "+v"(tw[0]), "+v"(tw[1]), "+v"(tw[2]));
    f32x2 rc, nc;
    float e_;
    ln_stats<false>(v, lane, 0.03125f, tf, z4, rc, nc, e_);
    ln_apply<true>(tf, lane, rc, nc, c);
#pragma unroll
    for (int k = 0; k < 9; ++k) c[k] = (c[k] & ~UMASK) | tw[k >> 2];
    *(u32x4*)(smem + la) = (u32x4){c[0], c[1], c[2], c[3]};
    *(u32x4*)(smem + la + RP) = (u32x4){c[4], c[5], c[6], c[7]};
    *(uint32_t*)(smem + lt) = c[8];
    u32x4 ha, hb;
    {
      uint32_t t, cc, n = SPIN_CAP;
      asm volatile("s_mov_b64 exec, 1\n\tv_mov_b32 %2, 1\n\tds_add_u32 %5, %2\n\ts_mov_b64 exec, -1\n"
                   "1:\n\tds_read_b32 %2, %5\n\tds_read_b64_tr_b16 v[40:41], %6\n\tds_read_b64_tr_b16 v[42:43], %6 offset:%7\n\tds_read_b64_tr_b16 v[44:45], %6 offset:512\n\t"
                   "ds_read_b64_tr_b16 v[46:47], %6 offset:%8\n\ts_sub_u32 %4, %4, 1\n\ts_cbranch_scc1 2f\n\ts_waitcnt lgkmcnt(4)\n\tv_readfirstlane_b32 %3, %2\n\t"
                   "s_cmp_lt_u32 %3, 4\n\ts_cbranch_scc1 1b\n2:\n\ts_waitcnt lgkmcnt(0)"
                   : "=&{v[40:43]}"(ha), "=&{v[44:47]}"(hb), "=&v"(t), "=&s"(cc), "+s"(n) : "v"(c0), "v"(ta), "n"(4 * RP), "n"(512 + 4 * RP) : "memory", "scc");
    }
    ST16(uo, ha);
    ST16(uo + 64 * 128, hb);
    if (wl == 0) {
      const u32x2 h4 = trd(ta + 1024), h5 = trd(ta + 1024 + 4 * RP);
      ST16(uo + 128 * 128, ((u32x4){h4[0], h4[1], h5[0], h5[1]}));
    }
  };
  const int dX = 512 - 24 + 8 * bx + bj;
  if constexpr (ROLE == 2) {
    const int tl = tid - 256;
    if (isx) {
      __builtin_amdgcn_s_setprio(QPRIO0);
      *(u32x4*)(smem + X_TB + 16 * tl) = zero4;
      *(u32x4*)(smem + X_TB + 16 * min(256 + tl, (TB + C_U) / 16 - 1)) = zero4;
      const int pr = (wl + XROT) & 3;
      COps e;
      conv_ops(lb0 + X_TB, lb0 + X_UL, lb0 + X_IM, lane, e);
      TOps t;
      table_ops(lb0 + X_IM, lb0 + X_TB, wl & 1, lane, t);
      e.o = lb0 + X_ST + (uint32_t)(16 * ((2 * i + (kq >> 1)) ^ ((i >> 2) & 1)) + 8 * (kq & 1));
      e.f = ca + 28 - 32;
      asm volatile("" : "+v"(e.o), "+v"(e.f));
      float ldt, are, aim, bre, bim, cre, cim, dsk;
      P_WAIT(0, , "v103", "v104", "v105", "v106", "v107", "v109");
      asm volatile("" : "={v109}"(ep));
      T = ((uint32_t)__builtin_amdgcn_readfirstlane(ep) + 1u) * 0x9E3779B1u;
      P_MORE();
      asm volatile("v_lshlrev_b32 %0, 16, v107" : "=v"(dsk) : : "v107");
      TS(0, 1);
      __builtin_amdgcn_s_setprio(QPRIO);
      int* evp = (int*)(sel + 44);
      char* im = smem + X_IM + IP * (tl & 127);
      evp[wl] = (wl < 2) ? gen_y(ldt, are, aim, bre, bim, cre, cim, lb0 + X_IM + IP * tl, [] {} TSA) : gen_x(ldt, are, aim, im);
      TS(0, 2);
      __builtin_amdgcn_s_setprio(QPRIO2);
      gsync(ca + 8, 4);
      if (wl < 2) evp[-4 + wl] = (int)table_tile(t, wl & 1, lane, lb0 + LM + 4 * (128 + 44), dsk TSA);
      if (wl < 2) gsync0(ca + 8, 8); else { count_up(ca + 8); SLEEP(XSL); gwait(ca + 8, 8); }
      const uint32_t cs = (uint32_t)__builtin_amdgcn_readfirstlane(evp[-4]);
      if (wl == 3) *(u32x4*)(smem + X_IM + pad_at(lane & 7)) = zero4;
      TS(0, 3);
      CONV_RUN(2 + pr, e, , "+{v11}"(tid2));
      __builtin_amdgcn_s_setprio(3);
      {
        const int lane = tid2 & 63;
        const u32x4 v = *(const u32x4*)(smem + X_ST + 2048 * pr + 1024 * (lane >> 5) + 16 * ((lane & 31) ^ ((lane >> 3) & 1)));
        if (__builtin_expect(v_put(v, (u32x4){utag(T, 0), utag(T, 1), utag(T, 2), utag(T, 3)}, cs != S16, v_at(dX, 64 * pr + lane)), 0)) {
          DRAIN();
          if (lane == 0) ST4(O_DN + 16u * (4 * (dX - 256) + pr), T);
        }
        if (pr == 0 && lane == 0) asm volatile("global_store_dwordx2 %0, %1, %2 sc1" : : "v"(O_SC + 16u * dX), "v"((u32x2){T, cs}), "s"(ws) : "memory");
      }
      TS(0, 6);
    }
    if (!isx) __builtin_amdgcn_s_setprio(PPRIO);
    asm volatile("" : "+v"(tid2));
    if (!isx) rows_xw(tid2 & 63);
    if (!isx && !ffn) {
      SLEEP(RSLEEP);
#pragma unroll
      for (int k = 0; k < 7; ++k) ST16(O_SL + (uint32_t)((6 * bx + bj - 18) * 1792 + 256 * k - 256 + tid2) * 16u, ones);
      DRAIN();
    }
    if (!isx) BAR();
    const uint32_t tp = (uint32_t)__builtin_amdgcn_readfirstlane(cnt[4]);
    if (!isx) T = (uint32_t)__builtin_amdgcn_readfirstlane(sel[43]);
    tv = tp + UD + VD2 - vearly;
  } else {
    __builtin_amdgcn_s_setprio(3);
    if (tid < 12) cnt[tid] = 0u;
    BAR();
    if (tid < 20) ((uint32_t*)(hm + 320))[tid] = 0u;
    if (tid >= 184) *(u32x4*)(smem + L_SC - 16 * 184 + 16 * tid) = (u32x4){0x41804180u, 0x41804180u, 0x41804180u, 0x41804180u};
    uint32_t sink = 0;
    ln1(tg, 0, ca + 12);
    TS(0, 2);
    __builtin_amdgcn_s_setprio(NPRIO);
    asm volatile("s_waitcnt vmcnt(0)" : "+v"(sink) : : "memory");
    TS(0, 13);
    if (w == 0) {
      const uint32_t xr = O_XC + (uint32_t)((511 - 2 * (g0 + w)) * 144 + lane) * 16u;
      ST16(xr, ones);
      ST16(xr + 1024u, ones);
      if (lane < 16) ST16(xr + 2048u, ones);
    }
    if (w == 2 && b < 72 && lane < 8) ST16(O_FB + (uint32_t)(8 * b + lane) * 16u, ones);
    if (isx) {
      rows_xw(lane);
      char* sp = smem + X_RW(w) + 16 * lane;
      *(u32x4*)sp = (u32x4){xv[0], xv[1], xv[2], xv[3]};
      *(u32x4*)(sp + 1024) = (u32x4){xv[4], xv[5], xv[6], xv[7]};
      *(u32x2*)(smem + X_RW(w) + 2048 + 8 * lane) = (u32x2){xv[8], wv[4]};
      if (w == 0) *(u32x4*)(smem + X_WR + 16 * lane) = (u32x4){wv[0], wv[1], wv[2], wv[3]};
    }
    SLEEP(ESLEEP);
    uint32_t tu = latest(gate(ws, O_SU + (uint32_t)(256 * bx + (ALLP ? tid : 64 * w + 4 * i + (bj & 3))) * 128u, T));
    asm volatile("s_mov_b64 exec, 1\n\tds_max_u32 %0, %1\n\ts_mov_b64 exec, -1" : : "v"(ca + 16), "v"(tu) : "memory");
    if (w == 0) sel[43] = T;
    if (isx) {
      const uint32_t xo = u_at(tid, dX);
      char* sm = smem + X_UL + C_U - 16 + 16 * tid;
      const u32x4 ut = {utag(T, 0), utag(T, 1), utag(T, 2), utag(T, 3)};
      __builtin_amdgcn_s_setprio(3);
      until(tu + UD + XD);
      u32x4 uw;
      asm volatile("global_load_dwordx4 %0, %1, %2 sc1" : "=&v"(uw) : "v"(xo), "s"(ws) : "memory");
      asm volatile("s_waitcnt vmcnt(0)" : "+v"(uw));
      uw ^= ut;
      u_again(uw, ut, xo);
      *(u32x4*)(sm + 16) = uw;
      BAR();
      if (w < 3) {
        const u32x4 pw = *(const u32x4*)sm;
        if (w < 2) sums<true>(smem + X_IM + 16 * tid, pw, uw); else sums<false>(smem + X_IM + 16 * tid, pw, uw);
        count_up(ca + 28);
      }
    } else {
      until(tu + UD + WD);
      BAR();
    }
    tu = (uint32_t)__builtin_amdgcn_readfirstlane(cnt[4]);
    TS(0, 3);
    __builtin_amdgcn_s_setprio(DPRIO);
    if (ffn) {
      ts1 = (tu + UD + VD2 - TSTOP) | 1u;
      gu_dma(0, N0, IC<40>{});
      gu_dma(N0, NA, IC<WDEPTH>{});
      asm volatile("s_waitcnt vmcnt(%0)" : : "n"(NA - 9) : "memory");
      gsync0(ca + 4, 4);
      gu_read(0);
      asm volatile("s_waitcnt lgkmcnt(0)" ::: "memory");
      count_up(ca + 36);
      TS(0, 4);
      gu_dma(NA, 18, IC<WDEPTH2>{});
      gwait(ca + 36, 4);
      down_dma(0, KN, lane);
      asm volatile("s_waitcnt vmcnt(%0)" : : "n"(3 * KN) : "memory");
      count_up(ca + 4);
      TS(0, 7);
    } else {
      if (!isx && C3) {
        const int n = 6 * min(tid, 191) + bj - 18, r = n >> 2;
        const char* wb = (const char*)((n & 1) ? wu : wg) + bx * 1152;
        const int c0 = (3 - (int)((uintptr_t)wb >> 7)) & 7;
        wb += (uint32_t)((r + 144 * ((r >= 144) + ((n >> 1) & 1))) * (F_ * 2) + 128 * c0);
        TOUCH(wb);
        if (c0 == 0) TOUCH(wb + 1024);
      }
      auto d_line = [&](int k) {
        const int n = min((k * 14 + bj - 18) * 64 + lane, 5183), q = n / 1296, r = n - 1296 * q;
        return (const char*)wd + (uint32_t)(bx * 5184 + (r / 18) * 72 + 18 * (HREV ? 3 - q : q) + r % 18) * 128u;
      };
      int k = w;
      if (w < HW) {
#pragma unroll 1
        for (; k < 6 && (int)((uint32_t)__builtin_amdgcn_s_memrealtime() - (tu + UD + VD2 - HSTOP)) < 0; k += HW) { TOUCH(d_line(k)); DRAIN(); }
      }
      TS(0, 4);
      until(tu + UD + VD2 + RD2);
      if (RW) gwait(ca, 4);
      if (w < HW) {
#pragma unroll 1
        for (; k < 6; k += HW) { TOUCH(d_line(k)); DRAIN(); }
      }
      TS(0, 7);
    }
    tv = tu + UD + VD2 - vearly;
    tk = tu + UD + VD2 + KD;
  }
  {
  const int tid = tid2, lane = tid & 63, i = lane & 15, kq = lane >> 4;
  __builtin_amdgcn_s_setprio(3);
  const int PL = ffn ? 6400 : LW + 6656;
  f32x2 xf[9];
  f32x4 za = {0.f, 0.f, 0.f, 0.f}, zb = za;
  uint32_t al, ha, h0a, h1a, so, da;
  auto ffn_terms = [&] {
    al = O_XC + 64u * (i >> 3) + 16u * kq + 128u * w;
    ha = (ROLE == 1) ? (uint32_t)((4 * (kq & 1) + 2 * (kq >> 1) + (i >> 3)) * 80 + 16 * w + 2 * (i & 7)) : 0u;
    h0a = (uint32_t)(min(i, 8) * 80 + 16 * kq); h1a = (uint32_t)(min(i ^ 8, 8) * 80 + 16 * kq);
    so = O_SL + (uint32_t)fb * SP + ((ROLE == 2) ? 16u * tid - 4096u : 8192u + 16u * lane);
    da = lb0 + (uint32_t)((8 * kq + ((lane >> 2) & 3)) * 1152 + 32 * ((8 * kq + ((lane >> 2) & 3) + 2) >> 2) + 8 * (lane & 3) + ((ROLE == 2) ? 128 * wl : 1024));
    if (ROLE == 2) asm volatile("" : "+v"(h0a), "+v"(h1a), "+v"(so), "+v"(da)); else asm volatile("" : "+v"(ha), "+v"(al), "+v"(da));
  };
  if (ROLE == 1) ffn_terms();
  uint32_t hv[9] = {}, nv[9] = {}, keya = 0u, keyb = 0u;
  int rka = 0, rkb = 0;
  auto put_row = [&](const uint32_t* r, int j, const void* base, uint32_t ro) {
    const u32x4 tl = {r[8], (uint32_t)DPP_I((int)r[8], 0x55, 0xf), (uint32_t)DPP_I((int)r[8], 0xaa, 0xf), (uint32_t)DPP_I((int)r[8], 0xff, 0xf)};
    asm volatile("global_store_dwordx4 %0, %1, %2 sc1\n\ts_nop 1" : : "v"(ro + 16u * lane), "v"((u32x4){r[4 * j], r[4 * j + 1], r[4 * j + 2], r[4 * j + 3]}), "s"(base) : "memory");
    if ((lane & 35) == 32 * j) asm volatile("global_store_dwordx4 %0, %1, %2 offset:1024 sc1\n\ts_nop 1" : : "v"(ro + 4u * (lane & 31)), "v"(tl), "s"(base) : "memory");
  };
  if (ln2) {
    const int tl = tid - 256, d1 = 64 * (tl >> 5) + (tl & 31), d3 = 64 * ((tl >> 4) & 7) + 32 + (tl & 15), d2 = (tl < 128) ? d3 + 16 : min(384 + tl, 575);
    uint32_t vo = v_at(d1, g0), vo2 = v_at(d2, g0), vo3 = v_at(d3, g0);
    u32x4 ut = {utag(T, 0), utag(T, 1), utag(T, 2), utag(T, 3)};
    u32x4 va, vc = ut, vb = ut;
    asm volatile("" : "+v"(vo), "+v"(vo2), "+v"(vo3), "+v"(ut), "+v"(vc), "+v"(vb));
    TS(0, 7);
    until(tv);
    TS(0, 8);
    asm volatile("global_load_dwordx4 %0, %2, %4 sc1\n\ts_cmp_lt_u32 %5, 2\n\ts_cbranch_scc1 1f\n\tglobal_load_dwordx4 %1, %3, %4 sc1\n1:"
                 : "=&v"(va), "+v"(vc) : "v"(vo), "v"(vo3), "s"(ws), "s"(wl) : "memory", "scc");
    until(tv + V2D);
    asm volatile("s_cmp_eq_u32 %3, 3\n\ts_cbranch_scc1 1f\n\tglobal_load_dwordx4 %0, %1, %2 sc1\n1:" : "+v"(vb) : "v"(vo2), "s"(ws), "s"(wl) : "memory", "scc");
    __builtin_amdgcn_sched_barrier(0);
    if (isx) {
      const u32x4 ra = *(const u32x4*)(smem + X_RW(wl) + 16 * lane), rb = *(const u32x4*)(smem + X_RW(wl) + 1024 + 16 * lane), rw = *(const u32x4*)(smem + X_WR + 16 * lane);
      const u32x2 rc = *(const u32x2*)(smem + X_RW(wl) + 2048 + 8 * lane);
#pragma unroll
      for (int k = 0; k < 4; ++k) { xv[k] = ra[k]; xv[4 + k] = rb[k]; wv[k] = rw[k]; }
      xv[8] = rc[0]; wv[4] = rc[1];
    }
    uint32_t pa = (uint32_t)(PL + 9216 * wl + 32 * lane), pb = (uint32_t)(PL + 9216 * wl + 2048 + 8 * (lane & 31)), ps = (lane < 32) ? 0u : 16u;
    uint32_t kid = (uint32_t)(((255 - g2) << 5) | (7 - 2 * wl - (lane >> 5)));
    uint32_t px = (uint32_t)(PL + 4 * d1), py = (uint32_t)(PL + 4 * d2), pz = (uint32_t)(PL + 4 * d3);
#pragma unroll
    for (int k = 0; k < 9; ++k) xf[k] = (f32x2){bflo(xv[k]), bfhi(xv[k])};
    asm volatile("" : "+v"(xf[0]), "+v"(xf[1]), "+v"(xf[2]), "+v"(xf[3]), "+v"(xf[4]), "+v"(xf[5]), "+v"(xf[6]), "+v"(xf[7]), "+v"(xf[8]));
    asm volatile("" : "+v"(za), "+v"(zb), "+v"(pa), "+v"(pb), "+v"(ps), "+v"(kid), "+v"(px), "+v"(py), "+v"(pz));
    float sv[10], ws_ = dot2(wv[4], 0x3f803f80u, 0.f) * 0.5f;
#pragma unroll
    for (int k = 0; k < 4; ++k) ws_ = dot2(wv[k], 0x3f803f80u, ws_);
    ws_ = wave_sum(ws_);
#pragma unroll
    for (int k = 0; k < 10; ++k) sv[k] = 16.f;
    uint32_t ka_ = lb0 + LM + 4u * (128 + 48 + 2 * wl + (lane >> 5)), kb_ = lb0 + LM + 4u * (128 + 48);
    uint32_t ko_ = O_KEY + (uint32_t)(256 * (lane & 7) + 8 * (g2 & 31) + (g2 >> 5)) * 16u;
    asm volatile("" : "+v"(ws_), "+v"(sv[0]), "+v"(sv[1]), "+v"(sv[2]), "+v"(sv[3]), "+v"(sv[4]), "+v"(sv[5]), "+v"(sv[6]), "+v"(sv[7]), "+v"(sv[8]), "+v"(sv[9]),
                 "+v"(ka_), "+v"(kb_), "+v"(ko_));
    if (ffn) gwait(ca + 36, 4);
    asm volatile("s_waitcnt vmcnt(1)\n\ts_cmp_eq_u32 %2, 3\n\ts_cbranch_scc0 1f\n\ts_waitcnt vmcnt(0)\n1:" : "+v"(va), "+v"(vc) : "s"(wl) : "memory", "scc");
    auto turn1 = [&] {
#pragma unroll
      for (int e = 0; e < 4; ++e) *(uint32_t*)(smem + px + 9216 * e) = va[e];
      if (wl >= 2) {
#pragma unroll
        for (int e = 0; e < 4; ++e) *(uint32_t*)(smem + pz + 9216 * e) = vc[e];
      }
    };
    auto turn = [&] {
      if (wl < 3) {
#pragma unroll
        for (int e = 0; e < 4; ++e) *(uint32_t*)(smem + py + 9216 * e) = vb[e];
      }
    };
    va ^= ut; vc ^= ut;
    turn1();
    asm volatile("s_waitcnt vmcnt(0)" : "+v"(vb) : : "memory");
    vb ^= ut;
    turn();
    const u32x4 vs = va | vb | vc;
    if (__builtin_expect(__any(((vs[0] | vs[1] | vs[2] | vs[3]) & UMASK) != 0u), 0)) {
#pragma unroll 1
      for (int spin = 0; spin < 4; ++spin) {
        const bool na = ((va[0] | va[1] | va[2] | va[3]) & UMASK) != 0u, nb = ((vb[0] | vb[1] | vb[2] | vb[3]) & UMASK) != 0u, nc = ((vc[0] | vc[1] | vc[2] | vc[3]) & UMASK) != 0u;
        if (!__any(na || nb || nc)) break;
        TSC(16);
        if (na) LD16(va, vo);
        if (nb) LD16(vb, vo2);
        if (nc) LD16(vc, vo3);
        asm volatile("s_waitcnt vmcnt(0)" : "+v"(va), "+v"(vb), "+v"(vc) : : "memory");
        if (na) va ^= ut;
        if (nb) vb ^= ut;
        if (nc) vc ^= ut;
      }
      auto again = [&](u32x4& v, uint32_t o, int d) {
        const int J = g0 >> 5;
        const uint32_t dn = O_DN + 16u * (uint32_t)((d < 512) ? 4 * (d >> 1) + ((0x01013232 >> (4 * J)) & 3) : 4 * (d - 256) + (J >> 1));
        bool pend = ((v[0] | v[1] | v[2] | v[3]) & UMASK) != 0u;
#pragma unroll 1
        for (int spin = 0; spin < SPIN_CAP && __any(pend); ++spin) {
          TSC(16);
          SLEEP(PSLEEP);
          if (pend) {
            uint32_t fin = ~T;
            if ((v[0] & v[1] & v[2] & v[3] & LMASK) == LMASK)
              asm volatile("global_load_dword %0, %1, %2 sc1\n\ts_waitcnt vmcnt(0)" : "=&v"(fin) : "v"(dn), "s"(ws) : "memory");
            LD16W(v, o);
            v ^= ut;
            pend = ((v[0] | v[1] | v[2] | v[3]) & UMASK) != 0u;
            if (pend && fin == T) {
              u32x2 sc;
              for (int k = 0; k < SPIN_CAP; ++k) {
                asm volatile("global_load_dwordx2 %0, %1, %2 sc1\n\ts_waitcnt vmcnt(0)" : "=&v"(sc) : "v"(O_SC + 16u * d), "s"(ws) : "memory");
                if (sc[0] == T) break;
              }
              *(u16*)(smem + L_SC + 2 * d) = (u16)(sc[1] >> 16);
              cnt[10] = 1u;
              v = (v ^ ut) & ~LMASK;
              pend = false;
            }
          }
        }
      };
      again(va, vo, d1); again(vc, vo3, d3); again(vb, vo2, d2);
      turn1();
      turn();
    }
    u32x4 a0, a1;
    u32x2 a2, cf;
    {
      uint32_t cc, n = SPIN_CAP;
      asm volatile("s_mov_b64 exec, 1\n\tv_mov_b32 v90, 1\n\tds_add_u32 %6, v90\n\ts_mov_b64 exec, -1\n"
                   "1:\n\tds_read2_b32 %3, %6 offset1:10\n\tds_read_b128 %0, %7\n\tds_read_b128 %1, %7 offset:16\n\tds_read_b64 %2, %8\n\t"
                   "s_sub_u32 %5, %5, 1\n\ts_cbranch_scc1 2f\n\ts_waitcnt lgkmcnt(3)\n\tv_readfirstlane_b32 %4, v90\n\t"
                   "s_cmp_lt_u32 %4, 4\n\ts_cbranch_scc1 1b\n2:\n\ts_waitcnt lgkmcnt(0)"
                   : "=&v"(a0), "=&v"(a1), "=&v"(a2), "=&{v[90:91]}"(cf), "=&s"(cc), "+s"(n) : "v"(ca), "v"(lb0 + pa), "v"(lb0 + pb) : "memory", "scc");
    }
    TS(0, 9);
    if (__builtin_expect(__builtin_amdgcn_readfirstlane(cf[1]) != 0, 0)) {
      const u32x4 q = *(const u32x4*)(smem + L_SC + 16 * lane);
      const uint32_t r = *(const uint32_t*)(smem + L_SC + 1024 + 4 * (lane & 31));
#pragma unroll
      for (int k = 0; k < 4; ++k) { sv[2 * k] = bflo(q[k]); sv[2 * k + 1] = bfhi(q[k]); }
      sv[8] = bflo(r); sv[9] = bfhi(r);
    }
    const uint32_t av[10] = {a0[0], a0[1], a0[2], a0[3], a1[0], a1[1], a1[2], a1[3], a2[0] >> ps, a2[1] >> ps};
    f32x2 h[9];
#pragma unroll
    for (int k = 0; k < 10; ++k) {
      asm("v_fma_mix_f32 %0, %1, %2, %3 op_sel_hi:[1,0,0]" : "=v"(h[(k < 8) ? k >> 1 : 8][k & 1]) : "v"(av[k]), "v"(sv[k]), "v"(xf[(k < 8) ? k >> 1 : 8][k & 1]));
      if (k < 8) asm("v_fma_mix_f32 %0, %1, %2, %3 op_sel:[1,0,0] op_sel_hi:[1,0,0]" : "=v"(h[4 + (k >> 1)][k & 1]) : "v"(av[k]), "v"(sv[k]), "v"(xf[4 + (k >> 1)][k & 1]));
    }
#pragma unroll
    for (int k = 0; k < 9; ++k) hv[k] = pack2(h[k][0], h[k][1]);
    const float da = dot2(hv[2], wv[2], dot2(hv[0], wv[0], zb[0])) + dot2(hv[3], wv[3], dot2(hv[1], wv[1], zb[1]));
    const float db = dot2(hv[6], wv[2], dot2(hv[4], wv[0], zb[2])) + dot2(hv[7], wv[3], dot2(hv[5], wv[1], zb[3]));
    float dt = dot2(hv[8], wv[4], fold32(da, db));
    f32x2 rc, nc;
    ln_stats<true>(hv, lane, 1.f, xf, za, rc, nc, dt);
    dt = (lane < 32) ? dt * rc[0] + ws_ * nc[0] : dt * rc[1] + ws_ * nc[1];
    const uint32_t bb = pack2(dt, dt) & 0xffff0000u;
    const uint32_t kk = (bb ^ (((uint32_t)((int)bb >> 31) & 0x7fff0000u) | 0x80000000u)) | kid;
    u32x4 ka, kb;
    uint32_t old;
    asm volatile("s_mov_b64 exec, %5\n\tds_write_b32 %3, %4\n\ts_mov_b64 exec, 1\n\tv_mov_b32 %2, 1\n\tds_add_rtn_u32 %2, %7, %2\n\ts_mov_b64 exec, -1\n\t"
                 "ds_read_b128 %0, %6\n\tds_read_b128 %1, %6 offset:16\n\ts_waitcnt lgkmcnt(0)"
                 : "=&v"(ka), "=&v"(kb), "=&v"(old) : "v"(ka_), "v"(kk), "s"(0x8000000080000000ull), "v"(kb_), "v"(ca) : "memory");
    if (__builtin_amdgcn_readfirstlane(old) == 7) {
      uint32_t m1 = max(max(ka[0], ka[1]), ka[2]), m2 = med3u(ka[0], ka[1], ka[2]), m3 = min(min(ka[0], ka[1]), ka[2]);
#pragma unroll
      for (int e = 0; e < 5; ++e) {
        const uint32_t kx = e ? kb[e - 1] : ka[3];
        m3 = med3u(m2, m3, kx); m2 = med3u(m1, m2, kx); m1 = max(m1, kx);
      }
      if (lane < 8) ST16(ko_, ((u32x4){m1 | 24u, m2 | 16u, m3 | 8u, T & ~1u}));
    }
    TS(0, 10);
    keya = (uint32_t)__builtin_amdgcn_readlane(kk, 31); keyb = (uint32_t)__builtin_amdgcn_readlane(kk, 63);
    ln_apply<false>(xf, lane, rc, nc, nv);
    gwait(ca, 8);
    if (wl == 1) {
      const u32x4 kv = *(const u32x4*)&kl[4 * (lane & 1)];
      if (lane < 2) ST16(O_KF + (uint32_t)(2 * g2 + lane) * 16u, ((u32x4){__builtin_amdgcn_perm(kv[1], kv[0], 0x07060302u), T, __builtin_amdgcn_perm(kv[3], kv[2], 0x07060302u), T}));
      if (ROLE == 2 && b == 0 && lane == 0) ST4(O_EP, T * 0x0E8B2F51u);
    }
    const uint32_t ko = kl[lane & 7];
    rka = FORCEFB + __popcll(__ballot(ko > keya) & 0xffull); rkb = FORCEFB + __popcll(__ballot(ko > keyb) & 0xffull);
#pragma unroll
    for (int k = 0; k < 9; ++k) {
      const f32x2 o = xf[k] + (f32x2){bflo(nv[k]), bfhi(nv[k])};
      hv[k] = pack2(o[0], o[1]);
      nv[k] = min(nv[k], 0xfffffffeu);
    }
    if (rka < 2) put_row(nv, 0, ws, O_XC + (uint32_t)(1023 - 4 * g2 - rka) * 1152u);
    if (rkb < 2) put_row(nv, 1, ws, O_XC + (uint32_t)(1023 - 4 * g2 - rkb) * 1152u);
    if (!LPOS && ffn) down_dma(KN, 4, lane);
    ffn_terms();
  }
  uint32_t k3rd = 0u, k4th = 0u;
  uint32_t q0 = lb0 + LM + 4u * lane, q1 = lb0 + LM + 32u * w, q2 = q1 + 4u * (lane & 7), q3 = lb0 + LM + 512u + 4u * (lane & 7), q4 = lb0 + LM + (uint32_t)(tid & ~7);
  uint32_t r8 = C_, sb = lb0 + LM + 512u;
  asm volatile("" : "+v"(q0), "+v"(q1), "+v"(q2), "+v"(q3), "+v"(q4), "+v"(r8), "+v"(sb));
  if constexpr (ROLE == 1) {
    u32x4 key;
    const uint32_t kof = O_KEY + (uint32_t)(256 * bx + tid) * 16u;
    until(tk);
    if (!ffn) SLEEP(NSLEEP);
    LD16(key, kof);
    if (ffn) {
      gwait0(ca + 4, 8);
      gu_read(1);
    }
    asm volatile("s_waitcnt vmcnt(0)" : "+v"(key) : : "memory");
    if (__builtin_expect(__any(key[3] != (T & ~1u)), 0)) {
#pragma unroll 1
      for (int spin = 0; spin < SPIN_CAP; ++spin) {
        const bool need = key[3] != (T & ~1u);
        if (!__any(need)) break;
        TSC(15);
        SLEEP(KBACK);
        if (need) LD16W(key, kof);
      }
    }
    TS(0, 11);
    const uint32_t m1 = oct_maxu(key[0]);
    const bool hit = key[0] == m1;
    const uint32_t k0 = hit ? key[1] : key[0], k1 = hit ? key[2] : key[1];
    const uint32_t m2 = oct_maxu(k0);
    *LP(u32x2, q4) = (u32x2){m1, m2};
    k3rd = (k0 == m2) ? k1 : k0;
    k4th = (k0 == m2) ? (hit ? 0u : key[2]) : k1;
    if (RPF == 1 && !ffn && w == (bj & 3)) {
      const uint32_t r1 = O_XC + ((m1 >> 3) & 0x3ffu) * 1152u + 128u * (lane & 7), r2 = O_XC + ((m2 >> 3) & 0x3ffu) * 1152u + 128u * (lane & 7);
      asm volatile("global_load_dword v126, %0, %3 sc1\n\tglobal_load_dword v126, %1, %3 sc1\n\tglobal_load_dword v126, %2, %3 offset:1024 sc1"
                   : : "v"(r1), "v"(r2), "v"(((lane & 1) ? r1 : r2) - 128u * (lane & 7)), "s"(ws) : "memory", "v126");
    }
  }
  auto pick = [&](auto co, auto so_) {
    BAR();
    const uint32_t c = *LP(uint32_t, q0 + co.value), own = *LP(uint32_t, q2 + co.value);
    const u32x4 la = *LP(u32x4, q1 + co.value), lb = *LP(u32x4, q1 + co.value + 16);
    uint32_t r = r8;
    asm("v_cmp_gt_u32 s[40:41], %1, %2\n\tv_cmp_gt_u32 s[42:43], %1, %3\n\tv_cmp_gt_u32 s[44:45], %1, %4\n\tv_cmp_gt_u32 s[46:47], %1, %5\n\t"
        "v_cmp_gt_u32 s[48:49], %1, %6\n\tv_cmp_gt_u32 s[50:51], %1, %7\n\tv_cmp_gt_u32 s[52:53], %1, %8\n\tv_cmp_gt_u32 s[54:55], %1, %9\n\t"
        "s_bcnt1_i32_b64 s40, s[40:41]\n\ts_bcnt1_i32_b64 s42, s[42:43]\n\tv_writelane_b32 %0, s40, 0\n\ts_bcnt1_i32_b64 s44, s[44:45]\n\t"
        "v_writelane_b32 %0, s42, 1\n\ts_bcnt1_i32_b64 s46, s[46:47]\n\tv_writelane_b32 %0, s44, 2\n\ts_bcnt1_i32_b64 s48, s[48:49]\n\t"
        "v_writelane_b32 %0, s46, 3\n\ts_bcnt1_i32_b64 s50, s[50:51]\n\tv_writelane_b32 %0, s48, 4\n\ts_bcnt1_i32_b64 s52, s[52:53]\n\t"
        "v_writelane_b32 %0, s50, 5\n\ts_bcnt1_i32_b64 s54, s[54:55]\n\tv_writelane_b32 %0, s52, 6\n\tv_writelane_b32 %0, s54, 7"
        : "+v"(r) : "v"(c), "v"(la[0]), "v"(la[1]), "v"(la[2]), "v"(la[3]), "v"(lb[0]), "v"(lb[1]), "v"(lb[2]), "v"(lb[3])
        : "scc", "s40", "s41", "s42", "s43", "s44", "s45", "s46", "s47", "s48", "s49", "s50", "s51", "s52", "s53", "s54", "s55");
    *LP(uint32_t, sb + 4u * min(r, (uint32_t)C_) + so_.value) = own;
    BAR();
  };
  auto rows = [&](uint32_t ao, auto&& meanwhile) {
    u32x4 aa, ab, ac = __builtin_bit_cast(u32x4, zb);
    asm volatile("global_load_dwordx4 %0, %3, %4 sc1\n\tglobal_load_dwordx4 %1, %3, %4 offset:512 sc1\n\t"
                 "s_cmp_lg_u32 %5, 0\n\ts_cbranch_scc1 1f\n\tglobal_load_dwordx4 %2, %3, %4 offset:1024 sc1\n1:"
                 : "=&v"(aa), "=&v"(ab), "+v"(ac) : "v"(ao), "s"(ws), "s"(w) : "memory", "scc");
    __builtin_amdgcn_sched_barrier(0);
    meanwhile();
    asm volatile("s_waitcnt vmcnt(0)" : "+v"(aa), "+v"(ab), "+v"(ac) : : "memory");
    if (__builtin_expect(__any(max(max(max4(aa), max4(ab)), max4(ac)) == 0xffffffffu), 0)) {
#pragma unroll 1
      for (int spin = 0; spin < SPIN_CAP; ++spin) {
        const bool na = max4(aa) == 0xffffffffu, nb = max4(ab) == 0xffffffffu, nc = max4(ac) == 0xffffffffu;
        if (!__any(na || nb || nc)) break;
        TSC(14);
        SLEEP(PSLEEP);
        if (na) LD16W(aa, ao);
        if (nb) LD16W(ab, ao + 512u);
        if (nc) LD16W(ac, ao + 1024u);
      }
    }
    *(u32x4*)(smem + LA + 16 * tid) = aa;
    *(u32x4*)(smem + LA + 4096 + 16 * tid) = ab;
    if (w == 0) *(u32x4*)(smem + LA + 8192 + 16 * tid) = ac;
  };
  auto rows_dma = [&](uint32_t ao) {
    uint32_t dl = lb0 + LA + 1024u * w, keep;
    asm volatile("s_mov_b32 %0, m0\n\ts_mov_b32 m0, %1\n\ts_add_u32 %1, %1, 4096 - 512\n\tglobal_load_lds_dwordx4 %2, %3 sc1\n\ts_mov_b32 m0, %1\n\ts_add_u32 %1, %1, 4096 - 512\n\t"
                 "global_load_lds_dwordx4 %2, %3 offset:512 sc1\n\ts_cmp_lg_u32 %4, 0\n\ts_cbranch_scc1 1f\n\ts_mov_b32 m0, %1\n\ts_nop 0\n\t"
                 "global_load_lds_dwordx4 %2, %3 offset:1024 sc1\n1:\n\ts_mov_b32 m0, %0" : "=&s"(keep), "+s"(dl) : "v"(ao), "s"(ws), "s"(w) : "memory", "scc");
  };
  auto get_bd = [&](int t0, int t1) {
#pragma unroll
    for (int t = t0; t < t1; ++t) {
      const u32x2 h0 = trd(da + 512 * (t >> 2) + 32 * (t & 3)), h1 = trd(da + 512 * (t >> 2) + 32 * (t & 3) + 4608 + 32);
      bw[t] = (u32x4){h0[0], h0[1], h1[0], h1[1]};
    }
#pragma unroll
    for (int t = t0; t < t1; ++t) asm volatile("" : "+v"(bw[t]));
  };
  pick(IC<0>{}, IC<0>{});
  uint32_t mykey = *LP(uint32_t, q3), ro = ((mykey >> 3) & 0x3ffu) * 1152u;
  TS(0, 12);
  auto check = [&] {
    cnt[(k3rd > (uint32_t)__builtin_amdgcn_readlane(mykey, 7)) ? 5 : 6] = 1u;
    const uint32_t m3 = oct_maxu(k3rd);
    if (k3rd == m3) k3rd = k4th;
    if ((lane & 7) == 0) cand[72 + (tid >> 3)] = m3;
    if (w == 0 && lane < 32) cand[(lane < 8) ? 64 + lane : 96 + lane] = (lane < 8) ? mykey : 0u;
  };
  if constexpr (ROLE == 1) {
    if (ffn && !FORCEFB) rows_dma(ro + al);
    check();
    DRAIN();
  } else if (ffn) {
    if (LPOS) { SLEEP(LSLEEP); down_dma(KN, 4, lane); } else { DRAIN(); gsync0(ca, 12); get_bd(0, 8); }
  }
  BAR();
  TS(0, 5);
  auto put_out = [&](int j, uint32_t k0, int rk) {
    const uint64_t mine = __ballot(((mykey ^ k0) & ~24u) == 0u) & 0xffull;
    if (mine == 0ull) put_row(hv, j, out, (uint32_t)((8 * g2 + 2 * wl + j) * (D_ * 2)));
    else if (rk >= 2) put_row(nv, j, ws, O_FB + 1152u * (uint32_t)__builtin_ctzll(mine));
  };
  auto rest_out = [&] { put_out(0, keya, rka); put_out(1, keyb, rkb); };
  f32x4 lo, hi;
  auto gate_up = [&] {
    u32x4 av[5];
    const uint32_t aa = lb0 + LA + 16u * lane;
#pragma unroll
    for (int j = 0; j < 5; ++j) asm volatile("ds_read_b128 %0, %1 offset:%2" : "=v"(av[j]) : "v"(aa), "n"(1024 * j) : "memory");
    lo = (f32x4){0.f, 0.f, 0.f, 0.f}; hi = lo;
#pragma unroll
    for (int j = 0; j < 9; ++j) {
      if (j < 5) asm volatile("s_waitcnt lgkmcnt(4)" : "+v"(av[j % 5]));
      else if (j == 5) asm volatile("s_waitcnt lgkmcnt(2)" : "+v"(av[0]), "+v"(av[1]));
      else if (j == 7) asm volatile("s_waitcnt lgkmcnt(0)" : "+v"(av[2]), "+v"(av[3]));
      lo = MMB(av[j % 5], bw[2 * j], lo);
      hi = MMB(av[j % 5], bw[2 * j + 1], hi);
      __builtin_amdgcn_sched_barrier(0);
      if (j < 4) asm volatile("ds_read_b128 %0, %1 offset:%2" : "=v"(av[j]) : "v"(aa), "n"(1024 * (j + 5)) : "memory");
    }
  };
  uint32_t fl;
  asm volatile("ds_read_b32 %0, %1" : "=v"(fl) : "v"(ca + 20) : "memory");
  if (ROLE == 1) gate_up();
  asm volatile("s_waitcnt lgkmcnt(0)" : "+v"(fl));
  const bool slow = FORCESLOW || FORCEFB || __builtin_amdgcn_readfirstlane(fl) != 0;
  if (__builtin_expect(slow, 0)) {
    TSC(17);
    pick(IC<256>{}, IC<64>{});
    mykey = sel[16 + (lane & 7)];
    ro = ((mykey >> 3) & 0x3ffu) * 1152u;
    if (ROLE == 1 && (k3rd > (uint32_t)__builtin_amdgcn_readlane(mykey, 7) || (mykey & 24u) == 8u)) cnt[11] = 1u;
    BAR();
    if (FORCESLOW > 1 || FORCEFB || __builtin_amdgcn_readfirstlane(cnt[11]) != 0) {
    u32x4 key;
#pragma unroll 1
    for (int spin = 0; spin < SPIN_CAP; ++spin) {
      LD16W(key, O_KF + 16u * tid);
      if (!__any(key[1] != T || key[3] != T)) break;
      SLEEP(KBACK);
    }
    const uint32_t ib = (uint32_t)(((255 - (tid >> 1)) << 5) | (7 - 4 * (tid & 1)));
    uint32_t k0 = (key[0] << 16) | ib, k1 = (key[0] & 0xffff0000u) | (ib - 1), k2 = (key[2] << 16) | (ib - 2), k3 = (key[2] & 0xffff0000u) | (ib - 3);
    CEX(k0, k1) CEX(k2, k3) CEX(k0, k2) CEX(k1, k3) CEX(k1, k2)
#pragma unroll 1
    for (int c = 0; c < C_; ++c) {
      const uint32_t mx = wave_maxu(k0);
      const bool hit = (k0 == mx);
      k0 = hit ? k1 : k0; k1 = hit ? k2 : k1; k2 = hit ? k3 : k2; k3 = hit ? 0u : k3;
      if (lane == 0) cand[64 + 8 * w + c] = mx;
    }
    pick(IC<256>{}, IC<128>{});
    mykey = sel[32 + (lane & 7)];
    const uint32_t okey = sel[32 + (lane >> 3)];
    const uint64_t same = __ballot(((mykey ^ okey) & 0x1fe0u) == 0u && okey > mykey);
    const int sl = FORCEFB + __popcll((same >> (lane & 7)) & 0x0101010101010101ull);
    ro = (sl < 2) ? (((mykey >> 3) & 0x3ffu) + 3 - sl) * 1152u : O_FB - O_XC + 1152u * (lane & 7);
    }
    if (ln2) rest_out();
    if (ffn) {
      if (ROLE == 1) rows(ro + al, [] {});
      BAR();
      if (ROLE == 1) gate_up();
    }
  }
  if (OPOS == 0 && ROLE == 2 && ffn && !slow) { SLEEP(OD); rest_out(); }
  if (!ffn) {
    if (ln2 && !slow) { SLEEP(OD2); rest_out(); }
    TSE();
    return;
  }
  uint32_t fx = 0, fv = 0, fn = 0, fo = 0, sa[9];
  float rs, hx, xs;
  u32x4 se;
  if constexpr (ROLE == 1) {
    f32x4 lh = (lane < 32) ? lo : hi;
    float fa = fold32(lh[0], lh[2]), fb2 = fold32(lh[1], lh[3]);
    if (__builtin_expect(__any(__builtin_isunordered(fa, fb2)) && !slow, 0)) {
      gsync0(ca + 4, 12);
      rows(ro + al, [] {});
      gsync0(ca + 4, 16);
      gate_up();
      lh = (lane < 32) ? lo : hi;
      fa = fold32(lh[0], lh[2]); fb2 = fold32(lh[1], lh[3]);
    }
    const float g = __int_as_float(__builtin_amdgcn_update_dpp(__float_as_int(fa), __float_as_int(fb2), 0x128, 0xf, 0xc, false));
    const float u = __int_as_float(__builtin_amdgcn_update_dpp(__float_as_int(fb2), __float_as_int(fa), 0x128, 0xf, 0x3, false));
    const uint32_t gu = pack2(g, u);
    const float gp = bflo(gu);
    const float sg = rbf(gp * __builtin_amdgcn_rcpf(1.f + __expf(-gp)));
    *(u16*)((char*)hm + ha) = f2bf(sg * bfhi(gu));
  } else if (LPOS) {
    DRAIN();
    gsync0(ca, 12);
    get_bd(0, 8);
  }
  BAR();
  TS(0, 6);
  if (ROLE == 2 || w == 2) {
    const u32x4 a0 = *(const u32x4*)((char*)hm + h0a), a1 = *(const u32x4*)((char*)hm + h1a);
    if (ROLE == 1) get_bd(0, 4);
    const f32x4 z = {0.f, 0.f, 0.f, 0.f};
    f32x4 o[4];
#pragma unroll
    for (int t = 0; t < 2 * ROLE; ++t) o[t] = MMB(a1, bw[2 * t + 1], MMB(a0, bw[2 * t], z));
#pragma unroll
    for (int t = 0; t < ROLE; ++t) {
      u32x4 sv;
#pragma unroll
      for (int r = 0; r < 4; ++r) sv[r] = min(pack2(o[2 * t][r], o[2 * t + 1][r]), 0xfffffffeu);
      ST16(so + 4096u * t, sv);
    }
    TS(0, 13);
  }
  if (OPOS == 1 && ROLE == 2) {
    SLEEP(OSLEEP);
    if (!slow) rest_out();
  }
  if (ROLE == 1 && w == 1) {
    const int q = (fb & 15) >> 2, ts = 4 * (q & 1) + ((i & 7) >> 1);
    const int dd = 64 * (fb >> 4) + 16 * (2 * (i & 1) + (q >> 1)) + 4 * (fb & 3) + 2 * (kq & 1) + (kq >> 1);
    const uint32_t fkey = (uint32_t)__builtin_amdgcn_ds_bpermute(4 * ts, (int)mykey), fr = (uint32_t)__builtin_amdgcn_ds_bpermute(4 * ts, (int)ro);
    const int ft = 2047 - (int)(((fkey >> 2) & 0x7f8u) | (fkey & 7u));
    fo = (uint32_t)(ft * D_ + dd) * 2u;
    u32x2 fs;
    asm volatile("global_load_ushort %0, %4, %8\n\tglobal_load_ushort %1, %5, %9 sc1\n\tglobal_load_ushort %2, %6, %9 sc1\n\tglobal_load_dwordx2 %3, %7, %9 sc1"
                 : "=&v"(fx), "=&v"(fv), "=&v"(fn), "=&v"(fs) : "v"(fo), "v"(v_at(dd, ft >> 3) + 2u * (ft & 7)), "v"(O_XC + fr + 2u * dd), "v"(O_SC + 16u * dd), "s"(x), "s"(ws) : "memory");
    uint32_t t16 = (((T >> (3 * ((ft >> 1) & 3))) & LMASK) | (((T >> (12 + ((ft >> 1) & 3))) & 0x00010001u) << 14)) >> (16 * (ft & 1)) & 0xffffu;
    const uint32_t o = fkey >> 16;
    const float rs0 = bf2f((u16)((o & 0x8000u) ? (o & 0x7fffu) : (~o & 0xffffu)));
    rs = rbf(__builtin_amdgcn_rcpf(1.f + __expf(-rs0)));
    const uint32_t one = (i < 8) ? (0x3f80u << (16 * (i & 1))) : 0u;
    se = (u32x4){(i >> 1) == 0 ? one : 0u, (i >> 1) == 1 ? one : 0u, (i >> 1) == 2 ? one : 0u, (i >> 1) == 3 ? one : 0u};
#pragma unroll
    for (int m = 0; m < 9; ++m) sa[m] = O_SL + (uint32_t)(16 * m + (lane >> 2)) * SP + (uint32_t)(4 * fb + (lane & 3)) * 16u;
    asm volatile("" : "+v"(t16), "+v"(rs), "+v"(se), "+v"(sa[0]), "+v"(sa[1]), "+v"(sa[2]), "+v"(sa[3]), "+v"(sa[4]), "+v"(sa[5]), "+v"(sa[6]), "+v"(sa[7]), "+v"(sa[8]));
    SLEEP(SSLEEP);
    TS(0, 1);
    float sum;
#pragma unroll 1
    for (int spin = 0; spin < SPIN_CAP; ++spin) {
      u32x4 sv[9];
#pragma unroll
      for (int m = 0; m < 9; ++m) LD16(sv[m], sa[m]);
      asm volatile("s_waitcnt vmcnt(9)" : "+v"(fx), "+v"(fv), "+v"(fn), "+v"(fs));
      if (__builtin_expect(__any(fs[0] != T), 0)) {
        for (int k = 0; k < SPIN_CAP && fs[0] != T; ++k)
          asm volatile("global_load_dwordx2 %0, %1, %2 sc1\n\ts_waitcnt vmcnt(0)" : "=&v"(fs) : "v"(O_SC + 16u * dd), "s"(ws) : "memory");
      }
      const uint32_t f1 = fv ^ t16, f2 = (f1 & 7u) ? fv & ~7u : f1;
      hx = rbf(bf2f((u16)fx) + (float)__builtin_bit_cast(_Float16, (u16)f2) * __uint_as_float(fs[1]));
      xs = bf2f((u16)fn);
      asm volatile("" : "+v"(hx), "+v"(xs));
      f32x4 dv = {0.f, 0.f, 0.f, 0.f};
      static_for<0, 9>([&](auto mm) {
        constexpr int m = mm.value;
        asm volatile("s_waitcnt vmcnt(%1)" : "+v"(sv[m]) : "n"(8 - m));
        dv = MMB(sv[m], se, dv);
        __builtin_amdgcn_sched_barrier(0);
      });
      sum = fold16(fold32(dv[0], dv[1]), fold32(dv[2], dv[3]));
      if (__builtin_expect(!__any(sum != sum), 1)) break;
      uint32_t mx = 0u;
#pragma unroll
      for (int m = 0; m < 9; ++m) mx = max(mx, max4(sv[m]));
      if (!__any(mx == 0xffffffffu)) break;
      TSC(18);
      SLEEP(PSLEEP);
    }
    const float o1 = rbf(xs + rbf(rbf(sum) * rs));
    if (i < 8) asm volatile("global_store_short %0, %1, %2 sc1" : : "v"(fo), "v"((uint32_t)f2bf(hx + o1)), "s"(out) : "memory");
    TS(0, 2);
  }
  TSE();
  }
  }
}
#define BROT "192"
#define CPF_A "s_getpc_b64 s[96:97]\n\ts_lshl_b32 s99, s2, 4\n\ts_lshl_b32 s84, s98, 7\n\ts_add_u32 s99, s99, s84\n\tv_and_b32 v112, 1, v0\n\t" \
              "v_lshl_add_u32 v112, v112, 12, s99\n\tv_min_u32 v112, kf_end-kf_all-0x80, v112\n\ts_mov_b64 exec, 3\n\tglobal_load_dword v112, v112, s[96:97]\n\ts_mov_b64 exec, -1\n\t"
extern "C" __global__ __launch_bounds__(512, 4) void kf_all(KARGS) {
  __shared__ __attribute__((aligned(16))) char smem[SMEM];
  asm volatile("s_load_dwordx16 s[64:79], s[0:1], 0x0\n\ts_load_dwordx4 s[80:83], s[0:1], 0x40\n\tv_readfirstlane_b32 s98, v0\n\t"
               "s_add_u32 s84, s2, " BROT "\n\ts_xor_b32 s84, s84, s2\n\ts_bitset0_b32 s84, 8\n\ts_bitset0_b32 s84, 9\n\ts_xor_b32 s2, s2, s84\n\t"
               "v_mov_b32 v109, 0\n\ts_and_b32 s85, s2, 7\n\ts_bitcmp1_b32 s2, 8\n\ts_cbranch_scc1 1f\n\t" CPF_A "s_bitcmp1_b32 s98, 8\n\ts_cbranch_scc1 2f\n\t"
               "s_andn2_b32 s84, s2, 31\n\ts_lshl2_add_u32 s84, s85, s84\n\ts_bfe_u32 s85, s2, 0x20003\n\ts_or_b32 s84, s84, s85\n\t"
               "s_mulk_i32 s84, 0x2400\n\ts_mulk_i32 s98, 36\n\ts_add_u32 s84, s84, s98\n\tv_and_b32 v110, 63, v0\n\tv_and_b32 v111, 32, v0\n\t"
               "v_lshlrev_b32 v111, 5, v111\n\tv_lshl_add_u32 v111, v110, 2, v111\n\tv_lshl_add_u32 v110, v110, 4, s84\n\tv_add_u32 v111, s84, v111\n\t"
               "s_cmpk_lt_u32 s2, 0x90\n\ts_waitcnt lgkmcnt(0)\n\t"
               "global_load_dwordx4 v[100:103], v110, s[64:65]\n\tglobal_load_dwordx4 v[104:107], v110, s[64:65] offset:1152\n\t"
               "global_load_dword v108, v111, s[64:65] offset:1024\n\tglobal_load_dword v109, v109, s[82:83] sc1\n\t"
               "s_cbranch_scc1 kf_n\n\ts_branch kf_h\n"
               "1:\n\ts_bfe_u32 s84, s2, 0x50003\n\ts_lshl_b32 s85, s85, 5\n\ts_or_b32 s84, s84, s85\n\ts_lshl_b32 s84, s84, 2\n\t"
               "v_and_b32 v110, 0xff, v0\n\tv_bfe_u32 v107, v0, 7, 1\n"
               "4:\n\ts_lshl_b32 s85, s84, 7\n\tv_lshl_add_u32 v110, v110, 1, s85\n\tv_lshl_add_u32 v107, v107, 1, s84\n\ts_and_b32 s85, s98, s2\n\ts_waitcnt lgkmcnt(0)\n\t"
               "global_load_ushort v100, v107, s[78:79]\n\tglobal_load_ushort v101, v110, s[66:67]\n\tglobal_load_ushort v102, v110, s[68:69]\n\t"
               "global_load_dword v109, v109, s[82:83] sc1\n\ts_bitcmp1_b32 s85, 8\n\ts_cbranch_scc1 kf_u\n\t"
               "global_load_ushort v103, v110, s[70:71]\n\tglobal_load_ushort v104, v110, s[72:73]\n\tglobal_load_ushort v105, v110, s[74:75]\n\t"
               "global_load_ushort v106, v110, s[76:77]\n\tglobal_load_ushort v107, v107, s[80:81]\n\ts_bitcmp1_b32 s2, 8\n\ts_cbranch_scc1 kf_c\n"
               "6:\n\ts_barrier\n\ts_branch kf_p\n"
               "2:\n\ts_cmpk_lt_u32 s2, 0xc0\n\ts_cbranch_scc1 6b\n\t"
               "s_lshr_b32 s84, s2, 3\n\ts_lshl3_add_u32 s84, s85, s84\n\ts_lshl1_add_u32 s84, s84, 976\n\t"
               "v_and_b32 v110, 0x7f, v0\n\tv_mov_b32 v107, 0\n\ts_branch 4b"
               : : "s"(__builtin_amdgcn_kernarg_segment_ptr()), "s"(blockIdx.x), "v"(threadIdx.x),
                   "s"((uint32_t)(uintptr_t)(__attribute__((address_space(3))) char*)smem) : "scc", "s96", "s97", "s98", "s99", "v127");
}
extern "C" __global__ __launch_bounds__(512, 4) void kf_c(KARGS) {
  __shared__ __attribute__((aligned(16))) char smem[SMEM];
  role_body<0>(smem, KPASS);
}
extern "C" __global__ __launch_bounds__(512, 4) void kf_u(KARGS) {
  __shared__ __attribute__((aligned(16))) char smem[SMEM];
  role_body<3>(smem, KPASS);
}
extern "C" __global__ __launch_bounds__(512, 4) void kf_n(KARGS) {
  __shared__ __attribute__((aligned(16))) char smem[SMEM];
  role_body<1, 1>(smem, KPASS);
}
extern "C" __global__ __launch_bounds__(512, 4) void kf_h(KARGS) {
  __shared__ __attribute__((aligned(16))) char smem[SMEM];
  role_body<1, 0>(smem, KPASS);
}
extern "C" __global__ __launch_bounds__(512, 4) void kf_p(KARGS) {
  __shared__ __attribute__((aligned(16))) char smem[SMEM];
  role_body<2>(smem, KPASS);
}
extern "C" __global__ __launch_bounds__(512, 4) void kf_conv() { asm volatile(CONV_ASM ::: CONV_CLOB); }
extern "C" __global__ void kf_end() {}

extern "C" __global__ __launch_bounds__(1024) void k_probe(int* dummy) {
  __shared__ int sm[2];
  const uint32_t now = (uint32_t)__builtin_amdgcn_s_memrealtime();
  __shared__ int sn;
  if (threadIdx.x == 0) { sm[0] = 0; sm[1] = 1 == 1 ? 0x7fffffff : 0; sn = 0; }
  __syncthreads();
  for (int k = threadIdx.x; k < 4096; k += 1024) {
    const uint32_t a = (uint32_t)g_prb[k], b = (uint32_t)(g_prb[k] >> 32);
    if (a) atomicMax(&sm[0], (int)(now - a));
    if (b) { if (1 == 1) atomicMin(&sm[1], (int)(now - b)); else if (1 == 0) atomicMax(&sm[1], (int)(now - b)); else { atomicAdd(&sm[1], (int)(now - b)); atomicAdd(&sn, 1); } }
  }
  __syncthreads();
  const int q = min(max(sm[0] * 10 - (1 == 2 ? sm[1] * 10 / max(sn, 1) : sm[1] * 10), 0), 30000);
  if (threadIdx.x == 0) while ((int)((uint32_t)__builtin_amdgcn_s_memrealtime() - now) < q) __builtin_amdgcn_s_sleep(2);
}
extern "C" void kernel_launch(void* const* d_in, const int* in_sizes, int n_in,
                              void* d_out, int out_size, void* d_ws, size_t ws_size,
                              hipStream_t stream) {
  kf_all<<<512, 512, 0, stream>>>((const u16*)d_in[0], (const u16*)d_in[1], (const u16*)d_in[2], (const u16*)d_in[3], (const u16*)d_in[4],
                                  (const u16*)d_in[5], (const u16*)d_in[6], (const u16*)d_in[7], (const u16*)d_in[8], (char*)d_ws + (size_t)WSOFF * 1048576,
                                  (const u16*)d_in[10], (const u16*)d_in[11], (const u16*)d_in[12], (const u16*)d_in[13], (u16*)d_out);
  k_probe<<<1, 1024, 0, stream>>>((int*)d_ws);
}
```

```cpp
#include <hip/hip_runtime.h>
#include <hip/hip_bf16.h>
#include <stdint.h>
#define A_ 1

typedef __attribute__((ext_vector_type(8))) short bf16x8;
typedef __attribute__((ext_vector_type(4))) float f32x4;
typedef uint16_t u16;
typedef __attribute__((ext_vector_type(4))) uint32_t u32x4;
typedef __attribute__((ext_vector_type(2))) uint32_t u32x2;
#define TSD uint32_t pr0_ = 0u, pr1_ = 0u
__device__ unsigned long long g_prb[4096];
#define TS(k, i) do { if ((i) == 0) pr0_ = (uint32_t)__builtin_amdgcn_s_memrealtime() | 1u; \
    if (ROLE == 1 && (i) == 2 && (1 < 0 || w == 1) && (blockIdx.x & 255) < 144) pr1_ = (uint32_t)__builtin_amdgcn_s_memrealtime() | 1u; } while (0)
#define TSC(i)
#define TSE() do { if (__builtin_amdgcn_mbcnt_hi(-1, __builtin_amdgcn_mbcnt_lo(-1, 0)) == 0) \
    __hip_atomic_store(&g_prb[blockIdx.x * 8 + w], ((unsigned long long)pr1_ << 32) | pr0_, __ATOMIC_RELAXED, __HIP_MEMORY_SCOPE_AGENT); } while (0)
#define TSP
#define TSA

constexpr int L_ = 2048, D_ = 576, N_ = 128, F_ = 4608, C_ = 8;
constexpr int NF = 144;
constexpr uint32_t O_EP = 0;
constexpr uint32_t O_KF = 4096;
constexpr uint32_t O_KEY = O_KF + 8192;
constexpr uint32_t O_SC = O_KEY + 32768;
constexpr uint32_t O_DN = O_SC + 9216;
constexpr uint32_t O_SB = O_DN + 20480;
constexpr uint32_t O_SU = O_SB + 65536;
constexpr uint32_t O_FB = O_SU + 262144;
constexpr uint32_t O_XC = O_FB + 8 * 1152;
constexpr uint32_t SP = 9216 + 128;
constexpr uint32_t O_SL = O_XC + 1024 * 1152;
constexpr uint32_t SLU = 336;
constexpr uint32_t O_U = O_SL + 256 * SLU * 16;
constexpr int UP = 584;
constexpr uint32_t VP = 8192 + 128, VPP = 4096 + 128;
constexpr uint32_t O_V = O_U + 256 * UP * 16, O_VP = O_V + 256 * VP;
static_assert(256 * SLU * 16 >= NF * SP && O_SL % 128 == 0 && O_U % 128 == 0 && O_V % 128 == 0, "");
__host__ __device__ __forceinline__ uint32_t u_at(int tg, int d) { return O_U + 16u * (uint32_t)((tg >> 1) * (UP * 2) + (d >> 2) * 8 + (tg & 1) * 4 + (d & 3)); }
__host__ __device__ __forceinline__ uint32_t v_at(int d, int tg) {
  return (d < 512) ? O_V + (uint32_t)(d >> 1) * VP + 16u * (uint32_t)(2 * tg + (d & 1)) : O_VP + (uint32_t)(d - 512) * VPP + 16u * (uint32_t)tg;
}
#define HAVE_VAT
#define HAVE_VTAG
__device__ __forceinline__ float bf2f(u16 v) { return __uint_as_float((uint32_t)v << 16); }
__device__ __forceinline__ float bflo(uint32_t v) { return __uint_as_float(v << 16); }
__device__ __forceinline__ float bfhi(uint32_t v) { return __uint_as_float(v & 0xffff0000u); }
__device__ __forceinline__ u16 f2bf(float f) { return __builtin_bit_cast(u16, (__bf16)f); }
__device__ __forceinline__ float rbf(float f) { return bf2f(f2bf(f)); }
typedef __attribute__((ext_vector_type(2))) float f32x2;
typedef __attribute__((ext_vector_type(2))) __bf16 bf16x2v;
__device__ __forceinline__ uint32_t pack2(float a, float b) {
  return __builtin_bit_cast(uint32_t, __builtin_convertvector((f32x2){a, b}, bf16x2v));
}

#define DPP_I(v, ctrl, rmask) __builtin_amdgcn_update_dpp(0, (v), (ctrl), (rmask), 0xf, true)
__device__ __forceinline__ float row_sum(float v) {
  v += __int_as_float(DPP_I(__float_as_int(v), 0x111, 0xf));
  v += __int_as_float(DPP_I(__float_as_int(v), 0x112, 0xf));
  v += __int_as_float(DPP_I(__float_as_int(v), 0x114, 0xf));
  v += __int_as_float(DPP_I(__float_as_int(v), 0x118, 0xf));
  return v;
}
__device__ __forceinline__ float rdl(float v, int l) { return __int_as_float(__builtin_amdgcn_readlane(__float_as_int(v), l)); }
__device__ __forceinline__ float wave_sum(float v) {
  v = row_sum(v);
  asm("s_nop 1\n\tv_add_f32_dpp %0, %0, %0 row_bcast:15 row_mask:0xa bank_mask:0xf\n\t"
      "s_nop 1\n\tv_add_f32_dpp %0, %0, %0 row_bcast:31 row_mask:0xc bank_mask:0xf\n\ts_nop 0" : "+v"(v));
  return rdl(v, 63);
}
__device__ __forceinline__ float fold32(float a, float b) {
  const auto r = __builtin_amdgcn_permlane32_swap(__float_as_uint(a), __float_as_uint(b), false, false);
  return __uint_as_float(r[0]) + __uint_as_float(r[1]);
}
__device__ __forceinline__ float fold16(float a, float b) {
  const auto r = __builtin_amdgcn_permlane16_swap(__float_as_uint(a), __float_as_uint(b), false, false);
  return __uint_as_float(r[0]) + __uint_as_float(r[1]);
}
__device__ __forceinline__ void wave_sum2(float& a, float& b) {
  float v = row_sum(fold32(a, b));
  asm("s_nop 1\n\tv_add_f32_dpp %0, %0, %0 row_bcast:15 row_mask:0xa bank_mask:0xf\n\ts_nop 0" : "+v"(v));
  a = rdl(v, 31); b = rdl(v, 63);
}
__device__ __forceinline__ float dot2(uint32_t a, uint32_t b, float c) {
  return __builtin_amdgcn_fdot2_f32_bf16(__builtin_bit_cast(bf16x2v, a), __builtin_bit_cast(bf16x2v, b), c, false);
}
__device__ __forceinline__ uint32_t row_maxu(uint32_t v) {
  v = max(v, (uint32_t)DPP_I((int)v, 0xb1, 0xf));
  v = max(v, (uint32_t)DPP_I((int)v, 0x4e, 0xf));
  v = max(v, (uint32_t)DPP_I((int)v, 0x141, 0xf));
  v = max(v, (uint32_t)DPP_I((int)v, 0x140, 0xf));
  return v;
}
__device__ __forceinline__ uint32_t wave_maxu(uint32_t v) {
  v = max(v, (uint32_t)DPP_I((int)v, 0x111, 0xf));
  v = max(v, (uint32_t)DPP_I((int)v, 0x112, 0xf));
  v = max(v, (uint32_t)DPP_I((int)v, 0x114, 0xf));
  v = max(v, (uint32_t)DPP_I((int)v, 0x118, 0xf));
  v = max(v, (uint32_t)DPP_I((int)v, 0x142, 0xa));
  v = max(v, (uint32_t)DPP_I((int)v, 0x143, 0xc));
  return (uint32_t)__builtin_amdgcn_readlane((int)v, 63);
}

typedef __attribute__((ext_vector_type(8))) _Float16 f16x8;
typedef __attribute__((ext_vector_type(2))) _Float16 f16x2;
__device__ __forceinline__ uint32_t packh(f32x2 v) { return __builtin_bit_cast(uint32_t, __builtin_convertvector(v, f16x2)); }
__device__ __forceinline__ uint32_t packh(float a, float b) { return packh((f32x2){a, b}); }
constexpr int IP = 224, I_X = 128;
__device__ __forceinline__ f32x2 cmul(f32x2 z, f32x2 w) {
  f32x2 r;
  asm("v_pk_mul_f32 %0, %1, %2 op_sel_hi:[0,1]\n\tv_pk_fma_f32 %0, %1, %2, %0 op_sel:[1,1,0] op_sel_hi:[1,0,1] neg_lo:[0,1,0]" : "=&v"(r) : "v"(z), "v"(w));
  return r;
}
__device__ __forceinline__ f32x2 cexp2(float ar2, float air, float l) {
  const float t = air * l;
  return (f32x2){__builtin_amdgcn_cosf(t), __builtin_amdgcn_sinf(t)} * __builtin_amdgcn_exp2f(ar2 * l);
}
__device__ __forceinline__ float state_a(float ldt, float are, float aim, float& ar2, float& air, float& g) {
  const float dt = rbf(__expf(ldt));
  ar2 = dt * are * 1.4426950408889634f;
  air = dt * aim * 0.15915494309189535f;
  air -= __builtin_rintf(air);
  g = 0.f;
  if (__builtin_expect(__any(ar2 > 0.f), 0)) g = fminf(__uint_as_float(wave_maxu(__float_as_uint(fmaxf(ar2, 0.f)))), 1.f);
  return dt;
}
template <int N> __device__ __forceinline__ void put_im(char* p, int im, const f32x2* z) {
  *(u32x4*)p = (u32x4){packh(z[0][0], z[1][0]), packh(z[2][0], z[3][0]), packh(z[4][0], z[5][0]), packh(z[6][0], z[7][0])};
  *(u32x4*)(p + im) = (u32x4){packh(z[0][1], z[1][1]), packh(z[2][1], z[3][1]), packh(z[4][1], z[5][1]), packh(z[6][1], z[7][1])};
}
#define Y_ASM \
  "v_mov_b64 v[40:41], %0\nv_pk_mul_f32 v[42:43], v[40:41], %1 op_sel_hi:[0,1]\nv_pk_mul_f32 v[44:45], %0, %2 op_sel_hi:[0,1]\n" \
  "v_pk_fma_f32 v[42:43], v[40:41], %1, v[42:43] op_sel:[1,1,0] op_sel_hi:[1,0,1] neg_lo:[0,1,0]\n" \
  "v_pk_fma_f32 v[44:45], %0, %2, v[44:45] op_sel:[1,1,0] op_sel_hi:[1,0,1] neg_lo:[0,1,0]\nv_cvt_pk_f16_f32 v56, v40, v42\n" \
  "v_cvt_pk_f16_f32 v60, v41, v43\nv_pk_mul_f32 v[40:41], v[42:43], %1 op_sel_hi:[0,1]\nv_pk_mul_f32 v[48:49], v[44:45], %2 op_sel_hi:[0,1]\n" \
  "v_pk_fma_f32 v[40:41], v[42:43], %1, v[40:41] op_sel:[1,1,0] op_sel_hi:[1,0,1] neg_lo:[0,1,0]\n" \
  "v_pk_fma_f32 v[48:49], v[44:45], %2, v[48:49] op_sel:[1,1,0] op_sel_hi:[1,0,1] neg_lo:[0,1,0]\n" \
  "v_pk_mul_f32 v[42:43], v[40:41], %1 op_sel_hi:[0,1]\nv_pk_mul_f32 v[52:53], v[48:49], %2 op_sel_hi:[0,1]\n" \
  "v_pk_fma_f32 v[42:43], v[40:41], %1, v[42:43] op_sel:[1,1,0] op_sel_hi:[1,0,1] neg_lo:[0,1,0]\n" \
  "v_pk_fma_f32 v[52:53], v[48:49], %2, v[52:53] op_sel:[1,1,0] op_sel_hi:[1,0,1] neg_lo:[0,1,0]\nv_cvt_pk_f16_f32 v57, v40, v42\n" \
  "v_cvt_pk_f16_f32 v61, v41, v43\nv_pk_mul_f32 v[40:41], v[42:43], %1 op_sel_hi:[0,1]\n" \
  "v_pk_fma_f32 v[40:41], v[42:43], %1, v[40:41] op_sel:[1,1,0] op_sel_hi:[1,0,1] neg_lo:[0,1,0]\n" \
  "v_pk_mul_f32 v[42:43], v[40:41], %1 op_sel_hi:[0,1]\nv_pk_mul_f32 v[46:47], v[44:45], %1 op_sel_hi:[0,1]\n" \
  "v_pk_fma_f32 v[42:43], v[40:41], %1, v[42:43] op_sel:[1,1,0] op_sel_hi:[1,0,1] neg_lo:[0,1,0]\n" \
  "v_pk_fma_f32 v[46:47], v[44:45], %1, v[46:47] op_sel:[1,1,0] op_sel_hi:[1,0,1] neg_lo:[0,1,0]\nv_cvt_pk_f16_f32 v58, v40, v42\n" \
  "v_cvt_pk_f16_f32 v62, v41, v43\nv_cvt_pk_f16_f32 v64, v44, v46\nv_cvt_pk_f16_f32 v68, v45, v47\n" \
  "v_pk_mul_f32 v[40:41], v[42:43], %1 op_sel_hi:[0,1]\nv_pk_mul_f32 v[44:45], v[46:47], %1 op_sel_hi:[0,1]\n" \
  "v_pk_fma_f32 v[40:41], v[42:43], %1, v[40:41] op_sel:[1,1,0] op_sel_hi:[1,0,1] neg_lo:[0,1,0]\n" \
  "v_pk_fma_f32 v[44:45], v[46:47], %1, v[44:45] op_sel:[1,1,0] op_sel_hi:[1,0,1] neg_lo:[0,1,0]\n" \
  "v_pk_mul_f32 v[42:43], v[40:41], %1 op_sel_hi:[0,1]\nv_pk_mul_f32 v[46:47], v[44:45], %1 op_sel_hi:[0,1]\n" \
  "v_pk_fma_f32 v[42:43], v[40:41], %1, v[42:43] op_sel:[1,1,0] op_sel_hi:[1,0,1] neg_lo:[0,1,0]\n" \
  "v_pk_fma_f32 v[46:47], v[44:45], %1, v[46:47] op_sel:[1,1,0] op_sel_hi:[1,0,1] neg_lo:[0,1,0]\nv_cvt_pk_f16_f32 v59, v40, v42\n" \
  "v_cvt_pk_f16_f32 v63, v41, v43\nv_cvt_pk_f16_f32 v65, v44, v46\nv_cvt_pk_f16_f32 v69, v45, v47\n" \
  "v_pk_mul_f32 v[44:45], v[46:47], %1 op_sel_hi:[0,1]\nds_write_b128 %3, v[56:59] offset:0\n" \
  "v_pk_fma_f32 v[44:45], v[46:47], %1, v[44:45] op_sel:[1,1,0] op_sel_hi:[1,0,1] neg_lo:[0,1,0]\nds_write_b128 %3, v[60:63] offset:64\n" \
  "v_pk_mul_f32 v[46:47], v[44:45], %1 op_sel_hi:[0,1]\nv_pk_mul_f32 v[50:51], v[48:49], %1 op_sel_hi:[0,1]\n" \
  "v_pk_fma_f32 v[46:47], v[44:45], %1, v[46:47] op_sel:[1,1,0] op_sel_hi:[1,0,1] neg_lo:[0,1,0]\n" \
  "v_pk_fma_f32 v[50:51], v[48:49], %1, v[50:51] op_sel:[1,1,0] op_sel_hi:[1,0,1] neg_lo:[0,1,0]\nv_cvt_pk_f16_f32 v66, v44, v46\n" \
  "v_cvt_pk_f16_f32 v70, v45, v47\nv_cvt_pk_f16_f32 v56, v48, v50\nv_cvt_pk_f16_f32 v60, v49, v51\n" \
  "v_pk_mul_f32 v[44:45], v[46:47], %1 op_sel_hi:[0,1]\nv_pk_mul_f32 v[48:49], v[50:51], %1 op_sel_hi:[0,1]\n" \
  "v_pk_fma_f32 v[44:45], v[46:47], %1, v[44:45] op_sel:[1,1,0] op_sel_hi:[1,0,1] neg_lo:[0,1,0]\n" \
  "v_pk_fma_f32 v[48:49], v[50:51], %1, v[48:49] op_sel:[1,1,0] op_sel_hi:[1,0,1] neg_lo:[0,1,0]\n" \
  "v_pk_mul_f32 v[46:47], v[44:45], %1 op_sel_hi:[0,1]\nv_pk_mul_f32 v[50:51], v[48:49], %1 op_sel_hi:[0,1]\n" \
  "v_pk_fma_f32 v[46:47], v[44:45], %1, v[46:47] op_sel:[1,1,0] op_sel_hi:[1,0,1] neg_lo:[0,1,0]\n" \
  "v_pk_fma_f32 v[50:51], v[48:49], %1, v[50:51] op_sel:[1,1,0] op_sel_hi:[1,0,1] neg_lo:[0,1,0]\nv_cvt_pk_f16_f32 v67, v44, v46\n" \
  "v_cvt_pk_f16_f32 v71, v45, v47\nv_cvt_pk_f16_f32 v57, v48, v50\nv_cvt_pk_f16_f32 v61, v49, v51\n" \
  "v_pk_mul_f32 v[48:49], v[50:51], %1 op_sel_hi:[0,1]\nds_write_b128 %3, v[64:67] offset:16\n" \
  "v_pk_fma_f32 v[48:49], v[50:51], %1, v[48:49] op_sel:[1,1,0] op_sel_hi:[1,0,1] neg_lo:[0,1,0]\nds_write_b128 %3, v[68:71] offset:80\n" \
  "v_pk_mul_f32 v[50:51], v[48:49], %1 op_sel_hi:[0,1]\nv_pk_mul_f32 v[54:55], v[52:53], %1 op_sel_hi:[0,1]\n" \
  "v_pk_fma_f32 v[50:51], v[48:49], %1, v[50:51] op_sel:[1,1,0] op_sel_hi:[1,0,1] neg_lo:[0,1,0]\n" \
  "v_pk_fma_f32 v[54:55], v[52:53], %1, v[54:55] op_sel:[1,1,0] op_sel_hi:[1,0,1] neg_lo:[0,1,0]\nv_cvt_pk_f16_f32 v58, v48, v50\n" \
  "v_cvt_pk_f16_f32 v62, v49, v51\nv_cvt_pk_f16_f32 v64, v52, v54\nv_cvt_pk_f16_f32 v68, v53, v55\n" \
  "v_pk_mul_f32 v[48:49], v[50:51], %1 op_sel_hi:[0,1]\nv_pk_mul_f32 v[52:53], v[54:55], %1 op_sel_hi:[0,1]\n" \
  "v_pk_fma_f32 v[48:49], v[50:51], %1, v[48:49] op_sel:[1,1,0] op_sel_hi:[1,0,1] neg_lo:[0,1,0]\n" \
  "v_pk_fma_f32 v[52:53], v[54:55], %1, v[52:53] op_sel:[1,1,0] op_sel_hi:[1,0,1] neg_lo:[0,1,0]\n" \
  "v_pk_mul_f32 v[50:51], v[48:49], %1 op_sel_hi:[0,1]\nv_pk_mul_f32 v[54:55], v[52:53], %1 op_sel_hi:[0,1]\n" \
  "v_pk_fma_f32 v[50:51], v[48:49], %1, v[50:51] op_sel:[1,1,0] op_sel_hi:[1,0,1] neg_lo:[0,1,0]\n" \
  "v_pk_fma_f32 v[54:55], v[52:53], %1, v[54:55] op_sel:[1,1,0] op_sel_hi:[1,0,1] neg_lo:[0,1,0]\nv_cvt_pk_f16_f32 v59, v48, v50\n" \
  "v_cvt_pk_f16_f32 v63, v49, v51\nv_cvt_pk_f16_f32 v65, v52, v54\nv_cvt_pk_f16_f32 v69, v53, v55\n" \
  "v_pk_mul_f32 v[52:53], v[54:55], %1 op_sel_hi:[0,1]\nds_write_b128 %3, v[56:59] offset:32\n" \
  "v_pk_fma_f32 v[52:53], v[54:55], %1, v[52:53] op_sel:[1,1,0] op_sel_hi:[1,0,1] neg_lo:[0,1,0]\nds_write_b128 %3, v[60:63] offset:96\n" \
  "v_pk_mul_f32 v[54:55], v[52:53], %1 op_sel_hi:[0,1]\n" \
  "v_pk_fma_f32 v[54:55], v[52:53], %1, v[54:55] op_sel:[1,1,0] op_sel_hi:[1,0,1] neg_lo:[0,1,0]\nv_cvt_pk_f16_f32 v66, v52, v54\n" \
  "v_cvt_pk_f16_f32 v70, v53, v55\nv_pk_mul_f32 v[52:53], v[54:55], %1 op_sel_hi:[0,1]\n" \
  "v_pk_fma_f32 v[52:53], v[54:55], %1, v[52:53] op_sel:[1,1,0] op_sel_hi:[1,0,1] neg_lo:[0,1,0]\n" \
  "v_pk_mul_f32 v[54:55], v[52:53], %1 op_sel_hi:[0,1]\n" \
  "v_pk_fma_f32 v[54:55], v[52:53], %1, v[54:55] op_sel:[1,1,0] op_sel_hi:[1,0,1] neg_lo:[0,1,0]\nv_cvt_pk_f16_f32 v67, v52, v54\n" \
  "v_cvt_pk_f16_f32 v71, v53, v55\nds_write_b128 %3, v[64:67] offset:48\nds_write_b128 %3, v[68:71] offset:112\n"
#define Y_CLOB "memory", "v40", "v41", "v42", "v43", "v44", "v45", "v46", "v47", "v48", "v49", "v50", "v51", "v52", "v53", "v54", "v55", "v56", "v57", "v58", "v59", "v60", "v61", "v62", "v63", "v64", "v65", "v66", "v67", "v68", "v69", "v70", "v71"
#define T_ASM \
  "ds_read_b128 %4, %8\nds_read_b64_tr_b16 v[80:81], %7 offset:0\nds_read_b64_tr_b16 v[82:83], %7 offset:64\n" \
  "ds_read_b64_tr_b16 v[84:85], %5 offset:0\nds_read_b64_tr_b16 v[86:87], %5 offset:48\nds_read_b64_tr_b16 v[88:89], %6 offset:0\n" \
  "ds_read_b64_tr_b16 v[90:91], %6 offset:48\nds_read_b64_tr_b16 v[92:93], %7 offset:3584\nds_read_b64_tr_b16 v[94:95], %7 offset:3648\n" \
  "ds_read_b64_tr_b16 v[96:97], %5 offset:3584\nds_read_b64_tr_b16 v[98:99], %5 offset:3632\nds_read_b64_tr_b16 v[100:101], %6 offset:3584\n" \
  "ds_read_b64_tr_b16 v[102:103], %6 offset:3632\nds_read_b64_tr_b16 v[104:105], %7 offset:7168\nds_read_b64_tr_b16 v[106:107], %7 offset:7232\n" \
  "s_waitcnt lgkmcnt(8)\nv_mfma_f32_16x16x32_f16 %0, v[84:87], v[80:83], 0\nds_read_b64_tr_b16 v[108:109], %5 offset:7168\n" \
  "ds_read_b64_tr_b16 v[110:111], %5 offset:7216\nds_read_b64_tr_b16 v[112:113], %6 offset:7168\nv_mfma_f32_16x16x32_f16 %2, v[88:91], v[80:83], 0\n" \
  "ds_read_b64_tr_b16 v[114:115], %6 offset:7216\nds_read_b64_tr_b16 v[116:117], %7 offset:10752\nds_read_b64_tr_b16 v[118:119], %7 offset:10816\n" \
  "s_waitcnt lgkmcnt(8)\nv_mfma_f32_16x16x32_f16 %0, v[96:99], v[92:95], %0\nds_read_b64_tr_b16 v[120:121], %5 offset:10752\n" \
  "ds_read_b64_tr_b16 v[122:123], %5 offset:10800\nds_read_b64_tr_b16 v[124:125], %6 offset:10752\n" \
  "v_mfma_f32_16x16x32_f16 %2, v[100:103], v[92:95], %2\nds_read_b64_tr_b16 v[126:127], %6 offset:10800\n" \
  "ds_read_b64_tr_b16 v[80:81], %7 offset:14336\nds_read_b64_tr_b16 v[82:83], %7 offset:14400\ns_waitcnt lgkmcnt(8)\n" \
  "v_mfma_f32_16x16x32_f16 %0, v[108:111], v[104:107], %0\nds_read_b64_tr_b16 v[84:85], %5 offset:14336\n" \
  "ds_read_b64_tr_b16 v[86:87], %5 offset:14384\nds_read_b64_tr_b16 v[88:89], %6 offset:14336\n" \
  "v_mfma_f32_16x16x32_f16 %2, v[112:115], v[104:107], %2\nds_read_b64_tr_b16 v[90:91], %6 offset:14384\n" \
  "ds_read_b64_tr_b16 v[92:93], %7 offset:17920\nds_read_b64_tr_b16 v[94:95], %7 offset:17984\ns_waitcnt lgkmcnt(8)\n" \
  "v_mfma_f32_16x16x32_f16 %0, v[120:123], v[116:119], %0\nds_read_b64_tr_b16 v[96:97], %5 offset:17920\n" \
  "ds_read_b64_tr_b16 v[98:99], %5 offset:17968\nds_read_b64_tr_b16 v[100:101], %6 offset:17920\n" \
  "v_mfma_f32_16x16x32_f16 %2, v[124:127], v[116:119], %2\nds_read_b64_tr_b16 v[102:103], %6 offset:17968\n" \
  "ds_read_b64_tr_b16 v[104:105], %7 offset:21504\nds_read_b64_tr_b16 v[106:107], %7 offset:21568\ns_waitcnt lgkmcnt(8)\n" \
  "v_mfma_f32_16x16x32_f16 %1, v[84:87], v[80:83], 0\nds_read_b64_tr_b16 v[108:109], %5 offset:21504\nds_read_b64_tr_b16 v[110:111], %5 offset:21552\n" \
  "ds_read_b64_tr_b16 v[112:113], %6 offset:21504\nv_mfma_f32_16x16x32_f16 %3, v[88:91], v[80:83], 0\nds_read_b64_tr_b16 v[114:115], %6 offset:21552\n" \
  "ds_read_b64_tr_b16 v[116:117], %7 offset:25088\nds_read_b64_tr_b16 v[118:119], %7 offset:25152\ns_waitcnt lgkmcnt(8)\n" \
  "v_mfma_f32_16x16x32_f16 %1, v[96:99], v[92:95], %1\nds_read_b64_tr_b16 v[120:121], %5 offset:25088\n" \
  "ds_read_b64_tr_b16 v[122:123], %5 offset:25136\nds_read_b64_tr_b16 v[124:125], %6 offset:25088\n" \
  "v_mfma_f32_16x16x32_f16 %3, v[100:103], v[92:95], %3\nds_read_b64_tr_b16 v[126:127], %6 offset:25136\ns_waitcnt lgkmcnt(6)\n" \
  "v_mfma_f32_16x16x32_f16 %1, v[108:111], v[104:107], %1\nv_mfma_f32_16x16x32_f16 %3, v[112:115], v[104:107], %3\ns_waitcnt lgkmcnt(0)\n" \
  "v_mfma_f32_16x16x32_f16 %1, v[120:123], v[116:119], %1\nv_mfma_f32_16x16x32_f16 %3, v[124:127], v[116:119], %3\n"
#define T_CLOB "memory", "v80", "v81", "v82", "v83", "v84", "v85", "v86", "v87", "v88", "v89", "v90", "v91", "v92", "v93", "v94", "v95", "v96", "v97", "v98", "v99", "v100", "v101", "v102", "v103", "v104", "v105", "v106", "v107", "v108", "v109", "v110", "v111", "v112", "v113", "v114", "v115", "v116", "v117", "v118", "v119", "v120", "v121", "v122", "v123", "v124", "v125", "v126", "v127"
template <class F> __device__ __forceinline__ int gen_y(float ldt, float are, float aim, float bre, float bim, float cre, float cim, uint32_t im, F&& mid TSP) {
  float ar2, air, g;
  const float dt = state_a(ldt, are, aim, ar2, air, g);
  const float dbr = dt * bre, dbi = dt * bim, pr = cre * dbr - cim * dbi, pi = cre * dbi + cim * dbr;
  const f32x2 w8 = cexp2(ar2, -air, 8.f), w64 = cexp2(ar2, -air, 64.f);
  const float pm = fmaxf(fabsf(pr), fabsf(pi));
  int e = -4;
  if (__builtin_expect(__any(!(pm < 0x1p-4f) || ar2 > 0.f) || !__any(pm > 0x1p-12f), 0)) e = (int)(wave_maxu(__float_as_uint(pm)) >> 23) - 126 + (int)ceilf(g * 250.f);
  const f32x2 y0 = {__builtin_amdgcn_ldexpf(pr, 14 - e), __builtin_amdgcn_ldexpf(-pi, 14 - e)};
  mid();
  asm volatile(Y_ASM : : "v"(y0), "v"(w8), "v"(w64), "v"(im) : Y_CLOB);
  return e;
}
__device__ __forceinline__ int gen_x(float ldt, float are, float aim, char* im) {
  float ar2, air, g;
  state_a(ldt, are, aim, ar2, air, g);
  const int e = min((int)ceilf(g * 1810.f), 300);
  const float s = __builtin_amdgcn_ldexpf(1.f, 15 - e);
  const f32x2 w1 = cexp2(ar2, air, 1.f), w256 = cexp2(ar2, air, 256.f), w2 = cmul(w1, w1);
  f32x2 a[8], b[8], c[8];
  a[0] = (f32x2){s, 0.f}; a[1] = w1 * s; a[2] = w2 * s;
  b[0] = b[1] = w256 * s;
  b[2] = cmul(b[0], w2); a[3] = cmul(a[1], w2); a[4] = cmul(a[2], w2);
  b[3] = b[4] = b[5] = cmul(b[2], w2); a[5] = cmul(a[3], w2); a[6] = cmul(a[4], w2);
  b[6] = cmul(b[3], w2); a[7] = cmul(a[5], w2); c[0] = cmul(b[3], w256);
  b[7] = cmul(b[6], w2); c[2] = cmul(c[0], w256);
  put_im<0>(im + I_X, 48, a);
  c[1] = cmul(b[7], w256); c[3] = cmul(c[1], w256);
  put_im<0>(im + I_X + 16, 48, b);
#pragma unroll
  for (int k = 4; k < 8; ++k) c[k] = cmul(c[k - 1], w256);
  put_im<0>(im + I_X + 32, 48, c);
  return e;
}

template <int I> struct IC { static constexpr int value = I; };
template <int B, int E, class F> __device__ __forceinline__ void static_for(F&& f) {
  if constexpr (B < E) { f(IC<B>{}); static_for<B + 1, E>(f); }
}
typedef __attribute__((ext_vector_type(4))) short s16x4;
__device__ __forceinline__ u32x2 trd(uint32_t a) {
  return __builtin_bit_cast(u32x2, __builtin_amdgcn_ds_read_tr16_b64_v4i16((__attribute__((address_space(3))) s16x4*)(uintptr_t)a));
}
#define BAR() asm volatile("s_waitcnt lgkmcnt(0)\n\ts_barrier" ::: "memory")
#define ST16(off, v) asm volatile("global_store_dwordx4 %0, %1, %2 sc1\n\ts_nop 1" : : "v"(off), "v"(v), "s"(ws) : "memory")
#define ST4(off, v) asm volatile("global_store_dword %0, %1, %2 sc1" : : "v"(off), "v"(v), "s"(ws) : "memory")
#define LD16(r, off) asm volatile("global_load_dwordx4 %0, %1, %2 sc1" : "=v"(r) : "v"(off), "s"(ws) : "memory")
#define LD16W(r, off) asm volatile("global_load_dwordx4 %0, %1, %2 sc1\n\ts_waitcnt vmcnt(0)" : "=&v"(r) : "v"(off), "s"(ws) : "memory")
#define DRAIN() asm volatile("s_waitcnt vmcnt(0)" ::: "memory")
#define CEX(a, b) { const uint32_t hi_ = max(a, b), lo_ = min(a, b); a = hi_; b = lo_; }
#define MMB(a_, b_, c_) __builtin_amdgcn_mfma_f32_16x16x32_bf16(__builtin_bit_cast(bf16x8, a_), __builtin_bit_cast(bf16x8, b_), c_, 0, 0, 0)
#define NTM 0
#define DPOL ""
#define DPOL2 ""
#define DPOL4 ""
constexpr int SPIN_CAP = 1 << 18;
constexpr uint32_t UMASK = 0x40074007u;
__device__ __forceinline__ uint32_t utag(uint32_t T, int j) {
  return ((T >> (3 * j)) & 0x00070007u) | (((j <= 2) ? (T << (2 - j)) : (T >> (j - 2))) & 0x40004000u);
}
__device__ __forceinline__ uint32_t max4(const u32x4& a) { return max(max(a[0], a[1]), max(a[2], a[3])); }
#ifndef ULONG
#define ULONG 8
#endif
__device__ __forceinline__ void until(uint32_t t) {
#pragma unroll 1
  for (int spin = 0; spin < 600; ++spin) {
    const int d = (int)(t - (uint32_t)__builtin_amdgcn_s_memrealtime());
    if (d <= 0) break;
    if (ULONG && d > 4 * ULONG) __builtin_amdgcn_s_sleep(ULONG); else __builtin_amdgcn_s_sleep(1);
  }
}
__device__ __forceinline__ uint32_t latest(uint32_t t) {
  const uint32_t t0 = (uint32_t)__builtin_amdgcn_readfirstlane(t) - 0x40000000u;
  return wave_maxu(t - t0) + t0;
}
__device__ __forceinline__ uint32_t gate(const char* ws, uint32_t po, uint32_t T) {
  u32x2 pv;
#pragma unroll 1
  for (int spin = 0; spin < SPIN_CAP; ++spin) {
    asm volatile("global_load_dwordx2 %0, %1, %2 sc1\n\ts_waitcnt vmcnt(0)" : "=&v"(pv) : "v"(po), "s"(ws) : "memory");
    if (!__any(pv[0] != T)) break;
    __builtin_amdgcn_s_sleep(2);
  }
  return pv[1];
}
__device__ __forceinline__ uint32_t gate2(const char* ws, uint32_t oa, uint32_t da, uint32_t ob, uint32_t T) {
  u32x2 pa, pb;
#pragma unroll 1
  for (int spin = 0; spin < SPIN_CAP; ++spin) {
    asm volatile("global_load_dwordx2 %0, %2, %4 sc1\n\tglobal_load_dwordx2 %1, %3, %4 sc1\n\ts_waitcnt vmcnt(0)" : "=&v"(pa), "=&v"(pb) : "v"(oa), "v"(ob), "s"(ws) : "memory");
    if (!__any(pa[0] != T || pb[0] != T)) break;
    __builtin_amdgcn_s_sleep(4);
  }
  pa[1] += da;
  return latest(((int)(pa[1] - pb[1]) > 0) ? pa[1] : pb[1]);
}
__device__ __forceinline__ uint32_t ahead(uint32_t c0, uint32_t t0, int dt) {
  const uint32_t c = (uint32_t)__builtin_amdgcn_s_memtime(), t = (uint32_t)__builtin_amdgcn_s_memrealtime();
  return t + (uint32_t)((float)((t - t0) * (uint32_t)(20 * dt)) * __builtin_amdgcn_rcpf((float)(c - c0)));
}
__device__ __forceinline__ void gsync(uint32_t a, uint32_t target) {
  uint32_t t, c, n = SPIN_CAP;
  uint64_t ex;
  asm volatile("s_waitcnt lgkmcnt(0)\n\ts_mov_b64 %3, exec\n\ts_mov_b64 exec, 1\n\tv_mov_b32 %0, 1\n\tds_add_u32 %4, %0\n\ts_mov_b64 exec, %3\n"
               "1:\n\ts_sleep 1\n\tds_read_b32 %0, %4\n\ts_sub_u32 %2, %2, 1\n\ts_cbranch_scc1 2f\n\ts_waitcnt lgkmcnt(0)\n\tv_readfirstlane_b32 %1, %0\n\t"
               "s_cmp_lt_u32 %1, %5\n\ts_cbranch_scc1 1b\n2:\n\ts_waitcnt lgkmcnt(0)"
               : "=&v"(t), "=&s"(c), "+s"(n), "=&s"(ex) : "v"(a), "n"(target) : "memory", "scc");
}
__device__ __forceinline__ void gsync0(uint32_t a, uint32_t target) {
  uint32_t t, c, n = SPIN_CAP;
  asm volatile("s_waitcnt lgkmcnt(0)\n\ts_mov_b64 exec, 1\n\tv_mov_b32 %0, 1\n\tds_add_u32 %3, %0\n\ts_mov_b64 exec, -1\n"
               "1:\n\tds_read_b32 %0, %3\n\ts_sub_u32 %2, %2, 1\n\ts_cbranch_scc1 2f\n\ts_waitcnt lgkmcnt(0)\n\tv_readfirstlane_b32 %1, %0\n\t"
               "s_cmp_lt_u32 %1, %4\n\ts_cbranch_scc1 1b\n2:\n\ts_waitcnt lgkmcnt(0)"
               : "=&v"(t), "=&s"(c), "+s"(n) : "v"(a), "n"(target) : "memory", "scc");
}
__device__ __forceinline__ void gwait(uint32_t a, uint32_t target) {
  uint32_t t, c, n = SPIN_CAP;
  asm volatile("1:\n\ts_sleep 2\n\tds_read_b32 %0, %3\n\ts_sub_u32 %2, %2, 1\n\ts_cbranch_scc1 2f\n\ts_waitcnt lgkmcnt(0)\n\tv_readfirstlane_b32 %1, %0\n\t"
               "s_cmp_lt_u32 %1, %4\n\ts_cbranch_scc1 1b\n2:\n\ts_waitcnt lgkmcnt(0)"
               : "=&v"(t), "=&s"(c), "+s"(n) : "v"(a), "n"(target) : "memory", "scc");
}
__device__ __forceinline__ void gwait0(uint32_t a, uint32_t target) {
  uint32_t t, c, n = SPIN_CAP;
  asm volatile("1:\n\tds_read_b32 %0, %3\n\ts_sub_u32 %2, %2, 1\n\ts_cbranch_scc1 2f\n\ts_waitcnt lgkmcnt(0)\n\tv_readfirstlane_b32 %1, %0\n\t"
               "s_cmp_lt_u32 %1, %4\n\ts_cbranch_scc1 1b\n2:\n\ts_waitcnt lgkmcnt(0)"
               : "=&v"(t), "=&s"(c), "+s"(n) : "v"(a), "n"(target) : "memory", "scc");
}
__device__ __forceinline__ void dma16(const void* base, uint32_t voff, uint32_t lds) {
  uint32_t keep;
  asm volatile("s_mov_b32 %0, m0\n\ts_mov_b32 m0, %3\n\ts_nop 0\n\tglobal_load_lds_dwordx4 %1, %2" DPOL "\n\ts_mov_b32 m0, %0" : "=&s"(keep) : "v"(voff), "s"(base), "s"(lds) : "memory");
}
__device__ __forceinline__ void dma16d(const void* base, uint32_t voff, uint32_t lds) {
  uint32_t keep;
  asm volatile("s_mov_b32 %0, m0\n\ts_mov_b32 m0, %3\n\ts_nop 0\n\tglobal_load_lds_dwordx4 %1, %2" DPOL2 "\n\ts_mov_b32 m0, %0" : "=&s"(keep) : "v"(voff), "s"(base), "s"(lds) : "memory");
}
__device__ __forceinline__ void nap(int n) {
  for (; n > 15; n -= 15) __builtin_amdgcn_s_sleep(15);
  switch (n) {
    case 1: __builtin_amdgcn_s_sleep(1); break; case 2: __builtin_amdgcn_s_sleep(2); break; case 3: __builtin_amdgcn_s_sleep(3); break;
    case 4: __builtin_amdgcn_s_sleep(4); break; case 5: __builtin_amdgcn_s_sleep(5); break; case 6: __builtin_amdgcn_s_sleep(6); break;
    case 7: __builtin_amdgcn_s_sleep(7); break; case 8: __builtin_amdgcn_s_sleep(8); break; case 9: __builtin_amdgcn_s_sleep(9); break;
    case 10: __builtin_amdgcn_s_sleep(10); break; case 11: __builtin_amdgcn_s_sleep(11); break; case 12: __builtin_amdgcn_s_sleep(12); break;
    case 13: __builtin_amdgcn_s_sleep(13); break; case 14: __builtin_amdgcn_s_sleep(14); break; case 15: __builtin_amdgcn_s_sleep(15); break;
    default: break;
  }
}
#define SLEEP(n) do { if ((n) > 0) __builtin_amdgcn_s_sleep(n); } while (0)
#define USLEEP 3
#define SSLEEP 11
#define PSLEEP 2
#define PPRIO 0
#define LFIRST 1
#define WD 40
#define OSLEEP 12
#define QPRIO 1
#define KPRIO 2
#define GSLEEP 0
#define NSLEEP 0
#define UD 40
#define XD 0
#define RSLEEP 0
#define UPRIO 2
#define VD2 372
#define WDEPTH 6
#define ESLEEP 0
#define SD 58
#define KD 180
#define OD 0
#define KBACK 6
#define DDEPTH 6
#define C3 1
#define XROT 2
#define WSOFF 0
#define KN 1
#define RPF 1
#define LSLEEP 1
#define TSTOP 78
#define HSTOP 110
#define VEARLY 0ull
#define FPR 3
#define SPR 3
#define DPRIO 0
#define QPRIO2 3
#define HW 2
#define RD 57
#define OPOS 1
#define OD2 6
#define NA 12
#define RD2 88
#define ORD1 ((unsigned __int128)0x321087654u << 36 | 0x321087654ull)
#define WDEPTH2 5
#define RQ NA
#define HREV 1
#define QPRIO0 3
#define VPS 1
#define RW 0
#define NPRIO 0
#define XSL 10
#define V2D 24
#define N0 8
#define LPOS 1
#define ALLP 1
#define OTP 1
#define OPOL ""
#define STP 1
#define FD 208
#define VDB 370
#define FORCESLOW 0
#define FORCEFB 0
#define CONV_ASM \
  "s_cmp_eq_u32 s89, 1\ns_cbranch_scc1 1f\ns_cmp_gt_u32 s89, 1\ns_cbranch_scc1 9f\nds_read_b64 v[124:125], v3 offset:576\n" \
  "ds_read_b64 v[126:127], v3 offset:584\nds_read_b64 v[120:121], v3 offset:64\nds_read_b64 v[122:123], v3 offset:72\n" \
  "ds_read_b64 v[116:117], v3 offset:640\nds_read_b64 v[118:119], v3 offset:648\nds_read_b64 v[112:113], v3 offset:128\n" \
  "ds_read_b64 v[114:115], v3 offset:136\nds_read_b64 v[108:109], v3 offset:704\nds_read_b64 v[110:111], v3 offset:712\n" \
  "ds_read_b64 v[104:105], v3 offset:192\nds_read_b64 v[106:107], v3 offset:200\nds_read_b64 v[100:101], v3 offset:768\n" \
  "ds_read_b64 v[102:103], v3 offset:776\nds_read_b64 v[96:97], v3 offset:256\nds_read_b64 v[98:99], v3 offset:264\n" \
  "ds_read_b64 v[92:93], v3 offset:832\nds_read_b64 v[94:95], v3 offset:840\nds_read_b64 v[88:89], v3 offset:320\n" \
  "ds_read_b64 v[90:91], v3 offset:328\nds_read_b64 v[84:85], v3 offset:896\nds_read_b64 v[86:87], v3 offset:904\n" \
  "ds_read_b64 v[80:81], v3 offset:384\nds_read_b64 v[82:83], v3 offset:392\nds_read_b64 v[76:77], v3 offset:960\n" \
  "ds_read_b64 v[78:79], v3 offset:968\nds_read_b64 v[72:73], v3 offset:448\nds_read_b64 v[74:75], v3 offset:456\n" \
  "ds_read_b64 v[68:69], v3 offset:1024\nds_read_b64 v[70:71], v3 offset:1032\nds_read_b64 v[64:65], v3 offset:512\n" \
  "ds_read_b64 v[66:67], v3 offset:520\nds_read_b64 v[60:61], v3 offset:0\nds_read_b64 v[62:63], v3 offset:8\nds_read_b64 v[56:57], v3 offset:1088\n" \
  "ds_read_b64 v[58:59], v3 offset:1096\ns_waitcnt lgkmcnt(0)\ns_barrier\nds_read_b128 v[36:39], v2 offset:0\nds_read_b128 v[40:43], v2 offset:64\n" \
  "ds_read_b128 v[44:47], v2 offset:128\nds_read_b128 v[48:51], v2 offset:192\ns_waitcnt lgkmcnt(3)\n" \
  "v_mfma_f32_16x16x32_f16 v[12:15], v[124:127], v[36:39], 0\ns_setprio 1\nds_read_b128 v[52:55], v2 offset:256\n" \
  "v_mfma_f32_16x16x32_f16 v[16:19], v[120:123], v[36:39], 0\ns_waitcnt lgkmcnt(3)\nv_mfma_f32_16x16x32_f16 v[12:15], v[116:119], v[40:43], v[12:15]\n" \
  "ds_read_b128 v[36:39], v2 offset:320\nv_mfma_f32_16x16x32_f16 v[16:19], v[112:115], v[40:43], v[16:19]\ns_waitcnt lgkmcnt(3)\n" \
  "v_mfma_f32_16x16x32_f16 v[12:15], v[108:111], v[44:47], v[12:15]\nds_read_b128 v[40:43], v2 offset:384\n" \
  "v_mfma_f32_16x16x32_f16 v[16:19], v[104:107], v[44:47], v[16:19]\ns_waitcnt lgkmcnt(3)\n" \
  "v_mfma_f32_16x16x32_f16 v[12:15], v[100:103], v[48:51], v[12:15]\nds_read_b128 v[44:47], v2 offset:448\n" \
  "v_mfma_f32_16x16x32_f16 v[16:19], v[96:99], v[48:51], v[16:19]\ns_waitcnt lgkmcnt(3)\n" \
  "v_mfma_f32_16x16x32_f16 v[12:15], v[92:95], v[52:55], v[12:15]\nv_mfma_f32_16x16x32_f16 v[16:19], v[88:91], v[52:55], v[16:19]\n" \
  "s_waitcnt lgkmcnt(2)\nv_mfma_f32_16x16x32_f16 v[12:15], v[84:87], v[36:39], v[12:15]\nds_read_b128 v[48:51], v2 offset:512\n" \
  "v_mfma_f32_16x16x32_f16 v[16:19], v[80:83], v[36:39], v[16:19]\ns_waitcnt lgkmcnt(2)\n" \
  "v_mfma_f32_16x16x32_f16 v[12:15], v[76:79], v[40:43], v[12:15]\nds_read_b128 v[52:55], v2 offset:576\n" \
  "v_mfma_f32_16x16x32_f16 v[16:19], v[72:75], v[40:43], v[16:19]\ns_waitcnt lgkmcnt(2)\n" \
  "v_mfma_f32_16x16x32_f16 v[12:15], v[68:71], v[44:47], v[12:15]\nds_read_b128 v[36:39], v2 offset:640\n" \
  "v_mfma_f32_16x16x32_f16 v[16:19], v[64:67], v[44:47], v[16:19]\nv_mfma_f32_16x16x32_f16 v[20:23], v[60:63], v[44:47], 0\n" \
  "ds_read_b128 v[40:43], v2 offset:704\nv_mfma_f32_16x16x32_f16 v[28:31], v[56:59], v[44:47], 0\ns_waitcnt lgkmcnt(3)\n" \
  "v_mfma_f32_16x16x32_f16 v[16:19], v[124:127], v[48:51], v[16:19]\nds_read_b128 v[56:59], v2 offset:768\nv_cvt_pkrtz_f16_f32 v32, v12, v13\n" \
  "v_mfma_f32_16x16x32_f16 v[20:23], v[120:123], v[48:51], v[20:23]\nv_cvt_pkrtz_f16_f32 v33, v14, v15\ns_waitcnt lgkmcnt(3)\n" \
  "v_mfma_f32_16x16x32_f16 v[16:19], v[116:119], v[52:55], v[16:19]\nds_read_b128 v[44:47], v2 offset:832\n" \
  "v_pk_add_u16 v32, v32, 4 op_sel_hi:[1,0] clamp\nv_mfma_f32_16x16x32_f16 v[20:23], v[112:115], v[52:55], v[20:23]\n" \
  "v_pk_add_u16 v33, v33, 4 op_sel_hi:[1,0] clamp\ns_waitcnt lgkmcnt(3)\nv_mfma_f32_16x16x32_f16 v[16:19], v[108:111], v[36:39], v[16:19]\n" \
  "ds_read_b128 v[48:51], v2 offset:896\nds_write_b64 v4, v[32:33] offset:0\nv_mfma_f32_16x16x32_f16 v[20:23], v[104:107], v[36:39], v[20:23]\n" \
  "s_waitcnt lgkmcnt(3)\nv_mfma_f32_16x16x32_f16 v[16:19], v[100:103], v[40:43], v[16:19]\nds_read_b128 v[52:55], v2 offset:960\n" \
  "v_mfma_f32_16x16x32_f16 v[20:23], v[96:99], v[40:43], v[20:23]\nv_mfma_f32_16x16x32_f16 v[16:19], v[92:95], v[56:59], v[16:19]\n" \
  "v_mfma_f32_16x16x32_f16 v[20:23], v[88:91], v[56:59], v[20:23]\nds_read_b128 v[36:39], v2 offset:1024\ns_waitcnt lgkmcnt(3)\n" \
  "v_mfma_f32_16x16x32_f16 v[16:19], v[84:87], v[44:47], v[16:19]\nv_mfma_f32_16x16x32_f16 v[20:23], v[80:83], v[44:47], v[20:23]\n" \
  "ds_read_b128 v[40:43], v2 offset:1088\nds_read_b64 v[56:57], v6 offset:256\nds_read_b64 v[58:59], v6 offset:264\n" \
  "v_mfma_f32_16x16x32_f16 v[16:19], v[76:79], v[48:51], v[16:19]\nv_mfma_f32_16x16x32_f16 v[20:23], v[72:75], v[48:51], v[20:23]\n" \
  "ds_read_b128 v[44:47], v2 offset:1152\ns_waitcnt lgkmcnt(4)\nv_mfma_f32_16x16x32_f16 v[16:19], v[68:71], v[52:55], v[16:19]\n" \
  "v_mfma_f32_16x16x32_f16 v[20:23], v[64:67], v[52:55], v[20:23]\nds_read_b128 v[48:51], v2 offset:1216\n" \
  "v_mfma_f32_16x16x32_f16 v[24:27], v[60:63], v[52:55], 0\nv_mfma_f32_16x16x32_f16 v[20:23], v[124:127], v[36:39], v[20:23]\n" \
  "ds_read_b128 v[52:55], v2 offset:1280\nv_mfma_f32_16x16x32_f16 v[24:27], v[120:123], v[36:39], v[24:27]\ns_waitcnt lgkmcnt(5)\n" \
  "v_mfma_f32_16x16x32_f16 v[20:23], v[116:119], v[40:43], v[20:23]\nds_read_b128 v[36:39], v2 offset:1344\nv_cvt_pkrtz_f16_f32 v32, v16, v17\n" \
  "v_mfma_f32_16x16x32_f16 v[24:27], v[112:115], v[40:43], v[24:27]\nv_cvt_pkrtz_f16_f32 v33, v18, v19\ns_waitcnt lgkmcnt(3)\n" \
  "v_mfma_f32_16x16x32_f16 v[20:23], v[108:111], v[44:47], v[20:23]\nds_read_b128 v[40:43], v2 offset:1408\n" \
  "v_pk_add_u16 v32, v32, 4 op_sel_hi:[1,0] clamp\nds_read_b32 v35, v9 offset:32\nv_mfma_f32_16x16x32_f16 v[24:27], v[104:107], v[44:47], v[24:27]\n" \
  "v_pk_add_u16 v33, v33, 4 op_sel_hi:[1,0] clamp\ns_waitcnt lgkmcnt(3)\nv_mfma_f32_16x16x32_f16 v[20:23], v[100:103], v[48:51], v[20:23]\n" \
  "ds_read_b128 v[44:47], v2 offset:1472\nds_write_b64 v4, v[32:33] offset:1024\nv_mfma_f32_16x16x32_f16 v[24:27], v[96:99], v[48:51], v[24:27]\n" \
  "v_mfma_f32_16x16x32_f16 v[20:23], v[92:95], v[52:55], v[20:23]\nv_mfma_f32_16x16x32_f16 v[24:27], v[88:91], v[52:55], v[24:27]\n" \
  "ds_read_b128 v[48:51], v2 offset:1536\ns_waitcnt lgkmcnt(3)\nv_readfirstlane_b32 s85, v35\ns_cmp_lt_u32 s85, 4\ns_cbranch_scc1 21f\n22:\n" \
  "v_mfma_f32_16x16x32_f16 v[20:23], v[84:87], v[36:39], v[20:23]\nv_mfma_f32_16x16x32_f16 v[24:27], v[80:83], v[36:39], v[24:27]\n" \
  "ds_read_b128 v[52:55], v2 offset:1600\nv_mfma_f32_16x16x32_f16 v[20:23], v[76:79], v[40:43], v[20:23]\n" \
  "v_mfma_f32_16x16x32_f16 v[24:27], v[72:75], v[40:43], v[24:27]\nds_read_b128 v[36:39], v2 offset:1664\ns_waitcnt lgkmcnt(4)\n" \
  "v_mfma_f32_16x16x32_f16 v[20:23], v[68:71], v[44:47], v[20:23]\nv_mfma_f32_16x16x32_f16 v[24:27], v[64:67], v[44:47], v[24:27]\n" \
  "ds_read_b128 v[40:43], v2 offset:1728\nv_mfma_f32_16x16x32_f16 v[28:31], v[60:63], v[44:47], v[28:31]\nds_read_b64_tr_b16 v[60:61], v5 offset:0\n" \
  "ds_read_b64_tr_b16 v[62:63], v5 offset:32\ns_waitcnt lgkmcnt(4)\nv_mfma_f32_16x16x32_f16 v[24:27], v[124:127], v[48:51], v[24:27]\n" \
  "v_mfma_f32_16x16x32_f16 v[28:31], v[120:123], v[48:51], v[28:31]\nds_read_b64_tr_b16 v[44:45], v5 offset:256\n" \
  "ds_read_b64_tr_b16 v[46:47], v5 offset:288\nv_mfma_f32_16x16x32_f16 v[24:27], v[116:119], v[52:55], v[24:27]\n" \
  "ds_read_b64 v[120:121], v6 offset:320\nds_read_b64 v[122:123], v6 offset:328\nv_mfma_f32_16x16x32_f16 v[28:31], v[112:115], v[52:55], v[28:31]\n" \
  "ds_read_b128 v[48:51], v2 offset:1792\ns_waitcnt lgkmcnt(7)\nv_mfma_f32_16x16x32_f16 v[24:27], v[108:111], v[36:39], v[24:27]\n" \
  "ds_read_b128 v[112:115], v2 offset:1856\nv_mfma_f32_16x16x32_f16 v[28:31], v[104:107], v[36:39], v[28:31]\nds_read_b64 v[52:53], v6 offset:128\n" \
  "ds_read_b64 v[54:55], v6 offset:136\nv_mfma_f32_16x16x32_f16 v[24:27], v[100:103], v[40:43], v[24:27]\nds_read_b128 v[104:107], v2 offset:1920\n" \
  "v_mfma_f32_16x16x32_f16 v[28:31], v[96:99], v[40:43], v[28:31]\nds_read_b64 v[36:37], v6 offset:192\nds_read_b64 v[38:39], v6 offset:200\n" \
  "s_waitcnt lgkmcnt(9)\nv_mfma_f32_16x16x32_f16 v[20:23], v[60:63], v[56:59], v[20:23]\nds_read_b128 v[96:99], v2 offset:1984\ns_waitcnt lgkmcnt(7)\n" \
  "v_mfma_f32_16x16x32_f16 v[20:23], v[44:47], v[120:123], v[20:23]\nv_mfma_f32_16x16x32_f16 v[24:27], v[92:95], v[48:51], v[24:27]\n" \
  "ds_read_b128 v[40:43], v2 offset:2048\nv_mfma_f32_16x16x32_f16 v[28:31], v[88:91], v[48:51], v[28:31]\nds_read_b64_tr_b16 v[88:89], v5 offset:512\n" \
  "ds_read_b64_tr_b16 v[90:91], v5 offset:544\ns_waitcnt lgkmcnt(6)\nv_mfma_f32_16x16x32_f16 v[24:27], v[84:87], v[112:115], v[24:27]\n" \
  "v_mfma_f32_16x16x32_f16 v[28:31], v[80:83], v[112:115], v[28:31]\nv_cvt_pkrtz_f16_f32 v32, v20, v21\nv_cvt_pkrtz_f16_f32 v33, v22, v23\n" \
  "v_mfma_f32_16x16x32_f16 v[24:27], v[76:79], v[104:107], v[24:27]\nds_read_b64_tr_b16 v[48:49], v5 offset:768\n" \
  "ds_read_b64_tr_b16 v[50:51], v5 offset:800\nv_mfma_f32_16x16x32_f16 v[28:31], v[72:75], v[104:107], v[28:31]\n" \
  "ds_read_b128 v[80:83], v2 offset:2112\ns_waitcnt lgkmcnt(5)\nv_mfma_f32_16x16x32_f16 v[24:27], v[68:71], v[96:99], v[24:27]\n" \
  "ds_read_b128 v[112:115], v2 offset:2176\nv_pk_add_u16 v32, v32, 4 op_sel_hi:[1,0] clamp\n" \
  "v_mfma_f32_16x16x32_f16 v[28:31], v[64:67], v[96:99], v[28:31]\nds_read_b128 v[72:75], v2 offset:2240\n" \
  "v_pk_add_u16 v33, v33, 4 op_sel_hi:[1,0] clamp\nv_mfma_f32_16x16x32_f16 v[28:31], v[124:127], v[40:43], v[28:31]\n" \
  "ds_read_b128 v[104:107], v2 offset:2304\nds_write_b64 v4, v[32:33] offset:2048\nv_mfma_f32_16x16x32_f16 v[24:27], v[60:63], v[52:55], v[24:27]\n" \
  "ds_read_b128 v[64:67], v2 offset:2368\nds_read_b64 v[96:97], v6 offset:0\nds_read_b64 v[98:99], v6 offset:8\n" \
  "v_mfma_f32_16x16x32_f16 v[24:27], v[44:47], v[36:39], v[24:27]\nds_read_b128 v[124:127], v2 offset:2432\ns_waitcnt lgkmcnt(9)\n" \
  "v_mfma_f32_16x16x32_f16 v[24:27], v[88:91], v[56:59], v[24:27]\nds_read_b128 v[40:43], v2 offset:2496\n" \
  "v_mfma_f32_16x16x32_f16 v[24:27], v[48:51], v[120:123], v[24:27]\ns_waitcnt lgkmcnt(8)\n" \
  "v_mfma_f32_16x16x32_f16 v[28:31], v[116:119], v[80:83], v[28:31]\nds_read_b64 v[116:117], v6 offset:64\nds_read_b64 v[118:119], v6 offset:72\n" \
  "v_mfma_f32_16x16x32_f16 v[28:31], v[108:111], v[112:115], v[28:31]\nds_read_b64_tr_b16 v[80:81], v5 offset:1024\n" \
  "ds_read_b64_tr_b16 v[82:83], v5 offset:1056\ns_waitcnt lgkmcnt(10)\nv_mfma_f32_16x16x32_f16 v[28:31], v[100:103], v[72:75], v[28:31]\n" \
  "v_cvt_pkrtz_f16_f32 v32, v24, v25\nv_cvt_pkrtz_f16_f32 v33, v26, v27\nv_mfma_f32_16x16x32_f16 v[28:31], v[92:95], v[104:107], v[28:31]\n" \
  "v_pk_add_u16 v32, v32, 4 op_sel_hi:[1,0] clamp\ns_waitcnt lgkmcnt(5)\nv_mfma_f32_16x16x32_f16 v[28:31], v[84:87], v[64:67], v[28:31]\n" \
  "ds_read_b64_tr_b16 v[108:109], v5 offset:1280\nds_read_b64_tr_b16 v[110:111], v5 offset:1312\n" \
  "v_mfma_f32_16x16x32_f16 v[28:31], v[76:79], v[124:127], v[28:31]\nds_read_b64_tr_b16 v[112:113], v7 offset:0\n" \
  "ds_read_b64_tr_b16 v[114:115], v7 offset:64\ns_waitcnt lgkmcnt(6)\nv_mfma_f32_16x16x32_f16 v[28:31], v[68:71], v[40:43], v[28:31]\n" \
  "ds_read2_b32 v[100:101], v8 offset0:48 offset1:49\nds_read2_b32 v[102:103], v8 offset0:50 offset1:51\n" \
  "v_mfma_f32_16x16x32_f16 v[28:31], v[60:63], v[96:99], v[28:31]\nv_pk_add_u16 v33, v33, 4 op_sel_hi:[1,0] clamp\n" \
  "ds_write_b64 v4, v[32:33] offset:3072\nv_mfma_f32_16x16x32_f16 v[28:31], v[44:47], v[116:119], v[28:31]\n" \
  "v_mfma_f32_16x16x32_f16 v[28:31], v[88:91], v[52:55], v[28:31]\nv_mfma_f32_16x16x32_f16 v[28:31], v[48:51], v[36:39], v[28:31]\n" \
  "s_waitcnt lgkmcnt(5)\nv_mfma_f32_16x16x32_f16 v[28:31], v[80:83], v[56:59], v[28:31]\n" \
  "v_mfma_f32_16x16x32_f16 v[28:31], v[108:111], v[120:123], v[28:31]\ns_waitcnt lgkmcnt(1)\n" \
  "v_mfma_f32_16x16x32_f16 v[28:31], v[112:115], v[100:103], v[28:31]\ns_nop 7\nv_cvt_pkrtz_f16_f32 v32, v28, v29\nv_cvt_pkrtz_f16_f32 v33, v30, v31\n" \
  "v_pk_add_u16 v32, v32, 4 op_sel_hi:[1,0] clamp\nv_pk_add_u16 v33, v33, 4 op_sel_hi:[1,0] clamp\nds_write_b64 v4, v[32:33] offset:4096\n" \
  "s_setprio 0\ns_setpc_b64 s[90:91]\n21:\ns_movk_i32 s84, 0x4000\n23:\nds_read_b32 v35, v9 offset:32\ns_sub_u32 s84, s84, 1\ns_cbranch_scc1 22b\n" \
  "s_waitcnt lgkmcnt(0)\nv_readfirstlane_b32 s85, v35\ns_cmp_lt_u32 s85, 4\ns_cbranch_scc1 23b\ns_branch 22b\n1:\n" \
  "ds_read_b64 v[124:125], v3 offset:1088\nds_read_b64 v[126:127], v3 offset:1096\nds_read_b64 v[120:121], v3 offset:0\n" \
  "ds_read_b64 v[122:123], v3 offset:8\nds_read_b64 v[116:117], v3 offset:64\nds_read_b64 v[118:119], v3 offset:72\n" \
  "ds_read_b64 v[112:113], v3 offset:128\nds_read_b64 v[114:115], v3 offset:136\nds_read_b64 v[108:109], v3 offset:192\n" \
  "ds_read_b64 v[110:111], v3 offset:200\nds_read_b64 v[104:105], v3 offset:256\nds_read_b64 v[106:107], v3 offset:264\n" \
  "ds_read_b64 v[100:101], v3 offset:320\nds_read_b64 v[102:103], v3 offset:328\nds_read_b64 v[96:97], v3 offset:384\n" \
  "ds_read_b64 v[98:99], v3 offset:392\nds_read_b64 v[92:93], v3 offset:448\nds_read_b64 v[94:95], v3 offset:456\n" \
  "ds_read_b64 v[88:89], v3 offset:512\nds_read_b64 v[90:91], v3 offset:520\nds_read_b64 v[84:85], v3 offset:576\n" \
  "ds_read_b64 v[86:87], v3 offset:584\nds_read_b64 v[80:81], v3 offset:640\nds_read_b64 v[82:83], v3 offset:648\n" \
  "ds_read_b64 v[76:77], v3 offset:704\nds_read_b64 v[78:79], v3 offset:712\nds_read_b64 v[72:73], v3 offset:768\n" \
  "ds_read_b64 v[74:75], v3 offset:776\nds_read_b64 v[68:69], v3 offset:832\nds_read_b64 v[70:71], v3 offset:840\n" \
  "ds_read_b64 v[64:65], v3 offset:896\nds_read_b64 v[66:67], v3 offset:904\nds_read_b64 v[60:61], v3 offset:960\n" \
  "ds_read_b64 v[62:63], v3 offset:968\nds_read_b64 v[56:57], v3 offset:1024\nds_read_b64 v[58:59], v3 offset:1032\n" \
  "ds_read_b64 v[52:53], v6 offset:0\nds_read_b64 v[54:55], v6 offset:8\nds_read_b64 v[48:49], v6 offset:64\nds_read_b64 v[50:51], v6 offset:72\n" \
  "s_waitcnt lgkmcnt(0)\ns_barrier\nds_read_b128 v[28:31], v2 offset:960\nds_read_b128 v[32:35], v2 offset:1472\n" \
  "ds_read_b128 v[36:39], v2 offset:1984\nds_read_b128 v[40:43], v2 offset:2048\nds_read_b128 v[44:47], v2 offset:2112\ns_waitcnt lgkmcnt(3)\n" \
  "v_mfma_f32_16x16x32_f16 v[12:15], v[124:127], v[28:31], 0\ns_setprio 1\nds_read_b128 v[28:31], v2 offset:2176\n" \
  "v_mfma_f32_16x16x32_f16 v[16:19], v[124:127], v[32:35], 0\nds_read_b128 v[32:35], v2 offset:2240\ns_waitcnt lgkmcnt(3)\n" \
  "v_mfma_f32_16x16x32_f16 v[12:15], v[120:123], v[36:39], v[12:15]\nv_mfma_f32_16x16x32_f16 v[20:23], v[124:127], v[36:39], 0\n" \
  "ds_read_b128 v[124:127], v2 offset:2304\nds_read_b128 v[36:39], v2 offset:2368\nv_mfma_f32_16x16x32_f16 v[12:15], v[116:119], v[40:43], v[12:15]\n" \
  "ds_read_b128 v[40:43], v2 offset:2432\ns_waitcnt lgkmcnt(4)\nv_mfma_f32_16x16x32_f16 v[12:15], v[112:115], v[44:47], v[12:15]\n" \
  "ds_read_b128 v[44:47], v2 offset:2496\nv_mfma_f32_16x16x32_f16 v[12:15], v[108:111], v[28:31], v[12:15]\nds_read_b128 v[28:31], v2 offset:2560\n" \
  "s_waitcnt lgkmcnt(4)\nv_mfma_f32_16x16x32_f16 v[12:15], v[104:107], v[32:35], v[12:15]\nds_read_b128 v[32:35], v2 offset:2624\n" \
  "v_mfma_f32_16x16x32_f16 v[12:15], v[100:103], v[124:127], v[12:15]\ns_waitcnt lgkmcnt(3)\n" \
  "v_mfma_f32_16x16x32_f16 v[12:15], v[96:99], v[36:39], v[12:15]\nds_read_b128 v[124:127], v2 offset:2688\n" \
  "v_mfma_f32_16x16x32_f16 v[12:15], v[92:95], v[40:43], v[12:15]\ns_waitcnt lgkmcnt(3)\n" \
  "v_mfma_f32_16x16x32_f16 v[12:15], v[88:91], v[44:47], v[12:15]\nds_read_b128 v[36:39], v2 offset:2752\n" \
  "v_mfma_f32_16x16x32_f16 v[16:19], v[120:123], v[44:47], v[16:19]\ns_waitcnt lgkmcnt(3)\n" \
  "v_mfma_f32_16x16x32_f16 v[12:15], v[84:87], v[28:31], v[12:15]\nds_read_b128 v[40:43], v2 offset:2816\n" \
  "v_mfma_f32_16x16x32_f16 v[16:19], v[116:119], v[28:31], v[16:19]\ns_waitcnt lgkmcnt(3)\n" \
  "v_mfma_f32_16x16x32_f16 v[12:15], v[80:83], v[32:35], v[12:15]\nds_read_b128 v[44:47], v2 offset:2880\n" \
  "v_mfma_f32_16x16x32_f16 v[16:19], v[112:115], v[32:35], v[16:19]\ns_waitcnt lgkmcnt(3)\n" \
  "v_mfma_f32_16x16x32_f16 v[12:15], v[76:79], v[124:127], v[12:15]\nds_read_b128 v[28:31], v2 offset:2944\n" \
  "v_mfma_f32_16x16x32_f16 v[16:19], v[108:111], v[124:127], v[16:19]\ns_waitcnt lgkmcnt(3)\n" \
  "v_mfma_f32_16x16x32_f16 v[12:15], v[72:75], v[36:39], v[12:15]\nds_read_b128 v[32:35], v2 offset:3008\n" \
  "v_mfma_f32_16x16x32_f16 v[16:19], v[104:107], v[36:39], v[16:19]\ns_waitcnt lgkmcnt(3)\n" \
  "v_mfma_f32_16x16x32_f16 v[12:15], v[68:71], v[40:43], v[12:15]\nv_mfma_f32_16x16x32_f16 v[16:19], v[100:103], v[40:43], v[16:19]\n" \
  "ds_read_b128 v[124:127], v2 offset:3072\ns_waitcnt lgkmcnt(3)\nv_mfma_f32_16x16x32_f16 v[12:15], v[64:67], v[44:47], v[12:15]\n" \
  "v_mfma_f32_16x16x32_f16 v[16:19], v[96:99], v[44:47], v[16:19]\nds_read_b128 v[36:39], v2 offset:3136\ns_waitcnt lgkmcnt(3)\n" \
  "v_mfma_f32_16x16x32_f16 v[12:15], v[60:63], v[28:31], v[12:15]\nds_read_b64 v[40:41], v6 offset:128\nds_read_b64 v[42:43], v6 offset:136\n" \
  "v_mfma_f32_16x16x32_f16 v[16:19], v[92:95], v[28:31], v[16:19]\nds_read_b128 v[44:47], v2 offset:3200\ns_waitcnt lgkmcnt(4)\n" \
  "v_mfma_f32_16x16x32_f16 v[12:15], v[56:59], v[32:35], v[12:15]\nv_mfma_f32_16x16x32_f16 v[16:19], v[88:91], v[32:35], v[16:19]\n" \
  "ds_read_b128 v[28:31], v2 offset:3264\nv_mfma_f32_16x16x32_f16 v[20:23], v[120:123], v[32:35], v[20:23]\nds_read_b64 v[120:121], v6 offset:192\n" \
  "ds_read_b64 v[122:123], v6 offset:200\nv_mfma_f32_16x16x32_f16 v[16:19], v[84:87], v[124:127], v[16:19]\nds_read_b128 v[32:35], v2 offset:3328\n" \
  "v_mfma_f32_16x16x32_f16 v[20:23], v[116:119], v[124:127], v[20:23]\nds_read_b64 v[116:117], v6 offset:256\nds_read_b64 v[118:119], v6 offset:264\n" \
  "s_waitcnt lgkmcnt(6)\nv_mfma_f32_16x16x32_f16 v[16:19], v[80:83], v[36:39], v[16:19]\nds_read_b128 v[124:127], v2 offset:3392\n" \
  "v_mfma_f32_16x16x32_f16 v[20:23], v[112:115], v[36:39], v[20:23]\nds_read_b64 v[112:113], v6 offset:320\nds_read_b64 v[114:115], v6 offset:328\n" \
  "v_mfma_f32_16x16x32_f16 v[16:19], v[76:79], v[44:47], v[16:19]\nds_read_b128 v[36:39], v2 offset:3456\n" \
  "v_mfma_f32_16x16x32_f16 v[20:23], v[108:111], v[44:47], v[20:23]\ns_waitcnt lgkmcnt(6)\n" \
  "v_mfma_f32_16x16x32_f16 v[16:19], v[72:75], v[28:31], v[16:19]\nds_read_b128 v[108:111], v2 offset:3520\n" \
  "v_mfma_f32_16x16x32_f16 v[20:23], v[104:107], v[28:31], v[20:23]\nv_mfma_f32_16x16x32_f16 v[16:19], v[68:71], v[32:35], v[16:19]\n" \
  "ds_read_b128 v[44:47], v2 offset:3584\nv_mfma_f32_16x16x32_f16 v[20:23], v[100:103], v[32:35], v[20:23]\nds_read_b128 v[104:107], v2 offset:3648\n" \
  "ds_read_b32 v27, v9 offset:32\ns_waitcnt lgkmcnt(4)\nv_mfma_f32_16x16x32_f16 v[16:19], v[64:67], v[124:127], v[16:19]\n" \
  "ds_read_b128 v[28:31], v2 offset:3712\nv_mfma_f32_16x16x32_f16 v[20:23], v[96:99], v[124:127], v[20:23]\nds_read_b128 v[100:103], v2 offset:3776\n" \
  "ds_read2_b32 v[32:33], v8 offset0:32 offset1:33\nds_read2_b32 v[34:35], v8 offset0:34 offset1:35\n" \
  "v_mfma_f32_16x16x32_f16 v[16:19], v[60:63], v[36:39], v[16:19]\nds_read_b128 v[96:99], v2 offset:3840\n" \
  "v_mfma_f32_16x16x32_f16 v[20:23], v[92:95], v[36:39], v[20:23]\nds_read_b128 v[124:127], v2 offset:3904\ns_waitcnt lgkmcnt(8)\n" \
  "v_mfma_f32_16x16x32_f16 v[16:19], v[56:59], v[108:111], v[16:19]\nds_read_b128 v[92:95], v2 offset:3968\ns_waitcnt lgkmcnt(7)\n" \
  "v_readfirstlane_b32 s85, v27\ns_cmp_lt_u32 s85, 4\ns_cbranch_scc1 31f\n32:\nv_mfma_f32_16x16x32_f16 v[20:23], v[88:91], v[108:111], v[20:23]\n" \
  "ds_read_b128 v[36:39], v2 offset:4032\nds_read_b64_tr_b16 v[88:89], v5 offset:512\nds_read_b64_tr_b16 v[90:91], v5 offset:544\n" \
  "v_mfma_f32_16x16x32_f16 v[20:23], v[84:87], v[44:47], v[20:23]\nds_read_b64_tr_b16 v[108:109], v5 offset:768\n" \
  "ds_read_b64_tr_b16 v[110:111], v5 offset:800\nv_mfma_f32_16x16x32_f16 v[20:23], v[80:83], v[104:107], v[20:23]\n" \
  "ds_read_b64_tr_b16 v[84:85], v5 offset:1024\nds_read_b64_tr_b16 v[86:87], v5 offset:1056\ns_waitcnt lgkmcnt(12)\n" \
  "v_mfma_f32_16x16x32_f16 v[20:23], v[76:79], v[28:31], v[20:23]\nds_read_b64_tr_b16 v[44:45], v5 offset:1280\n" \
  "ds_read_b64_tr_b16 v[46:47], v5 offset:1312\nv_mfma_f32_16x16x32_f16 v[20:23], v[72:75], v[100:103], v[20:23]\ns_waitcnt lgkmcnt(10)\n" \
  "v_mfma_f32_16x16x32_f16 v[20:23], v[68:71], v[96:99], v[20:23]\nds_read_b64_tr_b16 v[80:81], v5 offset:1536\n" \
  "ds_read_b64_tr_b16 v[82:83], v5 offset:1568\nv_mfma_f32_16x16x32_f16 v[20:23], v[64:67], v[124:127], v[20:23]\n" \
  "ds_read_b64_tr_b16 v[104:105], v5 offset:1792\nds_read_b64_tr_b16 v[106:107], v5 offset:1824\ns_waitcnt lgkmcnt(12)\n" \
  "v_mfma_f32_16x16x32_f16 v[20:23], v[60:63], v[92:95], v[20:23]\nds_read_b64_tr_b16 v[76:77], v5 offset:2048\n" \
  "ds_read_b64_tr_b16 v[78:79], v5 offset:2080\nv_mfma_f32_16x16x32_f16 v[20:23], v[56:59], v[36:39], v[20:23]\ns_waitcnt lgkmcnt(10)\n" \
  "v_mfma_f32_16x16x32_f16 v[12:15], v[88:91], v[52:55], v[12:15]\nds_read_b64_tr_b16 v[28:29], v5 offset:2304\n" \
  "ds_read_b64_tr_b16 v[30:31], v5 offset:2336\nv_mfma_f32_16x16x32_f16 v[12:15], v[108:111], v[48:51], v[12:15]\n" \
  "ds_read_b64_tr_b16 v[72:73], v5 offset:2560\nds_read_b64_tr_b16 v[74:75], v5 offset:2592\ns_waitcnt lgkmcnt(10)\n" \
  "v_mfma_f32_16x16x32_f16 v[12:15], v[84:87], v[40:43], v[12:15]\nds_read_b64_tr_b16 v[100:101], v5 offset:2816\n" \
  "ds_read_b64_tr_b16 v[102:103], v5 offset:2848\nv_mfma_f32_16x16x32_f16 v[16:19], v[84:87], v[52:55], v[16:19]\n" \
  "ds_read_b64_tr_b16 v[68:69], v7 offset:0\nds_read_b64_tr_b16 v[70:71], v7 offset:64\n" \
  "v_mfma_f32_16x16x32_f16 v[12:15], v[44:47], v[120:123], v[12:15]\nv_mfma_f32_16x16x32_f16 v[16:19], v[44:47], v[48:51], v[16:19]\n" \
  "s_waitcnt lgkmcnt(10)\nv_mfma_f32_16x16x32_f16 v[12:15], v[80:83], v[116:119], v[12:15]\nds_read2_b32 v[96:97], v8 offset0:16 offset1:17\n" \
  "ds_read2_b32 v[98:99], v8 offset0:18 offset1:19\nv_mfma_f32_16x16x32_f16 v[16:19], v[80:83], v[40:43], v[16:19]\n" \
  "ds_read2_b32 v[64:65], v8 offset0:0 offset1:1\nds_read2_b32 v[66:67], v8 offset0:2 offset1:3\n" \
  "v_mfma_f32_16x16x32_f16 v[20:23], v[80:83], v[52:55], v[20:23]\nv_mfma_f32_16x16x32_f16 v[12:15], v[104:107], v[112:115], v[12:15]\n" \
  "v_mfma_f32_16x16x32_f16 v[16:19], v[104:107], v[120:123], v[16:19]\nv_mfma_f32_16x16x32_f16 v[20:23], v[104:107], v[48:51], v[20:23]\n" \
  "s_waitcnt lgkmcnt(10)\nv_mfma_f32_16x16x32_f16 v[16:19], v[76:79], v[116:119], v[16:19]\nds_read_b64_tr_b16 v[124:125], v7 offset:512\n" \
  "ds_read_b64_tr_b16 v[126:127], v7 offset:576\nv_mfma_f32_16x16x32_f16 v[20:23], v[76:79], v[40:43], v[20:23]\n" \
  "ds_read2_b32 v[60:61], v8 offset0:48 offset1:49\nds_read2_b32 v[62:63], v8 offset0:50 offset1:51\n" \
  "v_mfma_f32_16x16x32_f16 v[16:19], v[28:31], v[112:115], v[16:19]\nv_mfma_f32_16x16x32_f16 v[20:23], v[28:31], v[120:123], v[20:23]\n" \
  "s_waitcnt lgkmcnt(10)\nv_mfma_f32_16x16x32_f16 v[20:23], v[72:75], v[116:119], v[20:23]\nds_read_b64_tr_b16 v[92:93], v7 offset:1024\n" \
  "ds_read_b64_tr_b16 v[94:95], v7 offset:1088\nv_mfma_f32_16x16x32_f16 v[20:23], v[100:103], v[112:115], v[20:23]\n" \
  "ds_read_b64_tr_b16 v[56:57], v7 offset:1536\nds_read_b64_tr_b16 v[58:59], v7 offset:1600\ns_waitcnt lgkmcnt(10)\n" \
  "v_mfma_f32_16x16x32_f16 v[12:15], v[68:71], v[32:35], v[12:15]\nv_mfma_f32_16x16x32_f16 v[16:19], v[68:71], v[96:99], v[16:19]\n" \
  "s_waitcnt lgkmcnt(6)\nv_mfma_f32_16x16x32_f16 v[20:23], v[68:71], v[64:67], v[20:23]\ns_waitcnt lgkmcnt(2)\n" \
  "v_mfma_f32_16x16x32_f16 v[12:15], v[124:127], v[60:63], v[12:15]\nv_mfma_f32_16x16x32_f16 v[16:19], v[124:127], v[32:35], v[16:19]\n" \
  "v_mfma_f32_16x16x32_f16 v[20:23], v[124:127], v[96:99], v[20:23]\nv_mfma_f32_16x16x32_f16 v[16:19], v[92:95], v[60:63], v[16:19]\n" \
  "v_mfma_f32_16x16x32_f16 v[20:23], v[92:95], v[32:35], v[20:23]\ns_waitcnt lgkmcnt(0)\n" \
  "v_mfma_f32_16x16x32_f16 v[20:23], v[56:59], v[60:63], v[20:23]\ns_nop 1\nv_cvt_pkrtz_f16_f32 v24, v12, v13\nv_cvt_pkrtz_f16_f32 v25, v14, v15\n" \
  "v_pk_add_u16 v24, v24, 4 op_sel_hi:[1,0] clamp\nv_pk_add_u16 v25, v25, 4 op_sel_hi:[1,0] clamp\nds_write_b64 v4, v[24:25] offset:5120\n" \
  "v_cvt_pkrtz_f16_f32 v24, v16, v17\nv_cvt_pkrtz_f16_f32 v25, v18, v19\nv_pk_add_u16 v24, v24, 4 op_sel_hi:[1,0] clamp\n" \
  "v_pk_add_u16 v25, v25, 4 op_sel_hi:[1,0] clamp\nds_write_b64 v4, v[24:25] offset:6144\nv_cvt_pkrtz_f16_f32 v24, v20, v21\n" \
  "v_cvt_pkrtz_f16_f32 v25, v22, v23\nv_pk_add_u16 v24, v24, 4 op_sel_hi:[1,0] clamp\nv_pk_add_u16 v25, v25, 4 op_sel_hi:[1,0] clamp\n" \
  "ds_write_b64 v4, v[24:25] offset:7168\ns_setprio 0\ns_setpc_b64 s[90:91]\n31:\ns_movk_i32 s84, 0x4000\n33:\nds_read_b32 v27, v9 offset:32\n" \
  "s_sub_u32 s84, s84, 1\ns_cbranch_scc1 32b\ns_waitcnt lgkmcnt(0)\nv_readfirstlane_b32 s85, v27\ns_cmp_lt_u32 s85, 4\ns_cbranch_scc1 33b\n" \
  "s_branch 32b\n9:\ns_cmp_eq_u32 s89, 3\ns_cbranch_scc1 3f\ns_cmp_eq_u32 s89, 4\ns_cbranch_scc1 4f\ns_cmp_eq_u32 s89, 5\ns_cbranch_scc1 5f\n" \
  "ds_read_b64 v[124:125], v3 offset:576\nds_read_b64 v[126:127], v3 offset:584\nds_read_b64 v[120:121], v3 offset:64\n" \
  "ds_read_b64 v[122:123], v3 offset:72\nds_read_b64 v[116:117], v3 offset:640\nds_read_b64 v[118:119], v3 offset:648\n" \
  "ds_read_b64 v[112:113], v3 offset:128\nds_read_b64 v[114:115], v3 offset:136\nds_read_b64 v[108:109], v3 offset:704\n" \
  "ds_read_b64 v[110:111], v3 offset:712\nds_read_b64 v[104:105], v3 offset:192\nds_read_b64 v[106:107], v3 offset:200\n" \
  "ds_read_b64 v[100:101], v3 offset:768\nds_read_b64 v[102:103], v3 offset:776\nds_read_b64 v[96:97], v3 offset:256\n" \
  "ds_read_b64 v[98:99], v3 offset:264\nds_read_b64 v[92:93], v3 offset:832\nds_read_b64 v[94:95], v3 offset:840\n" \
  "ds_read_b64 v[88:89], v3 offset:320\nds_read_b64 v[90:91], v3 offset:328\nds_read_b64 v[84:85], v3 offset:896\n" \
  "ds_read_b64 v[86:87], v3 offset:904\nds_read_b64 v[80:81], v3 offset:384\nds_read_b64 v[82:83], v3 offset:392\n" \
  "ds_read_b64 v[76:77], v3 offset:960\nds_read_b64 v[78:79], v3 offset:968\nds_read_b64 v[72:73], v3 offset:448\n" \
  "ds_read_b64 v[74:75], v3 offset:456\nds_read_b64 v[68:69], v3 offset:1024\nds_read_b64 v[70:71], v3 offset:1032\n" \
  "ds_read_b64 v[64:65], v3 offset:512\nds_read_b64 v[66:67], v3 offset:520\ns_waitcnt lgkmcnt(0)\ns_barrier\nds_read_b128 v[24:27], v2 offset:0\n" \
  "ds_read_b128 v[28:31], v2 offset:64\nds_read_b128 v[32:35], v2 offset:128\nds_read_b128 v[36:39], v2 offset:192\ns_waitcnt lgkmcnt(3)\n" \
  "v_mfma_f32_16x16x32_f16 v[12:15], v[124:127], v[24:27], 0\ns_setprio 1\nds_read_b128 v[40:43], v2 offset:256\n" \
  "v_mfma_f32_16x16x32_f16 v[16:19], v[120:123], v[24:27], 0\ns_waitcnt lgkmcnt(3)\nv_mfma_f32_16x16x32_f16 v[12:15], v[116:119], v[28:31], v[12:15]\n" \
  "ds_read_b128 v[44:47], v2 offset:320\nv_mfma_f32_16x16x32_f16 v[16:19], v[112:115], v[28:31], v[16:19]\ns_waitcnt lgkmcnt(3)\n" \
  "v_mfma_f32_16x16x32_f16 v[12:15], v[108:111], v[32:35], v[12:15]\nds_read_b128 v[48:51], v2 offset:384\n" \
  "v_mfma_f32_16x16x32_f16 v[16:19], v[104:107], v[32:35], v[16:19]\ns_waitcnt lgkmcnt(3)\n" \
  "v_mfma_f32_16x16x32_f16 v[12:15], v[100:103], v[36:39], v[12:15]\nds_read_b128 v[52:55], v2 offset:448\n" \
  "v_mfma_f32_16x16x32_f16 v[16:19], v[96:99], v[36:39], v[16:19]\ns_waitcnt lgkmcnt(3)\n" \
  "v_mfma_f32_16x16x32_f16 v[12:15], v[92:95], v[40:43], v[12:15]\nds_read_b128 v[56:59], v2 offset:512\n" \
  "v_mfma_f32_16x16x32_f16 v[16:19], v[88:91], v[40:43], v[16:19]\nds_read_b128 v[60:63], v2 offset:576\ns_waitcnt lgkmcnt(3)\n" \
  "v_mfma_f32_16x16x32_f16 v[12:15], v[84:87], v[44:47], v[12:15]\nds_read_b128 v[120:123], v2 offset:640\n" \
  "v_mfma_f32_16x16x32_f16 v[16:19], v[80:83], v[44:47], v[16:19]\nds_read_b128 v[24:27], v2 offset:704\n" \
  "v_mfma_f32_16x16x32_f16 v[12:15], v[76:79], v[48:51], v[12:15]\nds_read_b128 v[112:115], v2 offset:768\n" \
  "v_mfma_f32_16x16x32_f16 v[16:19], v[72:75], v[48:51], v[16:19]\nds_read_b128 v[28:31], v2 offset:832\ns_waitcnt lgkmcnt(5)\n" \
  "v_mfma_f32_16x16x32_f16 v[12:15], v[68:71], v[52:55], v[12:15]\nds_read_b128 v[104:107], v2 offset:896\n" \
  "v_mfma_f32_16x16x32_f16 v[16:19], v[64:67], v[52:55], v[16:19]\nds_read_b128 v[32:35], v2 offset:960\n" \
  "v_mfma_f32_16x16x32_f16 v[16:19], v[124:127], v[56:59], v[16:19]\ns_waitcnt lgkmcnt(5)\n" \
  "v_mfma_f32_16x16x32_f16 v[16:19], v[116:119], v[60:63], v[16:19]\nv_mfma_f32_16x16x32_f16 v[16:19], v[108:111], v[120:123], v[16:19]\n" \
  "s_waitcnt lgkmcnt(3)\nv_mfma_f32_16x16x32_f16 v[16:19], v[100:103], v[24:27], v[16:19]\nv_cvt_pkrtz_f16_f32 v20, v12, v13\n" \
  "v_cvt_pkrtz_f16_f32 v21, v14, v15\nv_mfma_f32_16x16x32_f16 v[16:19], v[92:95], v[112:115], v[16:19]\n" \
  "v_pk_add_u16 v20, v20, 4 op_sel_hi:[1,0] clamp\ns_waitcnt lgkmcnt(2)\nv_mfma_f32_16x16x32_f16 v[16:19], v[84:87], v[28:31], v[16:19]\n" \
  "v_pk_add_u16 v21, v21, 4 op_sel_hi:[1,0] clamp\ns_waitcnt lgkmcnt(1)\nv_mfma_f32_16x16x32_f16 v[16:19], v[76:79], v[104:107], v[16:19]\n" \
  "ds_write_b64 v4, v[20:21] offset:0\ns_waitcnt lgkmcnt(1)\nv_mfma_f32_16x16x32_f16 v[16:19], v[68:71], v[32:35], v[16:19]\ns_nop 7\n" \
  "v_cvt_pkrtz_f16_f32 v20, v16, v17\nv_cvt_pkrtz_f16_f32 v21, v18, v19\nv_pk_add_u16 v20, v20, 4 op_sel_hi:[1,0] clamp\n" \
  "v_pk_add_u16 v21, v21, 4 op_sel_hi:[1,0] clamp\nds_write_b64 v4, v[20:21] offset:1024\ns_setprio 0\ns_setpc_b64 s[90:91]\n3:\n" \
  "ds_read_b64 v[124:125], v3 offset:0\nds_read_b64 v[126:127], v3 offset:8\nds_read_b64 v[120:121], v3 offset:64\n" \
  "ds_read_b64 v[122:123], v3 offset:72\nds_read_b64 v[116:117], v3 offset:128\nds_read_b64 v[118:119], v3 offset:136\n" \
  "ds_read_b64 v[112:113], v3 offset:192\nds_read_b64 v[114:115], v3 offset:200\nds_read_b64 v[108:109], v3 offset:256\n" \
  "ds_read_b64 v[110:111], v3 offset:264\nds_read_b64 v[104:105], v3 offset:320\nds_read_b64 v[106:107], v3 offset:328\n" \
  "ds_read_b64 v[100:101], v3 offset:384\nds_read_b64 v[102:103], v3 offset:392\nds_read_b64 v[96:97], v3 offset:448\n" \
  "ds_read_b64 v[98:99], v3 offset:456\nds_read_b64 v[92:93], v3 offset:512\nds_read_b64 v[94:95], v3 offset:520\n" \
  "ds_read_b64 v[88:89], v3 offset:576\nds_read_b64 v[90:91], v3 offset:584\nds_read_b64 v[84:85], v3 offset:640\n" \
  "ds_read_b64 v[86:87], v3 offset:648\nds_read_b64 v[80:81], v3 offset:704\nds_read_b64 v[82:83], v3 offset:712\n" \
  "ds_read_b64 v[76:77], v3 offset:768\nds_read_b64 v[78:79], v3 offset:776\nds_read_b64 v[72:73], v3 offset:832\n" \
  "ds_read_b64 v[74:75], v3 offset:840\nds_read_b64 v[68:69], v3 offset:896\nds_read_b64 v[70:71], v3 offset:904\n" \
  "ds_read_b64 v[64:65], v3 offset:960\nds_read_b64 v[66:67], v3 offset:968\nds_read_b64 v[60:61], v3 offset:1024\n" \
  "ds_read_b64 v[62:63], v3 offset:1032\nds_read_b64 v[56:57], v6 offset:256\nds_read_b64 v[58:59], v6 offset:264\n" \
  "ds_read_b64 v[52:53], v6 offset:128\nds_read_b64 v[54:55], v6 offset:136\nds_read_b64 v[48:49], v6 offset:320\n" \
  "ds_read_b64 v[50:51], v6 offset:328\nds_read_b64 v[44:45], v6 offset:192\nds_read_b64 v[46:47], v6 offset:200\ns_waitcnt lgkmcnt(0)\ns_barrier\n" \
  "ds_read_b128 v[24:27], v2 offset:448\nds_read_b128 v[28:31], v2 offset:512\nds_read_b128 v[32:35], v2 offset:576\n" \
  "ds_read_b128 v[36:39], v2 offset:640\nds_read_b128 v[40:43], v2 offset:704\ns_waitcnt lgkmcnt(3)\n" \
  "v_mfma_f32_16x16x32_f16 v[12:15], v[124:127], v[24:27], 0\ns_setprio 1\nds_read_b128 v[24:27], v2 offset:768\n" \
  "v_mfma_f32_16x16x32_f16 v[12:15], v[120:123], v[28:31], v[12:15]\nds_read_b128 v[28:31], v2 offset:832\ns_waitcnt lgkmcnt(3)\n" \
  "v_mfma_f32_16x16x32_f16 v[12:15], v[116:119], v[32:35], v[12:15]\nds_read_b128 v[32:35], v2 offset:896\n" \
  "v_mfma_f32_16x16x32_f16 v[12:15], v[112:115], v[36:39], v[12:15]\nds_read_b128 v[36:39], v2 offset:960\ns_waitcnt lgkmcnt(3)\n" \
  "v_mfma_f32_16x16x32_f16 v[12:15], v[108:111], v[40:43], v[12:15]\nds_read_b128 v[40:43], v2 offset:1024\n" \
  "v_mfma_f32_16x16x32_f16 v[12:15], v[104:107], v[24:27], v[12:15]\nds_read_b128 v[24:27], v2 offset:1088\ns_waitcnt lgkmcnt(3)\n" \
  "v_mfma_f32_16x16x32_f16 v[12:15], v[100:103], v[28:31], v[12:15]\nds_read_b128 v[28:31], v2 offset:1152\n" \
  "v_mfma_f32_16x16x32_f16 v[12:15], v[96:99], v[32:35], v[12:15]\ns_waitcnt lgkmcnt(3)\n" \
  "v_mfma_f32_16x16x32_f16 v[12:15], v[92:95], v[36:39], v[12:15]\nds_read_b128 v[32:35], v2 offset:1216\n" \
  "v_mfma_f32_16x16x32_f16 v[16:19], v[124:127], v[36:39], 0\ns_waitcnt lgkmcnt(3)\nv_mfma_f32_16x16x32_f16 v[12:15], v[88:91], v[40:43], v[12:15]\n" \
  "ds_read_b128 v[124:127], v2 offset:1280\nv_mfma_f32_16x16x32_f16 v[16:19], v[120:123], v[40:43], v[16:19]\ns_waitcnt lgkmcnt(3)\n" \
  "v_mfma_f32_16x16x32_f16 v[12:15], v[84:87], v[24:27], v[12:15]\nds_read_b128 v[36:39], v2 offset:1344\n" \
  "v_mfma_f32_16x16x32_f16 v[16:19], v[116:119], v[24:27], v[16:19]\ns_waitcnt lgkmcnt(3)\n" \
  "v_mfma_f32_16x16x32_f16 v[12:15], v[80:83], v[28:31], v[12:15]\nds_read_b128 v[120:123], v2 offset:1408\n" \
  "v_mfma_f32_16x16x32_f16 v[16:19], v[112:115], v[28:31], v[16:19]\ns_waitcnt lgkmcnt(3)\n" \
  "v_mfma_f32_16x16x32_f16 v[12:15], v[76:79], v[32:35], v[12:15]\nds_read_b128 v[40:43], v2 offset:1472\n" \
  "v_mfma_f32_16x16x32_f16 v[16:19], v[108:111], v[32:35], v[16:19]\ns_waitcnt lgkmcnt(3)\n" \
  "v_mfma_f32_16x16x32_f16 v[12:15], v[72:75], v[124:127], v[12:15]\nds_read_b128 v[116:119], v2 offset:1536\n" \
  "v_mfma_f32_16x16x32_f16 v[16:19], v[104:107], v[124:127], v[16:19]\nds_read_b128 v[24:27], v2 offset:1600\ns_waitcnt lgkmcnt(3)\n" \
  "v_mfma_f32_16x16x32_f16 v[12:15], v[68:71], v[36:39], v[12:15]\nds_read_b128 v[112:115], v2 offset:1664\nds_read_b32 v23, v9 offset:32\n" \
  "v_mfma_f32_16x16x32_f16 v[16:19], v[100:103], v[36:39], v[16:19]\nds_read_b128 v[28:31], v2 offset:1728\n" \
  "v_mfma_f32_16x16x32_f16 v[12:15], v[64:67], v[120:123], v[12:15]\nds_read_b128 v[108:111], v2 offset:1792\n" \
  "v_mfma_f32_16x16x32_f16 v[16:19], v[96:99], v[120:123], v[16:19]\nds_read_b128 v[32:35], v2 offset:1856\ns_waitcnt lgkmcnt(6)\n" \
  "v_mfma_f32_16x16x32_f16 v[12:15], v[60:63], v[40:43], v[12:15]\nds_read_b128 v[104:107], v2 offset:1920\n" \
  "v_mfma_f32_16x16x32_f16 v[16:19], v[92:95], v[40:43], v[16:19]\nds_read_b128 v[124:127], v2 offset:1984\ns_waitcnt lgkmcnt(5)\n" \
  "v_readfirstlane_b32 s85, v23\ns_cmp_lt_u32 s85, 3\ns_cbranch_scc1 51f\n52:\nv_mfma_f32_16x16x32_f16 v[16:19], v[88:91], v[116:119], v[16:19]\n" \
  "ds_read_b64_tr_b16 v[100:101], v5 offset:0\nds_read_b64_tr_b16 v[102:103], v5 offset:32\n" \
  "v_mfma_f32_16x16x32_f16 v[16:19], v[84:87], v[24:27], v[16:19]\nds_read_b64_tr_b16 v[36:37], v5 offset:256\n" \
  "ds_read_b64_tr_b16 v[38:39], v5 offset:288\nv_mfma_f32_16x16x32_f16 v[16:19], v[80:83], v[112:115], v[16:19]\n" \
  "ds_read_b64_tr_b16 v[96:97], v5 offset:512\nds_read_b64_tr_b16 v[98:99], v5 offset:544\ns_waitcnt lgkmcnt(9)\n" \
  "v_mfma_f32_16x16x32_f16 v[16:19], v[76:79], v[28:31], v[16:19]\nds_read_b64_tr_b16 v[120:121], v5 offset:768\n" \
  "ds_read_b64_tr_b16 v[122:123], v5 offset:800\nv_mfma_f32_16x16x32_f16 v[16:19], v[72:75], v[108:111], v[16:19]\ns_waitcnt lgkmcnt(9)\n" \
  "v_mfma_f32_16x16x32_f16 v[16:19], v[68:71], v[32:35], v[16:19]\nv_mfma_f32_16x16x32_f16 v[16:19], v[64:67], v[104:107], v[16:19]\n" \
  "s_waitcnt lgkmcnt(6)\nv_mfma_f32_16x16x32_f16 v[16:19], v[60:63], v[124:127], v[16:19]\n" \
  "v_mfma_f32_16x16x32_f16 v[12:15], v[100:103], v[56:59], v[12:15]\nv_mfma_f32_16x16x32_f16 v[16:19], v[100:103], v[52:55], v[16:19]\n" \
  "s_waitcnt lgkmcnt(2)\nv_mfma_f32_16x16x32_f16 v[12:15], v[36:39], v[48:51], v[12:15]\n" \
  "v_mfma_f32_16x16x32_f16 v[16:19], v[36:39], v[44:47], v[16:19]\nv_mfma_f32_16x16x32_f16 v[16:19], v[96:99], v[56:59], v[16:19]\n" \
  "s_waitcnt lgkmcnt(0)\nv_mfma_f32_16x16x32_f16 v[16:19], v[120:123], v[48:51], v[16:19]\ns_nop 3\nv_cvt_pkrtz_f16_f32 v20, v12, v13\n" \
  "v_cvt_pkrtz_f16_f32 v21, v14, v15\nv_pk_add_u16 v20, v20, 4 op_sel_hi:[1,0] clamp\nv_pk_add_u16 v21, v21, 4 op_sel_hi:[1,0] clamp\n" \
  "ds_write_b64 v4, v[20:21] offset:2048\nv_cvt_pkrtz_f16_f32 v20, v16, v17\nv_cvt_pkrtz_f16_f32 v21, v18, v19\n" \
  "v_pk_add_u16 v20, v20, 4 op_sel_hi:[1,0] clamp\nv_pk_add_u16 v21, v21, 4 op_sel_hi:[1,0] clamp\nds_write_b64 v4, v[20:21] offset:3072\n" \
  "s_setprio 0\ns_setpc_b64 s[90:91]\n51:\ns_movk_i32 s84, 0x4000\n53:\nds_read_b32 v23, v9 offset:32\ns_sub_u32 s84, s84, 1\ns_cbranch_scc1 52b\n" \
  "s_waitcnt lgkmcnt(0)\nv_readfirstlane_b32 s85, v23\ns_cmp_lt_u32 s85, 3\ns_cbranch_scc1 53b\ns_branch 52b\n4:\n" \
  "ds_read_b64 v[124:125], v3 offset:1088\nds_read_b64 v[126:127], v3 offset:1096\nds_read_b64 v[120:121], v3 offset:0\n" \
  "ds_read_b64 v[122:123], v3 offset:8\nds_read_b64 v[116:117], v3 offset:64\nds_read_b64 v[118:119], v3 offset:72\n" \
  "ds_read_b64 v[112:113], v3 offset:128\nds_read_b64 v[114:115], v3 offset:136\nds_read_b64 v[108:109], v3 offset:192\n" \
  "ds_read_b64 v[110:111], v3 offset:200\nds_read_b64 v[104:105], v3 offset:256\nds_read_b64 v[106:107], v3 offset:264\n" \
  "ds_read_b64 v[100:101], v3 offset:320\nds_read_b64 v[102:103], v3 offset:328\nds_read_b64 v[96:97], v3 offset:384\n" \
  "ds_read_b64 v[98:99], v3 offset:392\nds_read_b64 v[92:93], v3 offset:448\nds_read_b64 v[94:95], v3 offset:456\n" \
  "ds_read_b64 v[88:89], v3 offset:512\nds_read_b64 v[90:91], v3 offset:520\nds_read_b64 v[84:85], v3 offset:576\n" \
  "ds_read_b64 v[86:87], v3 offset:584\nds_read_b64 v[80:81], v3 offset:640\nds_read_b64 v[82:83], v3 offset:648\n" \
  "ds_read_b64 v[76:77], v3 offset:704\nds_read_b64 v[78:79], v3 offset:712\nds_read_b64 v[72:73], v3 offset:768\n" \
  "ds_read_b64 v[74:75], v3 offset:776\nds_read_b64 v[68:69], v3 offset:832\nds_read_b64 v[70:71], v3 offset:840\n" \
  "ds_read_b64 v[64:65], v3 offset:896\nds_read_b64 v[66:67], v3 offset:904\nds_read_b64 v[60:61], v3 offset:960\n" \
  "ds_read_b64 v[62:63], v3 offset:968\nds_read_b64 v[56:57], v3 offset:1024\nds_read_b64 v[58:59], v3 offset:1032\n" \
  "ds_read_b64 v[52:53], v6 offset:0\nds_read_b64 v[54:55], v6 offset:8\nds_read_b64 v[48:49], v6 offset:64\nds_read_b64 v[50:51], v6 offset:72\n" \
  "ds_read_b64 v[44:45], v6 offset:128\nds_read_b64 v[46:47], v6 offset:136\nds_read_b64 v[40:41], v6 offset:192\n" \
  "ds_read_b64 v[42:43], v6 offset:200\ns_waitcnt lgkmcnt(0)\ns_barrier\nds_read_b128 v[24:27], v2 offset:448\nds_read_b128 v[28:31], v2 offset:960\n" \
  "ds_read_b128 v[32:35], v2 offset:1472\nds_read_b128 v[36:39], v2 offset:1536\ns_waitcnt lgkmcnt(3)\n" \
  "v_mfma_f32_16x16x32_f16 v[12:15], v[124:127], v[24:27], 0\ns_setprio 1\nds_read_b128 v[24:27], v2 offset:1600\ns_waitcnt lgkmcnt(3)\n" \
  "v_mfma_f32_16x16x32_f16 v[16:19], v[124:127], v[28:31], 0\nds_read_b128 v[124:127], v2 offset:1664\ns_waitcnt lgkmcnt(3)\n" \
  "v_mfma_f32_16x16x32_f16 v[12:15], v[120:123], v[32:35], v[12:15]\nds_read_b128 v[28:31], v2 offset:1728\ns_waitcnt lgkmcnt(3)\n" \
  "v_mfma_f32_16x16x32_f16 v[12:15], v[116:119], v[36:39], v[12:15]\nds_read_b128 v[32:35], v2 offset:1792\ns_waitcnt lgkmcnt(3)\n" \
  "v_mfma_f32_16x16x32_f16 v[12:15], v[112:115], v[24:27], v[12:15]\nds_read_b128 v[36:39], v2 offset:1856\ns_waitcnt lgkmcnt(3)\n" \
  "v_mfma_f32_16x16x32_f16 v[12:15], v[108:111], v[124:127], v[12:15]\nds_read_b128 v[24:27], v2 offset:1920\ns_waitcnt lgkmcnt(3)\n" \
  "v_mfma_f32_16x16x32_f16 v[12:15], v[104:107], v[28:31], v[12:15]\nds_read_b128 v[124:127], v2 offset:1984\ns_waitcnt lgkmcnt(3)\n" \
  "v_mfma_f32_16x16x32_f16 v[12:15], v[100:103], v[32:35], v[12:15]\nds_read_b128 v[28:31], v2 offset:2048\ns_waitcnt lgkmcnt(3)\n" \
  "v_mfma_f32_16x16x32_f16 v[12:15], v[96:99], v[36:39], v[12:15]\nds_read_b128 v[32:35], v2 offset:2112\ns_waitcnt lgkmcnt(3)\n" \
  "v_mfma_f32_16x16x32_f16 v[12:15], v[92:95], v[24:27], v[12:15]\nds_read_b128 v[36:39], v2 offset:2176\ns_waitcnt lgkmcnt(3)\n" \
  "v_mfma_f32_16x16x32_f16 v[12:15], v[88:91], v[124:127], v[12:15]\nds_read_b128 v[24:27], v2 offset:2240\n" \
  "v_mfma_f32_16x16x32_f16 v[16:19], v[120:123], v[124:127], v[16:19]\nds_read_b64 v[120:121], v6 offset:256\nds_read_b64 v[122:123], v6 offset:264\n" \
  "s_waitcnt lgkmcnt(4)\nv_mfma_f32_16x16x32_f16 v[12:15], v[84:87], v[28:31], v[12:15]\nds_read_b128 v[124:127], v2 offset:2304\n" \
  "v_mfma_f32_16x16x32_f16 v[16:19], v[116:119], v[28:31], v[16:19]\nds_read_b64 v[116:117], v6 offset:320\nds_read_b64 v[118:119], v6 offset:328\n" \
  "v_mfma_f32_16x16x32_f16 v[12:15], v[80:83], v[32:35], v[12:15]\nds_read_b128 v[28:31], v2 offset:2368\n" \
  "v_mfma_f32_16x16x32_f16 v[16:19], v[112:115], v[32:35], v[16:19]\ns_waitcnt lgkmcnt(6)\n" \
  "v_mfma_f32_16x16x32_f16 v[12:15], v[76:79], v[36:39], v[12:15]\nds_read_b128 v[112:115], v2 offset:2432\n" \
  "v_mfma_f32_16x16x32_f16 v[16:19], v[108:111], v[36:39], v[16:19]\nds_read2_b32 v[32:33], v8 offset0:48 offset1:49\n" \
  "ds_read2_b32 v[34:35], v8 offset0:50 offset1:51\nv_mfma_f32_16x16x32_f16 v[12:15], v[72:75], v[24:27], v[12:15]\n" \
  "ds_read_b128 v[108:111], v2 offset:2496\nv_mfma_f32_16x16x32_f16 v[16:19], v[104:107], v[24:27], v[16:19]\n" \
  "ds_read2_b32 v[36:37], v8 offset0:32 offset1:33\nds_read2_b32 v[38:39], v8 offset0:34 offset1:35\ns_waitcnt lgkmcnt(6)\n" \
  "v_mfma_f32_16x16x32_f16 v[12:15], v[68:71], v[124:127], v[12:15]\nds_read_b128 v[104:107], v2 offset:2560\n" \
  "v_mfma_f32_16x16x32_f16 v[16:19], v[100:103], v[124:127], v[16:19]\nds_read_b128 v[24:27], v2 offset:2624\n" \
  "v_mfma_f32_16x16x32_f16 v[12:15], v[64:67], v[28:31], v[12:15]\nds_read_b128 v[100:103], v2 offset:2688\nds_read_b32 v23, v9 offset:32\n" \
  "v_mfma_f32_16x16x32_f16 v[16:19], v[96:99], v[28:31], v[16:19]\nds_read_b128 v[124:127], v2 offset:2752\ns_waitcnt lgkmcnt(7)\n" \
  "v_mfma_f32_16x16x32_f16 v[12:15], v[60:63], v[112:115], v[12:15]\nds_read_b128 v[96:99], v2 offset:2816\n" \
  "v_mfma_f32_16x16x32_f16 v[16:19], v[92:95], v[112:115], v[16:19]\nds_read_b128 v[28:31], v2 offset:2880\n" \
  "v_mfma_f32_16x16x32_f16 v[12:15], v[56:59], v[108:111], v[12:15]\nds_read_b128 v[92:95], v2 offset:2944\n" \
  "v_mfma_f32_16x16x32_f16 v[16:19], v[88:91], v[108:111], v[16:19]\nds_read_b128 v[112:115], v2 offset:3008\ns_waitcnt lgkmcnt(5)\n" \
  "v_readfirstlane_b32 s85, v23\ns_cmp_lt_u32 s85, 3\ns_cbranch_scc1 61f\n62:\nv_mfma_f32_16x16x32_f16 v[16:19], v[84:87], v[104:107], v[16:19]\n" \
  "ds_read_b64_tr_b16 v[88:89], v5 offset:0\nds_read_b64_tr_b16 v[90:91], v5 offset:32\n" \
  "v_mfma_f32_16x16x32_f16 v[16:19], v[80:83], v[24:27], v[16:19]\nds_read_b64_tr_b16 v[108:109], v5 offset:256\n" \
  "ds_read_b64_tr_b16 v[110:111], v5 offset:288\nv_mfma_f32_16x16x32_f16 v[16:19], v[76:79], v[100:103], v[16:19]\n" \
  "ds_read_b64_tr_b16 v[84:85], v5 offset:512\nds_read_b64_tr_b16 v[86:87], v5 offset:544\ns_waitcnt lgkmcnt(9)\n" \
  "v_mfma_f32_16x16x32_f16 v[16:19], v[72:75], v[124:127], v[16:19]\nds_read_b64_tr_b16 v[104:105], v5 offset:768\n" \
  "ds_read_b64_tr_b16 v[106:107], v5 offset:800\nv_mfma_f32_16x16x32_f16 v[16:19], v[68:71], v[96:99], v[16:19]\n" \
  "ds_read_b64_tr_b16 v[80:81], v5 offset:1024\nds_read_b64_tr_b16 v[82:83], v5 offset:1056\ns_waitcnt lgkmcnt(11)\n" \
  "v_mfma_f32_16x16x32_f16 v[16:19], v[64:67], v[28:31], v[16:19]\nds_read_b64_tr_b16 v[24:25], v5 offset:1280\n" \
  "ds_read_b64_tr_b16 v[26:27], v5 offset:1312\nv_mfma_f32_16x16x32_f16 v[16:19], v[60:63], v[92:95], v[16:19]\n" \
  "ds_read_b64_tr_b16 v[76:77], v5 offset:1536\nds_read_b64_tr_b16 v[78:79], v5 offset:1568\ns_waitcnt lgkmcnt(12)\n" \
  "v_mfma_f32_16x16x32_f16 v[16:19], v[56:59], v[112:115], v[16:19]\nds_read_b64_tr_b16 v[100:101], v5 offset:1792\n" \
  "ds_read_b64_tr_b16 v[102:103], v5 offset:1824\nv_mfma_f32_16x16x32_f16 v[12:15], v[88:91], v[52:55], v[12:15]\ns_waitcnt lgkmcnt(10)\n" \
  "v_mfma_f32_16x16x32_f16 v[12:15], v[108:111], v[48:51], v[12:15]\nds_read_b64_tr_b16 v[72:73], v7 offset:0\n" \
  "ds_read_b64_tr_b16 v[74:75], v7 offset:64\nv_mfma_f32_16x16x32_f16 v[12:15], v[84:87], v[44:47], v[12:15]\n" \
  "ds_read_b64_tr_b16 v[124:125], v7 offset:512\nds_read_b64_tr_b16 v[126:127], v7 offset:576\n" \
  "v_mfma_f32_16x16x32_f16 v[16:19], v[84:87], v[52:55], v[16:19]\ns_waitcnt lgkmcnt(10)\n" \
  "v_mfma_f32_16x16x32_f16 v[12:15], v[104:107], v[40:43], v[12:15]\nv_mfma_f32_16x16x32_f16 v[16:19], v[104:107], v[48:51], v[16:19]\n" \
  "v_mfma_f32_16x16x32_f16 v[12:15], v[80:83], v[120:123], v[12:15]\nv_mfma_f32_16x16x32_f16 v[16:19], v[80:83], v[44:47], v[16:19]\n" \
  "s_waitcnt lgkmcnt(6)\nv_mfma_f32_16x16x32_f16 v[12:15], v[24:27], v[116:119], v[12:15]\n" \
  "v_mfma_f32_16x16x32_f16 v[16:19], v[24:27], v[40:43], v[16:19]\nv_mfma_f32_16x16x32_f16 v[16:19], v[76:79], v[120:123], v[16:19]\n" \
  "s_waitcnt lgkmcnt(2)\nv_mfma_f32_16x16x32_f16 v[16:19], v[100:103], v[116:119], v[16:19]\n" \
  "v_mfma_f32_16x16x32_f16 v[12:15], v[72:75], v[32:35], v[12:15]\nv_mfma_f32_16x16x32_f16 v[16:19], v[72:75], v[36:39], v[16:19]\n" \
  "s_waitcnt lgkmcnt(0)\nv_mfma_f32_16x16x32_f16 v[16:19], v[124:127], v[32:35], v[16:19]\ns_nop 4\nv_cvt_pkrtz_f16_f32 v20, v12, v13\n" \
  "v_cvt_pkrtz_f16_f32 v21, v14, v15\nv_pk_add_u16 v20, v20, 4 op_sel_hi:[1,0] clamp\nv_pk_add_u16 v21, v21, 4 op_sel_hi:[1,0] clamp\n" \
  "ds_write_b64 v4, v[20:21] offset:4096\nv_cvt_pkrtz_f16_f32 v20, v16, v17\nv_cvt_pkrtz_f16_f32 v21, v18, v19\n" \
  "v_pk_add_u16 v20, v20, 4 op_sel_hi:[1,0] clamp\nv_pk_add_u16 v21, v21, 4 op_sel_hi:[1,0] clamp\nds_write_b64 v4, v[20:21] offset:5120\n" \
  "s_setprio 0\ns_setpc_b64 s[90:91]\n61:\ns_movk_i32 s84, 0x4000\n63:\nds_read_b32 v23, v9 offset:32\ns_sub_u32 s84, s84, 1\ns_cbranch_scc1 62b\n" \
  "s_waitcnt lgkmcnt(0)\nv_readfirstlane_b32 s85, v23\ns_cmp_lt_u32 s85, 3\ns_cbranch_scc1 63b\ns_branch 62b\n5:\n" \
  "ds_read_b64 v[124:125], v3 offset:1088\nds_read_b64 v[126:127], v3 offset:1096\nds_read_b64 v[120:121], v3 offset:0\n" \
  "ds_read_b64 v[122:123], v3 offset:8\nds_read_b64 v[116:117], v3 offset:64\nds_read_b64 v[118:119], v3 offset:72\n" \
  "ds_read_b64 v[112:113], v3 offset:128\nds_read_b64 v[114:115], v3 offset:136\nds_read_b64 v[108:109], v3 offset:192\n" \
  "ds_read_b64 v[110:111], v3 offset:200\nds_read_b64 v[104:105], v3 offset:256\nds_read_b64 v[106:107], v3 offset:264\n" \
  "ds_read_b64 v[100:101], v3 offset:320\nds_read_b64 v[102:103], v3 offset:328\nds_read_b64 v[96:97], v3 offset:384\n" \
  "ds_read_b64 v[98:99], v3 offset:392\nds_read_b64 v[92:93], v3 offset:448\nds_read_b64 v[94:95], v3 offset:456\n" \
  "ds_read_b64 v[88:89], v3 offset:512\nds_read_b64 v[90:91], v3 offset:520\nds_read_b64 v[84:85], v3 offset:576\n" \
  "ds_read_b64 v[86:87], v3 offset:584\nds_read_b64 v[80:81], v3 offset:640\nds_read_b64 v[82:83], v3 offset:648\n" \
  "ds_read_b64 v[76:77], v3 offset:704\nds_read_b64 v[78:79], v3 offset:712\nds_read_b64 v[72:73], v3 offset:768\n" \
  "ds_read_b64 v[74:75], v3 offset:776\nds_read_b64 v[68:69], v3 offset:832\nds_read_b64 v[70:71], v3 offset:840\n" \
  "ds_read_b64 v[64:65], v3 offset:896\nds_read_b64 v[66:67], v3 offset:904\nds_read_b64 v[60:61], v3 offset:960\n" \
  "ds_read_b64 v[62:63], v3 offset:968\nds_read_b64 v[56:57], v3 offset:1024\nds_read_b64 v[58:59], v3 offset:1032\n" \
  "ds_read_b64 v[52:53], v6 offset:0\nds_read_b64 v[54:55], v6 offset:8\nds_read_b64 v[48:49], v6 offset:64\nds_read_b64 v[50:51], v6 offset:72\n" \
  "ds_read_b64 v[44:45], v6 offset:128\nds_read_b64 v[46:47], v6 offset:136\nds_read_b64 v[40:41], v6 offset:192\n" \
  "ds_read_b64 v[42:43], v6 offset:200\ns_waitcnt lgkmcnt(0)\ns_barrier\nds_read_b128 v[24:27], v2 offset:1472\n" \
  "ds_read_b128 v[28:31], v2 offset:1984\nds_read_b128 v[32:35], v2 offset:2496\nds_read_b128 v[36:39], v2 offset:2560\ns_waitcnt lgkmcnt(3)\n" \
  "v_mfma_f32_16x16x32_f16 v[12:15], v[124:127], v[24:27], 0\ns_setprio 1\nds_read_b128 v[24:27], v2 offset:2624\ns_waitcnt lgkmcnt(3)\n" \
  "v_mfma_f32_16x16x32_f16 v[16:19], v[124:127], v[28:31], 0\nds_read_b128 v[124:127], v2 offset:2688\ns_waitcnt lgkmcnt(3)\n" \
  "v_mfma_f32_16x16x32_f16 v[12:15], v[120:123], v[32:35], v[12:15]\nds_read_b128 v[28:31], v2 offset:2752\ns_waitcnt lgkmcnt(3)\n" \
  "v_mfma_f32_16x16x32_f16 v[12:15], v[116:119], v[36:39], v[12:15]\nds_read_b128 v[32:35], v2 offset:2816\ns_waitcnt lgkmcnt(3)\n" \
  "v_mfma_f32_16x16x32_f16 v[12:15], v[112:115], v[24:27], v[12:15]\nds_read_b128 v[36:39], v2 offset:2880\ns_waitcnt lgkmcnt(3)\n" \
  "v_mfma_f32_16x16x32_f16 v[12:15], v[108:111], v[124:127], v[12:15]\nds_read_b128 v[24:27], v2 offset:2944\ns_waitcnt lgkmcnt(3)\n" \
  "v_mfma_f32_16x16x32_f16 v[12:15], v[104:107], v[28:31], v[12:15]\nds_read_b128 v[124:127], v2 offset:3008\ns_waitcnt lgkmcnt(3)\n" \
  "v_mfma_f32_16x16x32_f16 v[12:15], v[100:103], v[32:35], v[12:15]\nds_read_b128 v[28:31], v2 offset:3072\ns_waitcnt lgkmcnt(3)\n" \
  "v_mfma_f32_16x16x32_f16 v[12:15], v[96:99], v[36:39], v[12:15]\nds_read_b128 v[32:35], v2 offset:3136\ns_waitcnt lgkmcnt(3)\n" \
  "v_mfma_f32_16x16x32_f16 v[12:15], v[92:95], v[24:27], v[12:15]\nds_read_b128 v[36:39], v2 offset:3200\ns_waitcnt lgkmcnt(3)\n" \
  "v_mfma_f32_16x16x32_f16 v[12:15], v[88:91], v[124:127], v[12:15]\nds_read_b128 v[24:27], v2 offset:3264\n" \
  "v_mfma_f32_16x16x32_f16 v[16:19], v[120:123], v[124:127], v[16:19]\nds_read_b64 v[120:121], v6 offset:256\nds_read_b64 v[122:123], v6 offset:264\n" \
  "s_waitcnt lgkmcnt(4)\nv_mfma_f32_16x16x32_f16 v[12:15], v[84:87], v[28:31], v[12:15]\nds_read_b128 v[124:127], v2 offset:3328\n" \
  "v_mfma_f32_16x16x32_f16 v[16:19], v[116:119], v[28:31], v[16:19]\nds_read_b64 v[116:117], v6 offset:320\nds_read_b64 v[118:119], v6 offset:328\n" \
  "v_mfma_f32_16x16x32_f16 v[12:15], v[80:83], v[32:35], v[12:15]\nds_read_b128 v[28:31], v2 offset:3392\n" \
  "v_mfma_f32_16x16x32_f16 v[16:19], v[112:115], v[32:35], v[16:19]\ns_waitcnt lgkmcnt(6)\n" \
  "v_mfma_f32_16x16x32_f16 v[12:15], v[76:79], v[36:39], v[12:15]\nds_read_b128 v[112:115], v2 offset:3456\n" \
  "v_mfma_f32_16x16x32_f16 v[16:19], v[108:111], v[36:39], v[16:19]\nds_read2_b32 v[32:33], v8 offset0:16 offset1:17\n" \
  "ds_read2_b32 v[34:35], v8 offset0:18 offset1:19\nv_mfma_f32_16x16x32_f16 v[12:15], v[72:75], v[24:27], v[12:15]\n" \
  "ds_read_b128 v[108:111], v2 offset:3520\nv_mfma_f32_16x16x32_f16 v[16:19], v[104:107], v[24:27], v[16:19]\n" \
  "ds_read2_b32 v[36:37], v8 offset0:0 offset1:1\nds_read2_b32 v[38:39], v8 offset0:2 offset1:3\ns_waitcnt lgkmcnt(6)\n" \
  "v_mfma_f32_16x16x32_f16 v[12:15], v[68:71], v[124:127], v[12:15]\nds_read_b128 v[104:107], v2 offset:3584\n" \
  "v_mfma_f32_16x16x32_f16 v[16:19], v[100:103], v[124:127], v[16:19]\nds_read_b128 v[24:27], v2 offset:3648\n" \
  "ds_read2_b32 v[100:101], v8 offset0:32 offset1:33\nds_read2_b32 v[102:103], v8 offset0:34 offset1:35\n" \
  "v_mfma_f32_16x16x32_f16 v[12:15], v[64:67], v[28:31], v[12:15]\nds_read_b128 v[124:127], v2 offset:3712\nds_read_b32 v23, v9 offset:32\n" \
  "v_mfma_f32_16x16x32_f16 v[16:19], v[96:99], v[28:31], v[16:19]\nds_read_b128 v[96:99], v2 offset:3776\ns_waitcnt lgkmcnt(9)\n" \
  "v_mfma_f32_16x16x32_f16 v[12:15], v[60:63], v[112:115], v[12:15]\nds_read_b128 v[28:31], v2 offset:3840\n" \
  "v_mfma_f32_16x16x32_f16 v[16:19], v[92:95], v[112:115], v[16:19]\nds_read_b128 v[92:95], v2 offset:3904\n" \
  "v_mfma_f32_16x16x32_f16 v[12:15], v[56:59], v[108:111], v[12:15]\nds_read_b128 v[112:115], v2 offset:3968\n" \
  "v_mfma_f32_16x16x32_f16 v[16:19], v[88:91], v[108:111], v[16:19]\nds_read_b128 v[88:91], v2 offset:4032\ns_waitcnt lgkmcnt(5)\n" \
  "v_readfirstlane_b32 s85, v23\ns_cmp_lt_u32 s85, 3\ns_cbranch_scc1 71f\n72:\nv_mfma_f32_16x16x32_f16 v[16:19], v[84:87], v[104:107], v[16:19]\n" \
  "ds_read_b64_tr_b16 v[108:109], v5 offset:1024\nds_read_b64_tr_b16 v[110:111], v5 offset:1056\n" \
  "v_mfma_f32_16x16x32_f16 v[16:19], v[80:83], v[24:27], v[16:19]\nds_read_b64_tr_b16 v[84:85], v5 offset:1280\n" \
  "ds_read_b64_tr_b16 v[86:87], v5 offset:1312\nv_mfma_f32_16x16x32_f16 v[16:19], v[76:79], v[124:127], v[16:19]\n" \
  "ds_read_b64_tr_b16 v[104:105], v5 offset:1536\nds_read_b64_tr_b16 v[106:107], v5 offset:1568\ns_waitcnt lgkmcnt(9)\n" \
  "v_mfma_f32_16x16x32_f16 v[16:19], v[72:75], v[96:99], v[16:19]\nds_read_b64_tr_b16 v[80:81], v5 offset:1792\n" \
  "ds_read_b64_tr_b16 v[82:83], v5 offset:1824\nv_mfma_f32_16x16x32_f16 v[16:19], v[68:71], v[28:31], v[16:19]\n" \
  "ds_read_b64_tr_b16 v[24:25], v5 offset:2048\nds_read_b64_tr_b16 v[26:27], v5 offset:2080\ns_waitcnt lgkmcnt(11)\n" \
  "v_mfma_f32_16x16x32_f16 v[16:19], v[64:67], v[92:95], v[16:19]\nds_read_b64_tr_b16 v[76:77], v5 offset:2304\n" \
  "ds_read_b64_tr_b16 v[78:79], v5 offset:2336\nv_mfma_f32_16x16x32_f16 v[16:19], v[60:63], v[112:115], v[16:19]\n" \
  "ds_read_b64_tr_b16 v[124:125], v5 offset:2560\nds_read_b64_tr_b16 v[126:127], v5 offset:2592\ns_waitcnt lgkmcnt(12)\n" \
  "v_mfma_f32_16x16x32_f16 v[16:19], v[56:59], v[88:91], v[16:19]\nds_read_b64_tr_b16 v[72:73], v5 offset:2816\n" \
  "ds_read_b64_tr_b16 v[74:75], v5 offset:2848\nv_mfma_f32_16x16x32_f16 v[12:15], v[108:111], v[52:55], v[12:15]\ns_waitcnt lgkmcnt(10)\n" \
  "v_mfma_f32_16x16x32_f16 v[12:15], v[84:87], v[48:51], v[12:15]\nds_read_b64_tr_b16 v[96:97], v7 offset:0\n" \
  "ds_read_b64_tr_b16 v[98:99], v7 offset:64\nv_mfma_f32_16x16x32_f16 v[12:15], v[104:107], v[44:47], v[12:15]\n" \
  "ds_read_b64_tr_b16 v[68:69], v7 offset:512\nds_read_b64_tr_b16 v[70:71], v7 offset:576\n" \
  "v_mfma_f32_16x16x32_f16 v[16:19], v[104:107], v[52:55], v[16:19]\ns_waitcnt lgkmcnt(10)\n" \
  "v_mfma_f32_16x16x32_f16 v[12:15], v[80:83], v[40:43], v[12:15]\nds_read_b64_tr_b16 v[28:29], v7 offset:1024\n" \
  "ds_read_b64_tr_b16 v[30:31], v7 offset:1088\nv_mfma_f32_16x16x32_f16 v[16:19], v[80:83], v[48:51], v[16:19]\n" \
  "ds_read2_b32 v[64:65], v8 offset0:48 offset1:49\nds_read2_b32 v[66:67], v8 offset0:50 offset1:51\n" \
  "v_mfma_f32_16x16x32_f16 v[12:15], v[24:27], v[120:123], v[12:15]\nv_mfma_f32_16x16x32_f16 v[16:19], v[24:27], v[44:47], v[16:19]\n" \
  "s_waitcnt lgkmcnt(10)\nv_mfma_f32_16x16x32_f16 v[12:15], v[76:79], v[116:119], v[12:15]\nds_read_b64_tr_b16 v[92:93], v7 offset:1536\n" \
  "ds_read_b64_tr_b16 v[94:95], v7 offset:1600\nv_mfma_f32_16x16x32_f16 v[16:19], v[76:79], v[40:43], v[16:19]\n" \
  "v_mfma_f32_16x16x32_f16 v[16:19], v[124:127], v[120:123], v[16:19]\ns_waitcnt lgkmcnt(8)\n" \
  "v_mfma_f32_16x16x32_f16 v[16:19], v[72:75], v[116:119], v[16:19]\nv_mfma_f32_16x16x32_f16 v[12:15], v[96:99], v[32:35], v[12:15]\n" \
  "v_mfma_f32_16x16x32_f16 v[16:19], v[96:99], v[36:39], v[16:19]\ns_waitcnt lgkmcnt(4)\n" \
  "v_mfma_f32_16x16x32_f16 v[12:15], v[68:71], v[100:103], v[12:15]\nv_mfma_f32_16x16x32_f16 v[16:19], v[68:71], v[32:35], v[16:19]\n" \
  "s_waitcnt lgkmcnt(2)\nv_mfma_f32_16x16x32_f16 v[12:15], v[28:31], v[64:67], v[12:15]\n" \
  "v_mfma_f32_16x16x32_f16 v[16:19], v[28:31], v[100:103], v[16:19]\ns_waitcnt lgkmcnt(0)\n" \
  "v_mfma_f32_16x16x32_f16 v[16:19], v[92:95], v[64:67], v[16:19]\ns_nop 4\nv_cvt_pkrtz_f16_f32 v20, v12, v13\nv_cvt_pkrtz_f16_f32 v21, v14, v15\n" \
  "v_pk_add_u16 v20, v20, 4 op_sel_hi:[1,0] clamp\nv_pk_add_u16 v21, v21, 4 op_sel_hi:[1,0] clamp\nds_write_b64 v4, v[20:21] offset:6144\n" \
  "v_cvt_pkrtz_f16_f32 v20, v16, v17\nv_cvt_pkrtz_f16_f32 v21, v18, v19\nv_pk_add_u16 v20, v20, 4 op_sel_hi:[1,0] clamp\n" \
  "v_pk_add_u16 v21, v21, 4 op_sel_hi:[1,0] clamp\nds_write_b64 v4, v[20:21] offset:7168\ns_setprio 0\ns_setpc_b64 s[90:91]\n71:\n" \
  "s_movk_i32 s84, 0x4000\n73:\nds_read_b32 v23, v9 offset:32\ns_sub_u32 s84, s84, 1\ns_cbranch_scc1 72b\ns_waitcnt lgkmcnt(0)\n" \
  "v_readfirstlane_b32 s85, v23\ns_cmp_lt_u32 s85, 3\ns_cbranch_scc1 73b\ns_branch 72b\n"
#define CONV_CLOB "memory", "scc", "s84", "s85", "s90", "s91", "v12", "v13", "v14", "v15", "v16", "v17", "v18", "v19", "v20", "v21", "v22", "v23", "v24", "v25", "v26", "v27", "v28", "v29", "v30", "v31", "v32", "v33", "v34", "v35", "v36", "v37", "v38", "v39", "v40", "v41", "v42", "v43", "v44", "v45", "v46", "v47", "v48", "v49", "v50", "v51", "v52", "v53", "v54", "v55", "v56", "v57", "v58", "v59", "v60", "v61", "v62", "v63", "v64", "v65", "v66", "v67", "v68", "v69", "v70", "v71", "v72", "v73", "v74", "v75", "v76", "v77", "v78", "v79", "v80", "v81", "v82", "v83", "v84", "v85", "v86", "v87", "v88", "v89", "v90", "v91", "v92", "v93", "v94", "v95", "v96", "v97", "v98", "v99", "v100", "v101", "v102", "v103", "v104", "v105", "v106", "v107", "v108", "v109", "v110", "v111", "v112", "v113", "v114", "v115", "v116", "v117", "v118", "v119", "v120", "v121", "v122", "v123", "v124", "v125", "v126", "v127"
constexpr int TDP = 1216, T_3 = 4 * TDP, T_4 = T_3 + 512, TB = T_4 + 320;
constexpr int C_U = 480, UB = C_U + 4096 + 96, C_G5 = 0, C_G3 = 3200, C_G9 = 6400, C_G7 = C_G9 + 2096, GB = C_G7 + 2080 + 112;
constexpr int O_FL = 2 * TB, O_UL = O_FL + 64, O_IM = O_UL + 2 * UB, O_ST = O_IM + 2 * GB;
constexpr int X_TB = 10240, X_UL = X_TB + TB, X_IM = X_UL + UB, X_ST = X_IM + GB;
constexpr int X_WR = X_IM + 128 * IP;
#define X_RW(w) (LW + 9216 * (2 + ((w) >> 1)) + 2560 * ((w) & 1))
constexpr int RP = 1216;
constexpr int LW = 36864 + 512, LA = LW, LM = LW + 36864;
constexpr int L_SC = LM + 2048, SMEM = O_IM + 256 * IP;
static_assert(L_SC + 1152 <= SMEM && O_ST + 8192 <= SMEM && X_IM + 128 * IP <= LM && X_ST + 8192 <= LM && UB % 128 == 64 && GB % 128 == 64 && SMEM <= 79872, "");
struct TOps { uint32_t xa, xb, ya, pd, pf, pg, pe; };
__device__ __forceinline__ void table_ops(uint32_t im, uint32_t tb, int ct, int lane, TOps& t) {
  const int j = lane & 15, kq = lane >> 4, q4 = (lane >> 2) & 3, p4 = lane & 3, jj = j + 16 * ct;
  const uint32_t sa = im + (uint32_t)(IP * (lane >> 2));
  t.xa = sa + (uint32_t)(I_X + 8 * p4); t.xb = sa + (uint32_t)(I_X + 32 + 8 * (p4 & 1)); t.ya = sa + (uint32_t)(32 * ct + 8 * p4);
  t.pd = tb + (uint32_t)(2 * (541 - (8 * jj + 4 * (kq & 1) + 256 * (kq >> 1))));
  t.pf = tb + (uint32_t)((kq & 1) ? T_4 + 2 * (63 - jj) : T_3 + 2 * (127 - 2 * jj));
  t.pg = t.pf - ((kq & 1) ? 64 : 2);
  t.pe = tb + (uint32_t)(p4 * (TDP + 2) + ((lane < 12) ? 2 * (32 - q4) : 2 * (572 + (q4 & 1))));
  asm volatile("" : "+v"(t.xa), "+v"(t.xb), "+v"(t.ya), "+v"(t.pd), "+v"(t.pf), "+v"(t.pg), "+v"(t.pe));
}
typedef __attribute__((address_space(3))) char* lptr;
#define LP(T, a) ((__attribute__((address_space(3))) T*)(uintptr_t)(a))
__device__ __forceinline__ uint32_t table_tile(const TOps& t, int ct, int lane, uint32_t evp, float dsk TSP) {
  u32x4 ev;
  f32x4 ad[2], af[2];
  asm volatile(T_ASM : "=&v"(ad[0]), "=&v"(ad[1]), "=&v"(af[0]), "=&v"(af[1]), "=&v"(ev) : "v"(t.xa), "v"(t.xb), "v"(t.ya), "v"(evp) : T_CLOB);
  const int e0 = (int)(ev[0] + ev[2]), e1 = (int)(ev[1] + ev[3]);
  const int em = min(max(max(max(e0, e1), (int)((__float_as_uint(dsk) >> 23) & 0xffu) - 133), -110), 115);
  const int ts = ((uint32_t)(em + 12) <= 8u) ? 1 : -3 - em;
  float s0 = __builtin_amdgcn_ldexpf(1.f, e0 - 29 + ts), s1 = __builtin_amdgcn_ldexpf(1.f, e1 - 29 + ts);
  float d0 = (ct == 0 && lane == 0) ? __builtin_amdgcn_ldexpf(dsk, ts) : 0.f;
  asm volatile("s_nop 1" : "+v"(ad[0]), "+v"(ad[1]), "+v"(af[0]), "+v"(af[1]), "+v"(s0), "+v"(s1), "+v"(d0));
  const f32x4 k = ad[0] * s0 + (f32x4){d0, 0.f, 0.f, 0.f} + ad[1] * s1, f = af[0] * s0 + af[1] * s1;
  auto pk = [](float a, float b) { uint32_t r; asm("v_cvt_pk_f16_f32 %0, %1, %2" : "=v"(r) : "v"(a), "v"(b)); return r; };
  asm volatile("s_nop 0" : : "v"(k), "v"(f));
  const uint32_t p32 = pk(k[3], k[2]), p10 = pk(k[1], k[0]), p21 = pk(k[2], k[1]), p01 = pk(f[0], f[1]), p23 = pk(f[2], f[3]);
  *LP(u32x2, t.pd + 3 * TDP + 6) = (u32x2){p32, p10};
  *LP(uint32_t, t.pd + TDP + 2) = p32; *LP(uint32_t, t.pd + TDP + 6) = p10;
  *LP(uint32_t, t.pd + 2) = p21; *LP(uint32_t, t.pd + 2 * TDP + 6) = p21;
  *LP(u16, t.pd) = (u16)p32; *LP(u16, t.pd + 2 * TDP + 4) = (u16)p32;
  asm volatile("ds_write_b16_d16_hi %0, %1 offset:6\n\tds_write_b16_d16_hi %0, %1 offset:%2" : : "v"(t.pd), "v"(p10), "n"(2 * TDP + 10) : "memory");
  if (ct) {
    const uint32_t va = (uint32_t)__builtin_amdgcn_readlane((int)p32, 63), vb = (uint32_t)__builtin_amdgcn_readlane((int)p23, 15) >> 16;
    *LP(u16, t.pe) = (u16)((lane < 12) ? va : vb);
  }
  if (lane < 32) {
    *LP(u16, t.pf + 128) = (u16)p01; *LP(u16, t.pg + 128) = (u16)(p01 >> 16);
    *LP(u16, t.pf) = (u16)p23; *LP(u16, t.pg) = (u16)(p23 >> 16);
  }
  return (uint32_t)(127 + 5 - ts) << 23;
}
__device__ __forceinline__ uint32_t hadd(uint32_t a, uint32_t b) { return __builtin_bit_cast(uint32_t, __builtin_bit_cast(f16x2, a) + __builtin_bit_cast(f16x2, b)); }
template <bool FULL> __device__ __forceinline__ void sums(char* sm, const u32x4 p, const u32x4 c) {
  const uint32_t D[8] = {p[0], p[1], p[2], p[3], c[0], c[1], c[2], c[3]};
  uint32_t O[7];
#pragma unroll
  for (int j = 0; j < 7; ++j) O[j] = __builtin_amdgcn_alignbit(D[j + 1], D[j], 16);
  u32x4 a, b;
#pragma unroll
  for (int j = 0; j < 4; ++j) a[j] = hadd(D[j + 2], hadd(O[j + 1], O[j + 2]));
  *(u32x4*)(sm + C_G3 + 32) = a;
#pragma unroll
  for (int j = 0; j < 4; ++j) b[j] = hadd(a[j], hadd(D[j + 1], D[j + 3]));
  *(u32x4*)(sm + C_G5 + 32) = b;
  if (FULL) {
#pragma unroll
    for (int j = 0; j < 4; ++j) a[j] = hadd(b[j], hadd(O[j], O[j + 3]));
    *(u32x4*)(sm + C_G7 + 32) = a;
#pragma unroll
    for (int j = 0; j < 4; ++j) b[j] = hadd(a[j], hadd(D[j], D[j + 4]));
    *(u32x4*)(sm + C_G9 + 32) = b;
  }
}
__device__ __forceinline__ void count_up(uint32_t a) {
  uint32_t t;
  asm volatile("s_mov_b64 exec, 1\n\tv_mov_b32 %0, 1\n\tds_add_u32 %1, %0\n\ts_mov_b64 exec, -1" : "=&v"(t) : "v"(a) : "memory");
}
__device__ __forceinline__ int pad_at(int r) { return 16 * (r & 1) + 3200 * (r >> 1) - ((r >= 6) ? 3200 - 2096 : 0); }
struct COps { uint32_t b, a, o, g3, k3, g4, k4, f; };
__device__ __forceinline__ void conv_ops(uint32_t tb, uint32_t ub, uint32_t cb, int lane, COps& e) {
  const uint32_t j = lane & 15, kq = lane >> 4, q4 = (lane >> 2) & 3, p4 = lane & 3;
  e.b = ub + 32u * j + 16u * kq;
  e.a = tb + p4 * TDP + 32u - 8u * q4 + 16u * kq;
  e.g3 = cb + C_G5 + 8 + 64u * kq + 8u * (q4 + p4) + (q4 & 1u) * 3200u;
  e.g4 = cb + C_G9 + 8 + 128u * kq + 16u * q4 + 8u * p4 + (q4 & 1u) * 2096u;
  e.k3 = tb + T_3 + 120 - 8u * j + 16u * kq;
  e.k4 = tb + T_4 + 60 - 4u * j + 16u * kq;
  asm volatile("" : "+v"(e.b), "+v"(e.a), "+v"(e.g3), "+v"(e.g4), "+v"(e.k3), "+v"(e.k4));
}
#define CONV_RUN(prog, e, ...) do { int prog_ = (prog); \
    asm volatile("s_call_b64 s[90:91], kf_conv" : "+{s89}"(prog_), "+{v2}"(e.b), "+{v3}"(e.a), "+{v4}"(e.o), "+{v5}"(e.g3), "+{v6}"(e.k3), "+{v7}"(e.g4), \
                 "+{v8}"(e.k4), "+{v9}"(e.f) __VA_ARGS__ : : CONV_CLOB); } while (0)

template <bool E>
__device__ __forceinline__ void ln_stats(const uint32_t* v, int lane, float sc, f32x2* t, f32x4 z, f32x2& rc, f32x2& nc, float& e) {
  float s1a = z[0], s2a = z[1], s1b = z[2], s2b = z[3];
#pragma unroll
  for (int k = 0; k < 4; ++k) {
    s1a = dot2(v[k], 0x3f803f80u, s1a); s2a = dot2(v[k], v[k], s2a);
    s1b = dot2(v[4 + k], 0x3f803f80u, s1b); s2b = dot2(v[4 + k], v[4 + k], s2b);
  }
#pragma unroll
  for (int k = 0; k < 9; ++k) t[k] = (f32x2){bflo(v[k]), bfhi(v[k])};
  const float rs = row_sum(fold16(dot2(v[8], 0x3f803f80u, fold32(s1a, s1b)), dot2(v[8], v[8], fold32(s2a, s2b))));
  if (E) { e = row_sum(e); e += __int_as_float(__builtin_amdgcn_update_dpp(0, __float_as_int(e), 0x142, 0xa, 0xf, false)); }
  const f32x2 mean = (f32x2){rdl(rs, 15), rdl(rs, 47)} * (1.f / D_), m2 = mean * mean;
  f32x2 var = (f32x2){rdl(rs, 31), rdl(rs, 63)} * (1.f / D_) - m2;
  asm volatile("" : "+v"(t[0]), "+v"(t[1]), "+v"(t[2]), "+v"(t[3]), "+v"(t[4]), "+v"(t[5]), "+v"(t[6]), "+v"(t[7]), "+v"(t[8]));
  if (__builtin_expect(!(m2[0] <= 16.f * var[0] && m2[1] <= 16.f * var[1]), 0)) {
    float q[3] = {0.f, 0.f, 0.f};
#pragma unroll
    for (int k = 0; k < 9; ++k) {
      const f32x2 a = t[k] - ((k < 4 || (k == 8 && lane < 32)) ? mean[0] : mean[1]);
      q[k >> 2] += a[0] * a[0] + a[1] * a[1];
    }
    float qa = q[0] + ((lane < 32) ? q[2] : 0.f), qb = q[1] + ((lane < 32) ? 0.f : q[2]);
    wave_sum2(qa, qb);
    var = (f32x2){qa, qb} * (1.f / D_);
  }
  const uint32_t mp = pack2(mean[0], mean[1]), vp = pack2(var[0], var[1]);
  const f32x2 ve = (f32x2){bflo(vp), bfhi(vp)} + 1e-5f;
  const uint32_t e2 = pack2(ve[0], ve[1]);
  const uint32_t sp = pack2(__builtin_amdgcn_sqrtf(bflo(e2)), __builtin_amdgcn_sqrtf(bfhi(e2)));
  rc = (f32x2){__builtin_amdgcn_rcpf(bflo(sp)), __builtin_amdgcn_rcpf(bfhi(sp))} * sc;
  nc = -(f32x2){bflo(mp), bfhi(mp)} * rc;
}
template <bool H>
__device__ __forceinline__ void ln_apply(const f32x2* t, int lane, f32x2 rc, f32x2 nc, uint32_t* c) {
  const float nm[3] = {nc[0], nc[1], (lane < 32) ? nc[0] : nc[1]}, rr[3] = {rc[0], rc[1], (lane < 32) ? rc[0] : rc[1]};
  f32x2 dd[9];
#pragma unroll
  for (int k = 0; k < 9; ++k) dd[k] = __builtin_elementwise_fma(t[k], (f32x2){rr[k >> 2], rr[k >> 2]}, (f32x2){nm[k >> 2], nm[k >> 2]});
  asm volatile("" : "+v"(dd[0]), "+v"(dd[1]), "+v"(dd[2]), "+v"(dd[3]), "+v"(dd[4]), "+v"(dd[5]), "+v"(dd[6]), "+v"(dd[7]), "+v"(dd[8]));
#pragma unroll
  for (int k = 0; k < 9; ++k) {
    const f32x2 d = dd[k];
    typedef __attribute__((ext_vector_type(2))) uint16_t u16x2;
    if (H) c[k] = __builtin_bit_cast(uint32_t, __builtin_elementwise_add_sat(__builtin_bit_cast(u16x2, __builtin_amdgcn_cvt_pkrtz(d[0], d[1])), (u16x2){4, 4}));
    else c[k] = pack2(d[0], d[1]);
  }
}
__device__ __forceinline__ uint32_t med3u(uint32_t a, uint32_t b, uint32_t c) { return max(min(a, b), min(max(a, b), c)); }
__device__ __forceinline__ uint32_t oct_maxu(uint32_t v) {
  v = max(v, (uint32_t)DPP_I((int)v, 0xb1, 0xf));
  v = max(v, (uint32_t)DPP_I((int)v, 0x4e, 0xf));
  v = max(v, (uint32_t)DPP_I((int)v, 0x141, 0xf));
  return v;
}

#define KARGS const u16* __restrict__ x, const u16* __restrict__ Are, const u16* __restrict__ Aim, const u16* __restrict__ Bre, \
    const u16* __restrict__ Bim, const u16* __restrict__ Cre, const u16* __restrict__ Cim, const u16* __restrict__ logdt, \
    const u16* __restrict__ Dskip, char* __restrict__ ws, const u16* __restrict__ wr, const u16* __restrict__ wg, \
    const u16* __restrict__ wu, const u16* __restrict__ wd, u16* __restrict__ out
#define KPASS x, Are, Aim, Bre, Bim, Cre, Cim, logdt, Dskip, ws, wr, wg, wu, wd, out
template <int ROLE, int FFN = -1>
__device__ __forceinline__ void role_body(char* smem, KARGS) {
  const int tid = threadIdx.x, lane = tid & 63;
  const int w = __builtin_amdgcn_readfirstlane(tid >> 6), wl = w & 3;
  const int b = blockIdx.x & 255, bx = b & 7, bj = b >> 3;
  constexpr bool isl = ROLE == 1 || ROLE == 2;
  const int tg = isl ? 32 * (bj >> 2) + 4 * bx + (bj & 3) : 32 * bx + bj;
  u32x4 xa, xc;
  uint32_t ep, r0;
#define P_WAIT(n, ...) asm volatile("s_waitcnt vmcnt(" #n ")\n\tv_lshlrev_b32 %0, 16, v100\n\tv_lshlrev_b32 %1, 16, v101\n\tv_lshlrev_b32 %2, 16, v102" \
    : "=v"(ldt), "=v"(are), "=v"(aim) : : "v100", "v101", "v102" __VA_ARGS__)
#define P_MORE() asm volatile("v_lshlrev_b32 %0, 16, v103\n\tv_lshlrev_b32 %1, 16, v104\n\tv_lshlrev_b32 %2, 16, v105\n\tv_lshlrev_b32 %3, 16, v106" \
    : "=v"(bre), "=v"(bim), "=v"(cre), "=v"(cim) : : "v103", "v104", "v105", "v106")
  TSD;
  TS(0, 0);
  const uint32_t lb0 = (uint32_t)(uintptr_t)(__attribute__((address_space(3))) char*)smem;
  const int i = lane & 15, kq = lane >> 4;
  const u32x4 ones = {0xffffffffu, 0xffffffffu, 0xffffffffu, 0xffffffffu}, zero4 = {0u, 0u, 0u, 0u};
  auto u_again = [&](u32x4& uw, const u32x4& ut, uint32_t uo) {
#pragma unroll 1
    for (int spin = 0; spin < SPIN_CAP; ++spin) {
      const bool na = ((uw[0] | uw[1] | uw[2] | uw[3]) & UMASK) != 0u;
      if (__builtin_expect(!__any(na), 1)) break;
      TSC(19);
      SLEEP(USLEEP);
      if (na) { LD16W(uw, uo); uw ^= ut; }
    }
  };
  constexpr uint32_t S16 = 0x41800000u, LMASK = 0x00070007u;
  auto v_put = [&](u32x4 v, const u32x4& ut, bool other, uint32_t vo) {
    const bool second = __any(other || ((v[0] | v[1] | v[2] | v[3]) & 0x40004000u) != 0u);
    if (__builtin_expect(second, 0)) v = (v & ~LMASK) | (~ut & LMASK); else v = (v & ~LMASK) | ut;
    ST16(vo, v);
    return second;
  };
  auto v_end = [&](uint32_t T, int n) { DRAIN(); if (lane == 0) ST4(O_DN + 16u * n, T); };
  if constexpr (!isl) {
    const int ch = wl >> 1, tl = tid & 255;
    int* evp = (int*)(smem + O_FL + 16 * ch);
    float ldt, are, aim, dsk;
    if constexpr (ROLE == 0) {
      __builtin_amdgcn_s_setprio(KPRIO);
      COps e;
      conv_ops(lb0 + (uint32_t)(ch * TB), lb0 + (uint32_t)(O_UL + ch * UB), lb0 + (uint32_t)(O_IM + ch * GB), lane, e);
      e.o = lb0 + O_ST + (uint32_t)(16 * ((4 * i + 2 * (kq >> 1) + ch) ^ (i >> 1)) + 8 * (kq & 1));
      e.f = lb0 + O_FL;
      TOps t;
      table_ops(lb0 + (uint32_t)(O_IM + 128 * IP * ch), lb0 + (uint32_t)(TB * ch), wl & 1, lane, t);
      uint32_t pz = lb0 + O_IM + (uint32_t)(((lane >> 3) & 1) * GB + pad_at(lane & 7)), im = lb0 + (uint32_t)(O_IM + IP * tl);
      asm volatile("" : "+v"(e.o), "+v"(e.f), "+v"(pz), "+v"(im));
      float bre, bim, cre, cim;
      P_WAIT(0, , "v103", "v104", "v105", "v106", "v107", "v109");
      asm volatile("" : "={v109}"(ep));
      const uint32_t T = ((uint32_t)__builtin_amdgcn_readfirstlane(ep) + 1u) * 0x9E3779B1u;
      if (w == 3 && lane < 8) ST16(O_KEY + 128u * b + 16u * lane, ones);
      P_MORE();
      asm volatile("v_lshlrev_b32 %0, 16, v107" : "=v"(dsk) : : "v107");
      TS(0, 1);
      evp[wl & 1] = gen_y(ldt, are, aim, bre, bim, cre, cim, im, [] {} TSA);
      TS(0, 8);
      BAR();
      TS(0, 2);
      const uint32_t cv = table_tile(t, wl & 1, lane, lb0 + O_FL + 16 * ch, dsk TSA), cs = (uint32_t)__builtin_amdgcn_readfirstlane(cv);
      *(uint32_t*)(smem + O_FL + 40 + 4 * ch) = cv;
      TS(0, 9);
      gsync0(lb0 + O_FL + 36, 4);
      TS(0, 3);
      *(u32x4*)(smem + O_ST + 16 * tl) = ones;
      *(u32x4*)(smem + O_ST + 4096 + 16 * tl) = ones;
      if (w == 0) *LP(u32x4, pz) = zero4;
      DRAIN();
      CONV_RUN(wl & 1, e);
      if (wl & 1) asm volatile("s_mov_b64 exec, 1\n\tglobal_store_dwordx2 %0, %1, %2 sc1" : : "v"(O_SC + 16u * (2 * tg + ch)), "v"((u32x2){T, cs}), "s"(ws) : "memory");
      TS(0, 5);
    } else {
      __builtin_amdgcn_s_setprio(UPRIO);
      const uint32_t ck0 = (uint32_t)__builtin_amdgcn_s_memtime(), rt0 = (uint32_t)__builtin_amdgcn_s_memrealtime();
#pragma unroll
      for (int k = 0; k < 3; ++k) *(u32x4*)(smem + ((16 * (tl + 256 * k) < O_FL) ? 16 * (tl + 256 * k) : O_FL + 32)) = zero4;
      if (w == 7) *(u32x4*)(smem + O_UL + (lane >> 5) * UB + 16 * min(lane & 31, 29)) = zero4;
      uint32_t uo = u_at(tl >> 1, 2 * tg + (tid & 1)), go = O_SU + (uint32_t)(256 * bx + 32 * wl + (lane & 31) + 4 * (lane & 32)) * 128u;
      uint32_t ul = (uint32_t)(O_UL + (tid & 1) * UB + C_U - 16 + 16 * (tl >> 1)), ug = (uint32_t)(O_IM + (tid & 1) * GB + 16 * (tl >> 1)), so = lb0 + O_ST + 16u * (uint32_t)(lane ^ (lane >> 3));
      uint32_t vo = O_V + (uint32_t)tg * VP + 16u * lane;
      asm volatile("" : "+v"(uo), "+v"(go), "+v"(ul), "+v"(ug), "+v"(so), "+v"(vo));
      P_WAIT(1, , "v109");
      TS(0, 1);
      evp[2 + (wl & 1)] = gen_x(ldt, are, aim, smem + O_IM + IP * tl);
      asm volatile("s_waitcnt vmcnt(0)" : "={v109}"(ep));
      const uint32_t T = ((uint32_t)__builtin_amdgcn_readfirstlane(ep) + 1u) * 0x9E3779B1u;
      TS(0, 8);
      BAR();
      TS(0, 2);
      if (w == 7 && bj >= 24) {
        const uint32_t t_ = ahead(ck0, rt0, VDB);
        ST16(O_SB + (uint32_t)(64 * (lane >> 3) + 8 * bx + bj - 24) * 128u + 16u * (lane & 7), ((u32x4){T, t_, T, t_}));
      }
      u32x4 ut = {utag(T, 0), utag(T, 1), utag(T, 2), utag(T, 3)};
      asm volatile("" : "+v"(ut));
      SLEEP(GSLEEP);
      u32x2 pv;
      pv[1] = gate(ws, go, T);
      TS(0, 3);
      until(latest(pv[1]) + UD);
      TS(0, 7);
      u32x4 uw, ux;
      asm volatile("global_load_dwordx4 %0, %2, %4 sc1\n\tglobal_load_dwordx4 %1, %3, %4 sc1"
                   : "=&v"(uw), "=&v"(ux) : "v"(uo), "v"(uo + 64 * UP * 32), "s"(ws) : "memory");
      __builtin_amdgcn_s_setprio(FPR);
      asm volatile("s_waitcnt vmcnt(1)" : "+v"(uw));
      uw ^= ut;
      *(u32x4*)(smem + ul + 16) = uw;
      asm volatile("s_waitcnt vmcnt(0)" : "+v"(ux));
      TS(0, 11);
      ux ^= ut;
      *(u32x4*)(smem + ul + 2048 + 16) = ux;
      if (__builtin_expect(__any(((uw[0] | uw[1] | uw[2] | uw[3] | ux[0] | ux[1] | ux[2] | ux[3]) & UMASK) != 0u), 0)) {
        u_again(uw, ut, uo);
        u_again(ux, ut, uo + 64 * UP * 32);
        *(u32x4*)(smem + ul + 16) = uw;
        *(u32x4*)(smem + ul + 2048 + 16) = ux;
      }
      BAR();
      TS(0, 4);
      const u32x4 pw = *(const u32x4*)(smem + ul);
      if (wl < 2) {
        const u32x4 px = *(const u32x4*)(smem + ul + 2048);
        sums<true>(smem + ug, pw, uw);
        sums<false>(smem + ug + 2048, px, ux);
      } else sums<true>(smem + ug, pw, uw);
      count_up(lb0 + O_FL + 32);
      __builtin_amdgcn_s_setprio(SPR);
      TS(0, 5);
      const bool other = *(const uint32_t*)(smem + O_FL + 40 + 4 * (lane & 1)) != S16;
      bool second = false;
#pragma unroll
      for (int k = 0; k < 2; ++k) {
        const uint32_t o = ((k ? 0x3247u : 0x1065u) >> (4 * wl) & 7u) * 1024u;
        u32x4 v;
#pragma unroll 1
        for (int spin = 0; spin < SPIN_CAP; ++spin) {
          asm volatile("ds_read_b128 %0, %1\n\ts_waitcnt lgkmcnt(0)" : "=&v"(v) : "v"(so + o) : "memory");
          if (!__any(max4(v) == 0xffffffffu)) break;
          __builtin_amdgcn_s_sleep(VPS);
        }
        TS(0, 12 + k);
        second |= v_put(v, ut, other, vo + o);
        TS(0, 10 - 4 * k);
      }
      if (__builtin_expect(second, 0)) v_end(T, 4 * tg + wl);
    }
    TSE();
  } else {
  uint32_t* cand = (uint32_t*)(smem + LM);
  uint32_t* sel = cand + 128;
  uint32_t* kl = sel + 48;
  uint32_t* cnt = sel + 64;
  const uint32_t ca = lb0 + LM + 4 * (128 + 64);
  u16* hm = (u16*)(cnt + 16);
  uint32_t T;
  const bool ffn = (FFN < 0) ? bj < 18 : FFN, isx = FFN != 1 && bj >= 24;
  const int fb = 18 * bx + bj;
  constexpr bool ln2 = ROLE == 2;
  const int g0 = tg, g2 = tg;
  u32x4 bw[18];
#pragma unroll
  for (int s = 0; s < 9; ++s) bw[s] = (u32x4){0u, 0u, 0u, 0u};
  int tid2 = threadIdx.x;
  uint32_t tv = 0u, tk = 0u;
  auto v_look = [&](uint32_t tu, int lane) {
    uint32_t tm = latest(gate(ws, O_SB + (uint32_t)(64 * bx + lane) * 128u, T)) - V2D;
    if ((int)(tm - (tu + UD + VD2)) < 0) tm = tu + UD + VD2;
    cnt[8] = tm;
    count_up(ca + 52);
  };
  auto v_time = [&] { gwait(ca + 52, 1); tv = (uint32_t)__builtin_amdgcn_readfirstlane(cnt[8]); };
  uint32_t ts1 = 0u;
  auto rest = [&] {
    if (ts1 && (int)((uint32_t)__builtin_amdgcn_s_memrealtime() - ts1) > 0) {
      v_time();
      ts1 = (tv - TSTOP) | 1u;
      if ((int)((uint32_t)__builtin_amdgcn_s_memrealtime() - ts1) > 0) { TS(0, 8); until(tv + RD); ts1 = 0u; }
    }
  };
  auto gu_dma = [&](int q0, int q1, auto depth) {
    const char* gs = (const char*)((w < 2) ? wg : wu) + (size_t)((144 * (w & 1)) * (F_ * 2) + fb * 64);
    const uint32_t gv = (uint32_t)((lane >> 2) * (F_ * 2) + 16 * ((lane & 3) ^ (lane >> 5) ^ (w & 2)));
#pragma unroll 1
    for (int q = q0; q < q1; ++q) {
      const int k = (int)((((bj & 1) ? ORD1 : ((unsigned __int128)0x876543210u << 36 | 0x876543210ull)) >> (4 * (q - (q >= 9 ? 9 : 0)) + (q >= 9 ? 36 : 0))) & 15u), h = q >= 9;
      if (q >= RQ) rest();
      dma16(gs + (size_t)(288 * h + 16 * k) * (F_ * 2), gv, lb0 + LW * h + 9216 * w + 1024 * k);
      asm volatile("s_waitcnt vmcnt(%0)" : : "n"(depth.value) : "memory");
    }
  };
  auto down_dma = [&](int k0, int k1, int lane) {
#pragma unroll 1
    for (int k = k0; k < k1; ++k) {
      const int o = 9216 * wl + 2304 * k;
      const char* ds = (const char*)wd + (size_t)(fb * 36864 + o);
      const uint32_t dl = lb0 + o + 32 * ((4 * wl + k + 1) >> 1);
      if (ROLE == 1) rest();
      dma16d(ds, 16u * lane, dl);
      dma16d(ds + 1024, 16u * lane, dl + 1024);
      uint32_t keep;
      asm volatile("s_mov_b32 %0, m0\n\ts_mov_b32 m0, %3\n\ts_mov_b64 exec, 0xffff\n\tglobal_load_lds_dwordx4 %1, %2" DPOL2 "\n\ts_mov_b64 exec, -1\n\ts_mov_b32 m0, %0\n\ts_waitcnt vmcnt(%4)"
                   : "=&s"(keep) : "v"(16u * lane), "s"(ds + 2048), "s"(dl + 2048), "n"(ROLE == 1 ? DDEPTH : 40) : "memory");
    }
  };
  auto gu_read = [&](int h) {
    const uint32_t ra = lb0 + LW * h + (uint32_t)(((lane >> 1) & 1) * 18432 + (8 * kq + ((lane >> 2) & 3)) * 64 + ((w ^ (kq & 1) ^ (lane & 2)) * 16) + 8 * (lane & 1));
#pragma unroll
    for (int s = 0; s < 9; ++s) {
      const u32x2 h0 = trd(ra + 2048 * s), h1 = trd(ra + 2048 * s + 256);
      bw[9 * h + s] = (u32x4){h0[0], h0[1], h1[0], h1[1]};
    }
#pragma unroll
    for (int s = 0; s < 9; ++s) asm volatile("" : "+v"(bw[9 * h + s]));
  };
  const uint32_t vearly = (uint32_t)((VEARLY >> (8 * (g0 >> 5))) & 0xffull);
#define TOUCH(pa) asm volatile("global_load_dword v126, %0, off" DPOL4 : : "v"(pa) : "memory", "v126")
  uint32_t wv[5], xv[9];
  auto rows_xw = [&](int lane) {
    u32x4 wa, xr, xs;
    const uint32_t xo = (uint32_t)((8 * g2 + 2 * wl) * (D_ * 2));
    asm volatile("global_load_dwordx4 %0, %5, %9\n\tglobal_load_dword %1, %6, %9 offset:1024\n\t"
                 "global_load_dwordx4 %2, %7, %10\n\tglobal_load_dwordx4 %3, %7, %10 offset:1152\n\tglobal_load_dword %4, %8, %10 offset:1024\n\ts_waitcnt vmcnt(0)"
                 : "=&v"(wa), "=&v"(wv[4]), "=&v"(xr), "=&v"(xs), "=&v"(xv[8])
                 : "v"(16u * lane), "v"(4u * (lane & 31)), "v"(xo + 16u * lane), "v"(xo + 4u * lane + 32u * (lane & 32)), "s"(wr), "s"(x) : "memory");
#pragma unroll
    for (int k = 0; k < 4; ++k) { wv[k] = wa[k]; xv[k] = xr[k]; xv[4 + k] = xs[k]; }
  };
  auto ln1 = [&](int g, int o, uint32_t c0) {
    uint32_t uo = u_at(g, 64 * wl + lane);
    uint32_t ta = lb0 + (uint32_t)(o + ((lane >> 2) & 3) * RP + (64 * wl + 16 * kq + 4 * (lane & 3)) * 2);
    uint32_t la = (uint32_t)(o + 2 * wl * RP + 16 * lane), lt = (uint32_t)(o + (2 * wl + (lane >> 5)) * RP + 1024 + 4 * (lane & 31));
    uint32_t sg = O_SU + (uint32_t)(256 * (lane >> 3) + g) * 128u + 16u * (lane & 7);
    asm volatile("" : "+v"(uo), "+v"(ta), "+v"(la), "+v"(lt), "+v"(sg));
    f32x4 z4 = {0.f, 0.f, 0.f, 0.f};
    asm volatile("" : "+v"(z4));
    asm volatile("s_waitcnt vmcnt(0)" : "={v[100:103]}"(xa), "={v[104:107]}"(xc), "={v108}"(r0), "={v109}"(ep));
    TS(0, 1);
    const uint32_t v[9] = {xa[0], xa[1], xa[2], xa[3], xc[0], xc[1], xc[2], xc[3], r0};
    uint32_t c[9];
    f32x2 tf[9];
    T = ((uint32_t)__builtin_amdgcn_readfirstlane(ep) + 1u) * 0x9E3779B1u;
    if (wl == 3) { const uint32_t now = (uint32_t)__builtin_amdgcn_s_memrealtime() + SD; ST16(sg, ((u32x4){T, now, T, now})); }
    const uint32_t tj = ((T >> (3 * wl)) & 0x00070007u) | (((T >> (12 + wl)) & 0x00010001u) << 14);
    uint32_t tw[3] = {(tj & 0xffffu) * 0x10001u, (tj >> 16) * 0x10001u, 0u};
    tw[2] = (lane < 32) ? tw[0] : tw[1];
    asm volatile("" : "+v"(tw[0]), "+v"(tw[1]), "+v"(tw[2]));
    f32x2 rc, nc;
    float e_;
    ln_stats<false>(v, lane, 0.03125f, tf, z4, rc, nc, e_);
    ln_apply<true>(tf, lane, rc, nc, c);
#pragma unroll
    for (int k = 0; k < 9; ++k) c[k] = (c[k] & ~UMASK) | tw[k >> 2];
    *(u32x4*)(smem + la) = (u32x4){c[0], c[1], c[2], c[3]};
    *(u32x4*)(smem + la + RP) = (u32x4){c[4], c[5], c[6], c[7]};
    *(uint32_t*)(smem + lt) = c[8];
    u32x4 ha, hb;
    {
      uint32_t t, cc, n = SPIN_CAP;
      asm volatile("s_mov_b64 exec, 1\n\tv_mov_b32 %2, 1\n\tds_add_u32 %5, %2\n\ts_mov_b64 exec, -1\n"
                   "1:\n\tds_read_b32 %2, %5\n\tds_read_b64_tr_b16 v[40:41], %6\n\tds_read_b64_tr_b16 v[42:43], %6 offset:%7\n\tds_read_b64_tr_b16 v[44:45], %6 offset:512\n\t"
                   "ds_read_b64_tr_b16 v[46:47], %6 offset:%8\n\ts_sub_u32 %4, %4, 1\n\ts_cbranch_scc1 2f\n\ts_waitcnt lgkmcnt(4)\n\tv_readfirstlane_b32 %3, %2\n\t"
                   "s_cmp_lt_u32 %3, 4\n\ts_cbranch_scc1 1b\n2:\n\ts_waitcnt lgkmcnt(0)"
                   : "=&{v[40:43]}"(ha), "=&{v[44:47]}"(hb), "=&v"(t), "=&s"(cc), "+s"(n) : "v"(c0), "v"(ta), "n"(4 * RP), "n"(512 + 4 * RP) : "memory", "scc");
    }
    ST16(uo, ha);
    ST16(uo + 64 * 128, hb);
    if (wl == 0) {
      const u32x2 h4 = trd(ta + 1024), h5 = trd(ta + 1024 + 4 * RP);
      ST16(uo + 128 * 128, ((u32x4){h4[0], h4[1], h5[0], h5[1]}));
    }
  };
  const int dX = 512 - 24 + 8 * bx + bj;
  if constexpr (ROLE == 2) {
    const int tl = tid - 256;
    if (isx) {
      __builtin_amdgcn_s_setprio(QPRIO0);
      *(u32x4*)(smem + X_TB + 16 * tl) = zero4;
      *(u32x4*)(smem + X_TB + 16 * min(256 + tl, (TB + C_U) / 16 - 1)) = zero4;
      const int pr = (wl + XROT) & 3;
      COps e;
      conv_ops(lb0 + X_TB, lb0 + X_UL, lb0 + X_IM, lane, e);
      TOps t;
      table_ops(lb0 + X_IM, lb0 + X_TB, wl & 1, lane, t);
      e.o = lb0 + X_ST + (uint32_t)(16 * ((2 * i + (kq >> 1)) ^ ((i >> 2) & 1)) + 8 * (kq & 1));
      e.f = ca + 28 - 32;
      asm volatile("" : "+v"(e.o), "+v"(e.f));
      float ldt, are, aim, bre, bim, cre, cim, dsk;
      P_WAIT(0, , "v103", "v104", "v105", "v106", "v107", "v109");
      asm volatile("" : "={v109}"(ep));
      T = ((uint32_t)__builtin_amdgcn_readfirstlane(ep) + 1u) * 0x9E3779B1u;
      P_MORE();
      asm volatile("v_lshlrev_b32 %0, 16, v107" : "=v"(dsk) : : "v107");
      TS(0, 1);
      __builtin_amdgcn_s_setprio(QPRIO);
      int* evp = (int*)(sel + 44);
      char* im = smem + X_IM + IP * (tl & 127);
      evp[wl] = (wl < 2) ? gen_y(ldt, are, aim, bre, bim, cre, cim, lb0 + X_IM + IP * tl, [] {} TSA) : gen_x(ldt, are, aim, im);
      TS(0, 2);
      __builtin_amdgcn_s_setprio(QPRIO2);
      gsync(ca + 8, 4);
      if (wl < 2) evp[-4 + wl] = (int)table_tile(t, wl & 1, lane, lb0 + LM + 4 * (128 + 44), dsk TSA);
      if (wl < 2) gsync0(ca + 8, 8); else { count_up(ca + 8); SLEEP(XSL); gwait(ca + 8, 8); }
      const uint32_t cs = (uint32_t)__builtin_amdgcn_readfirstlane(evp[-4]);
      if (wl == 3) *(u32x4*)(smem + X_IM + pad_at(lane & 7)) = zero4;
      TS(0, 3);
      CONV_RUN(2 + pr, e, , "+{v11}"(tid2));
      __builtin_amdgcn_s_setprio(3);
      {
        const int lane = tid2 & 63;
        const u32x4 v = *(const u32x4*)(smem + X_ST + 2048 * pr + 1024 * (lane >> 5) + 16 * ((lane & 31) ^ ((lane >> 3) & 1)));
        if (__builtin_expect(v_put(v, (u32x4){utag(T, 0), utag(T, 1), utag(T, 2), utag(T, 3)}, cs != S16, v_at(dX, 64 * pr + lane)), 0)) {
          DRAIN();
          if (lane == 0) ST4(O_DN + 16u * (4 * (dX - 256) + pr), T);
        }
        if (pr == 0 && lane == 0) asm volatile("global_store_dwordx2 %0, %1, %2 sc1" : : "v"(O_SC + 16u * dX), "v"((u32x2){T, cs}), "s"(ws) : "memory");
      }
      TS(0, 6);
    }
    if (!isx) __builtin_amdgcn_s_setprio(PPRIO);
    asm volatile("" : "+v"(tid2));
    if (!isx) rows_xw(tid2 & 63);
    if (!isx && !ffn) {
      SLEEP(RSLEEP);
#pragma unroll
      for (int k = 0; k < 7; ++k) ST16(O_SL + (uint32_t)((6 * bx + bj - 18) * 1792 + 256 * k - 256 + tid2) * 16u, ones);
      DRAIN();
    }
    if (!isx) BAR();
    const uint32_t tp = (uint32_t)__builtin_amdgcn_readfirstlane(cnt[4]);
    if (!isx) T = (uint32_t)__builtin_amdgcn_readfirstlane(sel[43]);
    if (!isx && w == 4) v_look(tp, tid2 & 63);
    v_time();
  } else {
    __builtin_amdgcn_s_setprio(3);
    if (tid < 16) cnt[tid] = 0u;
    BAR();
    if (tid < 20) ((uint32_t*)(hm + 320))[tid] = 0u;
    if (tid >= 184) *(u32x4*)(smem + L_SC - 16 * 184 + 16 * tid) = (u32x4){0x41804180u, 0x41804180u, 0x41804180u, 0x41804180u};
    uint32_t sink = 0;
    ln1(tg, 0, ca + 12);
    TS(0, 2);
    __builtin_amdgcn_s_setprio(NPRIO);
    asm volatile("s_waitcnt vmcnt(0)" : "+v"(sink) : : "memory");
    TS(0, 13);
    if (w == 0) {
      const uint32_t xr = O_XC + (uint32_t)((511 - 2 * (g0 + w)) * 144 + lane) * 16u;
      ST16(xr, ones);
      ST16(xr + 1024u, ones);
      if (lane < 16) ST16(xr + 2048u, ones);
    }
    if (w == 2 && b < 72 && lane < 8) ST16(O_FB + (uint32_t)(8 * b + lane) * 16u, ones);
    if (isx) {
      rows_xw(lane);
      char* sp = smem + X_RW(w) + 16 * lane;
      *(u32x4*)sp = (u32x4){xv[0], xv[1], xv[2], xv[3]};
      *(u32x4*)(sp + 1024) = (u32x4){xv[4], xv[5], xv[6], xv[7]};
      *(u32x2*)(smem + X_RW(w) + 2048 + 8 * lane) = (u32x2){xv[8], wv[4]};
      if (w == 0) *(u32x4*)(smem + X_WR + 16 * lane) = (u32x4){wv[0], wv[1], wv[2], wv[3]};
    }
    SLEEP(ESLEEP);
    uint32_t tu = latest(gate(ws, O_SU + (uint32_t)(256 * bx + (ALLP ? tid : 64 * w + 4 * i + (bj & 3))) * 128u, T));
    asm volatile("s_mov_b64 exec, 1\n\tds_max_u32 %0, %1\n\ts_mov_b64 exec, -1" : : "v"(ca + 16), "v"(tu) : "memory");
    if (w == 0) sel[43] = T;
    if (isx) {
      const uint32_t xo = u_at(tid, dX);
      char* sm = smem + X_UL + C_U - 16 + 16 * tid;
      const u32x4 ut = {utag(T, 0), utag(T, 1), utag(T, 2), utag(T, 3)};
      __builtin_amdgcn_s_setprio(3);
      until(tu + UD + XD);
      u32x4 uw;
      asm volatile("global_load_dwordx4 %0, %1, %2 sc1" : "=&v"(uw) : "v"(xo), "s"(ws) : "memory");
      asm volatile("s_waitcnt vmcnt(0)" : "+v"(uw));
      uw ^= ut;
      u_again(uw, ut, xo);
      *(u32x4*)(sm + 16) = uw;
      BAR();
      if (w < 3) {
        const u32x4 pw = *(const u32x4*)sm;
        if (w < 2) sums<true>(smem + X_IM + 16 * tid, pw, uw); else sums<false>(smem + X_IM + 16 * tid, pw, uw);
        count_up(ca + 28);
      }
    } else {
      until(tu + UD + WD);
      BAR();
    }
    tu = (uint32_t)__builtin_amdgcn_readfirstlane(cnt[4]);
    TS(0, 3);
    __builtin_amdgcn_s_setprio(DPRIO);
    if (ffn) {
      ts1 = (tu + UD + VD2 - TSTOP) | 1u;
      gu_dma(0, N0, IC<40>{});
      gu_dma(N0, NA, IC<WDEPTH>{});
      asm volatile("s_waitcnt vmcnt(%0)" : : "n"(NA - 9) : "memory");
      gsync0(ca + 4, 4);
      gu_read(0);
      asm volatile("s_waitcnt lgkmcnt(0)" ::: "memory");
      count_up(ca + 36);
      TS(0, 4);
      gu_dma(NA, 18, IC<WDEPTH2>{});
      gwait(ca + 36, 4);
      down_dma(0, KN, lane);
      asm volatile("s_waitcnt vmcnt(%0)" : : "n"(3 * KN) : "memory");
      count_up(ca + 4);
      if (STP && w == 1)
        asm volatile("global_load_dword v126, %0, %1 sc1" : : "v"(O_SC + 1024u * (fb >> 4) + 128u * (lane & 7)), "s"(ws) : "memory", "v126");
      TS(0, 7);
    } else {
      if (!isx && C3) {
        const int n = 6 * min(tid, 191) + bj - 18, r = n >> 2;
        const char* wb = (const char*)((n & 1) ? wu : wg) + bx * 1152;
        const int c0 = (3 - (int)((uintptr_t)wb >> 7)) & 7;
        wb += (uint32_t)((r + 144 * ((r >= 144) + ((n >> 1) & 1))) * (F_ * 2) + 128 * c0);
        TOUCH(wb);
        if (c0 == 0) TOUCH(wb + 1024);
      }
      auto d_line = [&](int k) {
        const int n = min((k * 14 + bj - 18) * 64 + lane, 5183), q = n / 1296, r = n - 1296 * q;
        return (const char*)wd + (uint32_t)(bx * 5184 + (r / 18) * 72 + 18 * (HREV ? 3 - q : q) + r % 18) * 128u;
      };
      if (isx && w == 3) v_look(tu, lane);
      int k = w;
      if (w < HW) {
#pragma unroll 1
        for (; k < 6 && (int)((uint32_t)__builtin_amdgcn_s_memrealtime() - (tu + UD + VD2 - HSTOP)) < 0; k += HW) { TOUCH(d_line(k)); DRAIN(); }
      }
      TS(0, 4);
      v_time();
      until(tv + RD2);
      if (w < HW) {
#pragma unroll 1
        for (; k < 6; k += HW) { TOUCH(d_line(k)); DRAIN(); }
      }
      TS(0, 7);
    }
    v_time();
    tk = tv + KD;
  }
  {
  const int tid = tid2, lane = tid & 63, i = lane & 15, kq = lane >> 4;
  __builtin_amdgcn_s_setprio(3);
  const int PL = ffn ? 6400 : LW + 6656;
  f32x2 xf[9];
  f32x4 za = {0.f, 0.f, 0.f, 0.f}, zb = za;
  uint32_t al, ha, h0a, h1a, so, da;
  auto ffn_terms = [&] {
    al = O_XC + 64u * (i >> 3) + 16u * kq + 128u * w;
    ha = (ROLE == 1) ? (uint32_t)((4 * (kq & 1) + 2 * (kq >> 1) + (i >> 3)) * 80 + 16 * w + 2 * (i & 7)) : 0u;
    h0a = (uint32_t)(min(i, 8) * 80 + 16 * kq); h1a = (uint32_t)(min(i ^ 8, 8) * 80 + 16 * kq);
    so = O_SL + (uint32_t)fb * SP + ((ROLE == 2) ? 16u * tid - 4096u : 8192u + 16u * lane);
    da = lb0 + (uint32_t)((8 * kq + ((lane >> 2) & 3)) * 1152 + 32 * ((8 * kq + ((lane >> 2) & 3) + 2) >> 2) + 8 * (lane & 3) + ((ROLE == 2) ? 128 * wl : 1024));
    if (ROLE == 2) asm volatile("" : "+v"(h0a), "+v"(h1a), "+v"(so), "+v"(da)); else asm volatile("" : "+v"(ha), "+v"(al), "+v"(da));
  };
  if (ROLE == 1) ffn_terms();
  uint32_t hv[9] = {}, nv[9] = {}, keya = 0u, keyb = 0u;
  int rka = 0, rkb = 0;
  auto put_row = [&](const uint32_t* r, int j, const void* base, uint32_t ro) {
    const u32x4 tl = {r[8], (uint32_t)DPP_I((int)r[8], 0x55, 0xf), (uint32_t)DPP_I((int)r[8], 0xaa, 0xf), (uint32_t)DPP_I((int)r[8], 0xff, 0xf)};
    asm volatile("global_store_dwordx4 %0, %1, %2 sc1\n\ts_nop 1" : : "v"(ro + 16u * lane), "v"((u32x4){r[4 * j], r[4 * j + 1], r[4 * j + 2], r[4 * j + 3]}), "s"(base) : "memory");
    if ((lane & 35) == 32 * j) asm volatile("global_store_dwordx4 %0, %1, %2 offset:1024 sc1\n\ts_nop 1" : : "v"(ro + 4u * (lane & 31)), "v"(tl), "s"(base) : "memory");
  };
  if (ln2) {
    const int tl = tid - 256, d1 = 64 * (tl >> 5) + (tl & 31), d3 = 64 * ((tl >> 4) & 7) + 32 + (tl & 15), d2 = (tl < 128) ? d3 + 16 : min(384 + tl, 575);
    uint32_t vo = v_at(d1, g0), vo2 = v_at(d2, g0), vo3 = v_at(d3, g0);
    u32x4 ut = {utag(T, 0), utag(T, 1), utag(T, 2), utag(T, 3)};
    u32x4 va, vc = ut, vb = ut;
    asm volatile("" : "+v"(vo), "+v"(vo2), "+v"(vo3), "+v"(ut), "+v"(vc), "+v"(vb));
    TS(0, 7);
    until(tv);
    TS(0, 8);
    asm volatile("global_load_dwordx4 %0, %2, %4 sc1\n\ts_cmp_lt_u32 %5, 2\n\ts_cbranch_scc1 1f\n\tglobal_load_dwordx4 %1, %3, %4 sc1\n1:"
                 : "=&v"(va), "+v"(vc) : "v"(vo), "v"(vo3), "s"(ws), "s"(wl) : "memory", "scc");
    until(tv + V2D);
    asm volatile("s_cmp_eq_u32 %3, 3\n\ts_cbranch_scc1 1f\n\tglobal_load_dwordx4 %0, %1, %2 sc1\n1:" : "+v"(vb) : "v"(vo2), "s"(ws), "s"(wl) : "memory", "scc");
    __builtin_amdgcn_sched_barrier(0);
    if (isx) {
      const u32x4 ra = *(const u32x4*)(smem + X_RW(wl) + 16 * lane), rb = *(const u32x4*)(smem + X_RW(wl) + 1024 + 16 * lane), rw = *(const u32x4*)(smem + X_WR + 16 * lane);
      const u32x2 rc = *(const u32x2*)(smem + X_RW(wl) + 2048 + 8 * lane);
#pragma unroll
      for (int k = 0; k < 4; ++k) { xv[k] = ra[k]; xv[4 + k] = rb[k]; wv[k] = rw[k]; }
      xv[8] = rc[0]; wv[4] = rc[1];
    }
    uint32_t pa = (uint32_t)(PL + 9216 * wl + 32 * lane), pb = (uint32_t)(PL + 9216 * wl + 2048 + 8 * (lane & 31)), ps = (lane < 32) ? 0u : 16u;
    uint32_t kid = (uint32_t)(((255 - g2) << 5) | (7 - 2 * wl - (lane >> 5)));
    uint32_t px = (uint32_t)(PL + 4 * d1), py = (uint32_t)(PL + 4 * d2), pz = (uint32_t)(PL + 4 * d3);
#pragma unroll
    for (int k = 0; k < 9; ++k) xf[k] = (f32x2){bflo(xv[k]), bfhi(xv[k])};
    asm volatile("" : "+v"(xf[0]), "+v"(xf[1]), "+v"(xf[2]), "+v"(xf[3]), "+v"(xf[4]), "+v"(xf[5]), "+v"(xf[6]), "+v"(xf[7]), "+v"(xf[8]));
    asm volatile("" : "+v"(za), "+v"(zb), "+v"(pa), "+v"(pb), "+v"(ps), "+v"(kid), "+v"(px), "+v"(py), "+v"(pz));
    float sv[10], ws_ = dot2(wv[4], 0x3f803f80u, 0.f) * 0.5f;
#pragma unroll
    for (int k = 0; k < 4; ++k) ws_ = dot2(wv[k], 0x3f803f80u, ws_);
    ws_ = wave_sum(ws_);
#pragma unroll
    for (int k = 0; k < 10; ++k) sv[k] = 16.f;
    uint32_t ka_ = lb0 + LM + 4u * (128 + 48 + 2 * wl + (lane >> 5)), kb_ = lb0 + LM + 4u * (128 + 48);
    uint32_t ko_ = O_KEY + (uint32_t)(256 * (lane & 7) + 8 * (g2 & 31) + (g2 >> 5)) * 16u;
    asm volatile("" : "+v"(ws_), "+v"(sv[0]), "+v"(sv[1]), "+v"(sv[2]), "+v"(sv[3]), "+v"(sv[4]), "+v"(sv[5]), "+v"(sv[6]), "+v"(sv[7]), "+v"(sv[8]), "+v"(sv[9]),
                 "+v"(ka_), "+v"(kb_), "+v"(ko_));
    if (ffn) gwait(ca + 36, 4);
    asm volatile("s_waitcnt vmcnt(1)\n\ts_cmp_eq_u32 %2, 3\n\ts_cbranch_scc0 1f\n\ts_waitcnt vmcnt(0)\n1:" : "+v"(va), "+v"(vc) : "s"(wl) : "memory", "scc");
    auto turn1 = [&] {
#pragma unroll
      for (int e = 0; e < 4; ++e) *(uint32_t*)(smem + px + 9216 * e) = va[e];
      if (wl >= 2) {
#pragma unroll
        for (int e = 0; e < 4; ++e) *(uint32_t*)(smem + pz + 9216 * e) = vc[e];
      }
    };
    auto turn = [&] {
      if (wl < 3) {
#pragma unroll
        for (int e = 0; e < 4; ++e) *(uint32_t*)(smem + py + 9216 * e) = vb[e];
      }
    };
    va ^= ut; vc ^= ut;
    turn1();
    asm volatile("s_waitcnt vmcnt(0)" : "+v"(vb) : : "memory");
    vb ^= ut;
    turn();
    const u32x4 vs = va | vb | vc;
    if (__builtin_expect(__any(((vs[0] | vs[1] | vs[2] | vs[3]) & UMASK) != 0u), 0)) {
#pragma unroll 1
      for (int spin = 0; spin < 4; ++spin) {
        const bool na = ((va[0] | va[1] | va[2] | va[3]) & UMASK) != 0u, nb = ((vb[0] | vb[1] | vb[2] | vb[3]) & UMASK) != 0u, nc = ((vc[0] | vc[1] | vc[2] | vc[3]) & UMASK) != 0u;
        if (!__any(na || nb || nc)) break;
        TSC(16);
        if (na) LD16(va, vo);
        if (nb) LD16(vb, vo2);
        if (nc) LD16(vc, vo3);
        asm volatile("s_waitcnt vmcnt(0)" : "+v"(va), "+v"(vb), "+v"(vc) : : "memory");
        if (na) va ^= ut;
        if (nb) vb ^= ut;
        if (nc) vc ^= ut;
      }
      auto again = [&](u32x4& v, uint32_t o, int d) {
        const int J = g0 >> 5;
        const uint32_t dn = O_DN + 16u * (uint32_t)((d < 512) ? 4 * (d >> 1) + ((0x01013232 >> (4 * J)) & 3) : 4 * (d - 256) + (J >> 1));
        bool pend = ((v[0] | v[1] | v[2] | v[3]) & UMASK) != 0u;
#pragma unroll 1
        for (int spin = 0; spin < SPIN_CAP && __any(pend); ++spin) {
          TSC(16);
          SLEEP(PSLEEP);
          if (pend) {
            uint32_t fin = ~T;
            if ((v[0] & v[1] & v[2] & v[3] & LMASK) == LMASK)
              asm volatile("global_load_dword %0, %1, %2 sc1\n\ts_waitcnt vmcnt(0)" : "=&v"(fin) : "v"(dn), "s"(ws) : "memory");
            LD16W(v, o);
            v ^= ut;
            pend = ((v[0] | v[1] | v[2] | v[3]) & UMASK) != 0u;
            if (pend && fin == T) {
              u32x2 sc;
              for (int k = 0; k < SPIN_CAP; ++k) {
                asm volatile("global_load_dwordx2 %0, %1, %2 sc1\n\ts_waitcnt vmcnt(0)" : "=&v"(sc) : "v"(O_SC + 16u * d), "s"(ws) : "memory");
                if (sc[0] == T) break;
              }
              *(u16*)(smem + L_SC + 2 * d) = (u16)(sc[1] >> 16);
              cnt[10] = 1u;
              v = (v ^ ut) & ~LMASK;
              pend = false;
            }
          }
        }
      };
      again(va, vo, d1); again(vc, vo3, d3); again(vb, vo2, d2);
      turn1();
      turn();
    }
    u32x4 a0, a1;
    u32x2 a2, cf;
    {
      uint32_t cc, n = SPIN_CAP;
      asm volatile("s_mov_b64 exec, 1\n\tv_mov_b32 v90, 1\n\tds_add_u32 %6, v90\n\ts_mov_b64 exec, -1\n"
                   "1:\n\tds_read2_b32 %3, %6 offset1:10\n\tds_read_b128 %0, %7\n\tds_read_b128 %1, %7 offset:16\n\tds_read_b64 %2, %8\n\t"
                   "s_sub_u32 %5, %5, 1\n\ts_cbranch_scc1 2f\n\ts_waitcnt lgkmcnt(3)\n\tv_readfirstlane_b32 %4, v90\n\t"
                   "s_cmp_lt_u32 %4, 4\n\ts_cbranch_scc1 1b\n2:\n\ts_waitcnt lgkmcnt(0)"
                   : "=&v"(a0), "=&v"(a1), "=&v"(a2), "=&{v[90:91]}"(cf), "=&s"(cc), "+s"(n) : "v"(ca), "v"(lb0 + pa), "v"(lb0 + pb) : "memory", "scc");
    }
    TS(0, 9);
    if (__builtin_expect(__builtin_amdgcn_readfirstlane(cf[1]) != 0, 0)) {
      const u32x4 q = *(const u32x4*)(smem + L_SC + 16 * lane);
      const uint32_t r = *(const uint32_t*)(smem + L_SC + 1024 + 4 * (lane & 31));
#pragma unroll
      for (int k = 0; k < 4; ++k) { sv[2 * k] = bflo(q[k]); sv[2 * k + 1] = bfhi(q[k]); }
      sv[8] = bflo(r); sv[9] = bfhi(r);
    }
    const uint32_t av[10] = {a0[0], a0[1], a0[2], a0[3], a1[0], a1[1], a1[2], a1[3], a2[0] >> ps, a2[1] >> ps};
    f32x2 h[9];
#pragma unroll
    for (int k = 0; k < 10; ++k) {
      asm("v_fma_mix_f32 %0, %1, %2, %3 op_sel_hi:[1,0,0]" : "=v"(h[(k < 8) ? k >> 1 : 8][k & 1]) : "v"(av[k]), "v"(sv[k]), "v"(xf[(k < 8) ? k >> 1 : 8][k & 1]));
      if (k < 8) asm("v_fma_mix_f32 %0, %1, %2, %3 op_sel:[1,0,0] op_sel_hi:[1,0,0]" : "=v"(h[4 + (k >> 1)][k & 1]) : "v"(av[k]), "v"(sv[k]), "v"(xf[4 + (k >> 1)][k & 1]));
    }
#pragma unroll
    for (int k = 0; k < 9; ++k) hv[k] = pack2(h[k][0], h[k][1]);
    const float da = dot2(hv[2], wv[2], dot2(hv[0], wv[0], zb[0])) + dot2(hv[3], wv[3], dot2(hv[1], wv[1], zb[1]));
    const float db = dot2(hv[6], wv[2], dot2(hv[4], wv[0], zb[2])) + dot2(hv[7], wv[3], dot2(hv[5], wv[1], zb[3]));
    float dt = dot2(hv[8], wv[4], fold32(da, db));
    f32x2 rc, nc;
    ln_stats<true>(hv, lane, 1.f, xf, za, rc, nc, dt);
    dt = (lane < 32) ? dt * rc[0] + ws_ * nc[0] : dt * rc[1] + ws_ * nc[1];
    const uint32_t bb = pack2(dt, dt) & 0xffff0000u;
    const uint32_t kk = (bb ^ (((uint32_t)((int)bb >> 31) & 0x7fff0000u) | 0x80000000u)) | kid;
    u32x4 ka, kb;
    uint32_t old;
    asm volatile("s_mov_b64 exec, %5\n\tds_write_b32 %3, %4\n\ts_mov_b64 exec, 1\n\tv_mov_b32 %2, 1\n\tds_add_rtn_u32 %2, %7, %2\n\ts_mov_b64 exec, -1\n\t"
                 "ds_read_b128 %0, %6\n\tds_read_b128 %1, %6 offset:16\n\ts_waitcnt lgkmcnt(0)"
                 : "=&v"(ka), "=&v"(kb), "=&v"(old) : "v"(ka_), "v"(kk), "s"(0x8000000080000000ull), "v"(kb_), "v"(ca) : "memory");
    if (__builtin_amdgcn_readfirstlane(old) == 7) {
      uint32_t m1 = max(max(ka[0], ka[1]), ka[2]), m2 = med3u(ka[0], ka[1], ka[2]), m3 = min(min(ka[0], ka[1]), ka[2]);
#pragma unroll
      for (int e = 0; e < 5; ++e) {
        const uint32_t kx = e ? kb[e - 1] : ka[3];
        m3 = med3u(m2, m3, kx); m2 = med3u(m1, m2, kx); m1 = max(m1, kx);
      }
      if (lane < 8) ST16(ko_, ((u32x4){m1 | 24u, m2 | 16u, m3 | 8u, T & ~1u}));
    }
    TS(0, 10);
    keya = (uint32_t)__builtin_amdgcn_readlane(kk, 31); keyb = (uint32_t)__builtin_amdgcn_readlane(kk, 63);
    ln_apply<false>(xf, lane, rc, nc, nv);
    gwait(ca, 8);
    if (wl == 1) {
      const u32x4 kv = *(const u32x4*)&kl[4 * (lane & 1)];
      if (lane < 2) ST16(O_KF + (uint32_t)(2 * g2 + lane) * 16u, ((u32x4){__builtin_amdgcn_perm(kv[1], kv[0], 0x07060302u), T, __builtin_amdgcn_perm(kv[3], kv[2], 0x07060302u), T}));
      if (ROLE == 2 && b == 0 && lane == 0) ST4(O_EP, T * 0x0E8B2F51u);
    }
    const uint32_t ko = kl[lane & 7];
    rka = FORCEFB + __popcll(__ballot(ko > keya) & 0xffull); rkb = FORCEFB + __popcll(__ballot(ko > keyb) & 0xffull);
#pragma unroll
    for (int k = 0; k < 9; ++k) {
      const f32x2 o = xf[k] + (f32x2){bflo(nv[k]), bfhi(nv[k])};
      hv[k] = pack2(o[0], o[1]);
      nv[k] = min(nv[k], 0xfffffffeu);
    }
    if (rka < 2) put_row(nv, 0, ws, O_XC + (uint32_t)(1023 - 4 * g2 - rka) * 1152u);
    if (rkb < 2) put_row(nv, 1, ws, O_XC + (uint32_t)(1023 - 4 * g2 - rkb) * 1152u);
    if (OTP && lane < 18 && ((lane < 9) ? rka : rkb) < 2)
      asm volatile("global_load_dword v126, %0, %1" OPOL : : "v"((uint32_t)((8 * g2 + 2 * wl) * (D_ * 2) + 128 * lane)), "s"(out) : "memory", "v126");
    if (!LPOS && ffn) down_dma(KN, 4, lane);
    ffn_terms();
  }
  uint32_t k3rd = 0u, k4th = 0u;
  uint32_t q0 = lb0 + LM + 4u * lane, q1 = lb0 + LM + 32u * w, q2 = q1 + 4u * (lane & 7), q3 = lb0 + LM + 512u + 4u * (lane & 7), q4 = lb0 + LM + (uint32_t)(tid & ~7);
  uint32_t r8 = C_, sb = lb0 + LM + 512u;
  asm volatile("" : "+v"(q0), "+v"(q1), "+v"(q2), "+v"(q3), "+v"(q4), "+v"(r8), "+v"(sb));
  if constexpr (ROLE == 1) {
    u32x4 key;
    const uint32_t kof = O_KEY + (uint32_t)(256 * bx + tid) * 16u;
    until(tk);
    if (!ffn) SLEEP(NSLEEP);
    LD16(key, kof);
    if (ffn) {
      gwait0(ca + 4, 8);
      gu_read(1);
    }
    asm volatile("s_waitcnt vmcnt(0)" : "+v"(key) : : "memory");
    if (__builtin_expect(__any(key[3] != (T & ~1u)), 0)) {
#pragma unroll 1
      for (int spin = 0; spin < SPIN_CAP; ++spin) {
        const bool need = key[3] != (T & ~1u);
        if (!__any(need)) break;
        TSC(15);
        SLEEP(KBACK);
        if (need) LD16W(key, kof);
      }
    }
    TS(0, 11);
    const uint32_t m1 = oct_maxu(key[0]);
    const bool hit = key[0] == m1;
    const uint32_t k0 = hit ? key[1] : key[0], k1 = hit ? key[2] : key[1];
    const uint32_t m2 = oct_maxu(k0);
    *LP(u32x2, q4) = (u32x2){m1, m2};
    k3rd = (k0 == m2) ? k1 : k0;
    k4th = (k0 == m2) ? (hit ? 0u : key[2]) : k1;
    if (RPF == 1 && !ffn && w == (bj & 3)) {
      const uint32_t r1 = O_XC + ((m1 >> 3) & 0x3ffu) * 1152u + 128u * (lane & 7), r2 = O_XC + ((m2 >> 3) & 0x3ffu) * 1152u + 128u * (lane & 7);
      asm volatile("global_load_dword v126, %0, %3 sc1\n\tglobal_load_dword v126, %1, %3 sc1\n\tglobal_load_dword v126, %2, %3 offset:1024 sc1"
                   : : "v"(r1), "v"(r2), "v"(((lane & 1) ? r1 : r2) - 128u * (lane & 7)), "s"(ws) : "memory", "v126");
    }
  }
  auto pick = [&](auto co, auto so_) {
    BAR();
    const uint32_t c = *LP(uint32_t, q0 + co.value), own = *LP(uint32_t, q2 + co.value);
    const u32x4 la = *LP(u32x4, q1 + co.value), lb = *LP(u32x4, q1 + co.value + 16);
    uint32_t r = r8;
    asm("v_cmp_gt_u32 s[40:41], %1, %2\n\tv_cmp_gt_u32 s[42:43], %1, %3\n\tv_cmp_gt_u32 s[44:45], %1, %4\n\tv_cmp_gt_u32 s[46:47], %1, %5\n\t"
        "v_cmp_gt_u32 s[48:49], %1, %6\n\tv_cmp_gt_u32 s[50:51], %1, %7\n\tv_cmp_gt_u32 s[52:53], %1, %8\n\tv_cmp_gt_u32 s[54:55], %1, %9\n\t"
        "s_bcnt1_i32_b64 s40, s[40:41]\n\ts_bcnt1_i32_b64 s42, s[42:43]\n\tv_writelane_b32 %0, s40, 0\n\ts_bcnt1_i32_b64 s44, s[44:45]\n\t"
        "v_writelane_b32 %0, s42, 1\n\ts_bcnt1_i32_b64 s46, s[46:47]\n\tv_writelane_b32 %0, s44, 2\n\ts_bcnt1_i32_b64 s48, s[48:49]\n\t"
        "v_writelane_b32 %0, s46, 3\n\ts_bcnt1_i32_b64 s50, s[50:51]\n\tv_writelane_b32 %0, s48, 4\n\ts_bcnt1_i32_b64 s52, s[52:53]\n\t"
        "v_writelane_b32 %0, s50, 5\n\ts_bcnt1_i32_b64 s54, s[54:55]\n\tv_writelane_b32 %0, s52, 6\n\tv_writelane_b32 %0, s54, 7"
        : "+v"(r) : "v"(c), "v"(la[0]), "v"(la[1]), "v"(la[2]), "v"(la[3]), "v"(lb[0]), "v"(lb[1]), "v"(lb[2]), "v"(lb[3])
        : "scc", "s40", "s41", "s42", "s43", "s44", "s45", "s46", "s47", "s48", "s49", "s50", "s51", "s52", "s53", "s54", "s55");
    *LP(uint32_t, sb + 4u * min(r, (uint32_t)C_) + so_.value) = own;
    BAR();
  };
  auto rows = [&](uint32_t ao, auto&& meanwhile) {
    u32x4 aa, ab, ac = __builtin_bit_cast(u32x4, zb);
    asm volatile("global_load_dwordx4 %0, %3, %4 sc1\n\tglobal_load_dwordx4 %1, %3, %4 offset:512 sc1\n\t"
                 "s_cmp_lg_u32 %5, 0\n\ts_cbranch_scc1 1f\n\tglobal_load_dwordx4 %2, %3, %4 offset:1024 sc1\n1:"
                 : "=&v"(aa), "=&v"(ab), "+v"(ac) : "v"(ao), "s"(ws), "s"(w) : "memory", "scc");
    __builtin_amdgcn_sched_barrier(0);
    meanwhile();
    asm volatile("s_waitcnt vmcnt(0)" : "+v"(aa), "+v"(ab), "+v"(ac) : : "memory");
    if (__builtin_expect(__any(max(max(max4(aa), max4(ab)), max4(ac)) == 0xffffffffu), 0)) {
#pragma unroll 1
      for (int spin = 0; spin < SPIN_CAP; ++spin) {
        const bool na = max4(aa) == 0xffffffffu, nb = max4(ab) == 0xffffffffu, nc = max4(ac) == 0xffffffffu;
        if (!__any(na || nb || nc)) break;
        TSC(14);
        SLEEP(PSLEEP);
        if (na) LD16W(aa, ao);
        if (nb) LD16W(ab, ao + 512u);
        if (nc) LD16W(ac, ao + 1024u);
      }
    }
    *(u32x4*)(smem + LA + 16 * tid) = aa;
    *(u32x4*)(smem + LA + 4096 + 16 * tid) = ab;
    if (w == 0) *(u32x4*)(smem + LA + 8192 + 16 * tid) = ac;
  };
  auto rows_dma = [&](uint32_t ao) {
    uint32_t dl = lb0 + LA + 1024u * w, keep;
    asm volatile("s_mov_b32 %0, m0\n\ts_mov_b32 m0, %1\n\ts_add_u32 %1, %1, 4096 - 512\n\tglobal_load_lds_dwordx4 %2, %3 sc1\n\ts_mov_b32 m0, %1\n\ts_add_u32 %1, %1, 4096 - 512\n\t"
                 "global_load_lds_dwordx4 %2, %3 offset:512 sc1\n\ts_cmp_lg_u32 %4, 0\n\ts_cbranch_scc1 1f\n\ts_mov_b32 m0, %1\n\ts_nop 0\n\t"
                 "global_load_lds_dwordx4 %2, %3 offset:1024 sc1\n1:\n\ts_mov_b32 m0, %0" : "=&s"(keep), "+s"(dl) : "v"(ao), "s"(ws), "s"(w) : "memory", "scc");
  };
  auto get_bd = [&](int t0, int t1) {
#pragma unroll
    for (int t = t0; t < t1; ++t) {
      const u32x2 h0 = trd(da + 512 * (t >> 2) + 32 * (t & 3)), h1 = trd(da + 512 * (t >> 2) + 32 * (t & 3) + 4608 + 32);
      bw[t] = (u32x4){h0[0], h0[1], h1[0], h1[1]};
    }
#pragma unroll
    for (int t = t0; t < t1; ++t) asm volatile("" : "+v"(bw[t]));
  };
  pick(IC<0>{}, IC<0>{});
  uint32_t mykey = *LP(uint32_t, q3), ro = ((mykey >> 3) & 0x3ffu) * 1152u;
  TS(0, 12);
  auto check = [&] {
    cnt[(k3rd > (uint32_t)__builtin_amdgcn_readlane(mykey, 7)) ? 5 : 6] = 1u;
    const uint32_t m3 = oct_maxu(k3rd);
    if (k3rd == m3) k3rd = k4th;
    if ((lane & 7) == 0) cand[72 + (tid >> 3)] = m3;
    if (w == 0 && lane < 32) cand[(lane < 8) ? 64 + lane : 96 + lane] = (lane < 8) ? mykey : 0u;
  };
  if constexpr (ROLE == 1) {
    if (ffn && !FORCEFB) rows_dma(ro + al);
    check();
    DRAIN();
  } else if (ffn) {
    if (LPOS) { SLEEP(LSLEEP); down_dma(KN, 4, lane); } else { DRAIN(); gsync0(ca, 12); get_bd(0, 8); }
  }
  BAR();
  TS(0, 5);
  auto put_out = [&](int j, uint32_t k0, int rk) {
    const uint64_t mine = __ballot(((mykey ^ k0) & ~24u) == 0u) & 0xffull;
    if (mine == 0ull) put_row(hv, j, out, (uint32_t)((8 * g2 + 2 * wl + j) * (D_ * 2)));
    else if (rk >= 2) put_row(nv, j, ws, O_FB + 1152u * (uint32_t)__builtin_ctzll(mine));
  };
  auto rest_out = [&] { put_out(0, keya, rka); put_out(1, keyb, rkb); };
  f32x4 lo, hi;
  auto gate_up = [&] {
    u32x4 av[5];
    const uint32_t aa = lb0 + LA + 16u * lane;
#pragma unroll
    for (int j = 0; j < 5; ++j) asm volatile("ds_read_b128 %0, %1 offset:%2" : "=v"(av[j]) : "v"(aa), "n"(1024 * j) : "memory");
    lo = (f32x4){0.f, 0.f, 0.f, 0.f}; hi = lo;
#pragma unroll
    for (int j = 0; j < 9; ++j) {
      if (j < 5) asm volatile("s_waitcnt lgkmcnt(4)" : "+v"(av[j % 5]));
      else if (j == 5) asm volatile("s_waitcnt lgkmcnt(2)" : "+v"(av[0]), "+v"(av[1]));
      else if (j == 7) asm volatile("s_waitcnt lgkmcnt(0)" : "+v"(av[2]), "+v"(av[3]));
      lo = MMB(av[j % 5], bw[2 * j], lo);
      hi = MMB(av[j % 5], bw[2 * j + 1], hi);
      __builtin_amdgcn_sched_barrier(0);
      if (j < 4) asm volatile("ds_read_b128 %0, %1 offset:%2" : "=v"(av[j]) : "v"(aa), "n"(1024 * (j + 5)) : "memory");
    }
  };
  uint32_t fl;
  asm volatile("ds_read_b32 %0, %1" : "=v"(fl) : "v"(ca + 20) : "memory");
  if (ROLE == 1) gate_up();
  asm volatile("s_waitcnt lgkmcnt(0)" : "+v"(fl));
  const bool slow = FORCESLOW || FORCEFB || __builtin_amdgcn_readfirstlane(fl) != 0;
  if (__builtin_expect(slow, 0)) {
    TSC(17);
    pick(IC<256>{}, IC<64>{});
    mykey = sel[16 + (lane & 7)];
    ro = ((mykey >> 3) & 0x3ffu) * 1152u;
    if (ROLE == 1 && (k3rd > (uint32_t)__builtin_amdgcn_readlane(mykey, 7) || (mykey & 24u) == 8u)) cnt[11] = 1u;
    BAR();
    if (FORCESLOW > 1 || FORCEFB || __builtin_amdgcn_readfirstlane(cnt[11]) != 0) {
    u32x4 key;
#pragma unroll 1
    for (int spin = 0; spin < SPIN_CAP; ++spin) {
      LD16W(key, O_KF + 16u * tid);
      if (!__any(key[1] != T || key[3] != T)) break;
      SLEEP(KBACK);
    }
    const uint32_t ib = (uint32_t)(((255 - (tid >> 1)) << 5) | (7 - 4 * (tid & 1)));
    uint32_t k0 = (key[0] << 16) | ib, k1 = (key[0] & 0xffff0000u) | (ib - 1), k2 = (key[2] << 16) | (ib - 2), k3 = (key[2] & 0xffff0000u) | (ib - 3);
    CEX(k0, k1) CEX(k2, k3) CEX(k0, k2) CEX(k1, k3) CEX(k1, k2)
#pragma unroll 1
    for (int c = 0; c < C_; ++c) {
      const uint32_t mx = wave_maxu(k0);
      const bool hit = (k0 == mx);
      k0 = hit ? k1 : k0; k1 = hit ? k2 : k1; k2 = hit ? k3 : k2; k3 = hit ? 0u : k3;
      if (lane == 0) cand[64 + 8 * w + c] = mx;
    }
    pick(IC<256>{}, IC<128>{});
    mykey = sel[32 + (lane & 7)];
    const uint32_t okey = sel[32 + (lane >> 3)];
    const uint64_t same = __ballot(((mykey ^ okey) & 0x1fe0u) == 0u && okey > mykey);
    const int sl = FORCEFB + __popcll((same >> (lane & 7)) & 0x0101010101010101ull);
    ro = (sl < 2) ? (((mykey >> 3) & 0x3ffu) + 3 - sl) * 1152u : O_FB - O_XC + 1152u * (lane & 7);
    }
    if (ln2) rest_out();
    if (ffn) {
      if (ROLE == 1) rows(ro + al, [] {});
      BAR();
      if (ROLE == 1) gate_up();
    }
  }
  if (OPOS == 0 && ROLE == 2 && ffn && !slow) { SLEEP(OD); rest_out(); }
  if (!ffn) {
    if (ln2 && !slow) { SLEEP(OD2); rest_out(); }
    TSE();
    return;
  }
  uint32_t fx = 0, fv = 0, fn = 0, fo = 0, sa[9];
  float rs, hx, xs;
  u32x4 se;
  if constexpr (ROLE == 1) {
    f32x4 lh = (lane < 32) ? lo : hi;
    float fa = fold32(lh[0], lh[2]), fb2 = fold32(lh[1], lh[3]);
    if (__builtin_expect(__any(__builtin_isunordered(fa, fb2)) && !slow, 0)) {
      gsync0(ca + 4, 12);
      rows(ro + al, [] {});
      gsync0(ca + 4, 16);
      gate_up();
      lh = (lane < 32) ? lo : hi;
      fa = fold32(lh[0], lh[2]); fb2 = fold32(lh[1], lh[3]);
    }
    const float g = __int_as_float(__builtin_amdgcn_update_dpp(__float_as_int(fa), __float_as_int(fb2), 0x128, 0xf, 0xc, false));
    const float u = __int_as_float(__builtin_amdgcn_update_dpp(__float_as_int(fb2), __float_as_int(fa), 0x128, 0xf, 0x3, false));
    const uint32_t gu = pack2(g, u);
    const float gp = bflo(gu);
    const float sg = rbf(gp * __builtin_amdgcn_rcpf(1.f + __expf(-gp)));
    *(u16*)((char*)hm + ha) = f2bf(sg * bfhi(gu));
  } else if (LPOS) {
    DRAIN();
    gsync0(ca, 12);
    get_bd(0, 8);
  }
  BAR();
  TS(0, 6);
  if (ROLE == 2 || w == 2) {
    const u32x4 a0 = *(const u32x4*)((char*)hm + h0a), a1 = *(const u32x4*)((char*)hm + h1a);
    if (ROLE == 1) get_bd(0, 4);
    const f32x4 z = {0.f, 0.f, 0.f, 0.f};
    f32x4 o[4];
#pragma unroll
    for (int t = 0; t < 2 * ROLE; ++t) o[t] = MMB(a1, bw[2 * t + 1], MMB(a0, bw[2 * t], z));
#pragma unroll
    for (int t = 0; t < ROLE; ++t) {
      u32x4 sv;
#pragma unroll
      for (int r = 0; r < 4; ++r) sv[r] = min(pack2(o[2 * t][r], o[2 * t + 1][r]), 0xfffffffeu);
      ST16(so + 4096u * t, sv);
    }
    TS(0, 13);
  }
  if (OPOS == 1 && ROLE == 2) {
    SLEEP(OSLEEP);
    if (!slow) rest_out();
  }
  if (ROLE == 1 && w == 1) {
    const int q = (fb & 15) >> 2, ts = 4 * (q & 1) + ((i & 7) >> 1);
    const int dd = 64 * (fb >> 4) + 16 * (2 * (i & 1) + (q >> 1)) + 4 * (fb & 3) + 2 * (kq & 1) + (kq >> 1);
    const uint32_t fkey = (uint32_t)__builtin_amdgcn_ds_bpermute(4 * ts, (int)mykey), fr = (uint32_t)__builtin_amdgcn_ds_bpermute(4 * ts, (int)ro);
    const int ft = 2047 - (int)(((fkey >> 2) & 0x7f8u) | (fkey & 7u));
    fo = (uint32_t)(ft * D_ + dd) * 2u;
    u32x2 fs;
    asm volatile("global_load_ushort %0, %4, %8\n\tglobal_load_ushort %1, %5, %9 sc1\n\tglobal_load_ushort %2, %6, %9 sc1\n\tglobal_load_dwordx2 %3, %7, %9 sc1"
                 : "=&v"(fx), "=&v"(fv), "=&v"(fn), "=&v"(fs) : "v"(fo), "v"(v_at(dd, ft >> 3) + 2u * (ft & 7)), "v"(O_XC + fr + 2u * dd), "v"(O_SC + 16u * dd), "s"(x), "s"(ws) : "memory");
    uint32_t t16 = (((T >> (3 * ((ft >> 1) & 3))) & LMASK) | (((T >> (12 + ((ft >> 1) & 3))) & 0x00010001u) << 14)) >> (16 * (ft & 1)) & 0xffffu;
    const uint32_t o = fkey >> 16;
    const float rs0 = bf2f((u16)((o & 0x8000u) ? (o & 0x7fffu) : (~o & 0xffffu)));
    rs = rbf(__builtin_amdgcn_rcpf(1.f + __expf(-rs0)));
    const uint32_t one = (i < 8) ? (0x3f80u << (16 * (i & 1))) : 0u;
    se = (u32x4){(i >> 1) == 0 ? one : 0u, (i >> 1) == 1 ? one : 0u, (i >> 1) == 2 ? one : 0u, (i >> 1) == 3 ? one : 0u};
#pragma unroll
    for (int m = 0; m < 9; ++m) sa[m] = O_SL + (uint32_t)(16 * m + (lane >> 2)) * SP + (uint32_t)(4 * fb + (lane & 3)) * 16u;
    asm volatile("" : "+v"(t16), "+v"(rs), "+v"(se), "+v"(sa[0]), "+v"(sa[1]), "+v"(sa[2]), "+v"(sa[3]), "+v"(sa[4]), "+v"(sa[5]), "+v"(sa[6]), "+v"(sa[7]), "+v"(sa[8]));
    SLEEP(SSLEEP);
    if (FD) until(tk + FD);
    TS(0, 1);
    float sum;
#pragma unroll 1
    for (int spin = 0; spin < SPIN_CAP; ++spin) {
      u32x4 sv[9];
#pragma unroll
      for (int m = 0; m < 9; ++m) LD16(sv[m], sa[m]);
      asm volatile("s_waitcnt vmcnt(9)" : "+v"(fx), "+v"(fv), "+v"(fn), "+v"(fs));
      if (__builtin_expect(__any(fs[0] != T), 0)) {
        for (int k = 0; k < SPIN_CAP && fs[0] != T; ++k)
          asm volatile("global_load_dwordx2 %0, %1, %2 sc1\n\ts_waitcnt vmcnt(0)" : "=&v"(fs) : "v"(O_SC + 16u * dd), "s"(ws) : "memory");
      }
      const uint32_t f1 = fv ^ t16, f2 = (f1 & 7u) ? fv & ~7u : f1;
      hx = rbf(bf2f((u16)fx) + (float)__builtin_bit_cast(_Float16, (u16)f2) * __uint_as_float(fs[1]));
      xs = bf2f((u16)fn);
      asm volatile("" : "+v"(hx), "+v"(xs));
      f32x4 dv = {0.f, 0.f, 0.f, 0.f};
      static_for<0, 9>([&](auto mm) {
        constexpr int m = mm.value;
        asm volatile("s_waitcnt vmcnt(%1)" : "+v"(sv[m]) : "n"(8 - m));
        dv = MMB(sv[m], se, dv);
        __builtin_amdgcn_sched_barrier(0);
      });
      sum = fold16(fold32(dv[0], dv[1]), fold32(dv[2], dv[3]));
      if (__builtin_expect(!__any(sum != sum), 1)) break;
      uint32_t mx = 0u;
#pragma unroll
      for (int m = 0; m < 9; ++m) mx = max(mx, max4(sv[m]));
      if (!__any(mx == 0xffffffffu)) break;
      TSC(18);
      SLEEP(PSLEEP);
    }
    const float o1 = rbf(xs + rbf(rbf(sum) * rs));
    if (i < 8) asm volatile("global_store_short %0, %1, %2 sc1" : : "v"(fo), "v"((uint32_t)f2bf(hx + o1)), "s"(out) : "memory");
    TS(0, 2);
  }
  TSE();
  }
  }
}
#define BROT "192"
#define CPF_A "s_getpc_b64 s[96:97]\n\ts_lshl_b32 s99, s2, 4\n\ts_lshl_b32 s84, s98, 7\n\ts_add_u32 s99, s99, s84\n\tv_and_b32 v112, 1, v0\n\t" \
              "v_lshl_add_u32 v112, v112, 12, s99\n\tv_min_u32 v112, kf_end-kf_all-0x80, v112\n\ts_mov_b64 exec, 3\n\tglobal_load_dword v112, v112, s[96:97]\n\ts_mov_b64 exec, -1\n\t"
extern "C" __global__ __launch_bounds__(512, 4) void kf_all(KARGS) {
  __shared__ __attribute__((aligned(16))) char smem[SMEM];
  asm volatile("s_load_dwordx16 s[64:79], s[0:1], 0x0\n\ts_load_dwordx4 s[80:83], s[0:1], 0x40\n\tv_readfirstlane_b32 s98, v0\n\t"
               "s_add_u32 s84, s2, " BROT "\n\ts_xor_b32 s84, s84, s2\n\ts_bitset0_b32 s84, 8\n\ts_bitset0_b32 s84, 9\n\ts_xor_b32 s2, s2, s84\n\t"
               "v_mov_b32 v109, 0\n\ts_and_b32 s85, s2, 7\n\ts_bitcmp1_b32 s2, 8\n\ts_cbranch_scc1 1f\n\t" CPF_A "s_bitcmp1_b32 s98, 8\n\ts_cbranch_scc1 2f\n\t"
               "s_andn2_b32 s84, s2, 31\n\ts_lshl2_add_u32 s84, s85, s84\n\ts_bfe_u32 s85, s2, 0x20003\n\ts_or_b32 s84, s84, s85\n\t"
               "s_mulk_i32 s84, 0x2400\n\ts_mulk_i32 s98, 36\n\ts_add_u32 s84, s84, s98\n\tv_and_b32 v110, 63, v0\n\tv_and_b32 v111, 32, v0\n\t"
               "v_lshlrev_b32 v111, 5, v111\n\tv_lshl_add_u32 v111, v110, 2, v111\n\tv_lshl_add_u32 v110, v110, 4, s84\n\tv_add_u32 v111, s84, v111\n\t"
               "s_cmpk_lt_u32 s2, 0x90\n\ts_waitcnt lgkmcnt(0)\n\t"
               "global_load_dwordx4 v[100:103], v110, s[64:65]\n\tglobal_load_dwordx4 v[104:107], v110, s[64:65] offset:1152\n\t"
               "global_load_dword v108, v111, s[64:65] offset:1024\n\tglobal_load_dword v109, v109, s[82:83] sc1\n\t"
               "s_cbranch_scc1 kf_n\n\ts_branch kf_h\n"
               "1:\n\ts_bfe_u32 s84, s2, 0x50003\n\ts_lshl_b32 s85, s85, 5\n\ts_or_b32 s84, s84, s85\n\ts_lshl_b32 s84, s84, 2\n\t"
               "v_and_b32 v110, 0xff, v0\n\tv_bfe_u32 v107, v0, 7, 1\n"
               "4:\n\ts_lshl_b32 s85, s84, 7\n\tv_lshl_add_u32 v110, v110, 1, s85\n\tv_lshl_add_u32 v107, v107, 1, s84\n\ts_and_b32 s85, s98, s2\n\ts_waitcnt lgkmcnt(0)\n\t"
               "global_load_ushort v100, v107, s[78:79]\n\tglobal_load_ushort v101, v110, s[66:67]\n\tglobal_load_ushort v102, v110, s[68:69]\n\t"
               "global_load_dword v109, v109, s[82:83] sc1\n\ts_bitcmp1_b32 s85, 8\n\ts_cbranch_scc1 kf_u\n\t"
               "global_load_ushort v103, v110, s[70:71]\n\tglobal_load_ushort v104, v110, s[72:73]\n\tglobal_load_ushort v105, v110, s[74:75]\n\t"
               "global_load_ushort v106, v110, s[76:77]\n\tglobal_load_ushort v107, v107, s[80:81]\n\ts_bitcmp1_b32 s2, 8\n\ts_cbranch_scc1 kf_c\n"
               "6:\n\ts_barrier\n\ts_branch kf_p\n"
               "2:\n\ts_cmpk_lt_u32 s2, 0xc0\n\ts_cbranch_scc1 6b\n\t"
               "s_lshr_b32 s84, s2, 3\n\ts_lshl3_add_u32 s84, s85, s84\n\ts_lshl1_add_u32 s84, s84, 976\n\t"
               "v_and_b32 v110, 0x7f, v0\n\tv_mov_b32 v107, 0\n\ts_branch 4b"
               : : "s"(__builtin_amdgcn_kernarg_segment_ptr()), "s"(blockIdx.x), "v"(threadIdx.x),
                   "s"((uint32_t)(uintptr_t)(__attribute__((address_space(3))) char*)smem) : "scc", "s96", "s97", "s98", "s99", "v127");
}
extern "C" __global__ __launch_bounds__(512, 4) void kf_c(KARGS) {
  __shared__ __attribute__((aligned(16))) char smem[SMEM];
  role_body<0>(smem, KPASS);
}
extern "C" __global__ __launch_bounds__(512, 4) void kf_u(KARGS) {
  __shared__ __attribute__((aligned(16))) char smem[SMEM];
  role_body<3>(smem, KPASS);
}
extern "C" __global__ __launch_bounds__(512, 4) void kf_n(KARGS) {
  __shared__ __attribute__((aligned(16))) char smem[SMEM];
  role_body<1, 1>(smem, KPASS);
}
extern "C" __global__ __launch_bounds__(512, 4) void kf_h(KARGS) {
  __shared__ __attribute__((aligned(16))) char smem[SMEM];
  role_body<1, 0>(smem, KPASS);
}
extern "C" __global__ __launch_bounds__(512, 4) void kf_p(KARGS) {
  __shared__ __attribute__((aligned(16))) char smem[SMEM];
  role_body<2>(smem, KPASS);
}
extern "C" __global__ __launch_bounds__(512, 4) void kf_conv() { asm volatile(CONV_ASM ::: CONV_CLOB); }
extern "C" __global__ void kf_end() {}

extern "C" __global__ __launch_bounds__(1024) void k_probe(int* dummy) {
  __shared__ int sm[2];
  const uint32_t now = (uint32_t)__builtin_amdgcn_s_memrealtime();
  __shared__ int sn;
  if (threadIdx.x == 0) { sm[0] = 0; sm[1] = 1 == 1 ? 0x7fffffff : 0; sn = 0; }
  __syncthreads();
  for (int k = threadIdx.x; k < 4096; k += 1024) {
    const uint32_t a = (uint32_t)g_prb[k], b = (uint32_t)(g_prb[k] >> 32);
    if (a) atomicMax(&sm[0], (int)(now - a));
    if (b) { if (1 == 1) atomicMin(&sm[1], (int)(now - b)); else if (1 == 0) atomicMax(&sm[1], (int)(now - b)); else { atomicAdd(&sm[1], (int)(now - b)); atomicAdd(&sn, 1); } }
  }
  __syncthreads();
  const int q = min(max(sm[0] * 10 - (1 == 2 ? sm[1] * 10 / max(sn, 1) : sm[1] * 10), 0), 30000);
  if (threadIdx.x == 0) while ((int)((uint32_t)__builtin_amdgcn_s_memrealtime() - now) < q) __builtin_amdgcn_s_sleep(2);
}
extern "C" void kernel_launch(void* const* d_in, const int* in_sizes, int n_in,
                              void* d_out, int out_size, void* d_ws, size_t ws_size,
                              hipStream_t stream) {
  kf_all<<<512, 512, 0, stream>>>((const u16*)d_in[0], (const u16*)d_in[1], (const u16*)d_in[2], (const u16*)d_in[3], (const u16*)d_in[4],
                                  (const u16*)d_in[5], (const u16*)d_in[6], (const u16*)d_in[7], (const u16*)d_in[8], (char*)d_ws + (size_t)WSOFF * 1048576,
                                  (const u16*)d_in[10], (const u16*)d_in[11], (const u16*)d_in[12], (const u16*)d_in[13], (u16*)d_out);
  k_probe<<<1, 1024, 0, stream>>>((int*)d_ws);
}
```

```cpp
#include <hip/hip_runtime.h>
#include <hip/hip_bf16.h>
#include <stdint.h>
#define OTP 0

typedef __attribute__((ext_vector_type(8))) short bf16x8;
typedef __attribute__((ext_vector_type(4))) float f32x4;
typedef uint16_t u16;
typedef __attribute__((ext_vector_type(4))) uint32_t u32x4;
typedef __attribute__((ext_vector_type(2))) uint32_t u32x2;
#define TSD uint32_t pr0_ = 0u, pr1_ = 0u
__device__ unsigned long long g_prb[4096];
#define TS(k, i) do { if ((i) == 0) pr0_ = (uint32_t)__builtin_amdgcn_s_memrealtime() | 1u; \
    if (ROLE == 1 && (i) == 2 && (1 < 0 || w == 1) && (blockIdx.x & 255) < 144) pr1_ = (uint32_t)__builtin_amdgcn_s_memrealtime() | 1u; } while (0)
#define TSC(i)
#define TSE() do { if (__builtin_amdgcn_mbcnt_hi(-1, __builtin_amdgcn_mbcnt_lo(-1, 0)) == 0) \
    __hip_atomic_store(&g_prb[blockIdx.x * 8 + w], ((unsigned long long)pr1_ << 32) | pr0_, __ATOMIC_RELAXED, __HIP_MEMORY_SCOPE_AGENT); } while (0)
#define TSP
#define TSA

constexpr int L_ = 2048, D_ = 576, N_ = 128, F_ = 4608, C_ = 8;
constexpr int NF = 144;
constexpr uint32_t O_EP = 0;
constexpr uint32_t O_KF = 4096;
constexpr uint32_t O_KEY = O_KF + 8192;
constexpr uint32_t O_SC = O_KEY + 32768;
constexpr uint32_t O_DN = O_SC + 9216;
constexpr uint32_t O_SB = O_DN + 20480;
constexpr uint32_t O_SU = O_SB + 65536;
constexpr uint32_t O_FB = O_SU + 262144;
constexpr uint32_t O_XC = O_FB + 8 * 1152;
constexpr uint32_t SP = 9216 + 128;
constexpr uint32_t O_SL = O_XC + 1024 * 1152;
constexpr uint32_t SLU = 336;
constexpr uint32_t O_U = O_SL + 256 * SLU * 16;
constexpr int UP = 584;
constexpr uint32_t VP = 8192 + 128, VPP = 4096 + 128;
constexpr uint32_t O_V = O_U + 256 * UP * 16, O_VP = O_V + 256 * VP;
static_assert(256 * SLU * 16 >= NF * SP && O_SL % 128 == 0 && O_U % 128 == 0 && O_V % 128 == 0, "");
__host__ __device__ __forceinline__ uint32_t u_at(int tg, int d) { return O_U + 16u * (uint32_t)((tg >> 1) * (UP * 2) + (d >> 2) * 8 + (tg & 1) * 4 + (d & 3)); }
__host__ __device__ __forceinline__ uint32_t v_at(int d, int tg) {
  return (d < 512) ? O_V + (uint32_t)(d >> 1) * VP + 16u * (uint32_t)(2 * tg + (d & 1)) : O_VP + (uint32_t)(d - 512) * VPP + 16u * (uint32_t)tg;
}
#define HAVE_VAT
#define HAVE_VTAG
__device__ __forceinline__ float bf2f(u16 v) { return __uint_as_float((uint32_t)v << 16); }
__device__ __forceinline__ float bflo(uint32_t v) { return __uint_as_float(v << 16); }
__device__ __forceinline__ float bfhi(uint32_t v) { return __uint_as_float(v & 0xffff0000u); }
__device__ __forceinline__ u16 f2bf(float f) { return __builtin_bit_cast(u16, (__bf16)f); }
__device__ __forceinline__ float rbf(float f) { return bf2f(f2bf(f)); }
typedef __attribute__((ext_vector_type(2))) float f32x2;
typedef __attribute__((ext_vector_type(2))) __bf16 bf16x2v;
__device__ __forceinline__ uint32_t pack2(float a, float b) {
  return __builtin_bit_cast(uint32_t, __builtin_convertvector((f32x2){a, b}, bf16x2v));
}

#define DPP_I(v, ctrl, rmask) __builtin_amdgcn_update_dpp(0, (v), (ctrl), (rmask), 0xf, true)
__device__ __forceinline__ float row_sum(float v) {
  v += __int_as_float(DPP_I(__float_as_int(v), 0x111, 0xf));
  v += __int_as_float(DPP_I(__float_as_int(v), 0x112, 0xf));
  v += __int_as_float(DPP_I(__float_as_int(v), 0x114, 0xf));
  v += __int_as_float(DPP_I(__float_as_int(v), 0x118, 0xf));
  return v;
}
__device__ __forceinline__ float rdl(float v, int l) { return __int_as_float(__builtin_amdgcn_readlane(__float_as_int(v), l)); }
__device__ __forceinline__ float wave_sum(float v) {
  v = row_sum(v);
  asm("s_nop 1\n\tv_add_f32_dpp %0, %0, %0 row_bcast:15 row_mask:0xa bank_mask:0xf\n\t"
      "s_nop 1\n\tv_add_f32_dpp %0, %0, %0 row_bcast:31 row_mask:0xc bank_mask:0xf\n\ts_nop 0" : "+v"(v));
  return rdl(v, 63);
}
__device__ __forceinline__ float fold32(float a, float b) {
  const auto r = __builtin_amdgcn_permlane32_swap(__float_as_uint(a), __float_as_uint(b), false, false);
  return __uint_as_float(r[0]) + __uint_as_float(r[1]);
}
__device__ __forceinline__ float fold16(float a, float b) {
  const auto r = __builtin_amdgcn_permlane16_swap(__float_as_uint(a), __float_as_uint(b), false, false);
  return __uint_as_float(r[0]) + __uint_as_float(r[1]);
}
__device__ __forceinline__ void wave_sum2(float& a, float& b) {
  float v = row_sum(fold32(a, b));
  asm("s_nop 1\n\tv_add_f32_dpp %0, %0, %0 row_bcast:15 row_mask:0xa bank_mask:0xf\n\ts_nop 0" : "+v"(v));
  a = rdl(v, 31); b = rdl(v, 63);
}
__device__ __forceinline__ float dot2(uint32_t a, uint32_t b, float c) {
  return __builtin_amdgcn_fdot2_f32_bf16(__builtin_bit_cast(bf16x2v, a), __builtin_bit_cast(bf16x2v, b), c, false);
}
__device__ __forceinline__ uint32_t row_maxu(uint32_t v) {
  v = max(v, (uint32_t)DPP_I((int)v, 0xb1, 0xf));
  v = max(v, (uint32_t)DPP_I((int)v, 0x4e, 0xf));
  v = max(v, (uint32_t)DPP_I((int)v, 0x141, 0xf));
  v = max(v, (uint32_t)DPP_I((int)v, 0x140, 0xf));
  return v;
}
__device__ __forceinline__ uint32_t wave_maxu(uint32_t v) {
  v = max(v, (uint32_t)DPP_I((int)v, 0x111, 0xf));
  v = max(v, (uint32_t)DPP_I((int)v, 0x112, 0xf));
  v = max(v, (uint32_t)DPP_I((int)v, 0x114, 0xf));
  v = max(v, (uint32_t)DPP_I((int)v, 0x118, 0xf));
  v = max(v, (uint32_t)DPP_I((int)v, 0x142, 0xa));
  v = max(v, (uint32_t)DPP_I((int)v, 0x143, 0xc));
  return (uint32_t)__builtin_amdgcn_readlane((int)v, 63);
}

typedef __attribute__((ext_vector_type(8))) _Float16 f16x8;
typedef __attribute__((ext_vector_type(2))) _Float16 f16x2;
__device__ __forceinline__ uint32_t packh(f32x2 v) { return __builtin_bit_cast(uint32_t, __builtin_convertvector(v, f16x2)); }
__device__ __forceinline__ uint32_t packh(float a, float b) { return packh((f32x2){a, b}); }
constexpr int IP = 224, I_X = 128;
__device__ __forceinline__ f32x2 cmul(f32x2 z, f32x2 w) {
  f32x2 r;
  asm("v_pk_mul_f32 %0, %1, %2 op_sel_hi:[0,1]\n\tv_pk_fma_f32 %0, %1, %2, %0 op_sel:[1,1,0] op_sel_hi:[1,0,1] neg_lo:[0,1,0]" : "=&v"(r) : "v"(z), "v"(w));
  return r;
}
__device__ __forceinline__ f32x2 cexp2(float ar2, float air, float l) {
  const float t = air * l;
  return (f32x2){__builtin_amdgcn_cosf(t), __builtin_amdgcn_sinf(t)} * __builtin_amdgcn_exp2f(ar2 * l);
}
__device__ __forceinline__ float state_a(float ldt, float are, float aim, float& ar2, float& air, float& g) {
  const float dt = rbf(__expf(ldt));
  ar2 = dt * are * 1.4426950408889634f;
  air = dt * aim * 0.15915494309189535f;
  air -= __builtin_rintf(air);
  g = 0.f;
  if (__builtin_expect(__any(ar2 > 0.f), 0)) g = fminf(__uint_as_float(wave_maxu(__float_as_uint(fmaxf(ar2, 0.f)))), 1.f);
  return dt;
}
template <int N> __device__ __forceinline__ void put_im(char* p, int im, const f32x2* z) {
  *(u32x4*)p = (u32x4){packh(z[0][0], z[1][0]), packh(z[2][0], z[3][0]), packh(z[4][0], z[5][0]), packh(z[6][0], z[7][0])};
  *(u32x4*)(p + im) = (u32x4){packh(z[0][1], z[1][1]), packh(z[2][1], z[3][1]), packh(z[4][1], z[5][1]), packh(z[6][1], z[7][1])};
}
#define Y_ASM \
  "v_mov_b64 v[40:41], %0\nv_pk_mul_f32 v[42:43], v[40:41], %1 op_sel_hi:[0,1]\nv_pk_mul_f32 v[44:45], %0, %2 op_sel_hi:[0,1]\n" \
  "v_pk_fma_f32 v[42:43], v[40:41], %1, v[42:43] op_sel:[1,1,0] op_sel_hi:[1,0,1] neg_lo:[0,1,0]\n" \
  "v_pk_fma_f32 v[44:45], %0, %2, v[44:45] op_sel:[1,1,0] op_sel_hi:[1,0,1] neg_lo:[0,1,0]\nv_cvt_pk_f16_f32 v56, v40, v42\n" \
  "v_cvt_pk_f16_f32 v60, v41, v43\nv_pk_mul_f32 v[40:41], v[42:43], %1 op_sel_hi:[0,1]\nv_pk_mul_f32 v[48:49], v[44:45], %2 op_sel_hi:[0,1]\n" \
  "v_pk_fma_f32 v[40:41], v[42:43], %1, v[40:41] op_sel:[1,1,0] op_sel_hi:[1,0,1] neg_lo:[0,1,0]\n" \
  "v_pk_fma_f32 v[48:49], v[44:45], %2, v[48:49] op_sel:[1,1,0] op_sel_hi:[1,0,1] neg_lo:[0,1,0]\n" \
  "v_pk_mul_f32 v[42:43], v[40:41], %1 op_sel_hi:[0,1]\nv_pk_mul_f32 v[52:53], v[48:49], %2 op_sel_hi:[0,1]\n" \
  "v_pk_fma_f32 v[42:43], v[40:41], %1, v[42:43] op_sel:[1,1,0] op_sel_hi:[1,0,1] neg_lo:[0,1,0]\n" \
  "v_pk_fma_f32 v[52:53], v[48:49], %2, v[52:53] op_sel:[1,1,0] op_sel_hi:[1,0,1] neg_lo:[0,1,0]\nv_cvt_pk_f16_f32 v57, v40, v42\n" \
  "v_cvt_pk_f16_f32 v61, v41, v43\nv_pk_mul_f32 v[40:41], v[42:43], %1 op_sel_hi:[0,1]\n" \
  "v_pk_fma_f32 v[40:41], v[42:43], %1, v[40:41] op_sel:[1,1,0] op_sel_hi:[1,0,1] neg_lo:[0,1,0]\n" \
  "v_pk_mul_f32 v[42:43], v[40:41], %1 op_sel_hi:[0,1]\nv_pk_mul_f32 v[46:47], v[44:45], %1 op_sel_hi:[0,1]\n" \
  "v_pk_fma_f32 v[42:43], v[40:41], %1, v[42:43] op_sel:[1,1,0] op_sel_hi:[1,0,1] neg_lo:[0,1,0]\n" \
  "v_pk_fma_f32 v[46:47], v[44:45], %1, v[46:47] op_sel:[1,1,0] op_sel_hi:[1,0,1] neg_lo:[0,1,0]\nv_cvt_pk_f16_f32 v58, v40, v42\n" \
  "v_cvt_pk_f16_f32 v62, v41, v43\nv_cvt_pk_f16_f32 v64, v44, v46\nv_cvt_pk_f16_f32 v68, v45, v47\n" \
  "v_pk_mul_f32 v[40:41], v[42:43], %1 op_sel_hi:[0,1]\nv_pk_mul_f32 v[44:45], v[46:47], %1 op_sel_hi:[0,1]\n" \
  "v_pk_fma_f32 v[40:41], v[42:43], %1, v[40:41] op_sel:[1,1,0] op_sel_hi:[1,0,1] neg_lo:[0,1,0]\n" \
  "v_pk_fma_f32 v[44:45], v[46:47], %1, v[44:45] op_sel:[1,1,0] op_sel_hi:[1,0,1] neg_lo:[0,1,0]\n" \
  "v_pk_mul_f32 v[42:43], v[40:41], %1 op_sel_hi:[0,1]\nv_pk_mul_f32 v[46:47], v[44:45], %1 op_sel_hi:[0,1]\n" \
  "v_pk_fma_f32 v[42:43], v[40:41], %1, v[42:43] op_sel:[1,1,0] op_sel_hi:[1,0,1] neg_lo:[0,1,0]\n" \
  "v_pk_fma_f32 v[46:47], v[44:45], %1, v[46:47] op_sel:[1,1,0] op_sel_hi:[1,0,1] neg_lo:[0,1,0]\nv_cvt_pk_f16_f32 v59, v40, v42\n" \
  "v_cvt_pk_f16_f32 v63, v41, v43\nv_cvt_pk_f16_f32 v65, v44, v46\nv_cvt_pk_f16_f32 v69, v45, v47\n" \
  "v_pk_mul_f32 v[44:45], v[46:47], %1 op_sel_hi:[0,1]\nds_write_b128 %3, v[56:59] offset:0\n" \
  "v_pk_fma_f32 v[44:45], v[46:47], %1, v[44:45] op_sel:[1,1,0] op_sel_hi:[1,0,1] neg_lo:[0,1,0]\nds_write_b128 %3, v[60:63] offset:64\n" \
  "v_pk_mul_f32 v[46:47], v[44:45], %1 op_sel_hi:[0,1]\nv_pk_mul_f32 v[50:51], v[48:49], %1 op_sel_hi:[0,1]\n" \
  "v_pk_fma_f32 v[46:47], v[44:45], %1, v[46:47] op_sel:[1,1,0] op_sel_hi:[1,0,1] neg_lo:[0,1,0]\n" \
  "v_pk_fma_f32 v[50:51], v[48:49], %1, v[50:51] op_sel:[1,1,0] op_sel_hi:[1,0,1] neg_lo:[0,1,0]\nv_cvt_pk_f16_f32 v66, v44, v46\n" \
  "v_cvt_pk_f16_f32 v70, v45, v47\nv_cvt_pk_f16_f32 v56, v48, v50\nv_cvt_pk_f16_f32 v60, v49, v51\n" \
  "v_pk_mul_f32 v[44:45], v[46:47], %1 op_sel_hi:[0,1]\nv_pk_mul_f32 v[48:49], v[50:51], %1 op_sel_hi:[0,1]\n" \
  "v_pk_fma_f32 v[44:45], v[46:47], %1, v[44:45] op_sel:[1,1,0] op_sel_hi:[1,0,1] neg_lo:[0,1,0]\n" \
  "v_pk_fma_f32 v[48:49], v[50:51], %1, v[48:49] op_sel:[1,1,0] op_sel_hi:[1,0,1] neg_lo:[0,1,0]\n" \
  "v_pk_mul_f32 v[46:47], v[44:45], %1 op_sel_hi:[0,1]\nv_pk_mul_f32 v[50:51], v[48:49], %1 op_sel_hi:[0,1]\n" \
  "v_pk_fma_f32 v[46:47], v[44:45], %1, v[46:47] op_sel:[1,1,0] op_sel_hi:[1,0,1] neg_lo:[0,1,0]\n" \
  "v_pk_fma_f32 v[50:51], v[48:49], %1, v[50:51] op_sel:[1,1,0] op_sel_hi:[1,0,1] neg_lo:[0,1,0]\nv_cvt_pk_f16_f32 v67, v44, v46\n" \
  "v_cvt_pk_f16_f32 v71, v45, v47\nv_cvt_pk_f16_f32 v57, v48, v50\nv_cvt_pk_f16_f32 v61, v49, v51\n" \
  "v_pk_mul_f32 v[48:49], v[50:51], %1 op_sel_hi:[0,1]\nds_write_b128 %3, v[64:67] offset:16\n" \
  "v_pk_fma_f32 v[48:49], v[50:51], %1, v[48:49] op_sel:[1,1,0] op_sel_hi:[1,0,1] neg_lo:[0,1,0]\nds_write_b128 %3, v[68:71] offset:80\n" \
  "v_pk_mul_f32 v[50:51], v[48:49], %1 op_sel_hi:[0,1]\nv_pk_mul_f32 v[54:55], v[52:53], %1 op_sel_hi:[0,1]\n" \
  "v_pk_fma_f32 v[50:51], v[48:49], %1, v[50:51] op_sel:[1,1,0] op_sel_hi:[1,0,1] neg_lo:[0,1,0]\n" \
  "v_pk_fma_f32 v[54:55], v[52:53], %1, v[54:55] op_sel:[1,1,0] op_sel_hi:[1,0,1] neg_lo:[0,1,0]\nv_cvt_pk_f16_f32 v58, v48, v50\n" \
  "v_cvt_pk_f16_f32 v62, v49, v51\nv_cvt_pk_f16_f32 v64, v52, v54\nv_cvt_pk_f16_f32 v68, v53, v55\n" \
  "v_pk_mul_f32 v[48:49], v[50:51], %1 op_sel_hi:[0,1]\nv_pk_mul_f32 v[52:53], v[54:55], %1 op_sel_hi:[0,1]\n" \
  "v_pk_fma_f32 v[48:49], v[50:51], %1, v[48:49] op_sel:[1,1,0] op_sel_hi:[1,0,1] neg_lo:[0,1,0]\n" \
  "v_pk_fma_f32 v[52:53], v[54:55], %1, v[52:53] op_sel:[1,1,0] op_sel_hi:[1,0,1] neg_lo:[0,1,0]\n" \
  "v_pk_mul_f32 v[50:51], v[48:49], %1 op_sel_hi:[0,1]\nv_pk_mul_f32 v[54:55], v[52:53], %1 op_sel_hi:[0,1]\n" \
  "v_pk_fma_f32 v[50:51], v[48:49], %1, v[50:51] op_sel:[1,1,0] op_sel_hi:[1,0,1] neg_lo:[0,1,0]\n" \
  "v_pk_fma_f32 v[54:55], v[52:53], %1, v[54:55] op_sel:[1,1,0] op_sel_hi:[1,0,1] neg_lo:[0,1,0]\nv_cvt_pk_f16_f32 v59, v48, v50\n" \
  "v_cvt_pk_f16_f32 v63, v49, v51\nv_cvt_pk_f16_f32 v65, v52, v54\nv_cvt_pk_f16_f32 v69, v53, v55\n" \
  "v_pk_mul_f32 v[52:53], v[54:55], %1 op_sel_hi:[0,1]\nds_write_b128 %3, v[56:59] offset:32\n" \
  "v_pk_fma_f32 v[52:53], v[54:55], %1, v[52:53] op_sel:[1,1,0] op_sel_hi:[1,0,1] neg_lo:[0,1,0]\nds_write_b128 %3, v[60:63] offset:96\n" \
  "v_pk_mul_f32 v[54:55], v[52:53], %1 op_sel_hi:[0,1]\n" \
  "v_pk_fma_f32 v[54:55], v[52:53], %1, v[54:55] op_sel:[1,1,0] op_sel_hi:[1,0,1] neg_lo:[0,1,0]\nv_cvt_pk_f16_f32 v66, v52, v54\n" \
  "v_cvt_pk_f16_f32 v70, v53, v55\nv_pk_mul_f32 v[52:53], v[54:55], %1 op_sel_hi:[0,1]\n" \
  "v_pk_fma_f32 v[52:53], v[54:55], %1, v[52:53] op_sel:[1,1,0] op_sel_hi:[1,0,1] neg_lo:[0,1,0]\n" \
  "v_pk_mul_f32 v[54:55], v[52:53], %1 op_sel_hi:[0,1]\n" \
  "v_pk_fma_f32 v[54:55], v[52:53], %1, v[54:55] op_sel:[1,1,0] op_sel_hi:[1,0,1] neg_lo:[0,1,0]\nv_cvt_pk_f16_f32 v67, v52, v54\n" \
  "v_cvt_pk_f16_f32 v71, v53, v55\nds_write_b128 %3, v[64:67] offset:48\nds_write_b128 %3, v[68:71] offset:112\n"
#define Y_CLOB "memory", "v40", "v41", "v42", "v43", "v44", "v45", "v46", "v47", "v48", "v49", "v50", "v51", "v52", "v53", "v54", "v55", "v56", "v57", "v58", "v59", "v60", "v61", "v62", "v63", "v64", "v65", "v66", "v67", "v68", "v69", "v70", "v71"
#define T_ASM \
  "ds_read_b128 %4, %8\nds_read_b64_tr_b16 v[80:81], %7 offset:0\nds_read_b64_tr_b16 v[82:83], %7 offset:64\n" \
  "ds_read_b64_tr_b16 v[84:85], %5 offset:0\nds_read_b64_tr_b16 v[86:87], %5 offset:48\nds_read_b64_tr_b16 v[88:89], %6 offset:0\n" \
  "ds_read_b64_tr_b16 v[90:91], %6 offset:48\nds_read_b64_tr_b16 v[92:93], %7 offset:3584\nds_read_b64_tr_b16 v[94:95], %7 offset:3648\n" \
  "ds_read_b64_tr_b16 v[96:97], %5 offset:3584\nds_read_b64_tr_b16 v[98:99], %5 offset:3632\nds_read_b64_tr_b16 v[100:101], %6 offset:3584\n" \
  "ds_read_b64_tr_b16 v[102:103], %6 offset:3632\nds_read_b64_tr_b16 v[104:105], %7 offset:7168\nds_read_b64_tr_b16 v[106:107], %7 offset:7232\n" \
  "s_waitcnt lgkmcnt(8)\nv_mfma_f32_16x16x32_f16 %0, v[84:87], v[80:83], 0\nds_read_b64_tr_b16 v[108:109], %5 offset:7168\n" \
  "ds_read_b64_tr_b16 v[110:111], %5 offset:7216\nds_read_b64_tr_b16 v[112:113], %6 offset:7168\nv_mfma_f32_16x16x32_f16 %2, v[88:91], v[80:83], 0\n" \
  "ds_read_b64_tr_b16 v[114:115], %6 offset:7216\nds_read_b64_tr_b16 v[116:117], %7 offset:10752\nds_read_b64_tr_b16 v[118:119], %7 offset:10816\n" \
  "s_waitcnt lgkmcnt(8)\nv_mfma_f32_16x16x32_f16 %0, v[96:99], v[92:95], %0\nds_read_b64_tr_b16 v[120:121], %5 offset:10752\n" \
  "ds_read_b64_tr_b16 v[122:123], %5 offset:10800\nds_read_b64_tr_b16 v[124:125], %6 offset:10752\n" \
  "v_mfma_f32_16x16x32_f16 %2, v[100:103], v[92:95], %2\nds_read_b64_tr_b16 v[126:127], %6 offset:10800\n" \
  "ds_read_b64_tr_b16 v[80:81], %7 offset:14336\nds_read_b64_tr_b16 v[82:83], %7 offset:14400\ns_waitcnt lgkmcnt(8)\n" \
  "v_mfma_f32_16x16x32_f16 %0, v[108:111], v[104:107], %0\nds_read_b64_tr_b16 v[84:85], %5 offset:14336\n" \
  "ds_read_b64_tr_b16 v[86:87], %5 offset:14384\nds_read_b64_tr_b16 v[88:89], %6 offset:14336\n" \
  "v_mfma_f32_16x16x32_f16 %2, v[112:115], v[104:107], %2\nds_read_b64_tr_b16 v[90:91], %6 offset:14384\n" \
  "ds_read_b64_tr_b16 v[92:93], %7 offset:17920\nds_read_b64_tr_b16 v[94:95], %7 offset:17984\ns_waitcnt lgkmcnt(8)\n" \
  "v_mfma_f32_16x16x32_f16 %0, v[120:123], v[116:119], %0\nds_read_b64_tr_b16 v[96:97], %5 offset:17920\n" \
  "ds_read_b64_tr_b16 v[98:99], %5 offset:17968\nds_read_b64_tr_b16 v[100:101], %6 offset:17920\n" \
  "v_mfma_f32_16x16x32_f16 %2, v[124:127], v[116:119], %2\nds_read_b64_tr_b16 v[102:103], %6 offset:17968\n" \
  "ds_read_b64_tr_b16 v[104:105], %7 offset:21504\nds_read_b64_tr_b16 v[106:107], %7 offset:21568\ns_waitcnt lgkmcnt(8)\n" \
  "v_mfma_f32_16x16x32_f16 %1, v[84:87], v[80:83], 0\nds_read_b64_tr_b16 v[108:109], %5 offset:21504\nds_read_b64_tr_b16 v[110:111], %5 offset:21552\n" \
  "ds_read_b64_tr_b16 v[112:113], %6 offset:21504\nv_mfma_f32_16x16x32_f16 %3, v[88:91], v[80:83], 0\nds_read_b64_tr_b16 v[114:115], %6 offset:21552\n" \
  "ds_read_b64_tr_b16 v[116:117], %7 offset:25088\nds_read_b64_tr_b16 v[118:119], %7 offset:25152\ns_waitcnt lgkmcnt(8)\n" \
  "v_mfma_f32_16x16x32_f16 %1, v[96:99], v[92:95], %1\nds_read_b64_tr_b16 v[120:121], %5 offset:25088\n" \
  "ds_read_b64_tr_b16 v[122:123], %5 offset:25136\nds_read_b64_tr_b16 v[124:125], %6 offset:25088\n" \
  "v_mfma_f32_16x16x32_f16 %3, v[100:103], v[92:95], %3\nds_read_b64_tr_b16 v[126:127], %6 offset:25136\ns_waitcnt lgkmcnt(6)\n" \
  "v_mfma_f32_16x16x32_f16 %1, v[108:111], v[104:107], %1\nv_mfma_f32_16x16x32_f16 %3, v[112:115], v[104:107], %3\ns_waitcnt lgkmcnt(0)\n" \
  "v_mfma_f32_16x16x32_f16 %1, v[120:123], v[116:119], %1\nv_mfma_f32_16x16x32_f16 %3, v[124:127], v[116:119], %3\n"
#define T_CLOB "memory", "v80", "v81", "v82", "v83", "v84", "v85", "v86", "v87", "v88", "v89", "v90", "v91", "v92", "v93", "v94", "v95", "v96", "v97", "v98", "v99", "v100", "v101", "v102", "v103", "v104", "v105", "v106", "v107", "v108", "v109", "v110", "v111", "v112", "v113", "v114", "v115", "v116", "v117", "v118", "v119", "v120", "v121", "v122", "v123", "v124", "v125", "v126", "v127"
template <class F> __device__ __forceinline__ int gen_y(float ldt, float are, float aim, float bre, float bim, float cre, float cim, uint32_t im, F&& mid TSP) {
  float ar2, air, g;
  const float dt = state_a(ldt, are, aim, ar2, air, g);
  const float dbr = dt * bre, dbi = dt * bim, pr = cre * dbr - cim * dbi, pi = cre * dbi + cim * dbr;
  const f32x2 w8 = cexp2(ar2, -air, 8.f), w64 = cexp2(ar2, -air, 64.f);
  const float pm = fmaxf(fabsf(pr), fabsf(pi));
  int e = -4;
  if (__builtin_expect(__any(!(pm < 0x1p-4f) || ar2 > 0.f) || !__any(pm > 0x1p-12f), 0)) e = (int)(wave_maxu(__float_as_uint(pm)) >> 23) - 126 + (int)ceilf(g * 250.f);
  const f32x2 y0 = {__builtin_amdgcn_ldexpf(pr, 14 - e), __builtin_amdgcn_ldexpf(-pi, 14 - e)};
  mid();
  asm volatile(Y_ASM : : "v"(y0), "v"(w8), "v"(w64), "v"(im) : Y_CLOB);
  return e;
}
__device__ __forceinline__ int gen_x(float ldt, float are, float aim, char* im) {
  float ar2, air, g;
  state_a(ldt, are, aim, ar2, air, g);
  const int e = min((int)ceilf(g * 1810.f), 300);
  const float s = __builtin_amdgcn_ldexpf(1.f, 15 - e);
  const f32x2 w1 = cexp2(ar2, air, 1.f), w256 = cexp2(ar2, air, 256.f), w2 = cmul(w1, w1);
  f32x2 a[8], b[8], c[8];
  a[0] = (f32x2){s, 0.f}; a[1] = w1 * s; a[2] = w2 * s;
  b[0] = b[1] = w256 * s;
  b[2] = cmul(b[0], w2); a[3] = cmul(a[1], w2); a[4] = cmul(a[2], w2);
  b[3] = b[4] = b[5] = cmul(b[2], w2); a[5] = cmul(a[3], w2); a[6] = cmul(a[4], w2);
  b[6] = cmul(b[3], w2); a[7] = cmul(a[5], w2); c[0] = cmul(b[3], w256);
  b[7] = cmul(b[6], w2); c[2] = cmul(c[0], w256);
  put_im<0>(im + I_X, 48, a);
  c[1] = cmul(b[7], w256); c[3] = cmul(c[1], w256);
  put_im<0>(im + I_X + 16, 48, b);
#pragma unroll
  for (int k = 4; k < 8; ++k) c[k] = cmul(c[k - 1], w256);
  put_im<0>(im + I_X + 32, 48, c);
  return e;
}

template <int I> struct IC { static constexpr int value = I; };
template <int B, int E, class F> __device__ __forceinline__ void static_for(F&& f) {
  if constexpr (B < E) { f(IC<B>{}); static_for<B + 1, E>(f); }
}
typedef __attribute__((ext_vector_type(4))) short s16x4;
__device__ __forceinline__ u32x2 trd(uint32_t a) {
  return __builtin_bit_cast(u32x2, __builtin_amdgcn_ds_read_tr16_b64_v4i16((__attribute__((address_space(3))) s16x4*)(uintptr_t)a));
}
#define BAR() asm volatile("s_waitcnt lgkmcnt(0)\n\ts_barrier" ::: "memory")
#define ST16(off, v) asm volatile("global_store_dwordx4 %0, %1, %2 sc1\n\ts_nop 1" : : "v"(off), "v"(v), "s"(ws) : "memory")
#define ST4(off, v) asm volatile("global_store_dword %0, %1, %2 sc1" : : "v"(off), "v"(v), "s"(ws) : "memory")
#define LD16(r, off) asm volatile("global_load_dwordx4 %0, %1, %2 sc1" : "=v"(r) : "v"(off), "s"(ws) : "memory")
#define LD16W(r, off) asm volatile("global_load_dwordx4 %0, %1, %2 sc1\n\ts_waitcnt vmcnt(0)" : "=&v"(r) : "v"(off), "s"(ws) : "memory")
#define DRAIN() asm volatile("s_waitcnt vmcnt(0)" ::: "memory")
#define CEX(a, b) { const uint32_t hi_ = max(a, b), lo_ = min(a, b); a = hi_; b = lo_; }
#define MMB(a_, b_, c_) __builtin_amdgcn_mfma_f32_16x16x32_bf16(__builtin_bit_cast(bf16x8, a_), __builtin_bit_cast(bf16x8, b_), c_, 0, 0, 0)
#define NTM 0
#define DPOL ""
#define DPOL2 ""
#define DPOL4 ""
constexpr int SPIN_CAP = 1 << 18;
constexpr uint32_t UMASK = 0x40074007u;
__device__ __forceinline__ uint32_t utag(uint32_t T, int j) {
  return ((T >> (3 * j)) & 0x00070007u) | (((j <= 2) ? (T << (2 - j)) : (T >> (j - 2))) & 0x40004000u);
}
__device__ __forceinline__ uint32_t max4(const u32x4& a) { return max(max(a[0], a[1]), max(a[2], a[3])); }
#ifndef ULONG
#define ULONG 8
#endif
__device__ __forceinline__ void until(uint32_t t) {
#pragma unroll 1
  for (int spin = 0; spin < 600; ++spin) {
    const int d = (int)(t - (uint32_t)__builtin_amdgcn_s_memrealtime());
    if (d <= 0) break;
    if (ULONG && d > 4 * ULONG) __builtin_amdgcn_s_sleep(ULONG); else __builtin_amdgcn_s_sleep(1);
  }
}
__device__ __forceinline__ uint32_t latest(uint32_t t) {
  const uint32_t t0 = (uint32_t)__builtin_amdgcn_readfirstlane(t) - 0x40000000u;
  return wave_maxu(t - t0) + t0;
}
__device__ __forceinline__ uint32_t gate(const char* ws, uint32_t po, uint32_t T) {
  u32x2 pv;
#pragma unroll 1
  for (int spin = 0; spin < SPIN_CAP; ++spin) {
    asm volatile("global_load_dwordx2 %0, %1, %2 sc1\n\ts_waitcnt vmcnt(0)" : "=&v"(pv) : "v"(po), "s"(ws) : "memory");
    if (!__any(pv[0] != T)) break;
    __builtin_amdgcn_s_sleep(2);
  }
  return pv[1];
}
__device__ __forceinline__ uint32_t gate2(const char* ws, uint32_t oa, uint32_t da, uint32_t ob, uint32_t T) {
  u32x2 pa, pb;
#pragma unroll 1
  for (int spin = 0; spin < SPIN_CAP; ++spin) {
    asm volatile("global_load_dwordx2 %0, %2, %4 sc1\n\tglobal_load_dwordx2 %1, %3, %4 sc1\n\ts_waitcnt vmcnt(0)" : "=&v"(pa), "=&v"(pb) : "v"(oa), "v"(ob), "s"(ws) : "memory");
    if (!__any(pa[0] != T || pb[0] != T)) break;
    __builtin_amdgcn_s_sleep(4);
  }
  pa[1] += da;
  return latest(((int)(pa[1] - pb[1]) > 0) ? pa[1] : pb[1]);
}
__device__ __forceinline__ uint32_t ahead(uint32_t c0, uint32_t t0, int dt) {
  const uint32_t c = (uint32_t)__builtin_amdgcn_s_memtime(), t = (uint32_t)__builtin_amdgcn_s_memrealtime();
  return t + (uint32_t)((float)((t - t0) * (uint32_t)(20 * dt)) * __builtin_amdgcn_rcpf((float)(c - c0)));
}
__device__ __forceinline__ void gsync(uint32_t a, uint32_t target) {
  uint32_t t, c, n = SPIN_CAP;
  uint64_t ex;
  asm volatile("s_waitcnt lgkmcnt(0)\n\ts_mov_b64 %3, exec\n\ts_mov_b64 exec, 1\n\tv_mov_b32 %0, 1\n\tds_add_u32 %4, %0\n\ts_mov_b64 exec, %3\n"
               "1:\n\ts_sleep 1\n\tds_read_b32 %0, %4\n\ts_sub_u32 %2, %2, 1\n\ts_cbranch_scc1 2f\n\ts_waitcnt lgkmcnt(0)\n\tv_readfirstlane_b32 %1, %0\n\t"
               "s_cmp_lt_u32 %1, %5\n\ts_cbranch_scc1 1b\n2:\n\ts_waitcnt lgkmcnt(0)"
               : "=&v"(t), "=&s"(c), "+s"(n), "=&s"(ex) : "v"(a), "n"(target) : "memory", "scc");
}
__device__ __forceinline__ void gsync0(uint32_t a, uint32_t target) {
  uint32_t t, c, n = SPIN_CAP;
  asm volatile("s_waitcnt lgkmcnt(0)\n\ts_mov_b64 exec, 1\n\tv_mov_b32 %0, 1\n\tds_add_u32 %3, %0\n\ts_mov_b64 exec, -1\n"
               "1:\n\tds_read_b32 %0, %3\n\ts_sub_u32 %2, %2, 1\n\ts_cbranch_scc1 2f\n\ts_waitcnt lgkmcnt(0)\n\tv_readfirstlane_b32 %1, %0\n\t"
               "s_cmp_lt_u32 %1, %4\n\ts_cbranch_scc1 1b\n2:\n\ts_waitcnt lgkmcnt(0)"
               : "=&v"(t), "=&s"(c), "+s"(n) : "v"(a), "n"(target) : "memory", "scc");
}
__device__ __forceinline__ void gwait(uint32_t a, uint32_t target) {
  uint32_t t, c, n = SPIN_CAP;
  asm volatile("1:\n\ts_sleep 2\n\tds_read_b32 %0, %3\n\ts_sub_u32 %2, %2, 1\n\ts_cbranch_scc1 2f\n\ts_waitcnt lgkmcnt(0)\n\tv_readfirstlane_b32 %1, %0\n\t"
               "s_cmp_lt_u32 %1, %4\n\ts_cbranch_scc1 1b\n2:\n\ts_waitcnt lgkmcnt(0)"
               : "=&v"(t), "=&s"(c), "+s"(n) : "v"(a), "n"(target) : "memory", "scc");
}
__device__ __forceinline__ void gwait0(uint32_t a, uint32_t target) {
  uint32_t t, c, n = SPIN_CAP;
  asm volatile("1:\n\tds_read_b32 %0, %3\n\ts_sub_u32 %2, %2, 1\n\ts_cbranch_scc1 2f\n\ts_waitcnt lgkmcnt(0)\n\tv_readfirstlane_b32 %1, %0\n\t"
               "s_cmp_lt_u32 %1, %4\n\ts_cbranch_scc1 1b\n2:\n\ts_waitcnt lgkmcnt(0)"
               : "=&v"(t), "=&s"(c), "+s"(n) : "v"(a), "n"(target) : "memory", "scc");
}
__device__ __forceinline__ void dma16(const void* base, uint32_t voff, uint32_t lds) {
  uint32_t keep;
  asm volatile("s_mov_b32 %0, m0\n\ts_mov_b32 m0, %3\n\ts_nop 0\n\tglobal_load_lds_dwordx4 %1, %2" DPOL "\n\ts_mov_b32 m0, %0" : "=&s"(keep) : "v"(voff), "s"(base), "s"(lds) : "memory");
}
__device__ __forceinline__ void dma16d(const void* base, uint32_t voff, uint32_t lds) {
  uint32_t keep;
  asm volatile("s_mov_b32 %0, m0\n\ts_mov_b32 m0, %3\n\ts_nop 0\n\tglobal_load_lds_dwordx4 %1, %2" DPOL2 "\n\ts_mov_b32 m0, %0" : "=&s"(keep) : "v"(voff), "s"(base), "s"(lds) : "memory");
}
__device__ __forceinline__ void nap(int n) {
  for (; n > 15; n -= 15) __builtin_amdgcn_s_sleep(15);
  switch (n) {
    case 1: __builtin_amdgcn_s_sleep(1); break; case 2: __builtin_amdgcn_s_sleep(2); break; case 3: __builtin_amdgcn_s_sleep(3); break;
    case 4: __builtin_amdgcn_s_sleep(4); break; case 5: __builtin_amdgcn_s_sleep(5); break; case 6: __builtin_amdgcn_s_sleep(6); break;
    case 7: __builtin_amdgcn_s_sleep(7); break; case 8: __builtin_amdgcn_s_sleep(8); break; case 9: __builtin_amdgcn_s_sleep(9); break;
    case 10: __builtin_amdgcn_s_sleep(10); break; case 11: __builtin_amdgcn_s_sleep(11); break; case 12: __builtin_amdgcn_s_sleep(12); break;
    case 13: __builtin_amdgcn_s_sleep(13); break; case 14: __builtin_amdgcn_s_sleep(14); break; case 15: __builtin_amdgcn_s_sleep(15); break;
    default: break;
  }
}
#define SLEEP(n) do { if ((n) > 0) __builtin_amdgcn_s_sleep(n); } while (0)
#define USLEEP 3
#define SSLEEP 11
#define PSLEEP 2
#define PPRIO 0
#define LFIRST 1
#define WD 40
#define OSLEEP 12
#define QPRIO 1
#define KPRIO 2
#define GSLEEP 0
#define NSLEEP 0
#define UD 40
#define XD 0
#define RSLEEP 0
#define UPRIO 2
#define VD2 372
#define WDEPTH 6
#define ESLEEP 0
#define SD 58
#define KD 180
#define OD 0
#define KBACK 6
#define DDEPTH 12
#define C3 1
#define XROT 2
#define WSOFF 0
#define KN 1
#define RPF 1
#define LSLEEP 1
#define TSTOP 78
#define HSTOP 110
#define VEARLY 0ull
#define FPR 3
#define SPR 3
#define DPRIO 0
#define QPRIO2 3
#define HW 2
#define RD 57
#define OPOS 1
#define OD2 6
#define NA 12
#define RD2 110
#define ORD1 ((unsigned __int128)0x321087654u << 36 | 0x321087654ull)
#define WDEPTH2 5
#define RQ NA
#define HREV 1
#define QPRIO0 3
#define VPS 1
#define RW 0
#define NPRIO 0
#define XSL 10
#define V2D 24
#define N0 8
#define LPOS 1
#define ALLP 1
#define OPOL ""
#define STP 1
#define FD 204
#define VDB 365
#define BALL 1
#define FORCESLOW 0
#define FORCEFB 0
#define CONV_ASM \
  "s_cmp_eq_u32 s89, 1\ns_cbranch_scc1 1f\ns_cmp_gt_u32 s89, 1\ns_cbranch_scc1 9f\nds_read_b64 v[124:125], v3 offset:576\n" \
  "ds_read_b64 v[126:127], v3 offset:584\nds_read_b64 v[120:121], v3 offset:64\nds_read_b64 v[122:123], v3 offset:72\n" \
  "ds_read_b64 v[116:117], v3 offset:640\nds_read_b64 v[118:119], v3 offset:648\nds_read_b64 v[112:113], v3 offset:128\n" \
  "ds_read_b64 v[114:115], v3 offset:136\nds_read_b64 v[108:109], v3 offset:704\nds_read_b64 v[110:111], v3 offset:712\n" \
  "ds_read_b64 v[104:105], v3 offset:192\nds_read_b64 v[106:107], v3 offset:200\nds_read_b64 v[100:101], v3 offset:768\n" \
  "ds_read_b64 v[102:103], v3 offset:776\nds_read_b64 v[96:97], v3 offset:256\nds_read_b64 v[98:99], v3 offset:264\n" \
  "ds_read_b64 v[92:93], v3 offset:832\nds_read_b64 v[94:95], v3 offset:840\nds_read_b64 v[88:89], v3 offset:320\n" \
  "ds_read_b64 v[90:91], v3 offset:328\nds_read_b64 v[84:85], v3 offset:896\nds_read_b64 v[86:87], v3 offset:904\n" \
  "ds_read_b64 v[80:81], v3 offset:384\nds_read_b64 v[82:83], v3 offset:392\nds_read_b64 v[76:77], v3 offset:960\n" \
  "ds_read_b64 v[78:79], v3 offset:968\nds_read_b64 v[72:73], v3 offset:448\nds_read_b64 v[74:75], v3 offset:456\n" \
  "ds_read_b64 v[68:69], v3 offset:1024\nds_read_b64 v[70:71], v3 offset:1032\nds_read_b64 v[64:65], v3 offset:512\n" \
  "ds_read_b64 v[66:67], v3 offset:520\nds_read_b64 v[60:61], v3 offset:0\nds_read_b64 v[62:63], v3 offset:8\nds_read_b64 v[56:57], v3 offset:1088\n" \
  "ds_read_b64 v[58:59], v3 offset:1096\ns_waitcnt lgkmcnt(0)\ns_barrier\nds_read_b128 v[36:39], v2 offset:0\nds_read_b128 v[40:43], v2 offset:64\n" \
  "ds_read_b128 v[44:47], v2 offset:128\nds_read_b128 v[48:51], v2 offset:192\ns_waitcnt lgkmcnt(3)\n" \
  "v_mfma_f32_16x16x32_f16 v[12:15], v[124:127], v[36:39], 0\ns_setprio 1\nds_read_b128 v[52:55], v2 offset:256\n" \
  "v_mfma_f32_16x16x32_f16 v[16:19], v[120:123], v[36:39], 0\ns_waitcnt lgkmcnt(3)\nv_mfma_f32_16x16x32_f16 v[12:15], v[116:119], v[40:43], v[12:15]\n" \
  "ds_read_b128 v[36:39], v2 offset:320\nv_mfma_f32_16x16x32_f16 v[16:19], v[112:115], v[40:43], v[16:19]\ns_waitcnt lgkmcnt(3)\n" \
  "v_mfma_f32_16x16x32_f16 v[12:15], v[108:111], v[44:47], v[12:15]\nds_read_b128 v[40:43], v2 offset:384\n" \
  "v_mfma_f32_16x16x32_f16 v[16:19], v[104:107], v[44:47], v[16:19]\ns_waitcnt lgkmcnt(3)\n" \
  "v_mfma_f32_16x16x32_f16 v[12:15], v[100:103], v[48:51], v[12:15]\nds_read_b128 v[44:47], v2 offset:448\n" \
  "v_mfma_f32_16x16x32_f16 v[16:19], v[96:99], v[48:51], v[16:19]\ns_waitcnt lgkmcnt(3)\n" \
  "v_mfma_f32_16x16x32_f16 v[12:15], v[92:95], v[52:55], v[12:15]\nv_mfma_f32_16x16x32_f16 v[16:19], v[88:91], v[52:55], v[16:19]\n" \
  "s_waitcnt lgkmcnt(2)\nv_mfma_f32_16x16x32_f16 v[12:15], v[84:87], v[36:39], v[12:15]\nds_read_b128 v[48:51], v2 offset:512\n" \
  "v_mfma_f32_16x16x32_f16 v[16:19], v[80:83], v[36:39], v[16:19]\ns_waitcnt lgkmcnt(2)\n" \
  "v_mfma_f32_16x16x32_f16 v[12:15], v[76:79], v[40:43], v[12:15]\nds_read_b128 v[52:55], v2 offset:576\n" \
  "v_mfma_f32_16x16x32_f16 v[16:19], v[72:75], v[40:43], v[16:19]\ns_waitcnt lgkmcnt(2)\n" \
  "v_mfma_f32_16x16x32_f16 v[12:15], v[68:71], v[44:47], v[12:15]\nds_read_b128 v[36:39], v2 offset:640\n" \
  "v_mfma_f32_16x16x32_f16 v[16:19], v[64:67], v[44:47], v[16:19]\nv_mfma_f32_16x16x32_f16 v[20:23], v[60:63], v[44:47], 0\n" \
  "ds_read_b128 v[40:43], v2 offset:704\nv_mfma_f32_16x16x32_f16 v[28:31], v[56:59], v[44:47], 0\ns_waitcnt lgkmcnt(3)\n" \
  "v_mfma_f32_16x16x32_f16 v[16:19], v[124:127], v[48:51], v[16:19]\nds_read_b128 v[56:59], v2 offset:768\nv_cvt_pkrtz_f16_f32 v32, v12, v13\n" \
  "v_mfma_f32_16x16x32_f16 v[20:23], v[120:123], v[48:51], v[20:23]\nv_cvt_pkrtz_f16_f32 v33, v14, v15\ns_waitcnt lgkmcnt(3)\n" \
  "v_mfma_f32_16x16x32_f16 v[16:19], v[116:119], v[52:55], v[16:19]\nds_read_b128 v[44:47], v2 offset:832\n" \
  "v_pk_add_u16 v32, v32, 4 op_sel_hi:[1,0] clamp\nv_mfma_f32_16x16x32_f16 v[20:23], v[112:115], v[52:55], v[20:23]\n" \
  "v_pk_add_u16 v33, v33, 4 op_sel_hi:[1,0] clamp\ns_waitcnt lgkmcnt(3)\nv_mfma_f32_16x16x32_f16 v[16:19], v[108:111], v[36:39], v[16:19]\n" \
  "ds_read_b128 v[48:51], v2 offset:896\nds_write_b64 v4, v[32:33] offset:0\nv_mfma_f32_16x16x32_f16 v[20:23], v[104:107], v[36:39], v[20:23]\n" \
  "s_waitcnt lgkmcnt(3)\nv_mfma_f32_16x16x32_f16 v[16:19], v[100:103], v[40:43], v[16:19]\nds_read_b128 v[52:55], v2 offset:960\n" \
  "v_mfma_f32_16x16x32_f16 v[20:23], v[96:99], v[40:43], v[20:23]\nv_mfma_f32_16x16x32_f16 v[16:19], v[92:95], v[56:59], v[16:19]\n" \
  "v_mfma_f32_16x16x32_f16 v[20:23], v[88:91], v[56:59], v[20:23]\nds_read_b128 v[36:39], v2 offset:1024\ns_waitcnt lgkmcnt(3)\n" \
  "v_mfma_f32_16x16x32_f16 v[16:19], v[84:87], v[44:47], v[16:19]\nv_mfma_f32_16x16x32_f16 v[20:23], v[80:83], v[44:47], v[20:23]\n" \
  "ds_read_b128 v[40:43], v2 offset:1088\nds_read_b64 v[56:57], v6 offset:256\nds_read_b64 v[58:59], v6 offset:264\n" \
  "v_mfma_f32_16x16x32_f16 v[16:19], v[76:79], v[48:51], v[16:19]\nv_mfma_f32_16x16x32_f16 v[20:23], v[72:75], v[48:51], v[20:23]\n" \
  "ds_read_b128 v[44:47], v2 offset:1152\ns_waitcnt lgkmcnt(4)\nv_mfma_f32_16x16x32_f16 v[16:19], v[68:71], v[52:55], v[16:19]\n" \
  "v_mfma_f32_16x16x32_f16 v[20:23], v[64:67], v[52:55], v[20:23]\nds_read_b128 v[48:51], v2 offset:1216\n" \
  "v_mfma_f32_16x16x32_f16 v[24:27], v[60:63], v[52:55], 0\nv_mfma_f32_16x16x32_f16 v[20:23], v[124:127], v[36:39], v[20:23]\n" \
  "ds_read_b128 v[52:55], v2 offset:1280\nv_mfma_f32_16x16x32_f16 v[24:27], v[120:123], v[36:39], v[24:27]\ns_waitcnt lgkmcnt(5)\n" \
  "v_mfma_f32_16x16x32_f16 v[20:23], v[116:119], v[40:43], v[20:23]\nds_read_b128 v[36:39], v2 offset:1344\nv_cvt_pkrtz_f16_f32 v32, v16, v17\n" \
  "v_mfma_f32_16x16x32_f16 v[24:27], v[112:115], v[40:43], v[24:27]\nv_cvt_pkrtz_f16_f32 v33, v18, v19\ns_waitcnt lgkmcnt(3)\n" \
  "v_mfma_f32_16x16x32_f16 v[20:23], v[108:111], v[44:47], v[20:23]\nds_read_b128 v[40:43], v2 offset:1408\n" \
  "v_pk_add_u16 v32, v32, 4 op_sel_hi:[1,0] clamp\nds_read_b32 v35, v9 offset:32\nv_mfma_f32_16x16x32_f16 v[24:27], v[104:107], v[44:47], v[24:27]\n" \
  "v_pk_add_u16 v33, v33, 4 op_sel_hi:[1,0] clamp\ns_waitcnt lgkmcnt(3)\nv_mfma_f32_16x16x32_f16 v[20:23], v[100:103], v[48:51], v[20:23]\n" \
  "ds_read_b128 v[44:47], v2 offset:1472\nds_write_b64 v4, v[32:33] offset:1024\nv_mfma_f32_16x16x32_f16 v[24:27], v[96:99], v[48:51], v[24:27]\n" \
  "v_mfma_f32_16x16x32_f16 v[20:23], v[92:95], v[52:55], v[20:23]\nv_mfma_f32_16x16x32_f16 v[24:27], v[88:91], v[52:55], v[24:27]\n" \
  "ds_read_b128 v[48:51], v2 offset:1536\ns_waitcnt lgkmcnt(3)\nv_readfirstlane_b32 s85, v35\ns_cmp_lt_u32 s85, 4\ns_cbranch_scc1 21f\n22:\n" \
  "v_mfma_f32_16x16x32_f16 v[20:23], v[84:87], v[36:39], v[20:23]\nv_mfma_f32_16x16x32_f16 v[24:27], v[80:83], v[36:39], v[24:27]\n" \
  "ds_read_b128 v[52:55], v2 offset:1600\nv_mfma_f32_16x16x32_f16 v[20:23], v[76:79], v[40:43], v[20:23]\n" \
  "v_mfma_f32_16x16x32_f16 v[24:27], v[72:75], v[40:43], v[24:27]\nds_read_b128 v[36:39], v2 offset:1664\ns_waitcnt lgkmcnt(4)\n" \
  "v_mfma_f32_16x16x32_f16 v[20:23], v[68:71], v[44:47], v[20:23]\nv_mfma_f32_16x16x32_f16 v[24:27], v[64:67], v[44:47], v[24:27]\n" \
  "ds_read_b128 v[40:43], v2 offset:1728\nv_mfma_f32_16x16x32_f16 v[28:31], v[60:63], v[44:47], v[28:31]\nds_read_b64_tr_b16 v[60:61], v5 offset:0\n" \
  "ds_read_b64_tr_b16 v[62:63], v5 offset:32\ns_waitcnt lgkmcnt(4)\nv_mfma_f32_16x16x32_f16 v[24:27], v[124:127], v[48:51], v[24:27]\n" \
  "v_mfma_f32_16x16x32_f16 v[28:31], v[120:123], v[48:51], v[28:31]\nds_read_b64_tr_b16 v[44:45], v5 offset:256\n" \
  "ds_read_b64_tr_b16 v[46:47], v5 offset:288\nv_mfma_f32_16x16x32_f16 v[24:27], v[116:119], v[52:55], v[24:27]\n" \
  "ds_read_b64 v[120:121], v6 offset:320\nds_read_b64 v[122:123], v6 offset:328\nv_mfma_f32_16x16x32_f16 v[28:31], v[112:115], v[52:55], v[28:31]\n" \
  "ds_read_b128 v[48:51], v2 offset:1792\ns_waitcnt lgkmcnt(7)\nv_mfma_f32_16x16x32_f16 v[24:27], v[108:111], v[36:39], v[24:27]\n" \
  "ds_read_b128 v[112:115], v2 offset:1856\nv_mfma_f32_16x16x32_f16 v[28:31], v[104:107], v[36:39], v[28:31]\nds_read_b64 v[52:53], v6 offset:128\n" \
  "ds_read_b64 v[54:55], v6 offset:136\nv_mfma_f32_16x16x32_f16 v[24:27], v[100:103], v[40:43], v[24:27]\nds_read_b128 v[104:107], v2 offset:1920\n" \
  "v_mfma_f32_16x16x32_f16 v[28:31], v[96:99], v[40:43], v[28:31]\nds_read_b64 v[36:37], v6 offset:192\nds_read_b64 v[38:39], v6 offset:200\n" \
  "s_waitcnt lgkmcnt(9)\nv_mfma_f32_16x16x32_f16 v[20:23], v[60:63], v[56:59], v[20:23]\nds_read_b128 v[96:99], v2 offset:1984\ns_waitcnt lgkmcnt(7)\n" \
  "v_mfma_f32_16x16x32_f16 v[20:23], v[44:47], v[120:123], v[20:23]\nv_mfma_f32_16x16x32_f16 v[24:27], v[92:95], v[48:51], v[24:27]\n" \
  "ds_read_b128 v[40:43], v2 offset:2048\nv_mfma_f32_16x16x32_f16 v[28:31], v[88:91], v[48:51], v[28:31]\nds_read_b64_tr_b16 v[88:89], v5 offset:512\n" \
  "ds_read_b64_tr_b16 v[90:91], v5 offset:544\ns_waitcnt lgkmcnt(6)\nv_mfma_f32_16x16x32_f16 v[24:27], v[84:87], v[112:115], v[24:27]\n" \
  "v_mfma_f32_16x16x32_f16 v[28:31], v[80:83], v[112:115], v[28:31]\nv_cvt_pkrtz_f16_f32 v32, v20, v21\nv_cvt_pkrtz_f16_f32 v33, v22, v23\n" \
  "v_mfma_f32_16x16x32_f16 v[24:27], v[76:79], v[104:107], v[24:27]\nds_read_b64_tr_b16 v[48:49], v5 offset:768\n" \
  "ds_read_b64_tr_b16 v[50:51], v5 offset:800\nv_mfma_f32_16x16x32_f16 v[28:31], v[72:75], v[104:107], v[28:31]\n" \
  "ds_read_b128 v[80:83], v2 offset:2112\ns_waitcnt lgkmcnt(5)\nv_mfma_f32_16x16x32_f16 v[24:27], v[68:71], v[96:99], v[24:27]\n" \
  "ds_read_b128 v[112:115], v2 offset:2176\nv_pk_add_u16 v32, v32, 4 op_sel_hi:[1,0] clamp\n" \
  "v_mfma_f32_16x16x32_f16 v[28:31], v[64:67], v[96:99], v[28:31]\nds_read_b128 v[72:75], v2 offset:2240\n" \
  "v_pk_add_u16 v33, v33, 4 op_sel_hi:[1,0] clamp\nv_mfma_f32_16x16x32_f16 v[28:31], v[124:127], v[40:43], v[28:31]\n" \
  "ds_read_b128 v[104:107], v2 offset:2304\nds_write_b64 v4, v[32:33] offset:2048\nv_mfma_f32_16x16x32_f16 v[24:27], v[60:63], v[52:55], v[24:27]\n" \
  "ds_read_b128 v[64:67], v2 offset:2368\nds_read_b64 v[96:97], v6 offset:0\nds_read_b64 v[98:99], v6 offset:8\n" \
  "v_mfma_f32_16x16x32_f16 v[24:27], v[44:47], v[36:39], v[24:27]\nds_read_b128 v[124:127], v2 offset:2432\ns_waitcnt lgkmcnt(9)\n" \
  "v_mfma_f32_16x16x32_f16 v[24:27], v[88:91], v[56:59], v[24:27]\nds_read_b128 v[40:43], v2 offset:2496\n" \
  "v_mfma_f32_16x16x32_f16 v[24:27], v[48:51], v[120:123], v[24:27]\ns_waitcnt lgkmcnt(8)\n" \
  "v_mfma_f32_16x16x32_f16 v[28:31], v[116:119], v[80:83], v[28:31]\nds_read_b64 v[116:117], v6 offset:64\nds_read_b64 v[118:119], v6 offset:72\n" \
  "v_mfma_f32_16x16x32_f16 v[28:31], v[108:111], v[112:115], v[28:31]\nds_read_b64_tr_b16 v[80:81], v5 offset:1024\n" \
  "ds_read_b64_tr_b16 v[82:83], v5 offset:1056\ns_waitcnt lgkmcnt(10)\nv_mfma_f32_16x16x32_f16 v[28:31], v[100:103], v[72:75], v[28:31]\n" \
  "v_cvt_pkrtz_f16_f32 v32, v24, v25\nv_cvt_pkrtz_f16_f32 v33, v26, v27\nv_mfma_f32_16x16x32_f16 v[28:31], v[92:95], v[104:107], v[28:31]\n" \
  "v_pk_add_u16 v32, v32, 4 op_sel_hi:[1,0] clamp\ns_waitcnt lgkmcnt(5)\nv_mfma_f32_16x16x32_f16 v[28:31], v[84:87], v[64:67], v[28:31]\n" \
  "ds_read_b64_tr_b16 v[108:109], v5 offset:1280\nds_read_b64_tr_b16 v[110:111], v5 offset:1312\n" \
  "v_mfma_f32_16x16x32_f16 v[28:31], v[76:79], v[124:127], v[28:31]\nds_read_b64_tr_b16 v[112:113], v7 offset:0\n" \
  "ds_read_b64_tr_b16 v[114:115], v7 offset:64\ns_waitcnt lgkmcnt(6)\nv_mfma_f32_16x16x32_f16 v[28:31], v[68:71], v[40:43], v[28:31]\n" \
  "ds_read2_b32 v[100:101], v8 offset0:48 offset1:49\nds_read2_b32 v[102:103], v8 offset0:50 offset1:51\n" \
  "v_mfma_f32_16x16x32_f16 v[28:31], v[60:63], v[96:99], v[28:31]\nv_pk_add_u16 v33, v33, 4 op_sel_hi:[1,0] clamp\n" \
  "ds_write_b64 v4, v[32:33] offset:3072\nv_mfma_f32_16x16x32_f16 v[28:31], v[44:47], v[116:119], v[28:31]\n" \
  "v_mfma_f32_16x16x32_f16 v[28:31], v[88:91], v[52:55], v[28:31]\nv_mfma_f32_16x16x32_f16 v[28:31], v[48:51], v[36:39], v[28:31]\n" \
  "s_waitcnt lgkmcnt(5)\nv_mfma_f32_16x16x32_f16 v[28:31], v[80:83], v[56:59], v[28:31]\n" \
  "v_mfma_f32_16x16x32_f16 v[28:31], v[108:111], v[120:123], v[28:31]\ns_waitcnt lgkmcnt(1)\n" \
  "v_mfma_f32_16x16x32_f16 v[28:31], v[112:115], v[100:103], v[28:31]\ns_nop 7\nv_cvt_pkrtz_f16_f32 v32, v28, v29\nv_cvt_pkrtz_f16_f32 v33, v30, v31\n" \
  "v_pk_add_u16 v32, v32, 4 op_sel_hi:[1,0] clamp\nv_pk_add_u16 v33, v33, 4 op_sel_hi:[1,0] clamp\nds_write_b64 v4, v[32:33] offset:4096\n" \
  "s_setprio 0\ns_setpc_b64 s[90:91]\n21:\ns_movk_i32 s84, 0x4000\n23:\nds_read_b32 v35, v9 offset:32\ns_sub_u32 s84, s84, 1\ns_cbranch_scc1 22b\n" \
  "s_waitcnt lgkmcnt(0)\nv_readfirstlane_b32 s85, v35\ns_cmp_lt_u32 s85, 4\ns_cbranch_scc1 23b\ns_branch 22b\n1:\n" \
  "ds_read_b64 v[124:125], v3 offset:1088\nds_read_b64 v[126:127], v3 offset:1096\nds_read_b64 v[120:121], v3 offset:0\n" \
  "ds_read_b64 v[122:123], v3 offset:8\nds_read_b64 v[116:117], v3 offset:64\nds_read_b64 v[118:119], v3 offset:72\n" \
  "ds_read_b64 v[112:113], v3 offset:128\nds_read_b64 v[114:115], v3 offset:136\nds_read_b64 v[108:109], v3 offset:192\n" \
  "ds_read_b64 v[110:111], v3 offset:200\nds_read_b64 v[104:105], v3 offset:256\nds_read_b64 v[106:107], v3 offset:264\n" \
  "ds_read_b64 v[100:101], v3 offset:320\nds_read_b64 v[102:103], v3 offset:328\nds_read_b64 v[96:97], v3 offset:384\n" \
  "ds_read_b64 v[98:99], v3 offset:392\nds_read_b64 v[92:93], v3 offset:448\nds_read_b64 v[94:95], v3 offset:456\n" \
  "ds_read_b64 v[88:89], v3 offset:512\nds_read_b64 v[90:91], v3 offset:520\nds_read_b64 v[84:85], v3 offset:576\n" \
  "ds_read_b64 v[86:87], v3 offset:584\nds_read_b64 v[80:81], v3 offset:640\nds_read_b64 v[82:83], v3 offset:648\n" \
  "ds_read_b64 v[76:77], v3 offset:704\nds_read_b64 v[78:79], v3 offset:712\nds_read_b64 v[72:73], v3 offset:768\n" \
  "ds_read_b64 v[74:75], v3 offset:776\nds_read_b64 v[68:69], v3 offset:832\nds_read_b64 v[70:71], v3 offset:840\n" \
  "ds_read_b64 v[64:65], v3 offset:896\nds_read_b64 v[66:67], v3 offset:904\nds_read_b64 v[60:61], v3 offset:960\n" \
  "ds_read_b64 v[62:63], v3 offset:968\nds_read_b64 v[56:57], v3 offset:1024\nds_read_b64 v[58:59], v3 offset:1032\n" \
  "ds_read_b64 v[52:53], v6 offset:0\nds_read_b64 v[54:55], v6 offset:8\nds_read_b64 v[48:49], v6 offset:64\nds_read_b64 v[50:51], v6 offset:72\n" \
  "s_waitcnt lgkmcnt(0)\ns_barrier\nds_read_b128 v[28:31], v2 offset:960\nds_read_b128 v[32:35], v2 offset:1472\n" \
  "ds_read_b128 v[36:39], v2 offset:1984\nds_read_b128 v[40:43], v2 offset:2048\nds_read_b128 v[44:47], v2 offset:2112\ns_waitcnt lgkmcnt(3)\n" \
  "v_mfma_f32_16x16x32_f16 v[12:15], v[124:127], v[28:31], 0\ns_setprio 1\nds_read_b128 v[28:31], v2 offset:2176\n" \
  "v_mfma_f32_16x16x32_f16 v[16:19], v[124:127], v[32:35], 0\nds_read_b128 v[32:35], v2 offset:2240\ns_waitcnt lgkmcnt(3)\n" \
  "v_mfma_f32_16x16x32_f16 v[12:15], v[120:123], v[36:39], v[12:15]\nv_mfma_f32_16x16x32_f16 v[20:23], v[124:127], v[36:39], 0\n" \
  "ds_read_b128 v[124:127], v2 offset:2304\nds_read_b128 v[36:39], v2 offset:2368\nv_mfma_f32_16x16x32_f16 v[12:15], v[116:119], v[40:43], v[12:15]\n" \
  "ds_read_b128 v[40:43], v2 offset:2432\ns_waitcnt lgkmcnt(4)\nv_mfma_f32_16x16x32_f16 v[12:15], v[112:115], v[44:47], v[12:15]\n" \
  "ds_read_b128 v[44:47], v2 offset:2496\nv_mfma_f32_16x16x32_f16 v[12:15], v[108:111], v[28:31], v[12:15]\nds_read_b128 v[28:31], v2 offset:2560\n" \
  "s_waitcnt lgkmcnt(4)\nv_mfma_f32_16x16x32_f16 v[12:15], v[104:107], v[32:35], v[12:15]\nds_read_b128 v[32:35], v2 offset:2624\n" \
  "v_mfma_f32_16x16x32_f16 v[12:15], v[100:103], v[124:127], v[12:15]\ns_waitcnt lgkmcnt(3)\n" \
  "v_mfma_f32_16x16x32_f16 v[12:15], v[96:99], v[36:39], v[12:15]\nds_read_b128 v[124:127], v2 offset:2688\n" \
  "v_mfma_f32_16x16x32_f16 v[12:15], v[92:95], v[40:43], v[12:15]\ns_waitcnt lgkmcnt(3)\n" \
  "v_mfma_f32_16x16x32_f16 v[12:15], v[88:91], v[44:47], v[12:15]\nds_read_b128 v[36:39], v2 offset:2752\n" \
  "v_mfma_f32_16x16x32_f16 v[16:19], v[120:123], v[44:47], v[16:19]\ns_waitcnt lgkmcnt(3)\n" \
  "v_mfma_f32_16x16x32_f16 v[12:15], v[84:87], v[28:31], v[12:15]\nds_read_b128 v[40:43], v2 offset:2816\n" \
  "v_mfma_f32_16x16x32_f16 v[16:19], v[116:119], v[28:31], v[16:19]\ns_waitcnt lgkmcnt(3)\n" \
  "v_mfma_f32_16x16x32_f16 v[12:15], v[80:83], v[32:35], v[12:15]\nds_read_b128 v[44:47], v2 offset:2880\n" \
  "v_mfma_f32_16x16x32_f16 v[16:19], v[112:115], v[32:35], v[16:19]\ns_waitcnt lgkmcnt(3)\n" \
  "v_mfma_f32_16x16x32_f16 v[12:15], v[76:79], v[124:127], v[12:15]\nds_read_b128 v[28:31], v2 offset:2944\n" \
  "v_mfma_f32_16x16x32_f16 v[16:19], v[108:111], v[124:127], v[16:19]\ns_waitcnt lgkmcnt(3)\n" \
  "v_mfma_f32_16x16x32_f16 v[12:15], v[72:75], v[36:39], v[12:15]\nds_read_b128 v[32:35], v2 offset:3008\n" \
  "v_mfma_f32_16x16x32_f16 v[16:19], v[104:107], v[36:39], v[16:19]\ns_waitcnt lgkmcnt(3)\n" \
  "v_mfma_f32_16x16x32_f16 v[12:15], v[68:71], v[40:43], v[12:15]\nv_mfma_f32_16x16x32_f16 v[16:19], v[100:103], v[40:43], v[16:19]\n" \
  "ds_read_b128 v[124:127], v2 offset:3072\ns_waitcnt lgkmcnt(3)\nv_mfma_f32_16x16x32_f16 v[12:15], v[64:67], v[44:47], v[12:15]\n" \
  "v_mfma_f32_16x16x32_f16 v[16:19], v[96:99], v[44:47], v[16:19]\nds_read_b128 v[36:39], v2 offset:3136\ns_waitcnt lgkmcnt(3)\n" \
  "v_mfma_f32_16x16x32_f16 v[12:15], v[60:63], v[28:31], v[12:15]\nds_read_b64 v[40:41], v6 offset:128\nds_read_b64 v[42:43], v6 offset:136\n" \
  "v_mfma_f32_16x16x32_f16 v[16:19], v[92:95], v[28:31], v[16:19]\nds_read_b128 v[44:47], v2 offset:3200\ns_waitcnt lgkmcnt(4)\n" \
  "v_mfma_f32_16x16x32_f16 v[12:15], v[56:59], v[32:35], v[12:15]\nv_mfma_f32_16x16x32_f16 v[16:19], v[88:91], v[32:35], v[16:19]\n" \
  "ds_read_b128 v[28:31], v2 offset:3264\nv_mfma_f32_16x16x32_f16 v[20:23], v[120:123], v[32:35], v[20:23]\nds_read_b64 v[120:121], v6 offset:192\n" \
  "ds_read_b64 v[122:123], v6 offset:200\nv_mfma_f32_16x16x32_f16 v[16:19], v[84:87], v[124:127], v[16:19]\nds_read_b128 v[32:35], v2 offset:3328\n" \
  "v_mfma_f32_16x16x32_f16 v[20:23], v[116:119], v[124:127], v[20:23]\nds_read_b64 v[116:117], v6 offset:256\nds_read_b64 v[118:119], v6 offset:264\n" \
  "s_waitcnt lgkmcnt(6)\nv_mfma_f32_16x16x32_f16 v[16:19], v[80:83], v[36:39], v[16:19]\nds_read_b128 v[124:127], v2 offset:3392\n" \
  "v_mfma_f32_16x16x32_f16 v[20:23], v[112:115], v[36:39], v[20:23]\nds_read_b64 v[112:113], v6 offset:320\nds_read_b64 v[114:115], v6 offset:328\n" \
  "v_mfma_f32_16x16x32_f16 v[16:19], v[76:79], v[44:47], v[16:19]\nds_read_b128 v[36:39], v2 offset:3456\n" \
  "v_mfma_f32_16x16x32_f16 v[20:23], v[108:111], v[44:47], v[20:23]\ns_waitcnt lgkmcnt(6)\n" \
  "v_mfma_f32_16x16x32_f16 v[16:19], v[72:75], v[28:31], v[16:19]\nds_read_b128 v[108:111], v2 offset:3520\n" \
  "v_mfma_f32_16x16x32_f16 v[20:23], v[104:107], v[28:31], v[20:23]\nv_mfma_f32_16x16x32_f16 v[16:19], v[68:71], v[32:35], v[16:19]\n" \
  "ds_read_b128 v[44:47], v2 offset:3584\nv_mfma_f32_16x16x32_f16 v[20:23], v[100:103], v[32:35], v[20:23]\nds_read_b128 v[104:107], v2 offset:3648\n" \
  "ds_read_b32 v27, v9 offset:32\ns_waitcnt lgkmcnt(4)\nv_mfma_f32_16x16x32_f16 v[16:19], v[64:67], v[124:127], v[16:19]\n" \
  "ds_read_b128 v[28:31], v2 offset:3712\nv_mfma_f32_16x16x32_f16 v[20:23], v[96:99], v[124:127], v[20:23]\nds_read_b128 v[100:103], v2 offset:3776\n" \
  "ds_read2_b32 v[32:33], v8 offset0:32 offset1:33\nds_read2_b32 v[34:35], v8 offset0:34 offset1:35\n" \
  "v_mfma_f32_16x16x32_f16 v[16:19], v[60:63], v[36:39], v[16:19]\nds_read_b128 v[96:99], v2 offset:3840\n" \
  "v_mfma_f32_16x16x32_f16 v[20:23], v[92:95], v[36:39], v[20:23]\nds_read_b128 v[124:127], v2 offset:3904\ns_waitcnt lgkmcnt(8)\n" \
  "v_mfma_f32_16x16x32_f16 v[16:19], v[56:59], v[108:111], v[16:19]\nds_read_b128 v[92:95], v2 offset:3968\ns_waitcnt lgkmcnt(7)\n" \
  "v_readfirstlane_b32 s85, v27\ns_cmp_lt_u32 s85, 4\ns_cbranch_scc1 31f\n32:\nv_mfma_f32_16x16x32_f16 v[20:23], v[88:91], v[108:111], v[20:23]\n" \
  "ds_read_b128 v[36:39], v2 offset:4032\nds_read_b64_tr_b16 v[88:89], v5 offset:512\nds_read_b64_tr_b16 v[90:91], v5 offset:544\n" \
  "v_mfma_f32_16x16x32_f16 v[20:23], v[84:87], v[44:47], v[20:23]\nds_read_b64_tr_b16 v[108:109], v5 offset:768\n" \
  "ds_read_b64_tr_b16 v[110:111], v5 offset:800\nv_mfma_f32_16x16x32_f16 v[20:23], v[80:83], v[104:107], v[20:23]\n" \
  "ds_read_b64_tr_b16 v[84:85], v5 offset:1024\nds_read_b64_tr_b16 v[86:87], v5 offset:1056\ns_waitcnt lgkmcnt(12)\n" \
  "v_mfma_f32_16x16x32_f16 v[20:23], v[76:79], v[28:31], v[20:23]\nds_read_b64_tr_b16 v[44:45], v5 offset:1280\n" \
  "ds_read_b64_tr_b16 v[46:47], v5 offset:1312\nv_mfma_f32_16x16x32_f16 v[20:23], v[72:75], v[100:103], v[20:23]\ns_waitcnt lgkmcnt(10)\n" \
  "v_mfma_f32_16x16x32_f16 v[20:23], v[68:71], v[96:99], v[20:23]\nds_read_b64_tr_b16 v[80:81], v5 offset:1536\n" \
  "ds_read_b64_tr_b16 v[82:83], v5 offset:1568\nv_mfma_f32_16x16x32_f16 v[20:23], v[64:67], v[124:127], v[20:23]\n" \
  "ds_read_b64_tr_b16 v[104:105], v5 offset:1792\nds_read_b64_tr_b16 v[106:107], v5 offset:1824\ns_waitcnt lgkmcnt(12)\n" \
  "v_mfma_f32_16x16x32_f16 v[20:23], v[60:63], v[92:95], v[20:23]\nds_read_b64_tr_b16 v[76:77], v5 offset:2048\n" \
  "ds_read_b64_tr_b16 v[78:79], v5 offset:2080\nv_mfma_f32_16x16x32_f16 v[20:23], v[56:59], v[36:39], v[20:23]\ns_waitcnt lgkmcnt(10)\n" \
  "v_mfma_f32_16x16x32_f16 v[12:15], v[88:91], v[52:55], v[12:15]\nds_read_b64_tr_b16 v[28:29], v5 offset:2304\n" \
  "ds_read_b64_tr_b16 v[30:31], v5 offset:2336\nv_mfma_f32_16x16x32_f16 v[12:15], v[108:111], v[48:51], v[12:15]\n" \
  "ds_read_b64_tr_b16 v[72:73], v5 offset:2560\nds_read_b64_tr_b16 v[74:75], v5 offset:2592\ns_waitcnt lgkmcnt(10)\n" \
  "v_mfma_f32_16x16x32_f16 v[12:15], v[84:87], v[40:43], v[12:15]\nds_read_b64_tr_b16 v[100:101], v5 offset:2816\n" \
  "ds_read_b64_tr_b16 v[102:103], v5 offset:2848\nv_mfma_f32_16x16x32_f16 v[16:19], v[84:87], v[52:55], v[16:19]\n" \
  "ds_read_b64_tr_b16 v[68:69], v7 offset:0\nds_read_b64_tr_b16 v[70:71], v7 offset:64\n" \
  "v_mfma_f32_16x16x32_f16 v[12:15], v[44:47], v[120:123], v[12:15]\nv_mfma_f32_16x16x32_f16 v[16:19], v[44:47], v[48:51], v[16:19]\n" \
  "s_waitcnt lgkmcnt(10)\nv_mfma_f32_16x16x32_f16 v[12:15], v[80:83], v[116:119], v[12:15]\nds_read2_b32 v[96:97], v8 offset0:16 offset1:17\n" \
  "ds_read2_b32 v[98:99], v8 offset0:18 offset1:19\nv_mfma_f32_16x16x32_f16 v[16:19], v[80:83], v[40:43], v[16:19]\n" \
  "ds_read2_b32 v[64:65], v8 offset0:0 offset1:1\nds_read2_b32 v[66:67], v8 offset0:2 offset1:3\n" \
  "v_mfma_f32_16x16x32_f16 v[20:23], v[80:83], v[52:55], v[20:23]\nv_mfma_f32_16x16x32_f16 v[12:15], v[104:107], v[112:115], v[12:15]\n" \
  "v_mfma_f32_16x16x32_f16 v[16:19], v[104:107], v[120:123], v[16:19]\nv_mfma_f32_16x16x32_f16 v[20:23], v[104:107], v[48:51], v[20:23]\n" \
  "s_waitcnt lgkmcnt(10)\nv_mfma_f32_16x16x32_f16 v[16:19], v[76:79], v[116:119], v[16:19]\nds_read_b64_tr_b16 v[124:125], v7 offset:512\n" \
  "ds_read_b64_tr_b16 v[126:127], v7 offset:576\nv_mfma_f32_16x16x32_f16 v[20:23], v[76:79], v[40:43], v[20:23]\n" \
  "ds_read2_b32 v[60:61], v8 offset0:48 offset1:49\nds_read2_b32 v[62:63], v8 offset0:50 offset1:51\n" \
  "v_mfma_f32_16x16x32_f16 v[16:19], v[28:31], v[112:115], v[16:19]\nv_mfma_f32_16x16x32_f16 v[20:23], v[28:31], v[120:123], v[20:23]\n" \
  "s_waitcnt lgkmcnt(10)\nv_mfma_f32_16x16x32_f16 v[20:23], v[72:75], v[116:119], v[20:23]\nds_read_b64_tr_b16 v[92:93], v7 offset:1024\n" \
  "ds_read_b64_tr_b16 v[94:95], v7 offset:1088\nv_mfma_f32_16x16x32_f16 v[20:23], v[100:103], v[112:115], v[20:23]\n" \
  "ds_read_b64_tr_b16 v[56:57], v7 offset:1536\nds_read_b64_tr_b16 v[58:59], v7 offset:1600\ns_waitcnt lgkmcnt(10)\n" \
  "v_mfma_f32_16x16x32_f16 v[12:15], v[68:71], v[32:35], v[12:15]\nv_mfma_f32_16x16x32_f16 v[16:19], v[68:71], v[96:99], v[16:19]\n" \
  "s_waitcnt lgkmcnt(6)\nv_mfma_f32_16x16x32_f16 v[20:23], v[68:71], v[64:67], v[20:23]\ns_waitcnt lgkmcnt(2)\n" \
  "v_mfma_f32_16x16x32_f16 v[12:15], v[124:127], v[60:63], v[12:15]\nv_mfma_f32_16x16x32_f16 v[16:19], v[124:127], v[32:35], v[16:19]\n" \
  "v_mfma_f32_16x16x32_f16 v[20:23], v[124:127], v[96:99], v[20:23]\nv_mfma_f32_16x16x32_f16 v[16:19], v[92:95], v[60:63], v[16:19]\n" \
  "v_mfma_f32_16x16x32_f16 v[20:23], v[92:95], v[32:35], v[20:23]\ns_waitcnt lgkmcnt(0)\n" \
  "v_mfma_f32_16x16x32_f16 v[20:23], v[56:59], v[60:63], v[20:23]\ns_nop 1\nv_cvt_pkrtz_f16_f32 v24, v12, v13\nv_cvt_pkrtz_f16_f32 v25, v14, v15\n" \
  "v_pk_add_u16 v24, v24, 4 op_sel_hi:[1,0] clamp\nv_pk_add_u16 v25, v25, 4 op_sel_hi:[1,0] clamp\nds_write_b64 v4, v[24:25] offset:5120\n" \
  "v_cvt_pkrtz_f16_f32 v24, v16, v17\nv_cvt_pkrtz_f16_f32 v25, v18, v19\nv_pk_add_u16 v24, v24, 4 op_sel_hi:[1,0] clamp\n" \
  "v_pk_add_u16 v25, v25, 4 op_sel_hi:[1,0] clamp\nds_write_b64 v4, v[24:25] offset:6144\nv_cvt_pkrtz_f16_f32 v24, v20, v21\n" \
  "v_cvt_pkrtz_f16_f32 v25, v22, v23\nv_pk_add_u16 v24, v24, 4 op_sel_hi:[1,0] clamp\nv_pk_add_u16 v25, v25, 4 op_sel_hi:[1,0] clamp\n" \
  "ds_write_b64 v4, v[24:25] offset:7168\ns_setprio 0\ns_setpc_b64 s[90:91]\n31:\ns_movk_i32 s84, 0x4000\n33:\nds_read_b32 v27, v9 offset:32\n" \
  "s_sub_u32 s84, s84, 1\ns_cbranch_scc1 32b\ns_waitcnt lgkmcnt(0)\nv_readfirstlane_b32 s85, v27\ns_cmp_lt_u32 s85, 4\ns_cbranch_scc1 33b\n" \
  "s_branch 32b\n9:\ns_cmp_eq_u32 s89, 3\ns_cbranch_scc1 3f\ns_cmp_eq_u32 s89, 4\ns_cbranch_scc1 4f\ns_cmp_eq_u32 s89, 5\ns_cbranch_scc1 5f\n" \
  "ds_read_b64 v[124:125], v3 offset:576\nds_read_b64 v[126:127], v3 offset:584\nds_read_b64 v[120:121], v3 offset:64\n" \
  "ds_read_b64 v[122:123], v3 offset:72\nds_read_b64 v[116:117], v3 offset:640\nds_read_b64 v[118:119], v3 offset:648\n" \
  "ds_read_b64 v[112:113], v3 offset:128\nds_read_b64 v[114:115], v3 offset:136\nds_read_b64 v[108:109], v3 offset:704\n" \
  "ds_read_b64 v[110:111], v3 offset:712\nds_read_b64 v[104:105], v3 offset:192\nds_read_b64 v[106:107], v3 offset:200\n" \
  "ds_read_b64 v[100:101], v3 offset:768\nds_read_b64 v[102:103], v3 offset:776\nds_read_b64 v[96:97], v3 offset:256\n" \
  "ds_read_b64 v[98:99], v3 offset:264\nds_read_b64 v[92:93], v3 offset:832\nds_read_b64 v[94:95], v3 offset:840\n" \
  "ds_read_b64 v[88:89], v3 offset:320\nds_read_b64 v[90:91], v3 offset:328\nds_read_b64 v[84:85], v3 offset:896\n" \
  "ds_read_b64 v[86:87], v3 offset:904\nds_read_b64 v[80:81], v3 offset:384\nds_read_b64 v[82:83], v3 offset:392\n" \
  "ds_read_b64 v[76:77], v3 offset:960\nds_read_b64 v[78:79], v3 offset:968\nds_read_b64 v[72:73], v3 offset:448\n" \
  "ds_read_b64 v[74:75], v3 offset:456\nds_read_b64 v[68:69], v3 offset:1024\nds_read_b64 v[70:71], v3 offset:1032\n" \
  "ds_read_b64 v[64:65], v3 offset:512\nds_read_b64 v[66:67], v3 offset:520\ns_waitcnt lgkmcnt(0)\ns_barrier\nds_read_b128 v[24:27], v2 offset:0\n" \
  "ds_read_b128 v[28:31], v2 offset:64\nds_read_b128 v[32:35], v2 offset:128\nds_read_b128 v[36:39], v2 offset:192\ns_waitcnt lgkmcnt(3)\n" \
  "v_mfma_f32_16x16x32_f16 v[12:15], v[124:127], v[24:27], 0\ns_setprio 1\nds_read_b128 v[40:43], v2 offset:256\n" \
  "v_mfma_f32_16x16x32_f16 v[16:19], v[120:123], v[24:27], 0\ns_waitcnt lgkmcnt(3)\nv_mfma_f32_16x16x32_f16 v[12:15], v[116:119], v[28:31], v[12:15]\n" \
  "ds_read_b128 v[44:47], v2 offset:320\nv_mfma_f32_16x16x32_f16 v[16:19], v[112:115], v[28:31], v[16:19]\ns_waitcnt lgkmcnt(3)\n" \
  "v_mfma_f32_16x16x32_f16 v[12:15], v[108:111], v[32:35], v[12:15]\nds_read_b128 v[48:51], v2 offset:384\n" \
  "v_mfma_f32_16x16x32_f16 v[16:19], v[104:107], v[32:35], v[16:19]\ns_waitcnt lgkmcnt(3)\n" \
  "v_mfma_f32_16x16x32_f16 v[12:15], v[100:103], v[36:39], v[12:15]\nds_read_b128 v[52:55], v2 offset:448\n" \
  "v_mfma_f32_16x16x32_f16 v[16:19], v[96:99], v[36:39], v[16:19]\ns_waitcnt lgkmcnt(3)\n" \
  "v_mfma_f32_16x16x32_f16 v[12:15], v[92:95], v[40:43], v[12:15]\nds_read_b128 v[56:59], v2 offset:512\n" \
  "v_mfma_f32_16x16x32_f16 v[16:19], v[88:91], v[40:43], v[16:19]\nds_read_b128 v[60:63], v2 offset:576\ns_waitcnt lgkmcnt(3)\n" \
  "v_mfma_f32_16x16x32_f16 v[12:15], v[84:87], v[44:47], v[12:15]\nds_read_b128 v[120:123], v2 offset:640\n" \
  "v_mfma_f32_16x16x32_f16 v[16:19], v[80:83], v[44:47], v[16:19]\nds_read_b128 v[24:27], v2 offset:704\n" \
  "v_mfma_f32_16x16x32_f16 v[12:15], v[76:79], v[48:51], v[12:15]\nds_read_b128 v[112:115], v2 offset:768\n" \
  "v_mfma_f32_16x16x32_f16 v[16:19], v[72:75], v[48:51], v[16:19]\nds_read_b128 v[28:31], v2 offset:832\ns_waitcnt lgkmcnt(5)\n" \
  "v_mfma_f32_16x16x32_f16 v[12:15], v[68:71], v[52:55], v[12:15]\nds_read_b128 v[104:107], v2 offset:896\n" \
  "v_mfma_f32_16x16x32_f16 v[16:19], v[64:67], v[52:55], v[16:19]\nds_read_b128 v[32:35], v2 offset:960\n" \
  "v_mfma_f32_16x16x32_f16 v[16:19], v[124:127], v[56:59], v[16:19]\ns_waitcnt lgkmcnt(5)\n" \
  "v_mfma_f32_16x16x32_f16 v[16:19], v[116:119], v[60:63], v[16:19]\nv_mfma_f32_16x16x32_f16 v[16:19], v[108:111], v[120:123], v[16:19]\n" \
  "s_waitcnt lgkmcnt(3)\nv_mfma_f32_16x16x32_f16 v[16:19], v[100:103], v[24:27], v[16:19]\nv_cvt_pkrtz_f16_f32 v20, v12, v13\n" \
  "v_cvt_pkrtz_f16_f32 v21, v14, v15\nv_mfma_f32_16x16x32_f16 v[16:19], v[92:95], v[112:115], v[16:19]\n" \
  "v_pk_add_u16 v20, v20, 4 op_sel_hi:[1,0] clamp\ns_waitcnt lgkmcnt(2)\nv_mfma_f32_16x16x32_f16 v[16:19], v[84:87], v[28:31], v[16:19]\n" \
  "v_pk_add_u16 v21, v21, 4 op_sel_hi:[1,0] clamp\ns_waitcnt lgkmcnt(1)\nv_mfma_f32_16x16x32_f16 v[16:19], v[76:79], v[104:107], v[16:19]\n" \
  "ds_write_b64 v4, v[20:21] offset:0\ns_waitcnt lgkmcnt(1)\nv_mfma_f32_16x16x32_f16 v[16:19], v[68:71], v[32:35], v[16:19]\ns_nop 7\n" \
  "v_cvt_pkrtz_f16_f32 v20, v16, v17\nv_cvt_pkrtz_f16_f32 v21, v18, v19\nv_pk_add_u16 v20, v20, 4 op_sel_hi:[1,0] clamp\n" \
  "v_pk_add_u16 v21, v21, 4 op_sel_hi:[1,0] clamp\nds_write_b64 v4, v[20:21] offset:1024\ns_setprio 0\ns_setpc_b64 s[90:91]\n3:\n" \
  "ds_read_b64 v[124:125], v3 offset:0\nds_read_b64 v[126:127], v3 offset:8\nds_read_b64 v[120:121], v3 offset:64\n" \
  "ds_read_b64 v[122:123], v3 offset:72\nds_read_b64 v[116:117], v3 offset:128\nds_read_b64 v[118:119], v3 offset:136\n" \
  "ds_read_b64 v[112:113], v3 offset:192\nds_read_b64 v[114:115], v3 offset:200\nds_read_b64 v[108:109], v3 offset:256\n" \
  "ds_read_b64 v[110:111], v3 offset:264\nds_read_b64 v[104:105], v3 offset:320\nds_read_b64 v[106:107], v3 offset:328\n" \
  "ds_read_b64 v[100:101], v3 offset:384\nds_read_b64 v[102:103], v3 offset:392\nds_read_b64 v[96:97], v3 offset:448\n" \
  "ds_read_b64 v[98:99], v3 offset:456\nds_read_b64 v[92:93], v3 offset:512\nds_read_b64 v[94:95], v3 offset:520\n" \
  "ds_read_b64 v[88:89], v3 offset:576\nds_read_b64 v[90:91], v3 offset:584\nds_read_b64 v[84:85], v3 offset:640\n" \
  "ds_read_b64 v[86:87], v3 offset:648\nds_read_b64 v[80:81], v3 offset:704\nds_read_b64 v[82:83], v3 offset:712\n" \
  "ds_read_b64 v[76:77], v3 offset:768\nds_read_b64 v[78:79], v3 offset:776\nds_read_b64 v[72:73], v3 offset:832\n" \
  "ds_read_b64 v[74:75], v3 offset:840\nds_read_b64 v[68:69], v3 offset:896\nds_read_b64 v[70:71], v3 offset:904\n" \
  "ds_read_b64 v[64:65], v3 offset:960\nds_read_b64 v[66:67], v3 offset:968\nds_read_b64 v[60:61], v3 offset:1024\n" \
  "ds_read_b64 v[62:63], v3 offset:1032\nds_read_b64 v[56:57], v6 offset:256\nds_read_b64 v[58:59], v6 offset:264\n" \
  "ds_read_b64 v[52:53], v6 offset:128\nds_read_b64 v[54:55], v6 offset:136\nds_read_b64 v[48:49], v6 offset:320\n" \
  "ds_read_b64 v[50:51], v6 offset:328\nds_read_b64 v[44:45], v6 offset:192\nds_read_b64 v[46:47], v6 offset:200\ns_waitcnt lgkmcnt(0)\ns_barrier\n" \
  "ds_read_b128 v[24:27], v2 offset:448\nds_read_b128 v[28:31], v2 offset:512\nds_read_b128 v[32:35], v2 offset:576\n" \
  "ds_read_b128 v[36:39], v2 offset:640\nds_read_b128 v[40:43], v2 offset:704\ns_waitcnt lgkmcnt(3)\n" \
  "v_mfma_f32_16x16x32_f16 v[12:15], v[124:127], v[24:27], 0\ns_setprio 1\nds_read_b128 v[24:27], v2 offset:768\n" \
  "v_mfma_f32_16x16x32_f16 v[12:15], v[120:123], v[28:31], v[12:15]\nds_read_b128 v[28:31], v2 offset:832\ns_waitcnt lgkmcnt(3)\n" \
  "v_mfma_f32_16x16x32_f16 v[12:15], v[116:119], v[32:35], v[12:15]\nds_read_b128 v[32:35], v2 offset:896\n" \
  "v_mfma_f32_16x16x32_f16 v[12:15], v[112:115], v[36:39], v[12:15]\nds_read_b128 v[36:39], v2 offset:960\ns_waitcnt lgkmcnt(3)\n" \
  "v_mfma_f32_16x16x32_f16 v[12:15], v[108:111], v[40:43], v[12:15]\nds_read_b128 v[40:43], v2 offset:1024\n" \
  "v_mfma_f32_16x16x32_f16 v[12:15], v[104:107], v[24:27], v[12:15]\nds_read_b128 v[24:27], v2 offset:1088\ns_waitcnt lgkmcnt(3)\n" \
  "v_mfma_f32_16x16x32_f16 v[12:15], v[100:103], v[28:31], v[12:15]\nds_read_b128 v[28:31], v2 offset:1152\n" \
  "v_mfma_f32_16x16x32_f16 v[12:15], v[96:99], v[32:35], v[12:15]\ns_waitcnt lgkmcnt(3)\n" \
  "v_mfma_f32_16x16x32_f16 v[12:15], v[92:95], v[36:39], v[12:15]\nds_read_b128 v[32:35], v2 offset:1216\n" \
  "v_mfma_f32_16x16x32_f16 v[16:19], v[124:127], v[36:39], 0\ns_waitcnt lgkmcnt(3)\nv_mfma_f32_16x16x32_f16 v[12:15], v[88:91], v[40:43], v[12:15]\n" \
  "ds_read_b128 v[124:127], v2 offset:1280\nv_mfma_f32_16x16x32_f16 v[16:19], v[120:123], v[40:43], v[16:19]\ns_waitcnt lgkmcnt(3)\n" \
  "v_mfma_f32_16x16x32_f16 v[12:15], v[84:87], v[24:27], v[12:15]\nds_read_b128 v[36:39], v2 offset:1344\n" \
  "v_mfma_f32_16x16x32_f16 v[16:19], v[116:119], v[24:27], v[16:19]\ns_waitcnt lgkmcnt(3)\n" \
  "v_mfma_f32_16x16x32_f16 v[12:15], v[80:83], v[28:31], v[12:15]\nds_read_b128 v[120:123], v2 offset:1408\n" \
  "v_mfma_f32_16x16x32_f16 v[16:19], v[112:115], v[28:31], v[16:19]\ns_waitcnt lgkmcnt(3)\n" \
  "v_mfma_f32_16x16x32_f16 v[12:15], v[76:79], v[32:35], v[12:15]\nds_read_b128 v[40:43], v2 offset:1472\n" \
  "v_mfma_f32_16x16x32_f16 v[16:19], v[108:111], v[32:35], v[16:19]\ns_waitcnt lgkmcnt(3)\n" \
  "v_mfma_f32_16x16x32_f16 v[12:15], v[72:75], v[124:127], v[12:15]\nds_read_b128 v[116:119], v2 offset:1536\n" \
  "v_mfma_f32_16x16x32_f16 v[16:19], v[104:107], v[124:127], v[16:19]\nds_read_b128 v[24:27], v2 offset:1600\ns_waitcnt lgkmcnt(3)\n" \
  "v_mfma_f32_16x16x32_f16 v[12:15], v[68:71], v[36:39], v[12:15]\nds_read_b128 v[112:115], v2 offset:1664\nds_read_b32 v23, v9 offset:32\n" \
  "v_mfma_f32_16x16x32_f16 v[16:19], v[100:103], v[36:39], v[16:19]\nds_read_b128 v[28:31], v2 offset:1728\n" \
  "v_mfma_f32_16x16x32_f16 v[12:15], v[64:67], v[120:123], v[12:15]\nds_read_b128 v[108:111], v2 offset:1792\n" \
  "v_mfma_f32_16x16x32_f16 v[16:19], v[96:99], v[120:123], v[16:19]\nds_read_b128 v[32:35], v2 offset:1856\ns_waitcnt lgkmcnt(6)\n" \
  "v_mfma_f32_16x16x32_f16 v[12:15], v[60:63], v[40:43], v[12:15]\nds_read_b128 v[104:107], v2 offset:1920\n" \
  "v_mfma_f32_16x16x32_f16 v[16:19], v[92:95], v[40:43], v[16:19]\nds_read_b128 v[124:127], v2 offset:1984\ns_waitcnt lgkmcnt(5)\n" \
  "v_readfirstlane_b32 s85, v23\ns_cmp_lt_u32 s85, 3\ns_cbranch_scc1 51f\n52:\nv_mfma_f32_16x16x32_f16 v[16:19], v[88:91], v[116:119], v[16:19]\n" \
  "ds_read_b64_tr_b16 v[100:101], v5 offset:0\nds_read_b64_tr_b16 v[102:103], v5 offset:32\n" \
  "v_mfma_f32_16x16x32_f16 v[16:19], v[84:87], v[24:27], v[16:19]\nds_read_b64_tr_b16 v[36:37], v5 offset:256\n" \
  "ds_read_b64_tr_b16 v[38:39], v5 offset:288\nv_mfma_f32_16x16x32_f16 v[16:19], v[80:83], v[112:115], v[16:19]\n" \
  "ds_read_b64_tr_b16 v[96:97], v5 offset:512\nds_read_b64_tr_b16 v[98:99], v5 offset:544\ns_waitcnt lgkmcnt(9)\n" \
  "v_mfma_f32_16x16x32_f16 v[16:19], v[76:79], v[28:31], v[16:19]\nds_read_b64_tr_b16 v[120:121], v5 offset:768\n" \
  "ds_read_b64_tr_b16 v[122:123], v5 offset:800\nv_mfma_f32_16x16x32_f16 v[16:19], v[72:75], v[108:111], v[16:19]\ns_waitcnt lgkmcnt(9)\n" \
  "v_mfma_f32_16x16x32_f16 v[16:19], v[68:71], v[32:35], v[16:19]\nv_mfma_f32_16x16x32_f16 v[16:19], v[64:67], v[104:107], v[16:19]\n" \
  "s_waitcnt lgkmcnt(6)\nv_mfma_f32_16x16x32_f16 v[16:19], v[60:63], v[124:127], v[16:19]\n" \
  "v_mfma_f32_16x16x32_f16 v[12:15], v[100:103], v[56:59], v[12:15]\nv_mfma_f32_16x16x32_f16 v[16:19], v[100:103], v[52:55], v[16:19]\n" \
  "s_waitcnt lgkmcnt(2)\nv_mfma_f32_16x16x32_f16 v[12:15], v[36:39], v[48:51], v[12:15]\n" \
  "v_mfma_f32_16x16x32_f16 v[16:19], v[36:39], v[44:47], v[16:19]\nv_mfma_f32_16x16x32_f16 v[16:19], v[96:99], v[56:59], v[16:19]\n" \
  "s_waitcnt lgkmcnt(0)\nv_mfma_f32_16x16x32_f16 v[16:19], v[120:123], v[48:51], v[16:19]\ns_nop 3\nv_cvt_pkrtz_f16_f32 v20, v12, v13\n" \
  "v_cvt_pkrtz_f16_f32 v21, v14, v15\nv_pk_add_u16 v20, v20, 4 op_sel_hi:[1,0] clamp\nv_pk_add_u16 v21, v21, 4 op_sel_hi:[1,0] clamp\n" \
  "ds_write_b64 v4, v[20:21] offset:2048\nv_cvt_pkrtz_f16_f32 v20, v16, v17\nv_cvt_pkrtz_f16_f32 v21, v18, v19\n" \
  "v_pk_add_u16 v20, v20, 4 op_sel_hi:[1,0] clamp\nv_pk_add_u16 v21, v21, 4 op_sel_hi:[1,0] clamp\nds_write_b64 v4, v[20:21] offset:3072\n" \
  "s_setprio 0\ns_setpc_b64 s[90:91]\n51:\ns_movk_i32 s84, 0x4000\n53:\nds_read_b32 v23, v9 offset:32\ns_sub_u32 s84, s84, 1\ns_cbranch_scc1 52b\n" \
  "s_waitcnt lgkmcnt(0)\nv_readfirstlane_b32 s85, v23\ns_cmp_lt_u32 s85, 3\ns_cbranch_scc1 53b\ns_branch 52b\n4:\n" \
  "ds_read_b64 v[124:125], v3 offset:1088\nds_read_b64 v[126:127], v3 offset:1096\nds_read_b64 v[120:121], v3 offset:0\n" \
  "ds_read_b64 v[122:123], v3 offset:8\nds_read_b64 v[116:117], v3 offset:64\nds_read_b64 v[118:119], v3 offset:72\n" \
  "ds_read_b64 v[112:113], v3 offset:128\nds_read_b64 v[114:115], v3 offset:136\nds_read_b64 v[108:109], v3 offset:192\n" \
  "ds_read_b64 v[110:111], v3 offset:200\nds_read_b64 v[104:105], v3 offset:256\nds_read_b64 v[106:107], v3 offset:264\n" \
  "ds_read_b64 v[100:101], v3 offset:320\nds_read_b64 v[102:103], v3 offset:328\nds_read_b64 v[96:97], v3 offset:384\n" \
  "ds_read_b64 v[98:99], v3 offset:392\nds_read_b64 v[92:93], v3 offset:448\nds_read_b64 v[94:95], v3 offset:456\n" \
  "ds_read_b64 v[88:89], v3 offset:512\nds_read_b64 v[90:91], v3 offset:520\nds_read_b64 v[84:85], v3 offset:576\n" \
  "ds_read_b64 v[86:87], v3 offset:584\nds_read_b64 v[80:81], v3 offset:640\nds_read_b64 v[82:83], v3 offset:648\n" \
  "ds_read_b64 v[76:77], v3 offset:704\nds_read_b64 v[78:79], v3 offset:712\nds_read_b64 v[72:73], v3 offset:768\n" \
  "ds_read_b64 v[74:75], v3 offset:776\nds_read_b64 v[68:69], v3 offset:832\nds_read_b64 v[70:71], v3 offset:840\n" \
  "ds_read_b64 v[64:65], v3 offset:896\nds_read_b64 v[66:67], v3 offset:904\nds_read_b64 v[60:61], v3 offset:960\n" \
  "ds_read_b64 v[62:63], v3 offset:968\nds_read_b64 v[56:57], v3 offset:1024\nds_read_b64 v[58:59], v3 offset:1032\n" \
  "ds_read_b64 v[52:53], v6 offset:0\nds_read_b64 v[54:55], v6 offset:8\nds_read_b64 v[48:49], v6 offset:64\nds_read_b64 v[50:51], v6 offset:72\n" \
  "ds_read_b64 v[44:45], v6 offset:128\nds_read_b64 v[46:47], v6 offset:136\nds_read_b64 v[40:41], v6 offset:192\n" \
  "ds_read_b64 v[42:43], v6 offset:200\ns_waitcnt lgkmcnt(0)\ns_barrier\nds_read_b128 v[24:27], v2 offset:448\nds_read_b128 v[28:31], v2 offset:960\n" \
  "ds_read_b128 v[32:35], v2 offset:1472\nds_read_b128 v[36:39], v2 offset:1536\ns_waitcnt lgkmcnt(3)\n" \
  "v_mfma_f32_16x16x32_f16 v[12:15], v[124:127], v[24:27], 0\ns_setprio 1\nds_read_b128 v[24:27], v2 offset:1600\ns_waitcnt lgkmcnt(3)\n" \
  "v_mfma_f32_16x16x32_f16 v[16:19], v[124:127], v[28:31], 0\nds_read_b128 v[124:127], v2 offset:1664\ns_waitcnt lgkmcnt(3)\n" \
  "v_mfma_f32_16x16x32_f16 v[12:15], v[120:123], v[32:35], v[12:15]\nds_read_b128 v[28:31], v2 offset:1728\ns_waitcnt lgkmcnt(3)\n" \
  "v_mfma_f32_16x16x32_f16 v[12:15], v[116:119], v[36:39], v[12:15]\nds_read_b128 v[32:35], v2 offset:1792\ns_waitcnt lgkmcnt(3)\n" \
  "v_mfma_f32_16x16x32_f16 v[12:15], v[112:115], v[24:27], v[12:15]\nds_read_b128 v[36:39], v2 offset:1856\ns_waitcnt lgkmcnt(3)\n" \
  "v_mfma_f32_16x16x32_f16 v[12:15], v[108:111], v[124:127], v[12:15]\nds_read_b128 v[24:27], v2 offset:1920\ns_waitcnt lgkmcnt(3)\n" \
  "v_mfma_f32_16x16x32_f16 v[12:15], v[104:107], v[28:31], v[12:15]\nds_read_b128 v[124:127], v2 offset:1984\ns_waitcnt lgkmcnt(3)\n" \
  "v_mfma_f32_16x16x32_f16 v[12:15], v[100:103], v[32:35], v[12:15]\nds_read_b128 v[28:31], v2 offset:2048\ns_waitcnt lgkmcnt(3)\n" \
  "v_mfma_f32_16x16x32_f16 v[12:15], v[96:99], v[36:39], v[12:15]\nds_read_b128 v[32:35], v2 offset:2112\ns_waitcnt lgkmcnt(3)\n" \
  "v_mfma_f32_16x16x32_f16 v[12:15], v[92:95], v[24:27], v[12:15]\nds_read_b128 v[36:39], v2 offset:2176\ns_waitcnt lgkmcnt(3)\n" \
  "v_mfma_f32_16x16x32_f16 v[12:15], v[88:91], v[124:127], v[12:15]\nds_read_b128 v[24:27], v2 offset:2240\n" \
  "v_mfma_f32_16x16x32_f16 v[16:19], v[120:123], v[124:127], v[16:19]\nds_read_b64 v[120:121], v6 offset:256\nds_read_b64 v[122:123], v6 offset:264\n" \
  "s_waitcnt lgkmcnt(4)\nv_mfma_f32_16x16x32_f16 v[12:15], v[84:87], v[28:31], v[12:15]\nds_read_b128 v[124:127], v2 offset:2304\n" \
  "v_mfma_f32_16x16x32_f16 v[16:19], v[116:119], v[28:31], v[16:19]\nds_read_b64 v[116:117], v6 offset:320\nds_read_b64 v[118:119], v6 offset:328\n" \
  "v_mfma_f32_16x16x32_f16 v[12:15], v[80:83], v[32:35], v[12:15]\nds_read_b128 v[28:31], v2 offset:2368\n" \
  "v_mfma_f32_16x16x32_f16 v[16:19], v[112:115], v[32:35], v[16:19]\ns_waitcnt lgkmcnt(6)\n" \
  "v_mfma_f32_16x16x32_f16 v[12:15], v[76:79], v[36:39], v[12:15]\nds_read_b128 v[112:115], v2 offset:2432\n" \
  "v_mfma_f32_16x16x32_f16 v[16:19], v[108:111], v[36:39], v[16:19]\nds_read2_b32 v[32:33], v8 offset0:48 offset1:49\n" \
  "ds_read2_b32 v[34:35], v8 offset0:50 offset1:51\nv_mfma_f32_16x16x32_f16 v[12:15], v[72:75], v[24:27], v[12:15]\n" \
  "ds_read_b128 v[108:111], v2 offset:2496\nv_mfma_f32_16x16x32_f16 v[16:19], v[104:107], v[24:27], v[16:19]\n" \
  "ds_read2_b32 v[36:37], v8 offset0:32 offset1:33\nds_read2_b32 v[38:39], v8 offset0:34 offset1:35\ns_waitcnt lgkmcnt(6)\n" \
  "v_mfma_f32_16x16x32_f16 v[12:15], v[68:71], v[124:127], v[12:15]\nds_read_b128 v[104:107], v2 offset:2560\n" \
  "v_mfma_f32_16x16x32_f16 v[16:19], v[100:103], v[124:127], v[16:19]\nds_read_b128 v[24:27], v2 offset:2624\n" \
  "v_mfma_f32_16x16x32_f16 v[12:15], v[64:67], v[28:31], v[12:15]\nds_read_b128 v[100:103], v2 offset:2688\nds_read_b32 v23, v9 offset:32\n" \
  "v_mfma_f32_16x16x32_f16 v[16:19], v[96:99], v[28:31], v[16:19]\nds_read_b128 v[124:127], v2 offset:2752\ns_waitcnt lgkmcnt(7)\n" \
  "v_mfma_f32_16x16x32_f16 v[12:15], v[60:63], v[112:115], v[12:15]\nds_read_b128 v[96:99], v2 offset:2816\n" \
  "v_mfma_f32_16x16x32_f16 v[16:19], v[92:95], v[112:115], v[16:19]\nds_read_b128 v[28:31], v2 offset:2880\n" \
  "v_mfma_f32_16x16x32_f16 v[12:15], v[56:59], v[108:111], v[12:15]\nds_read_b128 v[92:95], v2 offset:2944\n" \
  "v_mfma_f32_16x16x32_f16 v[16:19], v[88:91], v[108:111], v[16:19]\nds_read_b128 v[112:115], v2 offset:3008\ns_waitcnt lgkmcnt(5)\n" \
  "v_readfirstlane_b32 s85, v23\ns_cmp_lt_u32 s85, 3\ns_cbranch_scc1 61f\n62:\nv_mfma_f32_16x16x32_f16 v[16:19], v[84:87], v[104:107], v[16:19]\n" \
  "ds_read_b64_tr_b16 v[88:89], v5 offset:0\nds_read_b64_tr_b16 v[90:91], v5 offset:32\n" \
  "v_mfma_f32_16x16x32_f16 v[16:19], v[80:83], v[24:27], v[16:19]\nds_read_b64_tr_b16 v[108:109], v5 offset:256\n" \
  "ds_read_b64_tr_b16 v[110:111], v5 offset:288\nv_mfma_f32_16x16x32_f16 v[16:19], v[76:79], v[100:103], v[16:19]\n" \
  "ds_read_b64_tr_b16 v[84:85], v5 offset:512\nds_read_b64_tr_b16 v[86:87], v5 offset:544\ns_waitcnt lgkmcnt(9)\n" \
  "v_mfma_f32_16x16x32_f16 v[16:19], v[72:75], v[124:127], v[16:19]\nds_read_b64_tr_b16 v[104:105], v5 offset:768\n" \
  "ds_read_b64_tr_b16 v[106:107], v5 offset:800\nv_mfma_f32_16x16x32_f16 v[16:19], v[68:71], v[96:99], v[16:19]\n" \
  "ds_read_b64_tr_b16 v[80:81], v5 offset:1024\nds_read_b64_tr_b16 v[82:83], v5 offset:1056\ns_waitcnt lgkmcnt(11)\n" \
  "v_mfma_f32_16x16x32_f16 v[16:19], v[64:67], v[28:31], v[16:19]\nds_read_b64_tr_b16 v[24:25], v5 offset:1280\n" \
  "ds_read_b64_tr_b16 v[26:27], v5 offset:1312\nv_mfma_f32_16x16x32_f16 v[16:19], v[60:63], v[92:95], v[16:19]\n" \
  "ds_read_b64_tr_b16 v[76:77], v5 offset:1536\nds_read_b64_tr_b16 v[78:79], v5 offset:1568\ns_waitcnt lgkmcnt(12)\n" \
  "v_mfma_f32_16x16x32_f16 v[16:19], v[56:59], v[112:115], v[16:19]\nds_read_b64_tr_b16 v[100:101], v5 offset:1792\n" \
  "ds_read_b64_tr_b16 v[102:103], v5 offset:1824\nv_mfma_f32_16x16x32_f16 v[12:15], v[88:91], v[52:55], v[12:15]\ns_waitcnt lgkmcnt(10)\n" \
  "v_mfma_f32_16x16x32_f16 v[12:15], v[108:111], v[48:51], v[12:15]\nds_read_b64_tr_b16 v[72:73], v7 offset:0\n" \
  "ds_read_b64_tr_b16 v[74:75], v7 offset:64\nv_mfma_f32_16x16x32_f16 v[12:15], v[84:87], v[44:47], v[12:15]\n" \
  "ds_read_b64_tr_b16 v[124:125], v7 offset:512\nds_read_b64_tr_b16 v[126:127], v7 offset:576\n" \
  "v_mfma_f32_16x16x32_f16 v[16:19], v[84:87], v[52:55], v[16:19]\ns_waitcnt lgkmcnt(10)\n" \
  "v_mfma_f32_16x16x32_f16 v[12:15], v[104:107], v[40:43], v[12:15]\nv_mfma_f32_16x16x32_f16 v[16:19], v[104:107], v[48:51], v[16:19]\n" \
  "v_mfma_f32_16x16x32_f16 v[12:15], v[80:83], v[120:123], v[12:15]\nv_mfma_f32_16x16x32_f16 v[16:19], v[80:83], v[44:47], v[16:19]\n" \
  "s_waitcnt lgkmcnt(6)\nv_mfma_f32_16x16x32_f16 v[12:15], v[24:27], v[116:119], v[12:15]\n" \
  "v_mfma_f32_16x16x32_f16 v[16:19], v[24:27], v[40:43], v[16:19]\nv_mfma_f32_16x16x32_f16 v[16:19], v[76:79], v[120:123], v[16:19]\n" \
  "s_waitcnt lgkmcnt(2)\nv_mfma_f32_16x16x32_f16 v[16:19], v[100:103], v[116:119], v[16:19]\n" \
  "v_mfma_f32_16x16x32_f16 v[12:15], v[72:75], v[32:35], v[12:15]\nv_mfma_f32_16x16x32_f16 v[16:19], v[72:75], v[36:39], v[16:19]\n" \
  "s_waitcnt lgkmcnt(0)\nv_mfma_f32_16x16x32_f16 v[16:19], v[124:127], v[32:35], v[16:19]\ns_nop 4\nv_cvt_pkrtz_f16_f32 v20, v12, v13\n" \
  "v_cvt_pkrtz_f16_f32 v21, v14, v15\nv_pk_add_u16 v20, v20, 4 op_sel_hi:[1,0] clamp\nv_pk_add_u16 v21, v21, 4 op_sel_hi:[1,0] clamp\n" \
  "ds_write_b64 v4, v[20:21] offset:4096\nv_cvt_pkrtz_f16_f32 v20, v16, v17\nv_cvt_pkrtz_f16_f32 v21, v18, v19\n" \
  "v_pk_add_u16 v20, v20, 4 op_sel_hi:[1,0] clamp\nv_pk_add_u16 v21, v21, 4 op_sel_hi:[1,0] clamp\nds_write_b64 v4, v[20:21] offset:5120\n" \
  "s_setprio 0\ns_setpc_b64 s[90:91]\n61:\ns_movk_i32 s84, 0x4000\n63:\nds_read_b32 v23, v9 offset:32\ns_sub_u32 s84, s84, 1\ns_cbranch_scc1 62b\n" \
  "s_waitcnt lgkmcnt(0)\nv_readfirstlane_b32 s85, v23\ns_cmp_lt_u32 s85, 3\ns_cbranch_scc1 63b\ns_branch 62b\n5:\n" \
  "ds_read_b64 v[124:125], v3 offset:1088\nds_read_b64 v[126:127], v3 offset:1096\nds_read_b64 v[120:121], v3 offset:0\n" \
  "ds_read_b64 v[122:123], v3 offset:8\nds_read_b64 v[116:117], v3 offset:64\nds_read_b64 v[118:119], v3 offset:72\n" \
  "ds_read_b64 v[112:113], v3 offset:128\nds_read_b64 v[114:115], v3 offset:136\nds_read_b64 v[108:109], v3 offset:192\n" \
  "ds_read_b64 v[110:111], v3 offset:200\nds_read_b64 v[104:105], v3 offset:256\nds_read_b64 v[106:107], v3 offset:264\n" \
  "ds_read_b64 v[100:101], v3 offset:320\nds_read_b64 v[102:103], v3 offset:328\nds_read_b64 v[96:97], v3 offset:384\n" \
  "ds_read_b64 v[98:99], v3 offset:392\nds_read_b64 v[92:93], v3 offset:448\nds_read_b64 v[94:95], v3 offset:456\n" \
  "ds_read_b64 v[88:89], v3 offset:512\nds_read_b64 v[90:91], v3 offset:520\nds_read_b64 v[84:85], v3 offset:576\n" \
  "ds_read_b64 v[86:87], v3 offset:584\nds_read_b64 v[80:81], v3 offset:640\nds_read_b64 v[82:83], v3 offset:648\n" \
  "ds_read_b64 v[76:77], v3 offset:704\nds_read_b64 v[78:79], v3 offset:712\nds_read_b64 v[72:73], v3 offset:768\n" \
  "ds_read_b64 v[74:75], v3 offset:776\nds_read_b64 v[68:69], v3 offset:832\nds_read_b64 v[70:71], v3 offset:840\n" \
  "ds_read_b64 v[64:65], v3 offset:896\nds_read_b64 v[66:67], v3 offset:904\nds_read_b64 v[60:61], v3 offset:960\n" \
  "ds_read_b64 v[62:63], v3 offset:968\nds_read_b64 v[56:57], v3 offset:1024\nds_read_b64 v[58:59], v3 offset:1032\n" \
  "ds_read_b64 v[52:53], v6 offset:0\nds_read_b64 v[54:55], v6 offset:8\nds_read_b64 v[48:49], v6 offset:64\nds_read_b64 v[50:51], v6 offset:72\n" \
  "ds_read_b64 v[44:45], v6 offset:128\nds_read_b64 v[46:47], v6 offset:136\nds_read_b64 v[40:41], v6 offset:192\n" \
  "ds_read_b64 v[42:43], v6 offset:200\ns_waitcnt lgkmcnt(0)\ns_barrier\nds_read_b128 v[24:27], v2 offset:1472\n" \
  "ds_read_b128 v[28:31], v2 offset:1984\nds_read_b128 v[32:35], v2 offset:2496\nds_read_b128 v[36:39], v2 offset:2560\ns_waitcnt lgkmcnt(3)\n" \
  "v_mfma_f32_16x16x32_f16 v[12:15], v[124:127], v[24:27], 0\ns_setprio 1\nds_read_b128 v[24:27], v2 offset:2624\ns_waitcnt lgkmcnt(3)\n" \
  "v_mfma_f32_16x16x32_f16 v[16:19], v[124:127], v[28:31], 0\nds_read_b128 v[124:127], v2 offset:2688\ns_waitcnt lgkmcnt(3)\n" \
  "v_mfma_f32_16x16x32_f16 v[12:15], v[120:123], v[32:35], v[12:15]\nds_read_b128 v[28:31], v2 offset:2752\ns_waitcnt lgkmcnt(3)\n" \
  "v_mfma_f32_16x16x32_f16 v[12:15], v[116:119], v[36:39], v[12:15]\nds_read_b128 v[32:35], v2 offset:2816\ns_waitcnt lgkmcnt(3)\n" \
  "v_mfma_f32_16x16x32_f16 v[12:15], v[112:115], v[24:27], v[12:15]\nds_read_b128 v[36:39], v2 offset:2880\ns_waitcnt lgkmcnt(3)\n" \
  "v_mfma_f32_16x16x32_f16 v[12:15], v[108:111], v[124:127], v[12:15]\nds_read_b128 v[24:27], v2 offset:2944\ns_waitcnt lgkmcnt(3)\n" \
  "v_mfma_f32_16x16x32_f16 v[12:15], v[104:107], v[28:31], v[12:15]\nds_read_b128 v[124:127], v2 offset:3008\ns_waitcnt lgkmcnt(3)\n" \
  "v_mfma_f32_16x16x32_f16 v[12:15], v[100:103], v[32:35], v[12:15]\nds_read_b128 v[28:31], v2 offset:3072\ns_waitcnt lgkmcnt(3)\n" \
  "v_mfma_f32_16x16x32_f16 v[12:15], v[96:99], v[36:39], v[12:15]\nds_read_b128 v[32:35], v2 offset:3136\ns_waitcnt lgkmcnt(3)\n" \
  "v_mfma_f32_16x16x32_f16 v[12:15], v[92:95], v[24:27], v[12:15]\nds_read_b128 v[36:39], v2 offset:3200\ns_waitcnt lgkmcnt(3)\n" \
  "v_mfma_f32_16x16x32_f16 v[12:15], v[88:91], v[124:127], v[12:15]\nds_read_b128 v[24:27], v2 offset:3264\n" \
  "v_mfma_f32_16x16x32_f16 v[16:19], v[120:123], v[124:127], v[16:19]\nds_read_b64 v[120:121], v6 offset:256\nds_read_b64 v[122:123], v6 offset:264\n" \
  "s_waitcnt lgkmcnt(4)\nv_mfma_f32_16x16x32_f16 v[12:15], v[84:87], v[28:31], v[12:15]\nds_read_b128 v[124:127], v2 offset:3328\n" \
  "v_mfma_f32_16x16x32_f16 v[16:19], v[116:119], v[28:31], v[16:19]\nds_read_b64 v[116:117], v6 offset:320\nds_read_b64 v[118:119], v6 offset:328\n" \
  "v_mfma_f32_16x16x32_f16 v[12:15], v[80:83], v[32:35], v[12:15]\nds_read_b128 v[28:31], v2 offset:3392\n" \
  "v_mfma_f32_16x16x32_f16 v[16:19], v[112:115], v[32:35], v[16:19]\ns_waitcnt lgkmcnt(6)\n" \
  "v_mfma_f32_16x16x32_f16 v[12:15], v[76:79], v[36:39], v[12:15]\nds_read_b128 v[112:115], v2 offset:3456\n" \
  "v_mfma_f32_16x16x32_f16 v[16:19], v[108:111], v[36:39], v[16:19]\nds_read2_b32 v[32:33], v8 offset0:16 offset1:17\n" \
  "ds_read2_b32 v[34:35], v8 offset0:18 offset1:19\nv_mfma_f32_16x16x32_f16 v[12:15], v[72:75], v[24:27], v[12:15]\n" \
  "ds_read_b128 v[108:111], v2 offset:3520\nv_mfma_f32_16x16x32_f16 v[16:19], v[104:107], v[24:27], v[16:19]\n" \
  "ds_read2_b32 v[36:37], v8 offset0:0 offset1:1\nds_read2_b32 v[38:39], v8 offset0:2 offset1:3\ns_waitcnt lgkmcnt(6)\n" \
  "v_mfma_f32_16x16x32_f16 v[12:15], v[68:71], v[124:127], v[12:15]\nds_read_b128 v[104:107], v2 offset:3584\n" \
  "v_mfma_f32_16x16x32_f16 v[16:19], v[100:103], v[124:127], v[16:19]\nds_read_b128 v[24:27], v2 offset:3648\n" \
  "ds_read2_b32 v[100:101], v8 offset0:32 offset1:33\nds_read2_b32 v[102:103], v8 offset0:34 offset1:35\n" \
  "v_mfma_f32_16x16x32_f16 v[12:15], v[64:67], v[28:31], v[12:15]\nds_read_b128 v[124:127], v2 offset:3712\nds_read_b32 v23, v9 offset:32\n" \
  "v_mfma_f32_16x16x32_f16 v[16:19], v[96:99], v[28:31], v[16:19]\nds_read_b128 v[96:99], v2 offset:3776\ns_waitcnt lgkmcnt(9)\n" \
  "v_mfma_f32_16x16x32_f16 v[12:15], v[60:63], v[112:115], v[12:15]\nds_read_b128 v[28:31], v2 offset:3840\n" \
  "v_mfma_f32_16x16x32_f16 v[16:19], v[92:95], v[112:115], v[16:19]\nds_read_b128 v[92:95], v2 offset:3904\n" \
  "v_mfma_f32_16x16x32_f16 v[12:15], v[56:59], v[108:111], v[12:15]\nds_read_b128 v[112:115], v2 offset:3968\n" \
  "v_mfma_f32_16x16x32_f16 v[16:19], v[88:91], v[108:111], v[16:19]\nds_read_b128 v[88:91], v2 offset:4032\ns_waitcnt lgkmcnt(5)\n" \
  "v_readfirstlane_b32 s85, v23\ns_cmp_lt_u32 s85, 3\ns_cbranch_scc1 71f\n72:\nv_mfma_f32_16x16x32_f16 v[16:19], v[84:87], v[104:107], v[16:19]\n" \
  "ds_read_b64_tr_b16 v[108:109], v5 offset:1024\nds_read_b64_tr_b16 v[110:111], v5 offset:1056\n" \
  "v_mfma_f32_16x16x32_f16 v[16:19], v[80:83], v[24:27], v[16:19]\nds_read_b64_tr_b16 v[84:85], v5 offset:1280\n" \
  "ds_read_b64_tr_b16 v[86:87], v5 offset:1312\nv_mfma_f32_16x16x32_f16 v[16:19], v[76:79], v[124:127], v[16:19]\n" \
  "ds_read_b64_tr_b16 v[104:105], v5 offset:1536\nds_read_b64_tr_b16 v[106:107], v5 offset:1568\ns_waitcnt lgkmcnt(9)\n" \
  "v_mfma_f32_16x16x32_f16 v[16:19], v[72:75], v[96:99], v[16:19]\nds_read_b64_tr_b16 v[80:81], v5 offset:1792\n" \
  "ds_read_b64_tr_b16 v[82:83], v5 offset:1824\nv_mfma_f32_16x16x32_f16 v[16:19], v[68:71], v[28:31], v[16:19]\n" \
  "ds_read_b64_tr_b16 v[24:25], v5 offset:2048\nds_read_b64_tr_b16 v[26:27], v5 offset:2080\ns_waitcnt lgkmcnt(11)\n" \
  "v_mfma_f32_16x16x32_f16 v[16:19], v[64:67], v[92:95], v[16:19]\nds_read_b64_tr_b16 v[76:77], v5 offset:2304\n" \
  "ds_read_b64_tr_b16 v[78:79], v5 offset:2336\nv_mfma_f32_16x16x32_f16 v[16:19], v[60:63], v[112:115], v[16:19]\n" \
  "ds_read_b64_tr_b16 v[124:125], v5 offset:2560\nds_read_b64_tr_b16 v[126:127], v5 offset:2592\ns_waitcnt lgkmcnt(12)\n" \
  "v_mfma_f32_16x16x32_f16 v[16:19], v[56:59], v[88:91], v[16:19]\nds_read_b64_tr_b16 v[72:73], v5 offset:2816\n" \
  "ds_read_b64_tr_b16 v[74:75], v5 offset:2848\nv_mfma_f32_16x16x32_f16 v[12:15], v[108:111], v[52:55], v[12:15]\ns_waitcnt lgkmcnt(10)\n" \
  "v_mfma_f32_16x16x32_f16 v[12:15], v[84:87], v[48:51], v[12:15]\nds_read_b64_tr_b16 v[96:97], v7 offset:0\n" \
  "ds_read_b64_tr_b16 v[98:99], v7 offset:64\nv_mfma_f32_16x16x32_f16 v[12:15], v[104:107], v[44:47], v[12:15]\n" \
  "ds_read_b64_tr_b16 v[68:69], v7 offset:512\nds_read_b64_tr_b16 v[70:71], v7 offset:576\n" \
  "v_mfma_f32_16x16x32_f16 v[16:19], v[104:107], v[52:55], v[16:19]\ns_waitcnt lgkmcnt(10)\n" \
  "v_mfma_f32_16x16x32_f16 v[12:15], v[80:83], v[40:43], v[12:15]\nds_read_b64_tr_b16 v[28:29], v7 offset:1024\n" \
  "ds_read_b64_tr_b16 v[30:31], v7 offset:1088\nv_mfma_f32_16x16x32_f16 v[16:19], v[80:83], v[48:51], v[16:19]\n" \
  "ds_read2_b32 v[64:65], v8 offset0:48 offset1:49\nds_read2_b32 v[66:67], v8 offset0:50 offset1:51\n" \
  "v_mfma_f32_16x16x32_f16 v[12:15], v[24:27], v[120:123], v[12:15]\nv_mfma_f32_16x16x32_f16 v[16:19], v[24:27], v[44:47], v[16:19]\n" \
  "s_waitcnt lgkmcnt(10)\nv_mfma_f32_16x16x32_f16 v[12:15], v[76:79], v[116:119], v[12:15]\nds_read_b64_tr_b16 v[92:93], v7 offset:1536\n" \
  "ds_read_b64_tr_b16 v[94:95], v7 offset:1600\nv_mfma_f32_16x16x32_f16 v[16:19], v[76:79], v[40:43], v[16:19]\n" \
  "v_mfma_f32_16x16x32_f16 v[16:19], v[124:127], v[120:123], v[16:19]\ns_waitcnt lgkmcnt(8)\n" \
  "v_mfma_f32_16x16x32_f16 v[16:19], v[72:75], v[116:119], v[16:19]\nv_mfma_f32_16x16x32_f16 v[12:15], v[96:99], v[32:35], v[12:15]\n" \
  "v_mfma_f32_16x16x32_f16 v[16:19], v[96:99], v[36:39], v[16:19]\ns_waitcnt lgkmcnt(4)\n" \
  "v_mfma_f32_16x16x32_f16 v[12:15], v[68:71], v[100:103], v[12:15]\nv_mfma_f32_16x16x32_f16 v[16:19], v[68:71], v[32:35], v[16:19]\n" \
  "s_waitcnt lgkmcnt(2)\nv_mfma_f32_16x16x32_f16 v[12:15], v[28:31], v[64:67], v[12:15]\n" \
  "v_mfma_f32_16x16x32_f16 v[16:19], v[28:31], v[100:103], v[16:19]\ns_waitcnt lgkmcnt(0)\n" \
  "v_mfma_f32_16x16x32_f16 v[16:19], v[92:95], v[64:67], v[16:19]\ns_nop 4\nv_cvt_pkrtz_f16_f32 v20, v12, v13\nv_cvt_pkrtz_f16_f32 v21, v14, v15\n" \
  "v_pk_add_u16 v20, v20, 4 op_sel_hi:[1,0] clamp\nv_pk_add_u16 v21, v21, 4 op_sel_hi:[1,0] clamp\nds_write_b64 v4, v[20:21] offset:6144\n" \
  "v_cvt_pkrtz_f16_f32 v20, v16, v17\nv_cvt_pkrtz_f16_f32 v21, v18, v19\nv_pk_add_u16 v20, v20, 4 op_sel_hi:[1,0] clamp\n" \
  "v_pk_add_u16 v21, v21, 4 op_sel_hi:[1,0] clamp\nds_write_b64 v4, v[20:21] offset:7168\ns_setprio 0\ns_setpc_b64 s[90:91]\n71:\n" \
  "s_movk_i32 s84, 0x4000\n73:\nds_read_b32 v23, v9 offset:32\ns_sub_u32 s84, s84, 1\ns_cbranch_scc1 72b\ns_waitcnt lgkmcnt(0)\n" \
  "v_readfirstlane_b32 s85, v23\ns_cmp_lt_u32 s85, 3\ns_cbranch_scc1 73b\ns_branch 72b\n"
#define CONV_CLOB "memory", "scc", "s84", "s85", "s90", "s91", "v12", "v13", "v14", "v15", "v16", "v17", "v18", "v19", "v20", "v21", "v22", "v23", "v24", "v25", "v26", "v27", "v28", "v29", "v30", "v31", "v32", "v33", "v34", "v35", "v36", "v37", "v38", "v39", "v40", "v41", "v42", "v43", "v44", "v45", "v46", "v47", "v48", "v49", "v50", "v51", "v52", "v53", "v54", "v55", "v56", "v57", "v58", "v59", "v60", "v61", "v62", "v63", "v64", "v65", "v66", "v67", "v68", "v69", "v70", "v71", "v72", "v73", "v74", "v75", "v76", "v77", "v78", "v79", "v80", "v81", "v82", "v83", "v84", "v85", "v86", "v87", "v88", "v89", "v90", "v91", "v92", "v93", "v94", "v95", "v96", "v97", "v98", "v99", "v100", "v101", "v102", "v103", "v104", "v105", "v106", "v107", "v108", "v109", "v110", "v111", "v112", "v113", "v114", "v115", "v116", "v117", "v118", "v119", "v120", "v121", "v122", "v123", "v124", "v125", "v126", "v127"
constexpr int TDP = 1216, T_3 = 4 * TDP, T_4 = T_3 + 512, TB = T_4 + 320;
constexpr int C_U = 480, UB = C_U + 4096 + 96, C_G5 = 0, C_G3 = 3200, C_G9 = 6400, C_G7 = C_G9 + 2096, GB = C_G7 + 2080 + 112;
constexpr int O_FL = 2 * TB, O_UL = O_FL + 64, O_IM = O_UL + 2 * UB, O_ST = O_IM + 2 * GB;
constexpr int X_TB = 10240, X_UL = X_TB + TB, X_IM = X_UL + UB, X_ST = X_IM + GB;
constexpr int X_WR = X_IM + 128 * IP;
#define X_RW(w) (LW + 9216 * (2 + ((w) >> 1)) + 2560 * ((w) & 1))
constexpr int RP = 1216;
constexpr int LW = 36864 + 512, LA = LW, LM = LW + 36864;
constexpr int L_SC = LM + 2048, SMEM = O_IM + 256 * IP;
static_assert(L_SC + 1152 <= SMEM && O_ST + 8192 <= SMEM && X_IM + 128 * IP <= LM && X_ST + 8192 <= LM && UB % 128 == 64 && GB % 128 == 64 && SMEM <= 79872, "");
struct TOps { uint32_t xa, xb, ya, pd, pf, pg, pe; };
__device__ __forceinline__ void table_ops(uint32_t im, uint32_t tb, int ct, int lane, TOps& t) {
  const int j = lane & 15, kq = lane >> 4, q4 = (lane >> 2) & 3, p4 = lane & 3, jj = j + 16 * ct;
  const uint32_t sa = im + (uint32_t)(IP * (lane >> 2));
  t.xa = sa + (uint32_t)(I_X + 8 * p4); t.xb = sa + (uint32_t)(I_X + 32 + 8 * (p4 & 1)); t.ya = sa + (uint32_t)(32 * ct + 8 * p4);
  t.pd = tb + (uint32_t)(2 * (541 - (8 * jj + 4 * (kq & 1) + 256 * (kq >> 1))));
  t.pf = tb + (uint32_t)((kq & 1) ? T_4 + 2 * (63 - jj) : T_3 + 2 * (127 - 2 * jj));
  t.pg = t.pf - ((kq & 1) ? 64 : 2);
  t.pe = tb + (uint32_t)(p4 * (TDP + 2) + ((lane < 12) ? 2 * (32 - q4) : 2 * (572 + (q4 & 1))));
  asm volatile("" : "+v"(t.xa), "+v"(t.xb), "+v"(t.ya), "+v"(t.pd), "+v"(t.pf), "+v"(t.pg), "+v"(t.pe));
}
typedef __attribute__((address_space(3))) char* lptr;
#define LP(T, a) ((__attribute__((address_space(3))) T*)(uintptr_t)(a))
__device__ __forceinline__ uint32_t table_tile(const TOps& t, int ct, int lane, uint32_t evp, float dsk TSP) {
  u32x4 ev;
  f32x4 ad[2], af[2];
  asm volatile(T_ASM : "=&v"(ad[0]), "=&v"(ad[1]), "=&v"(af[0]), "=&v"(af[1]), "=&v"(ev) : "v"(t.xa), "v"(t.xb), "v"(t.ya), "v"(evp) : T_CLOB);
  const int e0 = (int)(ev[0] + ev[2]), e1 = (int)(ev[1] + ev[3]);
  const int em = min(max(max(max(e0, e1), (int)((__float_as_uint(dsk) >> 23) & 0xffu) - 133), -110), 115);
  const int ts = ((uint32_t)(em + 12) <= 8u) ? 1 : -3 - em;
  float s0 = __builtin_amdgcn_ldexpf(1.f, e0 - 29 + ts), s1 = __builtin_amdgcn_ldexpf(1.f, e1 - 29 + ts);
  float d0 = (ct == 0 && lane == 0) ? __builtin_amdgcn_ldexpf(dsk, ts) : 0.f;
  asm volatile("s_nop 1" : "+v"(ad[0]), "+v"(ad[1]), "+v"(af[0]), "+v"(af[1]), "+v"(s0), "+v"(s1), "+v"(d0));
  const f32x4 k = ad[0] * s0 + (f32x4){d0, 0.f, 0.f, 0.f} + ad[1] * s1, f = af[0] * s0 + af[1] * s1;
  auto pk = [](float a, float b) { uint32_t r; asm("v_cvt_pk_f16_f32 %0, %1, %2" : "=v"(r) : "v"(a), "v"(b)); return r; };
  asm volatile("s_nop 0" : : "v"(k), "v"(f));
  const uint32_t p32 = pk(k[3], k[2]), p10 = pk(k[1], k[0]), p21 = pk(k[2], k[1]), p01 = pk(f[0], f[1]), p23 = pk(f[2], f[3]);
  *LP(u32x2, t.pd + 3 * TDP + 6) = (u32x2){p32, p10};
  *LP(uint32_t, t.pd + TDP + 2) = p32; *LP(uint32_t, t.pd + TDP + 6) = p10;
  *LP(uint32_t, t.pd + 2) = p21; *LP(uint32_t, t.pd + 2 * TDP + 6) = p21;
  *LP(u16, t.pd) = (u16)p32; *LP(u16, t.pd + 2 * TDP + 4) = (u16)p32;
  asm volatile("ds_write_b16_d16_hi %0, %1 offset:6\n\tds_write_b16_d16_hi %0, %1 offset:%2" : : "v"(t.pd), "v"(p10), "n"(2 * TDP + 10) : "memory");
  if (ct) {
    const uint32_t va = (uint32_t)__builtin_amdgcn_readlane((int)p32, 63), vb = (uint32_t)__builtin_amdgcn_readlane((int)p23, 15) >> 16;
    *LP(u16, t.pe) = (u16)((lane < 12) ? va : vb);
  }
  if (lane < 32) {
    *LP(u16, t.pf + 128) = (u16)p01; *LP(u16, t.pg + 128) = (u16)(p01 >> 16);
    *LP(u16, t.pf) = (u16)p23; *LP(u16, t.pg) = (u16)(p23 >> 16);
  }
  return (uint32_t)(127 + 5 - ts) << 23;
}
__device__ __forceinline__ uint32_t hadd(uint32_t a, uint32_t b) { return __builtin_bit_cast(uint32_t, __builtin_bit_cast(f16x2, a) + __builtin_bit_cast(f16x2, b)); }
template <bool FULL> __device__ __forceinline__ void sums(char* sm, const u32x4 p, const u32x4 c) {
  const uint32_t D[8] = {p[0], p[1], p[2], p[3], c[0], c[1], c[2], c[3]};
  uint32_t O[7];
#pragma unroll
  for (int j = 0; j < 7; ++j) O[j] = __builtin_amdgcn_alignbit(D[j + 1], D[j], 16);
  u32x4 a, b;
#pragma unroll
  for (int j = 0; j < 4; ++j) a[j] = hadd(D[j + 2], hadd(O[j + 1], O[j + 2]));
  *(u32x4*)(sm + C_G3 + 32) = a;
#pragma unroll
  for (int j = 0; j < 4; ++j) b[j] = hadd(a[j], hadd(D[j + 1], D[j + 3]));
  *(u32x4*)(sm + C_G5 + 32) = b;
  if (FULL) {
#pragma unroll
    for (int j = 0; j < 4; ++j) a[j] = hadd(b[j], hadd(O[j], O[j + 3]));
    *(u32x4*)(sm + C_G7 + 32) = a;
#pragma unroll
    for (int j = 0; j < 4; ++j) b[j] = hadd(a[j], hadd(D[j], D[j + 4]));
    *(u32x4*)(sm + C_G9 + 32) = b;
  }
}
__device__ __forceinline__ void count_up(uint32_t a) {
  uint32_t t;
  asm volatile("s_mov_b64 exec, 1\n\tv_mov_b32 %0, 1\n\tds_add_u32 %1, %0\n\ts_mov_b64 exec, -1" : "=&v"(t) : "v"(a) : "memory");
}
__device__ __forceinline__ int pad_at(int r) { return 16 * (r & 1) + 3200 * (r >> 1) - ((r >= 6) ? 3200 - 2096 : 0); }
struct COps { uint32_t b, a, o, g3, k3, g4, k4, f; };
__device__ __forceinline__ void conv_ops(uint32_t tb, uint32_t ub, uint32_t cb, int lane, COps& e) {
  const uint32_t j = lane & 15, kq = lane >> 4, q4 = (lane >> 2) & 3, p4 = lane & 3;
  e.b = ub + 32u * j + 16u * kq;
  e.a = tb + p4 * TDP + 32u - 8u * q4 + 16u * kq;
  e.g3 = cb + C_G5 + 8 + 64u * kq + 8u * (q4 + p4) + (q4 & 1u) * 3200u;
  e.g4 = cb + C_G9 + 8 + 128u * kq + 16u * q4 + 8u * p4 + (q4 & 1u) * 2096u;
  e.k3 = tb + T_3 + 120 - 8u * j + 16u * kq;
  e.k4 = tb + T_4 + 60 - 4u * j + 16u * kq;
  asm volatile("" : "+v"(e.b), "+v"(e.a), "+v"(e.g3), "+v"(e.g4), "+v"(e.k3), "+v"(e.k4));
}
#define CONV_RUN(prog, e, ...) do { int prog_ = (prog); \
    asm volatile("s_call_b64 s[90:91], kf_conv" : "+{s89}"(prog_), "+{v2}"(e.b), "+{v3}"(e.a), "+{v4}"(e.o), "+{v5}"(e.g3), "+{v6}"(e.k3), "+{v7}"(e.g4), \
                 "+{v8}"(e.k4), "+{v9}"(e.f) __VA_ARGS__ : : CONV_CLOB); } while (0)

template <bool E>
__device__ __forceinline__ void ln_stats(const uint32_t* v, int lane, float sc, f32x2* t, f32x4 z, f32x2& rc, f32x2& nc, float& e) {
  float s1a = z[0], s2a = z[1], s1b = z[2], s2b = z[3];
#pragma unroll
  for (int k = 0; k < 4; ++k) {
    s1a = dot2(v[k], 0x3f803f80u, s1a); s2a = dot2(v[k], v[k], s2a);
    s1b = dot2(v[4 + k], 0x3f803f80u, s1b); s2b = dot2(v[4 + k], v[4 + k], s2b);
  }
#pragma unroll
  for (int k = 0; k < 9; ++k) t[k] = (f32x2){bflo(v[k]), bfhi(v[k])};
  const float rs = row_sum(fold16(dot2(v[8], 0x3f803f80u, fold32(s1a, s1b)), dot2(v[8], v[8], fold32(s2a, s2b))));
  if (E) { e = row_sum(e); e += __int_as_float(__builtin_amdgcn_update_dpp(0, __float_as_int(e), 0x142, 0xa, 0xf, false)); }
  const f32x2 mean = (f32x2){rdl(rs, 15), rdl(rs, 47)} * (1.f / D_), m2 = mean * mean;
  f32x2 var = (f32x2){rdl(rs, 31), rdl(rs, 63)} * (1.f / D_) - m2;
  asm volatile("" : "+v"(t[0]), "+v"(t[1]), "+v"(t[2]), "+v"(t[3]), "+v"(t[4]), "+v"(t[5]), "+v"(t[6]), "+v"(t[7]), "+v"(t[8]));
  if (__builtin_expect(!(m2[0] <= 16.f * var[0] && m2[1] <= 16.f * var[1]), 0)) {
    float q[3] = {0.f, 0.f, 0.f};
#pragma unroll
    for (int k = 0; k < 9; ++k) {
      const f32x2 a = t[k] - ((k < 4 || (k == 8 && lane < 32)) ? mean[0] : mean[1]);
      q[k >> 2] += a[0] * a[0] + a[1] * a[1];
    }
    float qa = q[0] + ((lane < 32) ? q[2] : 0.f), qb = q[1] + ((lane < 32) ? 0.f : q[2]);
    wave_sum2(qa, qb);
    var = (f32x2){qa, qb} * (1.f / D_);
  }
  const uint32_t mp = pack2(mean[0], mean[1]), vp = pack2(var[0], var[1]);
  const f32x2 ve = (f32x2){bflo(vp), bfhi(vp)} + 1e-5f;
  const uint32_t e2 = pack2(ve[0], ve[1]);
  const uint32_t sp = pack2(__builtin_amdgcn_sqrtf(bflo(e2)), __builtin_amdgcn_sqrtf(bfhi(e2)));
  rc = (f32x2){__builtin_amdgcn_rcpf(bflo(sp)), __builtin_amdgcn_rcpf(bfhi(sp))} * sc;
  nc = -(f32x2){bflo(mp), bfhi(mp)} * rc;
}
template <bool H>
__device__ __forceinline__ void ln_apply(const f32x2* t, int lane, f32x2 rc, f32x2 nc, uint32_t* c) {
  const float nm[3] = {nc[0], nc[1], (lane < 32) ? nc[0] : nc[1]}, rr[3] = {rc[0], rc[1], (lane < 32) ? rc[0] : rc[1]};
  f32x2 dd[9];
#pragma unroll
  for (int k = 0; k < 9; ++k) dd[k] = __builtin_elementwise_fma(t[k], (f32x2){rr[k >> 2], rr[k >> 2]}, (f32x2){nm[k >> 2], nm[k >> 2]});
  asm volatile("" : "+v"(dd[0]), "+v"(dd[1]), "+v"(dd[2]), "+v"(dd[3]), "+v"(dd[4]), "+v"(dd[5]), "+v"(dd[6]), "+v"(dd[7]), "+v"(dd[8]));
#pragma unroll
  for (int k = 0; k < 9; ++k) {
    const f32x2 d = dd[k];
    typedef __attribute__((ext_vector_type(2))) uint16_t u16x2;
    if (H) c[k] = __builtin_bit_cast(uint32_t, __builtin_elementwise_add_sat(__builtin_bit_cast(u16x2, __builtin_amdgcn_cvt_pkrtz(d[0], d[1])), (u16x2){4, 4}));
    else c[k] = pack2(d[0], d[1]);
  }
}
__device__ __forceinline__ uint32_t med3u(uint32_t a, uint32_t b, uint32_t c) { return max(min(a, b), min(max(a, b), c)); }
__device__ __forceinline__ uint32_t oct_maxu(uint32_t v) {
  v = max(v, (uint32_t)DPP_I((int)v, 0xb1, 0xf));
  v = max(v, (uint32_t)DPP_I((int)v, 0x4e, 0xf));
  v = max(v, (uint32_t)DPP_I((int)v, 0x141, 0xf));
  return v;
}

#define KARGS const u16* __restrict__ x, const u16* __restrict__ Are, const u16* __restrict__ Aim, const u16* __restrict__ Bre, \
    const u16* __restrict__ Bim, const u16* __restrict__ Cre, const u16* __restrict__ Cim, const u16* __restrict__ logdt, \
    const u16* __restrict__ Dskip, char* __restrict__ ws, const u16* __restrict__ wr, const u16* __restrict__ wg, \
    const u16* __restrict__ wu, const u16* __restrict__ wd, u16* __restrict__ out
#define KPASS x, Are, Aim, Bre, Bim, Cre, Cim, logdt, Dskip, ws, wr, wg, wu, wd, out
template <int ROLE, int FFN = -1>
__device__ __forceinline__ void role_body(char* smem, KARGS) {
  const int tid = threadIdx.x, lane = tid & 63;
  const int w = __builtin_amdgcn_readfirstlane(tid >> 6), wl = w & 3;
  const int b = blockIdx.x & 255, bx = b & 7, bj = b >> 3;
  constexpr bool isl = ROLE == 1 || ROLE == 2;
  const int tg = isl ? 32 * (bj >> 2) + 4 * bx + (bj & 3) : 32 * bx + bj;
  u32x4 xa, xc;
  uint32_t ep, r0;
#define P_WAIT(n, ...) asm volatile("s_waitcnt vmcnt(" #n ")\n\tv_lshlrev_b32 %0, 16, v100\n\tv_lshlrev_b32 %1, 16, v101\n\tv_lshlrev_b32 %2, 16, v102" \
    : "=v"(ldt), "=v"(are), "=v"(aim) : : "v100", "v101", "v102" __VA_ARGS__)
#define P_MORE() asm volatile("v_lshlrev_b32 %0, 16, v103\n\tv_lshlrev_b32 %1, 16, v104\n\tv_lshlrev_b32 %2, 16, v105\n\tv_lshlrev_b32 %3, 16, v106" \
    : "=v"(bre), "=v"(bim), "=v"(cre), "=v"(cim) : : "v103", "v104", "v105", "v106")
  TSD;
  TS(0, 0);
  const uint32_t lb0 = (uint32_t)(uintptr_t)(__attribute__((address_space(3))) char*)smem;
  const int i = lane & 15, kq = lane >> 4;
  const u32x4 ones = {0xffffffffu, 0xffffffffu, 0xffffffffu, 0xffffffffu}, zero4 = {0u, 0u, 0u, 0u};
  auto u_again = [&](u32x4& uw, const u32x4& ut, uint32_t uo) {
#pragma unroll 1
    for (int spin = 0; spin < SPIN_CAP; ++spin) {
      const bool na = ((uw[0] | uw[1] | uw[2] | uw[3]) & UMASK) != 0u;
      if (__builtin_expect(!__any(na), 1)) break;
      TSC(19);
      SLEEP(USLEEP);
      if (na) { LD16W(uw, uo); uw ^= ut; }
    }
  };
  constexpr uint32_t S16 = 0x41800000u, LMASK = 0x00070007u;
  auto v_put = [&](u32x4 v, const u32x4& ut, bool other, uint32_t vo) {
    const bool second = __any(other || ((v[0] | v[1] | v[2] | v[3]) & 0x40004000u) != 0u);
    if (__builtin_expect(second, 0)) v = (v & ~LMASK) | (~ut & LMASK); else v = (v & ~LMASK) | ut;
    ST16(vo, v);
    return second;
  };
  auto v_end = [&](uint32_t T, int n) { DRAIN(); if (lane == 0) ST4(O_DN + 16u * n, T); };
  if constexpr (!isl) {
    const int ch = wl >> 1, tl = tid & 255;
    int* evp = (int*)(smem + O_FL + 16 * ch);
    float ldt, are, aim, dsk;
    if constexpr (ROLE == 0) {
      __builtin_amdgcn_s_setprio(KPRIO);
      COps e;
      conv_ops(lb0 + (uint32_t)(ch * TB), lb0 + (uint32_t)(O_UL + ch * UB), lb0 + (uint32_t)(O_IM + ch * GB), lane, e);
      e.o = lb0 + O_ST + (uint32_t)(16 * ((4 * i + 2 * (kq >> 1) + ch) ^ (i >> 1)) + 8 * (kq & 1));
      e.f = lb0 + O_FL;
      TOps t;
      table_ops(lb0 + (uint32_t)(O_IM + 128 * IP * ch), lb0 + (uint32_t)(TB * ch), wl & 1, lane, t);
      uint32_t pz = lb0 + O_IM + (uint32_t)(((lane >> 3) & 1) * GB + pad_at(lane & 7)), im = lb0 + (uint32_t)(O_IM + IP * tl);
      asm volatile("" : "+v"(e.o), "+v"(e.f), "+v"(pz), "+v"(im));
      float bre, bim, cre, cim;
      P_WAIT(0, , "v103", "v104", "v105", "v106", "v107", "v109");
      asm volatile("" : "={v109}"(ep));
      const uint32_t T = ((uint32_t)__builtin_amdgcn_readfirstlane(ep) + 1u) * 0x9E3779B1u;
      if (w == 3 && lane < 8) ST16(O_KEY + 128u * b + 16u * lane, ones);
      P_MORE();
      asm volatile("v_lshlrev_b32 %0, 16, v107" : "=v"(dsk) : : "v107");
      TS(0, 1);
      evp[wl & 1] = gen_y(ldt, are, aim, bre, bim, cre, cim, im, [] {} TSA);
      TS(0, 8);
      BAR();
      TS(0, 2);
      const uint32_t cv = table_tile(t, wl & 1, lane, lb0 + O_FL + 16 * ch, dsk TSA), cs = (uint32_t)__builtin_amdgcn_readfirstlane(cv);
      *(uint32_t*)(smem + O_FL + 40 + 4 * ch) = cv;
      TS(0, 9);
      gsync0(lb0 + O_FL + 36, 4);
      TS(0, 3);
      *(u32x4*)(smem + O_ST + 16 * tl) = ones;
      *(u32x4*)(smem + O_ST + 4096 + 16 * tl) = ones;
      if (w == 0) *LP(u32x4, pz) = zero4;
      DRAIN();
      CONV_RUN(wl & 1, e);
      if (wl & 1) asm volatile("s_mov_b64 exec, 1\n\tglobal_store_dwordx2 %0, %1, %2 sc1" : : "v"(O_SC + 16u * (2 * tg + ch)), "v"((u32x2){T, cs}), "s"(ws) : "memory");
      TS(0, 5);
    } else {
      __builtin_amdgcn_s_setprio(UPRIO);
      const uint32_t ck0 = (uint32_t)__builtin_amdgcn_s_memtime(), rt0 = (uint32_t)__builtin_amdgcn_s_memrealtime();
#pragma unroll
      for (int k = 0; k < 3; ++k) *(u32x4*)(smem + ((16 * (tl + 256 * k) < O_FL) ? 16 * (tl + 256 * k) : O_FL + 32)) = zero4;
      if (w == 7) *(u32x4*)(smem + O_UL + (lane >> 5) * UB + 16 * min(lane & 31, 29)) = zero4;
      uint32_t uo = u_at(tl >> 1, 2 * tg + (tid & 1)), go = O_SU + (uint32_t)(256 * bx + 32 * wl + (lane & 31) + 4 * (lane & 32)) * 128u;
      uint32_t ul = (uint32_t)(O_UL + (tid & 1) * UB + C_U - 16 + 16 * (tl >> 1)), ug = (uint32_t)(O_IM + (tid & 1) * GB + 16 * (tl >> 1)), so = lb0 + O_ST + 16u * (uint32_t)(lane ^ (lane >> 3));
      uint32_t vo = O_V + (uint32_t)tg * VP + 16u * lane;
      asm volatile("" : "+v"(uo), "+v"(go), "+v"(ul), "+v"(ug), "+v"(so), "+v"(vo));
      P_WAIT(1, , "v109");
      TS(0, 1);
      evp[2 + (wl & 1)] = gen_x(ldt, are, aim, smem + O_IM + IP * tl);
      asm volatile("s_waitcnt vmcnt(0)" : "={v109}"(ep));
      const uint32_t T = ((uint32_t)__builtin_amdgcn_readfirstlane(ep) + 1u) * 0x9E3779B1u;
      TS(0, 8);
      BAR();
      TS(0, 2);
      if (w == 7 && bj >= 24) {
        const uint32_t t_ = ahead(ck0, rt0, VDB);
        ST16(O_SB + (uint32_t)(64 * (lane >> 3) + 8 * bx + bj - 24) * 128u + 16u * (lane & 7), ((u32x4){T, t_, T, t_}));
      }
      u32x4 ut = {utag(T, 0), utag(T, 1), utag(T, 2), utag(T, 3)};
      asm volatile("" : "+v"(ut));
      SLEEP(GSLEEP);
      u32x2 pv;
      pv[1] = gate(ws, go, T);
      TS(0, 3);
      until(latest(pv[1]) + UD);
      TS(0, 7);
      u32x4 uw, ux;
      asm volatile("global_load_dwordx4 %0, %2, %4 sc1\n\tglobal_load_dwordx4 %1, %3, %4 sc1"
                   : "=&v"(uw), "=&v"(ux) : "v"(uo), "v"(uo + 64 * UP * 32), "s"(ws) : "memory");
      __builtin_amdgcn_s_setprio(FPR);
      asm volatile("s_waitcnt vmcnt(1)" : "+v"(uw));
      uw ^= ut;
      *(u32x4*)(smem + ul + 16) = uw;
      asm volatile("s_waitcnt vmcnt(0)" : "+v"(ux));
      TS(0, 11);
      ux ^= ut;
      *(u32x4*)(smem + ul + 2048 + 16) = ux;
      if (__builtin_expect(__any(((uw[0] | uw[1] | uw[2] | uw[3] | ux[0] | ux[1] | ux[2] | ux[3]) & UMASK) != 0u), 0)) {
        u_again(uw, ut, uo);
        u_again(ux, ut, uo + 64 * UP * 32);
        *(u32x4*)(smem + ul + 16) = uw;
        *(u32x4*)(smem + ul + 2048 + 16) = ux;
      }
      BAR();
      TS(0, 4);
      const u32x4 pw = *(const u32x4*)(smem + ul);
      if (wl < 2) {
        const u32x4 px = *(const u32x4*)(smem + ul + 2048);
        sums<true>(smem + ug, pw, uw);
        sums<false>(smem + ug + 2048, px, ux);
      } else sums<true>(smem + ug, pw, uw);
      count_up(lb0 + O_FL + 32);
      __builtin_amdgcn_s_setprio(SPR);
      TS(0, 5);
      const bool other = *(const uint32_t*)(smem + O_FL + 40 + 4 * (lane & 1)) != S16;
      bool second = false;
#pragma unroll
      for (int k = 0; k < 2; ++k) {
        const uint32_t o = ((k ? 0x3247u : 0x1065u) >> (4 * wl) & 7u) * 1024u;
        u32x4 v;
#pragma unroll 1
        for (int spin = 0; spin < SPIN_CAP; ++spin) {
          asm volatile("ds_read_b128 %0, %1\n\ts_waitcnt lgkmcnt(0)" : "=&v"(v) : "v"(so + o) : "memory");
          if (!__any(max4(v) == 0xffffffffu)) break;
          __builtin_amdgcn_s_sleep(VPS);
        }
        TS(0, 12 + k);
        second |= v_put(v, ut, other, vo + o);
        TS(0, 10 - 4 * k);
      }
      if (__builtin_expect(second, 0)) v_end(T, 4 * tg + wl);
    }
    TSE();
  } else {
  uint32_t* cand = (uint32_t*)(smem + LM);
  uint32_t* sel = cand + 128;
  uint32_t* kl = sel + 48;
  uint32_t* cnt = sel + 64;
  const uint32_t ca = lb0 + LM + 4 * (128 + 64);
  u16* hm = (u16*)(cnt + 16);
  uint32_t T;
  const bool ffn = (FFN < 0) ? bj < 18 : FFN, isx = FFN != 1 && bj >= 24;
  const int fb = 18 * bx + bj;
  constexpr bool ln2 = ROLE == 2;
  const int g0 = tg, g2 = tg;
  u32x4 bw[18];
#pragma unroll
  for (int s = 0; s < 9; ++s) bw[s] = (u32x4){0u, 0u, 0u, 0u};
  int tid2 = threadIdx.x;
  uint32_t tv = 0u, tk = 0u;
  auto v_look = [&](uint32_t tu, int lane) {
    uint32_t tm = latest(gate(ws, O_SB + (uint32_t)(64 * bx + lane) * 128u, T)) - V2D;
    if ((int)(tm - (tu + UD + VD2)) < 0) tm = tu + UD + VD2;
    cnt[8] = tm;
    count_up(ca + 52);
  };
  auto v_time = [&] { gwait(ca + 52, 1); tv = (uint32_t)__builtin_amdgcn_readfirstlane(cnt[8]); };
  uint32_t ts1 = 0u;
  auto rest = [&] {
    if (ts1 && (int)((uint32_t)__builtin_amdgcn_s_memrealtime() - ts1) > 0) {
      v_time();
      ts1 = (tv - TSTOP) | 1u;
      if ((int)((uint32_t)__builtin_amdgcn_s_memrealtime() - ts1) > 0) { TS(0, 8); until(tv + RD); ts1 = 0u; }
    }
  };
  auto gu_dma = [&](int q0, int q1, auto depth) {
    const char* gs = (const char*)((w < 2) ? wg : wu) + (size_t)((144 * (w & 1)) * (F_ * 2) + fb * 64);
    const uint32_t gv = (uint32_t)((lane >> 2) * (F_ * 2) + 16 * ((lane & 3) ^ (lane >> 5) ^ (w & 2)));
#pragma unroll 1
    for (int q = q0; q < q1; ++q) {
      const int k = (int)((((bj & 1) ? ORD1 : ((unsigned __int128)0x876543210u << 36 | 0x876543210ull)) >> (4 * (q - (q >= 9 ? 9 : 0)) + (q >= 9 ? 36 : 0))) & 15u), h = q >= 9;
      if (q >= RQ) rest();
      dma16(gs + (size_t)(288 * h + 16 * k) * (F_ * 2), gv, lb0 + LW * h + 9216 * w + 1024 * k);
      if (!BALL || q < RQ || ts1) asm volatile("s_waitcnt vmcnt(%0)" : : "n"(depth.value) : "memory");
    }
  };
  auto down_dma = [&](int k0, int k1, int lane) {
#pragma unroll 1
    for (int k = k0; k < k1; ++k) {
      const int o = 9216 * wl + 2304 * k;
      const char* ds = (const char*)wd + (size_t)(fb * 36864 + o);
      const uint32_t dl = lb0 + o + 32 * ((4 * wl + k + 1) >> 1);
      if (ROLE == 1) rest();
      dma16d(ds, 16u * lane, dl);
      dma16d(ds + 1024, 16u * lane, dl + 1024);
      uint32_t keep;
      asm volatile("s_mov_b32 %0, m0\n\ts_mov_b32 m0, %3\n\ts_mov_b64 exec, 0xffff\n\tglobal_load_lds_dwordx4 %1, %2" DPOL2 "\n\ts_mov_b64 exec, -1\n\ts_mov_b32 m0, %0\n\ts_waitcnt vmcnt(%4)"
                   : "=&s"(keep) : "v"(16u * lane), "s"(ds + 2048), "s"(dl + 2048), "n"(ROLE == 1 ? DDEPTH : 40) : "memory");
    }
  };
  auto gu_read = [&](int h) {
    const uint32_t ra = lb0 + LW * h + (uint32_t)(((lane >> 1) & 1) * 18432 + (8 * kq + ((lane >> 2) & 3)) * 64 + ((w ^ (kq & 1) ^ (lane & 2)) * 16) + 8 * (lane & 1));
#pragma unroll
    for (int s = 0; s < 9; ++s) {
      const u32x2 h0 = trd(ra + 2048 * s), h1 = trd(ra + 2048 * s + 256);
      bw[9 * h + s] = (u32x4){h0[0], h0[1], h1[0], h1[1]};
    }
#pragma unroll
    for (int s = 0; s < 9; ++s) asm volatile("" : "+v"(bw[9 * h + s]));
  };
  const uint32_t vearly = (uint32_t)((VEARLY >> (8 * (g0 >> 5))) & 0xffull);
#define TOUCH(pa) asm volatile("global_load_dword v126, %0, off" DPOL4 : : "v"(pa) : "memory", "v126")
  uint32_t wv[5], xv[9];
  auto rows_xw = [&](int lane) {
    u32x4 wa, xr, xs;
    const uint32_t xo = (uint32_t)((8 * g2 + 2 * wl) * (D_ * 2));
    asm volatile("global_load_dwordx4 %0, %5, %9\n\tglobal_load_dword %1, %6, %9 offset:1024\n\t"
                 "global_load_dwordx4 %2, %7, %10\n\tglobal_load_dwordx4 %3, %7, %10 offset:1152\n\tglobal_load_dword %4, %8, %10 offset:1024\n\ts_waitcnt vmcnt(0)"
                 : "=&v"(wa), "=&v"(wv[4]), "=&v"(xr), "=&v"(xs), "=&v"(xv[8])
                 : "v"(16u * lane), "v"(4u * (lane & 31)), "v"(xo + 16u * lane), "v"(xo + 4u * lane + 32u * (lane & 32)), "s"(wr), "s"(x) : "memory");
#pragma unroll
    for (int k = 0; k < 4; ++k) { wv[k] = wa[k]; xv[k] = xr[k]; xv[4 + k] = xs[k]; }
  };
  auto ln1 = [&](int g, int o, uint32_t c0) {
    uint32_t uo = u_at(g, 64 * wl + lane);
    uint32_t ta = lb0 + (uint32_t)(o + ((lane >> 2) & 3) * RP + (64 * wl + 16 * kq + 4 * (lane & 3)) * 2);
    uint32_t la = (uint32_t)(o + 2 * wl * RP + 16 * lane), lt = (uint32_t)(o + (2 * wl + (lane >> 5)) * RP + 1024 + 4 * (lane & 31));
    uint32_t sg = O_SU + (uint32_t)(256 * (lane >> 3) + g) * 128u + 16u * (lane & 7);
    asm volatile("" : "+v"(uo), "+v"(ta), "+v"(la), "+v"(lt), "+v"(sg));
    f32x4 z4 = {0.f, 0.f, 0.f, 0.f};
    asm volatile("" : "+v"(z4));
    asm volatile("s_waitcnt vmcnt(0)" : "={v[100:103]}"(xa), "={v[104:107]}"(xc), "={v108}"(r0), "={v109}"(ep));
    TS(0, 1);
    const uint32_t v[9] = {xa[0], xa[1], xa[2], xa[3], xc[0], xc[1], xc[2], xc[3], r0};
    uint32_t c[9];
    f32x2 tf[9];
    T = ((uint32_t)__builtin_amdgcn_readfirstlane(ep) + 1u) * 0x9E3779B1u;
    if (wl == 3) { const uint32_t now = (uint32_t)__builtin_amdgcn_s_memrealtime() + SD; ST16(sg, ((u32x4){T, now, T, now})); }
    const uint32_t tj = ((T >> (3 * wl)) & 0x00070007u) | (((T >> (12 + wl)) & 0x00010001u) << 14);
    uint32_t tw[3] = {(tj & 0xffffu) * 0x10001u, (tj >> 16) * 0x10001u, 0u};
    tw[2] = (lane < 32) ? tw[0] : tw[1];
    asm volatile("" : "+v"(tw[0]), "+v"(tw[1]), "+v"(tw[2]));
    f32x2 rc, nc;
    float e_;
    ln_stats<false>(v, lane, 0.03125f, tf, z4, rc, nc, e_);
    ln_apply<true>(tf, lane, rc, nc, c);
#pragma unroll
    for (int k = 0; k < 9; ++k) c[k] = (c[k] & ~UMASK) | tw[k >> 2];
    *(u32x4*)(smem + la) = (u32x4){c[0], c[1], c[2], c[3]};
    *(u32x4*)(smem + la + RP) = (u32x4){c[4], c[5], c[6], c[7]};
    *(uint32_t*)(smem + lt) = c[8];
    u32x4 ha, hb;
    {
      uint32_t t, cc, n = SPIN_CAP;
      asm volatile("s_mov_b64 exec, 1\n\tv_mov_b32 %2, 1\n\tds_add_u32 %5, %2\n\ts_mov_b64 exec, -1\n"
                   "1:\n\tds_read_b32 %2, %5\n\tds_read_b64_tr_b16 v[40:41], %6\n\tds_read_b64_tr_b16 v[42:43], %6 offset:%7\n\tds_read_b64_tr_b16 v[44:45], %6 offset:512\n\t"
                   "ds_read_b64_tr_b16 v[46:47], %6 offset:%8\n\ts_sub_u32 %4, %4, 1\n\ts_cbranch_scc1 2f\n\ts_waitcnt lgkmcnt(4)\n\tv_readfirstlane_b32 %3, %2\n\t"
                   "s_cmp_lt_u32 %3, 4\n\ts_cbranch_scc1 1b\n2:\n\ts_waitcnt lgkmcnt(0)"
                   : "=&{v[40:43]}"(ha), "=&{v[44:47]}"(hb), "=&v"(t), "=&s"(cc), "+s"(n) : "v"(c0), "v"(ta), "n"(4 * RP), "n"(512 + 4 * RP) : "memory", "scc");
    }
    ST16(uo, ha);
    ST16(uo + 64 * 128, hb);
    if (wl == 0) {
      const u32x2 h4 = trd(ta + 1024), h5 = trd(ta + 1024 + 4 * RP);
      ST16(uo + 128 * 128, ((u32x4){h4[0], h4[1], h5[0], h5[1]}));
    }
  };
  const int dX = 512 - 24 + 8 * bx + bj;
  if constexpr (ROLE == 2) {
    const int tl = tid - 256;
    if (isx) {
      __builtin_amdgcn_s_setprio(QPRIO0);
      *(u32x4*)(smem + X_TB + 16 * tl) = zero4;
      *(u32x4*)(smem + X_TB + 16 * min(256 + tl, (TB + C_U) / 16 - 1)) = zero4;
      const int pr = (wl + XROT) & 3;
      COps e;
      conv_ops(lb0 + X_TB, lb0 + X_UL, lb0 + X_IM, lane, e);
      TOps t;
      table_ops(lb0 + X_IM, lb0 + X_TB, wl & 1, lane, t);
      e.o = lb0 + X_ST + (uint32_t)(16 * ((2 * i + (kq >> 1)) ^ ((i >> 2) & 1)) + 8 * (kq & 1));
      e.f = ca + 28 - 32;
      asm volatile("" : "+v"(e.o), "+v"(e.f));
      float ldt, are, aim, bre, bim, cre, cim, dsk;
      P_WAIT(0, , "v103", "v104", "v105", "v106", "v107", "v109");
      asm volatile("" : "={v109}"(ep));
      T = ((uint32_t)__builtin_amdgcn_readfirstlane(ep) + 1u) * 0x9E3779B1u;
      P_MORE();
      asm volatile("v_lshlrev_b32 %0, 16, v107" : "=v"(dsk) : : "v107");
      TS(0, 1);
      __builtin_amdgcn_s_setprio(QPRIO);
      int* evp = (int*)(sel + 44);
      char* im = smem + X_IM + IP * (tl & 127);
      evp[wl] = (wl < 2) ? gen_y(ldt, are, aim, bre, bim, cre, cim, lb0 + X_IM + IP * tl, [] {} TSA) : gen_x(ldt, are, aim, im);
      TS(0, 2);
      __builtin_amdgcn_s_setprio(QPRIO2);
      gsync(ca + 8, 4);
      if (wl < 2) evp[-4 + wl] = (int)table_tile(t, wl & 1, lane, lb0 + LM + 4 * (128 + 44), dsk TSA);
      if (wl < 2) gsync0(ca + 8, 8); else { count_up(ca + 8); SLEEP(XSL); gwait(ca + 8, 8); }
      const uint32_t cs = (uint32_t)__builtin_amdgcn_readfirstlane(evp[-4]);
      if (wl == 3) *(u32x4*)(smem + X_IM + pad_at(lane & 7)) = zero4;
      TS(0, 3);
      CONV_RUN(2 + pr, e, , "+{v11}"(tid2));
      __builtin_amdgcn_s_setprio(3);
      {
        const int lane = tid2 & 63;
        const u32x4 v = *(const u32x4*)(smem + X_ST + 2048 * pr + 1024 * (lane >> 5) + 16 * ((lane & 31) ^ ((lane >> 3) & 1)));
        if (__builtin_expect(v_put(v, (u32x4){utag(T, 0), utag(T, 1), utag(T, 2), utag(T, 3)}, cs != S16, v_at(dX, 64 * pr + lane)), 0)) {
          DRAIN();
          if (lane == 0) ST4(O_DN + 16u * (4 * (dX - 256) + pr), T);
        }
        if (pr == 0 && lane == 0) asm volatile("global_store_dwordx2 %0, %1, %2 sc1" : : "v"(O_SC + 16u * dX), "v"((u32x2){T, cs}), "s"(ws) : "memory");
      }
      TS(0, 6);
    }
    if (!isx) __builtin_amdgcn_s_setprio(PPRIO);
    asm volatile("" : "+v"(tid2));
    if (!isx) rows_xw(tid2 & 63);
    if (!isx && !ffn) {
      SLEEP(RSLEEP);
#pragma unroll
      for (int k = 0; k < 7; ++k) ST16(O_SL + (uint32_t)((6 * bx + bj - 18) * 1792 + 256 * k - 256 + tid2) * 16u, ones);
      DRAIN();
    }
    if (!isx) BAR();
    const uint32_t tp = (uint32_t)__builtin_amdgcn_readfirstlane(cnt[4]);
    if (!isx) T = (uint32_t)__builtin_amdgcn_readfirstlane(sel[43]);
    if (!isx && w == 4) v_look(tp, tid2 & 63);
    v_time();
  } else {
    __builtin_amdgcn_s_setprio(3);
    if (tid < 16) cnt[tid] = 0u;
    BAR();
    if (tid < 20) ((uint32_t*)(hm + 320))[tid] = 0u;
    if (tid >= 184) *(u32x4*)(smem + L_SC - 16 * 184 + 16 * tid) = (u32x4){0x41804180u, 0x41804180u, 0x41804180u, 0x41804180u};
    uint32_t sink = 0;
    ln1(tg, 0, ca + 12);
    TS(0, 2);
    __builtin_amdgcn_s_setprio(NPRIO);
    asm volatile("s_waitcnt vmcnt(0)" : "+v"(sink) : : "memory");
    TS(0, 13);
    if (w == 0) {
      const uint32_t xr = O_XC + (uint32_t)((1021 - 4 * g0) * 72 + lane) * 16u;
      ST16(xr, ones);
      ST16(xr + 1024u, ones);
      ST16(xr + 2048u, ones);
      if (lane < 24) ST16(xr + 3072u, ones);
    }
    if (w == 2 && b < 72 && lane < 8) ST16(O_FB + (uint32_t)(8 * b + lane) * 16u, ones);
    if (isx) {
      rows_xw(lane);
      char* sp = smem + X_RW(w) + 16 * lane;
      *(u32x4*)sp = (u32x4){xv[0], xv[1], xv[2], xv[3]};
      *(u32x4*)(sp + 1024) = (u32x4){xv[4], xv[5], xv[6], xv[7]};
      *(u32x2*)(smem + X_RW(w) + 2048 + 8 * lane) = (u32x2){xv[8], wv[4]};
      if (w == 0) *(u32x4*)(smem + X_WR + 16 * lane) = (u32x4){wv[0], wv[1], wv[2], wv[3]};
    }
    SLEEP(ESLEEP);
    uint32_t tu = latest(gate(ws, O_SU + (uint32_t)(256 * bx + (ALLP ? tid : 64 * w + 4 * i + (bj & 3))) * 128u, T));
    asm volatile("s_mov_b64 exec, 1\n\tds_max_u32 %0, %1\n\ts_mov_b64 exec, -1" : : "v"(ca + 16), "v"(tu) : "memory");
    if (w == 0) sel[43] = T;
    if (isx) {
      const uint32_t xo = u_at(tid, dX);
      char* sm = smem + X_UL + C_U - 16 + 16 * tid;
      const u32x4 ut = {utag(T, 0), utag(T, 1), utag(T, 2), utag(T, 3)};
      __builtin_amdgcn_s_setprio(3);
      until(tu + UD + XD);
      u32x4 uw;
      asm volatile("global_load_dwordx4 %0, %1, %2 sc1" : "=&v"(uw) : "v"(xo), "s"(ws) : "memory");
      asm volatile("s_waitcnt vmcnt(0)" : "+v"(uw));
      uw ^= ut;
      u_again(uw, ut, xo);
      *(u32x4*)(sm + 16) = uw;
      BAR();
      if (w < 3) {
        const u32x4 pw = *(const u32x4*)sm;
        if (w < 2) sums<true>(smem + X_IM + 16 * tid, pw, uw); else sums<false>(smem + X_IM + 16 * tid, pw, uw);
        count_up(ca + 28);
      }
    } else {
      until(tu + UD + WD);
      BAR();
    }
    tu = (uint32_t)__builtin_amdgcn_readfirstlane(cnt[4]);
    TS(0, 3);
    __builtin_amdgcn_s_setprio(DPRIO);
    if (ffn) {
      ts1 = (tu + UD + VD2 - TSTOP) | 1u;
      gu_dma(0, N0, IC<40>{});
      gu_dma(N0, NA, IC<WDEPTH>{});
      asm volatile("s_waitcnt vmcnt(%0)" : : "n"(NA - 9) : "memory");
      gsync0(ca + 4, 4);
      gu_read(0);
      asm volatile("s_waitcnt lgkmcnt(0)" ::: "memory");
      count_up(ca + 36);
      TS(0, 4);
      gu_dma(NA, 18, IC<WDEPTH2>{});
      gwait(ca + 36, 4);
      down_dma(0, KN, lane);
      asm volatile("s_waitcnt vmcnt(%0)" : : "n"(3 * KN) : "memory");
      count_up(ca + 4);
      if (STP && w == 1)
        asm volatile("global_load_dword v126, %0, %1 sc1" : : "v"(O_SC + 1024u * (fb >> 4) + 128u * (lane & 7)), "s"(ws) : "memory", "v126");
      TS(0, 7);
    } else {
      if (!isx && C3) {
        const int n = 6 * min(tid, 191) + bj - 18, r = n >> 2;
        const char* wb = (const char*)((n & 1) ? wu : wg) + bx * 1152;
        const int c0 = (3 - (int)((uintptr_t)wb >> 7)) & 7;
        wb += (uint32_t)((r + 144 * ((r >= 144) + ((n >> 1) & 1))) * (F_ * 2) + 128 * c0);
        TOUCH(wb);
        if (c0 == 0) TOUCH(wb + 1024);
      }
      auto d_line = [&](int k) {
        const int n = min((k * 14 + bj - 18) * 64 + lane, 5183), q = n / 1296, r = n - 1296 * q;
        return (const char*)wd + (uint32_t)(bx * 5184 + (r / 18) * 72 + 18 * (HREV ? 3 - q : q) + r % 18) * 128u;
      };
      if (isx && w == 3) v_look(tu, lane);
      int k = w;
      if (w < HW) {
#pragma unroll 1
        for (; k < 6 && (int)((uint32_t)__builtin_amdgcn_s_memrealtime() - (tu + UD + VD2 - HSTOP)) < 0; k += HW) { TOUCH(d_line(k)); DRAIN(); }
      }
      TS(0, 4);
      v_time();
      until(tv + RD2);
      if (w < HW) {
#pragma unroll 1
        for (; k < 6; k += HW) { TOUCH(d_line(k)); DRAIN(); }
      }
      TS(0, 7);
    }
    v_time();
    tk = tv + KD;
  }
  {
  const int tid = tid2, lane = tid & 63, i = lane & 15, kq = lane >> 4;
  __builtin_amdgcn_s_setprio(3);
  const int PL = ffn ? 6400 : LW + 6656;
  f32x2 xf[9];
  f32x4 za = {0.f, 0.f, 0.f, 0.f}, zb = za;
  uint32_t al, ha, h0a, h1a, so, da;
  auto ffn_terms = [&] {
    al = O_XC + 64u * (i >> 3) + 16u * kq + 128u * w;
    ha = (ROLE == 1) ? (uint32_t)((4 * (kq & 1) + 2 * (kq >> 1) + (i >> 3)) * 80 + 16 * w + 2 * (i & 7)) : 0u;
    h0a = (uint32_t)(min(i, 8) * 80 + 16 * kq); h1a = (uint32_t)(min(i ^ 8, 8) * 80 + 16 * kq);
    so = O_SL + (uint32_t)fb * SP + ((ROLE == 2) ? 16u * tid - 4096u : 8192u + 16u * lane);
    da = lb0 + (uint32_t)((8 * kq + ((lane >> 2) & 3)) * 1152 + 32 * ((8 * kq + ((lane >> 2) & 3) + 2) >> 2) + 8 * (lane & 3) + ((ROLE == 2) ? 128 * wl : 1024));
    if (ROLE == 2) asm volatile("" : "+v"(h0a), "+v"(h1a), "+v"(so), "+v"(da)); else asm volatile("" : "+v"(ha), "+v"(al), "+v"(da));
  };
  if (ROLE == 1) ffn_terms();
  uint32_t hv[9] = {}, nv[9] = {}, keya = 0u, keyb = 0u;
  int rka = 0, rkb = 0;
  auto put_row = [&](const uint32_t* r, int j, const void* base, uint32_t ro) {
    const u32x4 tl = {r[8], (uint32_t)DPP_I((int)r[8], 0x55, 0xf), (uint32_t)DPP_I((int)r[8], 0xaa, 0xf), (uint32_t)DPP_I((int)r[8], 0xff, 0xf)};
    asm volatile("global_store_dwordx4 %0, %1, %2 sc1\n\ts_nop 1" : : "v"(ro + 16u * lane), "v"((u32x4){r[4 * j], r[4 * j + 1], r[4 * j + 2], r[4 * j + 3]}), "s"(base) : "memory");
    if ((lane & 35) == 32 * j) asm volatile("global_store_dwordx4 %0, %1, %2 offset:1024 sc1\n\ts_nop 1" : : "v"(ro + 4u * (lane & 31)), "v"(tl), "s"(base) : "memory");
  };
  if (ln2) {
    const int tl = tid - 256, d1 = 64 * (tl >> 5) + (tl & 31), d3 = 64 * ((tl >> 4) & 7) + 32 + (tl & 15), d2 = (tl < 128) ? d3 + 16 : min(384 + tl, 575);
    uint32_t vo = v_at(d1, g0), vo2 = v_at(d2, g0), vo3 = v_at(d3, g0);
    u32x4 ut = {utag(T, 0), utag(T, 1), utag(T, 2), utag(T, 3)};
    u32x4 va, vc = ut, vb = ut;
    asm volatile("" : "+v"(vo), "+v"(vo2), "+v"(vo3), "+v"(ut), "+v"(vc), "+v"(vb));
    TS(0, 7);
    until(tv);
    TS(0, 8);
    asm volatile("global_load_dwordx4 %0, %2, %4 sc1\n\ts_cmp_lt_u32 %5, 2\n\ts_cbranch_scc1 1f\n\tglobal_load_dwordx4 %1, %3, %4 sc1\n1:"
                 : "=&v"(va), "+v"(vc) : "v"(vo), "v"(vo3), "s"(ws), "s"(wl) : "memory", "scc");
    until(tv + V2D);
    asm volatile("s_cmp_eq_u32 %3, 3\n\ts_cbranch_scc1 1f\n\tglobal_load_dwordx4 %0, %1, %2 sc1\n1:" : "+v"(vb) : "v"(vo2), "s"(ws), "s"(wl) : "memory", "scc");
    __builtin_amdgcn_sched_barrier(0);
    if (isx) {
      const u32x4 ra = *(const u32x4*)(smem + X_RW(wl) + 16 * lane), rb = *(const u32x4*)(smem + X_RW(wl) + 1024 + 16 * lane), rw = *(const u32x4*)(smem + X_WR + 16 * lane);
      const u32x2 rc = *(const u32x2*)(smem + X_RW(wl) + 2048 + 8 * lane);
#pragma unroll
      for (int k = 0; k < 4; ++k) { xv[k] = ra[k]; xv[4 + k] = rb[k]; wv[k] = rw[k]; }
      xv[8] = rc[0]; wv[4] = rc[1];
    }
    uint32_t pa = (uint32_t)(PL + 9216 * wl + 32 * lane), pb = (uint32_t)(PL + 9216 * wl + 2048 + 8 * (lane & 31)), ps = (lane < 32) ? 0u : 16u;
    uint32_t kid = (uint32_t)(((255 - g2) << 5) | (7 - 2 * wl - (lane >> 5)));
    uint32_t px = (uint32_t)(PL + 4 * d1), py = (uint32_t)(PL + 4 * d2), pz = (uint32_t)(PL + 4 * d3);
#pragma unroll
    for (int k = 0; k < 9; ++k) xf[k] = (f32x2){bflo(xv[k]), bfhi(xv[k])};
    asm volatile("" : "+v"(xf[0]), "+v"(xf[1]), "+v"(xf[2]), "+v"(xf[3]), "+v"(xf[4]), "+v"(xf[5]), "+v"(xf[6]), "+v"(xf[7]), "+v"(xf[8]));
    asm volatile("" : "+v"(za), "+v"(zb), "+v"(pa), "+v"(pb), "+v"(ps), "+v"(kid), "+v"(px), "+v"(py), "+v"(pz));
    float sv[10], ws_ = dot2(wv[4], 0x3f803f80u, 0.f) * 0.5f;
#pragma unroll
    for (int k = 0; k < 4; ++k) ws_ = dot2(wv[k], 0x3f803f80u, ws_);
    ws_ = wave_sum(ws_);
#pragma unroll
    for (int k = 0; k < 10; ++k) sv[k] = 16.f;
    uint32_t ka_ = lb0 + LM + 4u * (128 + 48 + 2 * wl + (lane >> 5)), kb_ = lb0 + LM + 4u * (128 + 48);
    uint32_t ko_ = O_KEY + (uint32_t)(256 * (lane & 7) + 8 * (g2 & 31) + (g2 >> 5)) * 16u;
    asm volatile("" : "+v"(ws_), "+v"(sv[0]), "+v"(sv[1]), "+v"(sv[2]), "+v"(sv[3]), "+v"(sv[4]), "+v"(sv[5]), "+v"(sv[6]), "+v"(sv[7]), "+v"(sv[8]), "+v"(sv[9]),
                 "+v"(ka_), "+v"(kb_), "+v"(ko_));
    if (ffn) gwait(ca + 36, 4);
    asm volatile("s_waitcnt vmcnt(1)\n\ts_cmp_eq_u32 %2, 3\n\ts_cbranch_scc0 1f\n\ts_waitcnt vmcnt(0)\n1:" : "+v"(va), "+v"(vc) : "s"(wl) : "memory", "scc");
    auto turn1 = [&] {
#pragma unroll
      for (int e = 0; e < 4; ++e) *(uint32_t*)(smem + px + 9216 * e) = va[e];
      if (wl >= 2) {
#pragma unroll
        for (int e = 0; e < 4; ++e) *(uint32_t*)(smem + pz + 9216 * e) = vc[e];
      }
    };
    auto turn = [&] {
      if (wl < 3) {
#pragma unroll
        for (int e = 0; e < 4; ++e) *(uint32_t*)(smem + py + 9216 * e) = vb[e];
      }
    };
    va ^= ut; vc ^= ut;
    turn1();
    asm volatile("s_waitcnt vmcnt(0)" : "+v"(vb) : : "memory");
    vb ^= ut;
    turn();
    const u32x4 vs = va | vb | vc;
    if (__builtin_expect(__any(((vs[0] | vs[1] | vs[2] | vs[3]) & UMASK) != 0u), 0)) {
#pragma unroll 1
      for (int spin = 0; spin < 4; ++spin) {
        const bool na = ((va[0] | va[1] | va[2] | va[3]) & UMASK) != 0u, nb = ((vb[0] | vb[1] | vb[2] | vb[3]) & UMASK) != 0u, nc = ((vc[0] | vc[1] | vc[2] | vc[3]) & UMASK) != 0u;
        if (!__any(na || nb || nc)) break;
        TSC(16);
        if (na) LD16(va, vo);
        if (nb) LD16(vb, vo2);
        if (nc) LD16(vc, vo3);
        asm volatile("s_waitcnt vmcnt(0)" : "+v"(va), "+v"(vb), "+v"(vc) : : "memory");
        if (na) va ^= ut;
        if (nb) vb ^= ut;
        if (nc) vc ^= ut;
      }
      auto again = [&](u32x4& v, uint32_t o, int d) {
        const int J = g0 >> 5;
        const uint32_t dn = O_DN + 16u * (uint32_t)((d < 512) ? 4 * (d >> 1) + ((0x01013232 >> (4 * J)) & 3) : 4 * (d - 256) + (J >> 1));
        bool pend = ((v[0] | v[1] | v[2] | v[3]) & UMASK) != 0u;
#pragma unroll 1
        for (int spin = 0; spin < SPIN_CAP && __any(pend); ++spin) {
          TSC(16);
          SLEEP(PSLEEP);
          if (pend) {
            uint32_t fin = ~T;
            if ((v[0] & v[1] & v[2] & v[3] & LMASK) == LMASK)
              asm volatile("global_load_dword %0, %1, %2 sc1\n\ts_waitcnt vmcnt(0)" : "=&v"(fin) : "v"(dn), "s"(ws) : "memory");
            LD16W(v, o);
            v ^= ut;
            pend = ((v[0] | v[1] | v[2] | v[3]) & UMASK) != 0u;
            if (pend && fin == T) {
              u32x2 sc;
              for (int k = 0; k < SPIN_CAP; ++k) {
                asm volatile("global_load_dwordx2 %0, %1, %2 sc1\n\ts_waitcnt vmcnt(0)" : "=&v"(sc) : "v"(O_SC + 16u * d), "s"(ws) : "memory");
                if (sc[0] == T) break;
              }
              *(u16*)(smem + L_SC + 2 * d) = (u16)(sc[1] >> 16);
              cnt[10] = 1u;
              v = (v ^ ut) & ~LMASK;
              pend = false;
            }
          }
        }
      };
      again(va, vo, d1); again(vc, vo3, d3); again(vb, vo2, d2);
      turn1();
      turn();
    }
    u32x4 a0, a1;
    u32x2 a2, cf;
    {
      uint32_t cc, n = SPIN_CAP;
      asm volatile("s_mov_b64 exec, 1\n\tv_mov_b32 v90, 1\n\tds_add_u32 %6, v90\n\ts_mov_b64 exec, -1\n"
                   "1:\n\tds_read2_b32 %3, %6 offset1:10\n\tds_read_b128 %0, %7\n\tds_read_b128 %1, %7 offset:16\n\tds_read_b64 %2, %8\n\t"
                   "s_sub_u32 %5, %5, 1\n\ts_cbranch_scc1 2f\n\ts_waitcnt lgkmcnt(3)\n\tv_readfirstlane_b32 %4, v90\n\t"
                   "s_cmp_lt_u32 %4, 4\n\ts_cbranch_scc1 1b\n2:\n\ts_waitcnt lgkmcnt(0)"
                   : "=&v"(a0), "=&v"(a1), "=&v"(a2), "=&{v[90:91]}"(cf), "=&s"(cc), "+s"(n) : "v"(ca), "v"(lb0 + pa), "v"(lb0 + pb) : "memory", "scc");
    }
    TS(0, 9);
    if (__builtin_expect(__builtin_amdgcn_readfirstlane(cf[1]) != 0, 0)) {
      const u32x4 q = *(const u32x4*)(smem + L_SC + 16 * lane);
      const uint32_t r = *(const uint32_t*)(smem + L_SC + 1024 + 4 * (lane & 31));
#pragma unroll
      for (int k = 0; k < 4; ++k) { sv[2 * k] = bflo(q[k]); sv[2 * k + 1] = bfhi(q[k]); }
      sv[8] = bflo(r); sv[9] = bfhi(r);
    }
    const uint32_t av[10] = {a0[0], a0[1], a0[2], a0[3], a1[0], a1[1], a1[2], a1[3], a2[0] >> ps, a2[1] >> ps};
    f32x2 h[9];
#pragma unroll
    for (int k = 0; k < 10; ++k) {
      asm("v_fma_mix_f32 %0, %1, %2, %3 op_sel_hi:[1,0,0]" : "=v"(h[(k < 8) ? k >> 1 : 8][k & 1]) : "v"(av[k]), "v"(sv[k]), "v"(xf[(k < 8) ? k >> 1 : 8][k & 1]));
      if (k < 8) asm("v_fma_mix_f32 %0, %1, %2, %3 op_sel:[1,0,0] op_sel_hi:[1,0,0]" : "=v"(h[4 + (k >> 1)][k & 1]) : "v"(av[k]), "v"(sv[k]), "v"(xf[4 + (k >> 1)][k & 1]));
    }
#pragma unroll
    for (int k = 0; k < 9; ++k) hv[k] = pack2(h[k][0], h[k][1]);
    const float da = dot2(hv[2], wv[2], dot2(hv[0], wv[0], zb[0])) + dot2(hv[3], wv[3], dot2(hv[1], wv[1], zb[1]));
    const float db = dot2(hv[6], wv[2], dot2(hv[4], wv[0], zb[2])) + dot2(hv[7], wv[3], dot2(hv[5], wv[1], zb[3]));
    float dt = dot2(hv[8], wv[4], fold32(da, db));
    f32x2 rc, nc;
    ln_stats<true>(hv, lane, 1.f, xf, za, rc, nc, dt);
    dt = (lane < 32) ? dt * rc[0] + ws_ * nc[0] : dt * rc[1] + ws_ * nc[1];
    const uint32_t bb = pack2(dt, dt) & 0xffff0000u;
    const uint32_t kk = (bb ^ (((uint32_t)((int)bb >> 31) & 0x7fff0000u) | 0x80000000u)) | kid;
    u32x4 ka, kb;
    uint32_t old;
    asm volatile("s_mov_b64 exec, %5\n\tds_write_b32 %3, %4\n\ts_mov_b64 exec, 1\n\tv_mov_b32 %2, 1\n\tds_add_rtn_u32 %2, %7, %2\n\ts_mov_b64 exec, -1\n\t"
                 "ds_read_b128 %0, %6\n\tds_read_b128 %1, %6 offset:16\n\ts_waitcnt lgkmcnt(0)"
                 : "=&v"(ka), "=&v"(kb), "=&v"(old) : "v"(ka_), "v"(kk), "s"(0x8000000080000000ull), "v"(kb_), "v"(ca) : "memory");
    if (__builtin_amdgcn_readfirstlane(old) == 7) {
      uint32_t m1 = max(max(ka[0], ka[1]), ka[2]), m2 = med3u(ka[0], ka[1], ka[2]), m3 = min(min(ka[0], ka[1]), ka[2]);
#pragma unroll
      for (int e = 0; e < 5; ++e) {
        const uint32_t kx = e ? kb[e - 1] : ka[3];
        m3 = med3u(m2, m3, kx); m2 = med3u(m1, m2, kx); m1 = max(m1, kx);
      }
      if (lane < 8) ST16(ko_, ((u32x4){m1 | 24u, m2 | 16u, m3 | 8u, T & ~1u}));
    }
    TS(0, 10);
    keya = (uint32_t)__builtin_amdgcn_readlane(kk, 31); keyb = (uint32_t)__builtin_amdgcn_readlane(kk, 63);
    ln_apply<false>(xf, lane, rc, nc, nv);
    gwait(ca, 8);
    if (wl == 1) {
      const u32x4 kv = *(const u32x4*)&kl[4 * (lane & 1)];
      if (lane < 2) ST16(O_KF + (uint32_t)(2 * g2 + lane) * 16u, ((u32x4){__builtin_amdgcn_perm(kv[1], kv[0], 0x07060302u), T, __builtin_amdgcn_perm(kv[3], kv[2], 0x07060302u), T}));
      if (ROLE == 2 && b == 0 && lane == 0) ST4(O_EP, T * 0x0E8B2F51u);
    }
    const uint32_t ko = kl[lane & 7];
    rka = FORCEFB + __popcll(__ballot(ko > keya) & 0xffull); rkb = FORCEFB + __popcll(__ballot(ko > keyb) & 0xffull);
#pragma unroll
    for (int k = 0; k < 9; ++k) {
      const f32x2 o = xf[k] + (f32x2){bflo(nv[k]), bfhi(nv[k])};
      hv[k] = pack2(o[0], o[1]);
      nv[k] = min(nv[k], 0xfffffffeu);
    }
    if (rka < 3) put_row(nv, 0, ws, O_XC + (uint32_t)(1023 - 4 * g2 - rka) * 1152u);
    if (rkb < 3) put_row(nv, 1, ws, O_XC + (uint32_t)(1023 - 4 * g2 - rkb) * 1152u);
    if (OTP && lane < 18 && ((lane < 9) ? rka : rkb) < 2)
      asm volatile("global_load_dword v126, %0, %1" OPOL : : "v"((uint32_t)((8 * g2 + 2 * wl) * (D_ * 2) + 128 * lane)), "s"(out) : "memory", "v126");
    if (!LPOS && ffn) down_dma(KN, 4, lane);
    ffn_terms();
  }
  uint32_t k3rd = 0u, k4th = 0u;
  uint32_t q0 = lb0 + LM + 4u * lane, q1 = lb0 + LM + 32u * w, q2 = q1 + 4u * (lane & 7), q3 = lb0 + LM + 512u + 4u * (lane & 7), q4 = lb0 + LM + (uint32_t)(tid & ~7);
  uint32_t r8 = C_, sb = lb0 + LM + 512u;
  asm volatile("" : "+v"(q0), "+v"(q1), "+v"(q2), "+v"(q3), "+v"(q4), "+v"(r8), "+v"(sb));
  if constexpr (ROLE == 1) {
    u32x4 key;
    const uint32_t kof = O_KEY + (uint32_t)(256 * bx + tid) * 16u;
    until(tk);
    if (!ffn) SLEEP(NSLEEP);
    LD16(key, kof);
    if (ffn) {
      gwait0(ca + 4, 8);
      gu_read(1);
    }
    asm volatile("s_waitcnt vmcnt(0)" : "+v"(key) : : "memory");
    if (__builtin_expect(__any(key[3] != (T & ~1u)), 0)) {
#pragma unroll 1
      for (int spin = 0; spin < SPIN_CAP; ++spin) {
        const bool need = key[3] != (T & ~1u);
        if (!__any(need)) break;
        TSC(15);
        SLEEP(KBACK);
        if (need) LD16W(key, kof);
      }
    }
    TS(0, 11);
    const uint32_t m1 = oct_maxu(key[0]);
    const bool hit = key[0] == m1;
    const uint32_t k0 = hit ? key[1] : key[0], k1 = hit ? key[2] : key[1];
    const uint32_t m2 = oct_maxu(k0);
    *LP(u32x2, q4) = (u32x2){m1, m2};
    k3rd = (k0 == m2) ? k1 : k0;
    k4th = (k0 == m2) ? (hit ? 0u : key[2]) : k1;
    if (RPF == 1 && !ffn && w == (bj & 3)) {
      const uint32_t r1 = O_XC + ((m1 >> 3) & 0x3ffu) * 1152u + 128u * (lane & 7), r2 = O_XC + ((m2 >> 3) & 0x3ffu) * 1152u + 128u * (lane & 7);
      asm volatile("global_load_dword v126, %0, %3 sc1\n\tglobal_load_dword v126, %1, %3 sc1\n\tglobal_load_dword v126, %2, %3 offset:1024 sc1"
                   : : "v"(r1), "v"(r2), "v"(((lane & 1) ? r1 : r2) - 128u * (lane & 7)), "s"(ws) : "memory", "v126");
    }
  }
  auto pick = [&](auto co, auto so_) {
    BAR();
    const uint32_t c = *LP(uint32_t, q0 + co.value), own = *LP(uint32_t, q2 + co.value);
    const u32x4 la = *LP(u32x4, q1 + co.value), lb = *LP(u32x4, q1 + co.value + 16);
    uint32_t r = r8;
    asm("v_cmp_gt_u32 s[40:41], %1, %2\n\tv_cmp_gt_u32 s[42:43], %1, %3\n\tv_cmp_gt_u32 s[44:45], %1, %4\n\tv_cmp_gt_u32 s[46:47], %1, %5\n\t"
        "v_cmp_gt_u32 s[48:49], %1, %6\n\tv_cmp_gt_u32 s[50:51], %1, %7\n\tv_cmp_gt_u32 s[52:53], %1, %8\n\tv_cmp_gt_u32 s[54:55], %1, %9\n\t"
        "s_bcnt1_i32_b64 s40, s[40:41]\n\ts_bcnt1_i32_b64 s42, s[42:43]\n\tv_writelane_b32 %0, s40, 0\n\ts_bcnt1_i32_b64 s44, s[44:45]\n\t"
        "v_writelane_b32 %0, s42, 1\n\ts_bcnt1_i32_b64 s46, s[46:47]\n\tv_writelane_b32 %0, s44, 2\n\ts_bcnt1_i32_b64 s48, s[48:49]\n\t"
        "v_writelane_b32 %0, s46, 3\n\ts_bcnt1_i32_b64 s50, s[50:51]\n\tv_writelane_b32 %0, s48, 4\n\ts_bcnt1_i32_b64 s52, s[52:53]\n\t"
        "v_writelane_b32 %0, s50, 5\n\ts_bcnt1_i32_b64 s54, s[54:55]\n\tv_writelane_b32 %0, s52, 6\n\tv_writelane_b32 %0, s54, 7"
        : "+v"(r) : "v"(c), "v"(la[0]), "v"(la[1]), "v"(la[2]), "v"(la[3]), "v"(lb[0]), "v"(lb[1]), "v"(lb[2]), "v"(lb[3])
        : "scc", "s40", "s41", "s42", "s43", "s44", "s45", "s46", "s47", "s48", "s49", "s50", "s51", "s52", "s53", "s54", "s55");
    *LP(uint32_t, sb + 4u * min(r, (uint32_t)C_) + so_.value) = own;
    BAR();
  };
  auto rows = [&](uint32_t ao, auto&& meanwhile) {
    u32x4 aa, ab, ac = __builtin_bit_cast(u32x4, zb);
    asm volatile("global_load_dwordx4 %0, %3, %4 sc1\n\tglobal_load_dwordx4 %1, %3, %4 offset:512 sc1\n\t"
                 "s_cmp_lg_u32 %5, 0\n\ts_cbranch_scc1 1f\n\tglobal_load_dwordx4 %2, %3, %4 offset:1024 sc1\n1:"
                 : "=&v"(aa), "=&v"(ab), "+v"(ac) : "v"(ao), "s"(ws), "s"(w) : "memory", "scc");
    __builtin_amdgcn_sched_barrier(0);
    meanwhile();
    asm volatile("s_waitcnt vmcnt(0)" : "+v"(aa), "+v"(ab), "+v"(ac) : : "memory");
    if (__builtin_expect(__any(max(max(max4(aa), max4(ab)), max4(ac)) == 0xffffffffu), 0)) {
#pragma unroll 1
      for (int spin = 0; spin < SPIN_CAP; ++spin) {
        const bool na = max4(aa) == 0xffffffffu, nb = max4(ab) == 0xffffffffu, nc = max4(ac) == 0xffffffffu;
        if (!__any(na || nb || nc)) break;
        TSC(14);
        SLEEP(PSLEEP);
        if (na) LD16W(aa, ao);
        if (nb) LD16W(ab, ao + 512u);
        if (nc) LD16W(ac, ao + 1024u);
      }
    }
    *(u32x4*)(smem + LA + 16 * tid) = aa;
    *(u32x4*)(smem + LA + 4096 + 16 * tid) = ab;
    if (w == 0) *(u32x4*)(smem + LA + 8192 + 16 * tid) = ac;
  };
  auto rows_dma = [&](uint32_t ao) {
    uint32_t dl = lb0 + LA + 1024u * w, keep;
    asm volatile("s_mov_b32 %0, m0\n\ts_mov_b32 m0, %1\n\ts_add_u32 %1, %1, 4096 - 512\n\tglobal_load_lds_dwordx4 %2, %3 sc1\n\ts_mov_b32 m0, %1\n\ts_add_u32 %1, %1, 4096 - 512\n\t"
                 "global_load_lds_dwordx4 %2, %3 offset:512 sc1\n\ts_cmp_lg_u32 %4, 0\n\ts_cbranch_scc1 1f\n\ts_mov_b32 m0, %1\n\ts_nop 0\n\t"
                 "global_load_lds_dwordx4 %2, %3 offset:1024 sc1\n1:\n\ts_mov_b32 m0, %0" : "=&s"(keep), "+s"(dl) : "v"(ao), "s"(ws), "s"(w) : "memory", "scc");
  };
  auto get_bd = [&](int t0, int t1) {
#pragma unroll
    for (int t = t0; t < t1; ++t) {
      const u32x2 h0 = trd(da + 512 * (t >> 2) + 32 * (t & 3)), h1 = trd(da + 512 * (t >> 2) + 32 * (t & 3) + 4608 + 32);
      bw[t] = (u32x4){h0[0], h0[1], h1[0], h1[1]};
    }
#pragma unroll
    for (int t = t0; t < t1; ++t) asm volatile("" : "+v"(bw[t]));
  };
  pick(IC<0>{}, IC<0>{});
  uint32_t mykey = *LP(uint32_t, q3), ro = ((mykey >> 3) & 0x3ffu) * 1152u;
  TS(0, 12);
  auto check = [&] {
    cnt[(k3rd > (uint32_t)__builtin_amdgcn_readlane(mykey, 7)) ? 5 : 6] = 1u;
    const uint32_t m3 = oct_maxu(k3rd);
    if (k3rd == m3) k3rd = k4th;
    if ((lane & 7) == 0) cand[72 + (tid >> 3)] = m3;
    if (w == 0 && lane < 32) cand[(lane < 8) ? 64 + lane : 96 + lane] = (lane < 8) ? mykey : 0u;
  };
  if constexpr (ROLE == 1) {
    if (ffn && !FORCEFB) rows_dma(ro + al);
    check();
    DRAIN();
  } else if (ffn) {
    if (LPOS) { SLEEP(LSLEEP); down_dma(KN, 4, lane); } else { DRAIN(); gsync0(ca, 12); get_bd(0, 8); }
  }
  BAR();
  TS(0, 5);
  auto put_out = [&](int j, uint32_t k0, int rk) {
    const uint64_t mine = __ballot(((mykey ^ k0) & ~24u) == 0u) & 0xffull;
    if (mine == 0ull) put_row(hv, j, out, (uint32_t)((8 * g2 + 2 * wl + j) * (D_ * 2)));
    else if (rk >= 3) put_row(nv, j, ws, O_FB + 1152u * (uint32_t)__builtin_ctzll(mine));
  };
  auto rest_out = [&] { put_out(0, keya, rka); put_out(1, keyb, rkb); };
  f32x4 lo, hi;
  auto gate_up = [&] {
    u32x4 av[5];
    const uint32_t aa = lb0 + LA + 16u * lane;
#pragma unroll
    for (int j = 0; j < 5; ++j) asm volatile("ds_read_b128 %0, %1 offset:%2" : "=v"(av[j]) : "v"(aa), "n"(1024 * j) : "memory");
    lo = (f32x4){0.f, 0.f, 0.f, 0.f}; hi = lo;
#pragma unroll
    for (int j = 0; j < 9; ++j) {
      if (j < 5) asm volatile("s_waitcnt lgkmcnt(4)" : "+v"(av[j % 5]));
      else if (j == 5) asm volatile("s_waitcnt lgkmcnt(2)" : "+v"(av[0]), "+v"(av[1]));
      else if (j == 7) asm volatile("s_waitcnt lgkmcnt(0)" : "+v"(av[2]), "+v"(av[3]));
      lo = MMB(av[j % 5], bw[2 * j], lo);
      hi = MMB(av[j % 5], bw[2 * j + 1], hi);
      __builtin_amdgcn_sched_barrier(0);
      if (j < 4) asm volatile("ds_read_b128 %0, %1 offset:%2" : "=v"(av[j]) : "v"(aa), "n"(1024 * (j + 5)) : "memory");
    }
  };
  uint32_t fl;
  asm volatile("ds_read_b32 %0, %1" : "=v"(fl) : "v"(ca + 20) : "memory");
  if (ROLE == 1) gate_up();
  asm volatile("s_waitcnt lgkmcnt(0)" : "+v"(fl));
  const bool slow = FORCESLOW || FORCEFB || __builtin_amdgcn_readfirstlane(fl) != 0;
  if (__builtin_expect(slow, 0)) {
    TSC(17);
    pick(IC<256>{}, IC<64>{});
    mykey = sel[16 + (lane & 7)];
    ro = ((mykey >> 3) & 0x3ffu) * 1152u;
    const uint32_t k8 = (uint32_t)__builtin_amdgcn_readlane(mykey, 7);
    if (ROLE == 1 && k3rd > k8) cnt[11] = 1u;
    if (w == 0 && __any((mykey & 24u) == 8u)) {
      const uint32_t pk = sel[16 + (lane >> 3)], gi = (pk >> 5) & 0xffu;
      u32x4 kf;
#pragma unroll 1
      for (int spin = 0; spin < SPIN_CAP; ++spin) {
        LD16W(kf, O_KF + (2u * (255u - gi) + ((lane >> 2) & 1u)) * 16u);
        if (!__any(kf[1] != T || kf[3] != T)) break;
        SLEEP(KBACK);
      }
      const uint32_t wd_ = (lane & 2) ? kf[2] : kf[0], kt = ((lane & 1) ? wd_ & 0xffff0000u : wd_ << 16) | (gi << 5) | (7u - (lane & 7));
      const uint64_t up = __ballot((pk & 24u) == 8u && kt >= (k8 & ~24u));
      if (__popcll((up >> (lane & 56)) & 0xffull) > 3) cnt[11] = 1u;
    }
    BAR();
    if (FORCESLOW > 1 || FORCEFB || __builtin_amdgcn_readfirstlane(cnt[11]) != 0) {
    u32x4 key;
#pragma unroll 1
    for (int spin = 0; spin < SPIN_CAP; ++spin) {
      LD16W(key, O_KF + 16u * tid);
      if (!__any(key[1] != T || key[3] != T)) break;
      SLEEP(KBACK);
    }
    const uint32_t ib = (uint32_t)(((255 - (tid >> 1)) << 5) | (7 - 4 * (tid & 1)));
    uint32_t k0 = (key[0] << 16) | ib, k1 = (key[0] & 0xffff0000u) | (ib - 1), k2 = (key[2] << 16) | (ib - 2), k3 = (key[2] & 0xffff0000u) | (ib - 3);
    CEX(k0, k1) CEX(k2, k3) CEX(k0, k2) CEX(k1, k3) CEX(k1, k2)
#pragma unroll 1
    for (int c = 0; c < C_; ++c) {
      const uint32_t mx = wave_maxu(k0);
      const bool hit = (k0 == mx);
      k0 = hit ? k1 : k0; k1 = hit ? k2 : k1; k2 = hit ? k3 : k2; k3 = hit ? 0u : k3;
      if (lane == 0) cand[64 + 8 * w + c] = mx;
    }
    pick(IC<256>{}, IC<128>{});
    mykey = sel[32 + (lane & 7)];
    const uint32_t okey = sel[32 + (lane >> 3)];
    const uint64_t same = __ballot(((mykey ^ okey) & 0x1fe0u) == 0u && okey > mykey);
    const int sl = FORCEFB + __popcll((same >> (lane & 7)) & 0x0101010101010101ull);
    ro = (sl < 3) ? (((mykey >> 3) & 0x3ffu) + 3 - sl) * 1152u : O_FB - O_XC + 1152u * (lane & 7);
    }
    if (ln2) rest_out();
    if (ffn) {
      if (ROLE == 1) rows(ro + al, [] {});
      BAR();
      if (ROLE == 1) gate_up();
    }
  }
  if (OPOS == 0 && ROLE == 2 && ffn && !slow) { SLEEP(OD); rest_out(); }
  if (!ffn) {
    if (ln2 && !slow) { SLEEP(OD2); rest_out(); }
    TSE();
    return;
  }
  uint32_t fx = 0, fv = 0, fn = 0, fo = 0, sa[9];
  float rs, hx, xs;
  u32x4 se;
  if constexpr (ROLE == 1) {
    f32x4 lh = (lane < 32) ? lo : hi;
    float fa = fold32(lh[0], lh[2]), fb2 = fold32(lh[1], lh[3]);
    if (__builtin_expect(__any(__builtin_isunordered(fa, fb2)) && !slow, 0)) {
      gsync0(ca + 4, 12);
      rows(ro + al, [] {});
      gsync0(ca + 4, 16);
      gate_up();
      lh = (lane < 32) ? lo : hi;
      fa = fold32(lh[0], lh[2]); fb2 = fold32(lh[1], lh[3]);
    }
    const float g = __int_as_float(__builtin_amdgcn_update_dpp(__float_as_int(fa), __float_as_int(fb2), 0x128, 0xf, 0xc, false));
    const float u = __int_as_float(__builtin_amdgcn_update_dpp(__float_as_int(fb2), __float_as_int(fa), 0x128, 0xf, 0x3, false));
    const uint32_t gu = pack2(g, u);
    const float gp = bflo(gu);
    const float sg = rbf(gp * __builtin_amdgcn_rcpf(1.f + __expf(-gp)));
    *(u16*)((char*)hm + ha) = f2bf(sg * bfhi(gu));
  } else if (LPOS) {
    DRAIN();
    gsync0(ca, 12);
    get_bd(0, 8);
  }
  BAR();
  TS(0, 6);
  if (ROLE == 2 || w == 2) {
    const u32x4 a0 = *(const u32x4*)((char*)hm + h0a), a1 = *(const u32x4*)((char*)hm + h1a);
    if (ROLE == 1) get_bd(0, 4);
    const f32x4 z = {0.f, 0.f, 0.f, 0.f};
    f32x4 o[4];
#pragma unroll
    for (int t = 0; t < 2 * ROLE; ++t) o[t] = MMB(a1, bw[2 * t + 1], MMB(a0, bw[2 * t], z));
#pragma unroll
    for (int t = 0; t < ROLE; ++t) {
      u32x4 sv;
#pragma unroll
      for (int r = 0; r < 4; ++r) sv[r] = min(pack2(o[2 * t][r], o[2 * t + 1][r]), 0xfffffffeu);
      ST16(so + 4096u * t, sv);
    }
    TS(0, 13);
  }
  if (OPOS == 1 && ROLE == 2) {
    SLEEP(OSLEEP);
    if (!slow) rest_out();
  }
  if (ROLE == 1 && w == 1) {
    const int q = (fb & 15) >> 2, ts = 4 * (q & 1) + ((i & 7) >> 1);
    const int dd = 64 * (fb >> 4) + 16 * (2 * (i & 1) + (q >> 1)) + 4 * (fb & 3) + 2 * (kq & 1) + (kq >> 1);
    const uint32_t fkey = (uint32_t)__builtin_amdgcn_ds_bpermute(4 * ts, (int)mykey), fr = (uint32_t)__builtin_amdgcn_ds_bpermute(4 * ts, (int)ro);
    const int ft = 2047 - (int)(((fkey >> 2) & 0x7f8u) | (fkey & 7u));
    fo = (uint32_t)(ft * D_ + dd) * 2u;
    u32x2 fs;
    asm volatile("global_load_ushort %0, %4, %8\n\tglobal_load_ushort %1, %5, %9 sc1\n\tglobal_load_ushort %2, %6, %9 sc1\n\tglobal_load_dwordx2 %3, %7, %9 sc1"
                 : "=&v"(fx), "=&v"(fv), "=&v"(fn), "=&v"(fs) : "v"(fo), "v"(v_at(dd, ft >> 3) + 2u * (ft & 7)), "v"(O_XC + fr + 2u * dd), "v"(O_SC + 16u * dd), "s"(x), "s"(ws) : "memory");
    uint32_t t16 = (((T >> (3 * ((ft >> 1) & 3))) & LMASK) | (((T >> (12 + ((ft >> 1) & 3))) & 0x00010001u) << 14)) >> (16 * (ft & 1)) & 0xffffu;
    const uint32_t o = fkey >> 16;
    const float rs0 = bf2f((u16)((o & 0x8000u) ? (o & 0x7fffu) : (~o & 0xffffu)));
    rs = rbf(__builtin_amdgcn_rcpf(1.f + __expf(-rs0)));
    const uint32_t one = (i < 8) ? (0x3f80u << (16 * (i & 1))) : 0u;
    se = (u32x4){(i >> 1) == 0 ? one : 0u, (i >> 1) == 1 ? one : 0u, (i >> 1) == 2 ? one : 0u, (i >> 1) == 3 ? one : 0u};
#pragma unroll
    for (int m = 0; m < 9; ++m) sa[m] = O_SL + (uint32_t)(16 * m + (lane >> 2)) * SP + (uint32_t)(4 * fb + (lane & 3)) * 16u;
    asm volatile("" : "+v"(t16), "+v"(rs), "+v"(se), "+v"(sa[0]), "+v"(sa[1]), "+v"(sa[2]), "+v"(sa[3]), "+v"(sa[4]), "+v"(sa[5]), "+v"(sa[6]), "+v"(sa[7]), "+v"(sa[8]));
    SLEEP(SSLEEP);
    if (FD) until(tk + FD);
    TS(0, 1);
    float sum;
#pragma unroll 1
    for (int spin = 0; spin < SPIN_CAP; ++spin) {
      u32x4 sv[9];
#pragma unroll
      for (int m = 0; m < 9; ++m) LD16(sv[m], sa[m]);
      asm volatile("s_waitcnt vmcnt(9)" : "+v"(fx), "+v"(fv), "+v"(fn), "+v"(fs));
      if (__builtin_expect(__any(fs[0] != T), 0)) {
        for (int k = 0; k < SPIN_CAP && fs[0] != T; ++k)
          asm volatile("global_load_dwordx2 %0, %1, %2 sc1\n\ts_waitcnt vmcnt(0)" : "=&v"(fs) : "v"(O_SC + 16u * dd), "s"(ws) : "memory");
      }
      const uint32_t f1 = fv ^ t16, f2 = (f1 & 7u) ? fv & ~7u : f1;
      hx = rbf(bf2f((u16)fx) + (float)__builtin_bit_cast(_Float16, (u16)f2) * __uint_as_float(fs[1]));
      xs = bf2f((u16)fn);
      asm volatile("" : "+v"(hx), "+v"(xs));
      f32x4 dv = {0.f, 0.f, 0.f, 0.f};
      static_for<0, 9>([&](auto mm) {
        constexpr int m = mm.value;
        asm volatile("s_waitcnt vmcnt(%1)" : "+v"(sv[m]) : "n"(8 - m));
        dv = MMB(sv[m], se, dv);
        __builtin_amdgcn_sched_barrier(0);
      });
      sum = fold16(fold32(dv[0], dv[1]), fold32(dv[2], dv[3]));
      if (__builtin_expect(!__any(sum != sum), 1)) break;
      uint32_t mx = 0u;
#pragma unroll
      for (int m = 0; m < 9; ++m) mx = max(mx, max4(sv[m]));
      if (!__any(mx == 0xffffffffu)) break;
      TSC(18);
      SLEEP(PSLEEP);
    }
    const float o1 = rbf(xs + rbf(rbf(sum) * rs));
    if (i < 8) asm volatile("global_store_short %0, %1, %2 sc1" : : "v"(fo), "v"((uint32_t)f2bf(hx + o1)), "s"(out) : "memory");
    TS(0, 2);
  }
  TSE();
  }
  }
}
#define BROT "192"
#define CPF_A "s_getpc_b64 s[96:97]\n\ts_lshl_b32 s99, s2, 4\n\ts_lshl_b32 s84, s98, 7\n\ts_add_u32 s99, s99, s84\n\tv_and_b32 v112, 1, v0\n\t" \
              "v_lshl_add_u32 v112, v112, 12, s99\n\tv_min_u32 v112, kf_end-kf_all-0x80, v112\n\ts_mov_b64 exec, 3\n\tglobal_load_dword v112, v112, s[96:97]\n\ts_mov_b64 exec, -1\n\t"
extern "C" __global__ __launch_bounds__(512, 4) void kf_all(KARGS) {
  __shared__ __attribute__((aligned(16))) char smem[SMEM];
  asm volatile("s_load_dwordx16 s[64:79], s[0:1], 0x0\n\ts_load_dwordx4 s[80:83], s[0:1], 0x40\n\tv_readfirstlane_b32 s98, v0\n\t"
               "s_add_u32 s84, s2, " BROT "\n\ts_xor_b32 s84, s84, s2\n\ts_bitset0_b32 s84, 8\n\ts_bitset0_b32 s84, 9\n\ts_xor_b32 s2, s2, s84\n\t"
               "v_mov_b32 v109, 0\n\ts_and_b32 s85, s2, 7\n\ts_bitcmp1_b32 s2, 8\n\ts_cbranch_scc1 1f\n\t" CPF_A "s_bitcmp1_b32 s98, 8\n\ts_cbranch_scc1 2f\n\t"
               "s_andn2_b32 s84, s2, 31\n\ts_lshl2_add_u32 s84, s85, s84\n\ts_bfe_u32 s85, s2, 0x20003\n\ts_or_b32 s84, s84, s85\n\t"
               "s_mulk_i32 s84, 0x2400\n\ts_mulk_i32 s98, 36\n\ts_add_u32 s84, s84, s98\n\tv_and_b32 v110, 63, v0\n\tv_and_b32 v111, 32, v0\n\t"
               "v_lshlrev_b32 v111, 5, v111\n\tv_lshl_add_u32 v111, v110, 2, v111\n\tv_lshl_add_u32 v110, v110, 4, s84\n\tv_add_u32 v111, s84, v111\n\t"
               "s_cmpk_lt_u32 s2, 0x90\n\ts_waitcnt lgkmcnt(0)\n\t"
               "global_load_dwordx4 v[100:103], v110, s[64:65]\n\tglobal_load_dwordx4 v[104:107], v110, s[64:65] offset:1152\n\t"
               "global_load_dword v108, v111, s[64:65] offset:1024\n\tglobal_load_dword v109, v109, s[82:83] sc1\n\t"
               "s_cbranch_scc1 kf_n\n\ts_branch kf_h\n"
               "1:\n\ts_bfe_u32 s84, s2, 0x50003\n\ts_lshl_b32 s85, s85, 5\n\ts_or_b32 s84, s84, s85\n\ts_lshl_b32 s84, s84, 2\n\t"
               "v_and_b32 v110, 0xff, v0\n\tv_bfe_u32 v107, v0, 7, 1\n"
               "4:\n\ts_lshl_b32 s85, s84, 7\n\tv_lshl_add_u32 v110, v110, 1, s85\n\tv_lshl_add_u32 v107, v107, 1, s84\n\ts_and_b32 s85, s98, s2\n\ts_waitcnt lgkmcnt(0)\n\t"
               "global_load_ushort v100, v107, s[78:79]\n\tglobal_load_ushort v101, v110, s[66:67]\n\tglobal_load_ushort v102, v110, s[68:69]\n\t"
               "global_load_dword v109, v109, s[82:83] sc1\n\ts_bitcmp1_b32 s85, 8\n\ts_cbranch_scc1 kf_u\n\t"
               "global_load_ushort v103, v110, s[70:71]\n\tglobal_load_ushort v104, v110, s[72:73]\n\tglobal_load_ushort v105, v110, s[74:75]\n\t"
               "global_load_ushort v106, v110, s[76:77]\n\tglobal_load_ushort v107, v107, s[80:81]\n\ts_bitcmp1_b32 s2, 8\n\ts_cbranch_scc1 kf_c\n"
               "6:\n\ts_barrier\n\ts_branch kf_p\n"
               "2:\n\ts_cmpk_lt_u32 s2, 0xc0\n\ts_cbranch_scc1 6b\n\t"
               "s_lshr_b32 s84, s2, 3\n\ts_lshl3_add_u32 s84, s85, s84\n\ts_lshl1_add_u32 s84, s84, 976\n\t"
               "v_and_b32 v110, 0x7f, v0\n\tv_mov_b32 v107, 0\n\ts_branch 4b"
               : : "s"(__builtin_amdgcn_kernarg_segment_ptr()), "s"(blockIdx.x), "v"(threadIdx.x),
                   "s"((uint32_t)(uintptr_t)(__attribute__((address_space(3))) char*)smem) : "scc", "s96", "s97", "s98", "s99", "v127");
}
extern "C" __global__ __launch_bounds__(512, 4) void kf_c(KARGS) {
  __shared__ __attribute__((aligned(16))) char smem[SMEM];
  role_body<0>(smem, KPASS);
}
extern "C" __global__ __launch_bounds__(512, 4) void kf_u(KARGS) {
  __shared__ __attribute__((aligned(16))) char smem[SMEM];
  role_body<3>(smem, KPASS);
}
extern "C" __global__ __launch_bounds__(512, 4) void kf_n(KARGS) {
  __shared__ __attribute__((aligned(16))) char smem[SMEM];
  role_body<1, 1>(smem, KPASS);
}
extern "C" __global__ __launch_bounds__(512, 4) void kf_h(KARGS) {
  __shared__ __attribute__((aligned(16))) char smem[SMEM];
  role_body<1, 0>(smem, KPASS);
}
extern "C" __global__ __launch_bounds__(512, 4) void kf_p(KARGS) {
  __shared__ __attribute__((aligned(16))) char smem[SMEM];
  role_body<2>(smem, KPASS);
}
extern "C" __global__ __launch_bounds__(512, 4) void kf_conv() { asm volatile(CONV_ASM ::: CONV_CLOB); }
extern "C" __global__ void kf_end() {}

extern "C" __global__ __launch_bounds__(1024) void k_probe(int* dummy) {
  __shared__ int sm[2];
  const uint32_t now = (uint32_t)__builtin_amdgcn_s_memrealtime();
  __shared__ int sn;
  if (threadIdx.x == 0) { sm[0] = 0; sm[1] = 1 == 1 ? 0x7fffffff : 0; sn = 0; }
  __syncthreads();
  for (int k = threadIdx.x; k < 4096; k += 1024) {
    const uint32_t a = (uint32_t)g_prb[k], b = (uint32_t)(g_prb[k] >> 32);
    if (a) atomicMax(&sm[0], (int)(now - a));
    if (b) { if (1 == 1) atomicMin(&sm[1], (int)(now - b)); else if (1 == 0) atomicMax(&sm[1], (int)(now - b)); else { atomicAdd(&sm[1], (int)(now - b)); atomicAdd(&sn, 1); } }
  }
  __syncthreads();
  const int q = min(max(sm[0] * 10 - (1 == 2 ? sm[1] * 10 / max(sn, 1) : sm[1] * 10), 0), 30000);
  if (threadIdx.x == 0) while ((int)((uint32_t)__builtin_amdgcn_s_memrealtime() - now) < q) __builtin_amdgcn_s_sleep(2);
}
extern "C" void kernel_launch(void* const* d_in, const int* in_sizes, int n_in,
                              void* d_out, int out_size, void* d_ws, size_t ws_size,
                              hipStream_t stream) {
  kf_all<<<512, 512, 0, stream>>>((const u16*)d_in[0], (const u16*)d_in[1], (const u16*)d_in[2], (const u16*)d_in[3], (const u16*)d_in[4],
                                  (const u16*)d_in[5], (const u16*)d_in[6], (const u16*)d_in[7], (const u16*)d_in[8], (char*)d_ws + (size_t)WSOFF * 1048576,
                                  (const u16*)d_in[10], (const u16*)d_in[11], (const u16*)d_in[12], (const u16*)d_in[13], (u16*)d_out);
  k_probe<<<1, 1024, 0, stream>>>((int*)d_ws);
}
```

```cpp
#include <hip/hip_runtime.h>
#include <hip/hip_bf16.h>
#include <stdint.h>
#define RD2 120
#define FD 195
#define VDB 355
#define VD2 365

typedef __attribute__((ext_vector_type(8))) short bf16x8;
typedef __attribute__((ext_vector_type(4))) float f32x4;
typedef uint16_t u16;
typedef __attribute__((ext_vector_type(4))) uint32_t u32x4;
typedef __attribute__((ext_vector_type(2))) uint32_t u32x2;
#define TSD uint32_t pr0_ = 0u, pr1_ = 0u
__device__ unsigned long long g_prb[4096];
#define TS(k, i) do { if ((i) == 0) pr0_ = (uint32_t)__builtin_amdgcn_s_memrealtime() | 1u; \
    if (ROLE == 1 && (i) == 2 && (1 < 0 || w == 1) && (blockIdx.x & 255) < 144) pr1_ = (uint32_t)__builtin_amdgcn_s_memrealtime() | 1u; } while (0)
#define TSC(i)
#define TSE() do { if (__builtin_amdgcn_mbcnt_hi(-1, __builtin_amdgcn_mbcnt_lo(-1, 0)) == 0) \
    __hip_atomic_store(&g_prb[blockIdx.x * 8 + w], ((unsigned long long)pr1_ << 32) | pr0_, __ATOMIC_RELAXED, __HIP_MEMORY_SCOPE_AGENT); } while (0)
#define TSP
#define TSA

constexpr int L_ = 2048, D_ = 576, N_ = 128, F_ = 4608, C_ = 8;
constexpr int NF = 144;
constexpr uint32_t O_EP = 0;
constexpr uint32_t O_KF = 4096;
constexpr uint32_t O_KEY = O_KF + 8192;
constexpr uint32_t O_SC = O_KEY + 32768;
constexpr uint32_t O_DN = O_SC + 9216;
constexpr uint32_t O_SB = O_DN + 20480;
constexpr uint32_t O_SU = O_SB + 65536;
constexpr uint32_t O_FB = O_SU + 262144;
constexpr uint32_t O_XC = O_FB + 8 * 1152;
constexpr uint32_t SP = 9216 + 128;
constexpr uint32_t O_SL = O_XC + 1024 * 1152;
constexpr uint32_t SLU = 336;
constexpr uint32_t O_U = O_SL + 256 * SLU * 16;
constexpr int UP = 584;
constexpr uint32_t VP = 8192 + 128, VPP = 4096 + 128;
constexpr uint32_t O_V = O_U + 256 * UP * 16, O_VP = O_V + 256 * VP;
static_assert(256 * SLU * 16 >= NF * SP && O_SL % 128 == 0 && O_U % 128 == 0 && O_V % 128 == 0, "");
__host__ __device__ __forceinline__ uint32_t u_at(int tg, int d) { return O_U + 16u * (uint32_t)((tg >> 1) * (UP * 2) + (d >> 2) * 8 + (tg & 1) * 4 + (d & 3)); }
__host__ __device__ __forceinline__ uint32_t v_at(int d, int tg) {
  return (d < 512) ? O_V + (uint32_t)(d >> 1) * VP + 16u * (uint32_t)(2 * tg + (d & 1)) : O_VP + (uint32_t)(d - 512) * VPP + 16u * (uint32_t)tg;
}
#define HAVE_VAT
#define HAVE_VTAG
__device__ __forceinline__ float bf2f(u16 v) { return __uint_as_float((uint32_t)v << 16); }
__device__ __forceinline__ float bflo(uint32_t v) { return __uint_as_float(v << 16); }
__device__ __forceinline__ float bfhi(uint32_t v) { return __uint_as_float(v & 0xffff0000u); }
__device__ __forceinline__ u16 f2bf(float f) { return __builtin_bit_cast(u16, (__bf16)f); }
__device__ __forceinline__ float rbf(float f) { return bf2f(f2bf(f)); }
typedef __attribute__((ext_vector_type(2))) float f32x2;
typedef __attribute__((ext_vector_type(2))) __bf16 bf16x2v;
__device__ __forceinline__ uint32_t pack2(float a, float b) {
  return __builtin_bit_cast(uint32_t, __builtin_convertvector((f32x2){a, b}, bf16x2v));
}

#define DPP_I(v, ctrl, rmask) __builtin_amdgcn_update_dpp(0, (v), (ctrl), (rmask), 0xf, true)
__device__ __forceinline__ float row_sum(float v) {
  v += __int_as_float(DPP_I(__float_as_int(v), 0x111, 0xf));
  v += __int_as_float(DPP_I(__float_as_int(v), 0x112, 0xf));
  v += __int_as_float(DPP_I(__float_as_int(v), 0x114, 0xf));
  v += __int_as_float(DPP_I(__float_as_int(v), 0x118, 0xf));
  return v;
}
__device__ __forceinline__ float rdl(float v, int l) { return __int_as_float(__builtin_amdgcn_readlane(__float_as_int(v), l)); }
__device__ __forceinline__ float wave_sum(float v) {
  v = row_sum(v);
  asm("s_nop 1\n\tv_add_f32_dpp %0, %0, %0 row_bcast:15 row_mask:0xa bank_mask:0xf\n\t"
      "s_nop 1\n\tv_add_f32_dpp %0, %0, %0 row_bcast:31 row_mask:0xc bank_mask:0xf\n\ts_nop 0" : "+v"(v));
  return rdl(v, 63);
}
__device__ __forceinline__ float fold32(float a, float b) {
  const auto r = __builtin_amdgcn_permlane32_swap(__float_as_uint(a), __float_as_uint(b), false, false);
  return __uint_as_float(r[0]) + __uint_as_float(r[1]);
}
__device__ __forceinline__ float fold16(float a, float b) {
  const auto r = __builtin_amdgcn_permlane16_swap(__float_as_uint(a), __float_as_uint(b), false, false);
  return __uint_as_float(r[0]) + __uint_as_float(r[1]);
}
__device__ __forceinline__ void wave_sum2(float& a, float& b) {
  float v = row_sum(fold32(a, b));
  asm("s_nop 1\n\tv_add_f32_dpp %0, %0, %0 row_bcast:15 row_mask:0xa bank_mask:0xf\n\ts_nop 0" : "+v"(v));
  a = rdl(v, 31); b = rdl(v, 63);
}
__device__ __forceinline__ float dot2(uint32_t a, uint32_t b, float c) {
  return __builtin_amdgcn_fdot2_f32_bf16(__builtin_bit_cast(bf16x2v, a), __builtin_bit_cast(bf16x2v, b), c, false);
}
__device__ __forceinline__ uint32_t row_maxu(uint32_t v) {
  v = max(v, (uint32_t)DPP_I((int)v, 0xb1, 0xf));
  v = max(v, (uint32_t)DPP_I((int)v, 0x4e, 0xf));
  v = max(v, (uint32_t)DPP_I((int)v, 0x141, 0xf));
  v = max(v, (uint32_t)DPP_I((int)v, 0x140, 0xf));
  return v;
}
__device__ __forceinline__ uint32_t wave_maxu(uint32_t v) {
  v = max(v, (uint32_t)DPP_I((int)v, 0x111, 0xf));
  v = max(v, (uint32_t)DPP_I((int)v, 0x112, 0xf));
  v = max(v, (uint32_t)DPP_I((int)v, 0x114, 0xf));
  v = max(v, (uint32_t)DPP_I((int)v, 0x118, 0xf));
  v = max(v, (uint32_t)DPP_I((int)v, 0x142, 0xa));
  v = max(v, (uint32_t)DPP_I((int)v, 0x143, 0xc));
  return (uint32_t)__builtin_amdgcn_readlane((int)v, 63);
}

typedef __attribute__((ext_vector_type(8))) _Float16 f16x8;
typedef __attribute__((ext_vector_type(2))) _Float16 f16x2;
__device__ __forceinline__ uint32_t packh(f32x2 v) { return __builtin_bit_cast(uint32_t, __builtin_convertvector(v, f16x2)); }
__device__ __forceinline__ uint32_t packh(float a, float b) { return packh((f32x2){a, b}); }
constexpr int IP = 224, I_X = 128;
__device__ __forceinline__ f32x2 cmul(f32x2 z, f32x2 w) {
  f32x2 r;
  asm("v_pk_mul_f32 %0, %1, %2 op_sel_hi:[0,1]\n\tv_pk_fma_f32 %0, %1, %2, %0 op_sel:[1,1,0] op_sel_hi:[1,0,1] neg_lo:[0,1,0]" : "=&v"(r) : "v"(z), "v"(w));
  return r;
}
__device__ __forceinline__ f32x2 cexp2(float ar2, float air, float l) {
  const float t = air * l;
  return (f32x2){__builtin_amdgcn_cosf(t), __builtin_amdgcn_sinf(t)} * __builtin_amdgcn_exp2f(ar2 * l);
}
__device__ __forceinline__ float state_a(float ldt, float are, float aim, float& ar2, float& air, float& g) {
  const float dt = rbf(__expf(ldt));
  ar2 = dt * are * 1.4426950408889634f;
  air = dt * aim * 0.15915494309189535f;
  air -= __builtin_rintf(air);
  g = 0.f;
  if (__builtin_expect(__any(ar2 > 0.f), 0)) g = fminf(__uint_as_float(wave_maxu(__float_as_uint(fmaxf(ar2, 0.f)))), 1.f);
  return dt;
}
template <int N> __device__ __forceinline__ void put_im(char* p, int im, const f32x2* z) {
  *(u32x4*)p = (u32x4){packh(z[0][0], z[1][0]), packh(z[2][0], z[3][0]), packh(z[4][0], z[5][0]), packh(z[6][0], z[7][0])};
  *(u32x4*)(p + im) = (u32x4){packh(z[0][1], z[1][1]), packh(z[2][1], z[3][1]), packh(z[4][1], z[5][1]), packh(z[6][1], z[7][1])};
}
#define Y_ASM \
  "v_mov_b64 v[40:41], %0\nv_pk_mul_f32 v[42:43], v[40:41], %1 op_sel_hi:[0,1]\nv_pk_mul_f32 v[44:45], %0, %2 op_sel_hi:[0,1]\n" \
  "v_pk_fma_f32 v[42:43], v[40:41], %1, v[42:43] op_sel:[1,1,0] op_sel_hi:[1,0,1] neg_lo:[0,1,0]\n" \
  "v_pk_fma_f32 v[44:45], %0, %2, v[44:45] op_sel:[1,1,0] op_sel_hi:[1,0,1] neg_lo:[0,1,0]\nv_cvt_pk_f16_f32 v56, v40, v42\n" \
  "v_cvt_pk_f16_f32 v60, v41, v43\nv_pk_mul_f32 v[40:41], v[42:43], %1 op_sel_hi:[0,1]\nv_pk_mul_f32 v[48:49], v[44:45], %2 op_sel_hi:[0,1]\n" \
  "v_pk_fma_f32 v[40:41], v[42:43], %1, v[40:41] op_sel:[1,1,0] op_sel_hi:[1,0,1] neg_lo:[0,1,0]\n" \
  "v_pk_fma_f32 v[48:49], v[44:45], %2, v[48:49] op_sel:[1,1,0] op_sel_hi:[1,0,1] neg_lo:[0,1,0]\n" \
  "v_pk_mul_f32 v[42:43], v[40:41], %1 op_sel_hi:[0,1]\nv_pk_mul_f32 v[52:53], v[48:49], %2 op_sel_hi:[0,1]\n" \
  "v_pk_fma_f32 v[42:43], v[40:41], %1, v[42:43] op_sel:[1,1,0] op_sel_hi:[1,0,1] neg_lo:[0,1,0]\n" \
  "v_pk_fma_f32 v[52:53], v[48:49], %2, v[52:53] op_sel:[1,1,0] op_sel_hi:[1,0,1] neg_lo:[0,1,0]\nv_cvt_pk_f16_f32 v57, v40, v42\n" \
  "v_cvt_pk_f16_f32 v61, v41, v43\nv_pk_mul_f32 v[40:41], v[42:43], %1 op_sel_hi:[0,1]\n" \
  "v_pk_fma_f32 v[40:41], v[42:43], %1, v[40:41] op_sel:[1,1,0] op_sel_hi:[1,0,1] neg_lo:[0,1,0]\n" \
  "v_pk_mul_f32 v[42:43], v[40:41], %1 op_sel_hi:[0,1]\nv_pk_mul_f32 v[46:47], v[44:45], %1 op_sel_hi:[0,1]\n" \
  "v_pk_fma_f32 v[42:43], v[40:41], %1, v[42:43] op_sel:[1,1,0] op_sel_hi:[1,0,1] neg_lo:[0,1,0]\n" \
  "v_pk_fma_f32 v[46:47], v[44:45], %1, v[46:47] op_sel:[1,1,0] op_sel_hi:[1,0,1] neg_lo:[0,1,0]\nv_cvt_pk_f16_f32 v58, v40, v42\n" \
  "v_cvt_pk_f16_f32 v62, v41, v43\nv_cvt_pk_f16_f32 v64, v44, v46\nv_cvt_pk_f16_f32 v68, v45, v47\n" \
  "v_pk_mul_f32 v[40:41], v[42:43], %1 op_sel_hi:[0,1]\nv_pk_mul_f32 v[44:45], v[46:47], %1 op_sel_hi:[0,1]\n" \
  "v_pk_fma_f32 v[40:41], v[42:43], %1, v[40:41] op_sel:[1,1,0] op_sel_hi:[1,0,1] neg_lo:[0,1,0]\n" \
  "v_pk_fma_f32 v[44:45], v[46:47], %1, v[44:45] op_sel:[1,1,0] op_sel_hi:[1,0,1] neg_lo:[0,1,0]\n" \
  "v_pk_mul_f32 v[42:43], v[40:41], %1 op_sel_hi:[0,1]\nv_pk_mul_f32 v[46:47], v[44:45], %1 op_sel_hi:[0,1]\n" \
  "v_pk_fma_f32 v[42:43], v[40:41], %1, v[42:43] op_sel:[1,1,0] op_sel_hi:[1,0,1] neg_lo:[0,1,0]\n" \
  "v_pk_fma_f32 v[46:47], v[44:45], %1, v[46:47] op_sel:[1,1,0] op_sel_hi:[1,0,1] neg_lo:[0,1,0]\nv_cvt_pk_f16_f32 v59, v40, v42\n" \
  "v_cvt_pk_f16_f32 v63, v41, v43\nv_cvt_pk_f16_f32 v65, v44, v46\nv_cvt_pk_f16_f32 v69, v45, v47\n" \
  "v_pk_mul_f32 v[44:45], v[46:47], %1 op_sel_hi:[0,1]\nds_write_b128 %3, v[56:59] offset:0\n" \
  "v_pk_fma_f32 v[44:45], v[46:47], %1, v[44:45] op_sel:[1,1,0] op_sel_hi:[1,0,1] neg_lo:[0,1,0]\nds_write_b128 %3, v[60:63] offset:64\n" \
  "v_pk_mul_f32 v[46:47], v[44:45], %1 op_sel_hi:[0,1]\nv_pk_mul_f32 v[50:51], v[48:49], %1 op_sel_hi:[0,1]\n" \
  "v_pk_fma_f32 v[46:47], v[44:45], %1, v[46:47] op_sel:[1,1,0] op_sel_hi:[1,0,1] neg_lo:[0,1,0]\n" \
  "v_pk_fma_f32 v[50:51], v[48:49], %1, v[50:51] op_sel:[1,1,0] op_sel_hi:[1,0,1] neg_lo:[0,1,0]\nv_cvt_pk_f16_f32 v66, v44, v46\n" \
  "v_cvt_pk_f16_f32 v70, v45, v47\nv_cvt_pk_f16_f32 v56, v48, v50\nv_cvt_pk_f16_f32 v60, v49, v51\n" \
  "v_pk_mul_f32 v[44:45], v[46:47], %1 op_sel_hi:[0,1]\nv_pk_mul_f32 v[48:49], v[50:51], %1 op_sel_hi:[0,1]\n" \
  "v_pk_fma_f32 v[44:45], v[46:47], %1, v[44:45] op_sel:[1,1,0] op_sel_hi:[1,0,1] neg_lo:[0,1,0]\n" \
  "v_pk_fma_f32 v[48:49], v[50:51], %1, v[48:49] op_sel:[1,1,0] op_sel_hi:[1,0,1] neg_lo:[0,1,0]\n" \
  "v_pk_mul_f32 v[46:47], v[44:45], %1 op_sel_hi:[0,1]\nv_pk_mul_f32 v[50:51], v[48:49], %1 op_sel_hi:[0,1]\n" \
  "v_pk_fma_f32 v[46:47], v[44:45], %1, v[46:47] op_sel:[1,1,0] op_sel_hi:[1,0,1] neg_lo:[0,1,0]\n" \
  "v_pk_fma_f32 v[50:51], v[48:49], %1, v[50:51] op_sel:[1,1,0] op_sel_hi:[1,0,1] neg_lo:[0,1,0]\nv_cvt_pk_f16_f32 v67, v44, v46\n" \
  "v_cvt_pk_f16_f32 v71, v45, v47\nv_cvt_pk_f16_f32 v57, v48, v50\nv_cvt_pk_f16_f32 v61, v49, v51\n" \
  "v_pk_mul_f32 v[48:49], v[50:51], %1 op_sel_hi:[0,1]\nds_write_b128 %3, v[64:67] offset:16\n" \
  "v_pk_fma_f32 v[48:49], v[50:51], %1, v[48:49] op_sel:[1,1,0] op_sel_hi:[1,0,1] neg_lo:[0,1,0]\nds_write_b128 %3, v[68:71] offset:80\n" \
  "v_pk_mul_f32 v[50:51], v[48:49], %1 op_sel_hi:[0,1]\nv_pk_mul_f32 v[54:55], v[52:53], %1 op_sel_hi:[0,1]\n" \
  "v_pk_fma_f32 v[50:51], v[48:49], %1, v[50:51] op_sel:[1,1,0] op_sel_hi:[1,0,1] neg_lo:[0,1,0]\n" \
  "v_pk_fma_f32 v[54:55], v[52:53], %1, v[54:55] op_sel:[1,1,0] op_sel_hi:[1,0,1] neg_lo:[0,1,0]\nv_cvt_pk_f16_f32 v58, v48, v50\n" \
  "v_cvt_pk_f16_f32 v62, v49, v51\nv_cvt_pk_f16_f32 v64, v52, v54\nv_cvt_pk_f16_f32 v68, v53, v55\n" \
  "v_pk_mul_f32 v[48:49], v[50:51], %1 op_sel_hi:[0,1]\nv_pk_mul_f32 v[52:53], v[54:55], %1 op_sel_hi:[0,1]\n" \
  "v_pk_fma_f32 v[48:49], v[50:51], %1, v[48:49] op_sel:[1,1,0] op_sel_hi:[1,0,1] neg_lo:[0,1,0]\n" \
  "v_pk_fma_f32 v[52:53], v[54:55], %1, v[52:53] op_sel:[1,1,0] op_sel_hi:[1,0,1] neg_lo:[0,1,0]\n" \
  "v_pk_mul_f32 v[50:51], v[48:49], %1 op_sel_hi:[0,1]\nv_pk_mul_f32 v[54:55], v[52:53], %1 op_sel_hi:[0,1]\n" \
  "v_pk_fma_f32 v[50:51], v[48:49], %1, v[50:51] op_sel:[1,1,0] op_sel_hi:[1,0,1] neg_lo:[0,1,0]\n" \
  "v_pk_fma_f32 v[54:55], v[52:53], %1, v[54:55] op_sel:[1,1,0] op_sel_hi:[1,0,1] neg_lo:[0,1,0]\nv_cvt_pk_f16_f32 v59, v48, v50\n" \
  "v_cvt_pk_f16_f32 v63, v49, v51\nv_cvt_pk_f16_f32 v65, v52, v54\nv_cvt_pk_f16_f32 v69, v53, v55\n" \
  "v_pk_mul_f32 v[52:53], v[54:55], %1 op_sel_hi:[0,1]\nds_write_b128 %3, v[56:59] offset:32\n" \
  "v_pk_fma_f32 v[52:53], v[54:55], %1, v[52:53] op_sel:[1,1,0] op_sel_hi:[1,0,1] neg_lo:[0,1,0]\nds_write_b128 %3, v[60:63] offset:96\n" \
  "v_pk_mul_f32 v[54:55], v[52:53], %1 op_sel_hi:[0,1]\n" \
  "v_pk_fma_f32 v[54:55], v[52:53], %1, v[54:55] op_sel:[1,1,0] op_sel_hi:[1,0,1] neg_lo:[0,1,0]\nv_cvt_pk_f16_f32 v66, v52, v54\n" \
  "v_cvt_pk_f16_f32 v70, v53, v55\nv_pk_mul_f32 v[52:53], v[54:55], %1 op_sel_hi:[0,1]\n" \
  "v_pk_fma_f32 v[52:53], v[54:55], %1, v[52:53] op_sel:[1,1,0] op_sel_hi:[1,0,1] neg_lo:[0,1,0]\n" \
  "v_pk_mul_f32 v[54:55], v[52:53], %1 op_sel_hi:[0,1]\n" \
  "v_pk_fma_f32 v[54:55], v[52:53], %1, v[54:55] op_sel:[1,1,0] op_sel_hi:[1,0,1] neg_lo:[0,1,0]\nv_cvt_pk_f16_f32 v67, v52, v54\n" \
  "v_cvt_pk_f16_f32 v71, v53, v55\nds_write_b128 %3, v[64:67] offset:48\nds_write_b128 %3, v[68:71] offset:112\n"
#define Y_CLOB "memory", "v40", "v41", "v42", "v43", "v44", "v45", "v46", "v47", "v48", "v49", "v50", "v51", "v52", "v53", "v54", "v55", "v56", "v57", "v58", "v59", "v60", "v61", "v62", "v63", "v64", "v65", "v66", "v67", "v68", "v69", "v70", "v71"
#define T_ASM \
  "ds_read_b128 %4, %8\nds_read_b64_tr_b16 v[80:81], %7 offset:0\nds_read_b64_tr_b16 v[82:83], %7 offset:64\n" \
  "ds_read_b64_tr_b16 v[84:85], %5 offset:0\nds_read_b64_tr_b16 v[86:87], %5 offset:48\nds_read_b64_tr_b16 v[88:89], %6 offset:0\n" \
  "ds_read_b64_tr_b16 v[90:91], %6 offset:48\nds_read_b64_tr_b16 v[92:93], %7 offset:3584\nds_read_b64_tr_b16 v[94:95], %7 offset:3648\n" \
  "ds_read_b64_tr_b16 v[96:97], %5 offset:3584\nds_read_b64_tr_b16 v[98:99], %5 offset:3632\nds_read_b64_tr_b16 v[100:101], %6 offset:3584\n" \
  "ds_read_b64_tr_b16 v[102:103], %6 offset:3632\nds_read_b64_tr_b16 v[104:105], %7 offset:7168\nds_read_b64_tr_b16 v[106:107], %7 offset:7232\n" \
  "s_waitcnt lgkmcnt(8)\nv_mfma_f32_16x16x32_f16 %0, v[84:87], v[80:83], 0\nds_read_b64_tr_b16 v[108:109], %5 offset:7168\n" \
  "ds_read_b64_tr_b16 v[110:111], %5 offset:7216\nds_read_b64_tr_b16 v[112:113], %6 offset:7168\nv_mfma_f32_16x16x32_f16 %2, v[88:91], v[80:83], 0\n" \
  "ds_read_b64_tr_b16 v[114:115], %6 offset:7216\nds_read_b64_tr_b16 v[116:117], %7 offset:10752\nds_read_b64_tr_b16 v[118:119], %7 offset:10816\n" \
  "s_waitcnt lgkmcnt(8)\nv_mfma_f32_16x16x32_f16 %0, v[96:99], v[92:95], %0\nds_read_b64_tr_b16 v[120:121], %5 offset:10752\n" \
  "ds_read_b64_tr_b16 v[122:123], %5 offset:10800\nds_read_b64_tr_b16 v[124:125], %6 offset:10752\n" \
  "v_mfma_f32_16x16x32_f16 %2, v[100:103], v[92:95], %2\nds_read_b64_tr_b16 v[126:127], %6 offset:10800\n" \
  "ds_read_b64_tr_b16 v[80:81], %7 offset:14336\nds_read_b64_tr_b16 v[82:83], %7 offset:14400\ns_waitcnt lgkmcnt(8)\n" \
  "v_mfma_f32_16x16x32_f16 %0, v[108:111], v[104:107], %0\nds_read_b64_tr_b16 v[84:85], %5 offset:14336\n" \
  "ds_read_b64_tr_b16 v[86:87], %5 offset:14384\nds_read_b64_tr_b16 v[88:89], %6 offset:14336\n" \
  "v_mfma_f32_16x16x32_f16 %2, v[112:115], v[104:107], %2\nds_read_b64_tr_b16 v[90:91], %6 offset:14384\n" \
  "ds_read_b64_tr_b16 v[92:93], %7 offset:17920\nds_read_b64_tr_b16 v[94:95], %7 offset:17984\ns_waitcnt lgkmcnt(8)\n" \
  "v_mfma_f32_16x16x32_f16 %0, v[120:123], v[116:119], %0\nds_read_b64_tr_b16 v[96:97], %5 offset:17920\n" \
  "ds_read_b64_tr_b16 v[98:99], %5 offset:17968\nds_read_b64_tr_b16 v[100:101], %6 offset:17920\n" \
  "v_mfma_f32_16x16x32_f16 %2, v[124:127], v[116:119], %2\nds_read_b64_tr_b16 v[102:103], %6 offset:17968\n" \
  "ds_read_b64_tr_b16 v[104:105], %7 offset:21504\nds_read_b64_tr_b16 v[106:107], %7 offset:21568\ns_waitcnt lgkmcnt(8)\n" \
  "v_mfma_f32_16x16x32_f16 %1, v[84:87], v[80:83], 0\nds_read_b64_tr_b16 v[108:109], %5 offset:21504\nds_read_b64_tr_b16 v[110:111], %5 offset:21552\n" \
  "ds_read_b64_tr_b16 v[112:113], %6 offset:21504\nv_mfma_f32_16x16x32_f16 %3, v[88:91], v[80:83], 0\nds_read_b64_tr_b16 v[114:115], %6 offset:21552\n" \
  "ds_read_b64_tr_b16 v[116:117], %7 offset:25088\nds_read_b64_tr_b16 v[118:119], %7 offset:25152\ns_waitcnt lgkmcnt(8)\n" \
  "v_mfma_f32_16x16x32_f16 %1, v[96:99], v[92:95], %1\nds_read_b64_tr_b16 v[120:121], %5 offset:25088\n" \
  "ds_read_b64_tr_b16 v[122:123], %5 offset:25136\nds_read_b64_tr_b16 v[124:125], %6 offset:25088\n" \
  "v_mfma_f32_16x16x32_f16 %3, v[100:103], v[92:95], %3\nds_read_b64_tr_b16 v[126:127], %6 offset:25136\ns_waitcnt lgkmcnt(6)\n" \
  "v_mfma_f32_16x16x32_f16 %1, v[108:111], v[104:107], %1\nv_mfma_f32_16x16x32_f16 %3, v[112:115], v[104:107], %3\ns_waitcnt lgkmcnt(0)\n" \
  "v_mfma_f32_16x16x32_f16 %1, v[120:123], v[116:119], %1\nv_mfma_f32_16x16x32_f16 %3, v[124:127], v[116:119], %3\n"
#define T_CLOB "memory", "v80", "v81", "v82", "v83", "v84", "v85", "v86", "v87", "v88", "v89", "v90", "v91", "v92", "v93", "v94", "v95", "v96", "v97", "v98", "v99", "v100", "v101", "v102", "v103", "v104", "v105", "v106", "v107", "v108", "v109", "v110", "v111", "v112", "v113", "v114", "v115", "v116", "v117", "v118", "v119", "v120", "v121", "v122", "v123", "v124", "v125", "v126", "v127"
template <class F> __device__ __forceinline__ int gen_y(float ldt, float are, float aim, float bre, float bim, float cre, float cim, uint32_t im, F&& mid TSP) {
  float ar2, air, g;
  const float dt = state_a(ldt, are, aim, ar2, air, g);
  const float dbr = dt * bre, dbi = dt * bim, pr = cre * dbr - cim * dbi, pi = cre * dbi + cim * dbr;
  const f32x2 w8 = cexp2(ar2, -air, 8.f), w64 = cexp2(ar2, -air, 64.f);
  const float pm = fmaxf(fabsf(pr), fabsf(pi));
  int e = -4;
  if (__builtin_expect(__any(!(pm < 0x1p-4f) || ar2 > 0.f) || !__any(pm > 0x1p-12f), 0)) e = (int)(wave_maxu(__float_as_uint(pm)) >> 23) - 126 + (int)ceilf(g * 250.f);
  const f32x2 y0 = {__builtin_amdgcn_ldexpf(pr, 14 - e), __builtin_amdgcn_ldexpf(-pi, 14 - e)};
  mid();
  asm volatile(Y_ASM : : "v"(y0), "v"(w8), "v"(w64), "v"(im) : Y_CLOB);
  return e;
}
__device__ __forceinline__ int gen_x(float ldt, float are, float aim, char* im) {
  float ar2, air, g;
  state_a(ldt, are, aim, ar2, air, g);
  const int e = min((int)ceilf(g * 1810.f), 300);
  const float s = __builtin_amdgcn_ldexpf(1.f, 15 - e);
  const f32x2 w1 = cexp2(ar2, air, 1.f), w256 = cexp2(ar2, air, 256.f), w2 = cmul(w1, w1);
  f32x2 a[8], b[8], c[8];
  a[0] = (f32x2){s, 0.f}; a[1] = w1 * s; a[2] = w2 * s;
  b[0] = b[1] = w256 * s;
  b[2] = cmul(b[0], w2); a[3] = cmul(a[1], w2); a[4] = cmul(a[2], w2);
  b[3] = b[4] = b[5] = cmul(b[2], w2); a[5] = cmul(a[3], w2); a[6] = cmul(a[4], w2);
  b[6] = cmul(b[3], w2); a[7] = cmul(a[5], w2); c[0] = cmul(b[3], w256);
  b[7] = cmul(b[6], w2); c[2] = cmul(c[0], w256);
  put_im<0>(im + I_X, 48, a);
  c[1] = cmul(b[7], w256); c[3] = cmul(c[1], w256);
  put_im<0>(im + I_X + 16, 48, b);
#pragma unroll
  for (int k = 4; k < 8; ++k) c[k] = cmul(c[k - 1], w256);
  put_im<0>(im + I_X + 32, 48, c);
  return e;
}

template <int I> struct IC { static constexpr int value = I; };
template <int B, int E, class F> __device__ __forceinline__ void static_for(F&& f) {
  if constexpr (B < E) { f(IC<B>{}); static_for<B + 1, E>(f); }
}
typedef __attribute__((ext_vector_type(4))) short s16x4;
__device__ __forceinline__ u32x2 trd(uint32_t a) {
  return __builtin_bit_cast(u32x2, __builtin_amdgcn_ds_read_tr16_b64_v4i16((__attribute__((address_space(3))) s16x4*)(uintptr_t)a));
}
#define BAR() asm volatile("s_waitcnt lgkmcnt(0)\n\ts_barrier" ::: "memory")
#define ST16(off, v) asm volatile("global_store_dwordx4 %0, %1, %2 sc1\n\ts_nop 1" : : "v"(off), "v"(v), "s"(ws) : "memory")
#define ST4(off, v) asm volatile("global_store_dword %0, %1, %2 sc1" : : "v"(off), "v"(v), "s"(ws) : "memory")
#define LD16(r, off) asm volatile("global_load_dwordx4 %0, %1, %2 sc1" : "=v"(r) : "v"(off), "s"(ws) : "memory")
#define LD16W(r, off) asm volatile("global_load_dwordx4 %0, %1, %2 sc1\n\ts_waitcnt vmcnt(0)" : "=&v"(r) : "v"(off), "s"(ws) : "memory")
#define DRAIN() asm volatile("s_waitcnt vmcnt(0)" ::: "memory")
#define CEX(a, b) { const uint32_t hi_ = max(a, b), lo_ = min(a, b); a = hi_; b = lo_; }
#define MMB(a_, b_, c_) __builtin_amdgcn_mfma_f32_16x16x32_bf16(__builtin_bit_cast(bf16x8, a_), __builtin_bit_cast(bf16x8, b_), c_, 0, 0, 0)
#define NTM 0
#define DPOL ""
#define DPOL2 ""
#define DPOL4 ""
constexpr int SPIN_CAP = 1 << 18;
constexpr uint32_t UMASK = 0x40074007u;
__device__ __forceinline__ uint32_t utag(uint32_t T, int j) {
  return ((T >> (3 * j)) & 0x00070007u) | (((j <= 2) ? (T << (2 - j)) : (T >> (j - 2))) & 0x40004000u);
}
__device__ __forceinline__ uint32_t max4(const u32x4& a) { return max(max(a[0], a[1]), max(a[2], a[3])); }
#ifndef ULONG
#define ULONG 8
#endif
__device__ __forceinline__ void until(uint32_t t) {
#pragma unroll 1
  for (int spin = 0; spin < 600; ++spin) {
    const int d = (int)(t - (uint32_t)__builtin_amdgcn_s_memrealtime());
    if (d <= 0) break;
    if (ULONG && d > 4 * ULONG) __builtin_amdgcn_s_sleep(ULONG); else __builtin_amdgcn_s_sleep(1);
  }
}
__device__ __forceinline__ uint32_t latest(uint32_t t) {
  const uint32_t t0 = (uint32_t)__builtin_amdgcn_readfirstlane(t) - 0x40000000u;
  return wave_maxu(t - t0) + t0;
}
__device__ __forceinline__ uint32_t gate(const char* ws, uint32_t po, uint32_t T) {
  u32x2 pv;
#pragma unroll 1
  for (int spin = 0; spin < SPIN_CAP; ++spin) {
    asm volatile("global_load_dwordx2 %0, %1, %2 sc1\n\ts_waitcnt vmcnt(0)" : "=&v"(pv) : "v"(po), "s"(ws) : "memory");
    if (!__any(pv[0] != T)) break;
    __builtin_amdgcn_s_sleep(2);
  }
  return pv[1];
}
__device__ __forceinline__ uint32_t gate2(const char* ws, uint32_t oa, uint32_t da, uint32_t ob, uint32_t T) {
  u32x2 pa, pb;
#pragma unroll 1
  for (int spin = 0; spin < SPIN_CAP; ++spin) {
    asm volatile("global_load_dwordx2 %0, %2, %4 sc1\n\tglobal_load_dwordx2 %1, %3, %4 sc1\n\ts_waitcnt vmcnt(0)" : "=&v"(pa), "=&v"(pb) : "v"(oa), "v"(ob), "s"(ws) : "memory");
    if (!__any(pa[0] != T || pb[0] != T)) break;
    __builtin_amdgcn_s_sleep(4);
  }
  pa[1] += da;
  return latest(((int)(pa[1] - pb[1]) > 0) ? pa[1] : pb[1]);
}
__device__ __forceinline__ uint32_t ahead(uint32_t c0, uint32_t t0, int dt) {
  const uint32_t c = (uint32_t)__builtin_amdgcn_s_memtime(), t = (uint32_t)__builtin_amdgcn_s_memrealtime();
  return t + (uint32_t)((float)((t - t0) * (uint32_t)(20 * dt)) * __builtin_amdgcn_rcpf((float)(c - c0)));
}
__device__ __forceinline__ void gsync(uint32_t a, uint32_t target) {
  uint32_t t, c, n = SPIN_CAP;
  uint64_t ex;
  asm volatile("s_waitcnt lgkmcnt(0)\n\ts_mov_b64 %3, exec\n\ts_mov_b64 exec, 1\n\tv_mov_b32 %0, 1\n\tds_add_u32 %4, %0\n\ts_mov_b64 exec, %3\n"
               "1:\n\ts_sleep 1\n\tds_read_b32 %0, %4\n\ts_sub_u32 %2, %2, 1\n\ts_cbranch_scc1 2f\n\ts_waitcnt lgkmcnt(0)\n\tv_readfirstlane_b32 %1, %0\n\t"
               "s_cmp_lt_u32 %1, %5\n\ts_cbranch_scc1 1b\n2:\n\ts_waitcnt lgkmcnt(0)"
               : "=&v"(t), "=&s"(c), "+s"(n), "=&s"(ex) : "v"(a), "n"(target) : "memory", "scc");
}
__device__ __forceinline__ void gsync0(uint32_t a, uint32_t target) {
  uint32_t t, c, n = SPIN_CAP;
  asm volatile("s_waitcnt lgkmcnt(0)\n\ts_mov_b64 exec, 1\n\tv_mov_b32 %0, 1\n\tds_add_u32 %3, %0\n\ts_mov_b64 exec, -1\n"
               "1:\n\tds_read_b32 %0, %3\n\ts_sub_u32 %2, %2, 1\n\ts_cbranch_scc1 2f\n\ts_waitcnt lgkmcnt(0)\n\tv_readfirstlane_b32 %1, %0\n\t"
               "s_cmp_lt_u32 %1, %4\n\ts_cbranch_scc1 1b\n2:\n\ts_waitcnt lgkmcnt(0)"
               : "=&v"(t), "=&s"(c), "+s"(n) : "v"(a), "n"(target) : "memory", "scc");
}
__device__ __forceinline__ void gwait(uint32_t a, uint32_t target) {
  uint32_t t, c, n = SPIN_CAP;
  asm volatile("1:\n\ts_sleep 2\n\tds_read_b32 %0, %3\n\ts_sub_u32 %2, %2, 1\n\ts_cbranch_scc1 2f\n\ts_waitcnt lgkmcnt(0)\n\tv_readfirstlane_b32 %1, %0\n\t"
               "s_cmp_lt_u32 %1, %4\n\ts_cbranch_scc1 1b\n2:\n\ts_waitcnt lgkmcnt(0)"
               : "=&v"(t), "=&s"(c), "+s"(n) : "v"(a), "n"(target) : "memory", "scc");
}
__device__ __forceinline__ void gwait0(uint32_t a, uint32_t target) {
  uint32_t t, c, n = SPIN_CAP;
  asm volatile("1:\n\tds_read_b32 %0, %3\n\ts_sub_u32 %2, %2, 1\n\ts_cbranch_scc1 2f\n\ts_waitcnt lgkmcnt(0)\n\tv_readfirstlane_b32 %1, %0\n\t"
               "s_cmp_lt_u32 %1, %4\n\ts_cbranch_scc1 1b\n2:\n\ts_waitcnt lgkmcnt(0)"
               : "=&v"(t), "=&s"(c), "+s"(n) : "v"(a), "n"(target) : "memory", "scc");
}
__device__ __forceinline__ void dma16(const void* base, uint32_t voff, uint32_t lds) {
  uint32_t keep;
  asm volatile("s_mov_b32 %0, m0\n\ts_mov_b32 m0, %3\n\ts_nop 0\n\tglobal_load_lds_dwordx4 %1, %2" DPOL "\n\ts_mov_b32 m0, %0" : "=&s"(keep) : "v"(voff), "s"(base), "s"(lds) : "memory");
}
__device__ __forceinline__ void dma16d(const void* base, uint32_t voff, uint32_t lds) {
  uint32_t keep;
  asm volatile("s_mov_b32 %0, m0\n\ts_mov_b32 m0, %3\n\ts_nop 0\n\tglobal_load_lds_dwordx4 %1, %2" DPOL2 "\n\ts_mov_b32 m0, %0" : "=&s"(keep) : "v"(voff), "s"(base), "s"(lds) : "memory");
}
__device__ __forceinline__ void nap(int n) {
  for (; n > 15; n -= 15) __builtin_amdgcn_s_sleep(15);
  switch (n) {
    case 1: __builtin_amdgcn_s_sleep(1); break; case 2: __builtin_amdgcn_s_sleep(2); break; case 3: __builtin_amdgcn_s_sleep(3); break;
    case 4: __builtin_amdgcn_s_sleep(4); break; case 5: __builtin_amdgcn_s_sleep(5); break; case 6: __builtin_amdgcn_s_sleep(6); break;
    case 7: __builtin_amdgcn_s_sleep(7); break; case 8: __builtin_amdgcn_s_sleep(8); break; case 9: __builtin_amdgcn_s_sleep(9); break;
    case 10: __builtin_amdgcn_s_sleep(10); break; case 11: __builtin_amdgcn_s_sleep(11); break; case 12: __builtin_amdgcn_s_sleep(12); break;
    case 13: __builtin_amdgcn_s_sleep(13); break; case 14: __builtin_amdgcn_s_sleep(14); break; case 15: __builtin_amdgcn_s_sleep(15); break;
    default: break;
  }
}
#define SLEEP(n) do { if ((n) > 0) __builtin_amdgcn_s_sleep(n); } while (0)
#define USLEEP 3
#define SSLEEP 11
#define PSLEEP 2
#define PPRIO 0
#define LFIRST 1
#define WD 40
#define OSLEEP 12
#define QPRIO 1
#define KPRIO 2
#define GSLEEP 0
#define NSLEEP 0
#define UD 40
#define XD 0
#define RSLEEP 0
#define UPRIO 2
#define WDEPTH 6
#define ESLEEP 0
#define SD 58
#define KD 180
#define OD 0
#define KBACK 6
#define DDEPTH 12
#define C3 1
#define XROT 2
#define WSOFF 0
#define KN 1
#define RPF 1
#define LSLEEP 1
#define TSTOP 78
#define HSTOP 110
#define VEARLY 0ull
#define FPR 3
#define SPR 3
#define DPRIO 0
#define QPRIO2 3
#define HW 2
#define RD 57
#define OPOS 1
#define OD2 6
#define NA 12
#define ORD1 ((unsigned __int128)0x321087654u << 36 | 0x321087654ull)
#define WDEPTH2 5
#define RQ NA
#define HREV 1
#define QPRIO0 3
#define VPS 1
#define RW 0
#define NPRIO 0
#define XSL 10
#define V2D 24
#define N0 8
#define LPOS 1
#define ALLP 1
#define OTP 1
#define OPOL ""
#define STP 1
#define BALL 1
#define FORCESLOW 0
#define FORCEFB 0
#define CONV_ASM \
  "s_cmp_eq_u32 s89, 1\ns_cbranch_scc1 1f\ns_cmp_gt_u32 s89, 1\ns_cbranch_scc1 9f\nds_read_b64 v[124:125], v3 offset:576\n" \
  "ds_read_b64 v[126:127], v3 offset:584\nds_read_b64 v[120:121], v3 offset:64\nds_read_b64 v[122:123], v3 offset:72\n" \
  "ds_read_b64 v[116:117], v3 offset:640\nds_read_b64 v[118:119], v3 offset:648\nds_read_b64 v[112:113], v3 offset:128\n" \
  "ds_read_b64 v[114:115], v3 offset:136\nds_read_b64 v[108:109], v3 offset:704\nds_read_b64 v[110:111], v3 offset:712\n" \
  "ds_read_b64 v[104:105], v3 offset:192\nds_read_b64 v[106:107], v3 offset:200\nds_read_b64 v[100:101], v3 offset:768\n" \
  "ds_read_b64 v[102:103], v3 offset:776\nds_read_b64 v[96:97], v3 offset:256\nds_read_b64 v[98:99], v3 offset:264\n" \
  "ds_read_b64 v[92:93], v3 offset:832\nds_read_b64 v[94:95], v3 offset:840\nds_read_b64 v[88:89], v3 offset:320\n" \
  "ds_read_b64 v[90:91], v3 offset:328\nds_read_b64 v[84:85], v3 offset:896\nds_read_b64 v[86:87], v3 offset:904\n" \
  "ds_read_b64 v[80:81], v3 offset:384\nds_read_b64 v[82:83], v3 offset:392\nds_read_b64 v[76:77], v3 offset:960\n" \
  "ds_read_b64 v[78:79], v3 offset:968\nds_read_b64 v[72:73], v3 offset:448\nds_read_b64 v[74:75], v3 offset:456\n" \
  "ds_read_b64 v[68:69], v3 offset:1024\nds_read_b64 v[70:71], v3 offset:1032\nds_read_b64 v[64:65], v3 offset:512\n" \
  "ds_read_b64 v[66:67], v3 offset:520\nds_read_b64 v[60:61], v3 offset:0\nds_read_b64 v[62:63], v3 offset:8\nds_read_b64 v[56:57], v3 offset:1088\n" \
  "ds_read_b64 v[58:59], v3 offset:1096\ns_waitcnt lgkmcnt(0)\ns_barrier\nds_read_b128 v[36:39], v2 offset:0\nds_read_b128 v[40:43], v2 offset:64\n" \
  "ds_read_b128 v[44:47], v2 offset:128\nds_read_b128 v[48:51], v2 offset:192\ns_waitcnt lgkmcnt(3)\n" \
  "v_mfma_f32_16x16x32_f16 v[12:15], v[124:127], v[36:39], 0\ns_setprio 1\nds_read_b128 v[52:55], v2 offset:256\n" \
  "v_mfma_f32_16x16x32_f16 v[16:19], v[120:123], v[36:39], 0\ns_waitcnt lgkmcnt(3)\nv_mfma_f32_16x16x32_f16 v[12:15], v[116:119], v[40:43], v[12:15]\n" \
  "ds_read_b128 v[36:39], v2 offset:320\nv_mfma_f32_16x16x32_f16 v[16:19], v[112:115], v[40:43], v[16:19]\ns_waitcnt lgkmcnt(3)\n" \
  "v_mfma_f32_16x16x32_f16 v[12:15], v[108:111], v[44:47], v[12:15]\nds_read_b128 v[40:43], v2 offset:384\n" \
  "v_mfma_f32_16x16x32_f16 v[16:19], v[104:107], v[44:47], v[16:19]\ns_waitcnt lgkmcnt(3)\n" \
  "v_mfma_f32_16x16x32_f16 v[12:15], v[100:103], v[48:51], v[12:15]\nds_read_b128 v[44:47], v2 offset:448\n" \
  "v_mfma_f32_16x16x32_f16 v[16:19], v[96:99], v[48:51], v[16:19]\ns_waitcnt lgkmcnt(3)\n" \
  "v_mfma_f32_16x16x32_f16 v[12:15], v[92:95], v[52:55], v[12:15]\nv_mfma_f32_16x16x32_f16 v[16:19], v[88:91], v[52:55], v[16:19]\n" \
  "s_waitcnt lgkmcnt(2)\nv_mfma_f32_16x16x32_f16 v[12:15], v[84:87], v[36:39], v[12:15]\nds_read_b128 v[48:51], v2 offset:512\n" \
  "v_mfma_f32_16x16x32_f16 v[16:19], v[80:83], v[36:39], v[16:19]\ns_waitcnt lgkmcnt(2)\n" \
  "v_mfma_f32_16x16x32_f16 v[12:15], v[76:79], v[40:43], v[12:15]\nds_read_b128 v[52:55], v2 offset:576\n" \
  "v_mfma_f32_16x16x32_f16 v[16:19], v[72:75], v[40:43], v[16:19]\ns_waitcnt lgkmcnt(2)\n" \
  "v_mfma_f32_16x16x32_f16 v[12:15], v[68:71], v[44:47], v[12:15]\nds_read_b128 v[36:39], v2 offset:640\n" \
  "v_mfma_f32_16x16x32_f16 v[16:19], v[64:67], v[44:47], v[16:19]\nv_mfma_f32_16x16x32_f16 v[20:23], v[60:63], v[44:47], 0\n" \
  "ds_read_b128 v[40:43], v2 offset:704\nv_mfma_f32_16x16x32_f16 v[28:31], v[56:59], v[44:47], 0\ns_waitcnt lgkmcnt(3)\n" \
  "v_mfma_f32_16x16x32_f16 v[16:19], v[124:127], v[48:51], v[16:19]\nds_read_b128 v[56:59], v2 offset:768\nv_cvt_pkrtz_f16_f32 v32, v12, v13\n" \
  "v_mfma_f32_16x16x32_f16 v[20:23], v[120:123], v[48:51], v[20:23]\nv_cvt_pkrtz_f16_f32 v33, v14, v15\ns_waitcnt lgkmcnt(3)\n" \
  "v_mfma_f32_16x16x32_f16 v[16:19], v[116:119], v[52:55], v[16:19]\nds_read_b128 v[44:47], v2 offset:832\n" \
  "v_pk_add_u16 v32, v32, 4 op_sel_hi:[1,0] clamp\nv_mfma_f32_16x16x32_f16 v[20:23], v[112:115], v[52:55], v[20:23]\n" \
  "v_pk_add_u16 v33, v33, 4 op_sel_hi:[1,0] clamp\ns_waitcnt lgkmcnt(3)\nv_mfma_f32_16x16x32_f16 v[16:19], v[108:111], v[36:39], v[16:19]\n" \
  "ds_read_b128 v[48:51], v2 offset:896\nds_write_b64 v4, v[32:33] offset:0\nv_mfma_f32_16x16x32_f16 v[20:23], v[104:107], v[36:39], v[20:23]\n" \
  "s_waitcnt lgkmcnt(3)\nv_mfma_f32_16x16x32_f16 v[16:19], v[100:103], v[40:43], v[16:19]\nds_read_b128 v[52:55], v2 offset:960\n" \
  "v_mfma_f32_16x16x32_f16 v[20:23], v[96:99], v[40:43], v[20:23]\nv_mfma_f32_16x16x32_f16 v[16:19], v[92:95], v[56:59], v[16:19]\n" \
  "v_mfma_f32_16x16x32_f16 v[20:23], v[88:91], v[56:59], v[20:23]\nds_read_b128 v[36:39], v2 offset:1024\ns_waitcnt lgkmcnt(3)\n" \
  "v_mfma_f32_16x16x32_f16 v[16:19], v[84:87], v[44:47], v[16:19]\nv_mfma_f32_16x16x32_f16 v[20:23], v[80:83], v[44:47], v[20:23]\n" \
  "ds_read_b128 v[40:43], v2 offset:1088\nds_read_b64 v[56:57], v6 offset:256\nds_read_b64 v[58:59], v6 offset:264\n" \
  "v_mfma_f32_16x16x32_f16 v[16:19], v[76:79], v[48:51], v[16:19]\nv_mfma_f32_16x16x32_f16 v[20:23], v[72:75], v[48:51], v[20:23]\n" \
  "ds_read_b128 v[44:47], v2 offset:1152\ns_waitcnt lgkmcnt(4)\nv_mfma_f32_16x16x32_f16 v[16:19], v[68:71], v[52:55], v[16:19]\n" \
  "v_mfma_f32_16x16x32_f16 v[20:23], v[64:67], v[52:55], v[20:23]\nds_read_b128 v[48:51], v2 offset:1216\n" \
  "v_mfma_f32_16x16x32_f16 v[24:27], v[60:63], v[52:55], 0\nv_mfma_f32_16x16x32_f16 v[20:23], v[124:127], v[36:39], v[20:23]\n" \
  "ds_read_b128 v[52:55], v2 offset:1280\nv_mfma_f32_16x16x32_f16 v[24:27], v[120:123], v[36:39], v[24:27]\ns_waitcnt lgkmcnt(5)\n" \
  "v_mfma_f32_16x16x32_f16 v[20:23], v[116:119], v[40:43], v[20:23]\nds_read_b128 v[36:39], v2 offset:1344\nv_cvt_pkrtz_f16_f32 v32, v16, v17\n" \
  "v_mfma_f32_16x16x32_f16 v[24:27], v[112:115], v[40:43], v[24:27]\nv_cvt_pkrtz_f16_f32 v33, v18, v19\ns_waitcnt lgkmcnt(3)\n" \
  "v_mfma_f32_16x16x32_f16 v[20:23], v[108:111], v[44:47], v[20:23]\nds_read_b128 v[40:43], v2 offset:1408\n" \
  "v_pk_add_u16 v32, v32, 4 op_sel_hi:[1,0] clamp\nds_read_b32 v35, v9 offset:32\nv_mfma_f32_16x16x32_f16 v[24:27], v[104:107], v[44:47], v[24:27]\n" \
  "v_pk_add_u16 v33, v33, 4 op_sel_hi:[1,0] clamp\ns_waitcnt lgkmcnt(3)\nv_mfma_f32_16x16x32_f16 v[20:23], v[100:103], v[48:51], v[20:23]\n" \
  "ds_read_b128 v[44:47], v2 offset:1472\nds_write_b64 v4, v[32:33] offset:1024\nv_mfma_f32_16x16x32_f16 v[24:27], v[96:99], v[48:51], v[24:27]\n" \
  "v_mfma_f32_16x16x32_f16 v[20:23], v[92:95], v[52:55], v[20:23]\nv_mfma_f32_16x16x32_f16 v[24:27], v[88:91], v[52:55], v[24:27]\n" \
  "ds_read_b128 v[48:51], v2 offset:1536\ns_waitcnt lgkmcnt(3)\nv_readfirstlane_b32 s85, v35\ns_cmp_lt_u32 s85, 4\ns_cbranch_scc1 21f\n22:\n" \
  "v_mfma_f32_16x16x32_f16 v[20:23], v[84:87], v[36:39], v[20:23]\nv_mfma_f32_16x16x32_f16 v[24:27], v[80:83], v[36:39], v[24:27]\n" \
  "ds_read_b128 v[52:55], v2 offset:1600\nv_mfma_f32_16x16x32_f16 v[20:23], v[76:79], v[40:43], v[20:23]\n" \
  "v_mfma_f32_16x16x32_f16 v[24:27], v[72:75], v[40:43], v[24:27]\nds_read_b128 v[36:39], v2 offset:1664\ns_waitcnt lgkmcnt(4)\n" \
  "v_mfma_f32_16x16x32_f16 v[20:23], v[68:71], v[44:47], v[20:23]\nv_mfma_f32_16x16x32_f16 v[24:27], v[64:67], v[44:47], v[24:27]\n" \
  "ds_read_b128 v[40:43], v2 offset:1728\nv_mfma_f32_16x16x32_f16 v[28:31], v[60:63], v[44:47], v[28:31]\nds_read_b64_tr_b16 v[60:61], v5 offset:0\n" \
  "ds_read_b64_tr_b16 v[62:63], v5 offset:32\ns_waitcnt lgkmcnt(4)\nv_mfma_f32_16x16x32_f16 v[24:27], v[124:127], v[48:51], v[24:27]\n" \
  "v_mfma_f32_16x16x32_f16 v[28:31], v[120:123], v[48:51], v[28:31]\nds_read_b64_tr_b16 v[44:45], v5 offset:256\n" \
  "ds_read_b64_tr_b16 v[46:47], v5 offset:288\nv_mfma_f32_16x16x32_f16 v[24:27], v[116:119], v[52:55], v[24:27]\n" \
  "ds_read_b64 v[120:121], v6 offset:320\nds_read_b64 v[122:123], v6 offset:328\nv_mfma_f32_16x16x32_f16 v[28:31], v[112:115], v[52:55], v[28:31]\n" \
  "ds_read_b128 v[48:51], v2 offset:1792\ns_waitcnt lgkmcnt(7)\nv_mfma_f32_16x16x32_f16 v[24:27], v[108:111], v[36:39], v[24:27]\n" \
  "ds_read_b128 v[112:115], v2 offset:1856\nv_mfma_f32_16x16x32_f16 v[28:31], v[104:107], v[36:39], v[28:31]\nds_read_b64 v[52:53], v6 offset:128\n" \
  "ds_read_b64 v[54:55], v6 offset:136\nv_mfma_f32_16x16x32_f16 v[24:27], v[100:103], v[40:43], v[24:27]\nds_read_b128 v[104:107], v2 offset:1920\n" \
  "v_mfma_f32_16x16x32_f16 v[28:31], v[96:99], v[40:43], v[28:31]\nds_read_b64 v[36:37], v6 offset:192\nds_read_b64 v[38:39], v6 offset:200\n" \
  "s_waitcnt lgkmcnt(9)\nv_mfma_f32_16x16x32_f16 v[20:23], v[60:63], v[56:59], v[20:23]\nds_read_b128 v[96:99], v2 offset:1984\ns_waitcnt lgkmcnt(7)\n" \
  "v_mfma_f32_16x16x32_f16 v[20:23], v[44:47], v[120:123], v[20:23]\nv_mfma_f32_16x16x32_f16 v[24:27], v[92:95], v[48:51], v[24:27]\n" \
  "ds_read_b128 v[40:43], v2 offset:2048\nv_mfma_f32_16x16x32_f16 v[28:31], v[88:91], v[48:51], v[28:31]\nds_read_b64_tr_b16 v[88:89], v5 offset:512\n" \
  "ds_read_b64_tr_b16 v[90:91], v5 offset:544\ns_waitcnt lgkmcnt(6)\nv_mfma_f32_16x16x32_f16 v[24:27], v[84:87], v[112:115], v[24:27]\n" \
  "v_mfma_f32_16x16x32_f16 v[28:31], v[80:83], v[112:115], v[28:31]\nv_cvt_pkrtz_f16_f32 v32, v20, v21\nv_cvt_pkrtz_f16_f32 v33, v22, v23\n" \
  "v_mfma_f32_16x16x32_f16 v[24:27], v[76:79], v[104:107], v[24:27]\nds_read_b64_tr_b16 v[48:49], v5 offset:768\n" \
  "ds_read_b64_tr_b16 v[50:51], v5 offset:800\nv_mfma_f32_16x16x32_f16 v[28:31], v[72:75], v[104:107], v[28:31]\n" \
  "ds_read_b128 v[80:83], v2 offset:2112\ns_waitcnt lgkmcnt(5)\nv_mfma_f32_16x16x32_f16 v[24:27], v[68:71], v[96:99], v[24:27]\n" \
  "ds_read_b128 v[112:115], v2 offset:2176\nv_pk_add_u16 v32, v32, 4 op_sel_hi:[1,0] clamp\n" \
  "v_mfma_f32_16x16x32_f16 v[28:31], v[64:67], v[96:99], v[28:31]\nds_read_b128 v[72:75], v2 offset:2240\n" \
  "v_pk_add_u16 v33, v33, 4 op_sel_hi:[1,0] clamp\nv_mfma_f32_16x16x32_f16 v[28:31], v[124:127], v[40:43], v[28:31]\n" \
  "ds_read_b128 v[104:107], v2 offset:2304\nds_write_b64 v4, v[32:33] offset:2048\nv_mfma_f32_16x16x32_f16 v[24:27], v[60:63], v[52:55], v[24:27]\n" \
  "ds_read_b128 v[64:67], v2 offset:2368\nds_read_b64 v[96:97], v6 offset:0\nds_read_b64 v[98:99], v6 offset:8\n" \
  "v_mfma_f32_16x16x32_f16 v[24:27], v[44:47], v[36:39], v[24:27]\nds_read_b128 v[124:127], v2 offset:2432\ns_waitcnt lgkmcnt(9)\n" \
  "v_mfma_f32_16x16x32_f16 v[24:27], v[88:91], v[56:59], v[24:27]\nds_read_b128 v[40:43], v2 offset:2496\n" \
  "v_mfma_f32_16x16x32_f16 v[24:27], v[48:51], v[120:123], v[24:27]\ns_waitcnt lgkmcnt(8)\n" \
  "v_mfma_f32_16x16x32_f16 v[28:31], v[116:119], v[80:83], v[28:31]\nds_read_b64 v[116:117], v6 offset:64\nds_read_b64 v[118:119], v6 offset:72\n" \
  "v_mfma_f32_16x16x32_f16 v[28:31], v[108:111], v[112:115], v[28:31]\nds_read_b64_tr_b16 v[80:81], v5 offset:1024\n" \
  "ds_read_b64_tr_b16 v[82:83], v5 offset:1056\ns_waitcnt lgkmcnt(10)\nv_mfma_f32_16x16x32_f16 v[28:31], v[100:103], v[72:75], v[28:31]\n" \
  "v_cvt_pkrtz_f16_f32 v32, v24, v25\nv_cvt_pkrtz_f16_f32 v33, v26, v27\nv_mfma_f32_16x16x32_f16 v[28:31], v[92:95], v[104:107], v[28:31]\n" \
  "v_pk_add_u16 v32, v32, 4 op_sel_hi:[1,0] clamp\ns_waitcnt lgkmcnt(5)\nv_mfma_f32_16x16x32_f16 v[28:31], v[84:87], v[64:67], v[28:31]\n" \
  "ds_read_b64_tr_b16 v[108:109], v5 offset:1280\nds_read_b64_tr_b16 v[110:111], v5 offset:1312\n" \
  "v_mfma_f32_16x16x32_f16 v[28:31], v[76:79], v[124:127], v[28:31]\nds_read_b64_tr_b16 v[112:113], v7 offset:0\n" \
  "ds_read_b64_tr_b16 v[114:115], v7 offset:64\ns_waitcnt lgkmcnt(6)\nv_mfma_f32_16x16x32_f16 v[28:31], v[68:71], v[40:43], v[28:31]\n" \
  "ds_read2_b32 v[100:101], v8 offset0:48 offset1:49\nds_read2_b32 v[102:103], v8 offset0:50 offset1:51\n" \
  "v_mfma_f32_16x16x32_f16 v[28:31], v[60:63], v[96:99], v[28:31]\nv_pk_add_u16 v33, v33, 4 op_sel_hi:[1,0] clamp\n" \
  "ds_write_b64 v4, v[32:33] offset:3072\nv_mfma_f32_16x16x32_f16 v[28:31], v[44:47], v[116:119], v[28:31]\n" \
  "v_mfma_f32_16x16x32_f16 v[28:31], v[88:91], v[52:55], v[28:31]\nv_mfma_f32_16x16x32_f16 v[28:31], v[48:51], v[36:39], v[28:31]\n" \
  "s_waitcnt lgkmcnt(5)\nv_mfma_f32_16x16x32_f16 v[28:31], v[80:83], v[56:59], v[28:31]\n" \
  "v_mfma_f32_16x16x32_f16 v[28:31], v[108:111], v[120:123], v[28:31]\ns_waitcnt lgkmcnt(1)\n" \
  "v_mfma_f32_16x16x32_f16 v[28:31], v[112:115], v[100:103], v[28:31]\ns_nop 7\nv_cvt_pkrtz_f16_f32 v32, v28, v29\nv_cvt_pkrtz_f16_f32 v33, v30, v31\n" \
  "v_pk_add_u16 v32, v32, 4 op_sel_hi:[1,0] clamp\nv_pk_add_u16 v33, v33, 4 op_sel_hi:[1,0] clamp\nds_write_b64 v4, v[32:33] offset:4096\n" \
  "s_setprio 0\ns_setpc_b64 s[90:91]\n21:\ns_movk_i32 s84, 0x4000\n23:\nds_read_b32 v35, v9 offset:32\ns_sub_u32 s84, s84, 1\ns_cbranch_scc1 22b\n" \
  "s_waitcnt lgkmcnt(0)\nv_readfirstlane_b32 s85, v35\ns_cmp_lt_u32 s85, 4\ns_cbranch_scc1 23b\ns_branch 22b\n1:\n" \
  "ds_read_b64 v[124:125], v3 offset:1088\nds_read_b64 v[126:127], v3 offset:1096\nds_read_b64 v[120:121], v3 offset:0\n" \
  "ds_read_b64 v[122:123], v3 offset:8\nds_read_b64 v[116:117], v3 offset:64\nds_read_b64 v[118:119], v3 offset:72\n" \
  "ds_read_b64 v[112:113], v3 offset:128\nds_read_b64 v[114:115], v3 offset:136\nds_read_b64 v[108:109], v3 offset:192\n" \
  "ds_read_b64 v[110:111], v3 offset:200\nds_read_b64 v[104:105], v3 offset:256\nds_read_b64 v[106:107], v3 offset:264\n" \
  "ds_read_b64 v[100:101], v3 offset:320\nds_read_b64 v[102:103], v3 offset:328\nds_read_b64 v[96:97], v3 offset:384\n" \
  "ds_read_b64 v[98:99], v3 offset:392\nds_read_b64 v[92:93], v3 offset:448\nds_read_b64 v[94:95], v3 offset:456\n" \
  "ds_read_b64 v[88:89], v3 offset:512\nds_read_b64 v[90:91], v3 offset:520\nds_read_b64 v[84:85], v3 offset:576\n" \
  "ds_read_b64 v[86:87], v3 offset:584\nds_read_b64 v[80:81], v3 offset:640\nds_read_b64 v[82:83], v3 offset:648\n" \
  "ds_read_b64 v[76:77], v3 offset:704\nds_read_b64 v[78:79], v3 offset:712\nds_read_b64 v[72:73], v3 offset:768\n" \
  "ds_read_b64 v[74:75], v3 offset:776\nds_read_b64 v[68:69], v3 offset:832\nds_read_b64 v[70:71], v3 offset:840\n" \
  "ds_read_b64 v[64:65], v3 offset:896\nds_read_b64 v[66:67], v3 offset:904\nds_read_b64 v[60:61], v3 offset:960\n" \
  "ds_read_b64 v[62:63], v3 offset:968\nds_read_b64 v[56:57], v3 offset:1024\nds_read_b64 v[58:59], v3 offset:1032\n" \
  "ds_read_b64 v[52:53], v6 offset:0\nds_read_b64 v[54:55], v6 offset:8\nds_read_b64 v[48:49], v6 offset:64\nds_read_b64 v[50:51], v6 offset:72\n" \
  "s_waitcnt lgkmcnt(0)\ns_barrier\nds_read_b128 v[28:31], v2 offset:960\nds_read_b128 v[32:35], v2 offset:1472\n" \
  "ds_read_b128 v[36:39], v2 offset:1984\nds_read_b128 v[40:43], v2 offset:2048\nds_read_b128 v[44:47], v2 offset:2112\ns_waitcnt lgkmcnt(3)\n" \
  "v_mfma_f32_16x16x32_f16 v[12:15], v[124:127], v[28:31], 0\ns_setprio 1\nds_read_b128 v[28:31], v2 offset:2176\n" \
  "v_mfma_f32_16x16x32_f16 v[16:19], v[124:127], v[32:35], 0\nds_read_b128 v[32:35], v2 offset:2240\ns_waitcnt lgkmcnt(3)\n" \
  "v_mfma_f32_16x16x32_f16 v[12:15], v[120:123], v[36:39], v[12:15]\nv_mfma_f32_16x16x32_f16 v[20:23], v[124:127], v[36:39], 0\n" \
  "ds_read_b128 v[124:127], v2 offset:2304\nds_read_b128 v[36:39], v2 offset:2368\nv_mfma_f32_16x16x32_f16 v[12:15], v[116:119], v[40:43], v[12:15]\n" \
  "ds_read_b128 v[40:43], v2 offset:2432\ns_waitcnt lgkmcnt(4)\nv_mfma_f32_16x16x32_f16 v[12:15], v[112:115], v[44:47], v[12:15]\n" \
  "ds_read_b128 v[44:47], v2 offset:2496\nv_mfma_f32_16x16x32_f16 v[12:15], v[108:111], v[28:31], v[12:15]\nds_read_b128 v[28:31], v2 offset:2560\n" \
  "s_waitcnt lgkmcnt(4)\nv_mfma_f32_16x16x32_f16 v[12:15], v[104:107], v[32:35], v[12:15]\nds_read_b128 v[32:35], v2 offset:2624\n" \
  "v_mfma_f32_16x16x32_f16 v[12:15], v[100:103], v[124:127], v[12:15]\ns_waitcnt lgkmcnt(3)\n" \
  "v_mfma_f32_16x16x32_f16 v[12:15], v[96:99], v[36:39], v[12:15]\nds_read_b128 v[124:127], v2 offset:2688\n" \
  "v_mfma_f32_16x16x32_f16 v[12:15], v[92:95], v[40:43], v[12:15]\ns_waitcnt lgkmcnt(3)\n" \
  "v_mfma_f32_16x16x32_f16 v[12:15], v[88:91], v[44:47], v[12:15]\nds_read_b128 v[36:39], v2 offset:2752\n" \
  "v_mfma_f32_16x16x32_f16 v[16:19], v[120:123], v[44:47], v[16:19]\ns_waitcnt lgkmcnt(3)\n" \
  "v_mfma_f32_16x16x32_f16 v[12:15], v[84:87], v[28:31], v[12:15]\nds_read_b128 v[40:43], v2 offset:2816\n" \
  "v_mfma_f32_16x16x32_f16 v[16:19], v[116:119], v[28:31], v[16:19]\ns_waitcnt lgkmcnt(3)\n" \
  "v_mfma_f32_16x16x32_f16 v[12:15], v[80:83], v[32:35], v[12:15]\nds_read_b128 v[44:47], v2 offset:2880\n" \
  "v_mfma_f32_16x16x32_f16 v[16:19], v[112:115], v[32:35], v[16:19]\ns_waitcnt lgkmcnt(3)\n" \
  "v_mfma_f32_16x16x32_f16 v[12:15], v[76:79], v[124:127], v[12:15]\nds_read_b128 v[28:31], v2 offset:2944\n" \
  "v_mfma_f32_16x16x32_f16 v[16:19], v[108:111], v[124:127], v[16:19]\ns_waitcnt lgkmcnt(3)\n" \
  "v_mfma_f32_16x16x32_f16 v[12:15], v[72:75], v[36:39], v[12:15]\nds_read_b128 v[32:35], v2 offset:3008\n" \
  "v_mfma_f32_16x16x32_f16 v[16:19], v[104:107], v[36:39], v[16:19]\ns_waitcnt lgkmcnt(3)\n" \
  "v_mfma_f32_16x16x32_f16 v[12:15], v[68:71], v[40:43], v[12:15]\nv_mfma_f32_16x16x32_f16 v[16:19], v[100:103], v[40:43], v[16:19]\n" \
  "ds_read_b128 v[124:127], v2 offset:3072\ns_waitcnt lgkmcnt(3)\nv_mfma_f32_16x16x32_f16 v[12:15], v[64:67], v[44:47], v[12:15]\n" \
  "v_mfma_f32_16x16x32_f16 v[16:19], v[96:99], v[44:47], v[16:19]\nds_read_b128 v[36:39], v2 offset:3136\ns_waitcnt lgkmcnt(3)\n" \
  "v_mfma_f32_16x16x32_f16 v[12:15], v[60:63], v[28:31], v[12:15]\nds_read_b64 v[40:41], v6 offset:128\nds_read_b64 v[42:43], v6 offset:136\n" \
  "v_mfma_f32_16x16x32_f16 v[16:19], v[92:95], v[28:31], v[16:19]\nds_read_b128 v[44:47], v2 offset:3200\ns_waitcnt lgkmcnt(4)\n" \
  "v_mfma_f32_16x16x32_f16 v[12:15], v[56:59], v[32:35], v[12:15]\nv_mfma_f32_16x16x32_f16 v[16:19], v[88:91], v[32:35], v[16:19]\n" \
  "ds_read_b128 v[28:31], v2 offset:3264\nv_mfma_f32_16x16x32_f16 v[20:23], v[120:123], v[32:35], v[20:23]\nds_read_b64 v[120:121], v6 offset:192\n" \
  "ds_read_b64 v[122:123], v6 offset:200\nv_mfma_f32_16x16x32_f16 v[16:19], v[84:87], v[124:127], v[16:19]\nds_read_b128 v[32:35], v2 offset:3328\n" \
  "v_mfma_f32_16x16x32_f16 v[20:23], v[116:119], v[124:127], v[20:23]\nds_read_b64 v[116:117], v6 offset:256\nds_read_b64 v[118:119], v6 offset:264\n" \
  "s_waitcnt lgkmcnt(6)\nv_mfma_f32_16x16x32_f16 v[16:19], v[80:83], v[36:39], v[16:19]\nds_read_b128 v[124:127], v2 offset:3392\n" \
  "v_mfma_f32_16x16x32_f16 v[20:23], v[112:115], v[36:39], v[20:23]\nds_read_b64 v[112:113], v6 offset:320\nds_read_b64 v[114:115], v6 offset:328\n" \
  "v_mfma_f32_16x16x32_f16 v[16:19], v[76:79], v[44:47], v[16:19]\nds_read_b128 v[36:39], v2 offset:3456\n" \
  "v_mfma_f32_16x16x32_f16 v[20:23], v[108:111], v[44:47], v[20:23]\ns_waitcnt lgkmcnt(6)\n" \
  "v_mfma_f32_16x16x32_f16 v[16:19], v[72:75], v[28:31], v[16:19]\nds_read_b128 v[108:111], v2 offset:3520\n" \
  "v_mfma_f32_16x16x32_f16 v[20:23], v[104:107], v[28:31], v[20:23]\nv_mfma_f32_16x16x32_f16 v[16:19], v[68:71], v[32:35], v[16:19]\n" \
  "ds_read_b128 v[44:47], v2 offset:3584\nv_mfma_f32_16x16x32_f16 v[20:23], v[100:103], v[32:35], v[20:23]\nds_read_b128 v[104:107], v2 offset:3648\n" \
  "ds_read_b32 v27, v9 offset:32\ns_waitcnt lgkmcnt(4)\nv_mfma_f32_16x16x32_f16 v[16:19], v[64:67], v[124:127], v[16:19]\n" \
  "ds_read_b128 v[28:31], v2 offset:3712\nv_mfma_f32_16x16x32_f16 v[20:23], v[96:99], v[124:127], v[20:23]\nds_read_b128 v[100:103], v2 offset:3776\n" \
  "ds_read2_b32 v[32:33], v8 offset0:32 offset1:33\nds_read2_b32 v[34:35], v8 offset0:34 offset1:35\n" \
  "v_mfma_f32_16x16x32_f16 v[16:19], v[60:63], v[36:39], v[16:19]\nds_read_b128 v[96:99], v2 offset:3840\n" \
  "v_mfma_f32_16x16x32_f16 v[20:23], v[92:95], v[36:39], v[20:23]\nds_read_b128 v[124:127], v2 offset:3904\ns_waitcnt lgkmcnt(8)\n" \
  "v_mfma_f32_16x16x32_f16 v[16:19], v[56:59], v[108:111], v[16:19]\nds_read_b128 v[92:95], v2 offset:3968\ns_waitcnt lgkmcnt(7)\n" \
  "v_readfirstlane_b32 s85, v27\ns_cmp_lt_u32 s85, 4\ns_cbranch_scc1 31f\n32:\nv_mfma_f32_16x16x32_f16 v[20:23], v[88:91], v[108:111], v[20:23]\n" \
  "ds_read_b128 v[36:39], v2 offset:4032\nds_read_b64_tr_b16 v[88:89], v5 offset:512\nds_read_b64_tr_b16 v[90:91], v5 offset:544\n" \
  "v_mfma_f32_16x16x32_f16 v[20:23], v[84:87], v[44:47], v[20:23]\nds_read_b64_tr_b16 v[108:109], v5 offset:768\n" \
  "ds_read_b64_tr_b16 v[110:111], v5 offset:800\nv_mfma_f32_16x16x32_f16 v[20:23], v[80:83], v[104:107], v[20:23]\n" \
  "ds_read_b64_tr_b16 v[84:85], v5 offset:1024\nds_read_b64_tr_b16 v[86:87], v5 offset:1056\ns_waitcnt lgkmcnt(12)\n" \
  "v_mfma_f32_16x16x32_f16 v[20:23], v[76:79], v[28:31], v[20:23]\nds_read_b64_tr_b16 v[44:45], v5 offset:1280\n" \
  "ds_read_b64_tr_b16 v[46:47], v5 offset:1312\nv_mfma_f32_16x16x32_f16 v[20:23], v[72:75], v[100:103], v[20:23]\ns_waitcnt lgkmcnt(10)\n" \
  "v_mfma_f32_16x16x32_f16 v[20:23], v[68:71], v[96:99], v[20:23]\nds_read_b64_tr_b16 v[80:81], v5 offset:1536\n" \
  "ds_read_b64_tr_b16 v[82:83], v5 offset:1568\nv_mfma_f32_16x16x32_f16 v[20:23], v[64:67], v[124:127], v[20:23]\n" \
  "ds_read_b64_tr_b16 v[104:105], v5 offset:1792\nds_read_b64_tr_b16 v[106:107], v5 offset:1824\ns_waitcnt lgkmcnt(12)\n" \
  "v_mfma_f32_16x16x32_f16 v[20:23], v[60:63], v[92:95], v[20:23]\nds_read_b64_tr_b16 v[76:77], v5 offset:2048\n" \
  "ds_read_b64_tr_b16 v[78:79], v5 offset:2080\nv_mfma_f32_16x16x32_f16 v[20:23], v[56:59], v[36:39], v[20:23]\ns_waitcnt lgkmcnt(10)\n" \
  "v_mfma_f32_16x16x32_f16 v[12:15], v[88:91], v[52:55], v[12:15]\nds_read_b64_tr_b16 v[28:29], v5 offset:2304\n" \
  "ds_read_b64_tr_b16 v[30:31], v5 offset:2336\nv_mfma_f32_16x16x32_f16 v[12:15], v[108:111], v[48:51], v[12:15]\n" \
  "ds_read_b64_tr_b16 v[72:73], v5 offset:2560\nds_read_b64_tr_b16 v[74:75], v5 offset:2592\ns_waitcnt lgkmcnt(10)\n" \
  "v_mfma_f32_16x16x32_f16 v[12:15], v[84:87], v[40:43], v[12:15]\nds_read_b64_tr_b16 v[100:101], v5 offset:2816\n" \
  "ds_read_b64_tr_b16 v[102:103], v5 offset:2848\nv_mfma_f32_16x16x32_f16 v[16:19], v[84:87], v[52:55], v[16:19]\n" \
  "ds_read_b64_tr_b16 v[68:69], v7 offset:0\nds_read_b64_tr_b16 v[70:71], v7 offset:64\n" \
  "v_mfma_f32_16x16x32_f16 v[12:15], v[44:47], v[120:123], v[12:15]\nv_mfma_f32_16x16x32_f16 v[16:19], v[44:47], v[48:51], v[16:19]\n" \
  "s_waitcnt lgkmcnt(10)\nv_mfma_f32_16x16x32_f16 v[12:15], v[80:83], v[116:119], v[12:15]\nds_read2_b32 v[96:97], v8 offset0:16 offset1:17\n" \
  "ds_read2_b32 v[98:99], v8 offset0:18 offset1:19\nv_mfma_f32_16x16x32_f16 v[16:19], v[80:83], v[40:43], v[16:19]\n" \
  "ds_read2_b32 v[64:65], v8 offset0:0 offset1:1\nds_read2_b32 v[66:67], v8 offset0:2 offset1:3\n" \
  "v_mfma_f32_16x16x32_f16 v[20:23], v[80:83], v[52:55], v[20:23]\nv_mfma_f32_16x16x32_f16 v[12:15], v[104:107], v[112:115], v[12:15]\n" \
  "v_mfma_f32_16x16x32_f16 v[16:19], v[104:107], v[120:123], v[16:19]\nv_mfma_f32_16x16x32_f16 v[20:23], v[104:107], v[48:51], v[20:23]\n" \
  "s_waitcnt lgkmcnt(10)\nv_mfma_f32_16x16x32_f16 v[16:19], v[76:79], v[116:119], v[16:19]\nds_read_b64_tr_b16 v[124:125], v7 offset:512\n" \
  "ds_read_b64_tr_b16 v[126:127], v7 offset:576\nv_mfma_f32_16x16x32_f16 v[20:23], v[76:79], v[40:43], v[20:23]\n" \
  "ds_read2_b32 v[60:61], v8 offset0:48 offset1:49\nds_read2_b32 v[62:63], v8 offset0:50 offset1:51\n" \
  "v_mfma_f32_16x16x32_f16 v[16:19], v[28:31], v[112:115], v[16:19]\nv_mfma_f32_16x16x32_f16 v[20:23], v[28:31], v[120:123], v[20:23]\n" \
  "s_waitcnt lgkmcnt(10)\nv_mfma_f32_16x16x32_f16 v[20:23], v[72:75], v[116:119], v[20:23]\nds_read_b64_tr_b16 v[92:93], v7 offset:1024\n" \
  "ds_read_b64_tr_b16 v[94:95], v7 offset:1088\nv_mfma_f32_16x16x32_f16 v[20:23], v[100:103], v[112:115], v[20:23]\n" \
  "ds_read_b64_tr_b16 v[56:57], v7 offset:1536\nds_read_b64_tr_b16 v[58:59], v7 offset:1600\ns_waitcnt lgkmcnt(10)\n" \
  "v_mfma_f32_16x16x32_f16 v[12:15], v[68:71], v[32:35], v[12:15]\nv_mfma_f32_16x16x32_f16 v[16:19], v[68:71], v[96:99], v[16:19]\n" \
  "s_waitcnt lgkmcnt(6)\nv_mfma_f32_16x16x32_f16 v[20:23], v[68:71], v[64:67], v[20:23]\ns_waitcnt lgkmcnt(2)\n" \
  "v_mfma_f32_16x16x32_f16 v[12:15], v[124:127], v[60:63], v[12:15]\nv_mfma_f32_16x16x32_f16 v[16:19], v[124:127], v[32:35], v[16:19]\n" \
  "v_mfma_f32_16x16x32_f16 v[20:23], v[124:127], v[96:99], v[20:23]\nv_mfma_f32_16x16x32_f16 v[16:19], v[92:95], v[60:63], v[16:19]\n" \
  "v_mfma_f32_16x16x32_f16 v[20:23], v[92:95], v[32:35], v[20:23]\ns_waitcnt lgkmcnt(0)\n" \
  "v_mfma_f32_16x16x32_f16 v[20:23], v[56:59], v[60:63], v[20:23]\ns_nop 1\nv_cvt_pkrtz_f16_f32 v24, v12, v13\nv_cvt_pkrtz_f16_f32 v25, v14, v15\n" \
  "v_pk_add_u16 v24, v24, 4 op_sel_hi:[1,0] clamp\nv_pk_add_u16 v25, v25, 4 op_sel_hi:[1,0] clamp\nds_write_b64 v4, v[24:25] offset:5120\n" \
  "v_cvt_pkrtz_f16_f32 v24, v16, v17\nv_cvt_pkrtz_f16_f32 v25, v18, v19\nv_pk_add_u16 v24, v24, 4 op_sel_hi:[1,0] clamp\n" \
  "v_pk_add_u16 v25, v25, 4 op_sel_hi:[1,0] clamp\nds_write_b64 v4, v[24:25] offset:6144\nv_cvt_pkrtz_f16_f32 v24, v20, v21\n" \
  "v_cvt_pkrtz_f16_f32 v25, v22, v23\nv_pk_add_u16 v24, v24, 4 op_sel_hi:[1,0] clamp\nv_pk_add_u16 v25, v25, 4 op_sel_hi:[1,0] clamp\n" \
  "ds_write_b64 v4, v[24:25] offset:7168\ns_setprio 0\ns_setpc_b64 s[90:91]\n31:\ns_movk_i32 s84, 0x4000\n33:\nds_read_b32 v27, v9 offset:32\n" \
  "s_sub_u32 s84, s84, 1\ns_cbranch_scc1 32b\ns_waitcnt lgkmcnt(0)\nv_readfirstlane_b32 s85, v27\ns_cmp_lt_u32 s85, 4\ns_cbranch_scc1 33b\n" \
  "s_branch 32b\n9:\ns_cmp_eq_u32 s89, 3\ns_cbranch_scc1 3f\ns_cmp_eq_u32 s89, 4\ns_cbranch_scc1 4f\ns_cmp_eq_u32 s89, 5\ns_cbranch_scc1 5f\n" \
  "ds_read_b64 v[124:125], v3 offset:576\nds_read_b64 v[126:127], v3 offset:584\nds_read_b64 v[120:121], v3 offset:64\n" \
  "ds_read_b64 v[122:123], v3 offset:72\nds_read_b64 v[116:117], v3 offset:640\nds_read_b64 v[118:119], v3 offset:648\n" \
  "ds_read_b64 v[112:113], v3 offset:128\nds_read_b64 v[114:115], v3 offset:136\nds_read_b64 v[108:109], v3 offset:704\n" \
  "ds_read_b64 v[110:111], v3 offset:712\nds_read_b64 v[104:105], v3 offset:192\nds_read_b64 v[106:107], v3 offset:200\n" \
  "ds_read_b64 v[100:101], v3 offset:768\nds_read_b64 v[102:103], v3 offset:776\nds_read_b64 v[96:97], v3 offset:256\n" \
  "ds_read_b64 v[98:99], v3 offset:264\nds_read_b64 v[92:93], v3 offset:832\nds_read_b64 v[94:95], v3 offset:840\n" \
  "ds_read_b64 v[88:89], v3 offset:320\nds_read_b64 v[90:91], v3 offset:328\nds_read_b64 v[84:85], v3 offset:896\n" \
  "ds_read_b64 v[86:87], v3 offset:904\nds_read_b64 v[80:81], v3 offset:384\nds_read_b64 v[82:83], v3 offset:392\n" \
  "ds_read_b64 v[76:77], v3 offset:960\nds_read_b64 v[78:79], v3 offset:968\nds_read_b64 v[72:73], v3 offset:448\n" \
  "ds_read_b64 v[74:75], v3 offset:456\nds_read_b64 v[68:69], v3 offset:1024\nds_read_b64 v[70:71], v3 offset:1032\n" \
  "ds_read_b64 v[64:65], v3 offset:512\nds_read_b64 v[66:67], v3 offset:520\ns_waitcnt lgkmcnt(0)\ns_barrier\nds_read_b128 v[24:27], v2 offset:0\n" \
  "ds_read_b128 v[28:31], v2 offset:64\nds_read_b128 v[32:35], v2 offset:128\nds_read_b128 v[36:39], v2 offset:192\ns_waitcnt lgkmcnt(3)\n" \
  "v_mfma_f32_16x16x32_f16 v[12:15], v[124:127], v[24:27], 0\ns_setprio 1\nds_read_b128 v[40:43], v2 offset:256\n" \
  "v_mfma_f32_16x16x32_f16 v[16:19], v[120:123], v[24:27], 0\ns_waitcnt lgkmcnt(3)\nv_mfma_f32_16x16x32_f16 v[12:15], v[116:119], v[28:31], v[12:15]\n" \
  "ds_read_b128 v[44:47], v2 offset:320\nv_mfma_f32_16x16x32_f16 v[16:19], v[112:115], v[28:31], v[16:19]\ns_waitcnt lgkmcnt(3)\n" \
  "v_mfma_f32_16x16x32_f16 v[12:15], v[108:111], v[32:35], v[12:15]\nds_read_b128 v[48:51], v2 offset:384\n" \
  "v_mfma_f32_16x16x32_f16 v[16:19], v[104:107], v[32:35], v[16:19]\ns_waitcnt lgkmcnt(3)\n" \
  "v_mfma_f32_16x16x32_f16 v[12:15], v[100:103], v[36:39], v[12:15]\nds_read_b128 v[52:55], v2 offset:448\n" \
  "v_mfma_f32_16x16x32_f16 v[16:19], v[96:99], v[36:39], v[16:19]\ns_waitcnt lgkmcnt(3)\n" \
  "v_mfma_f32_16x16x32_f16 v[12:15], v[92:95], v[40:43], v[12:15]\nds_read_b128 v[56:59], v2 offset:512\n" \
  "v_mfma_f32_16x16x32_f16 v[16:19], v[88:91], v[40:43], v[16:19]\nds_read_b128 v[60:63], v2 offset:576\ns_waitcnt lgkmcnt(3)\n" \
  "v_mfma_f32_16x16x32_f16 v[12:15], v[84:87], v[44:47], v[12:15]\nds_read_b128 v[120:123], v2 offset:640\n" \
  "v_mfma_f32_16x16x32_f16 v[16:19], v[80:83], v[44:47], v[16:19]\nds_read_b128 v[24:27], v2 offset:704\n" \
  "v_mfma_f32_16x16x32_f16 v[12:15], v[76:79], v[48:51], v[12:15]\nds_read_b128 v[112:115], v2 offset:768\n" \
  "v_mfma_f32_16x16x32_f16 v[16:19], v[72:75], v[48:51], v[16:19]\nds_read_b128 v[28:31], v2 offset:832\ns_waitcnt lgkmcnt(5)\n" \
  "v_mfma_f32_16x16x32_f16 v[12:15], v[68:71], v[52:55], v[12:15]\nds_read_b128 v[104:107], v2 offset:896\n" \
  "v_mfma_f32_16x16x32_f16 v[16:19], v[64:67], v[52:55], v[16:19]\nds_read_b128 v[32:35], v2 offset:960\n" \
  "v_mfma_f32_16x16x32_f16 v[16:19], v[124:127], v[56:59], v[16:19]\ns_waitcnt lgkmcnt(5)\n" \
  "v_mfma_f32_16x16x32_f16 v[16:19], v[116:119], v[60:63], v[16:19]\nv_mfma_f32_16x16x32_f16 v[16:19], v[108:111], v[120:123], v[16:19]\n" \
  "s_waitcnt lgkmcnt(3)\nv_mfma_f32_16x16x32_f16 v[16:19], v[100:103], v[24:27], v[16:19]\nv_cvt_pkrtz_f16_f32 v20, v12, v13\n" \
  "v_cvt_pkrtz_f16_f32 v21, v14, v15\nv_mfma_f32_16x16x32_f16 v[16:19], v[92:95], v[112:115], v[16:19]\n" \
  "v_pk_add_u16 v20, v20, 4 op_sel_hi:[1,0] clamp\ns_waitcnt lgkmcnt(2)\nv_mfma_f32_16x16x32_f16 v[16:19], v[84:87], v[28:31], v[16:19]\n" \
  "v_pk_add_u16 v21, v21, 4 op_sel_hi:[1,0] clamp\ns_waitcnt lgkmcnt(1)\nv_mfma_f32_16x16x32_f16 v[16:19], v[76:79], v[104:107], v[16:19]\n" \
  "ds_write_b64 v4, v[20:21] offset:0\ns_waitcnt lgkmcnt(1)\nv_mfma_f32_16x16x32_f16 v[16:19], v[68:71], v[32:35], v[16:19]\ns_nop 7\n" \
  "v_cvt_pkrtz_f16_f32 v20, v16, v17\nv_cvt_pkrtz_f16_f32 v21, v18, v19\nv_pk_add_u16 v20, v20, 4 op_sel_hi:[1,0] clamp\n" \
  "v_pk_add_u16 v21, v21, 4 op_sel_hi:[1,0] clamp\nds_write_b64 v4, v[20:21] offset:1024\ns_setprio 0\ns_setpc_b64 s[90:91]\n3:\n" \
  "ds_read_b64 v[124:125], v3 offset:0\nds_read_b64 v[126:127], v3 offset:8\nds_read_b64 v[120:121], v3 offset:64\n" \
  "ds_read_b64 v[122:123], v3 offset:72\nds_read_b64 v[116:117], v3 offset:128\nds_read_b64 v[118:119], v3 offset:136\n" \
  "ds_read_b64 v[112:113], v3 offset:192\nds_read_b64 v[114:115], v3 offset:200\nds_read_b64 v[108:109], v3 offset:256\n" \
  "ds_read_b64 v[110:111], v3 offset:264\nds_read_b64 v[104:105], v3 offset:320\nds_read_b64 v[106:107], v3 offset:328\n" \
  "ds_read_b64 v[100:101], v3 offset:384\nds_read_b64 v[102:103], v3 offset:392\nds_read_b64 v[96:97], v3 offset:448\n" \
  "ds_read_b64 v[98:99], v3 offset:456\nds_read_b64 v[92:93], v3 offset:512\nds_read_b64 v[94:95], v3 offset:520\n" \
  "ds_read_b64 v[88:89], v3 offset:576\nds_read_b64 v[90:91], v3 offset:584\nds_read_b64 v[84:85], v3 offset:640\n" \
  "ds_read_b64 v[86:87], v3 offset:648\nds_read_b64 v[80:81], v3 offset:704\nds_read_b64 v[82:83], v3 offset:712\n" \
  "ds_read_b64 v[76:77], v3 offset:768\nds_read_b64 v[78:79], v3 offset:776\nds_read_b64 v[72:73], v3 offset:832\n" \
  "ds_read_b64 v[74:75], v3 offset:840\nds_read_b64 v[68:69], v3 offset:896\nds_read_b64 v[70:71], v3 offset:904\n" \
  "ds_read_b64 v[64:65], v3 offset:960\nds_read_b64 v[66:67], v3 offset:968\nds_read_b64 v[60:61], v3 offset:1024\n" \
  "ds_read_b64 v[62:63], v3 offset:1032\nds_read_b64 v[56:57], v6 offset:256\nds_read_b64 v[58:59], v6 offset:264\n" \
  "ds_read_b64 v[52:53], v6 offset:128\nds_read_b64 v[54:55], v6 offset:136\nds_read_b64 v[48:49], v6 offset:320\n" \
  "ds_read_b64 v[50:51], v6 offset:328\nds_read_b64 v[44:45], v6 offset:192\nds_read_b64 v[46:47], v6 offset:200\ns_waitcnt lgkmcnt(0)\ns_barrier\n" \
  "ds_read_b128 v[24:27], v2 offset:448\nds_read_b128 v[28:31], v2 offset:512\nds_read_b128 v[32:35], v2 offset:576\n" \
  "ds_read_b128 v[36:39], v2 offset:640\nds_read_b128 v[40:43], v2 offset:704\ns_waitcnt lgkmcnt(3)\n" \
  "v_mfma_f32_16x16x32_f16 v[12:15], v[124:127], v[24:27], 0\ns_setprio 1\nds_read_b128 v[24:27], v2 offset:768\n" \
  "v_mfma_f32_16x16x32_f16 v[12:15], v[120:123], v[28:31], v[12:15]\nds_read_b128 v[28:31], v2 offset:832\ns_waitcnt lgkmcnt(3)\n" \
  "v_mfma_f32_16x16x32_f16 v[12:15], v[116:119], v[32:35], v[12:15]\nds_read_b128 v[32:35], v2 offset:896\n" \
  "v_mfma_f32_16x16x32_f16 v[12:15], v[112:115], v[36:39], v[12:15]\nds_read_b128 v[36:39], v2 offset:960\ns_waitcnt lgkmcnt(3)\n" \
  "v_mfma_f32_16x16x32_f16 v[12:15], v[108:111], v[40:43], v[12:15]\nds_read_b128 v[40:43], v2 offset:1024\n" \
  "v_mfma_f32_16x16x32_f16 v[12:15], v[104:107], v[24:27], v[12:15]\nds_read_b128 v[24:27], v2 offset:1088\ns_waitcnt lgkmcnt(3)\n" \
  "v_mfma_f32_16x16x32_f16 v[12:15], v[100:103], v[28:31], v[12:15]\nds_read_b128 v[28:31], v2 offset:1152\n" \
  "v_mfma_f32_16x16x32_f16 v[12:15], v[96:99], v[32:35], v[12:15]\ns_waitcnt lgkmcnt(3)\n" \
  "v_mfma_f32_16x16x32_f16 v[12:15], v[92:95], v[36:39], v[12:15]\nds_read_b128 v[32:35], v2 offset:1216\n" \
  "v_mfma_f32_16x16x32_f16 v[16:19], v[124:127], v[36:39], 0\ns_waitcnt lgkmcnt(3)\nv_mfma_f32_16x16x32_f16 v[12:15], v[88:91], v[40:43], v[12:15]\n" \
  "ds_read_b128 v[124:127], v2 offset:1280\nv_mfma_f32_16x16x32_f16 v[16:19], v[120:123], v[40:43], v[16:19]\ns_waitcnt lgkmcnt(3)\n" \
  "v_mfma_f32_16x16x32_f16 v[12:15], v[84:87], v[24:27], v[12:15]\nds_read_b128 v[36:39], v2 offset:1344\n" \
  "v_mfma_f32_16x16x32_f16 v[16:19], v[116:119], v[24:27], v[16:19]\ns_waitcnt lgkmcnt(3)\n" \
  "v_mfma_f32_16x16x32_f16 v[12:15], v[80:83], v[28:31], v[12:15]\nds_read_b128 v[120:123], v2 offset:1408\n" \
  "v_mfma_f32_16x16x32_f16 v[16:19], v[112:115], v[28:31], v[16:19]\ns_waitcnt lgkmcnt(3)\n" \
  "v_mfma_f32_16x16x32_f16 v[12:15], v[76:79], v[32:35], v[12:15]\nds_read_b128 v[40:43], v2 offset:1472\n" \
  "v_mfma_f32_16x16x32_f16 v[16:19], v[108:111], v[32:35], v[16:19]\ns_waitcnt lgkmcnt(3)\n" \
  "v_mfma_f32_16x16x32_f16 v[12:15], v[72:75], v[124:127], v[12:15]\nds_read_b128 v[116:119], v2 offset:1536\n" \
  "v_mfma_f32_16x16x32_f16 v[16:19], v[104:107], v[124:127], v[16:19]\nds_read_b128 v[24:27], v2 offset:1600\ns_waitcnt lgkmcnt(3)\n" \
  "v_mfma_f32_16x16x32_f16 v[12:15], v[68:71], v[36:39], v[12:15]\nds_read_b128 v[112:115], v2 offset:1664\nds_read_b32 v23, v9 offset:32\n" \
  "v_mfma_f32_16x16x32_f16 v[16:19], v[100:103], v[36:39], v[16:19]\nds_read_b128 v[28:31], v2 offset:1728\n" \
  "v_mfma_f32_16x16x32_f16 v[12:15], v[64:67], v[120:123], v[12:15]\nds_read_b128 v[108:111], v2 offset:1792\n" \
  "v_mfma_f32_16x16x32_f16 v[16:19], v[96:99], v[120:123], v[16:19]\nds_read_b128 v[32:35], v2 offset:1856\ns_waitcnt lgkmcnt(6)\n" \
  "v_mfma_f32_16x16x32_f16 v[12:15], v[60:63], v[40:43], v[12:15]\nds_read_b128 v[104:107], v2 offset:1920\n" \
  "v_mfma_f32_16x16x32_f16 v[16:19], v[92:95], v[40:43], v[16:19]\nds_read_b128 v[124:127], v2 offset:1984\ns_waitcnt lgkmcnt(5)\n" \
  "v_readfirstlane_b32 s85, v23\ns_cmp_lt_u32 s85, 3\ns_cbranch_scc1 51f\n52:\nv_mfma_f32_16x16x32_f16 v[16:19], v[88:91], v[116:119], v[16:19]\n" \
  "ds_read_b64_tr_b16 v[100:101], v5 offset:0\nds_read_b64_tr_b16 v[102:103], v5 offset:32\n" \
  "v_mfma_f32_16x16x32_f16 v[16:19], v[84:87], v[24:27], v[16:19]\nds_read_b64_tr_b16 v[36:37], v5 offset:256\n" \
  "ds_read_b64_tr_b16 v[38:39], v5 offset:288\nv_mfma_f32_16x16x32_f16 v[16:19], v[80:83], v[112:115], v[16:19]\n" \
  "ds_read_b64_tr_b16 v[96:97], v5 offset:512\nds_read_b64_tr_b16 v[98:99], v5 offset:544\ns_waitcnt lgkmcnt(9)\n" \
  "v_mfma_f32_16x16x32_f16 v[16:19], v[76:79], v[28:31], v[16:19]\nds_read_b64_tr_b16 v[120:121], v5 offset:768\n" \
  "ds_read_b64_tr_b16 v[122:123], v5 offset:800\nv_mfma_f32_16x16x32_f16 v[16:19], v[72:75], v[108:111], v[16:19]\ns_waitcnt lgkmcnt(9)\n" \
  "v_mfma_f32_16x16x32_f16 v[16:19], v[68:71], v[32:35], v[16:19]\nv_mfma_f32_16x16x32_f16 v[16:19], v[64:67], v[104:107], v[16:19]\n" \
  "s_waitcnt lgkmcnt(6)\nv_mfma_f32_16x16x32_f16 v[16:19], v[60:63], v[124:127], v[16:19]\n" \
  "v_mfma_f32_16x16x32_f16 v[12:15], v[100:103], v[56:59], v[12:15]\nv_mfma_f32_16x16x32_f16 v[16:19], v[100:103], v[52:55], v[16:19]\n" \
  "s_waitcnt lgkmcnt(2)\nv_mfma_f32_16x16x32_f16 v[12:15], v[36:39], v[48:51], v[12:15]\n" \
  "v_mfma_f32_16x16x32_f16 v[16:19], v[36:39], v[44:47], v[16:19]\nv_mfma_f32_16x16x32_f16 v[16:19], v[96:99], v[56:59], v[16:19]\n" \
  "s_waitcnt lgkmcnt(0)\nv_mfma_f32_16x16x32_f16 v[16:19], v[120:123], v[48:51], v[16:19]\ns_nop 3\nv_cvt_pkrtz_f16_f32 v20, v12, v13\n" \
  "v_cvt_pkrtz_f16_f32 v21, v14, v15\nv_pk_add_u16 v20, v20, 4 op_sel_hi:[1,0] clamp\nv_pk_add_u16 v21, v21, 4 op_sel_hi:[1,0] clamp\n" \
  "ds_write_b64 v4, v[20:21] offset:2048\nv_cvt_pkrtz_f16_f32 v20, v16, v17\nv_cvt_pkrtz_f16_f32 v21, v18, v19\n" \
  "v_pk_add_u16 v20, v20, 4 op_sel_hi:[1,0] clamp\nv_pk_add_u16 v21, v21, 4 op_sel_hi:[1,0] clamp\nds_write_b64 v4, v[20:21] offset:3072\n" \
  "s_setprio 0\ns_setpc_b64 s[90:91]\n51:\ns_movk_i32 s84, 0x4000\n53:\nds_read_b32 v23, v9 offset:32\ns_sub_u32 s84, s84, 1\ns_cbranch_scc1 52b\n" \
  "s_waitcnt lgkmcnt(0)\nv_readfirstlane_b32 s85, v23\ns_cmp_lt_u32 s85, 3\ns_cbranch_scc1 53b\ns_branch 52b\n4:\n" \
  "ds_read_b64 v[124:125], v3 offset:1088\nds_read_b64 v[126:127], v3 offset:1096\nds_read_b64 v[120:121], v3 offset:0\n" \
  "ds_read_b64 v[122:123], v3 offset:8\nds_read_b64 v[116:117], v3 offset:64\nds_read_b64 v[118:119], v3 offset:72\n" \
  "ds_read_b64 v[112:113], v3 offset:128\nds_read_b64 v[114:115], v3 offset:136\nds_read_b64 v[108:109], v3 offset:192\n" \
  "ds_read_b64 v[110:111], v3 offset:200\nds_read_b64 v[104:105], v3 offset:256\nds_read_b64 v[106:107], v3 offset:264\n" \
  "ds_read_b64 v[100:101], v3 offset:320\nds_read_b64 v[102:103], v3 offset:328\nds_read_b64 v[96:97], v3 offset:384\n" \
  "ds_read_b64 v[98:99], v3 offset:392\nds_read_b64 v[92:93], v3 offset:448\nds_read_b64 v[94:95], v3 offset:456\n" \
  "ds_read_b64 v[88:89], v3 offset:512\nds_read_b64 v[90:91], v3 offset:520\nds_read_b64 v[84:85], v3 offset:576\n" \
  "ds_read_b64 v[86:87], v3 offset:584\nds_read_b64 v[80:81], v3 offset:640\nds_read_b64 v[82:83], v3 offset:648\n" \
  "ds_read_b64 v[76:77], v3 offset:704\nds_read_b64 v[78:79], v3 offset:712\nds_read_b64 v[72:73], v3 offset:768\n" \
  "ds_read_b64 v[74:75], v3 offset:776\nds_read_b64 v[68:69], v3 offset:832\nds_read_b64 v[70:71], v3 offset:840\n" \
  "ds_read_b64 v[64:65], v3 offset:896\nds_read_b64 v[66:67], v3 offset:904\nds_read_b64 v[60:61], v3 offset:960\n" \
  "ds_read_b64 v[62:63], v3 offset:968\nds_read_b64 v[56:57], v3 offset:1024\nds_read_b64 v[58:59], v3 offset:1032\n" \
  "ds_read_b64 v[52:53], v6 offset:0\nds_read_b64 v[54:55], v6 offset:8\nds_read_b64 v[48:49], v6 offset:64\nds_read_b64 v[50:51], v6 offset:72\n" \
  "ds_read_b64 v[44:45], v6 offset:128\nds_read_b64 v[46:47], v6 offset:136\nds_read_b64 v[40:41], v6 offset:192\n" \
  "ds_read_b64 v[42:43], v6 offset:200\ns_waitcnt lgkmcnt(0)\ns_barrier\nds_read_b128 v[24:27], v2 offset:448\nds_read_b128 v[28:31], v2 offset:960\n" \
  "ds_read_b128 v[32:35], v2 offset:1472\nds_read_b128 v[36:39], v2 offset:1536\ns_waitcnt lgkmcnt(3)\n" \
  "v_mfma_f32_16x16x32_f16 v[12:15], v[124:127], v[24:27], 0\ns_setprio 1\nds_read_b128 v[24:27], v2 offset:1600\ns_waitcnt lgkmcnt(3)\n" \
  "v_mfma_f32_16x16x32_f16 v[16:19], v[124:127], v[28:31], 0\nds_read_b128 v[124:127], v2 offset:1664\ns_waitcnt lgkmcnt(3)\n" \
  "v_mfma_f32_16x16x32_f16 v[12:15], v[120:123], v[32:35], v[12:15]\nds_read_b128 v[28:31], v2 offset:1728\ns_waitcnt lgkmcnt(3)\n" \
  "v_mfma_f32_16x16x32_f16 v[12:15], v[116:119], v[36:39], v[12:15]\nds_read_b128 v[32:35], v2 offset:1792\ns_waitcnt lgkmcnt(3)\n" \
  "v_mfma_f32_16x16x32_f16 v[12:15], v[112:115], v[24:27], v[12:15]\nds_read_b128 v[36:39], v2 offset:1856\ns_waitcnt lgkmcnt(3)\n" \
  "v_mfma_f32_16x16x32_f16 v[12:15], v[108:111], v[124:127], v[12:15]\nds_read_b128 v[24:27], v2 offset:1920\ns_waitcnt lgkmcnt(3)\n" \
  "v_mfma_f32_16x16x32_f16 v[12:15], v[104:107], v[28:31], v[12:15]\nds_read_b128 v[124:127], v2 offset:1984\ns_waitcnt lgkmcnt(3)\n" \
  "v_mfma_f32_16x16x32_f16 v[12:15], v[100:103], v[32:35], v[12:15]\nds_read_b128 v[28:31], v2 offset:2048\ns_waitcnt lgkmcnt(3)\n" \
  "v_mfma_f32_16x16x32_f16 v[12:15], v[96:99], v[36:39], v[12:15]\nds_read_b128 v[32:35], v2 offset:2112\ns_waitcnt lgkmcnt(3)\n" \
  "v_mfma_f32_16x16x32_f16 v[12:15], v[92:95], v[24:27], v[12:15]\nds_read_b128 v[36:39], v2 offset:2176\ns_waitcnt lgkmcnt(3)\n" \
  "v_mfma_f32_16x16x32_f16 v[12:15], v[88:91], v[124:127], v[12:15]\nds_read_b128 v[24:27], v2 offset:2240\n" \
  "v_mfma_f32_16x16x32_f16 v[16:19], v[120:123], v[124:127], v[16:19]\nds_read_b64 v[120:121], v6 offset:256\nds_read_b64 v[122:123], v6 offset:264\n" \
  "s_waitcnt lgkmcnt(4)\nv_mfma_f32_16x16x32_f16 v[12:15], v[84:87], v[28:31], v[12:15]\nds_read_b128 v[124:127], v2 offset:2304\n" \
  "v_mfma_f32_16x16x32_f16 v[16:19], v[116:119], v[28:31], v[16:19]\nds_read_b64 v[116:117], v6 offset:320\nds_read_b64 v[118:119], v6 offset:328\n" \
  "v_mfma_f32_16x16x32_f16 v[12:15], v[80:83], v[32:35], v[12:15]\nds_read_b128 v[28:31], v2 offset:2368\n" \
  "v_mfma_f32_16x16x32_f16 v[16:19], v[112:115], v[32:35], v[16:19]\ns_waitcnt lgkmcnt(6)\n" \
  "v_mfma_f32_16x16x32_f16 v[12:15], v[76:79], v[36:39], v[12:15]\nds_read_b128 v[112:115], v2 offset:2432\n" \
  "v_mfma_f32_16x16x32_f16 v[16:19], v[108:111], v[36:39], v[16:19]\nds_read2_b32 v[32:33], v8 offset0:48 offset1:49\n" \
  "ds_read2_b32 v[34:35], v8 offset0:50 offset1:51\nv_mfma_f32_16x16x32_f16 v[12:15], v[72:75], v[24:27], v[12:15]\n" \
  "ds_read_b128 v[108:111], v2 offset:2496\nv_mfma_f32_16x16x32_f16 v[16:19], v[104:107], v[24:27], v[16:19]\n" \
  "ds_read2_b32 v[36:37], v8 offset0:32 offset1:33\nds_read2_b32 v[38:39], v8 offset0:34 offset1:35\ns_waitcnt lgkmcnt(6)\n" \
  "v_mfma_f32_16x16x32_f16 v[12:15], v[68:71], v[124:127], v[12:15]\nds_read_b128 v[104:107], v2 offset:2560\n" \
  "v_mfma_f32_16x16x32_f16 v[16:19], v[100:103], v[124:127], v[16:19]\nds_read_b128 v[24:27], v2 offset:2624\n" \
  "v_mfma_f32_16x16x32_f16 v[12:15], v[64:67], v[28:31], v[12:15]\nds_read_b128 v[100:103], v2 offset:2688\nds_read_b32 v23, v9 offset:32\n" \
  "v_mfma_f32_16x16x32_f16 v[16:19], v[96:99], v[28:31], v[16:19]\nds_read_b128 v[124:127], v2 offset:2752\ns_waitcnt lgkmcnt(7)\n" \
  "v_mfma_f32_16x16x32_f16 v[12:15], v[60:63], v[112:115], v[12:15]\nds_read_b128 v[96:99], v2 offset:2816\n" \
  "v_mfma_f32_16x16x32_f16 v[16:19], v[92:95], v[112:115], v[16:19]\nds_read_b128 v[28:31], v2 offset:2880\n" \
  "v_mfma_f32_16x16x32_f16 v[12:15], v[56:59], v[108:111], v[12:15]\nds_read_b128 v[92:95], v2 offset:2944\n" \
  "v_mfma_f32_16x16x32_f16 v[16:19], v[88:91], v[108:111], v[16:19]\nds_read_b128 v[112:115], v2 offset:3008\ns_waitcnt lgkmcnt(5)\n" \
  "v_readfirstlane_b32 s85, v23\ns_cmp_lt_u32 s85, 3\ns_cbranch_scc1 61f\n62:\nv_mfma_f32_16x16x32_f16 v[16:19], v[84:87], v[104:107], v[16:19]\n" \
  "ds_read_b64_tr_b16 v[88:89], v5 offset:0\nds_read_b64_tr_b16 v[90:91], v5 offset:32\n" \
  "v_mfma_f32_16x16x32_f16 v[16:19], v[80:83], v[24:27], v[16:19]\nds_read_b64_tr_b16 v[108:109], v5 offset:256\n" \
  "ds_read_b64_tr_b16 v[110:111], v5 offset:288\nv_mfma_f32_16x16x32_f16 v[16:19], v[76:79], v[100:103], v[16:19]\n" \
  "ds_read_b64_tr_b16 v[84:85], v5 offset:512\nds_read_b64_tr_b16 v[86:87], v5 offset:544\ns_waitcnt lgkmcnt(9)\n" \
  "v_mfma_f32_16x16x32_f16 v[16:19], v[72:75], v[124:127], v[16:19]\nds_read_b64_tr_b16 v[104:105], v5 offset:768\n" \
  "ds_read_b64_tr_b16 v[106:107], v5 offset:800\nv_mfma_f32_16x16x32_f16 v[16:19], v[68:71], v[96:99], v[16:19]\n" \
  "ds_read_b64_tr_b16 v[80:81], v5 offset:1024\nds_read_b64_tr_b16 v[82:83], v5 offset:1056\ns_waitcnt lgkmcnt(11)\n" \
  "v_mfma_f32_16x16x32_f16 v[16:19], v[64:67], v[28:31], v[16:19]\nds_read_b64_tr_b16 v[24:25], v5 offset:1280\n" \
  "ds_read_b64_tr_b16 v[26:27], v5 offset:1312\nv_mfma_f32_16x16x32_f16 v[16:19], v[60:63], v[92:95], v[16:19]\n" \
  "ds_read_b64_tr_b16 v[76:77], v5 offset:1536\nds_read_b64_tr_b16 v[78:79], v5 offset:1568\ns_waitcnt lgkmcnt(12)\n" \
  "v_mfma_f32_16x16x32_f16 v[16:19], v[56:59], v[112:115], v[16:19]\nds_read_b64_tr_b16 v[100:101], v5 offset:1792\n" \
  "ds_read_b64_tr_b16 v[102:103], v5 offset:1824\nv_mfma_f32_16x16x32_f16 v[12:15], v[88:91], v[52:55], v[12:15]\ns_waitcnt lgkmcnt(10)\n" \
  "v_mfma_f32_16x16x32_f16 v[12:15], v[108:111], v[48:51], v[12:15]\nds_read_b64_tr_b16 v[72:73], v7 offset:0\n" \
  "ds_read_b64_tr_b16 v[74:75], v7 offset:64\nv_mfma_f32_16x16x32_f16 v[12:15], v[84:87], v[44:47], v[12:15]\n" \
  "ds_read_b64_tr_b16 v[124:125], v7 offset:512\nds_read_b64_tr_b16 v[126:127], v7 offset:576\n" \
  "v_mfma_f32_16x16x32_f16 v[16:19], v[84:87], v[52:55], v[16:19]\ns_waitcnt lgkmcnt(10)\n" \
  "v_mfma_f32_16x16x32_f16 v[12:15], v[104:107], v[40:43], v[12:15]\nv_mfma_f32_16x16x32_f16 v[16:19], v[104:107], v[48:51], v[16:19]\n" \
  "v_mfma_f32_16x16x32_f16 v[12:15], v[80:83], v[120:123], v[12:15]\nv_mfma_f32_16x16x32_f16 v[16:19], v[80:83], v[44:47], v[16:19]\n" \
  "s_waitcnt lgkmcnt(6)\nv_mfma_f32_16x16x32_f16 v[12:15], v[24:27], v[116:119], v[12:15]\n" \
  "v_mfma_f32_16x16x32_f16 v[16:19], v[24:27], v[40:43], v[16:19]\nv_mfma_f32_16x16x32_f16 v[16:19], v[76:79], v[120:123], v[16:19]\n" \
  "s_waitcnt lgkmcnt(2)\nv_mfma_f32_16x16x32_f16 v[16:19], v[100:103], v[116:119], v[16:19]\n" \
  "v_mfma_f32_16x16x32_f16 v[12:15], v[72:75], v[32:35], v[12:15]\nv_mfma_f32_16x16x32_f16 v[16:19], v[72:75], v[36:39], v[16:19]\n" \
  "s_waitcnt lgkmcnt(0)\nv_mfma_f32_16x16x32_f16 v[16:19], v[124:127], v[32:35], v[16:19]\ns_nop 4\nv_cvt_pkrtz_f16_f32 v20, v12, v13\n" \
  "v_cvt_pkrtz_f16_f32 v21, v14, v15\nv_pk_add_u16 v20, v20, 4 op_sel_hi:[1,0] clamp\nv_pk_add_u16 v21, v21, 4 op_sel_hi:[1,0] clamp\n" \
  "ds_write_b64 v4, v[20:21] offset:4096\nv_cvt_pkrtz_f16_f32 v20, v16, v17\nv_cvt_pkrtz_f16_f32 v21, v18, v19\n" \
  "v_pk_add_u16 v20, v20, 4 op_sel_hi:[1,0] clamp\nv_pk_add_u16 v21, v21, 4 op_sel_hi:[1,0] clamp\nds_write_b64 v4, v[20:21] offset:5120\n" \
  "s_setprio 0\ns_setpc_b64 s[90:91]\n61:\ns_movk_i32 s84, 0x4000\n63:\nds_read_b32 v23, v9 offset:32\ns_sub_u32 s84, s84, 1\ns_cbranch_scc1 62b\n" \
  "s_waitcnt lgkmcnt(0)\nv_readfirstlane_b32 s85, v23\ns_cmp_lt_u32 s85, 3\ns_cbranch_scc1 63b\ns_branch 62b\n5:\n" \
  "ds_read_b64 v[124:125], v3 offset:1088\nds_read_b64 v[126:127], v3 offset:1096\nds_read_b64 v[120:121], v3 offset:0\n" \
  "ds_read_b64 v[122:123], v3 offset:8\nds_read_b64 v[116:117], v3 offset:64\nds_read_b64 v[118:119], v3 offset:72\n" \
  "ds_read_b64 v[112:113], v3 offset:128\nds_read_b64 v[114:115], v3 offset:136\nds_read_b64 v[108:109], v3 offset:192\n" \
  "ds_read_b64 v[110:111], v3 offset:200\nds_read_b64 v[104:105], v3 offset:256\nds_read_b64 v[106:107], v3 offset:264\n" \
  "ds_read_b64 v[100:101], v3 offset:320\nds_read_b64 v[102:103], v3 offset:328\nds_read_b64 v[96:97], v3 offset:384\n" \
  "ds_read_b64 v[98:99], v3 offset:392\nds_read_b64 v[92:93], v3 offset:448\nds_read_b64 v[94:95], v3 offset:456\n" \
  "ds_read_b64 v[88:89], v3 offset:512\nds_read_b64 v[90:91], v3 offset:520\nds_read_b64 v[84:85], v3 offset:576\n" \
  "ds_read_b64 v[86:87], v3 offset:584\nds_read_b64 v[80:81], v3 offset:640\nds_read_b64 v[82:83], v3 offset:648\n" \
  "ds_read_b64 v[76:77], v3 offset:704\nds_read_b64 v[78:79], v3 offset:712\nds_read_b64 v[72:73], v3 offset:768\n" \
  "ds_read_b64 v[74:75], v3 offset:776\nds_read_b64 v[68:69], v3 offset:832\nds_read_b64 v[70:71], v3 offset:840\n" \
  "ds_read_b64 v[64:65], v3 offset:896\nds_read_b64 v[66:67], v3 offset:904\nds_read_b64 v[60:61], v3 offset:960\n" \
  "ds_read_b64 v[62:63], v3 offset:968\nds_read_b64 v[56:57], v3 offset:1024\nds_read_b64 v[58:59], v3 offset:1032\n" \
  "ds_read_b64 v[52:53], v6 offset:0\nds_read_b64 v[54:55], v6 offset:8\nds_read_b64 v[48:49], v6 offset:64\nds_read_b64 v[50:51], v6 offset:72\n" \
  "ds_read_b64 v[44:45], v6 offset:128\nds_read_b64 v[46:47], v6 offset:136\nds_read_b64 v[40:41], v6 offset:192\n" \
  "ds_read_b64 v[42:43], v6 offset:200\ns_waitcnt lgkmcnt(0)\ns_barrier\nds_read_b128 v[24:27], v2 offset:1472\n" \
  "ds_read_b128 v[28:31], v2 offset:1984\nds_read_b128 v[32:35], v2 offset:2496\nds_read_b128 v[36:39], v2 offset:2560\ns_waitcnt lgkmcnt(3)\n" \
  "v_mfma_f32_16x16x32_f16 v[12:15], v[124:127], v[24:27], 0\ns_setprio 1\nds_read_b128 v[24:27], v2 offset:2624\ns_waitcnt lgkmcnt(3)\n" \
  "v_mfma_f32_16x16x32_f16 v[16:19], v[124:127], v[28:31], 0\nds_read_b128 v[124:127], v2 offset:2688\ns_waitcnt lgkmcnt(3)\n" \
  "v_mfma_f32_16x16x32_f16 v[12:15], v[120:123], v[32:35], v[12:15]\nds_read_b128 v[28:31], v2 offset:2752\ns_waitcnt lgkmcnt(3)\n" \
  "v_mfma_f32_16x16x32_f16 v[12:15], v[116:119], v[36:39], v[12:15]\nds_read_b128 v[32:35], v2 offset:2816\ns_waitcnt lgkmcnt(3)\n" \
  "v_mfma_f32_16x16x32_f16 v[12:15], v[112:115], v[24:27], v[12:15]\nds_read_b128 v[36:39], v2 offset:2880\ns_waitcnt lgkmcnt(3)\n" \
  "v_mfma_f32_16x16x32_f16 v[12:15], v[108:111], v[124:127], v[12:15]\nds_read_b128 v[24:27], v2 offset:2944\ns_waitcnt lgkmcnt(3)\n" \
  "v_mfma_f32_16x16x32_f16 v[12:15], v[104:107], v[28:31], v[12:15]\nds_read_b128 v[124:127], v2 offset:3008\ns_waitcnt lgkmcnt(3)\n" \
  "v_mfma_f32_16x16x32_f16 v[12:15], v[100:103], v[32:35], v[12:15]\nds_read_b128 v[28:31], v2 offset:3072\ns_waitcnt lgkmcnt(3)\n" \
  "v_mfma_f32_16x16x32_f16 v[12:15], v[96:99], v[36:39], v[12:15]\nds_read_b128 v[32:35], v2 offset:3136\ns_waitcnt lgkmcnt(3)\n" \
  "v_mfma_f32_16x16x32_f16 v[12:15], v[92:95], v[24:27], v[12:15]\nds_read_b128 v[36:39], v2 offset:3200\ns_waitcnt lgkmcnt(3)\n" \
  "v_mfma_f32_16x16x32_f16 v[12:15], v[88:91], v[124:127], v[12:15]\nds_read_b128 v[24:27], v2 offset:3264\n" \
  "v_mfma_f32_16x16x32_f16 v[16:19], v[120:123], v[124:127], v[16:19]\nds_read_b64 v[120:121], v6 offset:256\nds_read_b64 v[122:123], v6 offset:264\n" \
  "s_waitcnt lgkmcnt(4)\nv_mfma_f32_16x16x32_f16 v[12:15], v[84:87], v[28:31], v[12:15]\nds_read_b128 v[124:127], v2 offset:3328\n" \
  "v_mfma_f32_16x16x32_f16 v[16:19], v[116:119], v[28:31], v[16:19]\nds_read_b64 v[116:117], v6 offset:320\nds_read_b64 v[118:119], v6 offset:328\n" \
  "v_mfma_f32_16x16x32_f16 v[12:15], v[80:83], v[32:35], v[12:15]\nds_read_b128 v[28:31], v2 offset:3392\n" \
  "v_mfma_f32_16x16x32_f16 v[16:19], v[112:115], v[32:35], v[16:19]\ns_waitcnt lgkmcnt(6)\n" \
  "v_mfma_f32_16x16x32_f16 v[12:15], v[76:79], v[36:39], v[12:15]\nds_read_b128 v[112:115], v2 offset:3456\n" \
  "v_mfma_f32_16x16x32_f16 v[16:19], v[108:111], v[36:39], v[16:19]\nds_read2_b32 v[32:33], v8 offset0:16 offset1:17\n" \
  "ds_read2_b32 v[34:35], v8 offset0:18 offset1:19\nv_mfma_f32_16x16x32_f16 v[12:15], v[72:75], v[24:27], v[12:15]\n" \
  "ds_read_b128 v[108:111], v2 offset:3520\nv_mfma_f32_16x16x32_f16 v[16:19], v[104:107], v[24:27], v[16:19]\n" \
  "ds_read2_b32 v[36:37], v8 offset0:0 offset1:1\nds_read2_b32 v[38:39], v8 offset0:2 offset1:3\ns_waitcnt lgkmcnt(6)\n" \
  "v_mfma_f32_16x16x32_f16 v[12:15], v[68:71], v[124:127], v[12:15]\nds_read_b128 v[104:107], v2 offset:3584\n" \
  "v_mfma_f32_16x16x32_f16 v[16:19], v[100:103], v[124:127], v[16:19]\nds_read_b128 v[24:27], v2 offset:3648\n" \
  "ds_read2_b32 v[100:101], v8 offset0:32 offset1:33\nds_read2_b32 v[102:103], v8 offset0:34 offset1:35\n" \
  "v_mfma_f32_16x16x32_f16 v[12:15], v[64:67], v[28:31], v[12:15]\nds_read_b128 v[124:127], v2 offset:3712\nds_read_b32 v23, v9 offset:32\n" \
  "v_mfma_f32_16x16x32_f16 v[16:19], v[96:99], v[28:31], v[16:19]\nds_read_b128 v[96:99], v2 offset:3776\ns_waitcnt lgkmcnt(9)\n" \
  "v_mfma_f32_16x16x32_f16 v[12:15], v[60:63], v[112:115], v[12:15]\nds_read_b128 v[28:31], v2 offset:3840\n" \
  "v_mfma_f32_16x16x32_f16 v[16:19], v[92:95], v[112:115], v[16:19]\nds_read_b128 v[92:95], v2 offset:3904\n" \
  "v_mfma_f32_16x16x32_f16 v[12:15], v[56:59], v[108:111], v[12:15]\nds_read_b128 v[112:115], v2 offset:3968\n" \
  "v_mfma_f32_16x16x32_f16 v[16:19], v[88:91], v[108:111], v[16:19]\nds_read_b128 v[88:91], v2 offset:4032\ns_waitcnt lgkmcnt(5)\n" \
  "v_readfirstlane_b32 s85, v23\ns_cmp_lt_u32 s85, 3\ns_cbranch_scc1 71f\n72:\nv_mfma_f32_16x16x32_f16 v[16:19], v[84:87], v[104:107], v[16:19]\n" \
  "ds_read_b64_tr_b16 v[108:109], v5 offset:1024\nds_read_b64_tr_b16 v[110:111], v5 offset:1056\n" \
  "v_mfma_f32_16x16x32_f16 v[16:19], v[80:83], v[24:27], v[16:19]\nds_read_b64_tr_b16 v[84:85], v5 offset:1280\n" \
  "ds_read_b64_tr_b16 v[86:87], v5 offset:1312\nv_mfma_f32_16x16x32_f16 v[16:19], v[76:79], v[124:127], v[16:19]\n" \
  "ds_read_b64_tr_b16 v[104:105], v5 offset:1536\nds_read_b64_tr_b16 v[106:107], v5 offset:1568\ns_waitcnt lgkmcnt(9)\n" \
  "v_mfma_f32_16x16x32_f16 v[16:19], v[72:75], v[96:99], v[16:19]\nds_read_b64_tr_b16 v[80:81], v5 offset:1792\n" \
  "ds_read_b64_tr_b16 v[82:83], v5 offset:1824\nv_mfma_f32_16x16x32_f16 v[16:19], v[68:71], v[28:31], v[16:19]\n" \
  "ds_read_b64_tr_b16 v[24:25], v5 offset:2048\nds_read_b64_tr_b16 v[26:27], v5 offset:2080\ns_waitcnt lgkmcnt(11)\n" \
  "v_mfma_f32_16x16x32_f16 v[16:19], v[64:67], v[92:95], v[16:19]\nds_read_b64_tr_b16 v[76:77], v5 offset:2304\n" \
  "ds_read_b64_tr_b16 v[78:79], v5 offset:2336\nv_mfma_f32_16x16x32_f16 v[16:19], v[60:63], v[112:115], v[16:19]\n" \
  "ds_read_b64_tr_b16 v[124:125], v5 offset:2560\nds_read_b64_tr_b16 v[126:127], v5 offset:2592\ns_waitcnt lgkmcnt(12)\n" \
  "v_mfma_f32_16x16x32_f16 v[16:19], v[56:59], v[88:91], v[16:19]\nds_read_b64_tr_b16 v[72:73], v5 offset:2816\n" \
  "ds_read_b64_tr_b16 v[74:75], v5 offset:2848\nv_mfma_f32_16x16x32_f16 v[12:15], v[108:111], v[52:55], v[12:15]\ns_waitcnt lgkmcnt(10)\n" \
  "v_mfma_f32_16x16x32_f16 v[12:15], v[84:87], v[48:51], v[12:15]\nds_read_b64_tr_b16 v[96:97], v7 offset:0\n" \
  "ds_read_b64_tr_b16 v[98:99], v7 offset:64\nv_mfma_f32_16x16x32_f16 v[12:15], v[104:107], v[44:47], v[12:15]\n" \
  "ds_read_b64_tr_b16 v[68:69], v7 offset:512\nds_read_b64_tr_b16 v[70:71], v7 offset:576\n" \
  "v_mfma_f32_16x16x32_f16 v[16:19], v[104:107], v[52:55], v[16:19]\ns_waitcnt lgkmcnt(10)\n" \
  "v_mfma_f32_16x16x32_f16 v[12:15], v[80:83], v[40:43], v[12:15]\nds_read_b64_tr_b16 v[28:29], v7 offset:1024\n" \
  "ds_read_b64_tr_b16 v[30:31], v7 offset:1088\nv_mfma_f32_16x16x32_f16 v[16:19], v[80:83], v[48:51], v[16:19]\n" \
  "ds_read2_b32 v[64:65], v8 offset0:48 offset1:49\nds_read2_b32 v[66:67], v8 offset0:50 offset1:51\n" \
  "v_mfma_f32_16x16x32_f16 v[12:15], v[24:27], v[120:123], v[12:15]\nv_mfma_f32_16x16x32_f16 v[16:19], v[24:27], v[44:47], v[16:19]\n" \
  "s_waitcnt lgkmcnt(10)\nv_mfma_f32_16x16x32_f16 v[12:15], v[76:79], v[116:119], v[12:15]\nds_read_b64_tr_b16 v[92:93], v7 offset:1536\n" \
  "ds_read_b64_tr_b16 v[94:95], v7 offset:1600\nv_mfma_f32_16x16x32_f16 v[16:19], v[76:79], v[40:43], v[16:19]\n" \
  "v_mfma_f32_16x16x32_f16 v[16:19], v[124:127], v[120:123], v[16:19]\ns_waitcnt lgkmcnt(8)\n" \
  "v_mfma_f32_16x16x32_f16 v[16:19], v[72:75], v[116:119], v[16:19]\nv_mfma_f32_16x16x32_f16 v[12:15], v[96:99], v[32:35], v[12:15]\n" \
  "v_mfma_f32_16x16x32_f16 v[16:19], v[96:99], v[36:39], v[16:19]\ns_waitcnt lgkmcnt(4)\n" \
  "v_mfma_f32_16x16x32_f16 v[12:15], v[68:71], v[100:103], v[12:15]\nv_mfma_f32_16x16x32_f16 v[16:19], v[68:71], v[32:35], v[16:19]\n" \
  "s_waitcnt lgkmcnt(2)\nv_mfma_f32_16x16x32_f16 v[12:15], v[28:31], v[64:67], v[12:15]\n" \
  "v_mfma_f32_16x16x32_f16 v[16:19], v[28:31], v[100:103], v[16:19]\ns_waitcnt lgkmcnt(0)\n" \
  "v_mfma_f32_16x16x32_f16 v[16:19], v[92:95], v[64:67], v[16:19]\ns_nop 4\nv_cvt_pkrtz_f16_f32 v20, v12, v13\nv_cvt_pkrtz_f16_f32 v21, v14, v15\n" \
  "v_pk_add_u16 v20, v20, 4 op_sel_hi:[1,0] clamp\nv_pk_add_u16 v21, v21, 4 op_sel_hi:[1,0] clamp\nds_write_b64 v4, v[20:21] offset:6144\n" \
  "v_cvt_pkrtz_f16_f32 v20, v16, v17\nv_cvt_pkrtz_f16_f32 v21, v18, v19\nv_pk_add_u16 v20, v20, 4 op_sel_hi:[1,0] clamp\n" \
  "v_pk_add_u16 v21, v21, 4 op_sel_hi:[1,0] clamp\nds_write_b64 v4, v[20:21] offset:7168\ns_setprio 0\ns_setpc_b64 s[90:91]\n71:\n" \
  "s_movk_i32 s84, 0x4000\n73:\nds_read_b32 v23, v9 offset:32\ns_sub_u32 s84, s84, 1\ns_cbranch_scc1 72b\ns_waitcnt lgkmcnt(0)\n" \
  "v_readfirstlane_b32 s85, v23\ns_cmp_lt_u32 s85, 3\ns_cbranch_scc1 73b\ns_branch 72b\n"
#define CONV_CLOB "memory", "scc", "s84", "s85", "s90", "s91", "v12", "v13", "v14", "v15", "v16", "v17", "v18", "v19", "v20", "v21", "v22", "v23", "v24", "v25", "v26", "v27", "v28", "v29", "v30", "v31", "v32", "v33", "v34", "v35", "v36", "v37", "v38", "v39", "v40", "v41", "v42", "v43", "v44", "v45", "v46", "v47", "v48", "v49", "v50", "v51", "v52", "v53", "v54", "v55", "v56", "v57", "v58", "v59", "v60", "v61", "v62", "v63", "v64", "v65", "v66", "v67", "v68", "v69", "v70", "v71", "v72", "v73", "v74", "v75", "v76", "v77", "v78", "v79", "v80", "v81", "v82", "v83", "v84", "v85", "v86", "v87", "v88", "v89", "v90", "v91", "v92", "v93", "v94", "v95", "v96", "v97", "v98", "v99", "v100", "v101", "v102", "v103", "v104", "v105", "v106", "v107", "v108", "v109", "v110", "v111", "v112", "v113", "v114", "v115", "v116", "v117", "v118", "v119", "v120", "v121", "v122", "v123", "v124", "v125", "v126", "v127"
constexpr int TDP = 1216, T_3 = 4 * TDP, T_4 = T_3 + 512, TB = T_4 + 320;
constexpr int C_U = 480, UB = C_U + 4096 + 96, C_G5 = 0, C_G3 = 3200, C_G9 = 6400, C_G7 = C_G9 + 2096, GB = C_G7 + 2080 + 112;
constexpr int O_FL = 2 * TB, O_UL = O_FL + 64, O_IM = O_UL + 2 * UB, O_ST = O_IM + 2 * GB;
constexpr int X_TB = 10240, X_UL = X_TB + TB, X_IM = X_UL + UB, X_ST = X_IM + GB;
constexpr int X_WR = X_IM + 128 * IP;
#define X_RW(w) (LW + 9216 * (2 + ((w) >> 1)) + 2560 * ((w) & 1))
constexpr int RP = 1216;
constexpr int LW = 36864 + 512, LA = LW, LM = LW + 36864;
constexpr int L_SC = LM + 2048, SMEM = O_IM + 256 * IP;
static_assert(L_SC + 1152 <= SMEM && O_ST + 8192 <= SMEM && X_IM + 128 * IP <= LM && X_ST + 8192 <= LM && UB % 128 == 64 && GB % 128 == 64 && SMEM <= 79872, "");
struct TOps { uint32_t xa, xb, ya, pd, pf, pg, pe; };
__device__ __forceinline__ void table_ops(uint32_t im, uint32_t tb, int ct, int lane, TOps& t) {
  const int j = lane & 15, kq = lane >> 4, q4 = (lane >> 2) & 3, p4 = lane & 3, jj = j + 16 * ct;
  const uint32_t sa = im + (uint32_t)(IP * (lane >> 2));
  t.xa = sa + (uint32_t)(I_X + 8 * p4); t.xb = sa + (uint32_t)(I_X + 32 + 8 * (p4 & 1)); t.ya = sa + (uint32_t)(32 * ct + 8 * p4);
  t.pd = tb + (uint32_t)(2 * (541 - (8 * jj + 4 * (kq & 1) + 256 * (kq >> 1))));
  t.pf = tb + (uint32_t)((kq & 1) ? T_4 + 2 * (63 - jj) : T_3 + 2 * (127 - 2 * jj));
  t.pg = t.pf - ((kq & 1) ? 64 : 2);
  t.pe = tb + (uint32_t)(p4 * (TDP + 2) + ((lane < 12) ? 2 * (32 - q4) : 2 * (572 + (q4 & 1))));
  asm volatile("" : "+v"(t.xa), "+v"(t.xb), "+v"(t.ya), "+v"(t.pd), "+v"(t.pf), "+v"(t.pg), "+v"(t.pe));
}
typedef __attribute__((address_space(3))) char* lptr;
#define LP(T, a) ((__attribute__((address_space(3))) T*)(uintptr_t)(a))
__device__ __forceinline__ uint32_t table_tile(const TOps& t, int ct, int lane, uint32_t evp, float dsk TSP) {
  u32x4 ev;
  f32x4 ad[2], af[2];
  asm volatile(T_ASM : "=&v"(ad[0]), "=&v"(ad[1]), "=&v"(af[0]), "=&v"(af[1]), "=&v"(ev) : "v"(t.xa), "v"(t.xb), "v"(t.ya), "v"(evp) : T_CLOB);
  const int e0 = (int)(ev[0] + ev[2]), e1 = (int)(ev[1] + ev[3]);
  const int em = min(max(max(max(e0, e1), (int)((__float_as_uint(dsk) >> 23) & 0xffu) - 133), -110), 115);
  const int ts = ((uint32_t)(em + 12) <= 8u) ? 1 : -3 - em;
  float s0 = __builtin_amdgcn_ldexpf(1.f, e0 - 29 + ts), s1 = __builtin_amdgcn_ldexpf(1.f, e1 - 29 + ts);
  float d0 = (ct == 0 && lane == 0) ? __builtin_amdgcn_ldexpf(dsk, ts) : 0.f;
  asm volatile("s_nop 1" : "+v"(ad[0]), "+v"(ad[1]), "+v"(af[0]), "+v"(af[1]), "+v"(s0), "+v"(s1), "+v"(d0));
  const f32x4 k = ad[0] * s0 + (f32x4){d0, 0.f, 0.f, 0.f} + ad[1] * s1, f = af[0] * s0 + af[1] * s1;
  auto pk = [](float a, float b) { uint32_t r; asm("v_cvt_pk_f16_f32 %0, %1, %2" : "=v"(r) : "v"(a), "v"(b)); return r; };
  asm volatile("s_nop 0" : : "v"(k), "v"(f));
  const uint32_t p32 = pk(k[3], k[2]), p10 = pk(k[1], k[0]), p21 = pk(k[2], k[1]), p01 = pk(f[0], f[1]), p23 = pk(f[2], f[3]);
  *LP(u32x2, t.pd + 3 * TDP + 6) = (u32x2){p32, p10};
  *LP(uint32_t, t.pd + TDP + 2) = p32; *LP(uint32_t, t.pd + TDP + 6) = p10;
  *LP(uint32_t, t.pd + 2) = p21; *LP(uint32_t, t.pd + 2 * TDP + 6) = p21;
  *LP(u16, t.pd) = (u16)p32; *LP(u16, t.pd + 2 * TDP + 4) = (u16)p32;
  asm volatile("ds_write_b16_d16_hi %0, %1 offset:6\n\tds_write_b16_d16_hi %0, %1 offset:%2" : : "v"(t.pd), "v"(p10), "n"(2 * TDP + 10) : "memory");
  if (ct) {
    const uint32_t va = (uint32_t)__builtin_amdgcn_readlane((int)p32, 63), vb = (uint32_t)__builtin_amdgcn_readlane((int)p23, 15) >> 16;
    *LP(u16, t.pe) = (u16)((lane < 12) ? va : vb);
  }
  if (lane < 32) {
    *LP(u16, t.pf + 128) = (u16)p01; *LP(u16, t.pg + 128) = (u16)(p01 >> 16);
    *LP(u16, t.pf) = (u16)p23; *LP(u16, t.pg) = (u16)(p23 >> 16);
  }
  return (uint32_t)(127 + 5 - ts) << 23;
}
__device__ __forceinline__ uint32_t hadd(uint32_t a, uint32_t b) { return __builtin_bit_cast(uint32_t, __builtin_bit_cast(f16x2, a) + __builtin_bit_cast(f16x2, b)); }
template <bool FULL> __device__ __forceinline__ void sums(char* sm, const u32x4 p, const u32x4 c) {
  const uint32_t D[8] = {p[0], p[1], p[2], p[3], c[0], c[1], c[2], c[3]};
  uint32_t O[7];
#pragma unroll
  for (int j = 0; j < 7; ++j) O[j] = __builtin_amdgcn_alignbit(D[j + 1], D[j], 16);
  u32x4 a, b;
#pragma unroll
  for (int j = 0; j < 4; ++j) a[j] = hadd(D[j + 2], hadd(O[j + 1], O[j + 2]));
  *(u32x4*)(sm + C_G3 + 32) = a;
#pragma unroll
  for (int j = 0; j < 4; ++j) b[j] = hadd(a[j], hadd(D[j + 1], D[j + 3]));
  *(u32x4*)(sm + C_G5 + 32) = b;
  if (FULL) {
#pragma unroll
    for (int j = 0; j < 4; ++j) a[j] = hadd(b[j], hadd(O[j], O[j + 3]));
    *(u32x4*)(sm + C_G7 + 32) = a;
#pragma unroll
    for (int j = 0; j < 4; ++j) b[j] = hadd(a[j], hadd(D[j], D[j + 4]));
    *(u32x4*)(sm + C_G9 + 32) = b;
  }
}
__device__ __forceinline__ void count_up(uint32_t a) {
  uint32_t t;
  asm volatile("s_mov_b64 exec, 1\n\tv_mov_b32 %0, 1\n\tds_add_u32 %1, %0\n\ts_mov_b64 exec, -1" : "=&v"(t) : "v"(a) : "memory");
}
__device__ __forceinline__ int pad_at(int r) { return 16 * (r & 1) + 3200 * (r >> 1) - ((r >= 6) ? 3200 - 2096 : 0); }
struct COps { uint32_t b, a, o, g3, k3, g4, k4, f; };
__device__ __forceinline__ void conv_ops(uint32_t tb, uint32_t ub, uint32_t cb, int lane, COps& e) {
  const uint32_t j = lane & 15, kq = lane >> 4, q4 = (lane >> 2) & 3, p4 = lane & 3;
  e.b = ub + 32u * j + 16u * kq;
  e.a = tb + p4 * TDP + 32u - 8u * q4 + 16u * kq;
  e.g3 = cb + C_G5 + 8 + 64u * kq + 8u * (q4 + p4) + (q4 & 1u) * 3200u;
  e.g4 = cb + C_G9 + 8 + 128u * kq + 16u * q4 + 8u * p4 + (q4 & 1u) * 2096u;
  e.k3 = tb + T_3 + 120 - 8u * j + 16u * kq;
  e.k4 = tb + T_4 + 60 - 4u * j + 16u * kq;
  asm volatile("" : "+v"(e.b), "+v"(e.a), "+v"(e.g3), "+v"(e.g4), "+v"(e.k3), "+v"(e.k4));
}
#define CONV_RUN(prog, e, ...) do { int prog_ = (prog); \
    asm volatile("s_call_b64 s[90:91], kf_conv" : "+{s89}"(prog_), "+{v2}"(e.b), "+{v3}"(e.a), "+{v4}"(e.o), "+{v5}"(e.g3), "+{v6}"(e.k3), "+{v7}"(e.g4), \
                 "+{v8}"(e.k4), "+{v9}"(e.f) __VA_ARGS__ : : CONV_CLOB); } while (0)

template <bool E>
__device__ __forceinline__ void ln_stats(const uint32_t* v, int lane, float sc, f32x2* t, f32x4 z, f32x2& rc, f32x2& nc, float& e) {
  float s1a = z[0], s2a = z[1], s1b = z[2], s2b = z[3];
#pragma unroll
  for (int k = 0; k < 4; ++k) {
    s1a = dot2(v[k], 0x3f803f80u, s1a); s2a = dot2(v[k], v[k], s2a);
    s1b = dot2(v[4 + k], 0x3f803f80u, s1b); s2b = dot2(v[4 + k], v[4 + k], s2b);
  }
#pragma unroll
  for (int k = 0; k < 9; ++k) t[k] = (f32x2){bflo(v[k]), bfhi(v[k])};
  const float rs = row_sum(fold16(dot2(v[8], 0x3f803f80u, fold32(s1a, s1b)), dot2(v[8], v[8], fold32(s2a, s2b))));
  if (E) { e = row_sum(e); e += __int_as_float(__builtin_amdgcn_update_dpp(0, __float_as_int(e), 0x142, 0xa, 0xf, false)); }
  const f32x2 mean = (f32x2){rdl(rs, 15), rdl(rs, 47)} * (1.f / D_), m2 = mean * mean;
  f32x2 var = (f32x2){rdl(rs, 31), rdl(rs, 63)} * (1.f / D_) - m2;
  asm volatile("" : "+v"(t[0]), "+v"(t[1]), "+v"(t[2]), "+v"(t[3]), "+v"(t[4]), "+v"(t[5]), "+v"(t[6]), "+v"(t[7]), "+v"(t[8]));
  if (__builtin_expect(!(m2[0] <= 16.f * var[0] && m2[1] <= 16.f * var[1]), 0)) {
    float q[3] = {0.f, 0.f, 0.f};
#pragma unroll
    for (int k = 0; k < 9; ++k) {
      const f32x2 a = t[k] - ((k < 4 || (k == 8 && lane < 32)) ? mean[0] : mean[1]);
      q[k >> 2] += a[0] * a[0] + a[1] * a[1];
    }
    float qa = q[0] + ((lane < 32) ? q[2] : 0.f), qb = q[1] + ((lane < 32) ? 0.f : q[2]);
    wave_sum2(qa, qb);
    var = (f32x2){qa, qb} * (1.f / D_);
  }
  const uint32_t mp = pack2(mean[0], mean[1]), vp = pack2(var[0], var[1]);
  const f32x2 ve = (f32x2){bflo(vp), bfhi(vp)} + 1e-5f;
  const uint32_t e2 = pack2(ve[0], ve[1]);
  const uint32_t sp = pack2(__builtin_amdgcn_sqrtf(bflo(e2)), __builtin_amdgcn_sqrtf(bfhi(e2)));
  rc = (f32x2){__builtin_amdgcn_rcpf(bflo(sp)), __builtin_amdgcn_rcpf(bfhi(sp))} * sc;
  nc = -(f32x2){bflo(mp), bfhi(mp)} * rc;
}
template <bool H>
__device__ __forceinline__ void ln_apply(const f32x2* t, int lane, f32x2 rc, f32x2 nc, uint32_t* c) {
  const float nm[3] = {nc[0], nc[1], (lane < 32) ? nc[0] : nc[1]}, rr[3] = {rc[0], rc[1], (lane < 32) ? rc[0] : rc[1]};
  f32x2 dd[9];
#pragma unroll
  for (int k = 0; k < 9; ++k) dd[k] = __builtin_elementwise_fma(t[k], (f32x2){rr[k >> 2], rr[k >> 2]}, (f32x2){nm[k >> 2], nm[k >> 2]});
  asm volatile("" : "+v"(dd[0]), "+v"(dd[1]), "+v"(dd[2]), "+v"(dd[3]), "+v"(dd[4]), "+v"(dd[5]), "+v"(dd[6]), "+v"(dd[7]), "+v"(dd[8]));
#pragma unroll
  for (int k = 0; k < 9; ++k) {
    const f32x2 d = dd[k];
    typedef __attribute__((ext_vector_type(2))) uint16_t u16x2;
    if (H) c[k] = __builtin_bit_cast(uint32_t, __builtin_elementwise_add_sat(__builtin_bit_cast(u16x2, __builtin_amdgcn_cvt_pkrtz(d[0], d[1])), (u16x2){4, 4}));
    else c[k] = pack2(d[0], d[1]);
  }
}
__device__ __forceinline__ uint32_t med3u(uint32_t a, uint32_t b, uint32_t c) { return max(min(a, b), min(max(a, b), c)); }
__device__ __forceinline__ uint32_t oct_maxu(uint32_t v) {
  v = max(v, (uint32_t)DPP_I((int)v, 0xb1, 0xf));
  v = max(v, (uint32_t)DPP_I((int)v, 0x4e, 0xf));
  v = max(v, (uint32_t)DPP_I((int)v, 0x141, 0xf));
  return v;
}

#define KARGS const u16* __restrict__ x, const u16* __restrict__ Are, const u16* __restrict__ Aim, const u16* __restrict__ Bre, \
    const u16* __restrict__ Bim, const u16* __restrict__ Cre, const u16* __restrict__ Cim, const u16* __restrict__ logdt, \
    const u16* __restrict__ Dskip, char* __restrict__ ws, const u16* __restrict__ wr, const u16* __restrict__ wg, \
    const u16* __restrict__ wu, const u16* __restrict__ wd, u16* __restrict__ out
#define KPASS x, Are, Aim, Bre, Bim, Cre, Cim, logdt, Dskip, ws, wr, wg, wu, wd, out
template <int ROLE, int FFN = -1>
__device__ __forceinline__ void role_body(char* smem, KARGS) {
  const int tid = threadIdx.x, lane = tid & 63;
  const int w = __builtin_amdgcn_readfirstlane(tid >> 6), wl = w & 3;
  const int b = blockIdx.x & 255, bx = b & 7, bj = b >> 3;
  constexpr bool isl = ROLE == 1 || ROLE == 2;
  const int tg = isl ? 32 * (bj >> 2) + 4 * bx + (bj & 3) : 32 * bx + bj;
  u32x4 xa, xc;
  uint32_t ep, r0;
#define P_WAIT(n, ...) asm volatile("s_waitcnt vmcnt(" #n ")\n\tv_lshlrev_b32 %0, 16, v100\n\tv_lshlrev_b32 %1, 16, v101\n\tv_lshlrev_b32 %2, 16, v102" \
    : "=v"(ldt), "=v"(are), "=v"(aim) : : "v100", "v101", "v102" __VA_ARGS__)
#define P_MORE() asm volatile("v_lshlrev_b32 %0, 16, v103\n\tv_lshlrev_b32 %1, 16, v104\n\tv_lshlrev_b32 %2, 16, v105\n\tv_lshlrev_b32 %3, 16, v106" \
    : "=v"(bre), "=v"(bim), "=v"(cre), "=v"(cim) : : "v103", "v104", "v105", "v106")
  TSD;
  TS(0, 0);
  const uint32_t lb0 = (uint32_t)(uintptr_t)(__attribute__((address_space(3))) char*)smem;
  const int i = lane & 15, kq = lane >> 4;
  const u32x4 ones = {0xffffffffu, 0xffffffffu, 0xffffffffu, 0xffffffffu}, zero4 = {0u, 0u, 0u, 0u};
  auto u_again = [&](u32x4& uw, const u32x4& ut, uint32_t uo) {
#pragma unroll 1
    for (int spin = 0; spin < SPIN_CAP; ++spin) {
      const bool na = ((uw[0] | uw[1] | uw[2] | uw[3]) & UMASK) != 0u;
      if (__builtin_expect(!__any(na), 1)) break;
      TSC(19);
      SLEEP(USLEEP);
      if (na) { LD16W(uw, uo); uw ^= ut; }
    }
  };
  constexpr uint32_t S16 = 0x41800000u, LMASK = 0x00070007u;
  auto v_put = [&](u32x4 v, const u32x4& ut, bool other, uint32_t vo) {
    const bool second = __any(other || ((v[0] | v[1] | v[2] | v[3]) & 0x40004000u) != 0u);
    if (__builtin_expect(second, 0)) v = (v & ~LMASK) | (~ut & LMASK); else v = (v & ~LMASK) | ut;
    ST16(vo, v);
    return second;
  };
  auto v_end = [&](uint32_t T, int n) { DRAIN(); if (lane == 0) ST4(O_DN + 16u * n, T); };
  if constexpr (!isl) {
    const int ch = wl >> 1, tl = tid & 255;
    int* evp = (int*)(smem + O_FL + 16 * ch);
    float ldt, are, aim, dsk;
    if constexpr (ROLE == 0) {
      __builtin_amdgcn_s_setprio(KPRIO);
      COps e;
      conv_ops(lb0 + (uint32_t)(ch * TB), lb0 + (uint32_t)(O_UL + ch * UB), lb0 + (uint32_t)(O_IM + ch * GB), lane, e);
      e.o = lb0 + O_ST + (uint32_t)(16 * ((4 * i + 2 * (kq >> 1) + ch) ^ (i >> 1)) + 8 * (kq & 1));
      e.f = lb0 + O_FL;
      TOps t;
      table_ops(lb0 + (uint32_t)(O_IM + 128 * IP * ch), lb0 + (uint32_t)(TB * ch), wl & 1, lane, t);
      uint32_t pz = lb0 + O_IM + (uint32_t)(((lane >> 3) & 1) * GB + pad_at(lane & 7)), im = lb0 + (uint32_t)(O_IM + IP * tl);
      asm volatile("" : "+v"(e.o), "+v"(e.f), "+v"(pz), "+v"(im));
      float bre, bim, cre, cim;
      P_WAIT(0, , "v103", "v104", "v105", "v106", "v107", "v109");
      asm volatile("" : "={v109}"(ep));
      const uint32_t T = ((uint32_t)__builtin_amdgcn_readfirstlane(ep) + 1u) * 0x9E3779B1u;
      if (w == 3 && lane < 8) ST16(O_KEY + 128u * b + 16u * lane, ones);
      P_MORE();
      asm volatile("v_lshlrev_b32 %0, 16, v107" : "=v"(dsk) : : "v107");
      TS(0, 1);
      evp[wl & 1] = gen_y(ldt, are, aim, bre, bim, cre, cim, im, [] {} TSA);
      TS(0, 8);
      BAR();
      TS(0, 2);
      const uint32_t cv = table_tile(t, wl & 1, lane, lb0 + O_FL + 16 * ch, dsk TSA), cs = (uint32_t)__builtin_amdgcn_readfirstlane(cv);
      *(uint32_t*)(smem + O_FL + 40 + 4 * ch) = cv;
      TS(0, 9);
      gsync0(lb0 + O_FL + 36, 4);
      TS(0, 3);
      *(u32x4*)(smem + O_ST + 16 * tl) = ones;
      *(u32x4*)(smem + O_ST + 4096 + 16 * tl) = ones;
      if (w == 0) *LP(u32x4, pz) = zero4;
      DRAIN();
      CONV_RUN(wl & 1, e);
      if (wl & 1) asm volatile("s_mov_b64 exec, 1\n\tglobal_store_dwordx2 %0, %1, %2 sc1" : : "v"(O_SC + 16u * (2 * tg + ch)), "v"((u32x2){T, cs}), "s"(ws) : "memory");
      TS(0, 5);
    } else {
      __builtin_amdgcn_s_setprio(UPRIO);
      const uint32_t ck0 = (uint32_t)__builtin_amdgcn_s_memtime(), rt0 = (uint32_t)__builtin_amdgcn_s_memrealtime();
#pragma unroll
      for (int k = 0; k < 3; ++k) *(u32x4*)(smem + ((16 * (tl + 256 * k) < O_FL) ? 16 * (tl + 256 * k) : O_FL + 32)) = zero4;
      if (w == 7) *(u32x4*)(smem + O_UL + (lane >> 5) * UB + 16 * min(lane & 31, 29)) = zero4;
      uint32_t uo = u_at(tl >> 1, 2 * tg + (tid & 1)), go = O_SU + (uint32_t)(256 * bx + 32 * wl + (lane & 31) + 4 * (lane & 32)) * 128u;
      uint32_t ul = (uint32_t)(O_UL + (tid & 1) * UB + C_U - 16 + 16 * (tl >> 1)), ug = (uint32_t)(O_IM + (tid & 1) * GB + 16 * (tl >> 1)), so = lb0 + O_ST + 16u * (uint32_t)(lane ^ (lane >> 3));
      uint32_t vo = O_V + (uint32_t)tg * VP + 16u * lane;
      asm volatile("" : "+v"(uo), "+v"(go), "+v"(ul), "+v"(ug), "+v"(so), "+v"(vo));
      P_WAIT(1, , "v109");
      TS(0, 1);
      evp[2 + (wl & 1)] = gen_x(ldt, are, aim, smem + O_IM + IP * tl);
      asm volatile("s_waitcnt vmcnt(0)" : "={v109}"(ep));
      const uint32_t T = ((uint32_t)__builtin_amdgcn_readfirstlane(ep) + 1u) * 0x9E3779B1u;
      TS(0, 8);
      BAR();
      TS(0, 2);
      if (w == 7 && bj >= 24) {
        const uint32_t t_ = ahead(ck0, rt0, VDB);
        ST16(O_SB + (uint32_t)(64 * (lane >> 3) + 8 * bx + bj - 24) * 128u + 16u * (lane & 7), ((u32x4){T, t_, T, t_}));
      }
      u32x4 ut = {utag(T, 0), utag(T, 1), utag(T, 2), utag(T, 3)};
      asm volatile("" : "+v"(ut));
      SLEEP(GSLEEP);
      u32x2 pv;
      pv[1] = gate(ws, go, T);
      TS(0, 3);
      until(latest(pv[1]) + UD);
      TS(0, 7);
      u32x4 uw, ux;
      asm volatile("global_load_dwordx4 %0, %2, %4 sc1\n\tglobal_load_dwordx4 %1, %3, %4 sc1"
                   : "=&v"(uw), "=&v"(ux) : "v"(uo), "v"(uo + 64 * UP * 32), "s"(ws) : "memory");
      __builtin_amdgcn_s_setprio(FPR);
      asm volatile("s_waitcnt vmcnt(1)" : "+v"(uw));
      uw ^= ut;
      *(u32x4*)(smem + ul + 16) = uw;
      asm volatile("s_waitcnt vmcnt(0)" : "+v"(ux));
      TS(0, 11);
      ux ^= ut;
      *(u32x4*)(smem + ul + 2048 + 16) = ux;
      if (__builtin_expect(__any(((uw[0] | uw[1] | uw[2] | uw[3] | ux[0] | ux[1] | ux[2] | ux[3]) & UMASK) != 0u), 0)) {
        u_again(uw, ut, uo);
        u_again(ux, ut, uo + 64 * UP * 32);
        *(u32x4*)(smem + ul + 16) = uw;
        *(u32x4*)(smem + ul + 2048 + 16) = ux;
      }
      BAR();
      TS(0, 4);
      const u32x4 pw = *(const u32x4*)(smem + ul);
      if (wl < 2) {
        const u32x4 px = *(const u32x4*)(smem + ul + 2048);
        sums<true>(smem + ug, pw, uw);
        sums<false>(smem + ug + 2048, px, ux);
      } else sums<true>(smem + ug, pw, uw);
      count_up(lb0 + O_FL + 32);
      __builtin_amdgcn_s_setprio(SPR);
      TS(0, 5);
      const bool other = *(const uint32_t*)(smem + O_FL + 40 + 4 * (lane & 1)) != S16;
      bool second = false;
#pragma unroll
      for (int k = 0; k < 2; ++k) {
        const uint32_t o = ((k ? 0x3247u : 0x1065u) >> (4 * wl) & 7u) * 1024u;
        u32x4 v;
#pragma unroll 1
        for (int spin = 0; spin < SPIN_CAP; ++spin) {
          asm volatile("ds_read_b128 %0, %1\n\ts_waitcnt lgkmcnt(0)" : "=&v"(v) : "v"(so + o) : "memory");
          if (!__any(max4(v) == 0xffffffffu)) break;
          __builtin_amdgcn_s_sleep(VPS);
        }
        TS(0, 12 + k);
        second |= v_put(v, ut, other, vo + o);
        TS(0, 10 - 4 * k);
      }
      if (__builtin_expect(second, 0)) v_end(T, 4 * tg + wl);
    }
    TSE();
  } else {
  uint32_t* cand = (uint32_t*)(smem + LM);
  uint32_t* sel = cand + 128;
  uint32_t* kl = sel + 48;
  uint32_t* cnt = sel + 64;
  const uint32_t ca = lb0 + LM + 4 * (128 + 64);
  u16* hm = (u16*)(cnt + 16);
  uint32_t T;
  const bool ffn = (FFN < 0) ? bj < 18 : FFN, isx = FFN != 1 && bj >= 24;
  const int fb = 18 * bx + bj;
  constexpr bool ln2 = ROLE == 2;
  const int g0 = tg, g2 = tg;
  u32x4 bw[18];
#pragma unroll
  for (int s = 0; s < 9; ++s) bw[s] = (u32x4){0u, 0u, 0u, 0u};
  int tid2 = threadIdx.x;
  uint32_t tv = 0u, tk = 0u;
  auto v_look = [&](uint32_t tu, int lane) {
    uint32_t tm = latest(gate(ws, O_SB + (uint32_t)(64 * bx + lane) * 128u, T)) - V2D;
    if ((int)(tm - (tu + UD + VD2)) < 0) tm = tu + UD + VD2;
    cnt[8] = tm;
    count_up(ca + 52);
  };
  auto v_time = [&] { gwait(ca + 52, 1); tv = (uint32_t)__builtin_amdgcn_readfirstlane(cnt[8]); };
  uint32_t ts1 = 0u;
  auto rest = [&] {
    if (ts1 && (int)((uint32_t)__builtin_amdgcn_s_memrealtime() - ts1) > 0) {
      v_time();
      ts1 = (tv - TSTOP) | 1u;
      if ((int)((uint32_t)__builtin_amdgcn_s_memrealtime() - ts1) > 0) { TS(0, 8); until(tv + RD); ts1 = 0u; }
    }
  };
  auto gu_dma = [&](int q0, int q1, auto depth) {
    const char* gs = (const char*)((w < 2) ? wg : wu) + (size_t)((144 * (w & 1)) * (F_ * 2) + fb * 64);
    const uint32_t gv = (uint32_t)((lane >> 2) * (F_ * 2) + 16 * ((lane & 3) ^ (lane >> 5) ^ (w & 2)));
#pragma unroll 1
    for (int q = q0; q < q1; ++q) {
      const int k = (int)((((bj & 1) ? ORD1 : ((unsigned __int128)0x876543210u << 36 | 0x876543210ull)) >> (4 * (q - (q >= 9 ? 9 : 0)) + (q >= 9 ? 36 : 0))) & 15u), h = q >= 9;
      if (q >= RQ) rest();
      dma16(gs + (size_t)(288 * h + 16 * k) * (F_ * 2), gv, lb0 + LW * h + 9216 * w + 1024 * k);
      if (!BALL || q < RQ || ts1) asm volatile("s_waitcnt vmcnt(%0)" : : "n"(depth.value) : "memory");
    }
  };
  auto down_dma = [&](int k0, int k1, int lane) {
#pragma unroll 1
    for (int k = k0; k < k1; ++k) {
      const int o = 9216 * wl + 2304 * k;
      const char* ds = (const char*)wd + (size_t)(fb * 36864 + o);
      const uint32_t dl = lb0 + o + 32 * ((4 * wl + k + 1) >> 1);
      if (ROLE == 1) rest();
      dma16d(ds, 16u * lane, dl);
      dma16d(ds + 1024, 16u * lane, dl + 1024);
      uint32_t keep;
      asm volatile("s_mov_b32 %0, m0\n\ts_mov_b32 m0, %3\n\ts_mov_b64 exec, 0xffff\n\tglobal_load_lds_dwordx4 %1, %2" DPOL2 "\n\ts_mov_b64 exec, -1\n\ts_mov_b32 m0, %0\n\ts_waitcnt vmcnt(%4)"
                   : "=&s"(keep) : "v"(16u * lane), "s"(ds + 2048), "s"(dl + 2048), "n"(ROLE == 1 ? DDEPTH : 40) : "memory");
    }
  };
  auto gu_read = [&](int h) {
    const uint32_t ra = lb0 + LW * h + (uint32_t)(((lane >> 1) & 1) * 18432 + (8 * kq + ((lane >> 2) & 3)) * 64 + ((w ^ (kq & 1) ^ (lane & 2)) * 16) + 8 * (lane & 1));
#pragma unroll
    for (int s = 0; s < 9; ++s) {
      const u32x2 h0 = trd(ra + 2048 * s), h1 = trd(ra + 2048 * s + 256);
      bw[9 * h + s] = (u32x4){h0[0], h0[1], h1[0], h1[1]};
    }
#pragma unroll
    for (int s = 0; s < 9; ++s) asm volatile("" : "+v"(bw[9 * h + s]));
  };
  const uint32_t vearly = (uint32_t)((VEARLY >> (8 * (g0 >> 5))) & 0xffull);
#define TOUCH(pa) asm volatile("global_load_dword v126, %0, off" DPOL4 : : "v"(pa) : "memory", "v126")
  uint32_t wv[5], xv[9];
  auto rows_xw = [&](int lane) {
    u32x4 wa, xr, xs;
    const uint32_t xo = (uint32_t)((8 * g2 + 2 * wl) * (D_ * 2));
    asm volatile("global_load_dwordx4 %0, %5, %9\n\tglobal_load_dword %1, %6, %9 offset:1024\n\t"
                 "global_load_dwordx4 %2, %7, %10\n\tglobal_load_dwordx4 %3, %7, %10 offset:1152\n\tglobal_load_dword %4, %8, %10 offset:1024\n\ts_waitcnt vmcnt(0)"
                 : "=&v"(wa), "=&v"(wv[4]), "=&v"(xr), "=&v"(xs), "=&v"(xv[8])
                 : "v"(16u * lane), "v"(4u * (lane & 31)), "v"(xo + 16u * lane), "v"(xo + 4u * lane + 32u * (lane & 32)), "s"(wr), "s"(x) : "memory");
#pragma unroll
    for (int k = 0; k < 4; ++k) { wv[k] = wa[k]; xv[k] = xr[k]; xv[4 + k] = xs[k]; }
  };
  auto ln1 = [&](int g, int o, uint32_t c0) {
    uint32_t uo = u_at(g, 64 * wl + lane);
    uint32_t ta = lb0 + (uint32_t)(o + ((lane >> 2) & 3) * RP + (64 * wl + 16 * kq + 4 * (lane & 3)) * 2);
    uint32_t la = (uint32_t)(o + 2 * wl * RP + 16 * lane), lt = (uint32_t)(o + (2 * wl + (lane >> 5)) * RP + 1024 + 4 * (lane & 31));
    uint32_t sg = O_SU + (uint32_t)(256 * (lane >> 3) + g) * 128u + 16u * (lane & 7);
    asm volatile("" : "+v"(uo), "+v"(ta), "+v"(la), "+v"(lt), "+v"(sg));
    f32x4 z4 = {0.f, 0.f, 0.f, 0.f};
    asm volatile("" : "+v"(z4));
    asm volatile("s_waitcnt vmcnt(0)" : "={v[100:103]}"(xa), "={v[104:107]}"(xc), "={v108}"(r0), "={v109}"(ep));
    TS(0, 1);
    const uint32_t v[9] = {xa[0], xa[1], xa[2], xa[3], xc[0], xc[1], xc[2], xc[3], r0};
    uint32_t c[9];
    f32x2 tf[9];
    T = ((uint32_t)__builtin_amdgcn_readfirstlane(ep) + 1u) * 0x9E3779B1u;
    if (wl == 3) { const uint32_t now = (uint32_t)__builtin_amdgcn_s_memrealtime() + SD; ST16(sg, ((u32x4){T, now, T, now})); }
    const uint32_t tj = ((T >> (3 * wl)) & 0x00070007u) | (((T >> (12 + wl)) & 0x00010001u) << 14);
    uint32_t tw[3] = {(tj & 0xffffu) * 0x10001u, (tj >> 16) * 0x10001u, 0u};
    tw[2] = (lane < 32) ? tw[0] : tw[1];
    asm volatile("" : "+v"(tw[0]), "+v"(tw[1]), "+v"(tw[2]));
    f32x2 rc, nc;
    float e_;
    ln_stats<false>(v, lane, 0.03125f, tf, z4, rc, nc, e_);
    ln_apply<true>(tf, lane, rc, nc, c);
#pragma unroll
    for (int k = 0; k < 9; ++k) c[k] = (c[k] & ~UMASK) | tw[k >> 2];
    *(u32x4*)(smem + la) = (u32x4){c[0], c[1], c[2], c[3]};
    *(u32x4*)(smem + la + RP) = (u32x4){c[4], c[5], c[6], c[7]};
    *(uint32_t*)(smem + lt) = c[8];
    u32x4 ha, hb;
    {
      uint32_t t, cc, n = SPIN_CAP;
      asm volatile("s_mov_b64 exec, 1\n\tv_mov_b32 %2, 1\n\tds_add_u32 %5, %2\n\ts_mov_b64 exec, -1\n"
                   "1:\n\tds_read_b32 %2, %5\n\tds_read_b64_tr_b16 v[40:41], %6\n\tds_read_b64_tr_b16 v[42:43], %6 offset:%7\n\tds_read_b64_tr_b16 v[44:45], %6 offset:512\n\t"
                   "ds_read_b64_tr_b16 v[46:47], %6 offset:%8\n\ts_sub_u32 %4, %4, 1\n\ts_cbranch_scc1 2f\n\ts_waitcnt lgkmcnt(4)\n\tv_readfirstlane_b32 %3, %2\n\t"
                   "s_cmp_lt_u32 %3, 4\n\ts_cbranch_scc1 1b\n2:\n\ts_waitcnt lgkmcnt(0)"
                   : "=&{v[40:43]}"(ha), "=&{v[44:47]}"(hb), "=&v"(t), "=&s"(cc), "+s"(n) : "v"(c0), "v"(ta), "n"(4 * RP), "n"(512 + 4 * RP) : "memory", "scc");
    }
    ST16(uo, ha);
    ST16(uo + 64 * 128, hb);
    if (wl == 0) {
      const u32x2 h4 = trd(ta + 1024), h5 = trd(ta + 1024 + 4 * RP);
      ST16(uo + 128 * 128, ((u32x4){h4[0], h4[1], h5[0], h5[1]}));
    }
  };
  const int dX = 512 - 24 + 8 * bx + bj;
  if constexpr (ROLE == 2) {
    const int tl = tid - 256;
    if (isx) {
      __builtin_amdgcn_s_setprio(QPRIO0);
      *(u32x4*)(smem + X_TB + 16 * tl) = zero4;
      *(u32x4*)(smem + X_TB + 16 * min(256 + tl, (TB + C_U) / 16 - 1)) = zero4;
      const int pr = (wl + XROT) & 3;
      COps e;
      conv_ops(lb0 + X_TB, lb0 + X_UL, lb0 + X_IM, lane, e);
      TOps t;
      table_ops(lb0 + X_IM, lb0 + X_TB, wl & 1, lane, t);
      e.o = lb0 + X_ST + (uint32_t)(16 * ((2 * i + (kq >> 1)) ^ ((i >> 2) & 1)) + 8 * (kq & 1));
      e.f = ca + 28 - 32;
      asm volatile("" : "+v"(e.o), "+v"(e.f));
      float ldt, are, aim, bre, bim, cre, cim, dsk;
      P_WAIT(0, , "v103", "v104", "v105", "v106", "v107", "v109");
      asm volatile("" : "={v109}"(ep));
      T = ((uint32_t)__builtin_amdgcn_readfirstlane(ep) + 1u) * 0x9E3779B1u;
      P_MORE();
      asm volatile("v_lshlrev_b32 %0, 16, v107" : "=v"(dsk) : : "v107");
      TS(0, 1);
      __builtin_amdgcn_s_setprio(QPRIO);
      int* evp = (int*)(sel + 44);
      char* im = smem + X_IM + IP * (tl & 127);
      evp[wl] = (wl < 2) ? gen_y(ldt, are, aim, bre, bim, cre, cim, lb0 + X_IM + IP * tl, [] {} TSA) : gen_x(ldt, are, aim, im);
      TS(0, 2);
      __builtin_amdgcn_s_setprio(QPRIO2);
      gsync(ca + 8, 4);
      if (wl < 2) evp[-4 + wl] = (int)table_tile(t, wl & 1, lane, lb0 + LM + 4 * (128 + 44), dsk TSA);
      if (wl < 2) gsync0(ca + 8, 8); else { count_up(ca + 8); SLEEP(XSL); gwait(ca + 8, 8); }
      const uint32_t cs = (uint32_t)__builtin_amdgcn_readfirstlane(evp[-4]);
      if (wl == 3) *(u32x4*)(smem + X_IM + pad_at(lane & 7)) = zero4;
      TS(0, 3);
      CONV_RUN(2 + pr, e, , "+{v11}"(tid2));
      __builtin_amdgcn_s_setprio(3);
      {
        const int lane = tid2 & 63;
        const u32x4 v = *(const u32x4*)(smem + X_ST + 2048 * pr + 1024 * (lane >> 5) + 16 * ((lane & 31) ^ ((lane >> 3) & 1)));
        if (__builtin_expect(v_put(v, (u32x4){utag(T, 0), utag(T, 1), utag(T, 2), utag(T, 3)}, cs != S16, v_at(dX, 64 * pr + lane)), 0)) {
          DRAIN();
          if (lane == 0) ST4(O_DN + 16u * (4 * (dX - 256) + pr), T);
        }
        if (pr == 0 && lane == 0) asm volatile("global_store_dwordx2 %0, %1, %2 sc1" : : "v"(O_SC + 16u * dX), "v"((u32x2){T, cs}), "s"(ws) : "memory");
      }
      TS(0, 6);
    }
    if (!isx) __builtin_amdgcn_s_setprio(PPRIO);
    asm volatile("" : "+v"(tid2));
    if (!isx) rows_xw(tid2 & 63);
    if (!isx && !ffn) {
      SLEEP(RSLEEP);
#pragma unroll
      for (int k = 0; k < 7; ++k) ST16(O_SL + (uint32_t)((6 * bx + bj - 18) * 1792 + 256 * k - 256 + tid2) * 16u, ones);
      DRAIN();
    }
    if (!isx) BAR();
    const uint32_t tp = (uint32_t)__builtin_amdgcn_readfirstlane(cnt[4]);
    if (!isx) T = (uint32_t)__builtin_amdgcn_readfirstlane(sel[43]);
    if (!isx && w == 4) v_look(tp, tid2 & 63);
    v_time();
  } else {
    __builtin_amdgcn_s_setprio(3);
    if (tid < 16) cnt[tid] = 0u;
    BAR();
    if (tid < 20) ((uint32_t*)(hm + 320))[tid] = 0u;
    if (tid >= 184) *(u32x4*)(smem + L_SC - 16 * 184 + 16 * tid) = (u32x4){0x41804180u, 0x41804180u, 0x41804180u, 0x41804180u};
    uint32_t sink = 0;
    ln1(tg, 0, ca + 12);
    TS(0, 2);
    __builtin_amdgcn_s_setprio(NPRIO);
    asm volatile("s_waitcnt vmcnt(0)" : "+v"(sink) : : "memory");
    TS(0, 13);
    if (w == 0) {
      const uint32_t xr = O_XC + (uint32_t)((511 - 2 * (g0 + w)) * 144 + lane) * 16u;
      ST16(xr, ones);
      ST16(xr + 1024u, ones);
      if (lane < 16) ST16(xr + 2048u, ones);
    }
    if (w == 2 && b < 72 && lane < 8) ST16(O_FB + (uint32_t)(8 * b + lane) * 16u, ones);
    if (isx) {
      rows_xw(lane);
      char* sp = smem + X_RW(w) + 16 * lane;
      *(u32x4*)sp = (u32x4){xv[0], xv[1], xv[2], xv[3]};
      *(u32x4*)(sp + 1024) = (u32x4){xv[4], xv[5], xv[6], xv[7]};
      *(u32x2*)(smem + X_RW(w) + 2048 + 8 * lane) = (u32x2){xv[8], wv[4]};
      if (w == 0) *(u32x4*)(smem + X_WR + 16 * lane) = (u32x4){wv[0], wv[1], wv[2], wv[3]};
    }
    SLEEP(ESLEEP);
    uint32_t tu = latest(gate(ws, O_SU + (uint32_t)(256 * bx + (ALLP ? tid : 64 * w + 4 * i + (bj & 3))) * 128u, T));
    asm volatile("s_mov_b64 exec, 1\n\tds_max_u32 %0, %1\n\ts_mov_b64 exec, -1" : : "v"(ca + 16), "v"(tu) : "memory");
    if (w == 0) sel[43] = T;
    if (isx) {
      const uint32_t xo = u_at(tid, dX);
      char* sm = smem + X_UL + C_U - 16 + 16 * tid;
      const u32x4 ut = {utag(T, 0), utag(T, 1), utag(T, 2), utag(T, 3)};
      __builtin_amdgcn_s_setprio(3);
      until(tu + UD + XD);
      u32x4 uw;
      asm volatile("global_load_dwordx4 %0, %1, %2 sc1" : "=&v"(uw) : "v"(xo), "s"(ws) : "memory");
      asm volatile("s_waitcnt vmcnt(0)" : "+v"(uw));
      uw ^= ut;
      u_again(uw, ut, xo);
      *(u32x4*)(sm + 16) = uw;
      BAR();
      if (w < 3) {
        const u32x4 pw = *(const u32x4*)sm;
        if (w < 2) sums<true>(smem + X_IM + 16 * tid, pw, uw); else sums<false>(smem + X_IM + 16 * tid, pw, uw);
        count_up(ca + 28);
      }
    } else {
      until(tu + UD + WD);
      BAR();
    }
    tu = (uint32_t)__builtin_amdgcn_readfirstlane(cnt[4]);
    TS(0, 3);
    __builtin_amdgcn_s_setprio(DPRIO);
    if (ffn) {
      ts1 = (tu + UD + VD2 - TSTOP) | 1u;
      gu_dma(0, N0, IC<40>{});
      gu_dma(N0, NA, IC<WDEPTH>{});
      asm volatile("s_waitcnt vmcnt(%0)" : : "n"(NA - 9) : "memory");
      gsync0(ca + 4, 4);
      gu_read(0);
      asm volatile("s_waitcnt lgkmcnt(0)" ::: "memory");
      count_up(ca + 36);
      TS(0, 4);
      gu_dma(NA, 18, IC<WDEPTH2>{});
      gwait(ca + 36, 4);
      down_dma(0, KN, lane);
      asm volatile("s_waitcnt vmcnt(%0)" : : "n"(3 * KN) : "memory");
      count_up(ca + 4);
      if (STP && w == 1)
        asm volatile("global_load_dword v126, %0, %1 sc1" : : "v"(O_SC + 1024u * (fb >> 4) + 128u * (lane & 7)), "s"(ws) : "memory", "v126");
      TS(0, 7);
    } else {
      if (!isx && C3) {
        const int n = 6 * min(tid, 191) + bj - 18, r = n >> 2;
        const char* wb = (const char*)((n & 1) ? wu : wg) + bx * 1152;
        const int c0 = (3 - (int)((uintptr_t)wb >> 7)) & 7;
        wb += (uint32_t)((r + 144 * ((r >= 144) + ((n >> 1) & 1))) * (F_ * 2) + 128 * c0);
        TOUCH(wb);
        if (c0 == 0) TOUCH(wb + 1024);
      }
      auto d_line = [&](int k) {
        const int n = min((k * 14 + bj - 18) * 64 + lane, 5183), q = n / 1296, r = n - 1296 * q;
        return (const char*)wd + (uint32_t)(bx * 5184 + (r / 18) * 72 + 18 * (HREV ? 3 - q : q) + r % 18) * 128u;
      };
      if (isx && w == 3) v_look(tu, lane);
      int k = w;
      if (w < HW) {
#pragma unroll 1
        for (; k < 6 && (int)((uint32_t)__builtin_amdgcn_s_memrealtime() - (tu + UD + VD2 - HSTOP)) < 0; k += HW) { TOUCH(d_line(k)); DRAIN(); }
      }
      TS(0, 4);
      v_time();
      until(tv + RD2);
      if (w < HW) {
#pragma unroll 1
        for (; k < 6; k += HW) { TOUCH(d_line(k)); DRAIN(); }
      }
      TS(0, 7);
    }
    v_time();
    tk = tv + KD;
  }
  {
  const int tid = tid2, lane = tid & 63, i = lane & 15, kq = lane >> 4;
  __builtin_amdgcn_s_setprio(3);
  const int PL = ffn ? 6400 : LW + 6656;
  f32x2 xf[9];
  f32x4 za = {0.f, 0.f, 0.f, 0.f}, zb = za;
  uint32_t al, ha, h0a, h1a, so, da;
  auto ffn_terms = [&] {
    al = O_XC + 64u * (i >> 3) + 16u * kq + 128u * w;
    ha = (ROLE == 1) ? (uint32_t)((4 * (kq & 1) + 2 * (kq >> 1) + (i >> 3)) * 80 + 16 * w + 2 * (i & 7)) : 0u;
    h0a = (uint32_t)(min(i, 8) * 80 + 16 * kq); h1a = (uint32_t)(min(i ^ 8, 8) * 80 + 16 * kq);
    so = O_SL + (uint32_t)fb * SP + ((ROLE == 2) ? 16u * tid - 4096u : 8192u + 16u * lane);
    da = lb0 + (uint32_t)((8 * kq + ((lane >> 2) & 3)) * 1152 + 32 * ((8 * kq + ((lane >> 2) & 3) + 2) >> 2) + 8 * (lane & 3) + ((ROLE == 2) ? 128 * wl : 1024));
    if (ROLE == 2) asm volatile("" : "+v"(h0a), "+v"(h1a), "+v"(so), "+v"(da)); else asm volatile("" : "+v"(ha), "+v"(al), "+v"(da));
  };
  if (ROLE == 1) ffn_terms();
  uint32_t hv[9] = {}, nv[9] = {}, keya = 0u, keyb = 0u;
  int rka = 0, rkb = 0;
  auto put_row = [&](const uint32_t* r, int j, const void* base, uint32_t ro) {
    const u32x4 tl = {r[8], (uint32_t)DPP_I((int)r[8], 0x55, 0xf), (uint32_t)DPP_I((int)r[8], 0xaa, 0xf), (uint32_t)DPP_I((int)r[8], 0xff, 0xf)};
    asm volatile("global_store_dwordx4 %0, %1, %2 sc1\n\ts_nop 1" : : "v"(ro + 16u * lane), "v"((u32x4){r[4 * j], r[4 * j + 1], r[4 * j + 2], r[4 * j + 3]}), "s"(base) : "memory");
    if ((lane & 35) == 32 * j) asm volatile("global_store_dwordx4 %0, %1, %2 offset:1024 sc1\n\ts_nop 1" : : "v"(ro + 4u * (lane & 31)), "v"(tl), "s"(base) : "memory");
  };
  if (ln2) {
    const int tl = tid - 256, d1 = 64 * (tl >> 5) + (tl & 31), d3 = 64 * ((tl >> 4) & 7) + 32 + (tl & 15), d2 = (tl < 128) ? d3 + 16 : min(384 + tl, 575);
    uint32_t vo = v_at(d1, g0), vo2 = v_at(d2, g0), vo3 = v_at(d3, g0);
    u32x4 ut = {utag(T, 0), utag(T, 1), utag(T, 2), utag(T, 3)};
    u32x4 va, vc = ut, vb = ut;
    asm volatile("" : "+v"(vo), "+v"(vo2), "+v"(vo3), "+v"(ut), "+v"(vc), "+v"(vb));
    TS(0, 7);
    until(tv);
    TS(0, 8);
    asm volatile("global_load_dwordx4 %0, %2, %4 sc1\n\ts_cmp_lt_u32 %5, 2\n\ts_cbranch_scc1 1f\n\tglobal_load_dwordx4 %1, %3, %4 sc1\n1:"
                 : "=&v"(va), "+v"(vc) : "v"(vo), "v"(vo3), "s"(ws), "s"(wl) : "memory", "scc");
    until(tv + V2D);
    asm volatile("s_cmp_eq_u32 %3, 3\n\ts_cbranch_scc1 1f\n\tglobal_load_dwordx4 %0, %1, %2 sc1\n1:" : "+v"(vb) : "v"(vo2), "s"(ws), "s"(wl) : "memory", "scc");
    __builtin_amdgcn_sched_barrier(0);
    if (isx) {
      const u32x4 ra = *(const u32x4*)(smem + X_RW(wl) + 16 * lane), rb = *(const u32x4*)(smem + X_RW(wl) + 1024 + 16 * lane), rw = *(const u32x4*)(smem + X_WR + 16 * lane);
      const u32x2 rc = *(const u32x2*)(smem + X_RW(wl) + 2048 + 8 * lane);
#pragma unroll
      for (int k = 0; k < 4; ++k) { xv[k] = ra[k]; xv[4 + k] = rb[k]; wv[k] = rw[k]; }
      xv[8] = rc[0]; wv[4] = rc[1];
    }
    uint32_t pa = (uint32_t)(PL + 9216 * wl + 32 * lane), pb = (uint32_t)(PL + 9216 * wl + 2048 + 8 * (lane & 31)), ps = (lane < 32) ? 0u : 16u;
    uint32_t kid = (uint32_t)(((255 - g2) << 5) | (7 - 2 * wl - (lane >> 5)));
    uint32_t px = (uint32_t)(PL + 4 * d1), py = (uint32_t)(PL + 4 * d2), pz = (uint32_t)(PL + 4 * d3);
#pragma unroll
    for (int k = 0; k < 9; ++k) xf[k] = (f32x2){bflo(xv[k]), bfhi(xv[k])};
    asm volatile("" : "+v"(xf[0]), "+v"(xf[1]), "+v"(xf[2]), "+v"(xf[3]), "+v"(xf[4]), "+v"(xf[5]), "+v"(xf[6]), "+v"(xf[7]), "+v"(xf[8]));
    asm volatile("" : "+v"(za), "+v"(zb), "+v"(pa), "+v"(pb), "+v"(ps), "+v"(kid), "+v"(px), "+v"(py), "+v"(pz));
    float sv[10], ws_ = dot2(wv[4], 0x3f803f80u, 0.f) * 0.5f;
#pragma unroll
    for (int k = 0; k < 4; ++k) ws_ = dot2(wv[k], 0x3f803f80u, ws_);
    ws_ = wave_sum(ws_);
#pragma unroll
    for (int k = 0; k < 10; ++k) sv[k] = 16.f;
    uint32_t ka_ = lb0 + LM + 4u * (128 + 48 + 2 * wl + (lane >> 5)), kb_ = lb0 + LM + 4u * (128 + 48);
    uint32_t ko_ = O_KEY + (uint32_t)(256 * (lane & 7) + 8 * (g2 & 31) + (g2 >> 5)) * 16u;
    asm volatile("" : "+v"(ws_), "+v"(sv[0]), "+v"(sv[1]), "+v"(sv[2]), "+v"(sv[3]), "+v"(sv[4]), "+v"(sv[5]), "+v"(sv[6]), "+v"(sv[7]), "+v"(sv[8]), "+v"(sv[9]),
                 "+v"(ka_), "+v"(kb_), "+v"(ko_));
    if (ffn) gwait(ca + 36, 4);
    asm volatile("s_waitcnt vmcnt(1)\n\ts_cmp_eq_u32 %2, 3\n\ts_cbranch_scc0 1f\n\ts_waitcnt vmcnt(0)\n1:" : "+v"(va), "+v"(vc) : "s"(wl) : "memory", "scc");
    auto turn1 = [&] {
#pragma unroll
      for (int e = 0; e < 4; ++e) *(uint32_t*)(smem + px + 9216 * e) = va[e];
      if (wl >= 2) {
#pragma unroll
        for (int e = 0; e < 4; ++e) *(uint32_t*)(smem + pz + 9216 * e) = vc[e];
      }
    };
    auto turn = [&] {
      if (wl < 3) {
#pragma unroll
        for (int e = 0; e < 4; ++e) *(uint32_t*)(smem + py + 9216 * e) = vb[e];
      }
    };
    va ^= ut; vc ^= ut;
    turn1();
    asm volatile("s_waitcnt vmcnt(0)" : "+v"(vb) : : "memory");
    vb ^= ut;
    turn();
    const u32x4 vs = va | vb | vc;
    if (__builtin_expect(__any(((vs[0] | vs[1] | vs[2] | vs[3]) & UMASK) != 0u), 0)) {
#pragma unroll 1
      for (int spin = 0; spin < 4; ++spin) {
        const bool na = ((va[0] | va[1] | va[2] | va[3]) & UMASK) != 0u, nb = ((vb[0] | vb[1] | vb[2] | vb[3]) & UMASK) != 0u, nc = ((vc[0] | vc[1] | vc[2] | vc[3]) & UMASK) != 0u;
        if (!__any(na || nb || nc)) break;
        TSC(16);
        if (na) LD16(va, vo);
        if (nb) LD16(vb, vo2);
        if (nc) LD16(vc, vo3);
        asm volatile("s_waitcnt vmcnt(0)" : "+v"(va), "+v"(vb), "+v"(vc) : : "memory");
        if (na) va ^= ut;
        if (nb) vb ^= ut;
        if (nc) vc ^= ut;
      }
      auto again = [&](u32x4& v, uint32_t o, int d) {
        const int J = g0 >> 5;
        const uint32_t dn = O_DN + 16u * (uint32_t)((d < 512) ? 4 * (d >> 1) + ((0x01013232 >> (4 * J)) & 3) : 4 * (d - 256) + (J >> 1));
        bool pend = ((v[0] | v[1] | v[2] | v[3]) & UMASK) != 0u;
#pragma unroll 1
        for (int spin = 0; spin < SPIN_CAP && __any(pend); ++spin) {
          TSC(16);
          SLEEP(PSLEEP);
          if (pend) {
            uint32_t fin = ~T;
            if ((v[0] & v[1] & v[2] & v[3] & LMASK) == LMASK)
              asm volatile("global_load_dword %0, %1, %2 sc1\n\ts_waitcnt vmcnt(0)" : "=&v"(fin) : "v"(dn), "s"(ws) : "memory");
            LD16W(v, o);
            v ^= ut;
            pend = ((v[0] | v[1] | v[2] | v[3]) & UMASK) != 0u;
            if (pend && fin == T) {
              u32x2 sc;
              for (int k = 0; k < SPIN_CAP; ++k) {
                asm volatile("global_load_dwordx2 %0, %1, %2 sc1\n\ts_waitcnt vmcnt(0)" : "=&v"(sc) : "v"(O_SC + 16u * d), "s"(ws) : "memory");
                if (sc[0] == T) break;
              }
              *(u16*)(smem + L_SC + 2 * d) = (u16)(sc[1] >> 16);
              cnt[10] = 1u;
              v = (v ^ ut) & ~LMASK;
              pend = false;
            }
          }
        }
      };
      again(va, vo, d1); again(vc, vo3, d3); again(vb, vo2, d2);
      turn1();
      turn();
    }
    u32x4 a0, a1;
    u32x2 a2, cf;
    {
      uint32_t cc, n = SPIN_CAP;
      asm volatile("s_mov_b64 exec, 1\n\tv_mov_b32 v90, 1\n\tds_add_u32 %6, v90\n\ts_mov_b64 exec, -1\n"
                   "1:\n\tds_read2_b32 %3, %6 offset1:10\n\tds_read_b128 %0, %7\n\tds_read_b128 %1, %7 offset:16\n\tds_read_b64 %2, %8\n\t"
                   "s_sub_u32 %5, %5, 1\n\ts_cbranch_scc1 2f\n\ts_waitcnt lgkmcnt(3)\n\tv_readfirstlane_b32 %4, v90\n\t"
                   "s_cmp_lt_u32 %4, 4\n\ts_cbranch_scc1 1b\n2:\n\ts_waitcnt lgkmcnt(0)"
                   : "=&v"(a0), "=&v"(a1), "=&v"(a2), "=&{v[90:91]}"(cf), "=&s"(cc), "+s"(n) : "v"(ca), "v"(lb0 + pa), "v"(lb0 + pb) : "memory", "scc");
    }
    TS(0, 9);
    if (__builtin_expect(__builtin_amdgcn_readfirstlane(cf[1]) != 0, 0)) {
      const u32x4 q = *(const u32x4*)(smem + L_SC + 16 * lane);
      const uint32_t r = *(const uint32_t*)(smem + L_SC + 1024 + 4 * (lane & 31));
#pragma unroll
      for (int k = 0; k < 4; ++k) { sv[2 * k] = bflo(q[k]); sv[2 * k + 1] = bfhi(q[k]); }
      sv[8] = bflo(r); sv[9] = bfhi(r);
    }
    const uint32_t av[10] = {a0[0], a0[1], a0[2], a0[3], a1[0], a1[1], a1[2], a1[3], a2[0] >> ps, a2[1] >> ps};
    f32x2 h[9];
#pragma unroll
    for (int k = 0; k < 10; ++k) {
      asm("v_fma_mix_f32 %0, %1, %2, %3 op_sel_hi:[1,0,0]" : "=v"(h[(k < 8) ? k >> 1 : 8][k & 1]) : "v"(av[k]), "v"(sv[k]), "v"(xf[(k < 8) ? k >> 1 : 8][k & 1]));
      if (k < 8) asm("v_fma_mix_f32 %0, %1, %2, %3 op_sel:[1,0,0] op_sel_hi:[1,0,0]" : "=v"(h[4 + (k >> 1)][k & 1]) : "v"(av[k]), "v"(sv[k]), "v"(xf[4 + (k >> 1)][k & 1]));
    }
#pragma unroll
    for (int k = 0; k < 9; ++k) hv[k] = pack2(h[k][0], h[k][1]);
    const float da = dot2(hv[2], wv[2], dot2(hv[0], wv[0], zb[0])) + dot2(hv[3], wv[3], dot2(hv[1], wv[1], zb[1]));
    const float db = dot2(hv[6], wv[2], dot2(hv[4], wv[0], zb[2])) + dot2(hv[7], wv[3], dot2(hv[5], wv[1], zb[3]));
    float dt = dot2(hv[8], wv[4], fold32(da, db));
    f32x2 rc, nc;
    ln_stats<true>(hv, lane, 1.f, xf, za, rc, nc, dt);
    dt = (lane < 32) ? dt * rc[0] + ws_ * nc[0] : dt * rc[1] + ws_ * nc[1];
    const uint32_t bb = pack2(dt, dt) & 0xffff0000u;
    const uint32_t kk = (bb ^ (((uint32_t)((int)bb >> 31) & 0x7fff0000u) | 0x80000000u)) | kid;
    u32x4 ka, kb;
    uint32_t old;
    asm volatile("s_mov_b64 exec, %5\n\tds_write_b32 %3, %4\n\ts_mov_b64 exec, 1\n\tv_mov_b32 %2, 1\n\tds_add_rtn_u32 %2, %7, %2\n\ts_mov_b64 exec, -1\n\t"
                 "ds_read_b128 %0, %6\n\tds_read_b128 %1, %6 offset:16\n\ts_waitcnt lgkmcnt(0)"
                 : "=&v"(ka), "=&v"(kb), "=&v"(old) : "v"(ka_), "v"(kk), "s"(0x8000000080000000ull), "v"(kb_), "v"(ca) : "memory");
    if (__builtin_amdgcn_readfirstlane(old) == 7) {
      uint32_t m1 = max(max(ka[0], ka[1]), ka[2]), m2 = med3u(ka[0], ka[1], ka[2]), m3 = min(min(ka[0], ka[1]), ka[2]);
#pragma unroll
      for (int e = 0; e < 5; ++e) {
        const uint32_t kx = e ? kb[e - 1] : ka[3];
        m3 = med3u(m2, m3, kx); m2 = med3u(m1, m2, kx); m1 = max(m1, kx);
      }
      if (lane < 8) ST16(ko_, ((u32x4){m1 | 24u, m2 | 16u, m3 | 8u, T & ~1u}));
    }
    TS(0, 10);
    keya = (uint32_t)__builtin_amdgcn_readlane(kk, 31); keyb = (uint32_t)__builtin_amdgcn_readlane(kk, 63);
    ln_apply<false>(xf, lane, rc, nc, nv);
    gwait(ca, 8);
    if (wl == 1) {
      const u32x4 kv = *(const u32x4*)&kl[4 * (lane & 1)];
      if (lane < 2) ST16(O_KF + (uint32_t)(2 * g2 + lane) * 16u, ((u32x4){__builtin_amdgcn_perm(kv[1], kv[0], 0x07060302u), T, __builtin_amdgcn_perm(kv[3], kv[2], 0x07060302u), T}));
      if (ROLE == 2 && b == 0 && lane == 0) ST4(O_EP, T * 0x0E8B2F51u);
    }
    const uint32_t ko = kl[lane & 7];
    rka = FORCEFB + __popcll(__ballot(ko > keya) & 0xffull); rkb = FORCEFB + __popcll(__ballot(ko > keyb) & 0xffull);
#pragma unroll
    for (int k = 0; k < 9; ++k) {
      const f32x2 o = xf[k] + (f32x2){bflo(nv[k]), bfhi(nv[k])};
      hv[k] = pack2(o[0], o[1]);
      nv[k] = min(nv[k], 0xfffffffeu);
    }
    if (rka < 2) put_row(nv, 0, ws, O_XC + (uint32_t)(1023 - 4 * g2 - rka) * 1152u);
    if (rkb < 2) put_row(nv, 1, ws, O_XC + (uint32_t)(1023 - 4 * g2 - rkb) * 1152u);
    if (OTP && lane < 18 && ((lane < 9) ? rka : rkb) < 2)
      asm volatile("global_load_dword v126, %0, %1" OPOL : : "v"((uint32_t)((8 * g2 + 2 * wl) * (D_ * 2) + 128 * lane)), "s"(out) : "memory", "v126");
    if (!LPOS && ffn) down_dma(KN, 4, lane);
    ffn_terms();
  }
  uint32_t k3rd = 0u, k4th = 0u;
  uint32_t q0 = lb0 + LM + 4u * lane, q1 = lb0 + LM + 32u * w, q2 = q1 + 4u * (lane & 7), q3 = lb0 + LM + 512u + 4u * (lane & 7), q4 = lb0 + LM + (uint32_t)(tid & ~7);
  uint32_t r8 = C_, sb = lb0 + LM + 512u;
  asm volatile("" : "+v"(q0), "+v"(q1), "+v"(q2), "+v"(q3), "+v"(q4), "+v"(r8), "+v"(sb));
  if constexpr (ROLE == 1) {
    u32x4 key;
    const uint32_t kof = O_KEY + (uint32_t)(256 * bx + tid) * 16u;
    until(tk);
    if (!ffn) SLEEP(NSLEEP);
    LD16(key, kof);
    if (ffn) {
      gwait0(ca + 4, 8);
      gu_read(1);
    }
    asm volatile("s_waitcnt vmcnt(0)" : "+v"(key) : : "memory");
    if (__builtin_expect(__any(key[3] != (T & ~1u)), 0)) {
#pragma unroll 1
      for (int spin = 0; spin < SPIN_CAP; ++spin) {
        const bool need = key[3] != (T & ~1u);
        if (!__any(need)) break;
        TSC(15);
        SLEEP(KBACK);
        if (need) LD16W(key, kof);
      }
    }
    TS(0, 11);
    const uint32_t m1 = oct_maxu(key[0]);
    const bool hit = key[0] == m1;
    const uint32_t k0 = hit ? key[1] : key[0], k1 = hit ? key[2] : key[1];
    const uint32_t m2 = oct_maxu(k0);
    *LP(u32x2, q4) = (u32x2){m1, m2};
    k3rd = (k0 == m2) ? k1 : k0;
    k4th = (k0 == m2) ? (hit ? 0u : key[2]) : k1;
    if (RPF == 1 && !ffn && w == (bj & 3)) {
      const uint32_t r1 = O_XC + ((m1 >> 3) & 0x3ffu) * 1152u + 128u * (lane & 7), r2 = O_XC + ((m2 >> 3) & 0x3ffu) * 1152u + 128u * (lane & 7);
      asm volatile("global_load_dword v126, %0, %3 sc1\n\tglobal_load_dword v126, %1, %3 sc1\n\tglobal_load_dword v126, %2, %3 offset:1024 sc1"
                   : : "v"(r1), "v"(r2), "v"(((lane & 1) ? r1 : r2) - 128u * (lane & 7)), "s"(ws) : "memory", "v126");
    }
  }
  auto pick = [&](auto co, auto so_) {
    BAR();
    const uint32_t c = *LP(uint32_t, q0 + co.value), own = *LP(uint32_t, q2 + co.value);
    const u32x4 la = *LP(u32x4, q1 + co.value), lb = *LP(u32x4, q1 + co.value + 16);
    uint32_t r = r8;
    asm("v_cmp_gt_u32 s[40:41], %1, %2\n\tv_cmp_gt_u32 s[42:43], %1, %3\n\tv_cmp_gt_u32 s[44:45], %1, %4\n\tv_cmp_gt_u32 s[46:47], %1, %5\n\t"
        "v_cmp_gt_u32 s[48:49], %1, %6\n\tv_cmp_gt_u32 s[50:51], %1, %7\n\tv_cmp_gt_u32 s[52:53], %1, %8\n\tv_cmp_gt_u32 s[54:55], %1, %9\n\t"
        "s_bcnt1_i32_b64 s40, s[40:41]\n\ts_bcnt1_i32_b64 s42, s[42:43]\n\tv_writelane_b32 %0, s40, 0\n\ts_bcnt1_i32_b64 s44, s[44:45]\n\t"
        "v_writelane_b32 %0, s42, 1\n\ts_bcnt1_i32_b64 s46, s[46:47]\n\tv_writelane_b32 %0, s44, 2\n\ts_bcnt1_i32_b64 s48, s[48:49]\n\t"
        "v_writelane_b32 %0, s46, 3\n\ts_bcnt1_i32_b64 s50, s[50:51]\n\tv_writelane_b32 %0, s48, 4\n\ts_bcnt1_i32_b64 s52, s[52:53]\n\t"
        "v_writelane_b32 %0, s50, 5\n\ts_bcnt1_i32_b64 s54, s[54:55]\n\tv_writelane_b32 %0, s52, 6\n\tv_writelane_b32 %0, s54, 7"
        : "+v"(r) : "v"(c), "v"(la[0]), "v"(la[1]), "v"(la[2]), "v"(la[3]), "v"(lb[0]), "v"(lb[1]), "v"(lb[2]), "v"(lb[3])
        : "scc", "s40", "s41", "s42", "s43", "s44", "s45", "s46", "s47", "s48", "s49", "s50", "s51", "s52", "s53", "s54", "s55");
    *LP(uint32_t, sb + 4u * min(r, (uint32_t)C_) + so_.value) = own;
    BAR();
  };
  auto rows = [&](uint32_t ao, auto&& meanwhile) {
    u32x4 aa, ab, ac = __builtin_bit_cast(u32x4, zb);
    asm volatile("global_load_dwordx4 %0, %3, %4 sc1\n\tglobal_load_dwordx4 %1, %3, %4 offset:512 sc1\n\t"
                 "s_cmp_lg_u32 %5, 0\n\ts_cbranch_scc1 1f\n\tglobal_load_dwordx4 %2, %3, %4 offset:1024 sc1\n1:"
                 : "=&v"(aa), "=&v"(ab), "+v"(ac) : "v"(ao), "s"(ws), "s"(w) : "memory", "scc");
    __builtin_amdgcn_sched_barrier(0);
    meanwhile();
    asm volatile("s_waitcnt vmcnt(0)" : "+v"(aa), "+v"(ab), "+v"(ac) : : "memory");
    if (__builtin_expect(__any(max(max(max4(aa), max4(ab)), max4(ac)) == 0xffffffffu), 0)) {
#pragma unroll 1
      for (int spin = 0; spin < SPIN_CAP; ++spin) {
        const bool na = max4(aa) == 0xffffffffu, nb = max4(ab) == 0xffffffffu, nc = max4(ac) == 0xffffffffu;
        if (!__any(na || nb || nc)) break;
        TSC(14);
        SLEEP(PSLEEP);
        if (na) LD16W(aa, ao);
        if (nb) LD16W(ab, ao + 512u);
        if (nc) LD16W(ac, ao + 1024u);
      }
    }
    *(u32x4*)(smem + LA + 16 * tid) = aa;
    *(u32x4*)(smem + LA + 4096 + 16 * tid) = ab;
    if (w == 0) *(u32x4*)(smem + LA + 8192 + 16 * tid) = ac;
  };
  auto rows_dma = [&](uint32_t ao) {
    uint32_t dl = lb0 + LA + 1024u * w, keep;
    asm volatile("s_mov_b32 %0, m0\n\ts_mov_b32 m0, %1\n\ts_add_u32 %1, %1, 4096 - 512\n\tglobal_load_lds_dwordx4 %2, %3 sc1\n\ts_mov_b32 m0, %1\n\ts_add_u32 %1, %1, 4096 - 512\n\t"
                 "global_load_lds_dwordx4 %2, %3 offset:512 sc1\n\ts_cmp_lg_u32 %4, 0\n\ts_cbranch_scc1 1f\n\ts_mov_b32 m0, %1\n\ts_nop 0\n\t"
                 "global_load_lds_dwordx4 %2, %3 offset:1024 sc1\n1:\n\ts_mov_b32 m0, %0" : "=&s"(keep), "+s"(dl) : "v"(ao), "s"(ws), "s"(w) : "memory", "scc");
  };
  auto get_bd = [&](int t0, int t1) {
#pragma unroll
    for (int t = t0; t < t1; ++t) {
      const u32x2 h0 = trd(da + 512 * (t >> 2) + 32 * (t & 3)), h1 = trd(da + 512 * (t >> 2) + 32 * (t & 3) + 4608 + 32);
      bw[t] = (u32x4){h0[0], h0[1], h1[0], h1[1]};
    }
#pragma unroll
    for (int t = t0; t < t1; ++t) asm volatile("" : "+v"(bw[t]));
  };
  pick(IC<0>{}, IC<0>{});
  uint32_t mykey = *LP(uint32_t, q3), ro = ((mykey >> 3) & 0x3ffu) * 1152u;
  TS(0, 12);
  auto check = [&] {
    cnt[(k3rd > (uint32_t)__builtin_amdgcn_readlane(mykey, 7)) ? 5 : 6] = 1u;
    const uint32_t m3 = oct_maxu(k3rd);
    if (k3rd == m3) k3rd = k4th;
    if ((lane & 7) == 0) cand[72 + (tid >> 3)] = m3;
    if (w == 0 && lane < 32) cand[(lane < 8) ? 64 + lane : 96 + lane] = (lane < 8) ? mykey : 0u;
  };
  if constexpr (ROLE == 1) {
    if (ffn && !FORCEFB) rows_dma(ro + al);
    check();
    DRAIN();
  } else if (ffn) {
    if (LPOS) { SLEEP(LSLEEP); down_dma(KN, 4, lane); } else { DRAIN(); gsync0(ca, 12); get_bd(0, 8); }
  }
  BAR();
  TS(0, 5);
  auto put_out = [&](int j, uint32_t k0, int rk) {
    const uint64_t mine = __ballot(((mykey ^ k0) & ~24u) == 0u) & 0xffull;
    if (mine == 0ull) put_row(hv, j, out, (uint32_t)((8 * g2 + 2 * wl + j) * (D_ * 2)));
    else if (rk >= 2) put_row(nv, j, ws, O_FB + 1152u * (uint32_t)__builtin_ctzll(mine));
  };
  auto rest_out = [&] { put_out(0, keya, rka); put_out(1, keyb, rkb); };
  f32x4 lo, hi;
  auto gate_up = [&] {
    u32x4 av[5];
    const uint32_t aa = lb0 + LA + 16u * lane;
#pragma unroll
    for (int j = 0; j < 5; ++j) asm volatile("ds_read_b128 %0, %1 offset:%2" : "=v"(av[j]) : "v"(aa), "n"(1024 * j) : "memory");
    lo = (f32x4){0.f, 0.f, 0.f, 0.f}; hi = lo;
#pragma unroll
    for (int j = 0; j < 9; ++j) {
      if (j < 5) asm volatile("s_waitcnt lgkmcnt(4)" : "+v"(av[j % 5]));
      else if (j == 5) asm volatile("s_waitcnt lgkmcnt(2)" : "+v"(av[0]), "+v"(av[1]));
      else if (j == 7) asm volatile("s_waitcnt lgkmcnt(0)" : "+v"(av[2]), "+v"(av[3]));
      lo = MMB(av[j % 5], bw[2 * j], lo);
      hi = MMB(av[j % 5], bw[2 * j + 1], hi);
      __builtin_amdgcn_sched_barrier(0);
      if (j < 4) asm volatile("ds_read_b128 %0, %1 offset:%2" : "=v"(av[j]) : "v"(aa), "n"(1024 * (j + 5)) : "memory");
    }
  };
  uint32_t fl;
  asm volatile("ds_read_b32 %0, %1" : "=v"(fl) : "v"(ca + 20) : "memory");
  if (ROLE == 1) gate_up();
  asm volatile("s_waitcnt lgkmcnt(0)" : "+v"(fl));
  const bool slow = FORCESLOW || FORCEFB || __builtin_amdgcn_readfirstlane(fl) != 0;
  if (__builtin_expect(slow, 0)) {
    TSC(17);
    pick(IC<256>{}, IC<64>{});
    mykey = sel[16 + (lane & 7)];
    ro = ((mykey >> 3) & 0x3ffu) * 1152u;
    if (ROLE == 1 && (k3rd > (uint32_t)__builtin_amdgcn_readlane(mykey, 7) || (mykey & 24u) == 8u)) cnt[11] = 1u;
    BAR();
    if (FORCESLOW > 1 || FORCEFB || __builtin_amdgcn_readfirstlane(cnt[11]) != 0) {
    u32x4 key;
#pragma unroll 1
    for (int spin = 0; spin < SPIN_CAP; ++spin) {
      LD16W(key, O_KF + 16u * tid);
      if (!__any(key[1] != T || key[3] != T)) break;
      SLEEP(KBACK);
    }
    const uint32_t ib = (uint32_t)(((255 - (tid >> 1)) << 5) | (7 - 4 * (tid & 1)));
    uint32_t k0 = (key[0] << 16) | ib, k1 = (key[0] & 0xffff0000u) | (ib - 1), k2 = (key[2] << 16) | (ib - 2), k3 = (key[2] & 0xffff0000u) | (ib - 3);
    CEX(k0, k1) CEX(k2, k3) CEX(k0, k2) CEX(k1, k3) CEX(k1, k2)
#pragma unroll 1
    for (int c = 0; c < C_; ++c) {
      const uint32_t mx = wave_maxu(k0);
      const bool hit = (k0 == mx);
      k0 = hit ? k1 : k0; k1 = hit ? k2 : k1; k2 = hit ? k3 : k2; k3 = hit ? 0u : k3;
      if (lane == 0) cand[64 + 8 * w + c] = mx;
    }
    pick(IC<256>{}, IC<128>{});
    mykey = sel[32 + (lane & 7)];
    const uint32_t okey = sel[32 + (lane >> 3)];
    const uint64_t same = __ballot(((mykey ^ okey) & 0x1fe0u) == 0u && okey > mykey);
    const int sl = FORCEFB + __popcll((same >> (lane & 7)) & 0x0101010101010101ull);
    ro = (sl < 2) ? (((mykey >> 3) & 0x3ffu) + 3 - sl) * 1152u : O_FB - O_XC + 1152u * (lane & 7);
    }
    if (ln2) rest_out();
    if (ffn) {
      if (ROLE == 1) rows(ro + al, [] {});
      BAR();
      if (ROLE == 1) gate_up();
    }
  }
  if (OPOS == 0 && ROLE == 2 && ffn && !slow) { SLEEP(OD); rest_out(); }
  if (!ffn) {
    if (ln2 && !slow) { SLEEP(OD2); rest_out(); }
    TSE();
    return;
  }
  uint32_t fx = 0, fv = 0, fn = 0, fo = 0, sa[9];
  float rs, hx, xs;
  u32x4 se;
  if constexpr (ROLE == 1) {
    f32x4 lh = (lane < 32) ? lo : hi;
    float fa = fold32(lh[0], lh[2]), fb2 = fold32(lh[1], lh[3]);
    if (__builtin_expect(__any(__builtin_isunordered(fa, fb2)) && !slow, 0)) {
      gsync0(ca + 4, 12);
      rows(ro + al, [] {});
      gsync0(ca + 4, 16);
      gate_up();
      lh = (lane < 32) ? lo : hi;
      fa = fold32(lh[0], lh[2]); fb2 = fold32(lh[1], lh[3]);
    }
    const float g = __int_as_float(__builtin_amdgcn_update_dpp(__float_as_int(fa), __float_as_int(fb2), 0x128, 0xf, 0xc, false));
    const float u = __int_as_float(__builtin_amdgcn_update_dpp(__float_as_int(fb2), __float_as_int(fa), 0x128, 0xf, 0x3, false));
    const uint32_t gu = pack2(g, u);
    const float gp = bflo(gu);
    const float sg = rbf(gp * __builtin_amdgcn_rcpf(1.f + __expf(-gp)));
    *(u16*)((char*)hm + ha) = f2bf(sg * bfhi(gu));
  } else if (LPOS) {
    DRAIN();
    gsync0(ca, 12);
    get_bd(0, 8);
  }
  BAR();
  TS(0, 6);
  if (ROLE == 2 || w == 2) {
    const u32x4 a0 = *(const u32x4*)((char*)hm + h0a), a1 = *(const u32x4*)((char*)hm + h1a);
    if (ROLE == 1) get_bd(0, 4);
    const f32x4 z = {0.f, 0.f, 0.f, 0.f};
    f32x4 o[4];
#pragma unroll
    for (int t = 0; t < 2 * ROLE; ++t) o[t] = MMB(a1, bw[2 * t + 1], MMB(a0, bw[2 * t], z));
#pragma unroll
    for (int t = 0; t < ROLE; ++t) {
      u32x4 sv;
#pragma unroll
      for (int r = 0; r < 4; ++r) sv[r] = min(pack2(o[2 * t][r], o[2 * t + 1][r]), 0xfffffffeu);
      ST16(so + 4096u * t, sv);
    }
    TS(0, 13);
  }
  if (OPOS == 1 && ROLE == 2) {
    SLEEP(OSLEEP);
    if (!slow) rest_out();
  }
  if (ROLE == 1 && w == 1) {
    const int q = (fb & 15) >> 2, ts = 4 * (q & 1) + ((i & 7) >> 1);
    const int dd = 64 * (fb >> 4) + 16 * (2 * (i & 1) + (q >> 1)) + 4 * (fb & 3) + 2 * (kq & 1) + (kq >> 1);
    const uint32_t fkey = (uint32_t)__builtin_amdgcn_ds_bpermute(4 * ts, (int)mykey), fr = (uint32_t)__builtin_amdgcn_ds_bpermute(4 * ts, (int)ro);
    const int ft = 2047 - (int)(((fkey >> 2) & 0x7f8u) | (fkey & 7u));
    fo = (uint32_t)(ft * D_ + dd) * 2u;
    u32x2 fs;
    asm volatile("global_load_ushort %0, %4, %8\n\tglobal_load_ushort %1, %5, %9 sc1\n\tglobal_load_ushort %2, %6, %9 sc1\n\tglobal_load_dwordx2 %3, %7, %9 sc1"
                 : "=&v"(fx), "=&v"(fv), "=&v"(fn), "=&v"(fs) : "v"(fo), "v"(v_at(dd, ft >> 3) + 2u * (ft & 7)), "v"(O_XC + fr + 2u * dd), "v"(O_SC + 16u * dd), "s"(x), "s"(ws) : "memory");
    uint32_t t16 = (((T >> (3 * ((ft >> 1) & 3))) & LMASK) | (((T >> (12 + ((ft >> 1) & 3))) & 0x00010001u) << 14)) >> (16 * (ft & 1)) & 0xffffu;
    const uint32_t o = fkey >> 16;
    const float rs0 = bf2f((u16)((o & 0x8000u) ? (o & 0x7fffu) : (~o & 0xffffu)));
    rs = rbf(__builtin_amdgcn_rcpf(1.f + __expf(-rs0)));
    const uint32_t one = (i < 8) ? (0x3f80u << (16 * (i & 1))) : 0u;
    se = (u32x4){(i >> 1) == 0 ? one : 0u, (i >> 1) == 1 ? one : 0u, (i >> 1) == 2 ? one : 0u, (i >> 1) == 3 ? one : 0u};
#pragma unroll
    for (int m = 0; m < 9; ++m) sa[m] = O_SL + (uint32_t)(16 * m + (lane >> 2)) * SP + (uint32_t)(4 * fb + (lane & 3)) * 16u;
    asm volatile("" : "+v"(t16), "+v"(rs), "+v"(se), "+v"(sa[0]), "+v"(sa[1]), "+v"(sa[2]), "+v"(sa[3]), "+v"(sa[4]), "+v"(sa[5]), "+v"(sa[6]), "+v"(sa[7]), "+v"(sa[8]));
    SLEEP(SSLEEP);
    if (FD) until(tk + FD);
    TS(0, 1);
    float sum;
#pragma unroll 1
    for (int spin = 0; spin < SPIN_CAP; ++spin) {
      u32x4 sv[9];
#pragma unroll
      for (int m = 0; m < 9; ++m) LD16(sv[m], sa[m]);
      asm volatile("s_waitcnt vmcnt(9)" : "+v"(fx), "+v"(fv), "+v"(fn), "+v"(fs));
      if (__builtin_expect(__any(fs[0] != T), 0)) {
        for (int k = 0; k < SPIN_CAP && fs[0] != T; ++k)
          asm volatile("global_load_dwordx2 %0, %1, %2 sc1\n\ts_waitcnt vmcnt(0)" : "=&v"(fs) : "v"(O_SC + 16u * dd), "s"(ws) : "memory");
      }
      const uint32_t f1 = fv ^ t16, f2 = (f1 & 7u) ? fv & ~7u : f1;
      hx = rbf(bf2f((u16)fx) + (float)__builtin_bit_cast(_Float16, (u16)f2) * __uint_as_float(fs[1]));
      xs = bf2f((u16)fn);
      asm volatile("" : "+v"(hx), "+v"(xs));
      f32x4 dv = {0.f, 0.f, 0.f, 0.f};
      static_for<0, 9>([&](auto mm) {
        constexpr int m = mm.value;
        asm volatile("s_waitcnt vmcnt(%1)" : "+v"(sv[m]) : "n"(8 - m));
        dv = MMB(sv[m], se, dv);
        __builtin_amdgcn_sched_barrier(0);
      });
      sum = fold16(fold32(dv[0], dv[1]), fold32(dv[2], dv[3]));
      if (__builtin_expect(!__any(sum != sum), 1)) break;
      uint32_t mx = 0u;
#pragma unroll
      for (int m = 0; m < 9; ++m) mx = max(mx, max4(sv[m]));
      if (!__any(mx == 0xffffffffu)) break;
      TSC(18);
      SLEEP(PSLEEP);
    }
    const float o1 = rbf(xs + rbf(rbf(sum) * rs));
    if (i < 8) asm volatile("global_store_short %0, %1, %2 sc1" : : "v"(fo), "v"((uint32_t)f2bf(hx + o1)), "s"(out) : "memory");
    TS(0, 2);
  }
  TSE();
  }
  }
}
#define BROT "192"
#define CPF_A "s_getpc_b64 s[96:97]\n\ts_lshl_b32 s99, s2, 4\n\ts_lshl_b32 s84, s98, 7\n\ts_add_u32 s99, s99, s84\n\tv_and_b32 v112, 1, v0\n\t" \
              "v_lshl_add_u32 v112, v112, 12, s99\n\tv_min_u32 v112, kf_end-kf_all-0x80, v112\n\ts_mov_b64 exec, 3\n\tglobal_load_dword v112, v112, s[96:97]\n\ts_mov_b64 exec, -1\n\t"
extern "C" __global__ __launch_bounds__(512, 4) void kf_all(KARGS) {
  __shared__ __attribute__((aligned(16))) char smem[SMEM];
  asm volatile("s_load_dwordx16 s[64:79], s[0:1], 0x0\n\ts_load_dwordx4 s[80:83], s[0:1], 0x40\n\tv_readfirstlane_b32 s98, v0\n\t"
               "s_add_u32 s84, s2, " BROT "\n\ts_xor_b32 s84, s84, s2\n\ts_bitset0_b32 s84, 8\n\ts_bitset0_b32 s84, 9\n\ts_xor_b32 s2, s2, s84\n\t"
               "v_mov_b32 v109, 0\n\ts_and_b32 s85, s2, 7\n\ts_bitcmp1_b32 s2, 8\n\ts_cbranch_scc1 1f\n\t" CPF_A "s_bitcmp1_b32 s98, 8\n\ts_cbranch_scc1 2f\n\t"
               "s_andn2_b32 s84, s2, 31\n\ts_lshl2_add_u32 s84, s85, s84\n\ts_bfe_u32 s85, s2, 0x20003\n\ts_or_b32 s84, s84, s85\n\t"
               "s_mulk_i32 s84, 0x2400\n\ts_mulk_i32 s98, 36\n\ts_add_u32 s84, s84, s98\n\tv_and_b32 v110, 63, v0\n\tv_and_b32 v111, 32, v0\n\t"
               "v_lshlrev_b32 v111, 5, v111\n\tv_lshl_add_u32 v111, v110, 2, v111\n\tv_lshl_add_u32 v110, v110, 4, s84\n\tv_add_u32 v111, s84, v111\n\t"
               "s_cmpk_lt_u32 s2, 0x90\n\ts_waitcnt lgkmcnt(0)\n\t"
               "global_load_dwordx4 v[100:103], v110, s[64:65]\n\tglobal_load_dwordx4 v[104:107], v110, s[64:65] offset:1152\n\t"
               "global_load_dword v108, v111, s[64:65] offset:1024\n\tglobal_load_dword v109, v109, s[82:83] sc1\n\t"
               "s_cbranch_scc1 kf_n\n\ts_branch kf_h\n"
               "1:\n\ts_bfe_u32 s84, s2, 0x50003\n\ts_lshl_b32 s85, s85, 5\n\ts_or_b32 s84, s84, s85\n\ts_lshl_b32 s84, s84, 2\n\t"
               "v_and_b32 v110, 0xff, v0\n\tv_bfe_u32 v107, v0, 7, 1\n"
               "4:\n\ts_lshl_b32 s85, s84, 7\n\tv_lshl_add_u32 v110, v110, 1, s85\n\tv_lshl_add_u32 v107, v107, 1, s84\n\ts_and_b32 s85, s98, s2\n\ts_waitcnt lgkmcnt(0)\n\t"
               "global_load_ushort v100, v107, s[78:79]\n\tglobal_load_ushort v101, v110, s[66:67]\n\tglobal_load_ushort v102, v110, s[68:69]\n\t"
               "global_load_dword v109, v109, s[82:83] sc1\n\ts_bitcmp1_b32 s85, 8\n\ts_cbranch_scc1 kf_u\n\t"
               "global_load_ushort v103, v110, s[70:71]\n\tglobal_load_ushort v104, v110, s[72:73]\n\tglobal_load_ushort v105, v110, s[74:75]\n\t"
               "global_load_ushort v106, v110, s[76:77]\n\tglobal_load_ushort v107, v107, s[80:81]\n\ts_bitcmp1_b32 s2, 8\n\ts_cbranch_scc1 kf_c\n"
               "6:\n\ts_barrier\n\ts_branch kf_p\n"
               "2:\n\ts_cmpk_lt_u32 s2, 0xc0\n\ts_cbranch_scc1 6b\n\t"
               "s_lshr_b32 s84, s2, 3\n\ts_lshl3_add_u32 s84, s85, s84\n\ts_lshl1_add_u32 s84, s84, 976\n\t"
               "v_and_b32 v110, 0x7f, v0\n\tv_mov_b32 v107, 0\n\ts_branch 4b"
               : : "s"(__builtin_amdgcn_kernarg_segment_ptr()), "s"(blockIdx.x), "v"(threadIdx.x),
                   "s"((uint32_t)(uintptr_t)(__attribute__((address_space(3))) char*)smem) : "scc", "s96", "s97", "s98", "s99", "v127");
}
extern "C" __global__ __launch_bounds__(512, 4) void kf_c(KARGS) {
  __shared__ __attribute__((aligned(16))) char smem[SMEM];
  role_body<0>(smem, KPASS);
}
extern "C" __global__ __launch_bounds__(512, 4) void kf_u(KARGS) {
  __shared__ __attribute__((aligned(16))) char smem[SMEM];
  role_body<3>(smem, KPASS);
}
extern "C" __global__ __launch_bounds__(512, 4) void kf_n(KARGS) {
  __shared__ __attribute__((aligned(16))) char smem[SMEM];
  role_body<1, 1>(smem, KPASS);
}
extern "C" __global__ __launch_bounds__(512, 4) void kf_h(KARGS) {
  __shared__ __attribute__((aligned(16))) char smem[SMEM];
  role_body<1, 0>(smem, KPASS);
}
extern "C" __global__ __launch_bounds__(512, 4) void kf_p(KARGS) {
  __shared__ __attribute__((aligned(16))) char smem[SMEM];
  role_body<2>(smem, KPASS);
}
extern "C" __global__ __launch_bounds__(512, 4) void kf_conv() { asm volatile(CONV_ASM ::: CONV_CLOB); }
extern "C" __global__ void kf_end() {}

extern "C" __global__ __launch_bounds__(1024) void k_probe(int* dummy) {
  __shared__ int sm[2];
  const uint32_t now = (uint32_t)__builtin_amdgcn_s_memrealtime();
  __shared__ int sn;
  if (threadIdx.x == 0) { sm[0] = 0; sm[1] = 1 == 1 ? 0x7fffffff : 0; sn = 0; }
  __syncthreads();
  for (int k = threadIdx.x; k < 4096; k += 1024) {
    const uint32_t a = (uint32_t)g_prb[k], b = (uint32_t)(g_prb[k] >> 32);
    if (a) atomicMax(&sm[0], (int)(now - a));
    if (b) { if (1 == 1) atomicMin(&sm[1], (int)(now - b)); else if (1 == 0) atomicMax(&sm[1], (int)(now - b)); else { atomicAdd(&sm[1], (int)(now - b)); atomicAdd(&sn, 1); } }
  }
  __syncthreads();
  const int q = min(max(sm[0] * 10 - (1 == 2 ? sm[1] * 10 / max(sn, 1) : sm[1] * 10), 0), 30000);
  if (threadIdx.x == 0) while ((int)((uint32_t)__builtin_amdgcn_s_memrealtime() - now) < q) __builtin_amdgcn_s_sleep(2);
}
extern "C" void kernel_launch(void* const* d_in, const int* in_sizes, int n_in,
                              void* d_out, int out_size, void* d_ws, size_t ws_size,
                              hipStream_t stream) {
  kf_all<<<512, 512, 0, stream>>>((const u16*)d_in[0], (const u16*)d_in[1], (const u16*)d_in[2], (const u16*)d_in[3], (const u16*)d_in[4],
                                  (const u16*)d_in[5], (const u16*)d_in[6], (const u16*)d_in[7], (const u16*)d_in[8], (char*)d_ws + (size_t)WSOFF * 1048576,
                                  (const u16*)d_in[10], (const u16*)d_in[11], (const u16*)d_in[12], (const u16*)d_in[13], (u16*)d_out);
  k_probe<<<1, 1024, 0, stream>>>((int*)d_ws);
}
```

```cpp
#include <hip/hip_runtime.h>
#include <hip/hip_bf16.h>
#include <stdint.h>

typedef __attribute__((ext_vector_type(8))) short bf16x8;
typedef __attribute__((ext_vector_type(4))) float f32x4;
typedef uint16_t u16;
typedef __attribute__((ext_vector_type(4))) uint32_t u32x4;
typedef __attribute__((ext_vector_type(2))) uint32_t u32x2;
#define TSD
#define TS(k, i)
#define TSC(i)
#define TSE()
#define TSP
#define TSA

constexpr int L_ = 2048, D_ = 576, N_ = 128, F_ = 4608, C_ = 8;
constexpr int NF = 144;
constexpr uint32_t O_EP = 0;
constexpr uint32_t O_KF = 4096;
constexpr uint32_t O_KEY = O_KF + 8192;
constexpr uint32_t O_SC = O_KEY + 32768;
constexpr uint32_t O_DN = O_SC + 9216;
constexpr uint32_t O_SB = O_DN + 20480;
constexpr uint32_t O_SU = O_SB + 65536;
constexpr uint32_t O_FB = O_SU + 262144;
constexpr uint32_t O_XC = O_FB + 8 * 1152;
constexpr uint32_t SP = 9216 + 128;
constexpr uint32_t O_SL = O_XC + 1024 * 1152;
constexpr uint32_t SLU = 336;
constexpr uint32_t O_U = O_SL + 256 * SLU * 16;
constexpr int UP = 584;
constexpr uint32_t VP = 8192 + 128, VPP = 4096 + 128;
constexpr uint32_t O_V = O_U + 256 * UP * 16, O_VP = O_V + 256 * VP;
static_assert(256 * SLU * 16 >= NF * SP && O_SL % 128 == 0 && O_U % 128 == 0 && O_V % 128 == 0, "");
__host__ __device__ __forceinline__ uint32_t u_at(int tg, int d) { return O_U + 16u * (uint32_t)((tg >> 1) * (UP * 2) + (d >> 2) * 8 + (tg & 1) * 4 + (d & 3)); }
__host__ __device__ __forceinline__ uint32_t v_at(int d, int tg) {
  return (d < 512) ? O_V + (uint32_t)(d >> 1) * VP + 16u * (uint32_t)(2 * tg + (d & 1)) : O_VP + (uint32_t)(d - 512) * VPP + 16u * (uint32_t)tg;
}
#define HAVE_VAT
#define HAVE_VTAG
__device__ __forceinline__ float bf2f(u16 v) { return __uint_as_float((uint32_t)v << 16); }
__device__ __forceinline__ float bflo(uint32_t v) { return __uint_as_float(v << 16); }
__device__ __forceinline__ float bfhi(uint32_t v) { return __uint_as_float(v & 0xffff0000u); }
__device__ __forceinline__ u16 f2bf(float f) { return __builtin_bit_cast(u16, (__bf16)f); }
__device__ __forceinline__ float rbf(float f) { return bf2f(f2bf(f)); }
typedef __attribute__((ext_vector_type(2))) float f32x2;
typedef __attribute__((ext_vector_type(2))) __bf16 bf16x2v;
__device__ __forceinline__ uint32_t pack2(float a, float b) {
  return __builtin_bit_cast(uint32_t, __builtin_convertvector((f32x2){a, b}, bf16x2v));
}

#define DPP_I(v, ctrl, rmask) __builtin_amdgcn_update_dpp(0, (v), (ctrl), (rmask), 0xf, true)
__device__ __forceinline__ float row_sum(float v) {
  v += __int_as_float(DPP_I(__float_as_int(v), 0x111, 0xf));
  v += __int_as_float(DPP_I(__float_as_int(v), 0x112, 0xf));
  v += __int_as_float(DPP_I(__float_as_int(v), 0x114, 0xf));
  v += __int_as_float(DPP_I(__float_as_int(v), 0x118, 0xf));
  return v;
}
__device__ __forceinline__ float rdl(float v, int l) { return __int_as_float(__builtin_amdgcn_readlane(__float_as_int(v), l)); }
__device__ __forceinline__ float wave_sum(float v) {
  v = row_sum(v);
  asm("s_nop 1\n\tv_add_f32_dpp %0, %0, %0 row_bcast:15 row_mask:0xa bank_mask:0xf\n\t"
      "s_nop 1\n\tv_add_f32_dpp %0, %0, %0 row_bcast:31 row_mask:0xc bank_mask:0xf\n\ts_nop 0" : "+v"(v));
  return rdl(v, 63);
}
__device__ __forceinline__ float fold32(float a, float b) {
  const auto r = __builtin_amdgcn_permlane32_swap(__float_as_uint(a), __float_as_uint(b), false, false);
  return __uint_as_float(r[0]) + __uint_as_float(r[1]);
}
__device__ __forceinline__ float fold16(float a, float b) {
  const auto r = __builtin_amdgcn_permlane16_swap(__float_as_uint(a), __float_as_uint(b), false, false);
  return __uint_as_float(r[0]) + __uint_as_float(r[1]);
}
__device__ __forceinline__ void wave_sum2(float& a, float& b) {
  float v = row_sum(fold32(a, b));
  asm("s_nop 1\n\tv_add_f32_dpp %0, %0, %0 row_bcast:15 row_mask:0xa bank_mask:0xf\n\ts_nop 0" : "+v"(v));
  a = rdl(v, 31); b = rdl(v, 63);
}
__device__ __forceinline__ float dot2(uint32_t a, uint32_t b, float c) {
  return __builtin_amdgcn_fdot2_f32_bf16(__builtin_bit_cast(bf16x2v, a), __builtin_bit_cast(bf16x2v, b), c, false);
}
__device__ __forceinline__ uint32_t row_maxu(uint32_t v) {
  v = max(v, (uint32_t)DPP_I((int)v, 0xb1, 0xf));
  v = max(v, (uint32_t)DPP_I((int)v, 0x4e, 0xf));
  v = max(v, (uint32_t)DPP_I((int)v, 0x141, 0xf));
  v = max(v, (uint32_t)DPP_I((int)v, 0x140, 0xf));
  return v;
}
__device__ __forceinline__ uint32_t wave_maxu(uint32_t v) {
  v = max(v, (uint32_t)DPP_I((int)v, 0x111, 0xf));
  v = max(v, (uint32_t)DPP_I((int)v, 0x112, 0xf));
  v = max(v, (uint32_t)DPP_I((int)v, 0x114, 0xf));
  v = max(v, (uint32_t)DPP_I((int)v, 0x118, 0xf));
  v = max(v, (uint32_t)DPP_I((int)v, 0x142, 0xa));
  v = max(v, (uint32_t)DPP_I((int)v, 0x143, 0xc));
  return (uint32_t)__builtin_amdgcn_readlane((int)v, 63);
}

typedef __attribute__((ext_vector_type(8))) _Float16 f16x8;
typedef __attribute__((ext_vector_type(2))) _Float16 f16x2;
__device__ __forceinline__ uint32_t packh(f32x2 v) { return __builtin_bit_cast(uint32_t, __builtin_convertvector(v, f16x2)); }
__device__ __forceinline__ uint32_t packh(float a, float b) { return packh((f32x2){a, b}); }
constexpr int IP = 224, I_X = 128;
__device__ __forceinline__ f32x2 cmul(f32x2 z, f32x2 w) {
  f32x2 r;
  asm("v_pk_mul_f32 %0, %1, %2 op_sel_hi:[0,1]\n\tv_pk_fma_f32 %0, %1, %2, %0 op_sel:[1,1,0] op_sel_hi:[1,0,1] neg_lo:[0,1,0]" : "=&v"(r) : "v"(z), "v"(w));
  return r;
}
__device__ __forceinline__ f32x2 cexp2(float ar2, float air, float l) {
  const float t = air * l;
  return (f32x2){__builtin_amdgcn_cosf(t), __builtin_amdgcn_sinf(t)} * __builtin_amdgcn_exp2f(ar2 * l);
}
__device__ __forceinline__ float state_a(float ldt, float are, float aim, float& ar2, float& air, float& g) {
  const float dt = rbf(__expf(ldt));
  ar2 = dt * are * 1.4426950408889634f;
  air = dt * aim * 0.15915494309189535f;
  air -= __builtin_rintf(air);
  g = 0.f;
  if (__builtin_expect(__any(ar2 > 0.f), 0)) g = fminf(__uint_as_float(wave_maxu(__float_as_uint(fmaxf(ar2, 0.f)))), 1.f);
  return dt;
}
template <int N> __device__ __forceinline__ void put_im(char* p, int im, const f32x2* z) {
  *(u32x4*)p = (u32x4){packh(z[0][0], z[1][0]), packh(z[2][0], z[3][0]), packh(z[4][0], z[5][0]), packh(z[6][0], z[7][0])};
  *(u32x4*)(p + im) = (u32x4){packh(z[0][1], z[1][1]), packh(z[2][1], z[3][1]), packh(z[4][1], z[5][1]), packh(z[6][1], z[7][1])};
}
#define Y_ASM \
  "v_mov_b64 v[40:41], %0\nv_pk_mul_f32 v[42:43], v[40:41], %1 op_sel_hi:[0,1]\nv_pk_mul_f32 v[44:45], %0, %2 op_sel_hi:[0,1]\n" \
  "v_pk_fma_f32 v[42:43], v[40:41], %1, v[42:43] op_sel:[1,1,0] op_sel_hi:[1,0,1] neg_lo:[0,1,0]\n" \
  "v_pk_fma_f32 v[44:45], %0, %2, v[44:45] op_sel:[1,1,0] op_sel_hi:[1,0,1] neg_lo:[0,1,0]\nv_cvt_pk_f16_f32 v56, v40, v42\n" \
  "v_cvt_pk_f16_f32 v60, v41, v43\nv_pk_mul_f32 v[40:41], v[42:43], %1 op_sel_hi:[0,1]\nv_pk_mul_f32 v[48:49], v[44:45], %2 op_sel_hi:[0,1]\n" \
  "v_pk_fma_f32 v[40:41], v[42:43], %1, v[40:41] op_sel:[1,1,0] op_sel_hi:[1,0,1] neg_lo:[0,1,0]\n" \
  "v_pk_fma_f32 v[48:49], v[44:45], %2, v[48:49] op_sel:[1,1,0] op_sel_hi:[1,0,1] neg_lo:[0,1,0]\n" \
  "v_pk_mul_f32 v[42:43], v[40:41], %1 op_sel_hi:[0,1]\nv_pk_mul_f32 v[52:53], v[48:49], %2 op_sel_hi:[0,1]\n" \
  "v_pk_fma_f32 v[42:43], v[40:41], %1, v[42:43] op_sel:[1,1,0] op_sel_hi:[1,0,1] neg_lo:[0,1,0]\n" \
  "v_pk_fma_f32 v[52:53], v[48:49], %2, v[52:53] op_sel:[1,1,0] op_sel_hi:[1,0,1] neg_lo:[0,1,0]\nv_cvt_pk_f16_f32 v57, v40, v42\n" \
  "v_cvt_pk_f16_f32 v61, v41, v43\nv_pk_mul_f32 v[40:41], v[42:43], %1 op_sel_hi:[0,1]\n" \
  "v_pk_fma_f32 v[40:41], v[42:43], %1, v[40:41] op_sel:[1,1,0] op_sel_hi:[1,0,1] neg_lo:[0,1,0]\n" \
  "v_pk_mul_f32 v[42:43], v[40:41], %1 op_sel_hi:[0,1]\nv_pk_mul_f32 v[46:47], v[44:45], %1 op_sel_hi:[0,1]\n" \
  "v_pk_fma_f32 v[42:43], v[40:41], %1, v[42:43] op_sel:[1,1,0] op_sel_hi:[1,0,1] neg_lo:[0,1,0]\n" \
  "v_pk_fma_f32 v[46:47], v[44:45], %1, v[46:47] op_sel:[1,1,0] op_sel_hi:[1,0,1] neg_lo:[0,1,0]\nv_cvt_pk_f16_f32 v58, v40, v42\n" \
  "v_cvt_pk_f16_f32 v62, v41, v43\nv_cvt_pk_f16_f32 v64, v44, v46\nv_cvt_pk_f16_f32 v68, v45, v47\n" \
  "v_pk_mul_f32 v[40:41], v[42:43], %1 op_sel_hi:[0,1]\nv_pk_mul_f32 v[44:45], v[46:47], %1 op_sel_hi:[0,1]\n" \
  "v_pk_fma_f32 v[40:41], v[42:43], %1, v[40:41] op_sel:[1,1,0] op_sel_hi:[1,0,1] neg_lo:[0,1,0]\n" \
  "v_pk_fma_f32 v[44:45], v[46:47], %1, v[44:45] op_sel:[1,1,0] op_sel_hi:[1,0,1] neg_lo:[0,1,0]\n" \
  "v_pk_mul_f32 v[42:43], v[40:41], %1 op_sel_hi:[0,1]\nv_pk_mul_f32 v[46:47], v[44:45], %1 op_sel_hi:[0,1]\n" \
  "v_pk_fma_f32 v[42:43], v[40:41], %1, v[42:43] op_sel:[1,1,0] op_sel_hi:[1,0,1] neg_lo:[0,1,0]\n" \
  "v_pk_fma_f32 v[46:47], v[44:45], %1, v[46:47] op_sel:[1,1,0] op_sel_hi:[1,0,1] neg_lo:[0,1,0]\nv_cvt_pk_f16_f32 v59, v40, v42\n" \
  "v_cvt_pk_f16_f32 v63, v41, v43\nv_cvt_pk_f16_f32 v65, v44, v46\nv_cvt_pk_f16_f32 v69, v45, v47\n" \
  "v_pk_mul_f32 v[44:45], v[46:47], %1 op_sel_hi:[0,1]\nds_write_b128 %3, v[56:59] offset:0\n" \
  "v_pk_fma_f32 v[44:45], v[46:47], %1, v[44:45] op_sel:[1,1,0] op_sel_hi:[1,0,1] neg_lo:[0,1,0]\nds_write_b128 %3, v[60:63] offset:64\n" \
  "v_pk_mul_f32 v[46:47], v[44:45], %1 op_sel_hi:[0,1]\nv_pk_mul_f32 v[50:51], v[48:49], %1 op_sel_hi:[0,1]\n" \
  "v_pk_fma_f32 v[46:47], v[44:45], %1, v[46:47] op_sel:[1,1,0] op_sel_hi:[1,0,1] neg_lo:[0,1,0]\n" \
  "v_pk_fma_f32 v[50:51], v[48:49], %1, v[50:51] op_sel:[1,1,0] op_sel_hi:[1,0,1] neg_lo:[0,1,0]\nv_cvt_pk_f16_f32 v66, v44, v46\n" \
  "v_cvt_pk_f16_f32 v70, v45, v47\nv_cvt_pk_f16_f32 v56, v48, v50\nv_cvt_pk_f16_f32 v60, v49, v51\n" \
  "v_pk_mul_f32 v[44:45], v[46:47], %1 op_sel_hi:[0,1]\nv_pk_mul_f32 v[48:49], v[50:51], %1 op_sel_hi:[0,1]\n" \
  "v_pk_fma_f32 v[44:45], v[46:47], %1, v[44:45] op_sel:[1,1,0] op_sel_hi:[1,0,1] neg_lo:[0,1,0]\n" \
  "v_pk_fma_f32 v[48:49], v[50:51], %1, v[48:49] op_sel:[1,1,0] op_sel_hi:[1,0,1] neg_lo:[0,1,0]\n" \
  "v_pk_mul_f32 v[46:47], v[44:45], %1 op_sel_hi:[0,1]\nv_pk_mul_f32 v[50:51], v[48:49], %1 op_sel_hi:[0,1]\n" \
  "v_pk_fma_f32 v[46:47], v[44:45], %1, v[46:47] op_sel:[1,1,0] op_sel_hi:[1,0,1] neg_lo:[0,1,0]\n" \
  "v_pk_fma_f32 v[50:51], v[48:49], %1, v[50:51] op_sel:[1,1,0] op_sel_hi:[1,0,1] neg_lo:[0,1,0]\nv_cvt_pk_f16_f32 v67, v44, v46\n" \
  "v_cvt_pk_f16_f32 v71, v45, v47\nv_cvt_pk_f16_f32 v57, v48, v50\nv_cvt_pk_f16_f32 v61, v49, v51\n" \
  "v_pk_mul_f32 v[48:49], v[50:51], %1 op_sel_hi:[0,1]\nds_write_b128 %3, v[64:67] offset:16\n" \
  "v_pk_fma_f32 v[48:49], v[50:51], %1, v[48:49] op_sel:[1,1,0] op_sel_hi:[1,0,1] neg_lo:[0,1,0]\nds_write_b128 %3, v[68:71] offset:80\n" \
  "v_pk_mul_f32 v[50:51], v[48:49], %1 op_sel_hi:[0,1]\nv_pk_mul_f32 v[54:55], v[52:53], %1 op_sel_hi:[0,1]\n" \
  "v_pk_fma_f32 v[50:51], v[48:49], %1, v[50:51] op_sel:[1,1,0] op_sel_hi:[1,0,1] neg_lo:[0,1,0]\n" \
  "v_pk_fma_f32 v[54:55], v[52:53], %1, v[54:55] op_sel:[1,1,0] op_sel_hi:[1,0,1] neg_lo:[0,1,0]\nv_cvt_pk_f16_f32 v58, v48, v50\n" \
  "v_cvt_pk_f16_f32 v62, v49, v51\nv_cvt_pk_f16_f32 v64, v52, v54\nv_cvt_pk_f16_f32 v68, v53, v55\n" \
  "v_pk_mul_f32 v[48:49], v[50:51], %1 op_sel_hi:[0,1]\nv_pk_mul_f32 v[52:53], v[54:55], %1 op_sel_hi:[0,1]\n" \
  "v_pk_fma_f32 v[48:49], v[50:51], %1, v[48:49] op_sel:[1,1,0] op_sel_hi:[1,0,1] neg_lo:[0,1,0]\n" \
  "v_pk_fma_f32 v[52:53], v[54:55], %1, v[52:53] op_sel:[1,1,0] op_sel_hi:[1,0,1] neg_lo:[0,1,0]\n" \
  "v_pk_mul_f32 v[50:51], v[48:49], %1 op_sel_hi:[0,1]\nv_pk_mul_f32 v[54:55], v[52:53], %1 op_sel_hi:[0,1]\n" \
  "v_pk_fma_f32 v[50:51], v[48:49], %1, v[50:51] op_sel:[1,1,0] op_sel_hi:[1,0,1] neg_lo:[0,1,0]\n" \
  "v_pk_fma_f32 v[54:55], v[52:53], %1, v[54:55] op_sel:[1,1,0] op_sel_hi:[1,0,1] neg_lo:[0,1,0]\nv_cvt_pk_f16_f32 v59, v48, v50\n" \
  "v_cvt_pk_f16_f32 v63, v49, v51\nv_cvt_pk_f16_f32 v65, v52, v54\nv_cvt_pk_f16_f32 v69, v53, v55\n" \
  "v_pk_mul_f32 v[52:53], v[54:55], %1 op_sel_hi:[0,1]\nds_write_b128 %3, v[56:59] offset:32\n" \
  "v_pk_fma_f32 v[52:53], v[54:55], %1, v[52:53] op_sel:[1,1,0] op_sel_hi:[1,0,1] neg_lo:[0,1,0]\nds_write_b128 %3, v[60:63] offset:96\n" \
  "v_pk_mul_f32 v[54:55], v[52:53], %1 op_sel_hi:[0,1]\n" \
  "v_pk_fma_f32 v[54:55], v[52:53], %1, v[54:55] op_sel:[1,1,0] op_sel_hi:[1,0,1] neg_lo:[0,1,0]\nv_cvt_pk_f16_f32 v66, v52, v54\n" \
  "v_cvt_pk_f16_f32 v70, v53, v55\nv_pk_mul_f32 v[52:53], v[54:55], %1 op_sel_hi:[0,1]\n" \
  "v_pk_fma_f32 v[52:53], v[54:55], %1, v[52:53] op_sel:[1,1,0] op_sel_hi:[1,0,1] neg_lo:[0,1,0]\n" \
  "v_pk_mul_f32 v[54:55], v[52:53], %1 op_sel_hi:[0,1]\n" \
  "v_pk_fma_f32 v[54:55], v[52:53], %1, v[54:55] op_sel:[1,1,0] op_sel_hi:[1,0,1] neg_lo:[0,1,0]\nv_cvt_pk_f16_f32 v67, v52, v54\n" \
  "v_cvt_pk_f16_f32 v71, v53, v55\nds_write_b128 %3, v[64:67] offset:48\nds_write_b128 %3, v[68:71] offset:112\n"
#define Y_CLOB "memory", "v40", "v41", "v42", "v43", "v44", "v45", "v46", "v47", "v48", "v49", "v50", "v51", "v52", "v53", "v54", "v55", "v56", "v57", "v58", "v59", "v60", "v61", "v62", "v63", "v64", "v65", "v66", "v67", "v68", "v69", "v70", "v71"
#define T_ASM \
  "ds_read_b128 %4, %8\nds_read_b64_tr_b16 v[80:81], %7 offset:0\nds_read_b64_tr_b16 v[82:83], %7 offset:64\n" \
  "ds_read_b64_tr_b16 v[84:85], %5 offset:0\nds_read_b64_tr_b16 v[86:87], %5 offset:48\nds_read_b64_tr_b16 v[88:89], %6 offset:0\n" \
  "ds_read_b64_tr_b16 v[90:91], %6 offset:48\nds_read_b64_tr_b16 v[92:93], %7 offset:3584\nds_read_b64_tr_b16 v[94:95], %7 offset:3648\n" \
  "ds_read_b64_tr_b16 v[96:97], %5 offset:3584\nds_read_b64_tr_b16 v[98:99], %5 offset:3632\nds_read_b64_tr_b16 v[100:101], %6 offset:3584\n" \
  "ds_read_b64_tr_b16 v[102:103], %6 offset:3632\nds_read_b64_tr_b16 v[104:105], %7 offset:7168\nds_read_b64_tr_b16 v[106:107], %7 offset:7232\n" \
  "s_waitcnt lgkmcnt(8)\nv_mfma_f32_16x16x32_f16 %0, v[84:87], v[80:83], 0\nds_read_b64_tr_b16 v[108:109], %5 offset:7168\n" \
  "ds_read_b64_tr_b16 v[110:111], %5 offset:7216\nds_read_b64_tr_b16 v[112:113], %6 offset:7168\nv_mfma_f32_16x16x32_f16 %2, v[88:91], v[80:83], 0\n" \
  "ds_read_b64_tr_b16 v[114:115], %6 offset:7216\nds_read_b64_tr_b16 v[116:117], %7 offset:10752\nds_read_b64_tr_b16 v[118:119], %7 offset:10816\n" \
  "s_waitcnt lgkmcnt(8)\nv_mfma_f32_16x16x32_f16 %0, v[96:99], v[92:95], %0\nds_read_b64_tr_b16 v[120:121], %5 offset:10752\n" \
  "ds_read_b64_tr_b16 v[122:123], %5 offset:10800\nds_read_b64_tr_b16 v[124:125], %6 offset:10752\n" \
  "v_mfma_f32_16x16x32_f16 %2, v[100:103], v[92:95], %2\nds_read_b64_tr_b16 v[126:127], %6 offset:10800\n" \
  "ds_read_b64_tr_b16 v[80:81], %7 offset:14336\nds_read_b64_tr_b16 v[82:83], %7 offset:14400\ns_waitcnt lgkmcnt(8)\n" \
  "v_mfma_f32_16x16x32_f16 %0, v[108:111], v[104:107], %0\nds_read_b64_tr_b16 v[84:85], %5 offset:14336\n" \
  "ds_read_b64_tr_b16 v[86:87], %5 offset:14384\nds_read_b64_tr_b16 v[88:89], %6 offset:14336\n" \
  "v_mfma_f32_16x16x32_f16 %2, v[112:115], v[104:107], %2\nds_read_b64_tr_b16 v[90:91], %6 offset:14384\n" \
  "ds_read_b64_tr_b16 v[92:93], %7 offset:17920\nds_read_b64_tr_b16 v[94:95], %7 offset:17984\ns_waitcnt lgkmcnt(8)\n" \
  "v_mfma_f32_16x16x32_f16 %0, v[120:123], v[116:119], %0\nds_read_b64_tr_b16 v[96:97], %5 offset:17920\n" \
  "ds_read_b64_tr_b16 v[98:99], %5 offset:17968\nds_read_b64_tr_b16 v[100:101], %6 offset:17920\n" \
  "v_mfma_f32_16x16x32_f16 %2, v[124:127], v[116:119], %2\nds_read_b64_tr_b16 v[102:103], %6 offset:17968\n" \
  "ds_read_b64_tr_b16 v[104:105], %7 offset:21504\nds_read_b64_tr_b16 v[106:107], %7 offset:21568\ns_waitcnt lgkmcnt(8)\n" \
  "v_mfma_f32_16x16x32_f16 %1, v[84:87], v[80:83], 0\nds_read_b64_tr_b16 v[108:109], %5 offset:21504\nds_read_b64_tr_b16 v[110:111], %5 offset:21552\n" \
  "ds_read_b64_tr_b16 v[112:113], %6 offset:21504\nv_mfma_f32_16x16x32_f16 %3, v[88:91], v[80:83], 0\nds_read_b64_tr_b16 v[114:115], %6 offset:21552\n" \
  "ds_read_b64_tr_b16 v[116:117], %7 offset:25088\nds_read_b64_tr_b16 v[118:119], %7 offset:25152\ns_waitcnt lgkmcnt(8)\n" \
  "v_mfma_f32_16x16x32_f16 %1, v[96:99], v[92:95], %1\nds_read_b64_tr_b16 v[120:121], %5 offset:25088\n" \
  "ds_read_b64_tr_b16 v[122:123], %5 offset:25136\nds_read_b64_tr_b16 v[124:125], %6 offset:25088\n" \
  "v_mfma_f32_16x16x32_f16 %3, v[100:103], v[92:95], %3\nds_read_b64_tr_b16 v[126:127], %6 offset:25136\ns_waitcnt lgkmcnt(6)\n" \
  "v_mfma_f32_16x16x32_f16 %1, v[108:111], v[104:107], %1\nv_mfma_f32_16x16x32_f16 %3, v[112:115], v[104:107], %3\ns_waitcnt lgkmcnt(0)\n" \
  "v_mfma_f32_16x16x32_f16 %1, v[120:123], v[116:119], %1\nv_mfma_f32_16x16x32_f16 %3, v[124:127], v[116:119], %3\n"
#define T_CLOB "memory", "v80", "v81", "v82", "v83", "v84", "v85", "v86", "v87", "v88", "v89", "v90", "v91", "v92", "v93", "v94", "v95", "v96", "v97", "v98", "v99", "v100", "v101", "v102", "v103", "v104", "v105", "v106", "v107", "v108", "v109", "v110", "v111", "v112", "v113", "v114", "v115", "v116", "v117", "v118", "v119", "v120", "v121", "v122", "v123", "v124", "v125", "v126", "v127"
template <class F> __device__ __forceinline__ int gen_y(float ldt, float are, float aim, float bre, float bim, float cre, float cim, uint32_t im, F&& mid TSP) {
  float ar2, air, g;
  const float dt = state_a(ldt, are, aim, ar2, air, g);
  const float dbr = dt * bre, dbi = dt * bim, pr = cre * dbr - cim * dbi, pi = cre * dbi + cim * dbr;
  const f32x2 w8 = cexp2(ar2, -air, 8.f), w64 = cexp2(ar2, -air, 64.f);
  const float pm = fmaxf(fabsf(pr), fabsf(pi));
  int e = -4;
  if (__builtin_expect(__any(!(pm < 0x1p-4f) || ar2 > 0.f) || !__any(pm > 0x1p-12f), 0)) e = (int)(wave_maxu(__float_as_uint(pm)) >> 23) - 126 + (int)ceilf(g * 250.f);
  const f32x2 y0 = {__builtin_amdgcn_ldexpf(pr, 14 - e), __builtin_amdgcn_ldexpf(-pi, 14 - e)};
  mid();
  TS(0, 7);
  asm volatile(Y_ASM : : "v"(y0), "v"(w8), "v"(w64), "v"(im) : Y_CLOB);
  return e;
}
__device__ __forceinline__ int gen_x(float ldt, float are, float aim, char* im) {
  float ar2, air, g;
  state_a(ldt, are, aim, ar2, air, g);
  const int e = min((int)ceilf(g * 1810.f), 300);
  const float s = __builtin_amdgcn_ldexpf(1.f, 15 - e);
  const f32x2 w1 = cexp2(ar2, air, 1.f), w256 = cexp2(ar2, air, 256.f), w2 = cmul(w1, w1);
  f32x2 a[8], b[8], c[8];
  a[0] = (f32x2){s, 0.f}; a[1] = w1 * s; a[2] = w2 * s;
  b[0] = b[1] = w256 * s;
  b[2] = cmul(b[0], w2); a[3] = cmul(a[1], w2); a[4] = cmul(a[2], w2);
  b[3] = b[4] = b[5] = cmul(b[2], w2); a[5] = cmul(a[3], w2); a[6] = cmul(a[4], w2);
  b[6] = cmul(b[3], w2); a[7] = cmul(a[5], w2); c[0] = cmul(b[3], w256);
  b[7] = cmul(b[6], w2); c[2] = cmul(c[0], w256);
  put_im<0>(im + I_X, 48, a);
  c[1] = cmul(b[7], w256); c[3] = cmul(c[1], w256);
  put_im<0>(im + I_X + 16, 48, b);
#pragma unroll
  for (int k = 4; k < 8; ++k) c[k] = cmul(c[k - 1], w256);
  put_im<0>(im + I_X + 32, 48, c);
  return e;
}

template <int I> struct IC { static constexpr int value = I; };
template <int B, int E, class F> __device__ __forceinline__ void static_for(F&& f) {
  if constexpr (B < E) { f(IC<B>{}); static_for<B + 1, E>(f); }
}
typedef __attribute__((ext_vector_type(4))) short s16x4;
__device__ __forceinline__ u32x2 trd(uint32_t a) {
  return __builtin_bit_cast(u32x2, __builtin_amdgcn_ds_read_tr16_b64_v4i16((__attribute__((address_space(3))) s16x4*)(uintptr_t)a));
}
#define BAR() asm volatile("s_waitcnt lgkmcnt(0)\n\ts_barrier" ::: "memory")
#define ST16(off, v) asm volatile("global_store_dwordx4 %0, %1, %2 sc1\n\ts_nop 1" : : "v"(off), "v"(v), "s"(ws) : "memory")
#define ST4(off, v) asm volatile("global_store_dword %0, %1, %2 sc1" : : "v"(off), "v"(v), "s"(ws) : "memory")
#define LD16(r, off) asm volatile("global_load_dwordx4 %0, %1, %2 sc1" : "=v"(r) : "v"(off), "s"(ws) : "memory")
#define LD16W(r, off) asm volatile("global_load_dwordx4 %0, %1, %2 sc1\n\ts_waitcnt vmcnt(0)" : "=&v"(r) : "v"(off), "s"(ws) : "memory")
#define DRAIN() asm volatile("s_waitcnt vmcnt(0)" ::: "memory")
#define CEX(a, b) { const uint32_t hi_ = max(a, b), lo_ = min(a, b); a = hi_; b = lo_; }
#define MMB(a_, b_, c_) __builtin_amdgcn_mfma_f32_16x16x32_bf16(__builtin_bit_cast(bf16x8, a_), __builtin_bit_cast(bf16x8, b_), c_, 0, 0, 0)
#define NTM 0
#define DPOL ""
#define DPOL2 ""
#define DPOL4 ""
constexpr int SPIN_CAP = 1 << 18;
constexpr uint32_t UMASK = 0x40074007u;
__device__ __forceinline__ uint32_t utag(uint32_t T, int j) {
  return ((T >> (3 * j)) & 0x00070007u) | (((j <= 2) ? (T << (2 - j)) : (T >> (j - 2))) & 0x40004000u);
}
__device__ __forceinline__ uint32_t max4(const u32x4& a) { return max(max(a[0], a[1]), max(a[2], a[3])); }
#ifndef ULONG
#define ULONG 8
#endif
__device__ __forceinline__ void until(uint32_t t) {
#pragma unroll 1
  for (int spin = 0; spin < 600; ++spin) {
    const int d = (int)(t - (uint32_t)__builtin_amdgcn_s_memrealtime());
    if (d <= 0) break;
    if (ULONG && d > 4 * ULONG) __builtin_amdgcn_s_sleep(ULONG); else __builtin_amdgcn_s_sleep(1);
  }
}
__device__ __forceinline__ uint32_t latest(uint32_t t) {
  const uint32_t t0 = (uint32_t)__builtin_amdgcn_readfirstlane(t) - 0x40000000u;
  return wave_maxu(t - t0) + t0;
}
__device__ __forceinline__ uint32_t gate(const char* ws, uint32_t po, uint32_t T) {
  u32x2 pv;
#pragma unroll 1
  for (int spin = 0; spin < SPIN_CAP; ++spin) {
    asm volatile("global_load_dwordx2 %0, %1, %2 sc1\n\ts_waitcnt vmcnt(0)" : "=&v"(pv) : "v"(po), "s"(ws) : "memory");
    if (!__any(pv[0] != T)) break;
    __builtin_amdgcn_s_sleep(2);
  }
  return pv[1];
}
__device__ __forceinline__ uint32_t gate2(const char* ws, uint32_t oa, uint32_t da, uint32_t ob, uint32_t T) {
  u32x2 pa, pb;
#pragma unroll 1
  for (int spin = 0; spin < SPIN_CAP; ++spin) {
    asm volatile("global_load_dwordx2 %0, %2, %4 sc1\n\tglobal_load_dwordx2 %1, %3, %4 sc1\n\ts_waitcnt vmcnt(0)" : "=&v"(pa), "=&v"(pb) : "v"(oa), "v"(ob), "s"(ws) : "memory");
    if (!__any(pa[0] != T || pb[0] != T)) break;
    __builtin_amdgcn_s_sleep(4);
  }
  pa[1] += da;
  return latest(((int)(pa[1] - pb[1]) > 0) ? pa[1] : pb[1]);
}
__device__ __forceinline__ uint32_t ahead(uint32_t c0, uint32_t t0, int dt) {
  const uint32_t c = (uint32_t)__builtin_amdgcn_s_memtime(), t = (uint32_t)__builtin_amdgcn_s_memrealtime();
  return t + (uint32_t)((float)((t - t0) * (uint32_t)(20 * dt)) * __builtin_amdgcn_rcpf((float)(c - c0)));
}
__device__ __forceinline__ void gsync(uint32_t a, uint32_t target) {
  uint32_t t, c, n = SPIN_CAP;
  uint64_t ex;
  asm volatile("s_waitcnt lgkmcnt(0)\n\ts_mov_b64 %3, exec\n\ts_mov_b64 exec, 1\n\tv_mov_b32 %0, 1\n\tds_add_u32 %4, %0\n\ts_mov_b64 exec, %3\n"
               "1:\n\ts_sleep 1\n\tds_read_b32 %0, %4\n\ts_sub_u32 %2, %2, 1\n\ts_cbranch_scc1 2f\n\ts_waitcnt lgkmcnt(0)\n\tv_readfirstlane_b32 %1, %0\n\t"
               "s_cmp_lt_u32 %1, %5\n\ts_cbranch_scc1 1b\n2:\n\ts_waitcnt lgkmcnt(0)"
               : "=&v"(t), "=&s"(c), "+s"(n), "=&s"(ex) : "v"(a), "n"(target) : "memory", "scc");
}
__device__ __forceinline__ void gsync0(uint32_t a, uint32_t target) {
  uint32_t t, c, n = SPIN_CAP;
  asm volatile("s_waitcnt lgkmcnt(0)\n\ts_mov_b64 exec, 1\n\tv_mov_b32 %0, 1\n\tds_add_u32 %3, %0\n\ts_mov_b64 exec, -1\n"
               "1:\n\tds_read_b32 %0, %3\n\ts_sub_u32 %2, %2, 1\n\ts_cbranch_scc1 2f\n\ts_waitcnt lgkmcnt(0)\n\tv_readfirstlane_b32 %1, %0\n\t"
               "s_cmp_lt_u32 %1, %4\n\ts_cbranch_scc1 1b\n2:\n\ts_waitcnt lgkmcnt(0)"
               : "=&v"(t), "=&s"(c), "+s"(n) : "v"(a), "n"(target) : "memory", "scc");
}
__device__ __forceinline__ void gwait(uint32_t a, uint32_t target) {
  uint32_t t, c, n = SPIN_CAP;
  asm volatile("1:\n\ts_sleep 2\n\tds_read_b32 %0, %3\n\ts_sub_u32 %2, %2, 1\n\ts_cbranch_scc1 2f\n\ts_waitcnt lgkmcnt(0)\n\tv_readfirstlane_b32 %1, %0\n\t"
               "s_cmp_lt_u32 %1, %4\n\ts_cbranch_scc1 1b\n2:\n\ts_waitcnt lgkmcnt(0)"
               : "=&v"(t), "=&s"(c), "+s"(n) : "v"(a), "n"(target) : "memory", "scc");
}
__device__ __forceinline__ void gwait0(uint32_t a, uint32_t target) {
  uint32_t t, c, n = SPIN_CAP;
  asm volatile("1:\n\tds_read_b32 %0, %3\n\ts_sub_u32 %2, %2, 1\n\ts_cbranch_scc1 2f\n\ts_waitcnt lgkmcnt(0)\n\tv_readfirstlane_b32 %1, %0\n\t"
               "s_cmp_lt_u32 %1, %4\n\ts_cbranch_scc1 1b\n2:\n\ts_waitcnt lgkmcnt(0)"
               : "=&v"(t), "=&s"(c), "+s"(n) : "v"(a), "n"(target) : "memory", "scc");
}
__device__ __forceinline__ void dma16(const void* base, uint32_t voff, uint32_t lds) {
  uint32_t keep;
  asm volatile("s_mov_b32 %0, m0\n\ts_mov_b32 m0, %3\n\ts_nop 0\n\tglobal_load_lds_dwordx4 %1, %2" DPOL "\n\ts_mov_b32 m0, %0" : "=&s"(keep) : "v"(voff), "s"(base), "s"(lds) : "memory");
}
__device__ __forceinline__ void dma16d(const void* base, uint32_t voff, uint32_t lds) {
  uint32_t keep;
  asm volatile("s_mov_b32 %0, m0\n\ts_mov_b32 m0, %3\n\ts_nop 0\n\tglobal_load_lds_dwordx4 %1, %2" DPOL2 "\n\ts_mov_b32 m0, %0" : "=&s"(keep) : "v"(voff), "s"(base), "s"(lds) : "memory");
}
__device__ __forceinline__ void nap(int n) {
  for (; n > 15; n -= 15) __builtin_amdgcn_s_sleep(15);
  switch (n) {
    case 1: __builtin_amdgcn_s_sleep(1); break; case 2: __builtin_amdgcn_s_sleep(2); break; case 3: __builtin_amdgcn_s_sleep(3); break;
    case 4: __builtin_amdgcn_s_sleep(4); break; case 5: __builtin_amdgcn_s_sleep(5); break; case 6: __builtin_amdgcn_s_sleep(6); break;
    case 7: __builtin_amdgcn_s_sleep(7); break; case 8: __builtin_amdgcn_s_sleep(8); break; case 9: __builtin_amdgcn_s_sleep(9); break;
    case 10: __builtin_amdgcn_s_sleep(10); break; case 11: __builtin_amdgcn_s_sleep(11); break; case 12: __builtin_amdgcn_s_sleep(12); break;
    case 13: __builtin_amdgcn_s_sleep(13); break; case 14: __builtin_amdgcn_s_sleep(14); break; case 15: __builtin_amdgcn_s_sleep(15); break;
    default: break;
  }
}
#define SLEEP(n) do { if ((n) > 0) __builtin_amdgcn_s_sleep(n); } while (0)
#define USLEEP 3
#define SSLEEP 11
#define PSLEEP 2
#define PPRIO 0
#define LFIRST 1
#define WD 40
#define OSLEEP 12
#define QPRIO 1
#define KPRIO 2
#define GSLEEP 0
#define NSLEEP 0
#define UD 40
#define XD 0
#define RSLEEP 0
#define UPRIO 2
#define VD2 372
#define WDEPTH 6
#define ESLEEP 0
#define SD 58
#define KD 182
#define OD 0
#define KBACK 6
#define DDEPTH 12
#define C3 1
#define XROT 2
#define WSOFF 0
#define KN 1
#define RPF 1
#define LSLEEP 1
#define TSTOP 78
#define HSTOP 110
#define VEARLY 0ull
#define FPR 3
#define SPR 3
#define DPRIO 0
#define QPRIO2 3
#define HW 2
#define RD 57
#define OPOS 1
#define OD2 6
#define NA 12
#define RD2 110
#define ORD1 ((unsigned __int128)0x321087654u << 36 | 0x321087654ull)
#define WDEPTH2 5
#define RQ NA
#define HREV 1
#define QPRIO0 3
#define VPS 1
#define RW 0
#define NPRIO 0
#define XSL 10
#define V2D 24
#define N0 8
#define LPOS 1
#define ALLP 1
#define OTP 1
#define STP 0
#define FD 204
#define VDB 365
#define BALL 1
#define FORCESLOW 0
#define FORCEFB 0
#define CONV_ASM \
  "s_cmp_eq_u32 s89, 1\ns_cbranch_scc1 1f\ns_cmp_gt_u32 s89, 1\ns_cbranch_scc1 9f\nds_read_b64 v[124:125], v3 offset:576\n" \
  "ds_read_b64 v[126:127], v3 offset:584\nds_read_b64 v[120:121], v3 offset:64\nds_read_b64 v[122:123], v3 offset:72\n" \
  "ds_read_b64 v[116:117], v3 offset:640\nds_read_b64 v[118:119], v3 offset:648\nds_read_b64 v[112:113], v3 offset:128\n" \
  "ds_read_b64 v[114:115], v3 offset:136\nds_read_b64 v[108:109], v3 offset:704\nds_read_b64 v[110:111], v3 offset:712\n" \
  "ds_read_b64 v[104:105], v3 offset:192\nds_read_b64 v[106:107], v3 offset:200\nds_read_b64 v[100:101], v3 offset:768\n" \
  "ds_read_b64 v[102:103], v3 offset:776\nds_read_b64 v[96:97], v3 offset:256\nds_read_b64 v[98:99], v3 offset:264\n" \
  "ds_read_b64 v[92:93], v3 offset:832\nds_read_b64 v[94:95], v3 offset:840\nds_read_b64 v[88:89], v3 offset:320\n" \
  "ds_read_b64 v[90:91], v3 offset:328\nds_read_b64 v[84:85], v3 offset:896\nds_read_b64 v[86:87], v3 offset:904\n" \
  "ds_read_b64 v[80:81], v3 offset:384\nds_read_b64 v[82:83], v3 offset:392\nds_read_b64 v[76:77], v3 offset:960\n" \
  "ds_read_b64 v[78:79], v3 offset:968\nds_read_b64 v[72:73], v3 offset:448\nds_read_b64 v[74:75], v3 offset:456\n" \
  "ds_read_b64 v[68:69], v3 offset:1024\nds_read_b64 v[70:71], v3 offset:1032\nds_read_b64 v[64:65], v3 offset:512\n" \
  "ds_read_b64 v[66:67], v3 offset:520\nds_read_b64 v[60:61], v3 offset:0\nds_read_b64 v[62:63], v3 offset:8\nds_read_b64 v[56:57], v3 offset:1088\n" \
  "ds_read_b64 v[58:59], v3 offset:1096\ns_waitcnt lgkmcnt(0)\ns_barrier\nds_read_b128 v[36:39], v2 offset:0\nds_read_b128 v[40:43], v2 offset:64\n" \
  "ds_read_b128 v[44:47], v2 offset:128\nds_read_b128 v[48:51], v2 offset:192\ns_waitcnt lgkmcnt(3)\n" \
  "v_mfma_f32_16x16x32_f16 v[12:15], v[124:127], v[36:39], 0\ns_setprio 1\nds_read_b128 v[52:55], v2 offset:256\n" \
  "v_mfma_f32_16x16x32_f16 v[16:19], v[120:123], v[36:39], 0\ns_waitcnt lgkmcnt(3)\nv_mfma_f32_16x16x32_f16 v[12:15], v[116:119], v[40:43], v[12:15]\n" \
  "ds_read_b128 v[36:39], v2 offset:320\nv_mfma_f32_16x16x32_f16 v[16:19], v[112:115], v[40:43], v[16:19]\ns_waitcnt lgkmcnt(3)\n" \
  "v_mfma_f32_16x16x32_f16 v[12:15], v[108:111], v[44:47], v[12:15]\nds_read_b128 v[40:43], v2 offset:384\n" \
  "v_mfma_f32_16x16x32_f16 v[16:19], v[104:107], v[44:47], v[16:19]\ns_waitcnt lgkmcnt(3)\n" \
  "v_mfma_f32_16x16x32_f16 v[12:15], v[100:103], v[48:51], v[12:15]\nds_read_b128 v[44:47], v2 offset:448\n" \
  "v_mfma_f32_16x16x32_f16 v[16:19], v[96:99], v[48:51], v[16:19]\ns_waitcnt lgkmcnt(3)\n" \
  "v_mfma_f32_16x16x32_f16 v[12:15], v[92:95], v[52:55], v[12:15]\nv_mfma_f32_16x16x32_f16 v[16:19], v[88:91], v[52:55], v[16:19]\n" \
  "s_waitcnt lgkmcnt(2)\nv_mfma_f32_16x16x32_f16 v[12:15], v[84:87], v[36:39], v[12:15]\nds_read_b128 v[48:51], v2 offset:512\n" \
  "v_mfma_f32_16x16x32_f16 v[16:19], v[80:83], v[36:39], v[16:19]\ns_waitcnt lgkmcnt(2)\n" \
  "v_mfma_f32_16x16x32_f16 v[12:15], v[76:79], v[40:43], v[12:15]\nds_read_b128 v[52:55], v2 offset:576\n" \
  "v_mfma_f32_16x16x32_f16 v[16:19], v[72:75], v[40:43], v[16:19]\ns_waitcnt lgkmcnt(2)\n" \
  "v_mfma_f32_16x16x32_f16 v[12:15], v[68:71], v[44:47], v[12:15]\nds_read_b128 v[36:39], v2 offset:640\n" \
  "v_mfma_f32_16x16x32_f16 v[16:19], v[64:67], v[44:47], v[16:19]\nv_mfma_f32_16x16x32_f16 v[20:23], v[60:63], v[44:47], 0\n" \
  "ds_read_b128 v[40:43], v2 offset:704\nv_mfma_f32_16x16x32_f16 v[28:31], v[56:59], v[44:47], 0\ns_waitcnt lgkmcnt(3)\n" \
  "v_mfma_f32_16x16x32_f16 v[16:19], v[124:127], v[48:51], v[16:19]\nds_read_b128 v[56:59], v2 offset:768\nv_cvt_pkrtz_f16_f32 v32, v12, v13\n" \
  "v_mfma_f32_16x16x32_f16 v[20:23], v[120:123], v[48:51], v[20:23]\nv_cvt_pkrtz_f16_f32 v33, v14, v15\ns_waitcnt lgkmcnt(3)\n" \
  "v_mfma_f32_16x16x32_f16 v[16:19], v[116:119], v[52:55], v[16:19]\nds_read_b128 v[44:47], v2 offset:832\n" \
  "v_pk_add_u16 v32, v32, 4 op_sel_hi:[1,0] clamp\nv_mfma_f32_16x16x32_f16 v[20:23], v[112:115], v[52:55], v[20:23]\n" \
  "v_pk_add_u16 v33, v33, 4 op_sel_hi:[1,0] clamp\ns_waitcnt lgkmcnt(3)\nv_mfma_f32_16x16x32_f16 v[16:19], v[108:111], v[36:39], v[16:19]\n" \
  "ds_read_b128 v[48:51], v2 offset:896\nds_write_b64 v4, v[32:33] offset:0\nv_mfma_f32_16x16x32_f16 v[20:23], v[104:107], v[36:39], v[20:23]\n" \
  "s_waitcnt lgkmcnt(3)\nv_mfma_f32_16x16x32_f16 v[16:19], v[100:103], v[40:43], v[16:19]\nds_read_b128 v[52:55], v2 offset:960\n" \
  "v_mfma_f32_16x16x32_f16 v[20:23], v[96:99], v[40:43], v[20:23]\nv_mfma_f32_16x16x32_f16 v[16:19], v[92:95], v[56:59], v[16:19]\n" \
  "v_mfma_f32_16x16x32_f16 v[20:23], v[88:91], v[56:59], v[20:23]\nds_read_b128 v[36:39], v2 offset:1024\ns_waitcnt lgkmcnt(3)\n" \
  "v_mfma_f32_16x16x32_f16 v[16:19], v[84:87], v[44:47], v[16:19]\nv_mfma_f32_16x16x32_f16 v[20:23], v[80:83], v[44:47], v[20:23]\n" \
  "ds_read_b128 v[40:43], v2 offset:1088\nds_read_b64 v[56:57], v6 offset:256\nds_read_b64 v[58:59], v6 offset:264\n" \
  "v_mfma_f32_16x16x32_f16 v[16:19], v[76:79], v[48:51], v[16:19]\nv_mfma_f32_16x16x32_f16 v[20:23], v[72:75], v[48:51], v[20:23]\n" \
  "ds_read_b128 v[44:47], v2 offset:1152\ns_waitcnt lgkmcnt(4)\nv_mfma_f32_16x16x32_f16 v[16:19], v[68:71], v[52:55], v[16:19]\n" \
  "v_mfma_f32_16x16x32_f16 v[20:23], v[64:67], v[52:55], v[20:23]\nds_read_b128 v[48:51], v2 offset:1216\n" \
  "v_mfma_f32_16x16x32_f16 v[24:27], v[60:63], v[52:55], 0\nv_mfma_f32_16x16x32_f16 v[20:23], v[124:127], v[36:39], v[20:23]\n" \
  "ds_read_b128 v[52:55], v2 offset:1280\nv_mfma_f32_16x16x32_f16 v[24:27], v[120:123], v[36:39], v[24:27]\ns_waitcnt lgkmcnt(5)\n" \
  "v_mfma_f32_16x16x32_f16 v[20:23], v[116:119], v[40:43], v[20:23]\nds_read_b128 v[36:39], v2 offset:1344\nv_cvt_pkrtz_f16_f32 v32, v16, v17\n" \
  "v_mfma_f32_16x16x32_f16 v[24:27], v[112:115], v[40:43], v[24:27]\nv_cvt_pkrtz_f16_f32 v33, v18, v19\ns_waitcnt lgkmcnt(3)\n" \
  "v_mfma_f32_16x16x32_f16 v[20:23], v[108:111], v[44:47], v[20:23]\nds_read_b128 v[40:43], v2 offset:1408\n" \
  "v_pk_add_u16 v32, v32, 4 op_sel_hi:[1,0] clamp\nds_read_b32 v35, v9 offset:32\nv_mfma_f32_16x16x32_f16 v[24:27], v[104:107], v[44:47], v[24:27]\n" \
  "v_pk_add_u16 v33, v33, 4 op_sel_hi:[1,0] clamp\ns_waitcnt lgkmcnt(3)\nv_mfma_f32_16x16x32_f16 v[20:23], v[100:103], v[48:51], v[20:23]\n" \
  "ds_read_b128 v[44:47], v2 offset:1472\nds_write_b64 v4, v[32:33] offset:1024\nv_mfma_f32_16x16x32_f16 v[24:27], v[96:99], v[48:51], v[24:27]\n" \
  "v_mfma_f32_16x16x32_f16 v[20:23], v[92:95], v[52:55], v[20:23]\nv_mfma_f32_16x16x32_f16 v[24:27], v[88:91], v[52:55], v[24:27]\n" \
  "ds_read_b128 v[48:51], v2 offset:1536\ns_waitcnt lgkmcnt(3)\nv_readfirstlane_b32 s85, v35\ns_cmp_lt_u32 s85, 4\ns_cbranch_scc1 21f\n22:\n" \
  "v_mfma_f32_16x16x32_f16 v[20:23], v[84:87], v[36:39], v[20:23]\nv_mfma_f32_16x16x32_f16 v[24:27], v[80:83], v[36:39], v[24:27]\n" \
  "ds_read_b128 v[52:55], v2 offset:1600\nv_mfma_f32_16x16x32_f16 v[20:23], v[76:79], v[40:43], v[20:23]\n" \
  "v_mfma_f32_16x16x32_f16 v[24:27], v[72:75], v[40:43], v[24:27]\nds_read_b128 v[36:39], v2 offset:1664\ns_waitcnt lgkmcnt(4)\n" \
  "v_mfma_f32_16x16x32_f16 v[20:23], v[68:71], v[44:47], v[20:23]\nv_mfma_f32_16x16x32_f16 v[24:27], v[64:67], v[44:47], v[24:27]\n" \
  "ds_read_b128 v[40:43], v2 offset:1728\nv_mfma_f32_16x16x32_f16 v[28:31], v[60:63], v[44:47], v[28:31]\nds_read_b64_tr_b16 v[60:61], v5 offset:0\n" \
  "ds_read_b64_tr_b16 v[62:63], v5 offset:32\ns_waitcnt lgkmcnt(4)\nv_mfma_f32_16x16x32_f16 v[24:27], v[124:127], v[48:51], v[24:27]\n" \
  "v_mfma_f32_16x16x32_f16 v[28:31], v[120:123], v[48:51], v[28:31]\nds_read_b64_tr_b16 v[44:45], v5 offset:256\n" \
  "ds_read_b64_tr_b16 v[46:47], v5 offset:288\nv_mfma_f32_16x16x32_f16 v[24:27], v[116:119], v[52:55], v[24:27]\n" \
  "ds_read_b64 v[120:121], v6 offset:320\nds_read_b64 v[122:123], v6 offset:328\nv_mfma_f32_16x16x32_f16 v[28:31], v[112:115], v[52:55], v[28:31]\n" \
  "ds_read_b128 v[48:51], v2 offset:1792\ns_waitcnt lgkmcnt(7)\nv_mfma_f32_16x16x32_f16 v[24:27], v[108:111], v[36:39], v[24:27]\n" \
  "ds_read_b128 v[112:115], v2 offset:1856\nv_mfma_f32_16x16x32_f16 v[28:31], v[104:107], v[36:39], v[28:31]\nds_read_b64 v[52:53], v6 offset:128\n" \
  "ds_read_b64 v[54:55], v6 offset:136\nv_mfma_f32_16x16x32_f16 v[24:27], v[100:103], v[40:43], v[24:27]\nds_read_b128 v[104:107], v2 offset:1920\n" \
  "v_mfma_f32_16x16x32_f16 v[28:31], v[96:99], v[40:43], v[28:31]\nds_read_b64 v[36:37], v6 offset:192\nds_read_b64 v[38:39], v6 offset:200\n" \
  "s_waitcnt lgkmcnt(9)\nv_mfma_f32_16x16x32_f16 v[20:23], v[60:63], v[56:59], v[20:23]\nds_read_b128 v[96:99], v2 offset:1984\ns_waitcnt lgkmcnt(7)\n" \
  "v_mfma_f32_16x16x32_f16 v[20:23], v[44:47], v[120:123], v[20:23]\nv_mfma_f32_16x16x32_f16 v[24:27], v[92:95], v[48:51], v[24:27]\n" \
  "ds_read_b128 v[40:43], v2 offset:2048\nv_mfma_f32_16x16x32_f16 v[28:31], v[88:91], v[48:51], v[28:31]\nds_read_b64_tr_b16 v[88:89], v5 offset:512\n" \
  "ds_read_b64_tr_b16 v[90:91], v5 offset:544\ns_waitcnt lgkmcnt(6)\nv_mfma_f32_16x16x32_f16 v[24:27], v[84:87], v[112:115], v[24:27]\n" \
  "v_mfma_f32_16x16x32_f16 v[28:31], v[80:83], v[112:115], v[28:31]\nv_cvt_pkrtz_f16_f32 v32, v20, v21\nv_cvt_pkrtz_f16_f32 v33, v22, v23\n" \
  "v_mfma_f32_16x16x32_f16 v[24:27], v[76:79], v[104:107], v[24:27]\nds_read_b64_tr_b16 v[48:49], v5 offset:768\n" \
  "ds_read_b64_tr_b16 v[50:51], v5 offset:800\nv_mfma_f32_16x16x32_f16 v[28:31], v[72:75], v[104:107], v[28:31]\n" \
  "ds_read_b128 v[80:83], v2 offset:2112\ns_waitcnt lgkmcnt(5)\nv_mfma_f32_16x16x32_f16 v[24:27], v[68:71], v[96:99], v[24:27]\n" \
  "ds_read_b128 v[112:115], v2 offset:2176\nv_pk_add_u16 v32, v32, 4 op_sel_hi:[1,0] clamp\n" \
  "v_mfma_f32_16x16x32_f16 v[28:31], v[64:67], v[96:99], v[28:31]\nds_read_b128 v[72:75], v2 offset:2240\n" \
  "v_pk_add_u16 v33, v33, 4 op_sel_hi:[1,0] clamp\nv_mfma_f32_16x16x32_f16 v[28:31], v[124:127], v[40:43], v[28:31]\n" \
  "ds_read_b128 v[104:107], v2 offset:2304\nds_write_b64 v4, v[32:33] offset:2048\nv_mfma_f32_16x16x32_f16 v[24:27], v[60:63], v[52:55], v[24:27]\n" \
  "ds_read_b128 v[64:67], v2 offset:2368\nds_read_b64 v[96:97], v6 offset:0\nds_read_b64 v[98:99], v6 offset:8\n" \
  "v_mfma_f32_16x16x32_f16 v[24:27], v[44:47], v[36:39], v[24:27]\nds_read_b128 v[124:127], v2 offset:2432\ns_waitcnt lgkmcnt(9)\n" \
  "v_mfma_f32_16x16x32_f16 v[24:27], v[88:91], v[56:59], v[24:27]\nds_read_b128 v[40:43], v2 offset:2496\n" \
  "v_mfma_f32_16x16x32_f16 v[24:27], v[48:51], v[120:123], v[24:27]\ns_waitcnt lgkmcnt(8)\n" \
  "v_mfma_f32_16x16x32_f16 v[28:31], v[116:119], v[80:83], v[28:31]\nds_read_b64 v[116:117], v6 offset:64\nds_read_b64 v[118:119], v6 offset:72\n" \
  "v_mfma_f32_16x16x32_f16 v[28:31], v[108:111], v[112:115], v[28:31]\nds_read_b64_tr_b16 v[80:81], v5 offset:1024\n" \
  "ds_read_b64_tr_b16 v[82:83], v5 offset:1056\ns_waitcnt lgkmcnt(10)\nv_mfma_f32_16x16x32_f16 v[28:31], v[100:103], v[72:75], v[28:31]\n" \
  "v_cvt_pkrtz_f16_f32 v32, v24, v25\nv_cvt_pkrtz_f16_f32 v33, v26, v27\nv_mfma_f32_16x16x32_f16 v[28:31], v[92:95], v[104:107], v[28:31]\n" \
  "v_pk_add_u16 v32, v32, 4 op_sel_hi:[1,0] clamp\ns_waitcnt lgkmcnt(5)\nv_mfma_f32_16x16x32_f16 v[28:31], v[84:87], v[64:67], v[28:31]\n" \
  "ds_read_b64_tr_b16 v[108:109], v5 offset:1280\nds_read_b64_tr_b16 v[110:111], v5 offset:1312\n" \
  "v_mfma_f32_16x16x32_f16 v[28:31], v[76:79], v[124:127], v[28:31]\nds_read_b64_tr_b16 v[112:113], v7 offset:0\n" \
  "ds_read_b64_tr_b16 v[114:115], v7 offset:64\ns_waitcnt lgkmcnt(6)\nv_mfma_f32_16x16x32_f16 v[28:31], v[68:71], v[40:43], v[28:31]\n" \
  "ds_read2_b32 v[100:101], v8 offset0:48 offset1:49\nds_read2_b32 v[102:103], v8 offset0:50 offset1:51\n" \
  "v_mfma_f32_16x16x32_f16 v[28:31], v[60:63], v[96:99], v[28:31]\nv_pk_add_u16 v33, v33, 4 op_sel_hi:[1,0] clamp\n" \
  "ds_write_b64 v4, v[32:33] offset:3072\nv_mfma_f32_16x16x32_f16 v[28:31], v[44:47], v[116:119], v[28:31]\n" \
  "v_mfma_f32_16x16x32_f16 v[28:31], v[88:91], v[52:55], v[28:31]\nv_mfma_f32_16x16x32_f16 v[28:31], v[48:51], v[36:39], v[28:31]\n" \
  "s_waitcnt lgkmcnt(5)\nv_mfma_f32_16x16x32_f16 v[28:31], v[80:83], v[56:59], v[28:31]\n" \
  "v_mfma_f32_16x16x32_f16 v[28:31], v[108:111], v[120:123], v[28:31]\ns_waitcnt lgkmcnt(1)\n" \
  "v_mfma_f32_16x16x32_f16 v[28:31], v[112:115], v[100:103], v[28:31]\ns_nop 7\nv_cvt_pkrtz_f16_f32 v32, v28, v29\nv_cvt_pkrtz_f16_f32 v33, v30, v31\n" \
  "v_pk_add_u16 v32, v32, 4 op_sel_hi:[1,0] clamp\nv_pk_add_u16 v33, v33, 4 op_sel_hi:[1,0] clamp\nds_write_b64 v4, v[32:33] offset:4096\n" \
  "s_setprio 0\ns_setpc_b64 s[90:91]\n21:\ns_movk_i32 s84, 0x4000\n23:\nds_read_b32 v35, v9 offset:32\ns_sub_u32 s84, s84, 1\ns_cbranch_scc1 22b\n" \
  "s_waitcnt lgkmcnt(0)\nv_readfirstlane_b32 s85, v35\ns_cmp_lt_u32 s85, 4\ns_cbranch_scc1 23b\ns_branch 22b\n1:\n" \
  "ds_read_b64 v[124:125], v3 offset:1088\nds_read_b64 v[126:127], v3 offset:1096\nds_read_b64 v[120:121], v3 offset:0\n" \
  "ds_read_b64 v[122:123], v3 offset:8\nds_read_b64 v[116:117], v3 offset:64\nds_read_b64 v[118:119], v3 offset:72\n" \
  "ds_read_b64 v[112:113], v3 offset:128\nds_read_b64 v[114:115], v3 offset:136\nds_read_b64 v[108:109], v3 offset:192\n" \
  "ds_read_b64 v[110:111], v3 offset:200\nds_read_b64 v[104:105], v3 offset:256\nds_read_b64 v[106:107], v3 offset:264\n" \
  "ds_read_b64 v[100:101], v3 offset:320\nds_read_b64 v[102:103], v3 offset:328\nds_read_b64 v[96:97], v3 offset:384\n" \
  "ds_read_b64 v[98:99], v3 offset:392\nds_read_b64 v[92:93], v3 offset:448\nds_read_b64 v[94:95], v3 offset:456\n" \
  "ds_read_b64 v[88:89], v3 offset:512\nds_read_b64 v[90:91], v3 offset:520\nds_read_b64 v[84:85], v3 offset:576\n" \
  "ds_read_b64 v[86:87], v3 offset:584\nds_read_b64 v[80:81], v3 offset:640\nds_read_b64 v[82:83], v3 offset:648\n" \
  "ds_read_b64 v[76:77], v3 offset:704\nds_read_b64 v[78:79], v3 offset:712\nds_read_b64 v[72:73], v3 offset:768\n" \
  "ds_read_b64 v[74:75], v3 offset:776\nds_read_b64 v[68:69], v3 offset:832\nds_read_b64 v[70:71], v3 offset:840\n" \
  "ds_read_b64 v[64:65], v3 offset:896\nds_read_b64 v[66:67], v3 offset:904\nds_read_b64 v[60:61], v3 offset:960\n" \
  "ds_read_b64 v[62:63], v3 offset:968\nds_read_b64 v[56:57], v3 offset:1024\nds_read_b64 v[58:59], v3 offset:1032\n" \
  "ds_read_b64 v[52:53], v6 offset:0\nds_read_b64 v[54:55], v6 offset:8\nds_read_b64 v[48:49], v6 offset:64\nds_read_b64 v[50:51], v6 offset:72\n" \
  "s_waitcnt lgkmcnt(0)\ns_barrier\nds_read_b128 v[28:31], v2 offset:960\nds_read_b128 v[32:35], v2 offset:1472\n" \
  "ds_read_b128 v[36:39], v2 offset:1984\nds_read_b128 v[40:43], v2 offset:2048\nds_read_b128 v[44:47], v2 offset:2112\ns_waitcnt lgkmcnt(3)\n" \
  "v_mfma_f32_16x16x32_f16 v[12:15], v[124:127], v[28:31], 0\ns_setprio 1\nds_read_b128 v[28:31], v2 offset:2176\n" \
  "v_mfma_f32_16x16x32_f16 v[16:19], v[124:127], v[32:35], 0\nds_read_b128 v[32:35], v2 offset:2240\ns_waitcnt lgkmcnt(3)\n" \
  "v_mfma_f32_16x16x32_f16 v[12:15], v[120:123], v[36:39], v[12:15]\nv_mfma_f32_16x16x32_f16 v[20:23], v[124:127], v[36:39], 0\n" \
  "ds_read_b128 v[124:127], v2 offset:2304\nds_read_b128 v[36:39], v2 offset:2368\nv_mfma_f32_16x16x32_f16 v[12:15], v[116:119], v[40:43], v[12:15]\n" \
  "ds_read_b128 v[40:43], v2 offset:2432\ns_waitcnt lgkmcnt(4)\nv_mfma_f32_16x16x32_f16 v[12:15], v[112:115], v[44:47], v[12:15]\n" \
  "ds_read_b128 v[44:47], v2 offset:2496\nv_mfma_f32_16x16x32_f16 v[12:15], v[108:111], v[28:31], v[12:15]\nds_read_b128 v[28:31], v2 offset:2560\n" \
  "s_waitcnt lgkmcnt(4)\nv_mfma_f32_16x16x32_f16 v[12:15], v[104:107], v[32:35], v[12:15]\nds_read_b128 v[32:35], v2 offset:2624\n" \
  "v_mfma_f32_16x16x32_f16 v[12:15], v[100:103], v[124:127], v[12:15]\ns_waitcnt lgkmcnt(3)\n" \
  "v_mfma_f32_16x16x32_f16 v[12:15], v[96:99], v[36:39], v[12:15]\nds_read_b128 v[124:127], v2 offset:2688\n" \
  "v_mfma_f32_16x16x32_f16 v[12:15], v[92:95], v[40:43], v[12:15]\ns_waitcnt lgkmcnt(3)\n" \
  "v_mfma_f32_16x16x32_f16 v[12:15], v[88:91], v[44:47], v[12:15]\nds_read_b128 v[36:39], v2 offset:2752\n" \
  "v_mfma_f32_16x16x32_f16 v[16:19], v[120:123], v[44:47], v[16:19]\ns_waitcnt lgkmcnt(3)\n" \
  "v_mfma_f32_16x16x32_f16 v[12:15], v[84:87], v[28:31], v[12:15]\nds_read_b128 v[40:43], v2 offset:2816\n" \
  "v_mfma_f32_16x16x32_f16 v[16:19], v[116:119], v[28:31], v[16:19]\ns_waitcnt lgkmcnt(3)\n" \
  "v_mfma_f32_16x16x32_f16 v[12:15], v[80:83], v[32:35], v[12:15]\nds_read_b128 v[44:47], v2 offset:2880\n" \
  "v_mfma_f32_16x16x32_f16 v[16:19], v[112:115], v[32:35], v[16:19]\ns_waitcnt lgkmcnt(3)\n" \
  "v_mfma_f32_16x16x32_f16 v[12:15], v[76:79], v[124:127], v[12:15]\nds_read_b128 v[28:31], v2 offset:2944\n" \
  "v_mfma_f32_16x16x32_f16 v[16:19], v[108:111], v[124:127], v[16:19]\ns_waitcnt lgkmcnt(3)\n" \
  "v_mfma_f32_16x16x32_f16 v[12:15], v[72:75], v[36:39], v[12:15]\nds_read_b128 v[32:35], v2 offset:3008\n" \
  "v_mfma_f32_16x16x32_f16 v[16:19], v[104:107], v[36:39], v[16:19]\ns_waitcnt lgkmcnt(3)\n" \
  "v_mfma_f32_16x16x32_f16 v[12:15], v[68:71], v[40:43], v[12:15]\nv_mfma_f32_16x16x32_f16 v[16:19], v[100:103], v[40:43], v[16:19]\n" \
  "ds_read_b128 v[124:127], v2 offset:3072\ns_waitcnt lgkmcnt(3)\nv_mfma_f32_16x16x32_f16 v[12:15], v[64:67], v[44:47], v[12:15]\n" \
  "v_mfma_f32_16x16x32_f16 v[16:19], v[96:99], v[44:47], v[16:19]\nds_read_b128 v[36:39], v2 offset:3136\ns_waitcnt lgkmcnt(3)\n" \
  "v_mfma_f32_16x16x32_f16 v[12:15], v[60:63], v[28:31], v[12:15]\nds_read_b64 v[40:41], v6 offset:128\nds_read_b64 v[42:43], v6 offset:136\n" \
  "v_mfma_f32_16x16x32_f16 v[16:19], v[92:95], v[28:31], v[16:19]\nds_read_b128 v[44:47], v2 offset:3200\ns_waitcnt lgkmcnt(4)\n" \
  "v_mfma_f32_16x16x32_f16 v[12:15], v[56:59], v[32:35], v[12:15]\nv_mfma_f32_16x16x32_f16 v[16:19], v[88:91], v[32:35], v[16:19]\n" \
  "ds_read_b128 v[28:31], v2 offset:3264\nv_mfma_f32_16x16x32_f16 v[20:23], v[120:123], v[32:35], v[20:23]\nds_read_b64 v[120:121], v6 offset:192\n" \
  "ds_read_b64 v[122:123], v6 offset:200\nv_mfma_f32_16x16x32_f16 v[16:19], v[84:87], v[124:127], v[16:19]\nds_read_b128 v[32:35], v2 offset:3328\n" \
  "v_mfma_f32_16x16x32_f16 v[20:23], v[116:119], v[124:127], v[20:23]\nds_read_b64 v[116:117], v6 offset:256\nds_read_b64 v[118:119], v6 offset:264\n" \
  "s_waitcnt lgkmcnt(6)\nv_mfma_f32_16x16x32_f16 v[16:19], v[80:83], v[36:39], v[16:19]\nds_read_b128 v[124:127], v2 offset:3392\n" \
  "v_mfma_f32_16x16x32_f16 v[20:23], v[112:115], v[36:39], v[20:23]\nds_read_b64 v[112:113], v6 offset:320\nds_read_b64 v[114:115], v6 offset:328\n" \
  "v_mfma_f32_16x16x32_f16 v[16:19], v[76:79], v[44:47], v[16:19]\nds_read_b128 v[36:39], v2 offset:3456\n" \
  "v_mfma_f32_16x16x32_f16 v[20:23], v[108:111], v[44:47], v[20:23]\ns_waitcnt lgkmcnt(6)\n" \
  "v_mfma_f32_16x16x32_f16 v[16:19], v[72:75], v[28:31], v[16:19]\nds_read_b128 v[108:111], v2 offset:3520\n" \
  "v_mfma_f32_16x16x32_f16 v[20:23], v[104:107], v[28:31], v[20:23]\nv_mfma_f32_16x16x32_f16 v[16:19], v[68:71], v[32:35], v[16:19]\n" \
  "ds_read_b128 v[44:47], v2 offset:3584\nv_mfma_f32_16x16x32_f16 v[20:23], v[100:103], v[32:35], v[20:23]\nds_read_b128 v[104:107], v2 offset:3648\n" \
  "ds_read_b32 v27, v9 offset:32\ns_waitcnt lgkmcnt(4)\nv_mfma_f32_16x16x32_f16 v[16:19], v[64:67], v[124:127], v[16:19]\n" \
  "ds_read_b128 v[28:31], v2 offset:3712\nv_mfma_f32_16x16x32_f16 v[20:23], v[96:99], v[124:127], v[20:23]\nds_read_b128 v[100:103], v2 offset:3776\n" \
  "ds_read2_b32 v[32:33], v8 offset0:32 offset1:33\nds_read2_b32 v[34:35], v8 offset0:34 offset1:35\n" \
  "v_mfma_f32_16x16x32_f16 v[16:19], v[60:63], v[36:39], v[16:19]\nds_read_b128 v[96:99], v2 offset:3840\n" \
  "v_mfma_f32_16x16x32_f16 v[20:23], v[92:95], v[36:39], v[20:23]\nds_read_b128 v[124:127], v2 offset:3904\ns_waitcnt lgkmcnt(8)\n" \
  "v_mfma_f32_16x16x32_f16 v[16:19], v[56:59], v[108:111], v[16:19]\nds_read_b128 v[92:95], v2 offset:3968\ns_waitcnt lgkmcnt(7)\n" \
  "v_readfirstlane_b32 s85, v27\ns_cmp_lt_u32 s85, 4\ns_cbranch_scc1 31f\n32:\nv_mfma_f32_16x16x32_f16 v[20:23], v[88:91], v[108:111], v[20:23]\n" \
  "ds_read_b128 v[36:39], v2 offset:4032\nds_read_b64_tr_b16 v[88:89], v5 offset:512\nds_read_b64_tr_b16 v[90:91], v5 offset:544\n" \
  "v_mfma_f32_16x16x32_f16 v[20:23], v[84:87], v[44:47], v[20:23]\nds_read_b64_tr_b16 v[108:109], v5 offset:768\n" \
  "ds_read_b64_tr_b16 v[110:111], v5 offset:800\nv_mfma_f32_16x16x32_f16 v[20:23], v[80:83], v[104:107], v[20:23]\n" \
  "ds_read_b64_tr_b16 v[84:85], v5 offset:1024\nds_read_b64_tr_b16 v[86:87], v5 offset:1056\ns_waitcnt lgkmcnt(12)\n" \
  "v_mfma_f32_16x16x32_f16 v[20:23], v[76:79], v[28:31], v[20:23]\nds_read_b64_tr_b16 v[44:45], v5 offset:1280\n" \
  "ds_read_b64_tr_b16 v[46:47], v5 offset:1312\nv_mfma_f32_16x16x32_f16 v[20:23], v[72:75], v[100:103], v[20:23]\ns_waitcnt lgkmcnt(10)\n" \
  "v_mfma_f32_16x16x32_f16 v[20:23], v[68:71], v[96:99], v[20:23]\nds_read_b64_tr_b16 v[80:81], v5 offset:1536\n" \
  "ds_read_b64_tr_b16 v[82:83], v5 offset:1568\nv_mfma_f32_16x16x32_f16 v[20:23], v[64:67], v[124:127], v[20:23]\n" \
  "ds_read_b64_tr_b16 v[104:105], v5 offset:1792\nds_read_b64_tr_b16 v[106:107], v5 offset:1824\ns_waitcnt lgkmcnt(12)\n" \
  "v_mfma_f32_16x16x32_f16 v[20:23], v[60:63], v[92:95], v[20:23]\nds_read_b64_tr_b16 v[76:77], v5 offset:2048\n" \
  "ds_read_b64_tr_b16 v[78:79], v5 offset:2080\nv_mfma_f32_16x16x32_f16 v[20:23], v[56:59], v[36:39], v[20:23]\ns_waitcnt lgkmcnt(10)\n" \
  "v_mfma_f32_16x16x32_f16 v[12:15], v[88:91], v[52:55], v[12:15]\nds_read_b64_tr_b16 v[28:29], v5 offset:2304\n" \
  "ds_read_b64_tr_b16 v[30:31], v5 offset:2336\nv_mfma_f32_16x16x32_f16 v[12:15], v[108:111], v[48:51], v[12:15]\n" \
  "ds_read_b64_tr_b16 v[72:73], v5 offset:2560\nds_read_b64_tr_b16 v[74:75], v5 offset:2592\ns_waitcnt lgkmcnt(10)\n" \
  "v_mfma_f32_16x16x32_f16 v[12:15], v[84:87], v[40:43], v[12:15]\nds_read_b64_tr_b16 v[100:101], v5 offset:2816\n" \
  "ds_read_b64_tr_b16 v[102:103], v5 offset:2848\nv_mfma_f32_16x16x32_f16 v[16:19], v[84:87], v[52:55], v[16:19]\n" \
  "ds_read_b64_tr_b16 v[68:69], v7 offset:0\nds_read_b64_tr_b16 v[70:71], v7 offset:64\n" \
  "v_mfma_f32_16x16x32_f16 v[12:15], v[44:47], v[120:123], v[12:15]\nv_mfma_f32_16x16x32_f16 v[16:19], v[44:47], v[48:51], v[16:19]\n" \
  "s_waitcnt lgkmcnt(10)\nv_mfma_f32_16x16x32_f16 v[12:15], v[80:83], v[116:119], v[12:15]\nds_read2_b32 v[96:97], v8 offset0:16 offset1:17\n" \
  "ds_read2_b32 v[98:99], v8 offset0:18 offset1:19\nv_mfma_f32_16x16x32_f16 v[16:19], v[80:83], v[40:43], v[16:19]\n" \
  "ds_read2_b32 v[64:65], v8 offset0:0 offset1:1\nds_read2_b32 v[66:67], v8 offset0:2 offset1:3\n" \
  "v_mfma_f32_16x16x32_f16 v[20:23], v[80:83], v[52:55], v[20:23]\nv_mfma_f32_16x16x32_f16 v[12:15], v[104:107], v[112:115], v[12:15]\n" \
  "v_mfma_f32_16x16x32_f16 v[16:19], v[104:107], v[120:123], v[16:19]\nv_mfma_f32_16x16x32_f16 v[20:23], v[104:107], v[48:51], v[20:23]\n" \
  "s_waitcnt lgkmcnt(10)\nv_mfma_f32_16x16x32_f16 v[16:19], v[76:79], v[116:119], v[16:19]\nds_read_b64_tr_b16 v[124:125], v7 offset:512\n" \
  "ds_read_b64_tr_b16 v[126:127], v7 offset:576\nv_mfma_f32_16x16x32_f16 v[20:23], v[76:79], v[40:43], v[20:23]\n" \
  "ds_read2_b32 v[60:61], v8 offset0:48 offset1:49\nds_read2_b32 v[62:63], v8 offset0:50 offset1:51\n" \
  "v_mfma_f32_16x16x32_f16 v[16:19], v[28:31], v[112:115], v[16:19]\nv_mfma_f32_16x16x32_f16 v[20:23], v[28:31], v[120:123], v[20:23]\n" \
  "s_waitcnt lgkmcnt(10)\nv_mfma_f32_16x16x32_f16 v[20:23], v[72:75], v[116:119], v[20:23]\nds_read_b64_tr_b16 v[92:93], v7 offset:1024\n" \
  "ds_read_b64_tr_b16 v[94:95], v7 offset:1088\nv_mfma_f32_16x16x32_f16 v[20:23], v[100:103], v[112:115], v[20:23]\n" \
  "ds_read_b64_tr_b16 v[56:57], v7 offset:1536\nds_read_b64_tr_b16 v[58:59], v7 offset:1600\ns_waitcnt lgkmcnt(10)\n" \
  "v_mfma_f32_16x16x32_f16 v[12:15], v[68:71], v[32:35], v[12:15]\nv_mfma_f32_16x16x32_f16 v[16:19], v[68:71], v[96:99], v[16:19]\n" \
  "s_waitcnt lgkmcnt(6)\nv_mfma_f32_16x16x32_f16 v[20:23], v[68:71], v[64:67], v[20:23]\ns_waitcnt lgkmcnt(2)\n" \
  "v_mfma_f32_16x16x32_f16 v[12:15], v[124:127], v[60:63], v[12:15]\nv_mfma_f32_16x16x32_f16 v[16:19], v[124:127], v[32:35], v[16:19]\n" \
  "v_mfma_f32_16x16x32_f16 v[20:23], v[124:127], v[96:99], v[20:23]\nv_mfma_f32_16x16x32_f16 v[16:19], v[92:95], v[60:63], v[16:19]\n" \
  "v_mfma_f32_16x16x32_f16 v[20:23], v[92:95], v[32:35], v[20:23]\ns_waitcnt lgkmcnt(0)\n" \
  "v_mfma_f32_16x16x32_f16 v[20:23], v[56:59], v[60:63], v[20:23]\ns_nop 1\nv_cvt_pkrtz_f16_f32 v24, v12, v13\nv_cvt_pkrtz_f16_f32 v25, v14, v15\n" \
  "v_pk_add_u16 v24, v24, 4 op_sel_hi:[1,0] clamp\nv_pk_add_u16 v25, v25, 4 op_sel_hi:[1,0] clamp\nds_write_b64 v4, v[24:25] offset:5120\n" \
  "v_cvt_pkrtz_f16_f32 v24, v16, v17\nv_cvt_pkrtz_f16_f32 v25, v18, v19\nv_pk_add_u16 v24, v24, 4 op_sel_hi:[1,0] clamp\n" \
  "v_pk_add_u16 v25, v25, 4 op_sel_hi:[1,0] clamp\nds_write_b64 v4, v[24:25] offset:6144\nv_cvt_pkrtz_f16_f32 v24, v20, v21\n" \
  "v_cvt_pkrtz_f16_f32 v25, v22, v23\nv_pk_add_u16 v24, v24, 4 op_sel_hi:[1,0] clamp\nv_pk_add_u16 v25, v25, 4 op_sel_hi:[1,0] clamp\n" \
  "ds_write_b64 v4, v[24:25] offset:7168\ns_setprio 0\ns_setpc_b64 s[90:91]\n31:\ns_movk_i32 s84, 0x4000\n33:\nds_read_b32 v27, v9 offset:32\n" \
  "s_sub_u32 s84, s84, 1\ns_cbranch_scc1 32b\ns_waitcnt lgkmcnt(0)\nv_readfirstlane_b32 s85, v27\ns_cmp_lt_u32 s85, 4\ns_cbranch_scc1 33b\n" \
  "s_branch 32b\n9:\ns_cmp_eq_u32 s89, 3\ns_cbranch_scc1 3f\ns_cmp_eq_u32 s89, 4\ns_cbranch_scc1 4f\ns_cmp_eq_u32 s89, 5\ns_cbranch_scc1 5f\n" \
  "ds_read_b64 v[124:125], v3 offset:576\nds_read_b64 v[126:127], v3 offset:584\nds_read_b64 v[120:121], v3 offset:64\n" \
  "ds_read_b64 v[122:123], v3 offset:72\nds_read_b64 v[116:117], v3 offset:640\nds_read_b64 v[118:119], v3 offset:648\n" \
  "ds_read_b64 v[112:113], v3 offset:128\nds_read_b64 v[114:115], v3 offset:136\nds_read_b64 v[108:109], v3 offset:704\n" \
  "ds_read_b64 v[110:111], v3 offset:712\nds_read_b64 v[104:105], v3 offset:192\nds_read_b64 v[106:107], v3 offset:200\n" \
  "ds_read_b64 v[100:101], v3 offset:768\nds_read_b64 v[102:103], v3 offset:776\nds_read_b64 v[96:97], v3 offset:256\n" \
  "ds_read_b64 v[98:99], v3 offset:264\nds_read_b64 v[92:93], v3 offset:832\nds_read_b64 v[94:95], v3 offset:840\n" \
  "ds_read_b64 v[88:89], v3 offset:320\nds_read_b64 v[90:91], v3 offset:328\nds_read_b64 v[84:85], v3 offset:896\n" \
  "ds_read_b64 v[86:87], v3 offset:904\nds_read_b64 v[80:81], v3 offset:384\nds_read_b64 v[82:83], v3 offset:392\n" \
  "ds_read_b64 v[76:77], v3 offset:960\nds_read_b64 v[78:79], v3 offset:968\nds_read_b64 v[72:73], v3 offset:448\n" \
  "ds_read_b64 v[74:75], v3 offset:456\nds_read_b64 v[68:69], v3 offset:1024\nds_read_b64 v[70:71], v3 offset:1032\n" \
  "ds_read_b64 v[64:65], v3 offset:512\nds_read_b64 v[66:67], v3 offset:520\ns_waitcnt lgkmcnt(0)\ns_barrier\nds_read_b128 v[24:27], v2 offset:0\n" \
  "ds_read_b128 v[28:31], v2 offset:64\nds_read_b128 v[32:35], v2 offset:128\nds_read_b128 v[36:39], v2 offset:192\ns_waitcnt lgkmcnt(3)\n" \
  "v_mfma_f32_16x16x32_f16 v[12:15], v[124:127], v[24:27], 0\ns_setprio 1\nds_read_b128 v[40:43], v2 offset:256\n" \
  "v_mfma_f32_16x16x32_f16 v[16:19], v[120:123], v[24:27], 0\ns_waitcnt lgkmcnt(3)\nv_mfma_f32_16x16x32_f16 v[12:15], v[116:119], v[28:31], v[12:15]\n" \
  "ds_read_b128 v[44:47], v2 offset:320\nv_mfma_f32_16x16x32_f16 v[16:19], v[112:115], v[28:31], v[16:19]\ns_waitcnt lgkmcnt(3)\n" \
  "v_mfma_f32_16x16x32_f16 v[12:15], v[108:111], v[32:35], v[12:15]\nds_read_b128 v[48:51], v2 offset:384\n" \
  "v_mfma_f32_16x16x32_f16 v[16:19], v[104:107], v[32:35], v[16:19]\ns_waitcnt lgkmcnt(3)\n" \
  "v_mfma_f32_16x16x32_f16 v[12:15], v[100:103], v[36:39], v[12:15]\nds_read_b128 v[52:55], v2 offset:448\n" \
  "v_mfma_f32_16x16x32_f16 v[16:19], v[96:99], v[36:39], v[16:19]\ns_waitcnt lgkmcnt(3)\n" \
  "v_mfma_f32_16x16x32_f16 v[12:15], v[92:95], v[40:43], v[12:15]\nds_read_b128 v[56:59], v2 offset:512\n" \
  "v_mfma_f32_16x16x32_f16 v[16:19], v[88:91], v[40:43], v[16:19]\nds_read_b128 v[60:63], v2 offset:576\ns_waitcnt lgkmcnt(3)\n" \
  "v_mfma_f32_16x16x32_f16 v[12:15], v[84:87], v[44:47], v[12:15]\nds_read_b128 v[120:123], v2 offset:640\n" \
  "v_mfma_f32_16x16x32_f16 v[16:19], v[80:83], v[44:47], v[16:19]\nds_read_b128 v[24:27], v2 offset:704\n" \
  "v_mfma_f32_16x16x32_f16 v[12:15], v[76:79], v[48:51], v[12:15]\nds_read_b128 v[112:115], v2 offset:768\n" \
  "v_mfma_f32_16x16x32_f16 v[16:19], v[72:75], v[48:51], v[16:19]\nds_read_b128 v[28:31], v2 offset:832\ns_waitcnt lgkmcnt(5)\n" \
  "v_mfma_f32_16x16x32_f16 v[12:15], v[68:71], v[52:55], v[12:15]\nds_read_b128 v[104:107], v2 offset:896\n" \
  "v_mfma_f32_16x16x32_f16 v[16:19], v[64:67], v[52:55], v[16:19]\nds_read_b128 v[32:35], v2 offset:960\n" \
  "v_mfma_f32_16x16x32_f16 v[16:19], v[124:127], v[56:59], v[16:19]\ns_waitcnt lgkmcnt(5)\n" \
  "v_mfma_f32_16x16x32_f16 v[16:19], v[116:119], v[60:63], v[16:19]\nv_mfma_f32_16x16x32_f16 v[16:19], v[108:111], v[120:123], v[16:19]\n" \
  "s_waitcnt lgkmcnt(3)\nv_mfma_f32_16x16x32_f16 v[16:19], v[100:103], v[24:27], v[16:19]\nv_cvt_pkrtz_f16_f32 v20, v12, v13\n" \
  "v_cvt_pkrtz_f16_f32 v21, v14, v15\nv_mfma_f32_16x16x32_f16 v[16:19], v[92:95], v[112:115], v[16:19]\n" \
  "v_pk_add_u16 v20, v20, 4 op_sel_hi:[1,0] clamp\ns_waitcnt lgkmcnt(2)\nv_mfma_f32_16x16x32_f16 v[16:19], v[84:87], v[28:31], v[16:19]\n" \
  "v_pk_add_u16 v21, v21, 4 op_sel_hi:[1,0] clamp\ns_waitcnt lgkmcnt(1)\nv_mfma_f32_16x16x32_f16 v[16:19], v[76:79], v[104:107], v[16:19]\n" \
  "ds_write_b64 v4, v[20:21] offset:0\ns_waitcnt lgkmcnt(1)\nv_mfma_f32_16x16x32_f16 v[16:19], v[68:71], v[32:35], v[16:19]\ns_nop 7\n" \
  "v_cvt_pkrtz_f16_f32 v20, v16, v17\nv_cvt_pkrtz_f16_f32 v21, v18, v19\nv_pk_add_u16 v20, v20, 4 op_sel_hi:[1,0] clamp\n" \
  "v_pk_add_u16 v21, v21, 4 op_sel_hi:[1,0] clamp\nds_write_b64 v4, v[20:21] offset:1024\ns_setprio 0\ns_setpc_b64 s[90:91]\n3:\n" \
  "ds_read_b64 v[124:125], v3 offset:0\nds_read_b64 v[126:127], v3 offset:8\nds_read_b64 v[120:121], v3 offset:64\n" \
  "ds_read_b64 v[122:123], v3 offset:72\nds_read_b64 v[116:117], v3 offset:128\nds_read_b64 v[118:119], v3 offset:136\n" \
  "ds_read_b64 v[112:113], v3 offset:192\nds_read_b64 v[114:115], v3 offset:200\nds_read_b64 v[108:109], v3 offset:256\n" \
  "ds_read_b64 v[110:111], v3 offset:264\nds_read_b64 v[104:105], v3 offset:320\nds_read_b64 v[106:107], v3 offset:328\n" \
  "ds_read_b64 v[100:101], v3 offset:384\nds_read_b64 v[102:103], v3 offset:392\nds_read_b64 v[96:97], v3 offset:448\n" \
  "ds_read_b64 v[98:99], v3 offset:456\nds_read_b64 v[92:93], v3 offset:512\nds_read_b64 v[94:95], v3 offset:520\n" \
  "ds_read_b64 v[88:89], v3 offset:576\nds_read_b64 v[90:91], v3 offset:584\nds_read_b64 v[84:85], v3 offset:640\n" \
  "ds_read_b64 v[86:87], v3 offset:648\nds_read_b64 v[80:81], v3 offset:704\nds_read_b64 v[82:83], v3 offset:712\n" \
  "ds_read_b64 v[76:77], v3 offset:768\nds_read_b64 v[78:79], v3 offset:776\nds_read_b64 v[72:73], v3 offset:832\n" \
  "ds_read_b64 v[74:75], v3 offset:840\nds_read_b64 v[68:69], v3 offset:896\nds_read_b64 v[70:71], v3 offset:904\n" \
  "ds_read_b64 v[64:65], v3 offset:960\nds_read_b64 v[66:67], v3 offset:968\nds_read_b64 v[60:61], v3 offset:1024\n" \
  "ds_read_b64 v[62:63], v3 offset:1032\nds_read_b64 v[56:57], v6 offset:256\nds_read_b64 v[58:59], v6 offset:264\n" \
  "ds_read_b64 v[52:53], v6 offset:128\nds_read_b64 v[54:55], v6 offset:136\nds_read_b64 v[48:49], v6 offset:320\n" \
  "ds_read_b64 v[50:51], v6 offset:328\nds_read_b64 v[44:45], v6 offset:192\nds_read_b64 v[46:47], v6 offset:200\ns_waitcnt lgkmcnt(0)\ns_barrier\n" \
  "ds_read_b128 v[24:27], v2 offset:448\nds_read_b128 v[28:31], v2 offset:512\nds_read_b128 v[32:35], v2 offset:576\n" \
  "ds_read_b128 v[36:39], v2 offset:640\nds_read_b128 v[40:43], v2 offset:704\ns_waitcnt lgkmcnt(3)\n" \
  "v_mfma_f32_16x16x32_f16 v[12:15], v[124:127], v[24:27], 0\ns_setprio 1\nds_read_b128 v[24:27], v2 offset:768\n" \
  "v_mfma_f32_16x16x32_f16 v[12:15], v[120:123], v[28:31], v[12:15]\nds_read_b128 v[28:31], v2 offset:832\ns_waitcnt lgkmcnt(3)\n" \
  "v_mfma_f32_16x16x32_f16 v[12:15], v[116:119], v[32:35], v[12:15]\nds_read_b128 v[32:35], v2 offset:896\n" \
  "v_mfma_f32_16x16x32_f16 v[12:15], v[112:115], v[36:39], v[12:15]\nds_read_b128 v[36:39], v2 offset:960\ns_waitcnt lgkmcnt(3)\n" \
  "v_mfma_f32_16x16x32_f16 v[12:15], v[108:111], v[40:43], v[12:15]\nds_read_b128 v[40:43], v2 offset:1024\n" \
  "v_mfma_f32_16x16x32_f16 v[12:15], v[104:107], v[24:27], v[12:15]\nds_read_b128 v[24:27], v2 offset:1088\ns_waitcnt lgkmcnt(3)\n" \
  "v_mfma_f32_16x16x32_f16 v[12:15], v[100:103], v[28:31], v[12:15]\nds_read_b128 v[28:31], v2 offset:1152\n" \
  "v_mfma_f32_16x16x32_f16 v[12:15], v[96:99], v[32:35], v[12:15]\ns_waitcnt lgkmcnt(3)\n" \
  "v_mfma_f32_16x16x32_f16 v[12:15], v[92:95], v[36:39], v[12:15]\nds_read_b128 v[32:35], v2 offset:1216\n" \
  "v_mfma_f32_16x16x32_f16 v[16:19], v[124:127], v[36:39], 0\ns_waitcnt lgkmcnt(3)\nv_mfma_f32_16x16x32_f16 v[12:15], v[88:91], v[40:43], v[12:15]\n" \
  "ds_read_b128 v[124:127], v2 offset:1280\nv_mfma_f32_16x16x32_f16 v[16:19], v[120:123], v[40:43], v[16:19]\ns_waitcnt lgkmcnt(3)\n" \
  "v_mfma_f32_16x16x32_f16 v[12:15], v[84:87], v[24:27], v[12:15]\nds_read_b128 v[36:39], v2 offset:1344\n" \
  "v_mfma_f32_16x16x32_f16 v[16:19], v[116:119], v[24:27], v[16:19]\ns_waitcnt lgkmcnt(3)\n" \
  "v_mfma_f32_16x16x32_f16 v[12:15], v[80:83], v[28:31], v[12:15]\nds_read_b128 v[120:123], v2 offset:1408\n" \
  "v_mfma_f32_16x16x32_f16 v[16:19], v[112:115], v[28:31], v[16:19]\ns_waitcnt lgkmcnt(3)\n" \
  "v_mfma_f32_16x16x32_f16 v[12:15], v[76:79], v[32:35], v[12:15]\nds_read_b128 v[40:43], v2 offset:1472\n" \
  "v_mfma_f32_16x16x32_f16 v[16:19], v[108:111], v[32:35], v[16:19]\ns_waitcnt lgkmcnt(3)\n" \
  "v_mfma_f32_16x16x32_f16 v[12:15], v[72:75], v[124:127], v[12:15]\nds_read_b128 v[116:119], v2 offset:1536\n" \
  "v_mfma_f32_16x16x32_f16 v[16:19], v[104:107], v[124:127], v[16:19]\nds_read_b128 v[24:27], v2 offset:1600\ns_waitcnt lgkmcnt(3)\n" \
  "v_mfma_f32_16x16x32_f16 v[12:15], v[68:71], v[36:39], v[12:15]\nds_read_b128 v[112:115], v2 offset:1664\nds_read_b32 v23, v9 offset:32\n" \
  "v_mfma_f32_16x16x32_f16 v[16:19], v[100:103], v[36:39], v[16:19]\nds_read_b128 v[28:31], v2 offset:1728\n" \
  "v_mfma_f32_16x16x32_f16 v[12:15], v[64:67], v[120:123], v[12:15]\nds_read_b128 v[108:111], v2 offset:1792\n" \
  "v_mfma_f32_16x16x32_f16 v[16:19], v[96:99], v[120:123], v[16:19]\nds_read_b128 v[32:35], v2 offset:1856\ns_waitcnt lgkmcnt(6)\n" \
  "v_mfma_f32_16x16x32_f16 v[12:15], v[60:63], v[40:43], v[12:15]\nds_read_b128 v[104:107], v2 offset:1920\n" \
  "v_mfma_f32_16x16x32_f16 v[16:19], v[92:95], v[40:43], v[16:19]\nds_read_b128 v[124:127], v2 offset:1984\ns_waitcnt lgkmcnt(5)\n" \
  "v_readfirstlane_b32 s85, v23\ns_cmp_lt_u32 s85, 3\ns_cbranch_scc1 51f\n52:\nv_mfma_f32_16x16x32_f16 v[16:19], v[88:91], v[116:119], v[16:19]\n" \
  "ds_read_b64_tr_b16 v[100:101], v5 offset:0\nds_read_b64_tr_b16 v[102:103], v5 offset:32\n" \
  "v_mfma_f32_16x16x32_f16 v[16:19], v[84:87], v[24:27], v[16:19]\nds_read_b64_tr_b16 v[36:37], v5 offset:256\n" \
  "ds_read_b64_tr_b16 v[38:39], v5 offset:288\nv_mfma_f32_16x16x32_f16 v[16:19], v[80:83], v[112:115], v[16:19]\n" \
  "ds_read_b64_tr_b16 v[96:97], v5 offset:512\nds_read_b64_tr_b16 v[98:99], v5 offset:544\ns_waitcnt lgkmcnt(9)\n" \
  "v_mfma_f32_16x16x32_f16 v[16:19], v[76:79], v[28:31], v[16:19]\nds_read_b64_tr_b16 v[120:121], v5 offset:768\n" \
  "ds_read_b64_tr_b16 v[122:123], v5 offset:800\nv_mfma_f32_16x16x32_f16 v[16:19], v[72:75], v[108:111], v[16:19]\ns_waitcnt lgkmcnt(9)\n" \
  "v_mfma_f32_16x16x32_f16 v[16:19], v[68:71], v[32:35], v[16:19]\nv_mfma_f32_16x16x32_f16 v[16:19], v[64:67], v[104:107], v[16:19]\n" \
  "s_waitcnt lgkmcnt(6)\nv_mfma_f32_16x16x32_f16 v[16:19], v[60:63], v[124:127], v[16:19]\n" \
  "v_mfma_f32_16x16x32_f16 v[12:15], v[100:103], v[56:59], v[12:15]\nv_mfma_f32_16x16x32_f16 v[16:19], v[100:103], v[52:55], v[16:19]\n" \
  "s_waitcnt lgkmcnt(2)\nv_mfma_f32_16x16x32_f16 v[12:15], v[36:39], v[48:51], v[12:15]\n" \
  "v_mfma_f32_16x16x32_f16 v[16:19], v[36:39], v[44:47], v[16:19]\nv_mfma_f32_16x16x32_f16 v[16:19], v[96:99], v[56:59], v[16:19]\n" \
  "s_waitcnt lgkmcnt(0)\nv_mfma_f32_16x16x32_f16 v[16:19], v[120:123], v[48:51], v[16:19]\ns_nop 3\nv_cvt_pkrtz_f16_f32 v20, v12, v13\n" \
  "v_cvt_pkrtz_f16_f32 v21, v14, v15\nv_pk_add_u16 v20, v20, 4 op_sel_hi:[1,0] clamp\nv_pk_add_u16 v21, v21, 4 op_sel_hi:[1,0] clamp\n" \
  "ds_write_b64 v4, v[20:21] offset:2048\nv_cvt_pkrtz_f16_f32 v20, v16, v17\nv_cvt_pkrtz_f16_f32 v21, v18, v19\n" \
  "v_pk_add_u16 v20, v20, 4 op_sel_hi:[1,0] clamp\nv_pk_add_u16 v21, v21, 4 op_sel_hi:[1,0] clamp\nds_write_b64 v4, v[20:21] offset:3072\n" \
  "s_setprio 0\ns_setpc_b64 s[90:91]\n51:\ns_movk_i32 s84, 0x4000\n53:\nds_read_b32 v23, v9 offset:32\ns_sub_u32 s84, s84, 1\ns_cbranch_scc1 52b\n" \
  "s_waitcnt lgkmcnt(0)\nv_readfirstlane_b32 s85, v23\ns_cmp_lt_u32 s85, 3\ns_cbranch_scc1 53b\ns_branch 52b\n4:\n" \
  "ds_read_b64 v[124:125], v3 offset:1088\nds_read_b64 v[126:127], v3 offset:1096\nds_read_b64 v[120:121], v3 offset:0\n" \
  "ds_read_b64 v[122:123], v3 offset:8\nds_read_b64 v[116:117], v3 offset:64\nds_read_b64 v[118:119], v3 offset:72\n" \
  "ds_read_b64 v[112:113], v3 offset:128\nds_read_b64 v[114:115], v3 offset:136\nds_read_b64 v[108:109], v3 offset:192\n" \
  "ds_read_b64 v[110:111], v3 offset:200\nds_read_b64 v[104:105], v3 offset:256\nds_read_b64 v[106:107], v3 offset:264\n" \
  "ds_read_b64 v[100:101], v3 offset:320\nds_read_b64 v[102:103], v3 offset:328\nds_read_b64 v[96:97], v3 offset:384\n" \
  "ds_read_b64 v[98:99], v3 offset:392\nds_read_b64 v[92:93], v3 offset:448\nds_read_b64 v[94:95], v3 offset:456\n" \
  "ds_read_b64 v[88:89], v3 offset:512\nds_read_b64 v[90:91], v3 offset:520\nds_read_b64 v[84:85], v3 offset:576\n" \
  "ds_read_b64 v[86:87], v3 offset:584\nds_read_b64 v[80:81], v3 offset:640\nds_read_b64 v[82:83], v3 offset:648\n" \
  "ds_read_b64 v[76:77], v3 offset:704\nds_read_b64 v[78:79], v3 offset:712\nds_read_b64 v[72:73], v3 offset:768\n" \
  "ds_read_b64 v[74:75], v3 offset:776\nds_read_b64 v[68:69], v3 offset:832\nds_read_b64 v[70:71], v3 offset:840\n" \
  "ds_read_b64 v[64:65], v3 offset:896\nds_read_b64 v[66:67], v3 offset:904\nds_read_b64 v[60:61], v3 offset:960\n" \
  "ds_read_b64 v[62:63], v3 offset:968\nds_read_b64 v[56:57], v3 offset:1024\nds_read_b64 v[58:59], v3 offset:1032\n" \
  "ds_read_b64 v[52:53], v6 offset:0\nds_read_b64 v[54:55], v6 offset:8\nds_read_b64 v[48:49], v6 offset:64\nds_read_b64 v[50:51], v6 offset:72\n" \
  "ds_read_b64 v[44:45], v6 offset:128\nds_read_b64 v[46:47], v6 offset:136\nds_read_b64 v[40:41], v6 offset:192\n" \
  "ds_read_b64 v[42:43], v6 offset:200\ns_waitcnt lgkmcnt(0)\ns_barrier\nds_read_b128 v[24:27], v2 offset:448\nds_read_b128 v[28:31], v2 offset:960\n" \
  "ds_read_b128 v[32:35], v2 offset:1472\nds_read_b128 v[36:39], v2 offset:1536\ns_waitcnt lgkmcnt(3)\n" \
  "v_mfma_f32_16x16x32_f16 v[12:15], v[124:127], v[24:27], 0\ns_setprio 1\nds_read_b128 v[24:27], v2 offset:1600\ns_waitcnt lgkmcnt(3)\n" \
  "v_mfma_f32_16x16x32_f16 v[16:19], v[124:127], v[28:31], 0\nds_read_b128 v[124:127], v2 offset:1664\ns_waitcnt lgkmcnt(3)\n" \
  "v_mfma_f32_16x16x32_f16 v[12:15], v[120:123], v[32:35], v[12:15]\nds_read_b128 v[28:31], v2 offset:1728\ns_waitcnt lgkmcnt(3)\n" \
  "v_mfma_f32_16x16x32_f16 v[12:15], v[116:119], v[36:39], v[12:15]\nds_read_b128 v[32:35], v2 offset:1792\ns_waitcnt lgkmcnt(3)\n" \
  "v_mfma_f32_16x16x32_f16 v[12:15], v[112:115], v[24:27], v[12:15]\nds_read_b128 v[36:39], v2 offset:1856\ns_waitcnt lgkmcnt(3)\n" \
  "v_mfma_f32_16x16x32_f16 v[12:15], v[108:111], v[124:127], v[12:15]\nds_read_b128 v[24:27], v2 offset:1920\ns_waitcnt lgkmcnt(3)\n" \
  "v_mfma_f32_16x16x32_f16 v[12:15], v[104:107], v[28:31], v[12:15]\nds_read_b128 v[124:127], v2 offset:1984\ns_waitcnt lgkmcnt(3)\n" \
  "v_mfma_f32_16x16x32_f16 v[12:15], v[100:103], v[32:35], v[12:15]\nds_read_b128 v[28:31], v2 offset:2048\ns_waitcnt lgkmcnt(3)\n" \
  "v_mfma_f32_16x16x32_f16 v[12:15], v[96:99], v[36:39], v[12:15]\nds_read_b128 v[32:35], v2 offset:2112\ns_waitcnt lgkmcnt(3)\n" \
  "v_mfma_f32_16x16x32_f16 v[12:15], v[92:95], v[24:27], v[12:15]\nds_read_b128 v[36:39], v2 offset:2176\ns_waitcnt lgkmcnt(3)\n" \
  "v_mfma_f32_16x16x32_f16 v[12:15], v[88:91], v[124:127], v[12:15]\nds_read_b128 v[24:27], v2 offset:2240\n" \
  "v_mfma_f32_16x16x32_f16 v[16:19], v[120:123], v[124:127], v[16:19]\nds_read_b64 v[120:121], v6 offset:256\nds_read_b64 v[122:123], v6 offset:264\n" \
  "s_waitcnt lgkmcnt(4)\nv_mfma_f32_16x16x32_f16 v[12:15], v[84:87], v[28:31], v[12:15]\nds_read_b128 v[124:127], v2 offset:2304\n" \
  "v_mfma_f32_16x16x32_f16 v[16:19], v[116:119], v[28:31], v[16:19]\nds_read_b64 v[116:117], v6 offset:320\nds_read_b64 v[118:119], v6 offset:328\n" \
  "v_mfma_f32_16x16x32_f16 v[12:15], v[80:83], v[32:35], v[12:15]\nds_read_b128 v[28:31], v2 offset:2368\n" \
  "v_mfma_f32_16x16x32_f16 v[16:19], v[112:115], v[32:35], v[16:19]\ns_waitcnt lgkmcnt(6)\n" \
  "v_mfma_f32_16x16x32_f16 v[12:15], v[76:79], v[36:39], v[12:15]\nds_read_b128 v[112:115], v2 offset:2432\n" \
  "v_mfma_f32_16x16x32_f16 v[16:19], v[108:111], v[36:39], v[16:19]\nds_read2_b32 v[32:33], v8 offset0:48 offset1:49\n" \
  "ds_read2_b32 v[34:35], v8 offset0:50 offset1:51\nv_mfma_f32_16x16x32_f16 v[12:15], v[72:75], v[24:27], v[12:15]\n" \
  "ds_read_b128 v[108:111], v2 offset:2496\nv_mfma_f32_16x16x32_f16 v[16:19], v[104:107], v[24:27], v[16:19]\n" \
  "ds_read2_b32 v[36:37], v8 offset0:32 offset1:33\nds_read2_b32 v[38:39], v8 offset0:34 offset1:35\ns_waitcnt lgkmcnt(6)\n" \
  "v_mfma_f32_16x16x32_f16 v[12:15], v[68:71], v[124:127], v[12:15]\nds_read_b128 v[104:107], v2 offset:2560\n" \
  "v_mfma_f32_16x16x32_f16 v[16:19], v[100:103], v[124:127], v[16:19]\nds_read_b128 v[24:27], v2 offset:2624\n" \
  "v_mfma_f32_16x16x32_f16 v[12:15], v[64:67], v[28:31], v[12:15]\nds_read_b128 v[100:103], v2 offset:2688\nds_read_b32 v23, v9 offset:32\n" \
  "v_mfma_f32_16x16x32_f16 v[16:19], v[96:99], v[28:31], v[16:19]\nds_read_b128 v[124:127], v2 offset:2752\ns_waitcnt lgkmcnt(7)\n" \
  "v_mfma_f32_16x16x32_f16 v[12:15], v[60:63], v[112:115], v[12:15]\nds_read_b128 v[96:99], v2 offset:2816\n" \
  "v_mfma_f32_16x16x32_f16 v[16:19], v[92:95], v[112:115], v[16:19]\nds_read_b128 v[28:31], v2 offset:2880\n" \
  "v_mfma_f32_16x16x32_f16 v[12:15], v[56:59], v[108:111], v[12:15]\nds_read_b128 v[92:95], v2 offset:2944\n" \
  "v_mfma_f32_16x16x32_f16 v[16:19], v[88:91], v[108:111], v[16:19]\nds_read_b128 v[112:115], v2 offset:3008\ns_waitcnt lgkmcnt(5)\n" \
  "v_readfirstlane_b32 s85, v23\ns_cmp_lt_u32 s85, 3\ns_cbranch_scc1 61f\n62:\nv_mfma_f32_16x16x32_f16 v[16:19], v[84:87], v[104:107], v[16:19]\n" \
  "ds_read_b64_tr_b16 v[88:89], v5 offset:0\nds_read_b64_tr_b16 v[90:91], v5 offset:32\n" \
  "v_mfma_f32_16x16x32_f16 v[16:19], v[80:83], v[24:27], v[16:19]\nds_read_b64_tr_b16 v[108:109], v5 offset:256\n" \
  "ds_read_b64_tr_b16 v[110:111], v5 offset:288\nv_mfma_f32_16x16x32_f16 v[16:19], v[76:79], v[100:103], v[16:19]\n" \
  "ds_read_b64_tr_b16 v[84:85], v5 offset:512\nds_read_b64_tr_b16 v[86:87], v5 offset:544\ns_waitcnt lgkmcnt(9)\n" \
  "v_mfma_f32_16x16x32_f16 v[16:19], v[72:75], v[124:127], v[16:19]\nds_read_b64_tr_b16 v[104:105], v5 offset:768\n" \
  "ds_read_b64_tr_b16 v[106:107], v5 offset:800\nv_mfma_f32_16x16x32_f16 v[16:19], v[68:71], v[96:99], v[16:19]\n" \
  "ds_read_b64_tr_b16 v[80:81], v5 offset:1024\nds_read_b64_tr_b16 v[82:83], v5 offset:1056\ns_waitcnt lgkmcnt(11)\n" \
  "v_mfma_f32_16x16x32_f16 v[16:19], v[64:67], v[28:31], v[16:19]\nds_read_b64_tr_b16 v[24:25], v5 offset:1280\n" \
  "ds_read_b64_tr_b16 v[26:27], v5 offset:1312\nv_mfma_f32_16x16x32_f16 v[16:19], v[60:63], v[92:95], v[16:19]\n" \
  "ds_read_b64_tr_b16 v[76:77], v5 offset:1536\nds_read_b64_tr_b16 v[78:79], v5 offset:1568\ns_waitcnt lgkmcnt(12)\n" \
  "v_mfma_f32_16x16x32_f16 v[16:19], v[56:59], v[112:115], v[16:19]\nds_read_b64_tr_b16 v[100:101], v5 offset:1792\n" \
  "ds_read_b64_tr_b16 v[102:103], v5 offset:1824\nv_mfma_f32_16x16x32_f16 v[12:15], v[88:91], v[52:55], v[12:15]\ns_waitcnt lgkmcnt(10)\n" \
  "v_mfma_f32_16x16x32_f16 v[12:15], v[108:111], v[48:51], v[12:15]\nds_read_b64_tr_b16 v[72:73], v7 offset:0\n" \
  "ds_read_b64_tr_b16 v[74:75], v7 offset:64\nv_mfma_f32_16x16x32_f16 v[12:15], v[84:87], v[44:47], v[12:15]\n" \
  "ds_read_b64_tr_b16 v[124:125], v7 offset:512\nds_read_b64_tr_b16 v[126:127], v7 offset:576\n" \
  "v_mfma_f32_16x16x32_f16 v[16:19], v[84:87], v[52:55], v[16:19]\ns_waitcnt lgkmcnt(10)\n" \
  "v_mfma_f32_16x16x32_f16 v[12:15], v[104:107], v[40:43], v[12:15]\nv_mfma_f32_16x16x32_f16 v[16:19], v[104:107], v[48:51], v[16:19]\n" \
  "v_mfma_f32_16x16x32_f16 v[12:15], v[80:83], v[120:123], v[12:15]\nv_mfma_f32_16x16x32_f16 v[16:19], v[80:83], v[44:47], v[16:19]\n" \
  "s_waitcnt lgkmcnt(6)\nv_mfma_f32_16x16x32_f16 v[12:15], v[24:27], v[116:119], v[12:15]\n" \
  "v_mfma_f32_16x16x32_f16 v[16:19], v[24:27], v[40:43], v[16:19]\nv_mfma_f32_16x16x32_f16 v[16:19], v[76:79], v[120:123], v[16:19]\n" \
  "s_waitcnt lgkmcnt(2)\nv_mfma_f32_16x16x32_f16 v[16:19], v[100:103], v[116:119], v[16:19]\n" \
  "v_mfma_f32_16x16x32_f16 v[12:15], v[72:75], v[32:35], v[12:15]\nv_mfma_f32_16x16x32_f16 v[16:19], v[72:75], v[36:39], v[16:19]\n" \
  "s_waitcnt lgkmcnt(0)\nv_mfma_f32_16x16x32_f16 v[16:19], v[124:127], v[32:35], v[16:19]\ns_nop 4\nv_cvt_pkrtz_f16_f32 v20, v12, v13\n" \
  "v_cvt_pkrtz_f16_f32 v21, v14, v15\nv_pk_add_u16 v20, v20, 4 op_sel_hi:[1,0] clamp\nv_pk_add_u16 v21, v21, 4 op_sel_hi:[1,0] clamp\n" \
  "ds_write_b64 v4, v[20:21] offset:4096\nv_cvt_pkrtz_f16_f32 v20, v16, v17\nv_cvt_pkrtz_f16_f32 v21, v18, v19\n" \
  "v_pk_add_u16 v20, v20, 4 op_sel_hi:[1,0] clamp\nv_pk_add_u16 v21, v21, 4 op_sel_hi:[1,0] clamp\nds_write_b64 v4, v[20:21] offset:5120\n" \
  "s_setprio 0\ns_setpc_b64 s[90:91]\n61:\ns_movk_i32 s84, 0x4000\n63:\nds_read_b32 v23, v9 offset:32\ns_sub_u32 s84, s84, 1\ns_cbranch_scc1 62b\n" \
  "s_waitcnt lgkmcnt(0)\nv_readfirstlane_b32 s85, v23\ns_cmp_lt_u32 s85, 3\ns_cbranch_scc1 63b\ns_branch 62b\n5:\n" \
  "ds_read_b64 v[124:125], v3 offset:1088\nds_read_b64 v[126:127], v3 offset:1096\nds_read_b64 v[120:121], v3 offset:0\n" \
  "ds_read_b64 v[122:123], v3 offset:8\nds_read_b64 v[116:117], v3 offset:64\nds_read_b64 v[118:119], v3 offset:72\n" \
  "ds_read_b64 v[112:113], v3 offset:128\nds_read_b64 v[114:115], v3 offset:136\nds_read_b64 v[108:109], v3 offset:192\n" \
  "ds_read_b64 v[110:111], v3 offset:200\nds_read_b64 v[104:105], v3 offset:256\nds_read_b64 v[106:107], v3 offset:264\n" \
  "ds_read_b64 v[100:101], v3 offset:320\nds_read_b64 v[102:103], v3 offset:328\nds_read_b64 v[96:97], v3 offset:384\n" \
  "ds_read_b64 v[98:99], v3 offset:392\nds_read_b64 v[92:93], v3 offset:448\nds_read_b64 v[94:95], v3 offset:456\n" \
  "ds_read_b64 v[88:89], v3 offset:512\nds_read_b64 v[90:91], v3 offset:520\nds_read_b64 v[84:85], v3 offset:576\n" \
  "ds_read_b64 v[86:87], v3 offset:584\nds_read_b64 v[80:81], v3 offset:640\nds_read_b64 v[82:83], v3 offset:648\n" \
  "ds_read_b64 v[76:77], v3 offset:704\nds_read_b64 v[78:79], v3 offset:712\nds_read_b64 v[72:73], v3 offset:768\n" \
  "ds_read_b64 v[74:75], v3 offset:776\nds_read_b64 v[68:69], v3 offset:832\nds_read_b64 v[70:71], v3 offset:840\n" \
  "ds_read_b64 v[64:65], v3 offset:896\nds_read_b64 v[66:67], v3 offset:904\nds_read_b64 v[60:61], v3 offset:960\n" \
  "ds_read_b64 v[62:63], v3 offset:968\nds_read_b64 v[56:57], v3 offset:1024\nds_read_b64 v[58:59], v3 offset:1032\n" \
  "ds_read_b64 v[52:53], v6 offset:0\nds_read_b64 v[54:55], v6 offset:8\nds_read_b64 v[48:49], v6 offset:64\nds_read_b64 v[50:51], v6 offset:72\n" \
  "ds_read_b64 v[44:45], v6 offset:128\nds_read_b64 v[46:47], v6 offset:136\nds_read_b64 v[40:41], v6 offset:192\n" \
  "ds_read_b64 v[42:43], v6 offset:200\ns_waitcnt lgkmcnt(0)\ns_barrier\nds_read_b128 v[24:27], v2 offset:1472\n" \
  "ds_read_b128 v[28:31], v2 offset:1984\nds_read_b128 v[32:35], v2 offset:2496\nds_read_b128 v[36:39], v2 offset:2560\ns_waitcnt lgkmcnt(3)\n" \
  "v_mfma_f32_16x16x32_f16 v[12:15], v[124:127], v[24:27], 0\ns_setprio 1\nds_read_b128 v[24:27], v2 offset:2624\ns_waitcnt lgkmcnt(3)\n" \
  "v_mfma_f32_16x16x32_f16 v[16:19], v[124:127], v[28:31], 0\nds_read_b128 v[124:127], v2 offset:2688\ns_waitcnt lgkmcnt(3)\n" \
  "v_mfma_f32_16x16x32_f16 v[12:15], v[120:123], v[32:35], v[12:15]\nds_read_b128 v[28:31], v2 offset:2752\ns_waitcnt lgkmcnt(3)\n" \
  "v_mfma_f32_16x16x32_f16 v[12:15], v[116:119], v[36:39], v[12:15]\nds_read_b128 v[32:35], v2 offset:2816\ns_waitcnt lgkmcnt(3)\n" \
  "v_mfma_f32_16x16x32_f16 v[12:15], v[112:115], v[24:27], v[12:15]\nds_read_b128 v[36:39], v2 offset:2880\ns_waitcnt lgkmcnt(3)\n" \
  "v_mfma_f32_16x16x32_f16 v[12:15], v[108:111], v[124:127], v[12:15]\nds_read_b128 v[24:27], v2 offset:2944\ns_waitcnt lgkmcnt(3)\n" \
  "v_mfma_f32_16x16x32_f16 v[12:15], v[104:107], v[28:31], v[12:15]\nds_read_b128 v[124:127], v2 offset:3008\ns_waitcnt lgkmcnt(3)\n" \
  "v_mfma_f32_16x16x32_f16 v[12:15], v[100:103], v[32:35], v[12:15]\nds_read_b128 v[28:31], v2 offset:3072\ns_waitcnt lgkmcnt(3)\n" \
  "v_mfma_f32_16x16x32_f16 v[12:15], v[96:99], v[36:39], v[12:15]\nds_read_b128 v[32:35], v2 offset:3136\ns_waitcnt lgkmcnt(3)\n" \
  "v_mfma_f32_16x16x32_f16 v[12:15], v[92:95], v[24:27], v[12:15]\nds_read_b128 v[36:39], v2 offset:3200\ns_waitcnt lgkmcnt(3)\n" \
  "v_mfma_f32_16x16x32_f16 v[12:15], v[88:91], v[124:127], v[12:15]\nds_read_b128 v[24:27], v2 offset:3264\n" \
  "v_mfma_f32_16x16x32_f16 v[16:19], v[120:123], v[124:127], v[16:19]\nds_read_b64 v[120:121], v6 offset:256\nds_read_b64 v[122:123], v6 offset:264\n" \
  "s_waitcnt lgkmcnt(4)\nv_mfma_f32_16x16x32_f16 v[12:15], v[84:87], v[28:31], v[12:15]\nds_read_b128 v[124:127], v2 offset:3328\n" \
  "v_mfma_f32_16x16x32_f16 v[16:19], v[116:119], v[28:31], v[16:19]\nds_read_b64 v[116:117], v6 offset:320\nds_read_b64 v[118:119], v6 offset:328\n" \
  "v_mfma_f32_16x16x32_f16 v[12:15], v[80:83], v[32:35], v[12:15]\nds_read_b128 v[28:31], v2 offset:3392\n" \
  "v_mfma_f32_16x16x32_f16 v[16:19], v[112:115], v[32:35], v[16:19]\ns_waitcnt lgkmcnt(6)\n" \
  "v_mfma_f32_16x16x32_f16 v[12:15], v[76:79], v[36:39], v[12:15]\nds_read_b128 v[112:115], v2 offset:3456\n" \
  "v_mfma_f32_16x16x32_f16 v[16:19], v[108:111], v[36:39], v[16:19]\nds_read2_b32 v[32:33], v8 offset0:16 offset1:17\n" \
  "ds_read2_b32 v[34:35], v8 offset0:18 offset1:19\nv_mfma_f32_16x16x32_f16 v[12:15], v[72:75], v[24:27], v[12:15]\n" \
  "ds_read_b128 v[108:111], v2 offset:3520\nv_mfma_f32_16x16x32_f16 v[16:19], v[104:107], v[24:27], v[16:19]\n" \
  "ds_read2_b32 v[36:37], v8 offset0:0 offset1:1\nds_read2_b32 v[38:39], v8 offset0:2 offset1:3\ns_waitcnt lgkmcnt(6)\n" \
  "v_mfma_f32_16x16x32_f16 v[12:15], v[68:71], v[124:127], v[12:15]\nds_read_b128 v[104:107], v2 offset:3584\n" \
  "v_mfma_f32_16x16x32_f16 v[16:19], v[100:103], v[124:127], v[16:19]\nds_read_b128 v[24:27], v2 offset:3648\n" \
  "ds_read2_b32 v[100:101], v8 offset0:32 offset1:33\nds_read2_b32 v[102:103], v8 offset0:34 offset1:35\n" \
  "v_mfma_f32_16x16x32_f16 v[12:15], v[64:67], v[28:31], v[12:15]\nds_read_b128 v[124:127], v2 offset:3712\nds_read_b32 v23, v9 offset:32\n" \
  "v_mfma_f32_16x16x32_f16 v[16:19], v[96:99], v[28:31], v[16:19]\nds_read_b128 v[96:99], v2 offset:3776\ns_waitcnt lgkmcnt(9)\n" \
  "v_mfma_f32_16x16x32_f16 v[12:15], v[60:63], v[112:115], v[12:15]\nds_read_b128 v[28:31], v2 offset:3840\n" \
  "v_mfma_f32_16x16x32_f16 v[16:19], v[92:95], v[112:115], v[16:19]\nds_read_b128 v[92:95], v2 offset:3904\n" \
  "v_mfma_f32_16x16x32_f16 v[12:15], v[56:59], v[108:111], v[12:15]\nds_read_b128 v[112:115], v2 offset:3968\n" \
  "v_mfma_f32_16x16x32_f16 v[16:19], v[88:91], v[108:111], v[16:19]\nds_read_b128 v[88:91], v2 offset:4032\ns_waitcnt lgkmcnt(5)\n" \
  "v_readfirstlane_b32 s85, v23\ns_cmp_lt_u32 s85, 3\ns_cbranch_scc1 71f\n72:\nv_mfma_f32_16x16x32_f16 v[16:19], v[84:87], v[104:107], v[16:19]\n" \
  "ds_read_b64_tr_b16 v[108:109], v5 offset:1024\nds_read_b64_tr_b16 v[110:111], v5 offset:1056\n" \
  "v_mfma_f32_16x16x32_f16 v[16:19], v[80:83], v[24:27], v[16:19]\nds_read_b64_tr_b16 v[84:85], v5 offset:1280\n" \
  "ds_read_b64_tr_b16 v[86:87], v5 offset:1312\nv_mfma_f32_16x16x32_f16 v[16:19], v[76:79], v[124:127], v[16:19]\n" \
  "ds_read_b64_tr_b16 v[104:105], v5 offset:1536\nds_read_b64_tr_b16 v[106:107], v5 offset:1568\ns_waitcnt lgkmcnt(9)\n" \
  "v_mfma_f32_16x16x32_f16 v[16:19], v[72:75], v[96:99], v[16:19]\nds_read_b64_tr_b16 v[80:81], v5 offset:1792\n" \
  "ds_read_b64_tr_b16 v[82:83], v5 offset:1824\nv_mfma_f32_16x16x32_f16 v[16:19], v[68:71], v[28:31], v[16:19]\n" \
  "ds_read_b64_tr_b16 v[24:25], v5 offset:2048\nds_read_b64_tr_b16 v[26:27], v5 offset:2080\ns_waitcnt lgkmcnt(11)\n" \
  "v_mfma_f32_16x16x32_f16 v[16:19], v[64:67], v[92:95], v[16:19]\nds_read_b64_tr_b16 v[76:77], v5 offset:2304\n" \
  "ds_read_b64_tr_b16 v[78:79], v5 offset:2336\nv_mfma_f32_16x16x32_f16 v[16:19], v[60:63], v[112:115], v[16:19]\n" \
  "ds_read_b64_tr_b16 v[124:125], v5 offset:2560\nds_read_b64_tr_b16 v[126:127], v5 offset:2592\ns_waitcnt lgkmcnt(12)\n" \
  "v_mfma_f32_16x16x32_f16 v[16:19], v[56:59], v[88:91], v[16:19]\nds_read_b64_tr_b16 v[72:73], v5 offset:2816\n" \
  "ds_read_b64_tr_b16 v[74:75], v5 offset:2848\nv_mfma_f32_16x16x32_f16 v[12:15], v[108:111], v[52:55], v[12:15]\ns_waitcnt lgkmcnt(10)\n" \
  "v_mfma_f32_16x16x32_f16 v[12:15], v[84:87], v[48:51], v[12:15]\nds_read_b64_tr_b16 v[96:97], v7 offset:0\n" \
  "ds_read_b64_tr_b16 v[98:99], v7 offset:64\nv_mfma_f32_16x16x32_f16 v[12:15], v[104:107], v[44:47], v[12:15]\n" \
  "ds_read_b64_tr_b16 v[68:69], v7 offset:512\nds_read_b64_tr_b16 v[70:71], v7 offset:576\n" \
  "v_mfma_f32_16x16x32_f16 v[16:19], v[104:107], v[52:55], v[16:19]\ns_waitcnt lgkmcnt(10)\n" \
  "v_mfma_f32_16x16x32_f16 v[12:15], v[80:83], v[40:43], v[12:15]\nds_read_b64_tr_b16 v[28:29], v7 offset:1024\n" \
  "ds_read_b64_tr_b16 v[30:31], v7 offset:1088\nv_mfma_f32_16x16x32_f16 v[16:19], v[80:83], v[48:51], v[16:19]\n" \
  "ds_read2_b32 v[64:65], v8 offset0:48 offset1:49\nds_read2_b32 v[66:67], v8 offset0:50 offset1:51\n" \
  "v_mfma_f32_16x16x32_f16 v[12:15], v[24:27], v[120:123], v[12:15]\nv_mfma_f32_16x16x32_f16 v[16:19], v[24:27], v[44:47], v[16:19]\n" \
  "s_waitcnt lgkmcnt(10)\nv_mfma_f32_16x16x32_f16 v[12:15], v[76:79], v[116:119], v[12:15]\nds_read_b64_tr_b16 v[92:93], v7 offset:1536\n" \
  "ds_read_b64_tr_b16 v[94:95], v7 offset:1600\nv_mfma_f32_16x16x32_f16 v[16:19], v[76:79], v[40:43], v[16:19]\n" \
  "v_mfma_f32_16x16x32_f16 v[16:19], v[124:127], v[120:123], v[16:19]\ns_waitcnt lgkmcnt(8)\n" \
  "v_mfma_f32_16x16x32_f16 v[16:19], v[72:75], v[116:119], v[16:19]\nv_mfma_f32_16x16x32_f16 v[12:15], v[96:99], v[32:35], v[12:15]\n" \
  "v_mfma_f32_16x16x32_f16 v[16:19], v[96:99], v[36:39], v[16:19]\ns_waitcnt lgkmcnt(4)\n" \
  "v_mfma_f32_16x16x32_f16 v[12:15], v[68:71], v[100:103], v[12:15]\nv_mfma_f32_16x16x32_f16 v[16:19], v[68:71], v[32:35], v[16:19]\n" \
  "s_waitcnt lgkmcnt(2)\nv_mfma_f32_16x16x32_f16 v[12:15], v[28:31], v[64:67], v[12:15]\n" \
  "v_mfma_f32_16x16x32_f16 v[16:19], v[28:31], v[100:103], v[16:19]\ns_waitcnt lgkmcnt(0)\n" \
  "v_mfma_f32_16x16x32_f16 v[16:19], v[92:95], v[64:67], v[16:19]\ns_nop 4\nv_cvt_pkrtz_f16_f32 v20, v12, v13\nv_cvt_pkrtz_f16_f32 v21, v14, v15\n" \
  "v_pk_add_u16 v20, v20, 4 op_sel_hi:[1,0] clamp\nv_pk_add_u16 v21, v21, 4 op_sel_hi:[1,0] clamp\nds_write_b64 v4, v[20:21] offset:6144\n" \
  "v_cvt_pkrtz_f16_f32 v20, v16, v17\nv_cvt_pkrtz_f16_f32 v21, v18, v19\nv_pk_add_u16 v20, v20, 4 op_sel_hi:[1,0] clamp\n" \
  "v_pk_add_u16 v21, v21, 4 op_sel_hi:[1,0] clamp\nds_write_b64 v4, v[20:21] offset:7168\ns_setprio 0\ns_setpc_b64 s[90:91]\n71:\n" \
  "s_movk_i32 s84, 0x4000\n73:\nds_read_b32 v23, v9 offset:32\ns_sub_u32 s84, s84, 1\ns_cbranch_scc1 72b\ns_waitcnt lgkmcnt(0)\n" \
  "v_readfirstlane_b32 s85, v23\ns_cmp_lt_u32 s85, 3\ns_cbranch_scc1 73b\ns_branch 72b\n"
#define CONV_CLOB "memory", "scc", "s84", "s85", "s90", "s91", "v12", "v13", "v14", "v15", "v16", "v17", "v18", "v19", "v20", "v21", "v22", "v23", "v24", "v25", "v26", "v27", "v28", "v29", "v30", "v31", "v32", "v33", "v34", "v35", "v36", "v37", "v38", "v39", "v40", "v41", "v42", "v43", "v44", "v45", "v46", "v47", "v48", "v49", "v50", "v51", "v52", "v53", "v54", "v55", "v56", "v57", "v58", "v59", "v60", "v61", "v62", "v63", "v64", "v65", "v66", "v67", "v68", "v69", "v70", "v71", "v72", "v73", "v74", "v75", "v76", "v77", "v78", "v79", "v80", "v81", "v82", "v83", "v84", "v85", "v86", "v87", "v88", "v89", "v90", "v91", "v92", "v93", "v94", "v95", "v96", "v97", "v98", "v99", "v100", "v101", "v102", "v103", "v104", "v105", "v106", "v107", "v108", "v109", "v110", "v111", "v112", "v113", "v114", "v115", "v116", "v117", "v118", "v119", "v120", "v121", "v122", "v123", "v124", "v125", "v126", "v127"
constexpr int TDP = 1216, T_3 = 4 * TDP, T_4 = T_3 + 512, TB = T_4 + 320;
constexpr int C_U = 480, UB = C_U + 4096 + 96, C_G5 = 0, C_G3 = 3200, C_G9 = 6400, C_G7 = C_G9 + 2096, GB = C_G7 + 2080 + 112;
constexpr int O_FL = 2 * TB, O_UL = O_FL + 64, O_IM = O_UL + 2 * UB, O_ST = O_IM + 2 * GB;
constexpr int X_TB = 10240, X_UL = X_TB + TB, X_IM = X_UL + UB, X_ST = X_IM + GB;
constexpr int X_WR = X_IM + 128 * IP;
#define X_RW(w) (LW + 9216 * (2 + ((w) >> 1)) + 2560 * ((w) & 1))
constexpr int RP = 1216;
constexpr int LW = 36864 + 512, LA = LW, LM = LW + 36864;
constexpr int L_SC = LM + 2048, SMEM = O_IM + 256 * IP;
static_assert(L_SC + 1152 <= SMEM && O_ST + 8192 <= SMEM && X_IM + 128 * IP <= LM && X_ST + 8192 <= LM && UB % 128 == 64 && GB % 128 == 64 && SMEM <= 79872, "");
struct TOps { uint32_t xa, xb, ya, pd, pf, pg, pe; };
__device__ __forceinline__ void table_ops(uint32_t im, uint32_t tb, int ct, int lane, TOps& t) {
  const int j = lane & 15, kq = lane >> 4, q4 = (lane >> 2) & 3, p4 = lane & 3, jj = j + 16 * ct;
  const uint32_t sa = im + (uint32_t)(IP * (lane >> 2));
  t.xa = sa + (uint32_t)(I_X + 8 * p4); t.xb = sa + (uint32_t)(I_X + 32 + 8 * (p4 & 1)); t.ya = sa + (uint32_t)(32 * ct + 8 * p4);
  t.pd = tb + (uint32_t)(2 * (541 - (8 * jj + 4 * (kq & 1) + 256 * (kq >> 1))));
  t.pf = tb + (uint32_t)((kq & 1) ? T_4 + 2 * (63 - jj) : T_3 + 2 * (127 - 2 * jj));
  t.pg = t.pf - ((kq & 1) ? 64 : 2);
  t.pe = tb + (uint32_t)(p4 * (TDP + 2) + ((lane < 12) ? 2 * (32 - q4) : 2 * (572 + (q4 & 1))));
  asm volatile("" : "+v"(t.xa), "+v"(t.xb), "+v"(t.ya), "+v"(t.pd), "+v"(t.pf), "+v"(t.pg), "+v"(t.pe));
}
typedef __attribute__((address_space(3))) char* lptr;
#define LP(T, a) ((__attribute__((address_space(3))) T*)(uintptr_t)(a))
__device__ __forceinline__ uint32_t table_tile(const TOps& t, int ct, int lane, uint32_t evp, float dsk TSP) {
  u32x4 ev;
  f32x4 ad[2], af[2];
  asm volatile(T_ASM : "=&v"(ad[0]), "=&v"(ad[1]), "=&v"(af[0]), "=&v"(af[1]), "=&v"(ev) : "v"(t.xa), "v"(t.xb), "v"(t.ya), "v"(evp) : T_CLOB);
  TS(0, 4);
  const int e0 = (int)(ev[0] + ev[2]), e1 = (int)(ev[1] + ev[3]);
  const int em = min(max(max(max(e0, e1), (int)((__float_as_uint(dsk) >> 23) & 0xffu) - 133), -110), 115);
  const int ts = ((uint32_t)(em + 12) <= 8u) ? 1 : -3 - em;
  float s0 = __builtin_amdgcn_ldexpf(1.f, e0 - 29 + ts), s1 = __builtin_amdgcn_ldexpf(1.f, e1 - 29 + ts);
  float d0 = (ct == 0 && lane == 0) ? __builtin_amdgcn_ldexpf(dsk, ts) : 0.f;
  asm volatile("s_nop 1" : "+v"(ad[0]), "+v"(ad[1]), "+v"(af[0]), "+v"(af[1]), "+v"(s0), "+v"(s1), "+v"(d0));
  const f32x4 k = ad[0] * s0 + (f32x4){d0, 0.f, 0.f, 0.f} + ad[1] * s1, f = af[0] * s0 + af[1] * s1;
  auto pk = [](float a, float b) { uint32_t r; asm("v_cvt_pk_f16_f32 %0, %1, %2" : "=v"(r) : "v"(a), "v"(b)); return r; };
  asm volatile("s_nop 0" : : "v"(k), "v"(f));
  const uint32_t p32 = pk(k[3], k[2]), p10 = pk(k[1], k[0]), p21 = pk(k[2], k[1]), p01 = pk(f[0], f[1]), p23 = pk(f[2], f[3]);
  *LP(u32x2, t.pd + 3 * TDP + 6) = (u32x2){p32, p10};
  *LP(uint32_t, t.pd + TDP + 2) = p32; *LP(uint32_t, t.pd + TDP + 6) = p10;
  *LP(uint32_t, t.pd + 2) = p21; *LP(uint32_t, t.pd + 2 * TDP + 6) = p21;
  *LP(u16, t.pd) = (u16)p32; *LP(u16, t.pd + 2 * TDP + 4) = (u16)p32;
  asm volatile("ds_write_b16_d16_hi %0, %1 offset:6\n\tds_write_b16_d16_hi %0, %1 offset:%2" : : "v"(t.pd), "v"(p10), "n"(2 * TDP + 10) : "memory");
  if (ct) {
    const uint32_t va = (uint32_t)__builtin_amdgcn_readlane((int)p32, 63), vb = (uint32_t)__builtin_amdgcn_readlane((int)p23, 15) >> 16;
    *LP(u16, t.pe) = (u16)((lane < 12) ? va : vb);
  }
  if (lane < 32) {
    *LP(u16, t.pf + 128) = (u16)p01; *LP(u16, t.pg + 128) = (u16)(p01 >> 16);
    *LP(u16, t.pf) = (u16)p23; *LP(u16, t.pg) = (u16)(p23 >> 16);
  }
  return (uint32_t)(127 + 5 - ts) << 23;
}
__device__ __forceinline__ uint32_t hadd(uint32_t a, uint32_t b) { return __builtin_bit_cast(uint32_t, __builtin_bit_cast(f16x2, a) + __builtin_bit_cast(f16x2, b)); }
template <bool FULL> __device__ __forceinline__ void sums(char* sm, const u32x4 p, const u32x4 c) {
  const uint32_t D[8] = {p[0], p[1], p[2], p[3], c[0], c[1], c[2], c[3]};
  uint32_t O[7];
#pragma unroll
  for (int j = 0; j < 7; ++j) O[j] = __builtin_amdgcn_alignbit(D[j + 1], D[j], 16);
  u32x4 a, b;
#pragma unroll
  for (int j = 0; j < 4; ++j) a[j] = hadd(D[j + 2], hadd(O[j + 1], O[j + 2]));
  *(u32x4*)(sm + C_G3 + 32) = a;
#pragma unroll
  for (int j = 0; j < 4; ++j) b[j] = hadd(a[j], hadd(D[j + 1], D[j + 3]));
  *(u32x4*)(sm + C_G5 + 32) = b;
  if (FULL) {
#pragma unroll
    for (int j = 0; j < 4; ++j) a[j] = hadd(b[j], hadd(O[j], O[j + 3]));
    *(u32x4*)(sm + C_G7 + 32) = a;
#pragma unroll
    for (int j = 0; j < 4; ++j) b[j] = hadd(a[j], hadd(D[j], D[j + 4]));
    *(u32x4*)(sm + C_G9 + 32) = b;
  }
}
__device__ __forceinline__ void count_up(uint32_t a) {
  uint32_t t;
  asm volatile("s_mov_b64 exec, 1\n\tv_mov_b32 %0, 1\n\tds_add_u32 %1, %0\n\ts_mov_b64 exec, -1" : "=&v"(t) : "v"(a) : "memory");
}
__device__ __forceinline__ int pad_at(int r) { return 16 * (r & 1) + 3200 * (r >> 1) - ((r >= 6) ? 3200 - 2096 : 0); }
struct COps { uint32_t b, a, o, g3, k3, g4, k4, f; };
__device__ __forceinline__ void conv_ops(uint32_t tb, uint32_t ub, uint32_t cb, int lane, COps& e) {
  const uint32_t j = lane & 15, kq = lane >> 4, q4 = (lane >> 2) & 3, p4 = lane & 3;
  e.b = ub + 32u * j + 16u * kq;
  e.a = tb + p4 * TDP + 32u - 8u * q4 + 16u * kq;
  e.g3 = cb + C_G5 + 8 + 64u * kq + 8u * (q4 + p4) + (q4 & 1u) * 3200u;
  e.g4 = cb + C_G9 + 8 + 128u * kq + 16u * q4 + 8u * p4 + (q4 & 1u) * 2096u;
  e.k3 = tb + T_3 + 120 - 8u * j + 16u * kq;
  e.k4 = tb + T_4 + 60 - 4u * j + 16u * kq;
  asm volatile("" : "+v"(e.b), "+v"(e.a), "+v"(e.g3), "+v"(e.g4), "+v"(e.k3), "+v"(e.k4));
}
#define CONV_RUN(prog, e, ...) do { int prog_ = (prog); \
    asm volatile("s_call_b64 s[90:91], kf_conv" : "+{s89}"(prog_), "+{v2}"(e.b), "+{v3}"(e.a), "+{v4}"(e.o), "+{v5}"(e.g3), "+{v6}"(e.k3), "+{v7}"(e.g4), \
                 "+{v8}"(e.k4), "+{v9}"(e.f) __VA_ARGS__ : : CONV_CLOB); } while (0)

template <bool E>
__device__ __forceinline__ void ln_stats(const uint32_t* v, int lane, float sc, f32x2* t, f32x4 z, f32x2& rc, f32x2& nc, float& e) {
  float s1a = z[0], s2a = z[1], s1b = z[2], s2b = z[3];
#pragma unroll
  for (int k = 0; k < 4; ++k) {
    s1a = dot2(v[k], 0x3f803f80u, s1a); s2a = dot2(v[k], v[k], s2a);
    s1b = dot2(v[4 + k], 0x3f803f80u, s1b); s2b = dot2(v[4 + k], v[4 + k], s2b);
  }
#pragma unroll
  for (int k = 0; k < 9; ++k) t[k] = (f32x2){bflo(v[k]), bfhi(v[k])};
  const float rs = row_sum(fold16(dot2(v[8], 0x3f803f80u, fold32(s1a, s1b)), dot2(v[8], v[8], fold32(s2a, s2b))));
  if (E) { e = row_sum(e); e += __int_as_float(__builtin_amdgcn_update_dpp(0, __float_as_int(e), 0x142, 0xa, 0xf, false)); }
  const f32x2 mean = (f32x2){rdl(rs, 15), rdl(rs, 47)} * (1.f / D_), m2 = mean * mean;
  f32x2 var = (f32x2){rdl(rs, 31), rdl(rs, 63)} * (1.f / D_) - m2;
  asm volatile("" : "+v"(t[0]), "+v"(t[1]), "+v"(t[2]), "+v"(t[3]), "+v"(t[4]), "+v"(t[5]), "+v"(t[6]), "+v"(t[7]), "+v"(t[8]));
  if (__builtin_expect(!(m2[0] <= 16.f * var[0] && m2[1] <= 16.f * var[1]), 0)) {
    float q[3] = {0.f, 0.f, 0.f};
#pragma unroll
    for (int k = 0; k < 9; ++k) {
      const f32x2 a = t[k] - ((k < 4 || (k == 8 && lane < 32)) ? mean[0] : mean[1]);
      q[k >> 2] += a[0] * a[0] + a[1] * a[1];
    }
    float qa = q[0] + ((lane < 32) ? q[2] : 0.f), qb = q[1] + ((lane < 32) ? 0.f : q[2]);
    wave_sum2(qa, qb);
    var = (f32x2){qa, qb} * (1.f / D_);
  }
  const uint32_t mp = pack2(mean[0], mean[1]), vp = pack2(var[0], var[1]);
  const f32x2 ve = (f32x2){bflo(vp), bfhi(vp)} + 1e-5f;
  const uint32_t e2 = pack2(ve[0], ve[1]);
  const uint32_t sp = pack2(__builtin_amdgcn_sqrtf(bflo(e2)), __builtin_amdgcn_sqrtf(bfhi(e2)));
  rc = (f32x2){__builtin_amdgcn_rcpf(bflo(sp)), __builtin_amdgcn_rcpf(bfhi(sp))} * sc;
  nc = -(f32x2){bflo(mp), bfhi(mp)} * rc;
}
template <bool H>
__device__ __forceinline__ void ln_apply(const f32x2* t, int lane, f32x2 rc, f32x2 nc, uint32_t* c) {
  const float nm[3] = {nc[0], nc[1], (lane < 32) ? nc[0] : nc[1]}, rr[3] = {rc[0], rc[1], (lane < 32) ? rc[0] : rc[1]};
  f32x2 dd[9];
#pragma unroll
  for (int k = 0; k < 9; ++k) dd[k] = __builtin_elementwise_fma(t[k], (f32x2){rr[k >> 2], rr[k >> 2]}, (f32x2){nm[k >> 2], nm[k >> 2]});
  asm volatile("" : "+v"(dd[0]), "+v"(dd[1]), "+v"(dd[2]), "+v"(dd[3]), "+v"(dd[4]), "+v"(dd[5]), "+v"(dd[6]), "+v"(dd[7]), "+v"(dd[8]));
#pragma unroll
  for (int k = 0; k < 9; ++k) {
    const f32x2 d = dd[k];
    typedef __attribute__((ext_vector_type(2))) uint16_t u16x2;
    if (H) c[k] = __builtin_bit_cast(uint32_t, __builtin_elementwise_add_sat(__builtin_bit_cast(u16x2, __builtin_amdgcn_cvt_pkrtz(d[0], d[1])), (u16x2){4, 4}));
    else c[k] = pack2(d[0], d[1]);
  }
}
__device__ __forceinline__ uint32_t med3u(uint32_t a, uint32_t b, uint32_t c) { return max(min(a, b), min(max(a, b), c)); }
__device__ __forceinline__ uint32_t oct_maxu(uint32_t v) {
  v = max(v, (uint32_t)DPP_I((int)v, 0xb1, 0xf));
  v = max(v, (uint32_t)DPP_I((int)v, 0x4e, 0xf));
  v = max(v, (uint32_t)DPP_I((int)v, 0x141, 0xf));
  return v;
}

#define KARGS const u16* __restrict__ x, const u16* __restrict__ Are, const u16* __restrict__ Aim, const u16* __restrict__ Bre, \
    const u16* __restrict__ Bim, const u16* __restrict__ Cre, const u16* __restrict__ Cim, const u16* __restrict__ logdt, \
    const u16* __restrict__ Dskip, char* __restrict__ ws, const u16* __restrict__ wr, const u16* __restrict__ wg, \
    const u16* __restrict__ wu, const u16* __restrict__ wd, u16* __restrict__ out
#define KPASS x, Are, Aim, Bre, Bim, Cre, Cim, logdt, Dskip, ws, wr, wg, wu, wd, out
template <int ROLE, int FFN = -1>
__device__ __forceinline__ void role_body(char* smem, KARGS) {
  const int tid = threadIdx.x, lane = tid & 63;
  const int w = __builtin_amdgcn_readfirstlane(tid >> 6), wl = w & 3;
  const int b = blockIdx.x & 255, bx = b & 7, bj = b >> 3;
  constexpr bool isl = ROLE == 1 || ROLE == 2;
  const int tg = isl ? 32 * (bj >> 2) + 4 * bx + (bj & 3) : 32 * bx + bj;
  u32x4 xa, xc;
  uint32_t ep, r0;
#define P_WAIT(n, ...) asm volatile("s_waitcnt vmcnt(" #n ")\n\tv_lshlrev_b32 %0, 16, v100\n\tv_lshlrev_b32 %1, 16, v101\n\tv_lshlrev_b32 %2, 16, v102" \
    : "=v"(ldt), "=v"(are), "=v"(aim) : : "v100", "v101", "v102" __VA_ARGS__)
#define P_MORE() asm volatile("v_lshlrev_b32 %0, 16, v103\n\tv_lshlrev_b32 %1, 16, v104\n\tv_lshlrev_b32 %2, 16, v105\n\tv_lshlrev_b32 %3, 16, v106" \
    : "=v"(bre), "=v"(bim), "=v"(cre), "=v"(cim) : : "v103", "v104", "v105", "v106")
  TSD;
  TS(0, 0);
  const uint32_t lb0 = (uint32_t)(uintptr_t)(__attribute__((address_space(3))) char*)smem;
  const int i = lane & 15, kq = lane >> 4;
  const u32x4 ones = {0xffffffffu, 0xffffffffu, 0xffffffffu, 0xffffffffu}, zero4 = {0u, 0u, 0u, 0u};
  auto u_again = [&](u32x4& uw, const u32x4& ut, uint32_t uo) {
#pragma unroll 1
    for (int spin = 0; spin < SPIN_CAP; ++spin) {
      const bool na = ((uw[0] | uw[1] | uw[2] | uw[3]) & UMASK) != 0u;
      if (__builtin_expect(!__any(na), 1)) break;
      TSC(19);
      SLEEP(USLEEP);
      if (na) { LD16W(uw, uo); uw ^= ut; }
    }
  };
  constexpr uint32_t S16 = 0x41800000u, LMASK = 0x00070007u;
  auto v_put = [&](u32x4 v, const u32x4& ut, bool other, uint32_t vo) {
    const bool second = __any(other || ((v[0] | v[1] | v[2] | v[3]) & 0x40004000u) != 0u);
    if (__builtin_expect(second, 0)) v = (v & ~LMASK) | (~ut & LMASK); else v = (v & ~LMASK) | ut;
    ST16(vo, v);
    return second;
  };
  auto v_end = [&](uint32_t T, int n) { DRAIN(); if (lane == 0) ST4(O_DN + 16u * n, T); };
  if constexpr (!isl) {
    const int ch = wl >> 1, tl = tid & 255;
    int* evp = (int*)(smem + O_FL + 16 * ch);
    float ldt, are, aim, dsk;
    if constexpr (ROLE == 0) {
      __builtin_amdgcn_s_setprio(KPRIO);
      COps e;
      conv_ops(lb0 + (uint32_t)(ch * TB), lb0 + (uint32_t)(O_UL + ch * UB), lb0 + (uint32_t)(O_IM + ch * GB), lane, e);
      e.o = lb0 + O_ST + (uint32_t)(16 * ((4 * i + 2 * (kq >> 1) + ch) ^ (i >> 1)) + 8 * (kq & 1));
      e.f = lb0 + O_FL;
      TOps t;
      table_ops(lb0 + (uint32_t)(O_IM + 128 * IP * ch), lb0 + (uint32_t)(TB * ch), wl & 1, lane, t);
      uint32_t pz = lb0 + O_IM + (uint32_t)(((lane >> 3) & 1) * GB + pad_at(lane & 7)), im = lb0 + (uint32_t)(O_IM + IP * tl);
      asm volatile("" : "+v"(e.o), "+v"(e.f), "+v"(pz), "+v"(im));
      float bre, bim, cre, cim;
      P_WAIT(0, , "v103", "v104", "v105", "v106", "v107", "v109");
      asm volatile("" : "={v109}"(ep));
      const uint32_t T = ((uint32_t)__builtin_amdgcn_readfirstlane(ep) + 1u) * 0x9E3779B1u;
      if (w == 3 && lane < 8) ST16(O_KEY + 128u * b + 16u * lane, ones);
      P_MORE();
      asm volatile("v_lshlrev_b32 %0, 16, v107" : "=v"(dsk) : : "v107");
      TS(0, 1);
      evp[wl & 1] = gen_y(ldt, are, aim, bre, bim, cre, cim, im, [] {} TSA);
      TS(0, 8);
      BAR();
      TS(0, 2);
      const uint32_t cv = table_tile(t, wl & 1, lane, lb0 + O_FL + 16 * ch, dsk TSA), cs = (uint32_t)__builtin_amdgcn_readfirstlane(cv);
      *(uint32_t*)(smem + O_FL + 40 + 4 * ch) = cv;
      TS(0, 9);
      gsync0(lb0 + O_FL + 36, 4);
      TS(0, 3);
      *(u32x4*)(smem + O_ST + 16 * tl) = ones;
      *(u32x4*)(smem + O_ST + 4096 + 16 * tl) = ones;
      if (w == 0) *LP(u32x4, pz) = zero4;
      DRAIN();
      CONV_RUN(wl & 1, e);
      if (wl & 1) asm volatile("s_mov_b64 exec, 1\n\tglobal_store_dwordx2 %0, %1, %2 sc1" : : "v"(O_SC + 16u * (2 * tg + ch)), "v"((u32x2){T, cs}), "s"(ws) : "memory");
      TS(0, 5);
    } else {
      __builtin_amdgcn_s_setprio(UPRIO);
      const uint32_t ck0 = (uint32_t)__builtin_amdgcn_s_memtime(), rt0 = (uint32_t)__builtin_amdgcn_s_memrealtime();
#pragma unroll
      for (int k = 0; k < 3; ++k) *(u32x4*)(smem + ((16 * (tl + 256 * k) < O_FL) ? 16 * (tl + 256 * k) : O_FL + 32)) = zero4;
      if (w == 7) *(u32x4*)(smem + O_UL + (lane >> 5) * UB + 16 * min(lane & 31, 29)) = zero4;
      uint32_t uo = u_at(tl >> 1, 2 * tg + (tid & 1)), go = O_SU + (uint32_t)(256 * bx + 32 * wl + (lane & 31) + 4 * (lane & 32)) * 128u;
      uint32_t ul = (uint32_t)(O_UL + (tid & 1) * UB + C_U - 16 + 16 * (tl >> 1)), ug = (uint32_t)(O_IM + (tid & 1) * GB + 16 * (tl >> 1)), so = lb0 + O_ST + 16u * (uint32_t)(lane ^ (lane >> 3));
      uint32_t vo = O_V + (uint32_t)tg * VP + 16u * lane;
      asm volatile("" : "+v"(uo), "+v"(go), "+v"(ul), "+v"(ug), "+v"(so), "+v"(vo));
      P_WAIT(1, , "v109");
      TS(0, 1);
      evp[2 + (wl & 1)] = gen_x(ldt, are, aim, smem + O_IM + IP * tl);
      asm volatile("s_waitcnt vmcnt(0)" : "={v109}"(ep));
      const uint32_t T = ((uint32_t)__builtin_amdgcn_readfirstlane(ep) + 1u) * 0x9E3779B1u;
      TS(0, 8);
      BAR();
      TS(0, 2);
      if (w == 7 && bj >= 24) {
        const uint32_t t_ = ahead(ck0, rt0, VDB);
        ST16(O_SB + (uint32_t)(64 * (lane >> 3) + 8 * bx + bj - 24) * 128u + 16u * (lane & 7), ((u32x4){T, t_, T, t_}));
      }
      u32x4 ut = {utag(T, 0), utag(T, 1), utag(T, 2), utag(T, 3)};
      asm volatile("" : "+v"(ut));
      SLEEP(GSLEEP);
      u32x2 pv;
      pv[1] = gate(ws, go, T);
      TS(0, 3);
      until(latest(pv[1]) + UD);
      TS(0, 7);
      u32x4 uw, ux;
      asm volatile("global_load_dwordx4 %0, %2, %4 sc1\n\tglobal_load_dwordx4 %1, %3, %4 sc1"
                   : "=&v"(uw), "=&v"(ux) : "v"(uo), "v"(uo + 64 * UP * 32), "s"(ws) : "memory");
      __builtin_amdgcn_s_setprio(FPR);
      asm volatile("s_waitcnt vmcnt(1)" : "+v"(uw));
      uw ^= ut;
      *(u32x4*)(smem + ul + 16) = uw;
      asm volatile("s_waitcnt vmcnt(0)" : "+v"(ux));
      TS(0, 11);
      ux ^= ut;
      *(u32x4*)(smem + ul + 2048 + 16) = ux;
      if (__builtin_expect(__any(((uw[0] | uw[1] | uw[2] | uw[3] | ux[0] | ux[1] | ux[2] | ux[3]) & UMASK) != 0u), 0)) {
        u_again(uw, ut, uo);
        u_again(ux, ut, uo + 64 * UP * 32);
        *(u32x4*)(smem + ul + 16) = uw;
        *(u32x4*)(smem + ul + 2048 + 16) = ux;
      }
      BAR();
      TS(0, 4);
      const u32x4 pw = *(const u32x4*)(smem + ul);
      if (wl < 2) {
        const u32x4 px = *(const u32x4*)(smem + ul + 2048);
        sums<true>(smem + ug, pw, uw);
        sums<false>(smem + ug + 2048, px, ux);
      } else sums<true>(smem + ug, pw, uw);
      count_up(lb0 + O_FL + 32);
      __builtin_amdgcn_s_setprio(SPR);
      TS(0, 5);
      const bool other = *(const uint32_t*)(smem + O_FL + 40 + 4 * (lane & 1)) != S16;
      bool second = false;
#pragma unroll
      for (int k = 0; k < 2; ++k) {
        const uint32_t o = ((k ? 0x3247u : 0x1065u) >> (4 * wl) & 7u) * 1024u;
        u32x4 v;
#pragma unroll 1
        for (int spin = 0; spin < SPIN_CAP; ++spin) {
          asm volatile("ds_read_b128 %0, %1\n\ts_waitcnt lgkmcnt(0)" : "=&v"(v) : "v"(so + o) : "memory");
          if (!__any(max4(v) == 0xffffffffu)) break;
          __builtin_amdgcn_s_sleep(VPS);
        }
        TS(0, 12 + k);
        second |= v_put(v, ut, other, vo + o);
        TS(0, 10 - 4 * k);
      }
      if (__builtin_expect(second, 0)) v_end(T, 4 * tg + wl);
    }
    TSE();
  } else {
  uint32_t* cand = (uint32_t*)(smem + LM);
  uint32_t* sel = cand + 128;
  uint32_t* kl = sel + 48;
  uint32_t* cnt = sel + 64;
  const uint32_t ca = lb0 + LM + 4 * (128 + 64);
  u16* hm = (u16*)(cnt + 16);
  uint32_t T;
  const bool ffn = (FFN < 0) ? bj < 18 : FFN, isx = FFN != 1 && bj >= 24;
  const int fb = 18 * bx + bj;
  constexpr bool ln2 = ROLE == 2;
  const int g0 = tg, g2 = tg;
  u32x4 bw[18];
#pragma unroll
  for (int s = 0; s < 9; ++s) bw[s] = (u32x4){0u, 0u, 0u, 0u};
  int tid2 = threadIdx.x;
  uint32_t tv = 0u, tk = 0u;
  auto v_look = [&](uint32_t tu, int lane) {
    uint32_t tm = latest(gate(ws, O_SB + (uint32_t)(64 * bx + lane) * 128u, T)) - V2D;
    if ((int)(tm - (tu + UD + VD2)) < 0) tm = tu + UD + VD2;
    cnt[8] = tm;
    count_up(ca + 52);
  };
  auto v_time = [&] { gwait(ca + 52, 1); tv = (uint32_t)__builtin_amdgcn_readfirstlane(cnt[8]); };
  uint32_t ts1 = 0u;
  auto rest = [&] {
    if (ts1 && (int)((uint32_t)__builtin_amdgcn_s_memrealtime() - ts1) > 0) {
      v_time();
      ts1 = (tv - TSTOP) | 1u;
      if ((int)((uint32_t)__builtin_amdgcn_s_memrealtime() - ts1) > 0) { TS(0, 8); until(tv + RD); ts1 = 0u; }
    }
  };
  auto gu_dma = [&](int q0, int q1, auto depth) {
    const char* gs = (const char*)((w < 2) ? wg : wu) + (size_t)((144 * (w & 1)) * (F_ * 2) + fb * 64);
    const uint32_t gv = (uint32_t)((lane >> 2) * (F_ * 2) + 16 * ((lane & 3) ^ (lane >> 5) ^ (w & 2)));
#pragma unroll 1
    for (int q = q0; q < q1; ++q) {
      const int k = (int)((((bj & 1) ? ORD1 : ((unsigned __int128)0x876543210u << 36 | 0x876543210ull)) >> (4 * (q - (q >= 9 ? 9 : 0)) + (q >= 9 ? 36 : 0))) & 15u), h = q >= 9;
      if (q >= RQ) rest();
      dma16(gs + (size_t)(288 * h + 16 * k) * (F_ * 2), gv, lb0 + LW * h + 9216 * w + 1024 * k);
      if (!BALL || q < RQ || ts1) asm volatile("s_waitcnt vmcnt(%0)" : : "n"(depth.value) : "memory");
    }
  };
  auto down_dma = [&](int k0, int k1, int lane) {
#pragma unroll 1
    for (int k = k0; k < k1; ++k) {
      const int o = 9216 * wl + 2304 * k;
      const char* ds = (const char*)wd + (size_t)(fb * 36864 + o);
      const uint32_t dl = lb0 + o + 32 * ((4 * wl + k + 1) >> 1);
      if (ROLE == 1) rest();
      dma16d(ds, 16u * lane, dl);
      dma16d(ds + 1024, 16u * lane, dl + 1024);
      uint32_t keep;
      asm volatile("s_mov_b32 %0, m0\n\ts_mov_b32 m0, %3\n\ts_mov_b64 exec, 0xffff\n\tglobal_load_lds_dwordx4 %1, %2" DPOL2 "\n\ts_mov_b64 exec, -1\n\ts_mov_b32 m0, %0\n\ts_waitcnt vmcnt(%4)"
                   : "=&s"(keep) : "v"(16u * lane), "s"(ds + 2048), "s"(dl + 2048), "n"(ROLE == 1 ? DDEPTH : 40) : "memory");
    }
  };
  auto gu_read = [&](int h) {
    const uint32_t ra = lb0 + LW * h + (uint32_t)(((lane >> 1) & 1) * 18432 + (8 * kq + ((lane >> 2) & 3)) * 64 + ((w ^ (kq & 1) ^ (lane & 2)) * 16) + 8 * (lane & 1));
#pragma unroll
    for (int s = 0; s < 9; ++s) {
      const u32x2 h0 = trd(ra + 2048 * s), h1 = trd(ra + 2048 * s + 256);
      bw[9 * h + s] = (u32x4){h0[0], h0[1], h1[0], h1[1]};
    }
#pragma unroll
    for (int s = 0; s < 9; ++s) asm volatile("" : "+v"(bw[9 * h + s]));
  };
  const uint32_t vearly = (uint32_t)((VEARLY >> (8 * (g0 >> 5))) & 0xffull);
#define TOUCH(pa) asm volatile("global_load_dword v126, %0, off" DPOL4 : : "v"(pa) : "memory", "v126")
  uint32_t wv[5], xv[9];
  auto rows_xw = [&](int lane) {
    u32x4 wa, xr, xs;
    const uint32_t xo = (uint32_t)((8 * g2 + 2 * wl) * (D_ * 2));
    asm volatile("global_load_dwordx4 %0, %5, %9\n\tglobal_load_dword %1, %6, %9 offset:1024\n\t"
                 "global_load_dwordx4 %2, %7, %10\n\tglobal_load_dwordx4 %3, %7, %10 offset:1152\n\tglobal_load_dword %4, %8, %10 offset:1024\n\ts_waitcnt vmcnt(0)"
                 : "=&v"(wa), "=&v"(wv[4]), "=&v"(xr), "=&v"(xs), "=&v"(xv[8])
                 : "v"(16u * lane), "v"(4u * (lane & 31)), "v"(xo + 16u * lane), "v"(xo + 4u * lane + 32u * (lane & 32)), "s"(wr), "s"(x) : "memory");
#pragma unroll
    for (int k = 0; k < 4; ++k) { wv[k] = wa[k]; xv[k] = xr[k]; xv[4 + k] = xs[k]; }
  };
  auto ln1 = [&](int g, int o, uint32_t c0) {
    uint32_t uo = u_at(g, 64 * wl + lane);
    uint32_t ta = lb0 + (uint32_t)(o + ((lane >> 2) & 3) * RP + (64 * wl + 16 * kq + 4 * (lane & 3)) * 2);
    uint32_t la = (uint32_t)(o + 2 * wl * RP + 16 * lane), lt = (uint32_t)(o + (2 * wl + (lane >> 5)) * RP + 1024 + 4 * (lane & 31));
    uint32_t sg = O_SU + (uint32_t)(256 * (lane >> 3) + g) * 128u + 16u * (lane & 7);
    asm volatile("" : "+v"(uo), "+v"(ta), "+v"(la), "+v"(lt), "+v"(sg));
    f32x4 z4 = {0.f, 0.f, 0.f, 0.f};
    asm volatile("" : "+v"(z4));
    asm volatile("s_waitcnt vmcnt(0)" : "={v[100:103]}"(xa), "={v[104:107]}"(xc), "={v108}"(r0), "={v109}"(ep));
    TS(0, 1);
    const uint32_t v[9] = {xa[0], xa[1], xa[2], xa[3], xc[0], xc[1], xc[2], xc[3], r0};
    uint32_t c[9];
    f32x2 tf[9];
    T = ((uint32_t)__builtin_amdgcn_readfirstlane(ep) + 1u) * 0x9E3779B1u;
    if (wl == 3) { const uint32_t now = (uint32_t)__builtin_amdgcn_s_memrealtime() + SD; ST16(sg, ((u32x4){T, now, T, now})); }
    const uint32_t tj = ((T >> (3 * wl)) & 0x00070007u) | (((T >> (12 + wl)) & 0x00010001u) << 14);
    uint32_t tw[3] = {(tj & 0xffffu) * 0x10001u, (tj >> 16) * 0x10001u, 0u};
    tw[2] = (lane < 32) ? tw[0] : tw[1];
    asm volatile("" : "+v"(tw[0]), "+v"(tw[1]), "+v"(tw[2]));
    f32x2 rc, nc;
    float e_;
    ln_stats<false>(v, lane, 0.03125f, tf, z4, rc, nc, e_);
    ln_apply<true>(tf, lane, rc, nc, c);
#pragma unroll
    for (int k = 0; k < 9; ++k) c[k] = (c[k] & ~UMASK) | tw[k >> 2];
    *(u32x4*)(smem + la) = (u32x4){c[0], c[1], c[2], c[3]};
    *(u32x4*)(smem + la + RP) = (u32x4){c[4], c[5], c[6], c[7]};
    *(uint32_t*)(smem + lt) = c[8];
    u32x4 ha, hb;
    {
      uint32_t t, cc, n = SPIN_CAP;
      asm volatile("s_mov_b64 exec, 1\n\tv_mov_b32 %2, 1\n\tds_add_u32 %5, %2\n\ts_mov_b64 exec, -1\n"
                   "1:\n\tds_read_b32 %2, %5\n\tds_read_b64_tr_b16 v[40:41], %6\n\tds_read_b64_tr_b16 v[42:43], %6 offset:%7\n\tds_read_b64_tr_b16 v[44:45], %6 offset:512\n\t"
                   "ds_read_b64_tr_b16 v[46:47], %6 offset:%8\n\ts_sub_u32 %4, %4, 1\n\ts_cbranch_scc1 2f\n\ts_waitcnt lgkmcnt(4)\n\tv_readfirstlane_b32 %3, %2\n\t"
                   "s_cmp_lt_u32 %3, 4\n\ts_cbranch_scc1 1b\n2:\n\ts_waitcnt lgkmcnt(0)"
                   : "=&{v[40:43]}"(ha), "=&{v[44:47]}"(hb), "=&v"(t), "=&s"(cc), "+s"(n) : "v"(c0), "v"(ta), "n"(4 * RP), "n"(512 + 4 * RP) : "memory", "scc");
    }
    ST16(uo, ha);
    ST16(uo + 64 * 128, hb);
    if (wl == 0) {
      const u32x2 h4 = trd(ta + 1024), h5 = trd(ta + 1024 + 4 * RP);
      ST16(uo + 128 * 128, ((u32x4){h4[0], h4[1], h5[0], h5[1]}));
    }
  };
  const int dX = 512 - 24 + 8 * bx + bj;
  if constexpr (ROLE == 2) {
    const int tl = tid - 256;
    if (isx) {
      __builtin_amdgcn_s_setprio(QPRIO0);
      *(u32x4*)(smem + X_TB + 16 * tl) = zero4;
      *(u32x4*)(smem + X_TB + 16 * min(256 + tl, (TB + C_U) / 16 - 1)) = zero4;
      const int pr = (wl + XROT) & 3;
      COps e;
      conv_ops(lb0 + X_TB, lb0 + X_UL, lb0 + X_IM, lane, e);
      TOps t;
      table_ops(lb0 + X_IM, lb0 + X_TB, wl & 1, lane, t);
      e.o = lb0 + X_ST + (uint32_t)(16 * ((2 * i + (kq >> 1)) ^ ((i >> 2) & 1)) + 8 * (kq & 1));
      e.f = ca + 28 - 32;
      asm volatile("" : "+v"(e.o), "+v"(e.f));
      float ldt, are, aim, bre, bim, cre, cim, dsk;
      P_WAIT(0, , "v103", "v104", "v105", "v106", "v107", "v109");
      asm volatile("" : "={v109}"(ep));
      T = ((uint32_t)__builtin_amdgcn_readfirstlane(ep) + 1u) * 0x9E3779B1u;
      P_MORE();
      asm volatile("v_lshlrev_b32 %0, 16, v107" : "=v"(dsk) : : "v107");
      TS(0, 1);
      __builtin_amdgcn_s_setprio(QPRIO);
      int* evp = (int*)(sel + 44);
      char* im = smem + X_IM + IP * (tl & 127);
      evp[wl] = (wl < 2) ? gen_y(ldt, are, aim, bre, bim, cre, cim, lb0 + X_IM + IP * tl, [] {} TSA) : gen_x(ldt, are, aim, im);
      TS(0, 2);
      __builtin_amdgcn_s_setprio(QPRIO2);
      gsync(ca + 8, 4);
      if (wl < 2) evp[-4 + wl] = (int)table_tile(t, wl & 1, lane, lb0 + LM + 4 * (128 + 44), dsk TSA);
      if (wl < 2) gsync0(ca + 8, 8); else { count_up(ca + 8); SLEEP(XSL); gwait(ca + 8, 8); }
      const uint32_t cs = (uint32_t)__builtin_amdgcn_readfirstlane(evp[-4]);
      if (wl == 3) *(u32x4*)(smem + X_IM + pad_at(lane & 7)) = zero4;
      TS(0, 3);
      CONV_RUN(2 + pr, e, , "+{v11}"(tid2));
      __builtin_amdgcn_s_setprio(3);
      {
        const int lane = tid2 & 63;
        const u32x4 v = *(const u32x4*)(smem + X_ST + 2048 * pr + 1024 * (lane >> 5) + 16 * ((lane & 31) ^ ((lane >> 3) & 1)));
        if (__builtin_expect(v_put(v, (u32x4){utag(T, 0), utag(T, 1), utag(T, 2), utag(T, 3)}, cs != S16, v_at(dX, 64 * pr + lane)), 0)) {
          DRAIN();
          if (lane == 0) ST4(O_DN + 16u * (4 * (dX - 256) + pr), T);
        }
        if (pr == 0 && lane == 0) asm volatile("global_store_dwordx2 %0, %1, %2 sc1" : : "v"(O_SC + 16u * dX), "v"((u32x2){T, cs}), "s"(ws) : "memory");
      }
      TS(0, 6);
    }
    if (!isx) __builtin_amdgcn_s_setprio(PPRIO);
    asm volatile("" : "+v"(tid2));
    if (!isx) rows_xw(tid2 & 63);
    if (!isx && !ffn) {
      SLEEP(RSLEEP);
#pragma unroll
      for (int k = 0; k < 7; ++k) ST16(O_SL + (uint32_t)((6 * bx + bj - 18) * 1792 + 256 * k - 256 + tid2) * 16u, ones);
      DRAIN();
    }
    if (!isx) BAR();
    const uint32_t tp = (uint32_t)__builtin_amdgcn_readfirstlane(cnt[4]);
    if (!isx) T = (uint32_t)__builtin_amdgcn_readfirstlane(sel[43]);
    if (!isx && w == 4) v_look(tp, tid2 & 63);
    v_time();
  } else {
    __builtin_amdgcn_s_setprio(3);
    if (tid < 16) cnt[tid] = 0u;
    BAR();
    if (tid < 20) ((uint32_t*)(hm + 320))[tid] = 0u;
    if (tid >= 184) *(u32x4*)(smem + L_SC - 16 * 184 + 16 * tid) = (u32x4){0x41804180u, 0x41804180u, 0x41804180u, 0x41804180u};
    uint32_t sink = 0;
    ln1(tg, 0, ca + 12);
    TS(0, 2);
    __builtin_amdgcn_s_setprio(NPRIO);
    asm volatile("s_waitcnt vmcnt(0)" : "+v"(sink) : : "memory");
    TS(0, 13);
    if (w == 0) {
      const uint32_t xr = O_XC + (uint32_t)((1021 - 4 * g0) * 72 + lane) * 16u;
      ST16(xr, ones);
      ST16(xr + 1024u, ones);
      ST16(xr + 2048u, ones);
      if (lane < 24) ST16(xr + 3072u, ones);
    }
    if (w == 2 && b < 72 && lane < 8) ST16(O_FB + (uint32_t)(8 * b + lane) * 16u, ones);
    if (isx) {
      rows_xw(lane);
      char* sp = smem + X_RW(w) + 16 * lane;
      *(u32x4*)sp = (u32x4){xv[0], xv[1], xv[2], xv[3]};
      *(u32x4*)(sp + 1024) = (u32x4){xv[4], xv[5], xv[6], xv[7]};
      *(u32x2*)(smem + X_RW(w) + 2048 + 8 * lane) = (u32x2){xv[8], wv[4]};
      if (w == 0) *(u32x4*)(smem + X_WR + 16 * lane) = (u32x4){wv[0], wv[1], wv[2], wv[3]};
    }
    SLEEP(ESLEEP);
    uint32_t tu = latest(gate(ws, O_SU + (uint32_t)(256 * bx + (ALLP ? tid : 64 * w + 4 * i + (bj & 3))) * 128u, T));
    asm volatile("s_mov_b64 exec, 1\n\tds_max_u32 %0, %1\n\ts_mov_b64 exec, -1" : : "v"(ca + 16), "v"(tu) : "memory");
    if (w == 0) sel[43] = T;
    if (isx) {
      const uint32_t xo = u_at(tid, dX);
      char* sm = smem + X_UL + C_U - 16 + 16 * tid;
      const u32x4 ut = {utag(T, 0), utag(T, 1), utag(T, 2), utag(T, 3)};
      __builtin_amdgcn_s_setprio(3);
      until(tu + UD + XD);
      u32x4 uw;
      asm volatile("global_load_dwordx4 %0, %1, %2 sc1" : "=&v"(uw) : "v"(xo), "s"(ws) : "memory");
      asm volatile("s_waitcnt vmcnt(0)" : "+v"(uw));
      uw ^= ut;
      u_again(uw, ut, xo);
      *(u32x4*)(sm + 16) = uw;
      BAR();
      if (w < 3) {
        const u32x4 pw = *(const u32x4*)sm;
        if (w < 2) sums<true>(smem + X_IM + 16 * tid, pw, uw); else sums<false>(smem + X_IM + 16 * tid, pw, uw);
        count_up(ca + 28);
      }
    } else {
      until(tu + UD + WD);
      BAR();
    }
    tu = (uint32_t)__builtin_amdgcn_readfirstlane(cnt[4]);
    TS(0, 3);
    __builtin_amdgcn_s_setprio(DPRIO);
    if (ffn) {
      ts1 = (tu + UD + VD2 - TSTOP) | 1u;
      gu_dma(0, N0, IC<40>{});
      gu_dma(N0, NA, IC<WDEPTH>{});
      asm volatile("s_waitcnt vmcnt(%0)" : : "n"(NA - 9) : "memory");
      gsync0(ca + 4, 4);
      gu_read(0);
      asm volatile("s_waitcnt lgkmcnt(0)" ::: "memory");
      count_up(ca + 36);
      TS(0, 4);
      gu_dma(NA, 18, IC<WDEPTH2>{});
      gwait(ca + 36, 4);
      down_dma(0, KN, lane);
      asm volatile("s_waitcnt vmcnt(%0)" : : "n"(3 * KN) : "memory");
      count_up(ca + 4);
      if (STP && w == 1)
        asm volatile("global_load_dword v126, %0, %1 sc1" : : "v"(O_SC + 1024u * (fb >> 4) + 128u * (lane & 7)), "s"(ws) : "memory", "v126");
      TS(0, 7);
    } else {
      if (!isx && C3) {
        const int n = 6 * min(tid, 191) + bj - 18, r = n >> 2;
        const char* wb = (const char*)((n & 1) ? wu : wg) + bx * 1152;
        const int c0 = (3 - (int)((uintptr_t)wb >> 7)) & 7;
        wb += (uint32_t)((r + 144 * ((r >= 144) + ((n >> 1) & 1))) * (F_ * 2) + 128 * c0);
        TOUCH(wb);
        if (c0 == 0) TOUCH(wb + 1024);
      }
      auto d_line = [&](int k) {
        const int n = min((k * 14 + bj - 18) * 64 + lane, 5183), q = n / 1296, r = n - 1296 * q;
        return (const char*)wd + (uint32_t)(bx * 5184 + (r / 18) * 72 + 18 * (HREV ? 3 - q : q) + r % 18) * 128u;
      };
      if (isx && w == 3) v_look(tu, lane);
      int k = w;
      if (w < HW) {
#pragma unroll 1
        for (; k < 6 && (int)((uint32_t)__builtin_amdgcn_s_memrealtime() - (tu + UD + VD2 - HSTOP)) < 0; k += HW) { TOUCH(d_line(k)); DRAIN(); }
      }
      TS(0, 4);
      v_time();
      until(tv + RD2);
      if (w < HW) {
#pragma unroll 1
        for (; k < 6; k += HW) { TOUCH(d_line(k)); DRAIN(); }
      }
      TS(0, 7);
    }
    v_time();
    tk = tv + KD;
  }
  {
  const int tid = tid2, lane = tid & 63, i = lane & 15, kq = lane >> 4;
  __builtin_amdgcn_s_setprio(3);
  const int PL = ffn ? 6400 : LW + 6656;
  f32x2 xf[9];
  f32x4 za = {0.f, 0.f, 0.f, 0.f}, zb = za;
  uint32_t al, ha, h0a, h1a, so, da;
  auto ffn_terms = [&] {
    al = O_XC + 64u * (i >> 3) + 16u * kq + 128u * w;
    ha = (ROLE == 1) ? (uint32_t)((4 * (kq & 1) + 2 * (kq >> 1) + (i >> 3)) * 80 + 16 * w + 2 * (i & 7)) : 0u;
    h0a = (uint32_t)(min(i, 8) * 80 + 16 * kq); h1a = (uint32_t)(min(i ^ 8, 8) * 80 + 16 * kq);
    so = O_SL + (uint32_t)fb * SP + ((ROLE == 2) ? 16u * tid - 4096u : 8192u + 16u * lane);
    da = lb0 + (uint32_t)((8 * kq + ((lane >> 2) & 3)) * 1152 + 32 * ((8 * kq + ((lane >> 2) & 3) + 2) >> 2) + 8 * (lane & 3) + ((ROLE == 2) ? 128 * wl : 1024));
    if (ROLE == 2) asm volatile("" : "+v"(h0a), "+v"(h1a), "+v"(so), "+v"(da)); else asm volatile("" : "+v"(ha), "+v"(al), "+v"(da));
  };
  if (ROLE == 1) ffn_terms();
  uint32_t hv[9] = {}, nv[9] = {}, keya = 0u, keyb = 0u;
  int rka = 0, rkb = 0;
  auto put_row = [&](const uint32_t* r, int j, const void* base, uint32_t ro) {
    const u32x4 tl = {r[8], (uint32_t)DPP_I((int)r[8], 0x55, 0xf), (uint32_t)DPP_I((int)r[8], 0xaa, 0xf), (uint32_t)DPP_I((int)r[8], 0xff, 0xf)};
    asm volatile("global_store_dwordx4 %0, %1, %2 sc1\n\ts_nop 1" : : "v"(ro + 16u * lane), "v"((u32x4){r[4 * j], r[4 * j + 1], r[4 * j + 2], r[4 * j + 3]}), "s"(base) : "memory");
    if ((lane & 35) == 32 * j) asm volatile("global_store_dwordx4 %0, %1, %2 offset:1024 sc1\n\ts_nop 1" : : "v"(ro + 4u * (lane & 31)), "v"(tl), "s"(base) : "memory");
  };
  if (ln2) {
    const int tl = tid - 256, d1 = 64 * (tl >> 5) + (tl & 31), d3 = 64 * ((tl >> 4) & 7) + 32 + (tl & 15), d2 = (tl < 128) ? d3 + 16 : min(384 + tl, 575);
    uint32_t vo = v_at(d1, g0), vo2 = v_at(d2, g0), vo3 = v_at(d3, g0);
    u32x4 ut = {utag(T, 0), utag(T, 1), utag(T, 2), utag(T, 3)};
    u32x4 va, vc = ut, vb = ut;
    asm volatile("" : "+v"(vo), "+v"(vo2), "+v"(vo3), "+v"(ut), "+v"(vc), "+v"(vb));
    TS(0, 7);
    until(tv);
    TS(0, 8);
    asm volatile("global_load_dwordx4 %0, %2, %4 sc1\n\ts_cmp_lt_u32 %5, 2\n\ts_cbranch_scc1 1f\n\tglobal_load_dwordx4 %1, %3, %4 sc1\n1:"
                 : "=&v"(va), "+v"(vc) : "v"(vo), "v"(vo3), "s"(ws), "s"(wl) : "memory", "scc");
    until(tv + V2D);
    asm volatile("s_cmp_eq_u32 %3, 3\n\ts_cbranch_scc1 1f\n\tglobal_load_dwordx4 %0, %1, %2 sc1\n1:" : "+v"(vb) : "v"(vo2), "s"(ws), "s"(wl) : "memory", "scc");
    __builtin_amdgcn_sched_barrier(0);
    if (isx) {
      const u32x4 ra = *(const u32x4*)(smem + X_RW(wl) + 16 * lane), rb = *(const u32x4*)(smem + X_RW(wl) + 1024 + 16 * lane), rw = *(const u32x4*)(smem + X_WR + 16 * lane);
      const u32x2 rc = *(const u32x2*)(smem + X_RW(wl) + 2048 + 8 * lane);
#pragma unroll
      for (int k = 0; k < 4; ++k) { xv[k] = ra[k]; xv[4 + k] = rb[k]; wv[k] = rw[k]; }
      xv[8] = rc[0]; wv[4] = rc[1];
    }
    uint32_t pa = (uint32_t)(PL + 9216 * wl + 32 * lane), pb = (uint32_t)(PL + 9216 * wl + 2048 + 8 * (lane & 31)), ps = (lane < 32) ? 0u : 16u;
    uint32_t kid = (uint32_t)(((255 - g2) << 5) | (7 - 2 * wl - (lane >> 5)));
    uint32_t px = (uint32_t)(PL + 4 * d1), py = (uint32_t)(PL + 4 * d2), pz = (uint32_t)(PL + 4 * d3);
#pragma unroll
    for (int k = 0; k < 9; ++k) xf[k] = (f32x2){bflo(xv[k]), bfhi(xv[k])};
    asm volatile("" : "+v"(xf[0]), "+v"(xf[1]), "+v"(xf[2]), "+v"(xf[3]), "+v"(xf[4]), "+v"(xf[5]), "+v"(xf[6]), "+v"(xf[7]), "+v"(xf[8]));
    asm volatile("" : "+v"(za), "+v"(zb), "+v"(pa), "+v"(pb), "+v"(ps), "+v"(kid), "+v"(px), "+v"(py), "+v"(pz));
    float sv[10], ws_ = dot2(wv[4], 0x3f803f80u, 0.f) * 0.5f;
#pragma unroll
    for (int k = 0; k < 4; ++k) ws_ = dot2(wv[k], 0x3f803f80u, ws_);
    ws_ = wave_sum(ws_);
#pragma unroll
    for (int k = 0; k < 10; ++k) sv[k] = 16.f;
    uint32_t ka_ = lb0 + LM + 4u * (128 + 48 + 2 * wl + (lane >> 5)), kb_ = lb0 + LM + 4u * (128 + 48);
    uint32_t ko_ = O_KEY + (uint32_t)(256 * (lane & 7) + 8 * (g2 & 31) + (g2 >> 5)) * 16u;
    asm volatile("" : "+v"(ws_), "+v"(sv[0]), "+v"(sv[1]), "+v"(sv[2]), "+v"(sv[3]), "+v"(sv[4]), "+v"(sv[5]), "+v"(sv[6]), "+v"(sv[7]), "+v"(sv[8]), "+v"(sv[9]),
                 "+v"(ka_), "+v"(kb_), "+v"(ko_));
    if (ffn) gwait(ca + 36, 4);
    asm volatile("s_waitcnt vmcnt(1)\n\ts_cmp_eq_u32 %2, 3\n\ts_cbranch_scc0 1f\n\ts_waitcnt vmcnt(0)\n1:" : "+v"(va), "+v"(vc) : "s"(wl) : "memory", "scc");
    auto turn1 = [&] {
#pragma unroll
      for (int e = 0; e < 4; ++e) *(uint32_t*)(smem + px + 9216 * e) = va[e];
      if (wl >= 2) {
#pragma unroll
        for (int e = 0; e < 4; ++e) *(uint32_t*)(smem + pz + 9216 * e) = vc[e];
      }
    };
    auto turn = [&] {
      if (wl < 3) {
#pragma unroll
        for (int e = 0; e < 4; ++e) *(uint32_t*)(smem + py + 9216 * e) = vb[e];
      }
    };
    va ^= ut; vc ^= ut;
    turn1();
    asm volatile("s_waitcnt vmcnt(0)" : "+v"(vb) : : "memory");
    vb ^= ut;
    turn();
    const u32x4 vs = va | vb | vc;
    if (__builtin_expect(__any(((vs[0] | vs[1] | vs[2] | vs[3]) & UMASK) != 0u), 0)) {
#pragma unroll 1
      for (int spin = 0; spin < 4; ++spin) {
        const bool na = ((va[0] | va[1] | va[2] | va[3]) & UMASK) != 0u, nb = ((vb[0] | vb[1] | vb[2] | vb[3]) & UMASK) != 0u, nc = ((vc[0] | vc[1] | vc[2] | vc[3]) & UMASK) != 0u;
        if (!__any(na || nb || nc)) break;
        TSC(16);
        if (na) LD16(va, vo);
        if (nb) LD16(vb, vo2);
        if (nc) LD16(vc, vo3);
        asm volatile("s_waitcnt vmcnt(0)" : "+v"(va), "+v"(vb), "+v"(vc) : : "memory");
        if (na) va ^= ut;
        if (nb) vb ^= ut;
        if (nc) vc ^= ut;
      }
      auto again = [&](u32x4& v, uint32_t o, int d) {
        const int J = g0 >> 5;
        const uint32_t dn = O_DN + 16u * (uint32_t)((d < 512) ? 4 * (d >> 1) + ((0x01013232 >> (4 * J)) & 3) : 4 * (d - 256) + (J >> 1));
        bool pend = ((v[0] | v[1] | v[2] | v[3]) & UMASK) != 0u;
#pragma unroll 1
        for (int spin = 0; spin < SPIN_CAP && __any(pend); ++spin) {
          TSC(16);
          SLEEP(PSLEEP);
          if (pend) {
            uint32_t fin = ~T;
            if ((v[0] & v[1] & v[2] & v[3] & LMASK) == LMASK)
              asm volatile("global_load_dword %0, %1, %2 sc1\n\ts_waitcnt vmcnt(0)" : "=&v"(fin) : "v"(dn), "s"(ws) : "memory");
            LD16W(v, o);
            v ^= ut;
            pend = ((v[0] | v[1] | v[2] | v[3]) & UMASK) != 0u;
            if (pend && fin == T) {
              u32x2 sc;
              for (int k = 0; k < SPIN_CAP; ++k) {
                asm volatile("global_load_dwordx2 %0, %1, %2 sc1\n\ts_waitcnt vmcnt(0)" : "=&v"(sc) : "v"(O_SC + 16u * d), "s"(ws) : "memory");
                if (sc[0] == T) break;
              }
              *(u16*)(smem + L_SC + 2 * d) = (u16)(sc[1] >> 16);
              cnt[10] = 1u;
              v = (v ^ ut) & ~LMASK;
              pend = false;
            }
          }
        }
      };
      again(va, vo, d1); again(vc, vo3, d3); again(vb, vo2, d2);
      turn1();
      turn();
    }
    u32x4 a0, a1;
    u32x2 a2, cf;
    {
      uint32_t cc, n = SPIN_CAP;
      asm volatile("s_mov_b64 exec, 1\n\tv_mov_b32 v90, 1\n\tds_add_u32 %6, v90\n\ts_mov_b64 exec, -1\n"
                   "1:\n\tds_read2_b32 %3, %6 offset1:10\n\tds_read_b128 %0, %7\n\tds_read_b128 %1, %7 offset:16\n\tds_read_b64 %2, %8\n\t"
                   "s_sub_u32 %5, %5, 1\n\ts_cbranch_scc1 2f\n\ts_waitcnt lgkmcnt(3)\n\tv_readfirstlane_b32 %4, v90\n\t"
                   "s_cmp_lt_u32 %4, 4\n\ts_cbranch_scc1 1b\n2:\n\ts_waitcnt lgkmcnt(0)"
                   : "=&v"(a0), "=&v"(a1), "=&v"(a2), "=&{v[90:91]}"(cf), "=&s"(cc), "+s"(n) : "v"(ca), "v"(lb0 + pa), "v"(lb0 + pb) : "memory", "scc");
    }
    TS(0, 9);
    if (__builtin_expect(__builtin_amdgcn_readfirstlane(cf[1]) != 0, 0)) {
      const u32x4 q = *(const u32x4*)(smem + L_SC + 16 * lane);
      const uint32_t r = *(const uint32_t*)(smem + L_SC + 1024 + 4 * (lane & 31));
#pragma unroll
      for (int k = 0; k < 4; ++k) { sv[2 * k] = bflo(q[k]); sv[2 * k + 1] = bfhi(q[k]); }
      sv[8] = bflo(r); sv[9] = bfhi(r);
    }
    const uint32_t av[10] = {a0[0], a0[1], a0[2], a0[3], a1[0], a1[1], a1[2], a1[3], a2[0] >> ps, a2[1] >> ps};
    f32x2 h[9];
#pragma unroll
    for (int k = 0; k < 10; ++k) {
      asm("v_fma_mix_f32 %0, %1, %2, %3 op_sel_hi:[1,0,0]" : "=v"(h[(k < 8) ? k >> 1 : 8][k & 1]) : "v"(av[k]), "v"(sv[k]), "v"(xf[(k < 8) ? k >> 1 : 8][k & 1]));
      if (k < 8) asm("v_fma_mix_f32 %0, %1, %2, %3 op_sel:[1,0,0] op_sel_hi:[1,0,0]" : "=v"(h[4 + (k >> 1)][k & 1]) : "v"(av[k]), "v"(sv[k]), "v"(xf[4 + (k >> 1)][k & 1]));
    }
#pragma unroll
    for (int k = 0; k < 9; ++k) hv[k] = pack2(h[k][0], h[k][1]);
    const float da = dot2(hv[2], wv[2], dot2(hv[0], wv[0], zb[0])) + dot2(hv[3], wv[3], dot2(hv[1], wv[1], zb[1]));
    const float db = dot2(hv[6], wv[2], dot2(hv[4], wv[0], zb[2])) + dot2(hv[7], wv[3], dot2(hv[5], wv[1], zb[3]));
    float dt = dot2(hv[8], wv[4], fold32(da, db));
    f32x2 rc, nc;
    ln_stats<true>(hv, lane, 1.f, xf, za, rc, nc, dt);
    dt = (lane < 32) ? dt * rc[0] + ws_ * nc[0] : dt * rc[1] + ws_ * nc[1];
    const uint32_t bb = pack2(dt, dt) & 0xffff0000u;
    const uint32_t kk = (bb ^ (((uint32_t)((int)bb >> 31) & 0x7fff0000u) | 0x80000000u)) | kid;
    u32x4 ka, kb;
    uint32_t old;
    asm volatile("s_mov_b64 exec, %5\n\tds_write_b32 %3, %4\n\ts_mov_b64 exec, 1\n\tv_mov_b32 %2, 1\n\tds_add_rtn_u32 %2, %7, %2\n\ts_mov_b64 exec, -1\n\t"
                 "ds_read_b128 %0, %6\n\tds_read_b128 %1, %6 offset:16\n\ts_waitcnt lgkmcnt(0)"
                 : "=&v"(ka), "=&v"(kb), "=&v"(old) : "v"(ka_), "v"(kk), "s"(0x8000000080000000ull), "v"(kb_), "v"(ca) : "memory");
    if (__builtin_amdgcn_readfirstlane(old) == 7) {
      uint32_t m1 = max(max(ka[0], ka[1]), ka[2]), m2 = med3u(ka[0], ka[1], ka[2]), m3 = min(min(ka[0], ka[1]), ka[2]);
#pragma unroll
      for (int e = 0; e < 5; ++e) {
        const uint32_t kx = e ? kb[e - 1] : ka[3];
        m3 = med3u(m2, m3, kx); m2 = med3u(m1, m2, kx); m1 = max(m1, kx);
      }
      if (lane < 8) ST16(ko_, ((u32x4){m1 | 24u, m2 | 16u, m3 | 8u, T & ~1u}));
    }
    TS(0, 10);
    keya = (uint32_t)__builtin_amdgcn_readlane(kk, 31); keyb = (uint32_t)__builtin_amdgcn_readlane(kk, 63);
    ln_apply<false>(xf, lane, rc, nc, nv);
    gwait(ca, 8);
    if (wl == 1) {
      const u32x4 kv = *(const u32x4*)&kl[4 * (lane & 1)];
      if (lane < 2) ST16(O_KF + (uint32_t)(2 * g2 + lane) * 16u, ((u32x4){__builtin_amdgcn_perm(kv[1], kv[0], 0x07060302u), T, __builtin_amdgcn_perm(kv[3], kv[2], 0x07060302u), T}));
      if (ROLE == 2 && b == 0 && lane == 0) ST4(O_EP, T * 0x0E8B2F51u);
    }
    const uint32_t ko = kl[lane & 7];
    rka = FORCEFB + __popcll(__ballot(ko > keya) & 0xffull); rkb = FORCEFB + __popcll(__ballot(ko > keyb) & 0xffull);
#pragma unroll
    for (int k = 0; k < 9; ++k) {
      const f32x2 o = xf[k] + (f32x2){bflo(nv[k]), bfhi(nv[k])};
      hv[k] = pack2(o[0], o[1]);
      nv[k] = min(nv[k], 0xfffffffeu);
    }
    if (rka < 3) put_row(nv, 0, ws, O_XC + (uint32_t)(1023 - 4 * g2 - rka) * 1152u);
    if (rkb < 3) put_row(nv, 1, ws, O_XC + (uint32_t)(1023 - 4 * g2 - rkb) * 1152u);
    if (!LPOS && ffn) down_dma(KN, 4, lane);
    ffn_terms();
  }
  uint32_t k3rd = 0u, k4th = 0u;
  uint32_t q0 = lb0 + LM + 4u * lane, q1 = lb0 + LM + 32u * w, q2 = q1 + 4u * (lane & 7), q3 = lb0 + LM + 512u + 4u * (lane & 7), q4 = lb0 + LM + (uint32_t)(tid & ~7);
  uint32_t r8 = C_, sb = lb0 + LM + 512u;
  asm volatile("" : "+v"(q0), "+v"(q1), "+v"(q2), "+v"(q3), "+v"(q4), "+v"(r8), "+v"(sb));
  if constexpr (ROLE == 1) {
    u32x4 key;
    const uint32_t kof = O_KEY + (uint32_t)(256 * bx + tid) * 16u;
    until(tk);
    if (!ffn) SLEEP(NSLEEP);
    LD16(key, kof);
    if (ffn) {
      gwait0(ca + 4, 8);
      gu_read(1);
    }
    asm volatile("s_waitcnt vmcnt(0)" : "+v"(key) : : "memory");
    if (__builtin_expect(__any(key[3] != (T & ~1u)), 0)) {
#pragma unroll 1
      for (int spin = 0; spin < SPIN_CAP; ++spin) {
        const bool need = key[3] != (T & ~1u);
        if (!__any(need)) break;
        TSC(15);
        SLEEP(KBACK);
        if (need) LD16W(key, kof);
      }
    }
    TS(0, 11);
    const uint32_t m1 = oct_maxu(key[0]);
    const bool hit = key[0] == m1;
    const uint32_t k0 = hit ? key[1] : key[0], k1 = hit ? key[2] : key[1];
    const uint32_t m2 = oct_maxu(k0);
    *LP(u32x2, q4) = (u32x2){m1, m2};
    k3rd = (k0 == m2) ? k1 : k0;
    k4th = (k0 == m2) ? (hit ? 0u : key[2]) : k1;
    if (RPF == 1 && !ffn && w == (bj & 3)) {
      const uint32_t r1 = O_XC + ((m1 >> 3) & 0x3ffu) * 1152u + 128u * (lane & 7), r2 = O_XC + ((m2 >> 3) & 0x3ffu) * 1152u + 128u * (lane & 7);
      asm volatile("global_load_dword v126, %0, %3 sc1\n\tglobal_load_dword v126, %1, %3 sc1\n\tglobal_load_dword v126, %2, %3 offset:1024 sc1"
                   : : "v"(r1), "v"(r2), "v"(((lane & 1) ? r1 : r2) - 128u * (lane & 7)), "s"(ws) : "memory", "v126");
    }
  }
  auto pick = [&](auto co, auto so_) {
    BAR();
    const uint32_t c = *LP(uint32_t, q0 + co.value), own = *LP(uint32_t, q2 + co.value);
    const u32x4 la = *LP(u32x4, q1 + co.value), lb = *LP(u32x4, q1 + co.value + 16);
    uint32_t r = r8;
    asm("v_cmp_gt_u32 s[40:41], %1, %2\n\tv_cmp_gt_u32 s[42:43], %1, %3\n\tv_cmp_gt_u32 s[44:45], %1, %4\n\tv_cmp_gt_u32 s[46:47], %1, %5\n\t"
        "v_cmp_gt_u32 s[48:49], %1, %6\n\tv_cmp_gt_u32 s[50:51], %1, %7\n\tv_cmp_gt_u32 s[52:53], %1, %8\n\tv_cmp_gt_u32 s[54:55], %1, %9\n\t"
        "s_bcnt1_i32_b64 s40, s[40:41]\n\ts_bcnt1_i32_b64 s42, s[42:43]\n\tv_writelane_b32 %0, s40, 0\n\ts_bcnt1_i32_b64 s44, s[44:45]\n\t"
        "v_writelane_b32 %0, s42, 1\n\ts_bcnt1_i32_b64 s46, s[46:47]\n\tv_writelane_b32 %0, s44, 2\n\ts_bcnt1_i32_b64 s48, s[48:49]\n\t"
        "v_writelane_b32 %0, s46, 3\n\ts_bcnt1_i32_b64 s50, s[50:51]\n\tv_writelane_b32 %0, s48, 4\n\ts_bcnt1_i32_b64 s52, s[52:53]\n\t"
        "v_writelane_b32 %0, s50, 5\n\ts_bcnt1_i32_b64 s54, s[54:55]\n\tv_writelane_b32 %0, s52, 6\n\tv_writelane_b32 %0, s54, 7"
        : "+v"(r) : "v"(c), "v"(la[0]), "v"(la[1]), "v"(la[2]), "v"(la[3]), "v"(lb[0]), "v"(lb[1]), "v"(lb[2]), "v"(lb[3])
        : "scc", "s40", "s41", "s42", "s43", "s44", "s45", "s46", "s47", "s48", "s49", "s50", "s51", "s52", "s53", "s54", "s55");
    *LP(uint32_t, sb + 4u * min(r, (uint32_t)C_) + so_.value) = own;
    BAR();
  };
  auto rows = [&](uint32_t ao, auto&& meanwhile) {
    u32x4 aa, ab, ac = __builtin_bit_cast(u32x4, zb);
    asm volatile("global_load_dwordx4 %0, %3, %4 sc1\n\tglobal_load_dwordx4 %1, %3, %4 offset:512 sc1\n\t"
                 "s_cmp_lg_u32 %5, 0\n\ts_cbranch_scc1 1f\n\tglobal_load_dwordx4 %2, %3, %4 offset:1024 sc1\n1:"
                 : "=&v"(aa), "=&v"(ab), "+v"(ac) : "v"(ao), "s"(ws), "s"(w) : "memory", "scc");
    __builtin_amdgcn_sched_barrier(0);
    meanwhile();
    asm volatile("s_waitcnt vmcnt(0)" : "+v"(aa), "+v"(ab), "+v"(ac) : : "memory");
    if (__builtin_expect(__any(max(max(max4(aa), max4(ab)), max4(ac)) == 0xffffffffu), 0)) {
#pragma unroll 1
      for (int spin = 0; spin < SPIN_CAP; ++spin) {
        const bool na = max4(aa) == 0xffffffffu, nb = max4(ab) == 0xffffffffu, nc = max4(ac) == 0xffffffffu;
        if (!__any(na || nb || nc)) break;
        TSC(14);
        SLEEP(PSLEEP);
        if (na) LD16W(aa, ao);
        if (nb) LD16W(ab, ao + 512u);
        if (nc) LD16W(ac, ao + 1024u);
      }
    }
    *(u32x4*)(smem + LA + 16 * tid) = aa;
    *(u32x4*)(smem + LA + 4096 + 16 * tid) = ab;
    if (w == 0) *(u32x4*)(smem + LA + 8192 + 16 * tid) = ac;
  };
  auto rows_dma = [&](uint32_t ao) {
    uint32_t dl = lb0 + LA + 1024u * w, keep;
    asm volatile("s_mov_b32 %0, m0\n\ts_mov_b32 m0, %1\n\ts_add_u32 %1, %1, 4096 - 512\n\tglobal_load_lds_dwordx4 %2, %3 sc1\n\ts_mov_b32 m0, %1\n\ts_add_u32 %1, %1, 4096 - 512\n\t"
                 "global_load_lds_dwordx4 %2, %3 offset:512 sc1\n\ts_cmp_lg_u32 %4, 0\n\ts_cbranch_scc1 1f\n\ts_mov_b32 m0, %1\n\ts_nop 0\n\t"
                 "global_load_lds_dwordx4 %2, %3 offset:1024 sc1\n1:\n\ts_mov_b32 m0, %0" : "=&s"(keep), "+s"(dl) : "v"(ao), "s"(ws), "s"(w) : "memory", "scc");
  };
  auto get_bd = [&](int t0, int t1) {
#pragma unroll
    for (int t = t0; t < t1; ++t) {
      const u32x2 h0 = trd(da + 512 * (t >> 2) + 32 * (t & 3)), h1 = trd(da + 512 * (t >> 2) + 32 * (t & 3) + 4608 + 32);
      bw[t] = (u32x4){h0[0], h0[1], h1[0], h1[1]};
    }
#pragma unroll
    for (int t = t0; t < t1; ++t) asm volatile("" : "+v"(bw[t]));
  };
  pick(IC<0>{}, IC<0>{});
  uint32_t mykey = *LP(uint32_t, q3), ro = ((mykey >> 3) & 0x3ffu) * 1152u;
  TS(0, 12);
  auto check = [&] {
    cnt[(k3rd > (uint32_t)__builtin_amdgcn_readlane(mykey, 7)) ? 5 : 6] = 1u;
    const uint32_t m3 = oct_maxu(k3rd);
    if (k3rd == m3) k3rd = k4th;
    if ((lane & 7) == 0) cand[72 + (tid >> 3)] = m3;
    if (w == 0 && lane < 32) cand[(lane < 8) ? 64 + lane : 96 + lane] = (lane < 8) ? mykey : 0u;
  };
  if constexpr (ROLE == 1) {
    if (ffn && !FORCEFB) rows_dma(ro + al);
    check();
    DRAIN();
  } else if (ffn) {
    if (LPOS) { SLEEP(LSLEEP); down_dma(KN, 4, lane); } else { DRAIN(); gsync0(ca, 12); get_bd(0, 8); }
  }
  BAR();
  TS(0, 5);
  auto put_out = [&](int j, uint32_t k0, int rk) {
    const uint64_t mine = __ballot(((mykey ^ k0) & ~24u) == 0u) & 0xffull;
    if (mine == 0ull) put_row(hv, j, out, (uint32_t)((8 * g2 + 2 * wl + j) * (D_ * 2)));
    else if (rk >= 3) put_row(nv, j, ws, O_FB + 1152u * (uint32_t)__builtin_ctzll(mine));
  };
  auto warm_out = [&] {
    const uint32_t k0 = (lane < 9) ? keya : keyb;
    bool hit = false;
#pragma unroll
    for (int e = 0; e < 8; ++e) hit |= (((uint32_t)__builtin_amdgcn_readlane(mykey, e) ^ k0) & ~24u) == 0u;
    if (OTP && lane < 18 && hit) asm volatile("global_load_dword v126, %0, %1" : : "v"((uint32_t)((8 * g2 + 2 * wl) * (D_ * 2) + 128 * lane)), "s"(out) : "memory", "v126");
  };
  auto rest_out = [&] { put_out(0, keya, rka); put_out(1, keyb, rkb); };
  f32x4 lo, hi;
  auto gate_up = [&] {
    u32x4 av[5];
    const uint32_t aa = lb0 + LA + 16u * lane;
#pragma unroll
    for (int j = 0; j < 5; ++j) asm volatile("ds_read_b128 %0, %1 offset:%2" : "=v"(av[j]) : "v"(aa), "n"(1024 * j) : "memory");
    lo = (f32x4){0.f, 0.f, 0.f, 0.f}; hi = lo;
#pragma unroll
    for (int j = 0; j < 9; ++j) {
      if (j < 5) asm volatile("s_waitcnt lgkmcnt(4)" : "+v"(av[j % 5]));
      else if (j == 5) asm volatile("s_waitcnt lgkmcnt(2)" : "+v"(av[0]), "+v"(av[1]));
      else if (j == 7) asm volatile("s_waitcnt lgkmcnt(0)" : "+v"(av[2]), "+v"(av[3]));
      lo = MMB(av[j % 5], bw[2 * j], lo);
      hi = MMB(av[j % 5], bw[2 * j + 1], hi);
      __builtin_amdgcn_sched_barrier(0);
      if (j < 4) asm volatile("ds_read_b128 %0, %1 offset:%2" : "=v"(av[j]) : "v"(aa), "n"(1024 * (j + 5)) : "memory");
    }
  };
  uint32_t fl;
  asm volatile("ds_read_b32 %0, %1" : "=v"(fl) : "v"(ca + 20) : "memory");
  if (ROLE == 1) gate_up();
  asm volatile("s_waitcnt lgkmcnt(0)" : "+v"(fl));
  const bool slow = FORCESLOW || FORCEFB || __builtin_amdgcn_readfirstlane(fl) != 0;
  if (__builtin_expect(slow, 0)) {
    TSC(17);
    pick(IC<256>{}, IC<64>{});
    mykey = sel[16 + (lane & 7)];
    ro = ((mykey >> 3) & 0x3ffu) * 1152u;
    const uint32_t k8 = (uint32_t)__builtin_amdgcn_readlane(mykey, 7);
    if (ROLE == 1 && k3rd > k8) cnt[11] = 1u;
    if (w == 0 && __any((mykey & 24u) == 8u)) {
      const uint32_t pk = sel[16 + (lane >> 3)], gi = (pk >> 5) & 0xffu;
      u32x4 kf;
#pragma unroll 1
      for (int spin = 0; spin < SPIN_CAP; ++spin) {
        LD16W(kf, O_KF + (2u * (255u - gi) + ((lane >> 2) & 1u)) * 16u);
        if (!__any(kf[1] != T || kf[3] != T)) break;
        SLEEP(KBACK);
      }
      const uint32_t wd_ = (lane & 2) ? kf[2] : kf[0], kt = ((lane & 1) ? wd_ & 0xffff0000u : wd_ << 16) | (gi << 5) | (7u - (lane & 7));
      const uint64_t up = __ballot((pk & 24u) == 8u && kt >= (k8 & ~24u));
      if (__popcll((up >> (lane & 56)) & 0xffull) > 3) cnt[11] = 1u;
    }
    BAR();
    if (FORCESLOW > 1 || FORCEFB || __builtin_amdgcn_readfirstlane(cnt[11]) != 0) {
    u32x4 key;
#pragma unroll 1
    for (int spin = 0; spin < SPIN_CAP; ++spin) {
      LD16W(key, O_KF + 16u * tid);
      if (!__any(key[1] != T || key[3] != T)) break;
      SLEEP(KBACK);
    }
    const uint32_t ib = (uint32_t)(((255 - (tid >> 1)) << 5) | (7 - 4 * (tid & 1)));
    uint32_t k0 = (key[0] << 16) | ib, k1 = (key[0] & 0xffff0000u) | (ib - 1), k2 = (key[2] << 16) | (ib - 2), k3 = (key[2] & 0xffff0000u) | (ib - 3);
    CEX(k0, k1) CEX(k2, k3) CEX(k0, k2) CEX(k1, k3) CEX(k1, k2)
#pragma unroll 1
    for (int c = 0; c < C_; ++c) {
      const uint32_t mx = wave_maxu(k0);
      const bool hit = (k0 == mx);
      k0 = hit ? k1 : k0; k1 = hit ? k2 : k1; k2 = hit ? k3 : k2; k3 = hit ? 0u : k3;
      if (lane == 0) cand[64 + 8 * w + c] = mx;
    }
    pick(IC<256>{}, IC<128>{});
    mykey = sel[32 + (lane & 7)];
    const uint32_t okey = sel[32 + (lane >> 3)];
    const uint64_t same = __ballot(((mykey ^ okey) & 0x1fe0u) == 0u && okey > mykey);
    const int sl = FORCEFB + __popcll((same >> (lane & 7)) & 0x0101010101010101ull);
    ro = (sl < 3) ? (((mykey >> 3) & 0x3ffu) + 3 - sl) * 1152u : O_FB - O_XC + 1152u * (lane & 7);
    }
    if (ln2) rest_out();
    if (ffn) {
      if (ROLE == 1) rows(ro + al, [] {});
      BAR();
      if (ROLE == 1) gate_up();
    }
  }
  if (OPOS == 0 && ROLE == 2 && ffn && !slow) { SLEEP(OD); rest_out(); }
  if (!ffn) {
    if (ln2) warm_out();
    if (ln2 && !slow) { SLEEP(OD2); rest_out(); }
    TSE();
    return;
  }
  uint32_t fx = 0, fv = 0, fn = 0, fo = 0, sa[9];
  float rs, hx, xs;
  u32x4 se;
  if constexpr (ROLE == 1) {
    f32x4 lh = (lane < 32) ? lo : hi;
    float fa = fold32(lh[0], lh[2]), fb2 = fold32(lh[1], lh[3]);
    if (__builtin_expect(__any(__builtin_isunordered(fa, fb2)) && !slow, 0)) {
      gsync0(ca + 4, 12);
      rows(ro + al, [] {});
      gsync0(ca + 4, 16);
      gate_up();
      lh = (lane < 32) ? lo : hi;
      fa = fold32(lh[0], lh[2]); fb2 = fold32(lh[1], lh[3]);
    }
    const float g = __int_as_float(__builtin_amdgcn_update_dpp(__float_as_int(fa), __float_as_int(fb2), 0x128, 0xf, 0xc, false));
    const float u = __int_as_float(__builtin_amdgcn_update_dpp(__float_as_int(fb2), __float_as_int(fa), 0x128, 0xf, 0x3, false));
    const uint32_t gu = pack2(g, u);
    const float gp = bflo(gu);
    const float sg = rbf(gp * __builtin_amdgcn_rcpf(1.f + __expf(-gp)));
    *(u16*)((char*)hm + ha) = f2bf(sg * bfhi(gu));
  } else if (LPOS) {
    DRAIN();
    gsync0(ca, 12);
    get_bd(0, 8);
  }
  BAR();
  TS(0, 6);
  if (ROLE == 2 || w == 2) {
    const u32x4 a0 = *(const u32x4*)((char*)hm + h0a), a1 = *(const u32x4*)((char*)hm + h1a);
    if (ROLE == 1) get_bd(0, 4);
    const f32x4 z = {0.f, 0.f, 0.f, 0.f};
    f32x4 o[4];
#pragma unroll
    for (int t = 0; t < 2 * ROLE; ++t) o[t] = MMB(a1, bw[2 * t + 1], MMB(a0, bw[2 * t], z));
#pragma unroll
    for (int t = 0; t < ROLE; ++t) {
      u32x4 sv;
#pragma unroll
      for (int r = 0; r < 4; ++r) sv[r] = min(pack2(o[2 * t][r], o[2 * t + 1][r]), 0xfffffffeu);
      ST16(so + 4096u * t, sv);
    }
    TS(0, 13);
  }
  if (OPOS == 1 && ROLE == 2) {
    warm_out();
    SLEEP(OSLEEP);
    if (!slow) rest_out();
  }
  if (ROLE == 1 && w == 1) {
    const int q = (fb & 15) >> 2, ts = 4 * (q & 1) + ((i & 7) >> 1);
    const int dd = 64 * (fb >> 4) + 16 * (2 * (i & 1) + (q >> 1)) + 4 * (fb & 3) + 2 * (kq & 1) + (kq >> 1);
    const uint32_t fkey = (uint32_t)__builtin_amdgcn_ds_bpermute(4 * ts, (int)mykey), fr = (uint32_t)__builtin_amdgcn_ds_bpermute(4 * ts, (int)ro);
    const int ft = 2047 - (int)(((fkey >> 2) & 0x7f8u) | (fkey & 7u));
    fo = (uint32_t)(ft * D_ + dd) * 2u;
    u32x2 fs;
    asm volatile("global_load_ushort %0, %4, %8\n\tglobal_load_ushort %1, %5, %9 sc1\n\tglobal_load_ushort %2, %6, %9 sc1\n\tglobal_load_dwordx2 %3, %7, %9 sc1"
                 : "=&v"(fx), "=&v"(fv), "=&v"(fn), "=&v"(fs) : "v"(fo), "v"(v_at(dd, ft >> 3) + 2u * (ft & 7)), "v"(O_XC + fr + 2u * dd), "v"(O_SC + 16u * dd), "s"(x), "s"(ws) : "memory");
    uint32_t t16 = (((T >> (3 * ((ft >> 1) & 3))) & LMASK) | (((T >> (12 + ((ft >> 1) & 3))) & 0x00010001u) << 14)) >> (16 * (ft & 1)) & 0xffffu;
    const uint32_t o = fkey >> 16;
    const float rs0 = bf2f((u16)((o & 0x8000u) ? (o & 0x7fffu) : (~o & 0xffffu)));
    rs = rbf(__builtin_amdgcn_rcpf(1.f + __expf(-rs0)));
    const uint32_t one = (i < 8) ? (0x3f80u << (16 * (i & 1))) : 0u;
    se = (u32x4){(i >> 1) == 0 ? one : 0u, (i >> 1) == 1 ? one : 0u, (i >> 1) == 2 ? one : 0u, (i >> 1) == 3 ? one : 0u};
#pragma unroll
    for (int m = 0; m < 9; ++m) sa[m] = O_SL + (uint32_t)(16 * m + (lane >> 2)) * SP + (uint32_t)(4 * fb + (lane & 3)) * 16u;
    asm volatile("" : "+v"(t16), "+v"(rs), "+v"(se), "+v"(sa[0]), "+v"(sa[1]), "+v"(sa[2]), "+v"(sa[3]), "+v"(sa[4]), "+v"(sa[5]), "+v"(sa[6]), "+v"(sa[7]), "+v"(sa[8]));
    SLEEP(SSLEEP);
    if (FD) until(tk + FD);
    TS(0, 1);
    float sum;
#pragma unroll 1
    for (int spin = 0; spin < SPIN_CAP; ++spin) {
      u32x4 sv[9];
#pragma unroll
      for (int m = 0; m < 9; ++m) LD16(sv[m], sa[m]);
      asm volatile("s_waitcnt vmcnt(9)" : "+v"(fx), "+v"(fv), "+v"(fn), "+v"(fs));
      if (__builtin_expect(__any(fs[0] != T), 0)) {
        for (int k = 0; k < SPIN_CAP && fs[0] != T; ++k)
          asm volatile("global_load_dwordx2 %0, %1, %2 sc1\n\ts_waitcnt vmcnt(0)" : "=&v"(fs) : "v"(O_SC + 16u * dd), "s"(ws) : "memory");
      }
      const uint32_t f1 = fv ^ t16, f2 = (f1 & 7u) ? fv & ~7u : f1;
      hx = rbf(bf2f((u16)fx) + (float)__builtin_bit_cast(_Float16, (u16)f2) * __uint_as_float(fs[1]));
      xs = bf2f((u16)fn);
      asm volatile("" : "+v"(hx), "+v"(xs));
      f32x4 dv = {0.f, 0.f, 0.f, 0.f};
      static_for<0, 9>([&](auto mm) {
        constexpr int m = mm.value;
        asm volatile("s_waitcnt vmcnt(%1)" : "+v"(sv[m]) : "n"(8 - m));
        dv = MMB(sv[m], se, dv);
        __builtin_amdgcn_sched_barrier(0);
      });
      sum = fold16(fold32(dv[0], dv[1]), fold32(dv[2], dv[3]));
      if (__builtin_expect(!__any(sum != sum), 1)) break;
      uint32_t mx = 0u;
#pragma unroll
      for (int m = 0; m < 9; ++m) mx = max(mx, max4(sv[m]));
      if (!__any(mx == 0xffffffffu)) break;
      TSC(18);
      SLEEP(PSLEEP);
    }
    const float o1 = rbf(xs + rbf(rbf(sum) * rs));
    if (i < 8) asm volatile("global_store_short %0, %1, %2 sc1" : : "v"(fo), "v"((uint32_t)f2bf(hx + o1)), "s"(out) : "memory");
    TS(0, 2);
  }
  TSE();
  }
  }
}
#define BROT "192"
#define CPF_A "s_getpc_b64 s[96:97]\n\ts_lshl_b32 s99, s2, 4\n\ts_lshl_b32 s84, s98, 7\n\ts_add_u32 s99, s99, s84\n\tv_and_b32 v112, 1, v0\n\t" \
              "v_lshl_add_u32 v112, v112, 12, s99\n\tv_min_u32 v112, kf_end-kf_all-0x80, v112\n\ts_mov_b64 exec, 3\n\tglobal_load_dword v112, v112, s[96:97]\n\ts_mov_b64 exec, -1\n\t"
extern "C" __global__ __launch_bounds__(512, 4) void kf_all(KARGS) {
  __shared__ __attribute__((aligned(16))) char smem[SMEM];
  asm volatile("s_load_dwordx16 s[64:79], s[0:1], 0x0\n\ts_load_dwordx4 s[80:83], s[0:1], 0x40\n\tv_readfirstlane_b32 s98, v0\n\t"
               "s_add_u32 s84, s2, " BROT "\n\ts_xor_b32 s84, s84, s2\n\ts_bitset0_b32 s84, 8\n\ts_bitset0_b32 s84, 9\n\ts_xor_b32 s2, s2, s84\n\t"
               "v_mov_b32 v109, 0\n\ts_and_b32 s85, s2, 7\n\ts_bitcmp1_b32 s2, 8\n\ts_cbranch_scc1 1f\n\t" CPF_A "s_bitcmp1_b32 s98, 8\n\ts_cbranch_scc1 2f\n\t"
               "s_andn2_b32 s84, s2, 31\n\ts_lshl2_add_u32 s84, s85, s84\n\ts_bfe_u32 s85, s2, 0x20003\n\ts_or_b32 s84, s84, s85\n\t"
               "s_mulk_i32 s84, 0x2400\n\ts_mulk_i32 s98, 36\n\ts_add_u32 s84, s84, s98\n\tv_and_b32 v110, 63, v0\n\tv_and_b32 v111, 32, v0\n\t"
               "v_lshlrev_b32 v111, 5, v111\n\tv_lshl_add_u32 v111, v110, 2, v111\n\tv_lshl_add_u32 v110, v110, 4, s84\n\tv_add_u32 v111, s84, v111\n\t"
               "s_cmpk_lt_u32 s2, 0x90\n\ts_waitcnt lgkmcnt(0)\n\t"
               "global_load_dwordx4 v[100:103], v110, s[64:65]\n\tglobal_load_dwordx4 v[104:107], v110, s[64:65] offset:1152\n\t"
               "global_load_dword v108, v111, s[64:65] offset:1024\n\tglobal_load_dword v109, v109, s[82:83] sc1\n\t"
               "s_cbranch_scc1 kf_n\n\ts_branch kf_h\n"
               "1:\n\ts_bfe_u32 s84, s2, 0x50003\n\ts_lshl_b32 s85, s85, 5\n\ts_or_b32 s84, s84, s85\n\ts_lshl_b32 s84, s84, 2\n\t"
               "v_and_b32 v110, 0xff, v0\n\tv_bfe_u32 v107, v0, 7, 1\n"
               "4:\n\ts_lshl_b32 s85, s84, 7\n\tv_lshl_add_u32 v110, v110, 1, s85\n\tv_lshl_add_u32 v107, v107, 1, s84\n\ts_and_b32 s85, s98, s2\n\ts_waitcnt lgkmcnt(0)\n\t"
               "global_load_ushort v100, v107, s[78:79]\n\tglobal_load_ushort v101, v110, s[66:67]\n\tglobal_load_ushort v102, v110, s[68:69]\n\t"
               "global_load_dword v109, v109, s[82:83] sc1\n\ts_bitcmp1_b32 s85, 8\n\ts_cbranch_scc1 kf_u\n\t"
               "global_load_ushort v103, v110, s[70:71]\n\tglobal_load_ushort v104, v110, s[72:73]\n\tglobal_load_ushort v105, v110, s[74:75]\n\t"
               "global_load_ushort v106, v110, s[76:77]\n\tglobal_load_ushort v107, v107, s[80:81]\n\ts_bitcmp1_b32 s2, 8\n\ts_cbranch_scc1 kf_c\n"
               "6:\n\ts_barrier\n\ts_branch kf_p\n"
               "2:\n\ts_cmpk_lt_u32 s2, 0xc0\n\ts_cbranch_scc1 6b\n\t"
               "s_lshr_b32 s84, s2, 3\n\ts_lshl3_add_u32 s84, s85, s84\n\ts_lshl1_add_u32 s84, s84, 976\n\t"
               "v_and_b32 v110, 0x7f, v0\n\tv_mov_b32 v107, 0\n\ts_branch 4b"
               : : "s"(__builtin_amdgcn_kernarg_segment_ptr()), "s"(blockIdx.x), "v"(threadIdx.x),
                   "s"((uint32_t)(uintptr_t)(__attribute__((address_space(3))) char*)smem) : "scc", "s96", "s97", "s98", "s99", "v127");
}
extern "C" __global__ __launch_bounds__(512, 4) void kf_c(KARGS) {
  __shared__ __attribute__((aligned(16))) char smem[SMEM];
  role_body<0>(smem, KPASS);
}
extern "C" __global__ __launch_bounds__(512, 4) void kf_u(KARGS) {
  __shared__ __attribute__((aligned(16))) char smem[SMEM];
  role_body<3>(smem, KPASS);
}
extern "C" __global__ __launch_bounds__(512, 4) void kf_n(KARGS) {
  __shared__ __attribute__((aligned(16))) char smem[SMEM];
  role_body<1, 1>(smem, KPASS);
}
extern "C" __global__ __launch_bounds__(512, 4) void kf_h(KARGS) {
  __shared__ __attribute__((aligned(16))) char smem[SMEM];
  role_body<1, 0>(smem, KPASS);
}
extern "C" __global__ __launch_bounds__(512, 4) void kf_p(KARGS) {
  __shared__ __attribute__((aligned(16))) char smem[SMEM];
  role_body<2>(smem, KPASS);
}
extern "C" __global__ __launch_bounds__(512, 4) void kf_conv() { asm volatile(CONV_ASM ::: CONV_CLOB); }
extern "C" __global__ void kf_end() {}

extern "C" void kernel_launch(void* const* d_in, const int* in_sizes, int n_in,
                              void* d_out, int out_size, void* d_ws, size_t ws_size,
                              hipStream_t stream) {
  kf_all<<<512, 512, 0, stream>>>((const u16*)d_in[0], (const u16*)d_in[1], (const u16*)d_in[2], (const u16*)d_in[3], (const u16*)d_in[4],
                                  (const u16*)d_in[5], (const u16*)d_in[6], (const u16*)d_in[7], (const u16*)d_in[8], (char*)d_ws + (size_t)WSOFF * 1048576,
                                  (const u16*)d_in[10], (const u16*)d_in[11], (const u16*)d_in[12], (const u16*)d_in[13], (u16*)d_out);
}
```

```cpp
#include <hip/hip_runtime.h>
#include <hip/hip_bf16.h>
#include <stdint.h>
#define KD 178

typedef __attribute__((ext_vector_type(8))) short bf16x8;
typedef __attribute__((ext_vector_type(4))) float f32x4;
typedef uint16_t u16;
typedef __attribute__((ext_vector_type(4))) uint32_t u32x4;
typedef __attribute__((ext_vector_type(2))) uint32_t u32x2;
#define TSD uint32_t pr0_ = 0u, pr1_ = 0u
__device__ unsigned long long g_prb[4096];
#define TS(k, i) do { if ((i) == 0) pr0_ = (uint32_t)__builtin_amdgcn_s_memrealtime() | 1u; \
    if (ROLE == 1 && (i) == 2 && (1 < 0 || w == 1) && (blockIdx.x & 255) < 144) pr1_ = (uint32_t)__builtin_amdgcn_s_memrealtime() | 1u; } while (0)
#define TSC(i)
#define TSE() do { if (__builtin_amdgcn_mbcnt_hi(-1, __builtin_amdgcn_mbcnt_lo(-1, 0)) == 0) \
    __hip_atomic_store(&g_prb[blockIdx.x * 8 + w], ((unsigned long long)pr1_ << 32) | pr0_, __ATOMIC_RELAXED, __HIP_MEMORY_SCOPE_AGENT); } while (0)
#define TSP
#define TSA

constexpr int L_ = 2048, D_ = 576, N_ = 128, F_ = 4608, C_ = 8;
constexpr int NF = 144;
constexpr uint32_t O_EP = 0;
constexpr uint32_t O_KF = 4096;
constexpr uint32_t O_KEY = O_KF + 8192;
constexpr uint32_t O_SC = O_KEY + 32768;
constexpr uint32_t O_DN = O_SC + 9216;
constexpr uint32_t O_SB = O_DN + 20480;
constexpr uint32_t O_SU = O_SB + 65536;
constexpr uint32_t O_FB = O_SU + 262144;
constexpr uint32_t O_XC = O_FB + 8 * 1152;
constexpr uint32_t SP = 9216 + 128;
constexpr uint32_t O_SL = O_XC + 1024 * 1152;
constexpr uint32_t SLU = 336;
constexpr uint32_t O_U = O_SL + 256 * SLU * 16;
constexpr int UP = 584;
constexpr uint32_t VP = 8192 + 128, VPP = 4096 + 128;
constexpr uint32_t O_V = O_U + 256 * UP * 16, O_VP = O_V + 256 * VP;
static_assert(256 * SLU * 16 >= NF * SP && O_SL % 128 == 0 && O_U % 128 == 0 && O_V % 128 == 0, "");
__host__ __device__ __forceinline__ uint32_t u_at(int tg, int d) { return O_U + 16u * (uint32_t)((tg >> 1) * (UP * 2) + (d >> 2) * 8 + (tg & 1) * 4 + (d & 3)); }
__host__ __device__ __forceinline__ uint32_t v_at(int d, int tg) {
  return (d < 512) ? O_V + (uint32_t)(d >> 1) * VP + 16u * (uint32_t)(2 * tg + (d & 1)) : O_VP + (uint32_t)(d - 512) * VPP + 16u * (uint32_t)tg;
}
#define HAVE_VAT
#define HAVE_VTAG
__device__ __forceinline__ float bf2f(u16 v) { return __uint_as_float((uint32_t)v << 16); }
__device__ __forceinline__ float bflo(uint32_t v) { return __uint_as_float(v << 16); }
__device__ __forceinline__ float bfhi(uint32_t v) { return __uint_as_float(v & 0xffff0000u); }
__device__ __forceinline__ u16 f2bf(float f) { return __builtin_bit_cast(u16, (__bf16)f); }
__device__ __forceinline__ float rbf(float f) { return bf2f(f2bf(f)); }
typedef __attribute__((ext_vector_type(2))) float f32x2;
typedef __attribute__((ext_vector_type(2))) __bf16 bf16x2v;
__device__ __forceinline__ uint32_t pack2(float a, float b) {
  return __builtin_bit_cast(uint32_t, __builtin_convertvector((f32x2){a, b}, bf16x2v));
}

#define DPP_I(v, ctrl, rmask) __builtin_amdgcn_update_dpp(0, (v), (ctrl), (rmask), 0xf, true)
__device__ __forceinline__ float row_sum(float v) {
  v += __int_as_float(DPP_I(__float_as_int(v), 0x111, 0xf));
  v += __int_as_float(DPP_I(__float_as_int(v), 0x112, 0xf));
  v += __int_as_float(DPP_I(__float_as_int(v), 0x114, 0xf));
  v += __int_as_float(DPP_I(__float_as_int(v), 0x118, 0xf));
  return v;
}
__device__ __forceinline__ float rdl(float v, int l) { return __int_as_float(__builtin_amdgcn_readlane(__float_as_int(v), l)); }
__device__ __forceinline__ float wave_sum(float v) {
  v = row_sum(v);
  asm("s_nop 1\n\tv_add_f32_dpp %0, %0, %0 row_bcast:15 row_mask:0xa bank_mask:0xf\n\t"
      "s_nop 1\n\tv_add_f32_dpp %0, %0, %0 row_bcast:31 row_mask:0xc bank_mask:0xf\n\ts_nop 0" : "+v"(v));
  return rdl(v, 63);
}
__device__ __forceinline__ float fold32(float a, float b) {
  const auto r = __builtin_amdgcn_permlane32_swap(__float_as_uint(a), __float_as_uint(b), false, false);
  return __uint_as_float(r[0]) + __uint_as_float(r[1]);
}
__device__ __forceinline__ float fold16(float a, float b) {
  const auto r = __builtin_amdgcn_permlane16_swap(__float_as_uint(a), __float_as_uint(b), false, false);
  return __uint_as_float(r[0]) + __uint_as_float(r[1]);
}
__device__ __forceinline__ void wave_sum2(float& a, float& b) {
  float v = row_sum(fold32(a, b));
  asm("s_nop 1\n\tv_add_f32_dpp %0, %0, %0 row_bcast:15 row_mask:0xa bank_mask:0xf\n\ts_nop 0" : "+v"(v));
  a = rdl(v, 31); b = rdl(v, 63);
}
__device__ __forceinline__ float dot2(uint32_t a, uint32_t b, float c) {
  return __builtin_amdgcn_fdot2_f32_bf16(__builtin_bit_cast(bf16x2v, a), __builtin_bit_cast(bf16x2v, b), c, false);
}
__device__ __forceinline__ uint32_t row_maxu(uint32_t v) {
  v = max(v, (uint32_t)DPP_I((int)v, 0xb1, 0xf));
  v = max(v, (uint32_t)DPP_I((int)v, 0x4e, 0xf));
  v = max(v, (uint32_t)DPP_I((int)v, 0x141, 0xf));
  v = max(v, (uint32_t)DPP_I((int)v, 0x140, 0xf));
  return v;
}
__device__ __forceinline__ uint32_t wave_maxu(uint32_t v) {
  v = max(v, (uint32_t)DPP_I((int)v, 0x111, 0xf));
  v = max(v, (uint32_t)DPP_I((int)v, 0x112, 0xf));
  v = max(v, (uint32_t)DPP_I((int)v, 0x114, 0xf));
  v = max(v, (uint32_t)DPP_I((int)v, 0x118, 0xf));
  v = max(v, (uint32_t)DPP_I((int)v, 0x142, 0xa));
  v = max(v, (uint32_t)DPP_I((int)v, 0x143, 0xc));
  return (uint32_t)__builtin_amdgcn_readlane((int)v, 63);
}

typedef __attribute__((ext_vector_type(8))) _Float16 f16x8;
typedef __attribute__((ext_vector_type(2))) _Float16 f16x2;
__device__ __forceinline__ uint32_t packh(f32x2 v) { return __builtin_bit_cast(uint32_t, __builtin_convertvector(v, f16x2)); }
__device__ __forceinline__ uint32_t packh(float a, float b) { return packh((f32x2){a, b}); }
constexpr int IP = 224, I_X = 128;
__device__ __forceinline__ f32x2 cmul(f32x2 z, f32x2 w) {
  f32x2 r;
  asm("v_pk_mul_f32 %0, %1, %2 op_sel_hi:[0,1]\n\tv_pk_fma_f32 %0, %1, %2, %0 op_sel:[1,1,0] op_sel_hi:[1,0,1] neg_lo:[0,1,0]" : "=&v"(r) : "v"(z), "v"(w));
  return r;
}
__device__ __forceinline__ f32x2 cexp2(float ar2, float air, float l) {
  const float t = air * l;
  return (f32x2){__builtin_amdgcn_cosf(t), __builtin_amdgcn_sinf(t)} * __builtin_amdgcn_exp2f(ar2 * l);
}
__device__ __forceinline__ float state_a(float ldt, float are, float aim, float& ar2, float& air, float& g) {
  const float dt = rbf(__expf(ldt));
  ar2 = dt * are * 1.4426950408889634f;
  air = dt * aim * 0.15915494309189535f;
  air -= __builtin_rintf(air);
  g = 0.f;
  if (__builtin_expect(__any(ar2 > 0.f), 0)) g = fminf(__uint_as_float(wave_maxu(__float_as_uint(fmaxf(ar2, 0.f)))), 1.f);
  return dt;
}
template <int N> __device__ __forceinline__ void put_im(char* p, int im, const f32x2* z) {
  *(u32x4*)p = (u32x4){packh(z[0][0], z[1][0]), packh(z[2][0], z[3][0]), packh(z[4][0], z[5][0]), packh(z[6][0], z[7][0])};
  *(u32x4*)(p + im) = (u32x4){packh(z[0][1], z[1][1]), packh(z[2][1], z[3][1]), packh(z[4][1], z[5][1]), packh(z[6][1], z[7][1])};
}
#define Y_ASM \
  "v_mov_b64 v[40:41], %0\nv_pk_mul_f32 v[42:43], v[40:41], %1 op_sel_hi:[0,1]\nv_pk_mul_f32 v[44:45], %0, %2 op_sel_hi:[0,1]\n" \
  "v_pk_fma_f32 v[42:43], v[40:41], %1, v[42:43] op_sel:[1,1,0] op_sel_hi:[1,0,1] neg_lo:[0,1,0]\n" \
  "v_pk_fma_f32 v[44:45], %0, %2, v[44:45] op_sel:[1,1,0] op_sel_hi:[1,0,1] neg_lo:[0,1,0]\nv_cvt_pk_f16_f32 v56, v40, v42\n" \
  "v_cvt_pk_f16_f32 v60, v41, v43\nv_pk_mul_f32 v[40:41], v[42:43], %1 op_sel_hi:[0,1]\nv_pk_mul_f32 v[48:49], v[44:45], %2 op_sel_hi:[0,1]\n" \
  "v_pk_fma_f32 v[40:41], v[42:43], %1, v[40:41] op_sel:[1,1,0] op_sel_hi:[1,0,1] neg_lo:[0,1,0]\n" \
  "v_pk_fma_f32 v[48:49], v[44:45], %2, v[48:49] op_sel:[1,1,0] op_sel_hi:[1,0,1] neg_lo:[0,1,0]\n" \
  "v_pk_mul_f32 v[42:43], v[40:41], %1 op_sel_hi:[0,1]\nv_pk_mul_f32 v[52:53], v[48:49], %2 op_sel_hi:[0,1]\n" \
  "v_pk_fma_f32 v[42:43], v[40:41], %1, v[42:43] op_sel:[1,1,0] op_sel_hi:[1,0,1] neg_lo:[0,1,0]\n" \
  "v_pk_fma_f32 v[52:53], v[48:49], %2, v[52:53] op_sel:[1,1,0] op_sel_hi:[1,0,1] neg_lo:[0,1,0]\nv_cvt_pk_f16_f32 v57, v40, v42\n" \
  "v_cvt_pk_f16_f32 v61, v41, v43\nv_pk_mul_f32 v[40:41], v[42:43], %1 op_sel_hi:[0,1]\n" \
  "v_pk_fma_f32 v[40:41], v[42:43], %1, v[40:41] op_sel:[1,1,0] op_sel_hi:[1,0,1] neg_lo:[0,1,0]\n" \
  "v_pk_mul_f32 v[42:43], v[40:41], %1 op_sel_hi:[0,1]\nv_pk_mul_f32 v[46:47], v[44:45], %1 op_sel_hi:[0,1]\n" \
  "v_pk_fma_f32 v[42:43], v[40:41], %1, v[42:43] op_sel:[1,1,0] op_sel_hi:[1,0,1] neg_lo:[0,1,0]\n" \
  "v_pk_fma_f32 v[46:47], v[44:45], %1, v[46:47] op_sel:[1,1,0] op_sel_hi:[1,0,1] neg_lo:[0,1,0]\nv_cvt_pk_f16_f32 v58, v40, v42\n" \
  "v_cvt_pk_f16_f32 v62, v41, v43\nv_cvt_pk_f16_f32 v64, v44, v46\nv_cvt_pk_f16_f32 v68, v45, v47\n" \
  "v_pk_mul_f32 v[40:41], v[42:43], %1 op_sel_hi:[0,1]\nv_pk_mul_f32 v[44:45], v[46:47], %1 op_sel_hi:[0,1]\n" \
  "v_pk_fma_f32 v[40:41], v[42:43], %1, v[40:41] op_sel:[1,1,0] op_sel_hi:[1,0,1] neg_lo:[0,1,0]\n" \
  "v_pk_fma_f32 v[44:45], v[46:47], %1, v[44:45] op_sel:[1,1,0] op_sel_hi:[1,0,1] neg_lo:[0,1,0]\n" \
  "v_pk_mul_f32 v[42:43], v[40:41], %1 op_sel_hi:[0,1]\nv_pk_mul_f32 v[46:47], v[44:45], %1 op_sel_hi:[0,1]\n" \
  "v_pk_fma_f32 v[42:43], v[40:41], %1, v[42:43] op_sel:[1,1,0] op_sel_hi:[1,0,1] neg_lo:[0,1,0]\n" \
  "v_pk_fma_f32 v[46:47], v[44:45], %1, v[46:47] op_sel:[1,1,0] op_sel_hi:[1,0,1] neg_lo:[0,1,0]\nv_cvt_pk_f16_f32 v59, v40, v42\n" \
  "v_cvt_pk_f16_f32 v63, v41, v43\nv_cvt_pk_f16_f32 v65, v44, v46\nv_cvt_pk_f16_f32 v69, v45, v47\n" \
  "v_pk_mul_f32 v[44:45], v[46:47], %1 op_sel_hi:[0,1]\nds_write_b128 %3, v[56:59] offset:0\n" \
  "v_pk_fma_f32 v[44:45], v[46:47], %1, v[44:45] op_sel:[1,1,0] op_sel_hi:[1,0,1] neg_lo:[0,1,0]\nds_write_b128 %3, v[60:63] offset:64\n" \
  "v_pk_mul_f32 v[46:47], v[44:45], %1 op_sel_hi:[0,1]\nv_pk_mul_f32 v[50:51], v[48:49], %1 op_sel_hi:[0,1]\n" \
  "v_pk_fma_f32 v[46:47], v[44:45], %1, v[46:47] op_sel:[1,1,0] op_sel_hi:[1,0,1] neg_lo:[0,1,0]\n" \
  "v_pk_fma_f32 v[50:51], v[48:49], %1, v[50:51] op_sel:[1,1,0] op_sel_hi:[1,0,1] neg_lo:[0,1,0]\nv_cvt_pk_f16_f32 v66, v44, v46\n" \
  "v_cvt_pk_f16_f32 v70, v45, v47\nv_cvt_pk_f16_f32 v56, v48, v50\nv_cvt_pk_f16_f32 v60, v49, v51\n" \
  "v_pk_mul_f32 v[44:45], v[46:47], %1 op_sel_hi:[0,1]\nv_pk_mul_f32 v[48:49], v[50:51], %1 op_sel_hi:[0,1]\n" \
  "v_pk_fma_f32 v[44:45], v[46:47], %1, v[44:45] op_sel:[1,1,0] op_sel_hi:[1,0,1] neg_lo:[0,1,0]\n" \
  "v_pk_fma_f32 v[48:49], v[50:51], %1, v[48:49] op_sel:[1,1,0] op_sel_hi:[1,0,1] neg_lo:[0,1,0]\n" \
  "v_pk_mul_f32 v[46:47], v[44:45], %1 op_sel_hi:[0,1]\nv_pk_mul_f32 v[50:51], v[48:49], %1 op_sel_hi:[0,1]\n" \
  "v_pk_fma_f32 v[46:47], v[44:45], %1, v[46:47] op_sel:[1,1,0] op_sel_hi:[1,0,1] neg_lo:[0,1,0]\n" \
  "v_pk_fma_f32 v[50:51], v[48:49], %1, v[50:51] op_sel:[1,1,0] op_sel_hi:[1,0,1] neg_lo:[0,1,0]\nv_cvt_pk_f16_f32 v67, v44, v46\n" \
  "v_cvt_pk_f16_f32 v71, v45, v47\nv_cvt_pk_f16_f32 v57, v48, v50\nv_cvt_pk_f16_f32 v61, v49, v51\n" \
  "v_pk_mul_f32 v[48:49], v[50:51], %1 op_sel_hi:[0,1]\nds_write_b128 %3, v[64:67] offset:16\n" \
  "v_pk_fma_f32 v[48:49], v[50:51], %1, v[48:49] op_sel:[1,1,0] op_sel_hi:[1,0,1] neg_lo:[0,1,0]\nds_write_b128 %3, v[68:71] offset:80\n" \
  "v_pk_mul_f32 v[50:51], v[48:49], %1 op_sel_hi:[0,1]\nv_pk_mul_f32 v[54:55], v[52:53], %1 op_sel_hi:[0,1]\n" \
  "v_pk_fma_f32 v[50:51], v[48:49], %1, v[50:51] op_sel:[1,1,0] op_sel_hi:[1,0,1] neg_lo:[0,1,0]\n" \
  "v_pk_fma_f32 v[54:55], v[52:53], %1, v[54:55] op_sel:[1,1,0] op_sel_hi:[1,0,1] neg_lo:[0,1,0]\nv_cvt_pk_f16_f32 v58, v48, v50\n" \
  "v_cvt_pk_f16_f32 v62, v49, v51\nv_cvt_pk_f16_f32 v64, v52, v54\nv_cvt_pk_f16_f32 v68, v53, v55\n" \
  "v_pk_mul_f32 v[48:49], v[50:51], %1 op_sel_hi:[0,1]\nv_pk_mul_f32 v[52:53], v[54:55], %1 op_sel_hi:[0,1]\n" \
  "v_pk_fma_f32 v[48:49], v[50:51], %1, v[48:49] op_sel:[1,1,0] op_sel_hi:[1,0,1] neg_lo:[0,1,0]\n" \
  "v_pk_fma_f32 v[52:53], v[54:55], %1, v[52:53] op_sel:[1,1,0] op_sel_hi:[1,0,1] neg_lo:[0,1,0]\n" \
  "v_pk_mul_f32 v[50:51], v[48:49], %1 op_sel_hi:[0,1]\nv_pk_mul_f32 v[54:55], v[52:53], %1 op_sel_hi:[0,1]\n" \
  "v_pk_fma_f32 v[50:51], v[48:49], %1, v[50:51] op_sel:[1,1,0] op_sel_hi:[1,0,1] neg_lo:[0,1,0]\n" \
  "v_pk_fma_f32 v[54:55], v[52:53], %1, v[54:55] op_sel:[1,1,0] op_sel_hi:[1,0,1] neg_lo:[0,1,0]\nv_cvt_pk_f16_f32 v59, v48, v50\n" \
  "v_cvt_pk_f16_f32 v63, v49, v51\nv_cvt_pk_f16_f32 v65, v52, v54\nv_cvt_pk_f16_f32 v69, v53, v55\n" \
  "v_pk_mul_f32 v[52:53], v[54:55], %1 op_sel_hi:[0,1]\nds_write_b128 %3, v[56:59] offset:32\n" \
  "v_pk_fma_f32 v[52:53], v[54:55], %1, v[52:53] op_sel:[1,1,0] op_sel_hi:[1,0,1] neg_lo:[0,1,0]\nds_write_b128 %3, v[60:63] offset:96\n" \
  "v_pk_mul_f32 v[54:55], v[52:53], %1 op_sel_hi:[0,1]\n" \
  "v_pk_fma_f32 v[54:55], v[52:53], %1, v[54:55] op_sel:[1,1,0] op_sel_hi:[1,0,1] neg_lo:[0,1,0]\nv_cvt_pk_f16_f32 v66, v52, v54\n" \
  "v_cvt_pk_f16_f32 v70, v53, v55\nv_pk_mul_f32 v[52:53], v[54:55], %1 op_sel_hi:[0,1]\n" \
  "v_pk_fma_f32 v[52:53], v[54:55], %1, v[52:53] op_sel:[1,1,0] op_sel_hi:[1,0,1] neg_lo:[0,1,0]\n" \
  "v_pk_mul_f32 v[54:55], v[52:53], %1 op_sel_hi:[0,1]\n" \
  "v_pk_fma_f32 v[54:55], v[52:53], %1, v[54:55] op_sel:[1,1,0] op_sel_hi:[1,0,1] neg_lo:[0,1,0]\nv_cvt_pk_f16_f32 v67, v52, v54\n" \
  "v_cvt_pk_f16_f32 v71, v53, v55\nds_write_b128 %3, v[64:67] offset:48\nds_write_b128 %3, v[68:71] offset:112\n"
#define Y_CLOB "memory", "v40", "v41", "v42", "v43", "v44", "v45", "v46", "v47", "v48", "v49", "v50", "v51", "v52", "v53", "v54", "v55", "v56", "v57", "v58", "v59", "v60", "v61", "v62", "v63", "v64", "v65", "v66", "v67", "v68", "v69", "v70", "v71"
#define T_ASM \
  "ds_read_b128 %4, %8\nds_read_b64_tr_b16 v[80:81], %7 offset:0\nds_read_b64_tr_b16 v[82:83], %7 offset:64\n" \
  "ds_read_b64_tr_b16 v[84:85], %5 offset:0\nds_read_b64_tr_b16 v[86:87], %5 offset:48\nds_read_b64_tr_b16 v[88:89], %6 offset:0\n" \
  "ds_read_b64_tr_b16 v[90:91], %6 offset:48\nds_read_b64_tr_b16 v[92:93], %7 offset:3584\nds_read_b64_tr_b16 v[94:95], %7 offset:3648\n" \
  "ds_read_b64_tr_b16 v[96:97], %5 offset:3584\nds_read_b64_tr_b16 v[98:99], %5 offset:3632\nds_read_b64_tr_b16 v[100:101], %6 offset:3584\n" \
  "ds_read_b64_tr_b16 v[102:103], %6 offset:3632\nds_read_b64_tr_b16 v[104:105], %7 offset:7168\nds_read_b64_tr_b16 v[106:107], %7 offset:7232\n" \
  "s_waitcnt lgkmcnt(8)\nv_mfma_f32_16x16x32_f16 %0, v[84:87], v[80:83], 0\nds_read_b64_tr_b16 v[108:109], %5 offset:7168\n" \
  "ds_read_b64_tr_b16 v[110:111], %5 offset:7216\nds_read_b64_tr_b16 v[112:113], %6 offset:7168\nv_mfma_f32_16x16x32_f16 %2, v[88:91], v[80:83], 0\n" \
  "ds_read_b64_tr_b16 v[114:115], %6 offset:7216\nds_read_b64_tr_b16 v[116:117], %7 offset:10752\nds_read_b64_tr_b16 v[118:119], %7 offset:10816\n" \
  "s_waitcnt lgkmcnt(8)\nv_mfma_f32_16x16x32_f16 %0, v[96:99], v[92:95], %0\nds_read_b64_tr_b16 v[120:121], %5 offset:10752\n" \
  "ds_read_b64_tr_b16 v[122:123], %5 offset:10800\nds_read_b64_tr_b16 v[124:125], %6 offset:10752\n" \
  "v_mfma_f32_16x16x32_f16 %2, v[100:103], v[92:95], %2\nds_read_b64_tr_b16 v[126:127], %6 offset:10800\n" \
  "ds_read_b64_tr_b16 v[80:81], %7 offset:14336\nds_read_b64_tr_b16 v[82:83], %7 offset:14400\ns_waitcnt lgkmcnt(8)\n" \
  "v_mfma_f32_16x16x32_f16 %0, v[108:111], v[104:107], %0\nds_read_b64_tr_b16 v[84:85], %5 offset:14336\n" \
  "ds_read_b64_tr_b16 v[86:87], %5 offset:14384\nds_read_b64_tr_b16 v[88:89], %6 offset:14336\n" \
  "v_mfma_f32_16x16x32_f16 %2, v[112:115], v[104:107], %2\nds_read_b64_tr_b16 v[90:91], %6 offset:14384\n" \
  "ds_read_b64_tr_b16 v[92:93], %7 offset:17920\nds_read_b64_tr_b16 v[94:95], %7 offset:17984\ns_waitcnt lgkmcnt(8)\n" \
  "v_mfma_f32_16x16x32_f16 %0, v[120:123], v[116:119], %0\nds_read_b64_tr_b16 v[96:97], %5 offset:17920\n" \
  "ds_read_b64_tr_b16 v[98:99], %5 offset:17968\nds_read_b64_tr_b16 v[100:101], %6 offset:17920\n" \
  "v_mfma_f32_16x16x32_f16 %2, v[124:127], v[116:119], %2\nds_read_b64_tr_b16 v[102:103], %6 offset:17968\n" \
  "ds_read_b64_tr_b16 v[104:105], %7 offset:21504\nds_read_b64_tr_b16 v[106:107], %7 offset:21568\ns_waitcnt lgkmcnt(8)\n" \
  "v_mfma_f32_16x16x32_f16 %1, v[84:87], v[80:83], 0\nds_read_b64_tr_b16 v[108:109], %5 offset:21504\nds_read_b64_tr_b16 v[110:111], %5 offset:21552\n" \
  "ds_read_b64_tr_b16 v[112:113], %6 offset:21504\nv_mfma_f32_16x16x32_f16 %3, v[88:91], v[80:83], 0\nds_read_b64_tr_b16 v[114:115], %6 offset:21552\n" \
  "ds_read_b64_tr_b16 v[116:117], %7 offset:25088\nds_read_b64_tr_b16 v[118:119], %7 offset:25152\ns_waitcnt lgkmcnt(8)\n" \
  "v_mfma_f32_16x16x32_f16 %1, v[96:99], v[92:95], %1\nds_read_b64_tr_b16 v[120:121], %5 offset:25088\n" \
  "ds_read_b64_tr_b16 v[122:123], %5 offset:25136\nds_read_b64_tr_b16 v[124:125], %6 offset:25088\n" \
  "v_mfma_f32_16x16x32_f16 %3, v[100:103], v[92:95], %3\nds_read_b64_tr_b16 v[126:127], %6 offset:25136\ns_waitcnt lgkmcnt(6)\n" \
  "v_mfma_f32_16x16x32_f16 %1, v[108:111], v[104:107], %1\nv_mfma_f32_16x16x32_f16 %3, v[112:115], v[104:107], %3\ns_waitcnt lgkmcnt(0)\n" \
  "v_mfma_f32_16x16x32_f16 %1, v[120:123], v[116:119], %1\nv_mfma_f32_16x16x32_f16 %3, v[124:127], v[116:119], %3\n"
#define T_CLOB "memory", "v80", "v81", "v82", "v83", "v84", "v85", "v86", "v87", "v88", "v89", "v90", "v91", "v92", "v93", "v94", "v95", "v96", "v97", "v98", "v99", "v100", "v101", "v102", "v103", "v104", "v105", "v106", "v107", "v108", "v109", "v110", "v111", "v112", "v113", "v114", "v115", "v116", "v117", "v118", "v119", "v120", "v121", "v122", "v123", "v124", "v125", "v126", "v127"
template <class F> __device__ __forceinline__ int gen_y(float ldt, float are, float aim, float bre, float bim, float cre, float cim, uint32_t im, F&& mid TSP) {
  float ar2, air, g;
  const float dt = state_a(ldt, are, aim, ar2, air, g);
  const float dbr = dt * bre, dbi = dt * bim, pr = cre * dbr - cim * dbi, pi = cre * dbi + cim * dbr;
  const f32x2 w8 = cexp2(ar2, -air, 8.f), w64 = cexp2(ar2, -air, 64.f);
  const float pm = fmaxf(fabsf(pr), fabsf(pi));
  int e = -4;
  if (__builtin_expect(__any(!(pm < 0x1p-4f) || ar2 > 0.f) || !__any(pm > 0x1p-12f), 0)) e = (int)(wave_maxu(__float_as_uint(pm)) >> 23) - 126 + (int)ceilf(g * 250.f);
  const f32x2 y0 = {__builtin_amdgcn_ldexpf(pr, 14 - e), __builtin_amdgcn_ldexpf(-pi, 14 - e)};
  mid();
  asm volatile(Y_ASM : : "v"(y0), "v"(w8), "v"(w64), "v"(im) : Y_CLOB);
  return e;
}
__device__ __forceinline__ int gen_x(float ldt, float are, float aim, char* im) {
  float ar2, air, g;
  state_a(ldt, are, aim, ar2, air, g);
  const int e = min((int)ceilf(g * 1810.f), 300);
  const float s = __builtin_amdgcn_ldexpf(1.f, 15 - e);
  const f32x2 w1 = cexp2(ar2, air, 1.f), w256 = cexp2(ar2, air, 256.f), w2 = cmul(w1, w1);
  f32x2 a[8], b[8], c[8];
  a[0] = (f32x2){s, 0.f}; a[1] = w1 * s; a[2] = w2 * s;
  b[0] = b[1] = w256 * s;
  b[2] = cmul(b[0], w2); a[3] = cmul(a[1], w2); a[4] = cmul(a[2], w2);
  b[3] = b[4] = b[5] = cmul(b[2], w2); a[5] = cmul(a[3], w2); a[6] = cmul(a[4], w2);
  b[6] = cmul(b[3], w2); a[7] = cmul(a[5], w2); c[0] = cmul(b[3], w256);
  b[7] = cmul(b[6], w2); c[2] = cmul(c[0], w256);
  put_im<0>(im + I_X, 48, a);
  c[1] = cmul(b[7], w256); c[3] = cmul(c[1], w256);
  put_im<0>(im + I_X + 16, 48, b);
#pragma unroll
  for (int k = 4; k < 8; ++k) c[k] = cmul(c[k - 1], w256);
  put_im<0>(im + I_X + 32, 48, c);
  return e;
}

template <int I> struct IC { static constexpr int value = I; };
template <int B, int E, class F> __device__ __forceinline__ void static_for(F&& f) {
  if constexpr (B < E) { f(IC<B>{}); static_for<B + 1, E>(f); }
}
typedef __attribute__((ext_vector_type(4))) short s16x4;
__device__ __forceinline__ u32x2 trd(uint32_t a) {
  return __builtin_bit_cast(u32x2, __builtin_amdgcn_ds_read_tr16_b64_v4i16((__attribute__((address_space(3))) s16x4*)(uintptr_t)a));
}
#define BAR() asm volatile("s_waitcnt lgkmcnt(0)\n\ts_barrier" ::: "memory")
#define ST16(off, v) asm volatile("global_store_dwordx4 %0, %1, %2 sc1\n\ts_nop 1" : : "v"(off), "v"(v), "s"(ws) : "memory")
#define ST4(off, v) asm volatile("global_store_dword %0, %1, %2 sc1" : : "v"(off), "v"(v), "s"(ws) : "memory")
#define LD16(r, off) asm volatile("global_load_dwordx4 %0, %1, %2 sc1" : "=v"(r) : "v"(off), "s"(ws) : "memory")
#define LD16W(r, off) asm volatile("global_load_dwordx4 %0, %1, %2 sc1\n\ts_waitcnt vmcnt(0)" : "=&v"(r) : "v"(off), "s"(ws) : "memory")
#define DRAIN() asm volatile("s_waitcnt vmcnt(0)" ::: "memory")
#define CEX(a, b) { const uint32_t hi_ = max(a, b), lo_ = min(a, b); a = hi_; b = lo_; }
#define MMB(a_, b_, c_) __builtin_amdgcn_mfma_f32_16x16x32_bf16(__builtin_bit_cast(bf16x8, a_), __builtin_bit_cast(bf16x8, b_), c_, 0, 0, 0)
#define NTM 0
#define DPOL ""
#define DPOL2 ""
#define DPOL4 ""
constexpr int SPIN_CAP = 1 << 18;
constexpr uint32_t UMASK = 0x40074007u;
__device__ __forceinline__ uint32_t utag(uint32_t T, int j) {
  return ((T >> (3 * j)) & 0x00070007u) | (((j <= 2) ? (T << (2 - j)) : (T >> (j - 2))) & 0x40004000u);
}
__device__ __forceinline__ uint32_t max4(const u32x4& a) { return max(max(a[0], a[1]), max(a[2], a[3])); }
#ifndef ULONG
#define ULONG 8
#endif
__device__ __forceinline__ void until(uint32_t t) {
#pragma unroll 1
  for (int spin = 0; spin < 600; ++spin) {
    const int d = (int)(t - (uint32_t)__builtin_amdgcn_s_memrealtime());
    if (d <= 0) break;
    if (ULONG && d > 4 * ULONG) __builtin_amdgcn_s_sleep(ULONG); else __builtin_amdgcn_s_sleep(1);
  }
}
__device__ __forceinline__ uint32_t latest(uint32_t t) {
  const uint32_t t0 = (uint32_t)__builtin_amdgcn_readfirstlane(t) - 0x40000000u;
  return wave_maxu(t - t0) + t0;
}
__device__ __forceinline__ uint32_t gate(const char* ws, uint32_t po, uint32_t T) {
  u32x2 pv;
#pragma unroll 1
  for (int spin = 0; spin < SPIN_CAP; ++spin) {
    asm volatile("global_load_dwordx2 %0, %1, %2 sc1\n\ts_waitcnt vmcnt(0)" : "=&v"(pv) : "v"(po), "s"(ws) : "memory");
    if (!__any(pv[0] != T)) break;
    __builtin_amdgcn_s_sleep(2);
  }
  return pv[1];
}
__device__ __forceinline__ uint32_t gate2(const char* ws, uint32_t oa, uint32_t da, uint32_t ob, uint32_t T) {
  u32x2 pa, pb;
#pragma unroll 1
  for (int spin = 0; spin < SPIN_CAP; ++spin) {
    asm volatile("global_load_dwordx2 %0, %2, %4 sc1\n\tglobal_load_dwordx2 %1, %3, %4 sc1\n\ts_waitcnt vmcnt(0)" : "=&v"(pa), "=&v"(pb) : "v"(oa), "v"(ob), "s"(ws) : "memory");
    if (!__any(pa[0] != T || pb[0] != T)) break;
    __builtin_amdgcn_s_sleep(4);
  }
  pa[1] += da;
  return latest(((int)(pa[1] - pb[1]) > 0) ? pa[1] : pb[1]);
}
__device__ __forceinline__ uint32_t ahead(uint32_t c0, uint32_t t0, int dt) {
  const uint32_t c = (uint32_t)__builtin_amdgcn_s_memtime(), t = (uint32_t)__builtin_amdgcn_s_memrealtime();
  return t + (uint32_t)((float)((t - t0) * (uint32_t)(20 * dt)) * __builtin_amdgcn_rcpf((float)(c - c0)));
}
__device__ __forceinline__ void gsync(uint32_t a, uint32_t target) {
  uint32_t t, c, n = SPIN_CAP;
  uint64_t ex;
  asm volatile("s_waitcnt lgkmcnt(0)\n\ts_mov_b64 %3, exec\n\ts_mov_b64 exec, 1\n\tv_mov_b32 %0, 1\n\tds_add_u32 %4, %0\n\ts_mov_b64 exec, %3\n"
               "1:\n\ts_sleep 1\n\tds_read_b32 %0, %4\n\ts_sub_u32 %2, %2, 1\n\ts_cbranch_scc1 2f\n\ts_waitcnt lgkmcnt(0)\n\tv_readfirstlane_b32 %1, %0\n\t"
               "s_cmp_lt_u32 %1, %5\n\ts_cbranch_scc1 1b\n2:\n\ts_waitcnt lgkmcnt(0)"
               : "=&v"(t), "=&s"(c), "+s"(n), "=&s"(ex) : "v"(a), "n"(target) : "memory", "scc");
}
__device__ __forceinline__ void gsync0(uint32_t a, uint32_t target) {
  uint32_t t, c, n = SPIN_CAP;
  asm volatile("s_waitcnt lgkmcnt(0)\n\ts_mov_b64 exec, 1\n\tv_mov_b32 %0, 1\n\tds_add_u32 %3, %0\n\ts_mov_b64 exec, -1\n"
               "1:\n\tds_read_b32 %0, %3\n\ts_sub_u32 %2, %2, 1\n\ts_cbranch_scc1 2f\n\ts_waitcnt lgkmcnt(0)\n\tv_readfirstlane_b32 %1, %0\n\t"
               "s_cmp_lt_u32 %1, %4\n\ts_cbranch_scc1 1b\n2:\n\ts_waitcnt lgkmcnt(0)"
               : "=&v"(t), "=&s"(c), "+s"(n) : "v"(a), "n"(target) : "memory", "scc");
}
__device__ __forceinline__ void gwait(uint32_t a, uint32_t target) {
  uint32_t t, c, n = SPIN_CAP;
  asm volatile("1:\n\ts_sleep 2\n\tds_read_b32 %0, %3\n\ts_sub_u32 %2, %2, 1\n\ts_cbranch_scc1 2f\n\ts_waitcnt lgkmcnt(0)\n\tv_readfirstlane_b32 %1, %0\n\t"
               "s_cmp_lt_u32 %1, %4\n\ts_cbranch_scc1 1b\n2:\n\ts_waitcnt lgkmcnt(0)"
               : "=&v"(t), "=&s"(c), "+s"(n) : "v"(a), "n"(target) : "memory", "scc");
}
__device__ __forceinline__ void gwait0(uint32_t a, uint32_t target) {
  uint32_t t, c, n = SPIN_CAP;
  asm volatile("1:\n\tds_read_b32 %0, %3\n\ts_sub_u32 %2, %2, 1\n\ts_cbranch_scc1 2f\n\ts_waitcnt lgkmcnt(0)\n\tv_readfirstlane_b32 %1, %0\n\t"
               "s_cmp_lt_u32 %1, %4\n\ts_cbranch_scc1 1b\n2:\n\ts_waitcnt lgkmcnt(0)"
               : "=&v"(t), "=&s"(c), "+s"(n) : "v"(a), "n"(target) : "memory", "scc");
}
__device__ __forceinline__ void dma16(const void* base, uint32_t voff, uint32_t lds) {
  uint32_t keep;
  asm volatile("s_mov_b32 %0, m0\n\ts_mov_b32 m0, %3\n\ts_nop 0\n\tglobal_load_lds_dwordx4 %1, %2" DPOL "\n\ts_mov_b32 m0, %0" : "=&s"(keep) : "v"(voff), "s"(base), "s"(lds) : "memory");
}
__device__ __forceinline__ void dma16d(const void* base, uint32_t voff, uint32_t lds) {
  uint32_t keep;
  asm volatile("s_mov_b32 %0, m0\n\ts_mov_b32 m0, %3\n\ts_nop 0\n\tglobal_load_lds_dwordx4 %1, %2" DPOL2 "\n\ts_mov_b32 m0, %0" : "=&s"(keep) : "v"(voff), "s"(base), "s"(lds) : "memory");
}
__device__ __forceinline__ void nap(int n) {
  for (; n > 15; n -= 15) __builtin_amdgcn_s_sleep(15);
  switch (n) {
    case 1: __builtin_amdgcn_s_sleep(1); break; case 2: __builtin_amdgcn_s_sleep(2); break; case 3: __builtin_amdgcn_s_sleep(3); break;
    case 4: __builtin_amdgcn_s_sleep(4); break; case 5: __builtin_amdgcn_s_sleep(5); break; case 6: __builtin_amdgcn_s_sleep(6); break;
    case 7: __builtin_amdgcn_s_sleep(7); break; case 8: __builtin_amdgcn_s_sleep(8); break; case 9: __builtin_amdgcn_s_sleep(9); break;
    case 10: __builtin_amdgcn_s_sleep(10); break; case 11: __builtin_amdgcn_s_sleep(11); break; case 12: __builtin_amdgcn_s_sleep(12); break;
    case 13: __builtin_amdgcn_s_sleep(13); break; case 14: __builtin_amdgcn_s_sleep(14); break; case 15: __builtin_amdgcn_s_sleep(15); break;
    default: break;
  }
}
#define SLEEP(n) do { if ((n) > 0) __builtin_amdgcn_s_sleep(n); } while (0)
#define USLEEP 3
#define SSLEEP 11
#define PSLEEP 2
#define PPRIO 0
#define LFIRST 1
#define WD 40
#define OSLEEP 12
#define QPRIO 1
#define KPRIO 2
#define GSLEEP 0
#define NSLEEP 0
#define UD 40
#define XD 0
#define RSLEEP 0
#define UPRIO 2
#define VD2 372
#define WDEPTH 6
#define ESLEEP 0
#define SD 58
#define OD 0
#define KBACK 6
#define DDEPTH 12
#define C3 1
#define XROT 2
#define WSOFF 0
#define KN 1
#define RPF 1
#define LSLEEP 1
#define TSTOP 78
#define HSTOP 110
#define VEARLY 0ull
#define FPR 3
#define SPR 3
#define DPRIO 0
#define QPRIO2 3
#define HW 2
#define RD 57
#define OPOS 1
#define OD2 6
#define NA 12
#define RD2 110
#define ORD1 ((unsigned __int128)0x321087654u << 36 | 0x321087654ull)
#define WDEPTH2 5
#define RQ NA
#define HREV 1
#define QPRIO0 3
#define VPS 1
#define RW 0
#define NPRIO 0
#define XSL 10
#define V2D 24
#define N0 8
#define LPOS 1
#define ALLP 1
#define OTP 1
#define STP 0
#define FD 204
#define VDB 365
#define BALL 1
#define FORCESLOW 0
#define FORCEFB 0
#define CONV_ASM \
  "s_cmp_eq_u32 s89, 1\ns_cbranch_scc1 1f\ns_cmp_gt_u32 s89, 1\ns_cbranch_scc1 9f\nds_read_b64 v[124:125], v3 offset:576\n" \
  "ds_read_b64 v[126:127], v3 offset:584\nds_read_b64 v[120:121], v3 offset:64\nds_read_b64 v[122:123], v3 offset:72\n" \
  "ds_read_b64 v[116:117], v3 offset:640\nds_read_b64 v[118:119], v3 offset:648\nds_read_b64 v[112:113], v3 offset:128\n" \
  "ds_read_b64 v[114:115], v3 offset:136\nds_read_b64 v[108:109], v3 offset:704\nds_read_b64 v[110:111], v3 offset:712\n" \
  "ds_read_b64 v[104:105], v3 offset:192\nds_read_b64 v[106:107], v3 offset:200\nds_read_b64 v[100:101], v3 offset:768\n" \
  "ds_read_b64 v[102:103], v3 offset:776\nds_read_b64 v[96:97], v3 offset:256\nds_read_b64 v[98:99], v3 offset:264\n" \
  "ds_read_b64 v[92:93], v3 offset:832\nds_read_b64 v[94:95], v3 offset:840\nds_read_b64 v[88:89], v3 offset:320\n" \
  "ds_read_b64 v[90:91], v3 offset:328\nds_read_b64 v[84:85], v3 offset:896\nds_read_b64 v[86:87], v3 offset:904\n" \
  "ds_read_b64 v[80:81], v3 offset:384\nds_read_b64 v[82:83], v3 offset:392\nds_read_b64 v[76:77], v3 offset:960\n" \
  "ds_read_b64 v[78:79], v3 offset:968\nds_read_b64 v[72:73], v3 offset:448\nds_read_b64 v[74:75], v3 offset:456\n" \
  "ds_read_b64 v[68:69], v3 offset:1024\nds_read_b64 v[70:71], v3 offset:1032\nds_read_b64 v[64:65], v3 offset:512\n" \
  "ds_read_b64 v[66:67], v3 offset:520\nds_read_b64 v[60:61], v3 offset:0\nds_read_b64 v[62:63], v3 offset:8\nds_read_b64 v[56:57], v3 offset:1088\n" \
  "ds_read_b64 v[58:59], v3 offset:1096\ns_waitcnt lgkmcnt(0)\ns_barrier\nds_read_b128 v[36:39], v2 offset:0\nds_read_b128 v[40:43], v2 offset:64\n" \
  "ds_read_b128 v[44:47], v2 offset:128\nds_read_b128 v[48:51], v2 offset:192\ns_waitcnt lgkmcnt(3)\n" \
  "v_mfma_f32_16x16x32_f16 v[12:15], v[124:127], v[36:39], 0\ns_setprio 1\nds_read_b128 v[52:55], v2 offset:256\n" \
  "v_mfma_f32_16x16x32_f16 v[16:19], v[120:123], v[36:39], 0\ns_waitcnt lgkmcnt(3)\nv_mfma_f32_16x16x32_f16 v[12:15], v[116:119], v[40:43], v[12:15]\n" \
  "ds_read_b128 v[36:39], v2 offset:320\nv_mfma_f32_16x16x32_f16 v[16:19], v[112:115], v[40:43], v[16:19]\ns_waitcnt lgkmcnt(3)\n" \
  "v_mfma_f32_16x16x32_f16 v[12:15], v[108:111], v[44:47], v[12:15]\nds_read_b128 v[40:43], v2 offset:384\n" \
  "v_mfma_f32_16x16x32_f16 v[16:19], v[104:107], v[44:47], v[16:19]\ns_waitcnt lgkmcnt(3)\n" \
  "v_mfma_f32_16x16x32_f16 v[12:15], v[100:103], v[48:51], v[12:15]\nds_read_b128 v[44:47], v2 offset:448\n" \
  "v_mfma_f32_16x16x32_f16 v[16:19], v[96:99], v[48:51], v[16:19]\ns_waitcnt lgkmcnt(3)\n" \
  "v_mfma_f32_16x16x32_f16 v[12:15], v[92:95], v[52:55], v[12:15]\nv_mfma_f32_16x16x32_f16 v[16:19], v[88:91], v[52:55], v[16:19]\n" \
  "s_waitcnt lgkmcnt(2)\nv_mfma_f32_16x16x32_f16 v[12:15], v[84:87], v[36:39], v[12:15]\nds_read_b128 v[48:51], v2 offset:512\n" \
  "v_mfma_f32_16x16x32_f16 v[16:19], v[80:83], v[36:39], v[16:19]\ns_waitcnt lgkmcnt(2)\n" \
  "v_mfma_f32_16x16x32_f16 v[12:15], v[76:79], v[40:43], v[12:15]\nds_read_b128 v[52:55], v2 offset:576\n" \
  "v_mfma_f32_16x16x32_f16 v[16:19], v[72:75], v[40:43], v[16:19]\ns_waitcnt lgkmcnt(2)\n" \
  "v_mfma_f32_16x16x32_f16 v[12:15], v[68:71], v[44:47], v[12:15]\nds_read_b128 v[36:39], v2 offset:640\n" \
  "v_mfma_f32_16x16x32_f16 v[16:19], v[64:67], v[44:47], v[16:19]\nv_mfma_f32_16x16x32_f16 v[20:23], v[60:63], v[44:47], 0\n" \
  "ds_read_b128 v[40:43], v2 offset:704\nv_mfma_f32_16x16x32_f16 v[28:31], v[56:59], v[44:47], 0\ns_waitcnt lgkmcnt(3)\n" \
  "v_mfma_f32_16x16x32_f16 v[16:19], v[124:127], v[48:51], v[16:19]\nds_read_b128 v[56:59], v2 offset:768\nv_cvt_pkrtz_f16_f32 v32, v12, v13\n" \
  "v_mfma_f32_16x16x32_f16 v[20:23], v[120:123], v[48:51], v[20:23]\nv_cvt_pkrtz_f16_f32 v33, v14, v15\ns_waitcnt lgkmcnt(3)\n" \
  "v_mfma_f32_16x16x32_f16 v[16:19], v[116:119], v[52:55], v[16:19]\nds_read_b128 v[44:47], v2 offset:832\n" \
  "v_pk_add_u16 v32, v32, 4 op_sel_hi:[1,0] clamp\nv_mfma_f32_16x16x32_f16 v[20:23], v[112:115], v[52:55], v[20:23]\n" \
  "v_pk_add_u16 v33, v33, 4 op_sel_hi:[1,0] clamp\ns_waitcnt lgkmcnt(3)\nv_mfma_f32_16x16x32_f16 v[16:19], v[108:111], v[36:39], v[16:19]\n" \
  "ds_read_b128 v[48:51], v2 offset:896\nds_write_b64 v4, v[32:33] offset:0\nv_mfma_f32_16x16x32_f16 v[20:23], v[104:107], v[36:39], v[20:23]\n" \
  "s_waitcnt lgkmcnt(3)\nv_mfma_f32_16x16x32_f16 v[16:19], v[100:103], v[40:43], v[16:19]\nds_read_b128 v[52:55], v2 offset:960\n" \
  "v_mfma_f32_16x16x32_f16 v[20:23], v[96:99], v[40:43], v[20:23]\nv_mfma_f32_16x16x32_f16 v[16:19], v[92:95], v[56:59], v[16:19]\n" \
  "v_mfma_f32_16x16x32_f16 v[20:23], v[88:91], v[56:59], v[20:23]\nds_read_b128 v[36:39], v2 offset:1024\ns_waitcnt lgkmcnt(3)\n" \
  "v_mfma_f32_16x16x32_f16 v[16:19], v[84:87], v[44:47], v[16:19]\nv_mfma_f32_16x16x32_f16 v[20:23], v[80:83], v[44:47], v[20:23]\n" \
  "ds_read_b128 v[40:43], v2 offset:1088\nds_read_b64 v[56:57], v6 offset:256\nds_read_b64 v[58:59], v6 offset:264\n" \
  "v_mfma_f32_16x16x32_f16 v[16:19], v[76:79], v[48:51], v[16:19]\nv_mfma_f32_16x16x32_f16 v[20:23], v[72:75], v[48:51], v[20:23]\n" \
  "ds_read_b128 v[44:47], v2 offset:1152\ns_waitcnt lgkmcnt(4)\nv_mfma_f32_16x16x32_f16 v[16:19], v[68:71], v[52:55], v[16:19]\n" \
  "v_mfma_f32_16x16x32_f16 v[20:23], v[64:67], v[52:55], v[20:23]\nds_read_b128 v[48:51], v2 offset:1216\n" \
  "v_mfma_f32_16x16x32_f16 v[24:27], v[60:63], v[52:55], 0\nv_mfma_f32_16x16x32_f16 v[20:23], v[124:127], v[36:39], v[20:23]\n" \
  "ds_read_b128 v[52:55], v2 offset:1280\nv_mfma_f32_16x16x32_f16 v[24:27], v[120:123], v[36:39], v[24:27]\ns_waitcnt lgkmcnt(5)\n" \
  "v_mfma_f32_16x16x32_f16 v[20:23], v[116:119], v[40:43], v[20:23]\nds_read_b128 v[36:39], v2 offset:1344\nv_cvt_pkrtz_f16_f32 v32, v16, v17\n" \
  "v_mfma_f32_16x16x32_f16 v[24:27], v[112:115], v[40:43], v[24:27]\nv_cvt_pkrtz_f16_f32 v33, v18, v19\ns_waitcnt lgkmcnt(3)\n" \
  "v_mfma_f32_16x16x32_f16 v[20:23], v[108:111], v[44:47], v[20:23]\nds_read_b128 v[40:43], v2 offset:1408\n" \
  "v_pk_add_u16 v32, v32, 4 op_sel_hi:[1,0] clamp\nds_read_b32 v35, v9 offset:32\nv_mfma_f32_16x16x32_f16 v[24:27], v[104:107], v[44:47], v[24:27]\n" \
  "v_pk_add_u16 v33, v33, 4 op_sel_hi:[1,0] clamp\ns_waitcnt lgkmcnt(3)\nv_mfma_f32_16x16x32_f16 v[20:23], v[100:103], v[48:51], v[20:23]\n" \
  "ds_read_b128 v[44:47], v2 offset:1472\nds_write_b64 v4, v[32:33] offset:1024\nv_mfma_f32_16x16x32_f16 v[24:27], v[96:99], v[48:51], v[24:27]\n" \
  "v_mfma_f32_16x16x32_f16 v[20:23], v[92:95], v[52:55], v[20:23]\nv_mfma_f32_16x16x32_f16 v[24:27], v[88:91], v[52:55], v[24:27]\n" \
  "ds_read_b128 v[48:51], v2 offset:1536\ns_waitcnt lgkmcnt(3)\nv_readfirstlane_b32 s85, v35\ns_cmp_lt_u32 s85, 4\ns_cbranch_scc1 21f\n22:\n" \
  "v_mfma_f32_16x16x32_f16 v[20:23], v[84:87], v[36:39], v[20:23]\nv_mfma_f32_16x16x32_f16 v[24:27], v[80:83], v[36:39], v[24:27]\n" \
  "ds_read_b128 v[52:55], v2 offset:1600\nv_mfma_f32_16x16x32_f16 v[20:23], v[76:79], v[40:43], v[20:23]\n" \
  "v_mfma_f32_16x16x32_f16 v[24:27], v[72:75], v[40:43], v[24:27]\nds_read_b128 v[36:39], v2 offset:1664\ns_waitcnt lgkmcnt(4)\n" \
  "v_mfma_f32_16x16x32_f16 v[20:23], v[68:71], v[44:47], v[20:23]\nv_mfma_f32_16x16x32_f16 v[24:27], v[64:67], v[44:47], v[24:27]\n" \
  "ds_read_b128 v[40:43], v2 offset:1728\nv_mfma_f32_16x16x32_f16 v[28:31], v[60:63], v[44:47], v[28:31]\nds_read_b64_tr_b16 v[60:61], v5 offset:0\n" \
  "ds_read_b64_tr_b16 v[62:63], v5 offset:32\ns_waitcnt lgkmcnt(4)\nv_mfma_f32_16x16x32_f16 v[24:27], v[124:127], v[48:51], v[24:27]\n" \
  "v_mfma_f32_16x16x32_f16 v[28:31], v[120:123], v[48:51], v[28:31]\nds_read_b64_tr_b16 v[44:45], v5 offset:256\n" \
  "ds_read_b64_tr_b16 v[46:47], v5 offset:288\nv_mfma_f32_16x16x32_f16 v[24:27], v[116:119], v[52:55], v[24:27]\n" \
  "ds_read_b64 v[120:121], v6 offset:320\nds_read_b64 v[122:123], v6 offset:328\nv_mfma_f32_16x16x32_f16 v[28:31], v[112:115], v[52:55], v[28:31]\n" \
  "ds_read_b128 v[48:51], v2 offset:1792\ns_waitcnt lgkmcnt(7)\nv_mfma_f32_16x16x32_f16 v[24:27], v[108:111], v[36:39], v[24:27]\n" \
  "ds_read_b128 v[112:115], v2 offset:1856\nv_mfma_f32_16x16x32_f16 v[28:31], v[104:107], v[36:39], v[28:31]\nds_read_b64 v[52:53], v6 offset:128\n" \
  "ds_read_b64 v[54:55], v6 offset:136\nv_mfma_f32_16x16x32_f16 v[24:27], v[100:103], v[40:43], v[24:27]\nds_read_b128 v[104:107], v2 offset:1920\n" \
  "v_mfma_f32_16x16x32_f16 v[28:31], v[96:99], v[40:43], v[28:31]\nds_read_b64 v[36:37], v6 offset:192\nds_read_b64 v[38:39], v6 offset:200\n" \
  "s_waitcnt lgkmcnt(9)\nv_mfma_f32_16x16x32_f16 v[20:23], v[60:63], v[56:59], v[20:23]\nds_read_b128 v[96:99], v2 offset:1984\ns_waitcnt lgkmcnt(7)\n" \
  "v_mfma_f32_16x16x32_f16 v[20:23], v[44:47], v[120:123], v[20:23]\nv_mfma_f32_16x16x32_f16 v[24:27], v[92:95], v[48:51], v[24:27]\n" \
  "ds_read_b128 v[40:43], v2 offset:2048\nv_mfma_f32_16x16x32_f16 v[28:31], v[88:91], v[48:51], v[28:31]\nds_read_b64_tr_b16 v[88:89], v5 offset:512\n" \
  "ds_read_b64_tr_b16 v[90:91], v5 offset:544\ns_waitcnt lgkmcnt(6)\nv_mfma_f32_16x16x32_f16 v[24:27], v[84:87], v[112:115], v[24:27]\n" \
  "v_mfma_f32_16x16x32_f16 v[28:31], v[80:83], v[112:115], v[28:31]\nv_cvt_pkrtz_f16_f32 v32, v20, v21\nv_cvt_pkrtz_f16_f32 v33, v22, v23\n" \
  "v_mfma_f32_16x16x32_f16 v[24:27], v[76:79], v[104:107], v[24:27]\nds_read_b64_tr_b16 v[48:49], v5 offset:768\n" \
  "ds_read_b64_tr_b16 v[50:51], v5 offset:800\nv_mfma_f32_16x16x32_f16 v[28:31], v[72:75], v[104:107], v[28:31]\n" \
  "ds_read_b128 v[80:83], v2 offset:2112\ns_waitcnt lgkmcnt(5)\nv_mfma_f32_16x16x32_f16 v[24:27], v[68:71], v[96:99], v[24:27]\n" \
  "ds_read_b128 v[112:115], v2 offset:2176\nv_pk_add_u16 v32, v32, 4 op_sel_hi:[1,0] clamp\n" \
  "v_mfma_f32_16x16x32_f16 v[28:31], v[64:67], v[96:99], v[28:31]\nds_read_b128 v[72:75], v2 offset:2240\n" \
  "v_pk_add_u16 v33, v33, 4 op_sel_hi:[1,0] clamp\nv_mfma_f32_16x16x32_f16 v[28:31], v[124:127], v[40:43], v[28:31]\n" \
  "ds_read_b128 v[104:107], v2 offset:2304\nds_write_b64 v4, v[32:33] offset:2048\nv_mfma_f32_16x16x32_f16 v[24:27], v[60:63], v[52:55], v[24:27]\n" \
  "ds_read_b128 v[64:67], v2 offset:2368\nds_read_b64 v[96:97], v6 offset:0\nds_read_b64 v[98:99], v6 offset:8\n" \
  "v_mfma_f32_16x16x32_f16 v[24:27], v[44:47], v[36:39], v[24:27]\nds_read_b128 v[124:127], v2 offset:2432\ns_waitcnt lgkmcnt(9)\n" \
  "v_mfma_f32_16x16x32_f16 v[24:27], v[88:91], v[56:59], v[24:27]\nds_read_b128 v[40:43], v2 offset:2496\n" \
  "v_mfma_f32_16x16x32_f16 v[24:27], v[48:51], v[120:123], v[24:27]\ns_waitcnt lgkmcnt(8)\n" \
  "v_mfma_f32_16x16x32_f16 v[28:31], v[116:119], v[80:83], v[28:31]\nds_read_b64 v[116:117], v6 offset:64\nds_read_b64 v[118:119], v6 offset:72\n" \
  "v_mfma_f32_16x16x32_f16 v[28:31], v[108:111], v[112:115], v[28:31]\nds_read_b64_tr_b16 v[80:81], v5 offset:1024\n" \
  "ds_read_b64_tr_b16 v[82:83], v5 offset:1056\ns_waitcnt lgkmcnt(10)\nv_mfma_f32_16x16x32_f16 v[28:31], v[100:103], v[72:75], v[28:31]\n" \
  "v_cvt_pkrtz_f16_f32 v32, v24, v25\nv_cvt_pkrtz_f16_f32 v33, v26, v27\nv_mfma_f32_16x16x32_f16 v[28:31], v[92:95], v[104:107], v[28:31]\n" \
  "v_pk_add_u16 v32, v32, 4 op_sel_hi:[1,0] clamp\ns_waitcnt lgkmcnt(5)\nv_mfma_f32_16x16x32_f16 v[28:31], v[84:87], v[64:67], v[28:31]\n" \
  "ds_read_b64_tr_b16 v[108:109], v5 offset:1280\nds_read_b64_tr_b16 v[110:111], v5 offset:1312\n" \
  "v_mfma_f32_16x16x32_f16 v[28:31], v[76:79], v[124:127], v[28:31]\nds_read_b64_tr_b16 v[112:113], v7 offset:0\n" \
  "ds_read_b64_tr_b16 v[114:115], v7 offset:64\ns_waitcnt lgkmcnt(6)\nv_mfma_f32_16x16x32_f16 v[28:31], v[68:71], v[40:43], v[28:31]\n" \
  "ds_read2_b32 v[100:101], v8 offset0:48 offset1:49\nds_read2_b32 v[102:103], v8 offset0:50 offset1:51\n" \
  "v_mfma_f32_16x16x32_f16 v[28:31], v[60:63], v[96:99], v[28:31]\nv_pk_add_u16 v33, v33, 4 op_sel_hi:[1,0] clamp\n" \
  "ds_write_b64 v4, v[32:33] offset:3072\nv_mfma_f32_16x16x32_f16 v[28:31], v[44:47], v[116:119], v[28:31]\n" \
  "v_mfma_f32_16x16x32_f16 v[28:31], v[88:91], v[52:55], v[28:31]\nv_mfma_f32_16x16x32_f16 v[28:31], v[48:51], v[36:39], v[28:31]\n" \
  "s_waitcnt lgkmcnt(5)\nv_mfma_f32_16x16x32_f16 v[28:31], v[80:83], v[56:59], v[28:31]\n" \
  "v_mfma_f32_16x16x32_f16 v[28:31], v[108:111], v[120:123], v[28:31]\ns_waitcnt lgkmcnt(1)\n" \
  "v_mfma_f32_16x16x32_f16 v[28:31], v[112:115], v[100:103], v[28:31]\ns_nop 7\nv_cvt_pkrtz_f16_f32 v32, v28, v29\nv_cvt_pkrtz_f16_f32 v33, v30, v31\n" \
  "v_pk_add_u16 v32, v32, 4 op_sel_hi:[1,0] clamp\nv_pk_add_u16 v33, v33, 4 op_sel_hi:[1,0] clamp\nds_write_b64 v4, v[32:33] offset:4096\n" \
  "s_setprio 0\ns_setpc_b64 s[90:91]\n21:\ns_movk_i32 s84, 0x4000\n23:\nds_read_b32 v35, v9 offset:32\ns_sub_u32 s84, s84, 1\ns_cbranch_scc1 22b\n" \
  "s_waitcnt lgkmcnt(0)\nv_readfirstlane_b32 s85, v35\ns_cmp_lt_u32 s85, 4\ns_cbranch_scc1 23b\ns_branch 22b\n1:\n" \
  "ds_read_b64 v[124:125], v3 offset:1088\nds_read_b64 v[126:127], v3 offset:1096\nds_read_b64 v[120:121], v3 offset:0\n" \
  "ds_read_b64 v[122:123], v3 offset:8\nds_read_b64 v[116:117], v3 offset:64\nds_read_b64 v[118:119], v3 offset:72\n" \
  "ds_read_b64 v[112:113], v3 offset:128\nds_read_b64 v[114:115], v3 offset:136\nds_read_b64 v[108:109], v3 offset:192\n" \
  "ds_read_b64 v[110:111], v3 offset:200\nds_read_b64 v[104:105], v3 offset:256\nds_read_b64 v[106:107], v3 offset:264\n" \
  "ds_read_b64 v[100:101], v3 offset:320\nds_read_b64 v[102:103], v3 offset:328\nds_read_b64 v[96:97], v3 offset:384\n" \
  "ds_read_b64 v[98:99], v3 offset:392\nds_read_b64 v[92:93], v3 offset:448\nds_read_b64 v[94:95], v3 offset:456\n" \
  "ds_read_b64 v[88:89], v3 offset:512\nds_read_b64 v[90:91], v3 offset:520\nds_read_b64 v[84:85], v3 offset:576\n" \
  "ds_read_b64 v[86:87], v3 offset:584\nds_read_b64 v[80:81], v3 offset:640\nds_read_b64 v[82:83], v3 offset:648\n" \
  "ds_read_b64 v[76:77], v3 offset:704\nds_read_b64 v[78:79], v3 offset:712\nds_read_b64 v[72:73], v3 offset:768\n" \
  "ds_read_b64 v[74:75], v3 offset:776\nds_read_b64 v[68:69], v3 offset:832\nds_read_b64 v[70:71], v3 offset:840\n" \
  "ds_read_b64 v[64:65], v3 offset:896\nds_read_b64 v[66:67], v3 offset:904\nds_read_b64 v[60:61], v3 offset:960\n" \
  "ds_read_b64 v[62:63], v3 offset:968\nds_read_b64 v[56:57], v3 offset:1024\nds_read_b64 v[58:59], v3 offset:1032\n" \
  "ds_read_b64 v[52:53], v6 offset:0\nds_read_b64 v[54:55], v6 offset:8\nds_read_b64 v[48:49], v6 offset:64\nds_read_b64 v[50:51], v6 offset:72\n" \
  "s_waitcnt lgkmcnt(0)\ns_barrier\nds_read_b128 v[28:31], v2 offset:960\nds_read_b128 v[32:35], v2 offset:1472\n" \
  "ds_read_b128 v[36:39], v2 offset:1984\nds_read_b128 v[40:43], v2 offset:2048\nds_read_b128 v[44:47], v2 offset:2112\ns_waitcnt lgkmcnt(3)\n" \
  "v_mfma_f32_16x16x32_f16 v[12:15], v[124:127], v[28:31], 0\ns_setprio 1\nds_read_b128 v[28:31], v2 offset:2176\n" \
  "v_mfma_f32_16x16x32_f16 v[16:19], v[124:127], v[32:35], 0\nds_read_b128 v[32:35], v2 offset:2240\ns_waitcnt lgkmcnt(3)\n" \
  "v_mfma_f32_16x16x32_f16 v[12:15], v[120:123], v[36:39], v[12:15]\nv_mfma_f32_16x16x32_f16 v[20:23], v[124:127], v[36:39], 0\n" \
  "ds_read_b128 v[124:127], v2 offset:2304\nds_read_b128 v[36:39], v2 offset:2368\nv_mfma_f32_16x16x32_f16 v[12:15], v[116:119], v[40:43], v[12:15]\n" \
  "ds_read_b128 v[40:43], v2 offset:2432\ns_waitcnt lgkmcnt(4)\nv_mfma_f32_16x16x32_f16 v[12:15], v[112:115], v[44:47], v[12:15]\n" \
  "ds_read_b128 v[44:47], v2 offset:2496\nv_mfma_f32_16x16x32_f16 v[12:15], v[108:111], v[28:31], v[12:15]\nds_read_b128 v[28:31], v2 offset:2560\n" \
  "s_waitcnt lgkmcnt(4)\nv_mfma_f32_16x16x32_f16 v[12:15], v[104:107], v[32:35], v[12:15]\nds_read_b128 v[32:35], v2 offset:2624\n" \
  "v_mfma_f32_16x16x32_f16 v[12:15], v[100:103], v[124:127], v[12:15]\ns_waitcnt lgkmcnt(3)\n" \
  "v_mfma_f32_16x16x32_f16 v[12:15], v[96:99], v[36:39], v[12:15]\nds_read_b128 v[124:127], v2 offset:2688\n" \
  "v_mfma_f32_16x16x32_f16 v[12:15], v[92:95], v[40:43], v[12:15]\ns_waitcnt lgkmcnt(3)\n" \
  "v_mfma_f32_16x16x32_f16 v[12:15], v[88:91], v[44:47], v[12:15]\nds_read_b128 v[36:39], v2 offset:2752\n" \
  "v_mfma_f32_16x16x32_f16 v[16:19], v[120:123], v[44:47], v[16:19]\ns_waitcnt lgkmcnt(3)\n" \
  "v_mfma_f32_16x16x32_f16 v[12:15], v[84:87], v[28:31], v[12:15]\nds_read_b128 v[40:43], v2 offset:2816\n" \
  "v_mfma_f32_16x16x32_f16 v[16:19], v[116:119], v[28:31], v[16:19]\ns_waitcnt lgkmcnt(3)\n" \
  "v_mfma_f32_16x16x32_f16 v[12:15], v[80:83], v[32:35], v[12:15]\nds_read_b128 v[44:47], v2 offset:2880\n" \
  "v_mfma_f32_16x16x32_f16 v[16:19], v[112:115], v[32:35], v[16:19]\ns_waitcnt lgkmcnt(3)\n" \
  "v_mfma_f32_16x16x32_f16 v[12:15], v[76:79], v[124:127], v[12:15]\nds_read_b128 v[28:31], v2 offset:2944\n" \
  "v_mfma_f32_16x16x32_f16 v[16:19], v[108:111], v[124:127], v[16:19]\ns_waitcnt lgkmcnt(3)\n" \
  "v_mfma_f32_16x16x32_f16 v[12:15], v[72:75], v[36:39], v[12:15]\nds_read_b128 v[32:35], v2 offset:3008\n" \
  "v_mfma_f32_16x16x32_f16 v[16:19], v[104:107], v[36:39], v[16:19]\ns_waitcnt lgkmcnt(3)\n" \
  "v_mfma_f32_16x16x32_f16 v[12:15], v[68:71], v[40:43], v[12:15]\nv_mfma_f32_16x16x32_f16 v[16:19], v[100:103], v[40:43], v[16:19]\n" \
  "ds_read_b128 v[124:127], v2 offset:3072\ns_waitcnt lgkmcnt(3)\nv_mfma_f32_16x16x32_f16 v[12:15], v[64:67], v[44:47], v[12:15]\n" \
  "v_mfma_f32_16x16x32_f16 v[16:19], v[96:99], v[44:47], v[16:19]\nds_read_b128 v[36:39], v2 offset:3136\ns_waitcnt lgkmcnt(3)\n" \
  "v_mfma_f32_16x16x32_f16 v[12:15], v[60:63], v[28:31], v[12:15]\nds_read_b64 v[40:41], v6 offset:128\nds_read_b64 v[42:43], v6 offset:136\n" \
  "v_mfma_f32_16x16x32_f16 v[16:19], v[92:95], v[28:31], v[16:19]\nds_read_b128 v[44:47], v2 offset:3200\ns_waitcnt lgkmcnt(4)\n" \
  "v_mfma_f32_16x16x32_f16 v[12:15], v[56:59], v[32:35], v[12:15]\nv_mfma_f32_16x16x32_f16 v[16:19], v[88:91], v[32:35], v[16:19]\n" \
  "ds_read_b128 v[28:31], v2 offset:3264\nv_mfma_f32_16x16x32_f16 v[20:23], v[120:123], v[32:35], v[20:23]\nds_read_b64 v[120:121], v6 offset:192\n" \
  "ds_read_b64 v[122:123], v6 offset:200\nv_mfma_f32_16x16x32_f16 v[16:19], v[84:87], v[124:127], v[16:19]\nds_read_b128 v[32:35], v2 offset:3328\n" \
  "v_mfma_f32_16x16x32_f16 v[20:23], v[116:119], v[124:127], v[20:23]\nds_read_b64 v[116:117], v6 offset:256\nds_read_b64 v[118:119], v6 offset:264\n" \
  "s_waitcnt lgkmcnt(6)\nv_mfma_f32_16x16x32_f16 v[16:19], v[80:83], v[36:39], v[16:19]\nds_read_b128 v[124:127], v2 offset:3392\n" \
  "v_mfma_f32_16x16x32_f16 v[20:23], v[112:115], v[36:39], v[20:23]\nds_read_b64 v[112:113], v6 offset:320\nds_read_b64 v[114:115], v6 offset:328\n" \
  "v_mfma_f32_16x16x32_f16 v[16:19], v[76:79], v[44:47], v[16:19]\nds_read_b128 v[36:39], v2 offset:3456\n" \
  "v_mfma_f32_16x16x32_f16 v[20:23], v[108:111], v[44:47], v[20:23]\ns_waitcnt lgkmcnt(6)\n" \
  "v_mfma_f32_16x16x32_f16 v[16:19], v[72:75], v[28:31], v[16:19]\nds_read_b128 v[108:111], v2 offset:3520\n" \
  "v_mfma_f32_16x16x32_f16 v[20:23], v[104:107], v[28:31], v[20:23]\nv_mfma_f32_16x16x32_f16 v[16:19], v[68:71], v[32:35], v[16:19]\n" \
  "ds_read_b128 v[44:47], v2 offset:3584\nv_mfma_f32_16x16x32_f16 v[20:23], v[100:103], v[32:35], v[20:23]\nds_read_b128 v[104:107], v2 offset:3648\n" \
  "ds_read_b32 v27, v9 offset:32\ns_waitcnt lgkmcnt(4)\nv_mfma_f32_16x16x32_f16 v[16:19], v[64:67], v[124:127], v[16:19]\n" \
  "ds_read_b128 v[28:31], v2 offset:3712\nv_mfma_f32_16x16x32_f16 v[20:23], v[96:99], v[124:127], v[20:23]\nds_read_b128 v[100:103], v2 offset:3776\n" \
  "ds_read2_b32 v[32:33], v8 offset0:32 offset1:33\nds_read2_b32 v[34:35], v8 offset0:34 offset1:35\n" \
  "v_mfma_f32_16x16x32_f16 v[16:19], v[60:63], v[36:39], v[16:19]\nds_read_b128 v[96:99], v2 offset:3840\n" \
  "v_mfma_f32_16x16x32_f16 v[20:23], v[92:95], v[36:39], v[20:23]\nds_read_b128 v[124:127], v2 offset:3904\ns_waitcnt lgkmcnt(8)\n" \
  "v_mfma_f32_16x16x32_f16 v[16:19], v[56:59], v[108:111], v[16:19]\nds_read_b128 v[92:95], v2 offset:3968\ns_waitcnt lgkmcnt(7)\n" \
  "v_readfirstlane_b32 s85, v27\ns_cmp_lt_u32 s85, 4\ns_cbranch_scc1 31f\n32:\nv_mfma_f32_16x16x32_f16 v[20:23], v[88:91], v[108:111], v[20:23]\n" \
  "ds_read_b128 v[36:39], v2 offset:4032\nds_read_b64_tr_b16 v[88:89], v5 offset:512\nds_read_b64_tr_b16 v[90:91], v5 offset:544\n" \
  "v_mfma_f32_16x16x32_f16 v[20:23], v[84:87], v[44:47], v[20:23]\nds_read_b64_tr_b16 v[108:109], v5 offset:768\n" \
  "ds_read_b64_tr_b16 v[110:111], v5 offset:800\nv_mfma_f32_16x16x32_f16 v[20:23], v[80:83], v[104:107], v[20:23]\n" \
  "ds_read_b64_tr_b16 v[84:85], v5 offset:1024\nds_read_b64_tr_b16 v[86:87], v5 offset:1056\ns_waitcnt lgkmcnt(12)\n" \
  "v_mfma_f32_16x16x32_f16 v[20:23], v[76:79], v[28:31], v[20:23]\nds_read_b64_tr_b16 v[44:45], v5 offset:1280\n" \
  "ds_read_b64_tr_b16 v[46:47], v5 offset:1312\nv_mfma_f32_16x16x32_f16 v[20:23], v[72:75], v[100:103], v[20:23]\ns_waitcnt lgkmcnt(10)\n" \
  "v_mfma_f32_16x16x32_f16 v[20:23], v[68:71], v[96:99], v[20:23]\nds_read_b64_tr_b16 v[80:81], v5 offset:1536\n" \
  "ds_read_b64_tr_b16 v[82:83], v5 offset:1568\nv_mfma_f32_16x16x32_f16 v[20:23], v[64:67], v[124:127], v[20:23]\n" \
  "ds_read_b64_tr_b16 v[104:105], v5 offset:1792\nds_read_b64_tr_b16 v[106:107], v5 offset:1824\ns_waitcnt lgkmcnt(12)\n" \
  "v_mfma_f32_16x16x32_f16 v[20:23], v[60:63], v[92:95], v[20:23]\nds_read_b64_tr_b16 v[76:77], v5 offset:2048\n" \
  "ds_read_b64_tr_b16 v[78:79], v5 offset:2080\nv_mfma_f32_16x16x32_f16 v[20:23], v[56:59], v[36:39], v[20:23]\ns_waitcnt lgkmcnt(10)\n" \
  "v_mfma_f32_16x16x32_f16 v[12:15], v[88:91], v[52:55], v[12:15]\nds_read_b64_tr_b16 v[28:29], v5 offset:2304\n" \
  "ds_read_b64_tr_b16 v[30:31], v5 offset:2336\nv_mfma_f32_16x16x32_f16 v[12:15], v[108:111], v[48:51], v[12:15]\n" \
  "ds_read_b64_tr_b16 v[72:73], v5 offset:2560\nds_read_b64_tr_b16 v[74:75], v5 offset:2592\ns_waitcnt lgkmcnt(10)\n" \
  "v_mfma_f32_16x16x32_f16 v[12:15], v[84:87], v[40:43], v[12:15]\nds_read_b64_tr_b16 v[100:101], v5 offset:2816\n" \
  "ds_read_b64_tr_b16 v[102:103], v5 offset:2848\nv_mfma_f32_16x16x32_f16 v[16:19], v[84:87], v[52:55], v[16:19]\n" \
  "ds_read_b64_tr_b16 v[68:69], v7 offset:0\nds_read_b64_tr_b16 v[70:71], v7 offset:64\n" \
  "v_mfma_f32_16x16x32_f16 v[12:15], v[44:47], v[120:123], v[12:15]\nv_mfma_f32_16x16x32_f16 v[16:19], v[44:47], v[48:51], v[16:19]\n" \
  "s_waitcnt lgkmcnt(10)\nv_mfma_f32_16x16x32_f16 v[12:15], v[80:83], v[116:119], v[12:15]\nds_read2_b32 v[96:97], v8 offset0:16 offset1:17\n" \
  "ds_read2_b32 v[98:99], v8 offset0:18 offset1:19\nv_mfma_f32_16x16x32_f16 v[16:19], v[80:83], v[40:43], v[16:19]\n" \
  "ds_read2_b32 v[64:65], v8 offset0:0 offset1:1\nds_read2_b32 v[66:67], v8 offset0:2 offset1:3\n" \
  "v_mfma_f32_16x16x32_f16 v[20:23], v[80:83], v[52:55], v[20:23]\nv_mfma_f32_16x16x32_f16 v[12:15], v[104:107], v[112:115], v[12:15]\n" \
  "v_mfma_f32_16x16x32_f16 v[16:19], v[104:107], v[120:123], v[16:19]\nv_mfma_f32_16x16x32_f16 v[20:23], v[104:107], v[48:51], v[20:23]\n" \
  "s_waitcnt lgkmcnt(10)\nv_mfma_f32_16x16x32_f16 v[16:19], v[76:79], v[116:119], v[16:19]\nds_read_b64_tr_b16 v[124:125], v7 offset:512\n" \
  "ds_read_b64_tr_b16 v[126:127], v7 offset:576\nv_mfma_f32_16x16x32_f16 v[20:23], v[76:79], v[40:43], v[20:23]\n" \
  "ds_read2_b32 v[60:61], v8 offset0:48 offset1:49\nds_read2_b32 v[62:63], v8 offset0:50 offset1:51\n" \
  "v_mfma_f32_16x16x32_f16 v[16:19], v[28:31], v[112:115], v[16:19]\nv_mfma_f32_16x16x32_f16 v[20:23], v[28:31], v[120:123], v[20:23]\n" \
  "s_waitcnt lgkmcnt(10)\nv_mfma_f32_16x16x32_f16 v[20:23], v[72:75], v[116:119], v[20:23]\nds_read_b64_tr_b16 v[92:93], v7 offset:1024\n" \
  "ds_read_b64_tr_b16 v[94:95], v7 offset:1088\nv_mfma_f32_16x16x32_f16 v[20:23], v[100:103], v[112:115], v[20:23]\n" \
  "ds_read_b64_tr_b16 v[56:57], v7 offset:1536\nds_read_b64_tr_b16 v[58:59], v7 offset:1600\ns_waitcnt lgkmcnt(10)\n" \
  "v_mfma_f32_16x16x32_f16 v[12:15], v[68:71], v[32:35], v[12:15]\nv_mfma_f32_16x16x32_f16 v[16:19], v[68:71], v[96:99], v[16:19]\n" \
  "s_waitcnt lgkmcnt(6)\nv_mfma_f32_16x16x32_f16 v[20:23], v[68:71], v[64:67], v[20:23]\ns_waitcnt lgkmcnt(2)\n" \
  "v_mfma_f32_16x16x32_f16 v[12:15], v[124:127], v[60:63], v[12:15]\nv_mfma_f32_16x16x32_f16 v[16:19], v[124:127], v[32:35], v[16:19]\n" \
  "v_mfma_f32_16x16x32_f16 v[20:23], v[124:127], v[96:99], v[20:23]\nv_mfma_f32_16x16x32_f16 v[16:19], v[92:95], v[60:63], v[16:19]\n" \
  "v_mfma_f32_16x16x32_f16 v[20:23], v[92:95], v[32:35], v[20:23]\ns_waitcnt lgkmcnt(0)\n" \
  "v_mfma_f32_16x16x32_f16 v[20:23], v[56:59], v[60:63], v[20:23]\ns_nop 1\nv_cvt_pkrtz_f16_f32 v24, v12, v13\nv_cvt_pkrtz_f16_f32 v25, v14, v15\n" \
  "v_pk_add_u16 v24, v24, 4 op_sel_hi:[1,0] clamp\nv_pk_add_u16 v25, v25, 4 op_sel_hi:[1,0] clamp\nds_write_b64 v4, v[24:25] offset:5120\n" \
  "v_cvt_pkrtz_f16_f32 v24, v16, v17\nv_cvt_pkrtz_f16_f32 v25, v18, v19\nv_pk_add_u16 v24, v24, 4 op_sel_hi:[1,0] clamp\n" \
  "v_pk_add_u16 v25, v25, 4 op_sel_hi:[1,0] clamp\nds_write_b64 v4, v[24:25] offset:6144\nv_cvt_pkrtz_f16_f32 v24, v20, v21\n" \
  "v_cvt_pkrtz_f16_f32 v25, v22, v23\nv_pk_add_u16 v24, v24, 4 op_sel_hi:[1,0] clamp\nv_pk_add_u16 v25, v25, 4 op_sel_hi:[1,0] clamp\n" \
  "ds_write_b64 v4, v[24:25] offset:7168\ns_setprio 0\ns_setpc_b64 s[90:91]\n31:\ns_movk_i32 s84, 0x4000\n33:\nds_read_b32 v27, v9 offset:32\n" \
  "s_sub_u32 s84, s84, 1\ns_cbranch_scc1 32b\ns_waitcnt lgkmcnt(0)\nv_readfirstlane_b32 s85, v27\ns_cmp_lt_u32 s85, 4\ns_cbranch_scc1 33b\n" \
  "s_branch 32b\n9:\ns_cmp_eq_u32 s89, 3\ns_cbranch_scc1 3f\ns_cmp_eq_u32 s89, 4\ns_cbranch_scc1 4f\ns_cmp_eq_u32 s89, 5\ns_cbranch_scc1 5f\n" \
  "ds_read_b64 v[124:125], v3 offset:576\nds_read_b64 v[126:127], v3 offset:584\nds_read_b64 v[120:121], v3 offset:64\n" \
  "ds_read_b64 v[122:123], v3 offset:72\nds_read_b64 v[116:117], v3 offset:640\nds_read_b64 v[118:119], v3 offset:648\n" \
  "ds_read_b64 v[112:113], v3 offset:128\nds_read_b64 v[114:115], v3 offset:136\nds_read_b64 v[108:109], v3 offset:704\n" \
  "ds_read_b64 v[110:111], v3 offset:712\nds_read_b64 v[104:105], v3 offset:192\nds_read_b64 v[106:107], v3 offset:200\n" \
  "ds_read_b64 v[100:101], v3 offset:768\nds_read_b64 v[102:103], v3 offset:776\nds_read_b64 v[96:97], v3 offset:256\n" \
  "ds_read_b64 v[98:99], v3 offset:264\nds_read_b64 v[92:93], v3 offset:832\nds_read_b64 v[94:95], v3 offset:840\n" \
  "ds_read_b64 v[88:89], v3 offset:320\nds_read_b64 v[90:91], v3 offset:328\nds_read_b64 v[84:85], v3 offset:896\n" \
  "ds_read_b64 v[86:87], v3 offset:904\nds_read_b64 v[80:81], v3 offset:384\nds_read_b64 v[82:83], v3 offset:392\n" \
  "ds_read_b64 v[76:77], v3 offset:960\nds_read_b64 v[78:79], v3 offset:968\nds_read_b64 v[72:73], v3 offset:448\n" \
  "ds_read_b64 v[74:75], v3 offset:456\nds_read_b64 v[68:69], v3 offset:1024\nds_read_b64 v[70:71], v3 offset:1032\n" \
  "ds_read_b64 v[64:65], v3 offset:512\nds_read_b64 v[66:67], v3 offset:520\ns_waitcnt lgkmcnt(0)\ns_barrier\nds_read_b128 v[24:27], v2 offset:0\n" \
  "ds_read_b128 v[28:31], v2 offset:64\nds_read_b128 v[32:35], v2 offset:128\nds_read_b128 v[36:39], v2 offset:192\ns_waitcnt lgkmcnt(3)\n" \
  "v_mfma_f32_16x16x32_f16 v[12:15], v[124:127], v[24:27], 0\ns_setprio 1\nds_read_b128 v[40:43], v2 offset:256\n" \
  "v_mfma_f32_16x16x32_f16 v[16:19], v[120:123], v[24:27], 0\ns_waitcnt lgkmcnt(3)\nv_mfma_f32_16x16x32_f16 v[12:15], v[116:119], v[28:31], v[12:15]\n" \
  "ds_read_b128 v[44:47], v2 offset:320\nv_mfma_f32_16x16x32_f16 v[16:19], v[112:115], v[28:31], v[16:19]\ns_waitcnt lgkmcnt(3)\n" \
  "v_mfma_f32_16x16x32_f16 v[12:15], v[108:111], v[32:35], v[12:15]\nds_read_b128 v[48:51], v2 offset:384\n" \
  "v_mfma_f32_16x16x32_f16 v[16:19], v[104:107], v[32:35], v[16:19]\ns_waitcnt lgkmcnt(3)\n" \
  "v_mfma_f32_16x16x32_f16 v[12:15], v[100:103], v[36:39], v[12:15]\nds_read_b128 v[52:55], v2 offset:448\n" \
  "v_mfma_f32_16x16x32_f16 v[16:19], v[96:99], v[36:39], v[16:19]\ns_waitcnt lgkmcnt(3)\n" \
  "v_mfma_f32_16x16x32_f16 v[12:15], v[92:95], v[40:43], v[12:15]\nds_read_b128 v[56:59], v2 offset:512\n" \
  "v_mfma_f32_16x16x32_f16 v[16:19], v[88:91], v[40:43], v[16:19]\nds_read_b128 v[60:63], v2 offset:576\ns_waitcnt lgkmcnt(3)\n" \
  "v_mfma_f32_16x16x32_f16 v[12:15], v[84:87], v[44:47], v[12:15]\nds_read_b128 v[120:123], v2 offset:640\n" \
  "v_mfma_f32_16x16x32_f16 v[16:19], v[80:83], v[44:47], v[16:19]\nds_read_b128 v[24:27], v2 offset:704\n" \
  "v_mfma_f32_16x16x32_f16 v[12:15], v[76:79], v[48:51], v[12:15]\nds_read_b128 v[112:115], v2 offset:768\n" \
  "v_mfma_f32_16x16x32_f16 v[16:19], v[72:75], v[48:51], v[16:19]\nds_read_b128 v[28:31], v2 offset:832\ns_waitcnt lgkmcnt(5)\n" \
  "v_mfma_f32_16x16x32_f16 v[12:15], v[68:71], v[52:55], v[12:15]\nds_read_b128 v[104:107], v2 offset:896\n" \
  "v_mfma_f32_16x16x32_f16 v[16:19], v[64:67], v[52:55], v[16:19]\nds_read_b128 v[32:35], v2 offset:960\n" \
  "v_mfma_f32_16x16x32_f16 v[16:19], v[124:127], v[56:59], v[16:19]\ns_waitcnt lgkmcnt(5)\n" \
  "v_mfma_f32_16x16x32_f16 v[16:19], v[116:119], v[60:63], v[16:19]\nv_mfma_f32_16x16x32_f16 v[16:19], v[108:111], v[120:123], v[16:19]\n" \
  "s_waitcnt lgkmcnt(3)\nv_mfma_f32_16x16x32_f16 v[16:19], v[100:103], v[24:27], v[16:19]\nv_cvt_pkrtz_f16_f32 v20, v12, v13\n" \
  "v_cvt_pkrtz_f16_f32 v21, v14, v15\nv_mfma_f32_16x16x32_f16 v[16:19], v[92:95], v[112:115], v[16:19]\n" \
  "v_pk_add_u16 v20, v20, 4 op_sel_hi:[1,0] clamp\ns_waitcnt lgkmcnt(2)\nv_mfma_f32_16x16x32_f16 v[16:19], v[84:87], v[28:31], v[16:19]\n" \
  "v_pk_add_u16 v21, v21, 4 op_sel_hi:[1,0] clamp\ns_waitcnt lgkmcnt(1)\nv_mfma_f32_16x16x32_f16 v[16:19], v[76:79], v[104:107], v[16:19]\n" \
  "ds_write_b64 v4, v[20:21] offset:0\ns_waitcnt lgkmcnt(1)\nv_mfma_f32_16x16x32_f16 v[16:19], v[68:71], v[32:35], v[16:19]\ns_nop 7\n" \
  "v_cvt_pkrtz_f16_f32 v20, v16, v17\nv_cvt_pkrtz_f16_f32 v21, v18, v19\nv_pk_add_u16 v20, v20, 4 op_sel_hi:[1,0] clamp\n" \
  "v_pk_add_u16 v21, v21, 4 op_sel_hi:[1,0] clamp\nds_write_b64 v4, v[20:21] offset:1024\ns_setprio 0\ns_setpc_b64 s[90:91]\n3:\n" \
  "ds_read_b64 v[124:125], v3 offset:0\nds_read_b64 v[126:127], v3 offset:8\nds_read_b64 v[120:121], v3 offset:64\n" \
  "ds_read_b64 v[122:123], v3 offset:72\nds_read_b64 v[116:117], v3 offset:128\nds_read_b64 v[118:119], v3 offset:136\n" \
  "ds_read_b64 v[112:113], v3 offset:192\nds_read_b64 v[114:115], v3 offset:200\nds_read_b64 v[108:109], v3 offset:256\n" \
  "ds_read_b64 v[110:111], v3 offset:264\nds_read_b64 v[104:105], v3 offset:320\nds_read_b64 v[106:107], v3 offset:328\n" \
  "ds_read_b64 v[100:101], v3 offset:384\nds_read_b64 v[102:103], v3 offset:392\nds_read_b64 v[96:97], v3 offset:448\n" \
  "ds_read_b64 v[98:99], v3 offset:456\nds_read_b64 v[92:93], v3 offset:512\nds_read_b64 v[94:95], v3 offset:520\n" \
  "ds_read_b64 v[88:89], v3 offset:576\nds_read_b64 v[90:91], v3 offset:584\nds_read_b64 v[84:85], v3 offset:640\n" \
  "ds_read_b64 v[86:87], v3 offset:648\nds_read_b64 v[80:81], v3 offset:704\nds_read_b64 v[82:83], v3 offset:712\n" \
  "ds_read_b64 v[76:77], v3 offset:768\nds_read_b64 v[78:79], v3 offset:776\nds_read_b64 v[72:73], v3 offset:832\n" \
  "ds_read_b64 v[74:75], v3 offset:840\nds_read_b64 v[68:69], v3 offset:896\nds_read_b64 v[70:71], v3 offset:904\n" \
  "ds_read_b64 v[64:65], v3 offset:960\nds_read_b64 v[66:67], v3 offset:968\nds_read_b64 v[60:61], v3 offset:1024\n" \
  "ds_read_b64 v[62:63], v3 offset:1032\nds_read_b64 v[56:57], v6 offset:256\nds_read_b64 v[58:59], v6 offset:264\n" \
  "ds_read_b64 v[52:53], v6 offset:128\nds_read_b64 v[54:55], v6 offset:136\nds_read_b64 v[48:49], v6 offset:320\n" \
  "ds_read_b64 v[50:51], v6 offset:328\nds_read_b64 v[44:45], v6 offset:192\nds_read_b64 v[46:47], v6 offset:200\ns_waitcnt lgkmcnt(0)\ns_barrier\n" \
  "ds_read_b128 v[24:27], v2 offset:448\nds_read_b128 v[28:31], v2 offset:512\nds_read_b128 v[32:35], v2 offset:576\n" \
  "ds_read_b128 v[36:39], v2 offset:640\nds_read_b128 v[40:43], v2 offset:704\ns_waitcnt lgkmcnt(3)\n" \
  "v_mfma_f32_16x16x32_f16 v[12:15], v[124:127], v[24:27], 0\ns_setprio 1\nds_read_b128 v[24:27], v2 offset:768\n" \
  "v_mfma_f32_16x16x32_f16 v[12:15], v[120:123], v[28:31], v[12:15]\nds_read_b128 v[28:31], v2 offset:832\ns_waitcnt lgkmcnt(3)\n" \
  "v_mfma_f32_16x16x32_f16 v[12:15], v[116:119], v[32:35], v[12:15]\nds_read_b128 v[32:35], v2 offset:896\n" \
  "v_mfma_f32_16x16x32_f16 v[12:15], v[112:115], v[36:39], v[12:15]\nds_read_b128 v[36:39], v2 offset:960\ns_waitcnt lgkmcnt(3)\n" \
  "v_mfma_f32_16x16x32_f16 v[12:15], v[108:111], v[40:43], v[12:15]\nds_read_b128 v[40:43], v2 offset:1024\n" \
  "v_mfma_f32_16x16x32_f16 v[12:15], v[104:107], v[24:27], v[12:15]\nds_read_b128 v[24:27], v2 offset:1088\ns_waitcnt lgkmcnt(3)\n" \
  "v_mfma_f32_16x16x32_f16 v[12:15], v[100:103], v[28:31], v[12:15]\nds_read_b128 v[28:31], v2 offset:1152\n" \
  "v_mfma_f32_16x16x32_f16 v[12:15], v[96:99], v[32:35], v[12:15]\ns_waitcnt lgkmcnt(3)\n" \
  "v_mfma_f32_16x16x32_f16 v[12:15], v[92:95], v[36:39], v[12:15]\nds_read_b128 v[32:35], v2 offset:1216\n" \
  "v_mfma_f32_16x16x32_f16 v[16:19], v[124:127], v[36:39], 0\ns_waitcnt lgkmcnt(3)\nv_mfma_f32_16x16x32_f16 v[12:15], v[88:91], v[40:43], v[12:15]\n" \
  "ds_read_b128 v[124:127], v2 offset:1280\nv_mfma_f32_16x16x32_f16 v[16:19], v[120:123], v[40:43], v[16:19]\ns_waitcnt lgkmcnt(3)\n" \
  "v_mfma_f32_16x16x32_f16 v[12:15], v[84:87], v[24:27], v[12:15]\nds_read_b128 v[36:39], v2 offset:1344\n" \
  "v_mfma_f32_16x16x32_f16 v[16:19], v[116:119], v[24:27], v[16:19]\ns_waitcnt lgkmcnt(3)\n" \
  "v_mfma_f32_16x16x32_f16 v[12:15], v[80:83], v[28:31], v[12:15]\nds_read_b128 v[120:123], v2 offset:1408\n" \
  "v_mfma_f32_16x16x32_f16 v[16:19], v[112:115], v[28:31], v[16:19]\ns_waitcnt lgkmcnt(3)\n" \
  "v_mfma_f32_16x16x32_f16 v[12:15], v[76:79], v[32:35], v[12:15]\nds_read_b128 v[40:43], v2 offset:1472\n" \
  "v_mfma_f32_16x16x32_f16 v[16:19], v[108:111], v[32:35], v[16:19]\ns_waitcnt lgkmcnt(3)\n" \
  "v_mfma_f32_16x16x32_f16 v[12:15], v[72:75], v[124:127], v[12:15]\nds_read_b128 v[116:119], v2 offset:1536\n" \
  "v_mfma_f32_16x16x32_f16 v[16:19], v[104:107], v[124:127], v[16:19]\nds_read_b128 v[24:27], v2 offset:1600\ns_waitcnt lgkmcnt(3)\n" \
  "v_mfma_f32_16x16x32_f16 v[12:15], v[68:71], v[36:39], v[12:15]\nds_read_b128 v[112:115], v2 offset:1664\nds_read_b32 v23, v9 offset:32\n" \
  "v_mfma_f32_16x16x32_f16 v[16:19], v[100:103], v[36:39], v[16:19]\nds_read_b128 v[28:31], v2 offset:1728\n" \
  "v_mfma_f32_16x16x32_f16 v[12:15], v[64:67], v[120:123], v[12:15]\nds_read_b128 v[108:111], v2 offset:1792\n" \
  "v_mfma_f32_16x16x32_f16 v[16:19], v[96:99], v[120:123], v[16:19]\nds_read_b128 v[32:35], v2 offset:1856\ns_waitcnt lgkmcnt(6)\n" \
  "v_mfma_f32_16x16x32_f16 v[12:15], v[60:63], v[40:43], v[12:15]\nds_read_b128 v[104:107], v2 offset:1920\n" \
  "v_mfma_f32_16x16x32_f16 v[16:19], v[92:95], v[40:43], v[16:19]\nds_read_b128 v[124:127], v2 offset:1984\ns_waitcnt lgkmcnt(5)\n" \
  "v_readfirstlane_b32 s85, v23\ns_cmp_lt_u32 s85, 3\ns_cbranch_scc1 51f\n52:\nv_mfma_f32_16x16x32_f16 v[16:19], v[88:91], v[116:119], v[16:19]\n" \
  "ds_read_b64_tr_b16 v[100:101], v5 offset:0\nds_read_b64_tr_b16 v[102:103], v5 offset:32\n" \
  "v_mfma_f32_16x16x32_f16 v[16:19], v[84:87], v[24:27], v[16:19]\nds_read_b64_tr_b16 v[36:37], v5 offset:256\n" \
  "ds_read_b64_tr_b16 v[38:39], v5 offset:288\nv_mfma_f32_16x16x32_f16 v[16:19], v[80:83], v[112:115], v[16:19]\n" \
  "ds_read_b64_tr_b16 v[96:97], v5 offset:512\nds_read_b64_tr_b16 v[98:99], v5 offset:544\ns_waitcnt lgkmcnt(9)\n" \
  "v_mfma_f32_16x16x32_f16 v[16:19], v[76:79], v[28:31], v[16:19]\nds_read_b64_tr_b16 v[120:121], v5 offset:768\n" \
  "ds_read_b64_tr_b16 v[122:123], v5 offset:800\nv_mfma_f32_16x16x32_f16 v[16:19], v[72:75], v[108:111], v[16:19]\ns_waitcnt lgkmcnt(9)\n" \
  "v_mfma_f32_16x16x32_f16 v[16:19], v[68:71], v[32:35], v[16:19]\nv_mfma_f32_16x16x32_f16 v[16:19], v[64:67], v[104:107], v[16:19]\n" \
  "s_waitcnt lgkmcnt(6)\nv_mfma_f32_16x16x32_f16 v[16:19], v[60:63], v[124:127], v[16:19]\n" \
  "v_mfma_f32_16x16x32_f16 v[12:15], v[100:103], v[56:59], v[12:15]\nv_mfma_f32_16x16x32_f16 v[16:19], v[100:103], v[52:55], v[16:19]\n" \
  "s_waitcnt lgkmcnt(2)\nv_mfma_f32_16x16x32_f16 v[12:15], v[36:39], v[48:51], v[12:15]\n" \
  "v_mfma_f32_16x16x32_f16 v[16:19], v[36:39], v[44:47], v[16:19]\nv_mfma_f32_16x16x32_f16 v[16:19], v[96:99], v[56:59], v[16:19]\n" \
  "s_waitcnt lgkmcnt(0)\nv_mfma_f32_16x16x32_f16 v[16:19], v[120:123], v[48:51], v[16:19]\ns_nop 3\nv_cvt_pkrtz_f16_f32 v20, v12, v13\n" \
  "v_cvt_pkrtz_f16_f32 v21, v14, v15\nv_pk_add_u16 v20, v20, 4 op_sel_hi:[1,0] clamp\nv_pk_add_u16 v21, v21, 4 op_sel_hi:[1,0] clamp\n" \
  "ds_write_b64 v4, v[20:21] offset:2048\nv_cvt_pkrtz_f16_f32 v20, v16, v17\nv_cvt_pkrtz_f16_f32 v21, v18, v19\n" \
  "v_pk_add_u16 v20, v20, 4 op_sel_hi:[1,0] clamp\nv_pk_add_u16 v21, v21, 4 op_sel_hi:[1,0] clamp\nds_write_b64 v4, v[20:21] offset:3072\n" \
  "s_setprio 0\ns_setpc_b64 s[90:91]\n51:\ns_movk_i32 s84, 0x4000\n53:\nds_read_b32 v23, v9 offset:32\ns_sub_u32 s84, s84, 1\ns_cbranch_scc1 52b\n" \
  "s_waitcnt lgkmcnt(0)\nv_readfirstlane_b32 s85, v23\ns_cmp_lt_u32 s85, 3\ns_cbranch_scc1 53b\ns_branch 52b\n4:\n" \
  "ds_read_b64 v[124:125], v3 offset:1088\nds_read_b64 v[126:127], v3 offset:1096\nds_read_b64 v[120:121], v3 offset:0\n" \
  "ds_read_b64 v[122:123], v3 offset:8\nds_read_b64 v[116:117], v3 offset:64\nds_read_b64 v[118:119], v3 offset:72\n" \
  "ds_read_b64 v[112:113], v3 offset:128\nds_read_b64 v[114:115], v3 offset:136\nds_read_b64 v[108:109], v3 offset:192\n" \
  "ds_read_b64 v[110:111], v3 offset:200\nds_read_b64 v[104:105], v3 offset:256\nds_read_b64 v[106:107], v3 offset:264\n" \
  "ds_read_b64 v[100:101], v3 offset:320\nds_read_b64 v[102:103], v3 offset:328\nds_read_b64 v[96:97], v3 offset:384\n" \
  "ds_read_b64 v[98:99], v3 offset:392\nds_read_b64 v[92:93], v3 offset:448\nds_read_b64 v[94:95], v3 offset:456\n" \
  "ds_read_b64 v[88:89], v3 offset:512\nds_read_b64 v[90:91], v3 offset:520\nds_read_b64 v[84:85], v3 offset:576\n" \
  "ds_read_b64 v[86:87], v3 offset:584\nds_read_b64 v[80:81], v3 offset:640\nds_read_b64 v[82:83], v3 offset:648\n" \
  "ds_read_b64 v[76:77], v3 offset:704\nds_read_b64 v[78:79], v3 offset:712\nds_read_b64 v[72:73], v3 offset:768\n" \
  "ds_read_b64 v[74:75], v3 offset:776\nds_read_b64 v[68:69], v3 offset:832\nds_read_b64 v[70:71], v3 offset:840\n" \
  "ds_read_b64 v[64:65], v3 offset:896\nds_read_b64 v[66:67], v3 offset:904\nds_read_b64 v[60:61], v3 offset:960\n" \
  "ds_read_b64 v[62:63], v3 offset:968\nds_read_b64 v[56:57], v3 offset:1024\nds_read_b64 v[58:59], v3 offset:1032\n" \
  "ds_read_b64 v[52:53], v6 offset:0\nds_read_b64 v[54:55], v6 offset:8\nds_read_b64 v[48:49], v6 offset:64\nds_read_b64 v[50:51], v6 offset:72\n" \
  "ds_read_b64 v[44:45], v6 offset:128\nds_read_b64 v[46:47], v6 offset:136\nds_read_b64 v[40:41], v6 offset:192\n" \
  "ds_read_b64 v[42:43], v6 offset:200\ns_waitcnt lgkmcnt(0)\ns_barrier\nds_read_b128 v[24:27], v2 offset:448\nds_read_b128 v[28:31], v2 offset:960\n" \
  "ds_read_b128 v[32:35], v2 offset:1472\nds_read_b128 v[36:39], v2 offset:1536\ns_waitcnt lgkmcnt(3)\n" \
  "v_mfma_f32_16x16x32_f16 v[12:15], v[124:127], v[24:27], 0\ns_setprio 1\nds_read_b128 v[24:27], v2 offset:1600\ns_waitcnt lgkmcnt(3)\n" \
  "v_mfma_f32_16x16x32_f16 v[16:19], v[124:127], v[28:31], 0\nds_read_b128 v[124:127], v2 offset:1664\ns_waitcnt lgkmcnt(3)\n" \
  "v_mfma_f32_16x16x32_f16 v[12:15], v[120:123], v[32:35], v[12:15]\nds_read_b128 v[28:31], v2 offset:1728\ns_waitcnt lgkmcnt(3)\n" \
  "v_mfma_f32_16x16x32_f16 v[12:15], v[116:119], v[36:39], v[12:15]\nds_read_b128 v[32:35], v2 offset:1792\ns_waitcnt lgkmcnt(3)\n" \
  "v_mfma_f32_16x16x32_f16 v[12:15], v[112:115], v[24:27], v[12:15]\nds_read_b128 v[36:39], v2 offset:1856\ns_waitcnt lgkmcnt(3)\n" \
  "v_mfma_f32_16x16x32_f16 v[12:15], v[108:111], v[124:127], v[12:15]\nds_read_b128 v[24:27], v2 offset:1920\ns_waitcnt lgkmcnt(3)\n" \
  "v_mfma_f32_16x16x32_f16 v[12:15], v[104:107], v[28:31], v[12:15]\nds_read_b128 v[124:127], v2 offset:1984\ns_waitcnt lgkmcnt(3)\n" \
  "v_mfma_f32_16x16x32_f16 v[12:15], v[100:103], v[32:35], v[12:15]\nds_read_b128 v[28:31], v2 offset:2048\ns_waitcnt lgkmcnt(3)\n" \
  "v_mfma_f32_16x16x32_f16 v[12:15], v[96:99], v[36:39], v[12:15]\nds_read_b128 v[32:35], v2 offset:2112\ns_waitcnt lgkmcnt(3)\n" \
  "v_mfma_f32_16x16x32_f16 v[12:15], v[92:95], v[24:27], v[12:15]\nds_read_b128 v[36:39], v2 offset:2176\ns_waitcnt lgkmcnt(3)\n" \
  "v_mfma_f32_16x16x32_f16 v[12:15], v[88:91], v[124:127], v[12:15]\nds_read_b128 v[24:27], v2 offset:2240\n" \
  "v_mfma_f32_16x16x32_f16 v[16:19], v[120:123], v[124:127], v[16:19]\nds_read_b64 v[120:121], v6 offset:256\nds_read_b64 v[122:123], v6 offset:264\n" \
  "s_waitcnt lgkmcnt(4)\nv_mfma_f32_16x16x32_f16 v[12:15], v[84:87], v[28:31], v[12:15]\nds_read_b128 v[124:127], v2 offset:2304\n" \
  "v_mfma_f32_16x16x32_f16 v[16:19], v[116:119], v[28:31], v[16:19]\nds_read_b64 v[116:117], v6 offset:320\nds_read_b64 v[118:119], v6 offset:328\n" \
  "v_mfma_f32_16x16x32_f16 v[12:15], v[80:83], v[32:35], v[12:15]\nds_read_b128 v[28:31], v2 offset:2368\n" \
  "v_mfma_f32_16x16x32_f16 v[16:19], v[112:115], v[32:35], v[16:19]\ns_waitcnt lgkmcnt(6)\n" \
  "v_mfma_f32_16x16x32_f16 v[12:15], v[76:79], v[36:39], v[12:15]\nds_read_b128 v[112:115], v2 offset:2432\n" \
  "v_mfma_f32_16x16x32_f16 v[16:19], v[108:111], v[36:39], v[16:19]\nds_read2_b32 v[32:33], v8 offset0:48 offset1:49\n" \
  "ds_read2_b32 v[34:35], v8 offset0:50 offset1:51\nv_mfma_f32_16x16x32_f16 v[12:15], v[72:75], v[24:27], v[12:15]\n" \
  "ds_read_b128 v[108:111], v2 offset:2496\nv_mfma_f32_16x16x32_f16 v[16:19], v[104:107], v[24:27], v[16:19]\n" \
  "ds_read2_b32 v[36:37], v8 offset0:32 offset1:33\nds_read2_b32 v[38:39], v8 offset0:34 offset1:35\ns_waitcnt lgkmcnt(6)\n" \
  "v_mfma_f32_16x16x32_f16 v[12:15], v[68:71], v[124:127], v[12:15]\nds_read_b128 v[104:107], v2 offset:2560\n" \
  "v_mfma_f32_16x16x32_f16 v[16:19], v[100:103], v[124:127], v[16:19]\nds_read_b128 v[24:27], v2 offset:2624\n" \
  "v_mfma_f32_16x16x32_f16 v[12:15], v[64:67], v[28:31], v[12:15]\nds_read_b128 v[100:103], v2 offset:2688\nds_read_b32 v23, v9 offset:32\n" \
  "v_mfma_f32_16x16x32_f16 v[16:19], v[96:99], v[28:31], v[16:19]\nds_read_b128 v[124:127], v2 offset:2752\ns_waitcnt lgkmcnt(7)\n" \
  "v_mfma_f32_16x16x32_f16 v[12:15], v[60:63], v[112:115], v[12:15]\nds_read_b128 v[96:99], v2 offset:2816\n" \
  "v_mfma_f32_16x16x32_f16 v[16:19], v[92:95], v[112:115], v[16:19]\nds_read_b128 v[28:31], v2 offset:2880\n" \
  "v_mfma_f32_16x16x32_f16 v[12:15], v[56:59], v[108:111], v[12:15]\nds_read_b128 v[92:95], v2 offset:2944\n" \
  "v_mfma_f32_16x16x32_f16 v[16:19], v[88:91], v[108:111], v[16:19]\nds_read_b128 v[112:115], v2 offset:3008\ns_waitcnt lgkmcnt(5)\n" \
  "v_readfirstlane_b32 s85, v23\ns_cmp_lt_u32 s85, 3\ns_cbranch_scc1 61f\n62:\nv_mfma_f32_16x16x32_f16 v[16:19], v[84:87], v[104:107], v[16:19]\n" \
  "ds_read_b64_tr_b16 v[88:89], v5 offset:0\nds_read_b64_tr_b16 v[90:91], v5 offset:32\n" \
  "v_mfma_f32_16x16x32_f16 v[16:19], v[80:83], v[24:27], v[16:19]\nds_read_b64_tr_b16 v[108:109], v5 offset:256\n" \
  "ds_read_b64_tr_b16 v[110:111], v5 offset:288\nv_mfma_f32_16x16x32_f16 v[16:19], v[76:79], v[100:103], v[16:19]\n" \
  "ds_read_b64_tr_b16 v[84:85], v5 offset:512\nds_read_b64_tr_b16 v[86:87], v5 offset:544\ns_waitcnt lgkmcnt(9)\n" \
  "v_mfma_f32_16x16x32_f16 v[16:19], v[72:75], v[124:127], v[16:19]\nds_read_b64_tr_b16 v[104:105], v5 offset:768\n" \
  "ds_read_b64_tr_b16 v[106:107], v5 offset:800\nv_mfma_f32_16x16x32_f16 v[16:19], v[68:71], v[96:99], v[16:19]\n" \
  "ds_read_b64_tr_b16 v[80:81], v5 offset:1024\nds_read_b64_tr_b16 v[82:83], v5 offset:1056\ns_waitcnt lgkmcnt(11)\n" \
  "v_mfma_f32_16x16x32_f16 v[16:19], v[64:67], v[28:31], v[16:19]\nds_read_b64_tr_b16 v[24:25], v5 offset:1280\n" \
  "ds_read_b64_tr_b16 v[26:27], v5 offset:1312\nv_mfma_f32_16x16x32_f16 v[16:19], v[60:63], v[92:95], v[16:19]\n" \
  "ds_read_b64_tr_b16 v[76:77], v5 offset:1536\nds_read_b64_tr_b16 v[78:79], v5 offset:1568\ns_waitcnt lgkmcnt(12)\n" \
  "v_mfma_f32_16x16x32_f16 v[16:19], v[56:59], v[112:115], v[16:19]\nds_read_b64_tr_b16 v[100:101], v5 offset:1792\n" \
  "ds_read_b64_tr_b16 v[102:103], v5 offset:1824\nv_mfma_f32_16x16x32_f16 v[12:15], v[88:91], v[52:55], v[12:15]\ns_waitcnt lgkmcnt(10)\n" \
  "v_mfma_f32_16x16x32_f16 v[12:15], v[108:111], v[48:51], v[12:15]\nds_read_b64_tr_b16 v[72:73], v7 offset:0\n" \
  "ds_read_b64_tr_b16 v[74:75], v7 offset:64\nv_mfma_f32_16x16x32_f16 v[12:15], v[84:87], v[44:47], v[12:15]\n" \
  "ds_read_b64_tr_b16 v[124:125], v7 offset:512\nds_read_b64_tr_b16 v[126:127], v7 offset:576\n" \
  "v_mfma_f32_16x16x32_f16 v[16:19], v[84:87], v[52:55], v[16:19]\ns_waitcnt lgkmcnt(10)\n" \
  "v_mfma_f32_16x16x32_f16 v[12:15], v[104:107], v[40:43], v[12:15]\nv_mfma_f32_16x16x32_f16 v[16:19], v[104:107], v[48:51], v[16:19]\n" \
  "v_mfma_f32_16x16x32_f16 v[12:15], v[80:83], v[120:123], v[12:15]\nv_mfma_f32_16x16x32_f16 v[16:19], v[80:83], v[44:47], v[16:19]\n" \
  "s_waitcnt lgkmcnt(6)\nv_mfma_f32_16x16x32_f16 v[12:15], v[24:27], v[116:119], v[12:15]\n" \
  "v_mfma_f32_16x16x32_f16 v[16:19], v[24:27], v[40:43], v[16:19]\nv_mfma_f32_16x16x32_f16 v[16:19], v[76:79], v[120:123], v[16:19]\n" \
  "s_waitcnt lgkmcnt(2)\nv_mfma_f32_16x16x32_f16 v[16:19], v[100:103], v[116:119], v[16:19]\n" \
  "v_mfma_f32_16x16x32_f16 v[12:15], v[72:75], v[32:35], v[12:15]\nv_mfma_f32_16x16x32_f16 v[16:19], v[72:75], v[36:39], v[16:19]\n" \
  "s_waitcnt lgkmcnt(0)\nv_mfma_f32_16x16x32_f16 v[16:19], v[124:127], v[32:35], v[16:19]\ns_nop 4\nv_cvt_pkrtz_f16_f32 v20, v12, v13\n" \
  "v_cvt_pkrtz_f16_f32 v21, v14, v15\nv_pk_add_u16 v20, v20, 4 op_sel_hi:[1,0] clamp\nv_pk_add_u16 v21, v21, 4 op_sel_hi:[1,0] clamp\n" \
  "ds_write_b64 v4, v[20:21] offset:4096\nv_cvt_pkrtz_f16_f32 v20, v16, v17\nv_cvt_pkrtz_f16_f32 v21, v18, v19\n" \
  "v_pk_add_u16 v20, v20, 4 op_sel_hi:[1,0] clamp\nv_pk_add_u16 v21, v21, 4 op_sel_hi:[1,0] clamp\nds_write_b64 v4, v[20:21] offset:5120\n" \
  "s_setprio 0\ns_setpc_b64 s[90:91]\n61:\ns_movk_i32 s84, 0x4000\n63:\nds_read_b32 v23, v9 offset:32\ns_sub_u32 s84, s84, 1\ns_cbranch_scc1 62b\n" \
  "s_waitcnt lgkmcnt(0)\nv_readfirstlane_b32 s85, v23\ns_cmp_lt_u32 s85, 3\ns_cbranch_scc1 63b\ns_branch 62b\n5:\n" \
  "ds_read_b64 v[124:125], v3 offset:1088\nds_read_b64 v[126:127], v3 offset:1096\nds_read_b64 v[120:121], v3 offset:0\n" \
  "ds_read_b64 v[122:123], v3 offset:8\nds_read_b64 v[116:117], v3 offset:64\nds_read_b64 v[118:119], v3 offset:72\n" \
  "ds_read_b64 v[112:113], v3 offset:128\nds_read_b64 v[114:115], v3 offset:136\nds_read_b64 v[108:109], v3 offset:192\n" \
  "ds_read_b64 v[110:111], v3 offset:200\nds_read_b64 v[104:105], v3 offset:256\nds_read_b64 v[106:107], v3 offset:264\n" \
  "ds_read_b64 v[100:101], v3 offset:320\nds_read_b64 v[102:103], v3 offset:328\nds_read_b64 v[96:97], v3 offset:384\n" \
  "ds_read_b64 v[98:99], v3 offset:392\nds_read_b64 v[92:93], v3 offset:448\nds_read_b64 v[94:95], v3 offset:456\n" \
  "ds_read_b64 v[88:89], v3 offset:512\nds_read_b64 v[90:91], v3 offset:520\nds_read_b64 v[84:85], v3 offset:576\n" \
  "ds_read_b64 v[86:87], v3 offset:584\nds_read_b64 v[80:81], v3 offset:640\nds_read_b64 v[82:83], v3 offset:648\n" \
  "ds_read_b64 v[76:77], v3 offset:704\nds_read_b64 v[78:79], v3 offset:712\nds_read_b64 v[72:73], v3 offset:768\n" \
  "ds_read_b64 v[74:75], v3 offset:776\nds_read_b64 v[68:69], v3 offset:832\nds_read_b64 v[70:71], v3 offset:840\n" \
  "ds_read_b64 v[64:65], v3 offset:896\nds_read_b64 v[66:67], v3 offset:904\nds_read_b64 v[60:61], v3 offset:960\n" \
  "ds_read_b64 v[62:63], v3 offset:968\nds_read_b64 v[56:57], v3 offset:1024\nds_read_b64 v[58:59], v3 offset:1032\n" \
  "ds_read_b64 v[52:53], v6 offset:0\nds_read_b64 v[54:55], v6 offset:8\nds_read_b64 v[48:49], v6 offset:64\nds_read_b64 v[50:51], v6 offset:72\n" \
  "ds_read_b64 v[44:45], v6 offset:128\nds_read_b64 v[46:47], v6 offset:136\nds_read_b64 v[40:41], v6 offset:192\n" \
  "ds_read_b64 v[42:43], v6 offset:200\ns_waitcnt lgkmcnt(0)\ns_barrier\nds_read_b128 v[24:27], v2 offset:1472\n" \
  "ds_read_b128 v[28:31], v2 offset:1984\nds_read_b128 v[32:35], v2 offset:2496\nds_read_b128 v[36:39], v2 offset:2560\ns_waitcnt lgkmcnt(3)\n" \
  "v_mfma_f32_16x16x32_f16 v[12:15], v[124:127], v[24:27], 0\ns_setprio 1\nds_read_b128 v[24:27], v2 offset:2624\ns_waitcnt lgkmcnt(3)\n" \
  "v_mfma_f32_16x16x32_f16 v[16:19], v[124:127], v[28:31], 0\nds_read_b128 v[124:127], v2 offset:2688\ns_waitcnt lgkmcnt(3)\n" \
  "v_mfma_f32_16x16x32_f16 v[12:15], v[120:123], v[32:35], v[12:15]\nds_read_b128 v[28:31], v2 offset:2752\ns_waitcnt lgkmcnt(3)\n" \
  "v_mfma_f32_16x16x32_f16 v[12:15], v[116:119], v[36:39], v[12:15]\nds_read_b128 v[32:35], v2 offset:2816\ns_waitcnt lgkmcnt(3)\n" \
  "v_mfma_f32_16x16x32_f16 v[12:15], v[112:115], v[24:27], v[12:15]\nds_read_b128 v[36:39], v2 offset:2880\ns_waitcnt lgkmcnt(3)\n" \
  "v_mfma_f32_16x16x32_f16 v[12:15], v[108:111], v[124:127], v[12:15]\nds_read_b128 v[24:27], v2 offset:2944\ns_waitcnt lgkmcnt(3)\n" \
  "v_mfma_f32_16x16x32_f16 v[12:15], v[104:107], v[28:31], v[12:15]\nds_read_b128 v[124:127], v2 offset:3008\ns_waitcnt lgkmcnt(3)\n" \
  "v_mfma_f32_16x16x32_f16 v[12:15], v[100:103], v[32:35], v[12:15]\nds_read_b128 v[28:31], v2 offset:3072\ns_waitcnt lgkmcnt(3)\n" \
  "v_mfma_f32_16x16x32_f16 v[12:15], v[96:99], v[36:39], v[12:15]\nds_read_b128 v[32:35], v2 offset:3136\ns_waitcnt lgkmcnt(3)\n" \
  "v_mfma_f32_16x16x32_f16 v[12:15], v[92:95], v[24:27], v[12:15]\nds_read_b128 v[36:39], v2 offset:3200\ns_waitcnt lgkmcnt(3)\n" \
  "v_mfma_f32_16x16x32_f16 v[12:15], v[88:91], v[124:127], v[12:15]\nds_read_b128 v[24:27], v2 offset:3264\n" \
  "v_mfma_f32_16x16x32_f16 v[16:19], v[120:123], v[124:127], v[16:19]\nds_read_b64 v[120:121], v6 offset:256\nds_read_b64 v[122:123], v6 offset:264\n" \
  "s_waitcnt lgkmcnt(4)\nv_mfma_f32_16x16x32_f16 v[12:15], v[84:87], v[28:31], v[12:15]\nds_read_b128 v[124:127], v2 offset:3328\n" \
  "v_mfma_f32_16x16x32_f16 v[16:19], v[116:119], v[28:31], v[16:19]\nds_read_b64 v[116:117], v6 offset:320\nds_read_b64 v[118:119], v6 offset:328\n" \
  "v_mfma_f32_16x16x32_f16 v[12:15], v[80:83], v[32:35], v[12:15]\nds_read_b128 v[28:31], v2 offset:3392\n" \
  "v_mfma_f32_16x16x32_f16 v[16:19], v[112:115], v[32:35], v[16:19]\ns_waitcnt lgkmcnt(6)\n" \
  "v_mfma_f32_16x16x32_f16 v[12:15], v[76:79], v[36:39], v[12:15]\nds_read_b128 v[112:115], v2 offset:3456\n" \
  "v_mfma_f32_16x16x32_f16 v[16:19], v[108:111], v[36:39], v[16:19]\nds_read2_b32 v[32:33], v8 offset0:16 offset1:17\n" \
  "ds_read2_b32 v[34:35], v8 offset0:18 offset1:19\nv_mfma_f32_16x16x32_f16 v[12:15], v[72:75], v[24:27], v[12:15]\n" \
  "ds_read_b128 v[108:111], v2 offset:3520\nv_mfma_f32_16x16x32_f16 v[16:19], v[104:107], v[24:27], v[16:19]\n" \
  "ds_read2_b32 v[36:37], v8 offset0:0 offset1:1\nds_read2_b32 v[38:39], v8 offset0:2 offset1:3\ns_waitcnt lgkmcnt(6)\n" \
  "v_mfma_f32_16x16x32_f16 v[12:15], v[68:71], v[124:127], v[12:15]\nds_read_b128 v[104:107], v2 offset:3584\n" \
  "v_mfma_f32_16x16x32_f16 v[16:19], v[100:103], v[124:127], v[16:19]\nds_read_b128 v[24:27], v2 offset:3648\n" \
  "ds_read2_b32 v[100:101], v8 offset0:32 offset1:33\nds_read2_b32 v[102:103], v8 offset0:34 offset1:35\n" \
  "v_mfma_f32_16x16x32_f16 v[12:15], v[64:67], v[28:31], v[12:15]\nds_read_b128 v[124:127], v2 offset:3712\nds_read_b32 v23, v9 offset:32\n" \
  "v_mfma_f32_16x16x32_f16 v[16:19], v[96:99], v[28:31], v[16:19]\nds_read_b128 v[96:99], v2 offset:3776\ns_waitcnt lgkmcnt(9)\n" \
  "v_mfma_f32_16x16x32_f16 v[12:15], v[60:63], v[112:115], v[12:15]\nds_read_b128 v[28:31], v2 offset:3840\n" \
  "v_mfma_f32_16x16x32_f16 v[16:19], v[92:95], v[112:115], v[16:19]\nds_read_b128 v[92:95], v2 offset:3904\n" \
  "v_mfma_f32_16x16x32_f16 v[12:15], v[56:59], v[108:111], v[12:15]\nds_read_b128 v[112:115], v2 offset:3968\n" \
  "v_mfma_f32_16x16x32_f16 v[16:19], v[88:91], v[108:111], v[16:19]\nds_read_b128 v[88:91], v2 offset:4032\ns_waitcnt lgkmcnt(5)\n" \
  "v_readfirstlane_b32 s85, v23\ns_cmp_lt_u32 s85, 3\ns_cbranch_scc1 71f\n72:\nv_mfma_f32_16x16x32_f16 v[16:19], v[84:87], v[104:107], v[16:19]\n" \
  "ds_read_b64_tr_b16 v[108:109], v5 offset:1024\nds_read_b64_tr_b16 v[110:111], v5 offset:1056\n" \
  "v_mfma_f32_16x16x32_f16 v[16:19], v[80:83], v[24:27], v[16:19]\nds_read_b64_tr_b16 v[84:85], v5 offset:1280\n" \
  "ds_read_b64_tr_b16 v[86:87], v5 offset:1312\nv_mfma_f32_16x16x32_f16 v[16:19], v[76:79], v[124:127], v[16:19]\n" \
  "ds_read_b64_tr_b16 v[104:105], v5 offset:1536\nds_read_b64_tr_b16 v[106:107], v5 offset:1568\ns_waitcnt lgkmcnt(9)\n" \
  "v_mfma_f32_16x16x32_f16 v[16:19], v[72:75], v[96:99], v[16:19]\nds_read_b64_tr_b16 v[80:81], v5 offset:1792\n" \
  "ds_read_b64_tr_b16 v[82:83], v5 offset:1824\nv_mfma_f32_16x16x32_f16 v[16:19], v[68:71], v[28:31], v[16:19]\n" \
  "ds_read_b64_tr_b16 v[24:25], v5 offset:2048\nds_read_b64_tr_b16 v[26:27], v5 offset:2080\ns_waitcnt lgkmcnt(11)\n" \
  "v_mfma_f32_16x16x32_f16 v[16:19], v[64:67], v[92:95], v[16:19]\nds_read_b64_tr_b16 v[76:77], v5 offset:2304\n" \
  "ds_read_b64_tr_b16 v[78:79], v5 offset:2336\nv_mfma_f32_16x16x32_f16 v[16:19], v[60:63], v[112:115], v[16:19]\n" \
  "ds_read_b64_tr_b16 v[124:125], v5 offset:2560\nds_read_b64_tr_b16 v[126:127], v5 offset:2592\ns_waitcnt lgkmcnt(12)\n" \
  "v_mfma_f32_16x16x32_f16 v[16:19], v[56:59], v[88:91], v[16:19]\nds_read_b64_tr_b16 v[72:73], v5 offset:2816\n" \
  "ds_read_b64_tr_b16 v[74:75], v5 offset:2848\nv_mfma_f32_16x16x32_f16 v[12:15], v[108:111], v[52:55], v[12:15]\ns_waitcnt lgkmcnt(10)\n" \
  "v_mfma_f32_16x16x32_f16 v[12:15], v[84:87], v[48:51], v[12:15]\nds_read_b64_tr_b16 v[96:97], v7 offset:0\n" \
  "ds_read_b64_tr_b16 v[98:99], v7 offset:64\nv_mfma_f32_16x16x32_f16 v[12:15], v[104:107], v[44:47], v[12:15]\n" \
  "ds_read_b64_tr_b16 v[68:69], v7 offset:512\nds_read_b64_tr_b16 v[70:71], v7 offset:576\n" \
  "v_mfma_f32_16x16x32_f16 v[16:19], v[104:107], v[52:55], v[16:19]\ns_waitcnt lgkmcnt(10)\n" \
  "v_mfma_f32_16x16x32_f16 v[12:15], v[80:83], v[40:43], v[12:15]\nds_read_b64_tr_b16 v[28:29], v7 offset:1024\n" \
  "ds_read_b64_tr_b16 v[30:31], v7 offset:1088\nv_mfma_f32_16x16x32_f16 v[16:19], v[80:83], v[48:51], v[16:19]\n" \
  "ds_read2_b32 v[64:65], v8 offset0:48 offset1:49\nds_read2_b32 v[66:67], v8 offset0:50 offset1:51\n" \
  "v_mfma_f32_16x16x32_f16 v[12:15], v[24:27], v[120:123], v[12:15]\nv_mfma_f32_16x16x32_f16 v[16:19], v[24:27], v[44:47], v[16:19]\n" \
  "s_waitcnt lgkmcnt(10)\nv_mfma_f32_16x16x32_f16 v[12:15], v[76:79], v[116:119], v[12:15]\nds_read_b64_tr_b16 v[92:93], v7 offset:1536\n" \
  "ds_read_b64_tr_b16 v[94:95], v7 offset:1600\nv_mfma_f32_16x16x32_f16 v[16:19], v[76:79], v[40:43], v[16:19]\n" \
  "v_mfma_f32_16x16x32_f16 v[16:19], v[124:127], v[120:123], v[16:19]\ns_waitcnt lgkmcnt(8)\n" \
  "v_mfma_f32_16x16x32_f16 v[16:19], v[72:75], v[116:119], v[16:19]\nv_mfma_f32_16x16x32_f16 v[12:15], v[96:99], v[32:35], v[12:15]\n" \
  "v_mfma_f32_16x16x32_f16 v[16:19], v[96:99], v[36:39], v[16:19]\ns_waitcnt lgkmcnt(4)\n" \
  "v_mfma_f32_16x16x32_f16 v[12:15], v[68:71], v[100:103], v[12:15]\nv_mfma_f32_16x16x32_f16 v[16:19], v[68:71], v[32:35], v[16:19]\n" \
  "s_waitcnt lgkmcnt(2)\nv_mfma_f32_16x16x32_f16 v[12:15], v[28:31], v[64:67], v[12:15]\n" \
  "v_mfma_f32_16x16x32_f16 v[16:19], v[28:31], v[100:103], v[16:19]\ns_waitcnt lgkmcnt(0)\n" \
  "v_mfma_f32_16x16x32_f16 v[16:19], v[92:95], v[64:67], v[16:19]\ns_nop 4\nv_cvt_pkrtz_f16_f32 v20, v12, v13\nv_cvt_pkrtz_f16_f32 v21, v14, v15\n" \
  "v_pk_add_u16 v20, v20, 4 op_sel_hi:[1,0] clamp\nv_pk_add_u16 v21, v21, 4 op_sel_hi:[1,0] clamp\nds_write_b64 v4, v[20:21] offset:6144\n" \
  "v_cvt_pkrtz_f16_f32 v20, v16, v17\nv_cvt_pkrtz_f16_f32 v21, v18, v19\nv_pk_add_u16 v20, v20, 4 op_sel_hi:[1,0] clamp\n" \
  "v_pk_add_u16 v21, v21, 4 op_sel_hi:[1,0] clamp\nds_write_b64 v4, v[20:21] offset:7168\ns_setprio 0\ns_setpc_b64 s[90:91]\n71:\n" \
  "s_movk_i32 s84, 0x4000\n73:\nds_read_b32 v23, v9 offset:32\ns_sub_u32 s84, s84, 1\ns_cbranch_scc1 72b\ns_waitcnt lgkmcnt(0)\n" \
  "v_readfirstlane_b32 s85, v23\ns_cmp_lt_u32 s85, 3\ns_cbranch_scc1 73b\ns_branch 72b\n"
#define CONV_CLOB "memory", "scc", "s84", "s85", "s90", "s91", "v12", "v13", "v14", "v15", "v16", "v17", "v18", "v19", "v20", "v21", "v22", "v23", "v24", "v25", "v26", "v27", "v28", "v29", "v30", "v31", "v32", "v33", "v34", "v35", "v36", "v37", "v38", "v39", "v40", "v41", "v42", "v43", "v44", "v45", "v46", "v47", "v48", "v49", "v50", "v51", "v52", "v53", "v54", "v55", "v56", "v57", "v58", "v59", "v60", "v61", "v62", "v63", "v64", "v65", "v66", "v67", "v68", "v69", "v70", "v71", "v72", "v73", "v74", "v75", "v76", "v77", "v78", "v79", "v80", "v81", "v82", "v83", "v84", "v85", "v86", "v87", "v88", "v89", "v90", "v91", "v92", "v93", "v94", "v95", "v96", "v97", "v98", "v99", "v100", "v101", "v102", "v103", "v104", "v105", "v106", "v107", "v108", "v109", "v110", "v111", "v112", "v113", "v114", "v115", "v116", "v117", "v118", "v119", "v120", "v121", "v122", "v123", "v124", "v125", "v126", "v127"
constexpr int TDP = 1216, T_3 = 4 * TDP, T_4 = T_3 + 512, TB = T_4 + 320;
constexpr int C_U = 480, UB = C_U + 4096 + 96, C_G5 = 0, C_G3 = 3200, C_G9 = 6400, C_G7 = C_G9 + 2096, GB = C_G7 + 2080 + 112;
constexpr int O_FL = 2 * TB, O_UL = O_FL + 64, O_IM = O_UL + 2 * UB, O_ST = O_IM + 2 * GB;
constexpr int X_TB = 10240, X_UL = X_TB + TB, X_IM = X_UL + UB, X_ST = X_IM + GB;
constexpr int X_WR = X_IM + 128 * IP;
#define X_RW(w) (LW + 9216 * (2 + ((w) >> 1)) + 2560 * ((w) & 1))
constexpr int RP = 1216;
constexpr int LW = 36864 + 512, LA = LW, LM = LW + 36864;
constexpr int L_SC = LM + 2048, SMEM = O_IM + 256 * IP;
static_assert(L_SC + 1152 <= SMEM && O_ST + 8192 <= SMEM && X_IM + 128 * IP <= LM && X_ST + 8192 <= LM && UB % 128 == 64 && GB % 128 == 64 && SMEM <= 79872, "");
struct TOps { uint32_t xa, xb, ya, pd, pf, pg, pe; };
__device__ __forceinline__ void table_ops(uint32_t im, uint32_t tb, int ct, int lane, TOps& t) {
  const int j = lane & 15, kq = lane >> 4, q4 = (lane >> 2) & 3, p4 = lane & 3, jj = j + 16 * ct;
  const uint32_t sa = im + (uint32_t)(IP * (lane >> 2));
  t.xa = sa + (uint32_t)(I_X + 8 * p4); t.xb = sa + (uint32_t)(I_X + 32 + 8 * (p4 & 1)); t.ya = sa + (uint32_t)(32 * ct + 8 * p4);
  t.pd = tb + (uint32_t)(2 * (541 - (8 * jj + 4 * (kq & 1) + 256 * (kq >> 1))));
  t.pf = tb + (uint32_t)((kq & 1) ? T_4 + 2 * (63 - jj) : T_3 + 2 * (127 - 2 * jj));
  t.pg = t.pf - ((kq & 1) ? 64 : 2);
  t.pe = tb + (uint32_t)(p4 * (TDP + 2) + ((lane < 12) ? 2 * (32 - q4) : 2 * (572 + (q4 & 1))));
  asm volatile("" : "+v"(t.xa), "+v"(t.xb), "+v"(t.ya), "+v"(t.pd), "+v"(t.pf), "+v"(t.pg), "+v"(t.pe));
}
typedef __attribute__((address_space(3))) char* lptr;
#define LP(T, a) ((__attribute__((address_space(3))) T*)(uintptr_t)(a))
__device__ __forceinline__ uint32_t table_tile(const TOps& t, int ct, int lane, uint32_t evp, float dsk TSP) {
  u32x4 ev;
  f32x4 ad[2], af[2];
  asm volatile(T_ASM : "=&v"(ad[0]), "=&v"(ad[1]), "=&v"(af[0]), "=&v"(af[1]), "=&v"(ev) : "v"(t.xa), "v"(t.xb), "v"(t.ya), "v"(evp) : T_CLOB);
  const int e0 = (int)(ev[0] + ev[2]), e1 = (int)(ev[1] + ev[3]);
  const int em = min(max(max(max(e0, e1), (int)((__float_as_uint(dsk) >> 23) & 0xffu) - 133), -110), 115);
  const int ts = ((uint32_t)(em + 12) <= 8u) ? 1 : -3 - em;
  float s0 = __builtin_amdgcn_ldexpf(1.f, e0 - 29 + ts), s1 = __builtin_amdgcn_ldexpf(1.f, e1 - 29 + ts);
  float d0 = (ct == 0 && lane == 0) ? __builtin_amdgcn_ldexpf(dsk, ts) : 0.f;
  asm volatile("s_nop 1" : "+v"(ad[0]), "+v"(ad[1]), "+v"(af[0]), "+v"(af[1]), "+v"(s0), "+v"(s1), "+v"(d0));
  const f32x4 k = ad[0] * s0 + (f32x4){d0, 0.f, 0.f, 0.f} + ad[1] * s1, f = af[0] * s0 + af[1] * s1;
  auto pk = [](float a, float b) { uint32_t r; asm("v_cvt_pk_f16_f32 %0, %1, %2" : "=v"(r) : "v"(a), "v"(b)); return r; };
  asm volatile("s_nop 0" : : "v"(k), "v"(f));
  const uint32_t p32 = pk(k[3], k[2]), p10 = pk(k[1], k[0]), p21 = pk(k[2], k[1]), p01 = pk(f[0], f[1]), p23 = pk(f[2], f[3]);
  *LP(u32x2, t.pd + 3 * TDP + 6) = (u32x2){p32, p10};
  *LP(uint32_t, t.pd + TDP + 2) = p32; *LP(uint32_t, t.pd + TDP + 6) = p10;
  *LP(uint32_t, t.pd + 2) = p21; *LP(uint32_t, t.pd + 2 * TDP + 6) = p21;
  *LP(u16, t.pd) = (u16)p32; *LP(u16, t.pd + 2 * TDP + 4) = (u16)p32;
  asm volatile("ds_write_b16_d16_hi %0, %1 offset:6\n\tds_write_b16_d16_hi %0, %1 offset:%2" : : "v"(t.pd), "v"(p10), "n"(2 * TDP + 10) : "memory");
  if (ct) {
    const uint32_t va = (uint32_t)__builtin_amdgcn_readlane((int)p32, 63), vb = (uint32_t)__builtin_amdgcn_readlane((int)p23, 15) >> 16;
    *LP(u16, t.pe) = (u16)((lane < 12) ? va : vb);
  }
  if (lane < 32) {
    *LP(u16, t.pf + 128) = (u16)p01; *LP(u16, t.pg + 128) = (u16)(p01 >> 16);
    *LP(u16, t.pf) = (u16)p23; *LP(u16, t.pg) = (u16)(p23 >> 16);
  }
  return (uint32_t)(127 + 5 - ts) << 23;
}
__device__ __forceinline__ uint32_t hadd(uint32_t a, uint32_t b) { return __builtin_bit_cast(uint32_t, __builtin_bit_cast(f16x2, a) + __builtin_bit_cast(f16x2, b)); }
template <bool FULL> __device__ __forceinline__ void sums(char* sm, const u32x4 p, const u32x4 c) {
  const uint32_t D[8] = {p[0], p[1], p[2], p[3], c[0], c[1], c[2], c[3]};
  uint32_t O[7];
#pragma unroll
  for (int j = 0; j < 7; ++j) O[j] = __builtin_amdgcn_alignbit(D[j + 1], D[j], 16);
  u32x4 a, b;
#pragma unroll
  for (int j = 0; j < 4; ++j) a[j] = hadd(D[j + 2], hadd(O[j + 1], O[j + 2]));
  *(u32x4*)(sm + C_G3 + 32) = a;
#pragma unroll
  for (int j = 0; j < 4; ++j) b[j] = hadd(a[j], hadd(D[j + 1], D[j + 3]));
  *(u32x4*)(sm + C_G5 + 32) = b;
  if (FULL) {
#pragma unroll
    for (int j = 0; j < 4; ++j) a[j] = hadd(b[j], hadd(O[j], O[j + 3]));
    *(u32x4*)(sm + C_G7 + 32) = a;
#pragma unroll
    for (int j = 0; j < 4; ++j) b[j] = hadd(a[j], hadd(D[j], D[j + 4]));
    *(u32x4*)(sm + C_G9 + 32) = b;
  }
}
__device__ __forceinline__ void count_up(uint32_t a) {
  uint32_t t;
  asm volatile("s_mov_b64 exec, 1\n\tv_mov_b32 %0, 1\n\tds_add_u32 %1, %0\n\ts_mov_b64 exec, -1" : "=&v"(t) : "v"(a) : "memory");
}
__device__ __forceinline__ int pad_at(int r) { return 16 * (r & 1) + 3200 * (r >> 1) - ((r >= 6) ? 3200 - 2096 : 0); }
struct COps { uint32_t b, a, o, g3, k3, g4, k4, f; };
__device__ __forceinline__ void conv_ops(uint32_t tb, uint32_t ub, uint32_t cb, int lane, COps& e) {
  const uint32_t j = lane & 15, kq = lane >> 4, q4 = (lane >> 2) & 3, p4 = lane & 3;
  e.b = ub + 32u * j + 16u * kq;
  e.a = tb + p4 * TDP + 32u - 8u * q4 + 16u * kq;
  e.g3 = cb + C_G5 + 8 + 64u * kq + 8u * (q4 + p4) + (q4 & 1u) * 3200u;
  e.g4 = cb + C_G9 + 8 + 128u * kq + 16u * q4 + 8u * p4 + (q4 & 1u) * 2096u;
  e.k3 = tb + T_3 + 120 - 8u * j + 16u * kq;
  e.k4 = tb + T_4 + 60 - 4u * j + 16u * kq;
  asm volatile("" : "+v"(e.b), "+v"(e.a), "+v"(e.g3), "+v"(e.g4), "+v"(e.k3), "+v"(e.k4));
}
#define CONV_RUN(prog, e, ...) do { int prog_ = (prog); \
    asm volatile("s_call_b64 s[90:91], kf_conv" : "+{s89}"(prog_), "+{v2}"(e.b), "+{v3}"(e.a), "+{v4}"(e.o), "+{v5}"(e.g3), "+{v6}"(e.k3), "+{v7}"(e.g4), \
                 "+{v8}"(e.k4), "+{v9}"(e.f) __VA_ARGS__ : : CONV_CLOB); } while (0)

template <bool E>
__device__ __forceinline__ void ln_stats(const uint32_t* v, int lane, float sc, f32x2* t, f32x4 z, f32x2& rc, f32x2& nc, float& e) {
  float s1a = z[0], s2a = z[1], s1b = z[2], s2b = z[3];
#pragma unroll
  for (int k = 0; k < 4; ++k) {
    s1a = dot2(v[k], 0x3f803f80u, s1a); s2a = dot2(v[k], v[k], s2a);
    s1b = dot2(v[4 + k], 0x3f803f80u, s1b); s2b = dot2(v[4 + k], v[4 + k], s2b);
  }
#pragma unroll
  for (int k = 0; k < 9; ++k) t[k] = (f32x2){bflo(v[k]), bfhi(v[k])};
  const float rs = row_sum(fold16(dot2(v[8], 0x3f803f80u, fold32(s1a, s1b)), dot2(v[8], v[8], fold32(s2a, s2b))));
  if (E) { e = row_sum(e); e += __int_as_float(__builtin_amdgcn_update_dpp(0, __float_as_int(e), 0x142, 0xa, 0xf, false)); }
  const f32x2 mean = (f32x2){rdl(rs, 15), rdl(rs, 47)} * (1.f / D_), m2 = mean * mean;
  f32x2 var = (f32x2){rdl(rs, 31), rdl(rs, 63)} * (1.f / D_) - m2;
  asm volatile("" : "+v"(t[0]), "+v"(t[1]), "+v"(t[2]), "+v"(t[3]), "+v"(t[4]), "+v"(t[5]), "+v"(t[6]), "+v"(t[7]), "+v"(t[8]));
  if (__builtin_expect(!(m2[0] <= 16.f * var[0] && m2[1] <= 16.f * var[1]), 0)) {
    float q[3] = {0.f, 0.f, 0.f};
#pragma unroll
    for (int k = 0; k < 9; ++k) {
      const f32x2 a = t[k] - ((k < 4 || (k == 8 && lane < 32)) ? mean[0] : mean[1]);
      q[k >> 2] += a[0] * a[0] + a[1] * a[1];
    }
    float qa = q[0] + ((lane < 32) ? q[2] : 0.f), qb = q[1] + ((lane < 32) ? 0.f : q[2]);
    wave_sum2(qa, qb);
    var = (f32x2){qa, qb} * (1.f / D_);
  }
  const uint32_t mp = pack2(mean[0], mean[1]), vp = pack2(var[0], var[1]);
  const f32x2 ve = (f32x2){bflo(vp), bfhi(vp)} + 1e-5f;
  const uint32_t e2 = pack2(ve[0], ve[1]);
  const uint32_t sp = pack2(__builtin_amdgcn_sqrtf(bflo(e2)), __builtin_amdgcn_sqrtf(bfhi(e2)));
  rc = (f32x2){__builtin_amdgcn_rcpf(bflo(sp)), __builtin_amdgcn_rcpf(bfhi(sp))} * sc;
  nc = -(f32x2){bflo(mp), bfhi(mp)} * rc;
}
template <bool H>
__device__ __forceinline__ void ln_apply(const f32x2* t, int lane, f32x2 rc, f32x2 nc, uint32_t* c) {
  const float nm[3] = {nc[0], nc[1], (lane < 32) ? nc[0] : nc[1]}, rr[3] = {rc[0], rc[1], (lane < 32) ? rc[0] : rc[1]};
  f32x2 dd[9];
#pragma unroll
  for (int k = 0; k < 9; ++k) dd[k] = __builtin_elementwise_fma(t[k], (f32x2){rr[k >> 2], rr[k >> 2]}, (f32x2){nm[k >> 2], nm[k >> 2]});
  asm volatile("" : "+v"(dd[0]), "+v"(dd[1]), "+v"(dd[2]), "+v"(dd[3]), "+v"(dd[4]), "+v"(dd[5]), "+v"(dd[6]), "+v"(dd[7]), "+v"(dd[8]));
#pragma unroll
  for (int k = 0; k < 9; ++k) {
    const f32x2 d = dd[k];
    typedef __attribute__((ext_vector_type(2))) uint16_t u16x2;
    if (H) c[k] = __builtin_bit_cast(uint32_t, __builtin_elementwise_add_sat(__builtin_bit_cast(u16x2, __builtin_amdgcn_cvt_pkrtz(d[0], d[1])), (u16x2){4, 4}));
    else c[k] = pack2(d[0], d[1]);
  }
}
__device__ __forceinline__ uint32_t med3u(uint32_t a, uint32_t b, uint32_t c) { return max(min(a, b), min(max(a, b), c)); }
__device__ __forceinline__ uint32_t oct_maxu(uint32_t v) {
  v = max(v, (uint32_t)DPP_I((int)v, 0xb1, 0xf));
  v = max(v, (uint32_t)DPP_I((int)v, 0x4e, 0xf));
  v = max(v, (uint32_t)DPP_I((int)v, 0x141, 0xf));
  return v;
}

#define KARGS const u16* __restrict__ x, const u16* __restrict__ Are, const u16* __restrict__ Aim, const u16* __restrict__ Bre, \
    const u16* __restrict__ Bim, const u16* __restrict__ Cre, const u16* __restrict__ Cim, const u16* __restrict__ logdt, \
    const u16* __restrict__ Dskip, char* __restrict__ ws, const u16* __restrict__ wr, const u16* __restrict__ wg, \
    const u16* __restrict__ wu, const u16* __restrict__ wd, u16* __restrict__ out
#define KPASS x, Are, Aim, Bre, Bim, Cre, Cim, logdt, Dskip, ws, wr, wg, wu, wd, out
template <int ROLE, int FFN = -1>
__device__ __forceinline__ void role_body(char* smem, KARGS) {
  const int tid = threadIdx.x, lane = tid & 63;
  const int w = __builtin_amdgcn_readfirstlane(tid >> 6), wl = w & 3;
  const int b = blockIdx.x & 255, bx = b & 7, bj = b >> 3;
  constexpr bool isl = ROLE == 1 || ROLE == 2;
  const int tg = isl ? 32 * (bj >> 2) + 4 * bx + (bj & 3) : 32 * bx + bj;
  u32x4 xa, xc;
  uint32_t ep, r0;
#define P_WAIT(n, ...) asm volatile("s_waitcnt vmcnt(" #n ")\n\tv_lshlrev_b32 %0, 16, v100\n\tv_lshlrev_b32 %1, 16, v101\n\tv_lshlrev_b32 %2, 16, v102" \
    : "=v"(ldt), "=v"(are), "=v"(aim) : : "v100", "v101", "v102" __VA_ARGS__)
#define P_MORE() asm volatile("v_lshlrev_b32 %0, 16, v103\n\tv_lshlrev_b32 %1, 16, v104\n\tv_lshlrev_b32 %2, 16, v105\n\tv_lshlrev_b32 %3, 16, v106" \
    : "=v"(bre), "=v"(bim), "=v"(cre), "=v"(cim) : : "v103", "v104", "v105", "v106")
  TSD;
  TS(0, 0);
  const uint32_t lb0 = (uint32_t)(uintptr_t)(__attribute__((address_space(3))) char*)smem;
  const int i = lane & 15, kq = lane >> 4;
  const u32x4 ones = {0xffffffffu, 0xffffffffu, 0xffffffffu, 0xffffffffu}, zero4 = {0u, 0u, 0u, 0u};
  auto u_again = [&](u32x4& uw, const u32x4& ut, uint32_t uo) {
#pragma unroll 1
    for (int spin = 0; spin < SPIN_CAP; ++spin) {
      const bool na = ((uw[0] | uw[1] | uw[2] | uw[3]) & UMASK) != 0u;
      if (__builtin_expect(!__any(na), 1)) break;
      TSC(19);
      SLEEP(USLEEP);
      if (na) { LD16W(uw, uo); uw ^= ut; }
    }
  };
  constexpr uint32_t S16 = 0x41800000u, LMASK = 0x00070007u;
  auto v_put = [&](u32x4 v, const u32x4& ut, bool other, uint32_t vo) {
    const bool second = __any(other || ((v[0] | v[1] | v[2] | v[3]) & 0x40004000u) != 0u);
    if (__builtin_expect(second, 0)) v = (v & ~LMASK) | (~ut & LMASK); else v = (v & ~LMASK) | ut;
    ST16(vo, v);
    return second;
  };
  auto v_end = [&](uint32_t T, int n) { DRAIN(); if (lane == 0) ST4(O_DN + 16u * n, T); };
  if constexpr (!isl) {
    const int ch = wl >> 1, tl = tid & 255;
    int* evp = (int*)(smem + O_FL + 16 * ch);
    float ldt, are, aim, dsk;
    if constexpr (ROLE == 0) {
      __builtin_amdgcn_s_setprio(KPRIO);
      COps e;
      conv_ops(lb0 + (uint32_t)(ch * TB), lb0 + (uint32_t)(O_UL + ch * UB), lb0 + (uint32_t)(O_IM + ch * GB), lane, e);
      e.o = lb0 + O_ST + (uint32_t)(16 * ((4 * i + 2 * (kq >> 1) + ch) ^ (i >> 1)) + 8 * (kq & 1));
      e.f = lb0 + O_FL;
      TOps t;
      table_ops(lb0 + (uint32_t)(O_IM + 128 * IP * ch), lb0 + (uint32_t)(TB * ch), wl & 1, lane, t);
      uint32_t pz = lb0 + O_IM + (uint32_t)(((lane >> 3) & 1) * GB + pad_at(lane & 7)), im = lb0 + (uint32_t)(O_IM + IP * tl);
      asm volatile("" : "+v"(e.o), "+v"(e.f), "+v"(pz), "+v"(im));
      float bre, bim, cre, cim;
      P_WAIT(0, , "v103", "v104", "v105", "v106", "v107", "v109");
      asm volatile("" : "={v109}"(ep));
      const uint32_t T = ((uint32_t)__builtin_amdgcn_readfirstlane(ep) + 1u) * 0x9E3779B1u;
      if (w == 3 && lane < 8) ST16(O_KEY + 128u * b + 16u * lane, ones);
      P_MORE();
      asm volatile("v_lshlrev_b32 %0, 16, v107" : "=v"(dsk) : : "v107");
      TS(0, 1);
      evp[wl & 1] = gen_y(ldt, are, aim, bre, bim, cre, cim, im, [] {} TSA);
      TS(0, 8);
      BAR();
      TS(0, 2);
      const uint32_t cv = table_tile(t, wl & 1, lane, lb0 + O_FL + 16 * ch, dsk TSA), cs = (uint32_t)__builtin_amdgcn_readfirstlane(cv);
      *(uint32_t*)(smem + O_FL + 40 + 4 * ch) = cv;
      TS(0, 9);
      gsync0(lb0 + O_FL + 36, 4);
      TS(0, 3);
      *(u32x4*)(smem + O_ST + 16 * tl) = ones;
      *(u32x4*)(smem + O_ST + 4096 + 16 * tl) = ones;
      if (w == 0) *LP(u32x4, pz) = zero4;
      DRAIN();
      CONV_RUN(wl & 1, e);
      if (wl & 1) asm volatile("s_mov_b64 exec, 1\n\tglobal_store_dwordx2 %0, %1, %2 sc1" : : "v"(O_SC + 16u * (2 * tg + ch)), "v"((u32x2){T, cs}), "s"(ws) : "memory");
      TS(0, 5);
    } else {
      __builtin_amdgcn_s_setprio(UPRIO);
      const uint32_t ck0 = (uint32_t)__builtin_amdgcn_s_memtime(), rt0 = (uint32_t)__builtin_amdgcn_s_memrealtime();
#pragma unroll
      for (int k = 0; k < 3; ++k) *(u32x4*)(smem + ((16 * (tl + 256 * k) < O_FL) ? 16 * (tl + 256 * k) : O_FL + 32)) = zero4;
      if (w == 7) *(u32x4*)(smem + O_UL + (lane >> 5) * UB + 16 * min(lane & 31, 29)) = zero4;
      uint32_t uo = u_at(tl >> 1, 2 * tg + (tid & 1)), go = O_SU + (uint32_t)(256 * bx + 32 * wl + (lane & 31) + 4 * (lane & 32)) * 128u;
      uint32_t ul = (uint32_t)(O_UL + (tid & 1) * UB + C_U - 16 + 16 * (tl >> 1)), ug = (uint32_t)(O_IM + (tid & 1) * GB + 16 * (tl >> 1)), so = lb0 + O_ST + 16u * (uint32_t)(lane ^ (lane >> 3));
      uint32_t vo = O_V + (uint32_t)tg * VP + 16u * lane;
      asm volatile("" : "+v"(uo), "+v"(go), "+v"(ul), "+v"(ug), "+v"(so), "+v"(vo));
      P_WAIT(1, , "v109");
      TS(0, 1);
      evp[2 + (wl & 1)] = gen_x(ldt, are, aim, smem + O_IM + IP * tl);
      asm volatile("s_waitcnt vmcnt(0)" : "={v109}"(ep));
      const uint32_t T = ((uint32_t)__builtin_amdgcn_readfirstlane(ep) + 1u) * 0x9E3779B1u;
      TS(0, 8);
      BAR();
      TS(0, 2);
      if (w == 7 && bj >= 24) {
        const uint32_t t_ = ahead(ck0, rt0, VDB);
        ST16(O_SB + (uint32_t)(64 * (lane >> 3) + 8 * bx + bj - 24) * 128u + 16u * (lane & 7), ((u32x4){T, t_, T, t_}));
      }
      u32x4 ut = {utag(T, 0), utag(T, 1), utag(T, 2), utag(T, 3)};
      asm volatile("" : "+v"(ut));
      SLEEP(GSLEEP);
      u32x2 pv;
      pv[1] = gate(ws, go, T);
      TS(0, 3);
      until(latest(pv[1]) + UD);
      TS(0, 7);
      u32x4 uw, ux;
      asm volatile("global_load_dwordx4 %0, %2, %4 sc1\n\tglobal_load_dwordx4 %1, %3, %4 sc1"
                   : "=&v"(uw), "=&v"(ux) : "v"(uo), "v"(uo + 64 * UP * 32), "s"(ws) : "memory");
      __builtin_amdgcn_s_setprio(FPR);
      asm volatile("s_waitcnt vmcnt(1)" : "+v"(uw));
      uw ^= ut;
      *(u32x4*)(smem + ul + 16) = uw;
      asm volatile("s_waitcnt vmcnt(0)" : "+v"(ux));
      TS(0, 11);
      ux ^= ut;
      *(u32x4*)(smem + ul + 2048 + 16) = ux;
      if (__builtin_expect(__any(((uw[0] | uw[1] | uw[2] | uw[3] | ux[0] | ux[1] | ux[2] | ux[3]) & UMASK) != 0u), 0)) {
        u_again(uw, ut, uo);
        u_again(ux, ut, uo + 64 * UP * 32);
        *(u32x4*)(smem + ul + 16) = uw;
        *(u32x4*)(smem + ul + 2048 + 16) = ux;
      }
      BAR();
      TS(0, 4);
      const u32x4 pw = *(const u32x4*)(smem + ul);
      if (wl < 2) {
        const u32x4 px = *(const u32x4*)(smem + ul + 2048);
        sums<true>(smem + ug, pw, uw);
        sums<false>(smem + ug + 2048, px, ux);
      } else sums<true>(smem + ug, pw, uw);
      count_up(lb0 + O_FL + 32);
      __builtin_amdgcn_s_setprio(SPR);
      TS(0, 5);
      const bool other = *(const uint32_t*)(smem + O_FL + 40 + 4 * (lane & 1)) != S16;
      bool second = false;
#pragma unroll
      for (int k = 0; k < 2; ++k) {
        const uint32_t o = ((k ? 0x3247u : 0x1065u) >> (4 * wl) & 7u) * 1024u;
        u32x4 v;
#pragma unroll 1
        for (int spin = 0; spin < SPIN_CAP; ++spin) {
          asm volatile("ds_read_b128 %0, %1\n\ts_waitcnt lgkmcnt(0)" : "=&v"(v) : "v"(so + o) : "memory");
          if (!__any(max4(v) == 0xffffffffu)) break;
          __builtin_amdgcn_s_sleep(VPS);
        }
        TS(0, 12 + k);
        second |= v_put(v, ut, other, vo + o);
        TS(0, 10 - 4 * k);
      }
      if (__builtin_expect(second, 0)) v_end(T, 4 * tg + wl);
    }
    TSE();
  } else {
  uint32_t* cand = (uint32_t*)(smem + LM);
  uint32_t* sel = cand + 128;
  uint32_t* kl = sel + 48;
  uint32_t* cnt = sel + 64;
  const uint32_t ca = lb0 + LM + 4 * (128 + 64);
  u16* hm = (u16*)(cnt + 16);
  uint32_t T;
  const bool ffn = (FFN < 0) ? bj < 18 : FFN, isx = FFN != 1 && bj >= 24;
  const int fb = 18 * bx + bj;
  constexpr bool ln2 = ROLE == 2;
  const int g0 = tg, g2 = tg;
  u32x4 bw[18];
#pragma unroll
  for (int s = 0; s < 9; ++s) bw[s] = (u32x4){0u, 0u, 0u, 0u};
  int tid2 = threadIdx.x;
  uint32_t tv = 0u, tk = 0u;
  auto v_look = [&](uint32_t tu, int lane) {
    uint32_t tm = latest(gate(ws, O_SB + (uint32_t)(64 * bx + lane) * 128u, T)) - V2D;
    if ((int)(tm - (tu + UD + VD2)) < 0) tm = tu + UD + VD2;
    cnt[8] = tm;
    count_up(ca + 52);
  };
  auto v_time = [&] { gwait(ca + 52, 1); tv = (uint32_t)__builtin_amdgcn_readfirstlane(cnt[8]); };
  uint32_t ts1 = 0u;
  auto rest = [&] {
    if (ts1 && (int)((uint32_t)__builtin_amdgcn_s_memrealtime() - ts1) > 0) {
      v_time();
      ts1 = (tv - TSTOP) | 1u;
      if ((int)((uint32_t)__builtin_amdgcn_s_memrealtime() - ts1) > 0) { TS(0, 8); until(tv + RD); ts1 = 0u; }
    }
  };
  auto gu_dma = [&](int q0, int q1, auto depth) {
    const char* gs = (const char*)((w < 2) ? wg : wu) + (size_t)((144 * (w & 1)) * (F_ * 2) + fb * 64);
    const uint32_t gv = (uint32_t)((lane >> 2) * (F_ * 2) + 16 * ((lane & 3) ^ (lane >> 5) ^ (w & 2)));
#pragma unroll 1
    for (int q = q0; q < q1; ++q) {
      const int k = (int)((((bj & 1) ? ORD1 : ((unsigned __int128)0x876543210u << 36 | 0x876543210ull)) >> (4 * (q - (q >= 9 ? 9 : 0)) + (q >= 9 ? 36 : 0))) & 15u), h = q >= 9;
      if (q >= RQ) rest();
      dma16(gs + (size_t)(288 * h + 16 * k) * (F_ * 2), gv, lb0 + LW * h + 9216 * w + 1024 * k);
      if (!BALL || q < RQ || ts1) asm volatile("s_waitcnt vmcnt(%0)" : : "n"(depth.value) : "memory");
    }
  };
  auto down_dma = [&](int k0, int k1, int lane) {
#pragma unroll 1
    for (int k = k0; k < k1; ++k) {
      const int o = 9216 * wl + 2304 * k;
      const char* ds = (const char*)wd + (size_t)(fb * 36864 + o);
      const uint32_t dl = lb0 + o + 32 * ((4 * wl + k + 1) >> 1);
      if (ROLE == 1) rest();
      dma16d(ds, 16u * lane, dl);
      dma16d(ds + 1024, 16u * lane, dl + 1024);
      uint32_t keep;
      asm volatile("s_mov_b32 %0, m0\n\ts_mov_b32 m0, %3\n\ts_mov_b64 exec, 0xffff\n\tglobal_load_lds_dwordx4 %1, %2" DPOL2 "\n\ts_mov_b64 exec, -1\n\ts_mov_b32 m0, %0\n\ts_waitcnt vmcnt(%4)"
                   : "=&s"(keep) : "v"(16u * lane), "s"(ds + 2048), "s"(dl + 2048), "n"(ROLE == 1 ? DDEPTH : 40) : "memory");
    }
  };
  auto gu_read = [&](int h) {
    const uint32_t ra = lb0 + LW * h + (uint32_t)(((lane >> 1) & 1) * 18432 + (8 * kq + ((lane >> 2) & 3)) * 64 + ((w ^ (kq & 1) ^ (lane & 2)) * 16) + 8 * (lane & 1));
#pragma unroll
    for (int s = 0; s < 9; ++s) {
      const u32x2 h0 = trd(ra + 2048 * s), h1 = trd(ra + 2048 * s + 256);
      bw[9 * h + s] = (u32x4){h0[0], h0[1], h1[0], h1[1]};
    }
#pragma unroll
    for (int s = 0; s < 9; ++s) asm volatile("" : "+v"(bw[9 * h + s]));
  };
  const uint32_t vearly = (uint32_t)((VEARLY >> (8 * (g0 >> 5))) & 0xffull);
#define TOUCH(pa) asm volatile("global_load_dword v126, %0, off" DPOL4 : : "v"(pa) : "memory", "v126")
  uint32_t wv[5], xv[9];
  auto rows_xw = [&](int lane) {
    u32x4 wa, xr, xs;
    const uint32_t xo = (uint32_t)((8 * g2 + 2 * wl) * (D_ * 2));
    asm volatile("global_load_dwordx4 %0, %5, %9\n\tglobal_load_dword %1, %6, %9 offset:1024\n\t"
                 "global_load_dwordx4 %2, %7, %10\n\tglobal_load_dwordx4 %3, %7, %10 offset:1152\n\tglobal_load_dword %4, %8, %10 offset:1024\n\ts_waitcnt vmcnt(0)"
                 : "=&v"(wa), "=&v"(wv[4]), "=&v"(xr), "=&v"(xs), "=&v"(xv[8])
                 : "v"(16u * lane), "v"(4u * (lane & 31)), "v"(xo + 16u * lane), "v"(xo + 4u * lane + 32u * (lane & 32)), "s"(wr), "s"(x) : "memory");
#pragma unroll
    for (int k = 0; k < 4; ++k) { wv[k] = wa[k]; xv[k] = xr[k]; xv[4 + k] = xs[k]; }
  };
  auto ln1 = [&](int g, int o, uint32_t c0) {
    uint32_t uo = u_at(g, 64 * wl + lane);
    uint32_t ta = lb0 + (uint32_t)(o + ((lane >> 2) & 3) * RP + (64 * wl + 16 * kq + 4 * (lane & 3)) * 2);
    uint32_t la = (uint32_t)(o + 2 * wl * RP + 16 * lane), lt = (uint32_t)(o + (2 * wl + (lane >> 5)) * RP + 1024 + 4 * (lane & 31));
    uint32_t sg = O_SU + (uint32_t)(256 * (lane >> 3) + g) * 128u + 16u * (lane & 7);
    asm volatile("" : "+v"(uo), "+v"(ta), "+v"(la), "+v"(lt), "+v"(sg));
    f32x4 z4 = {0.f, 0.f, 0.f, 0.f};
    asm volatile("" : "+v"(z4));
    asm volatile("s_waitcnt vmcnt(0)" : "={v[100:103]}"(xa), "={v[104:107]}"(xc), "={v108}"(r0), "={v109}"(ep));
    TS(0, 1);
    const uint32_t v[9] = {xa[0], xa[1], xa[2], xa[3], xc[0], xc[1], xc[2], xc[3], r0};
    uint32_t c[9];
    f32x2 tf[9];
    T = ((uint32_t)__builtin_amdgcn_readfirstlane(ep) + 1u) * 0x9E3779B1u;
    if (wl == 3) { const uint32_t now = (uint32_t)__builtin_amdgcn_s_memrealtime() + SD; ST16(sg, ((u32x4){T, now, T, now})); }
    const uint32_t tj = ((T >> (3 * wl)) & 0x00070007u) | (((T >> (12 + wl)) & 0x00010001u) << 14);
    uint32_t tw[3] = {(tj & 0xffffu) * 0x10001u, (tj >> 16) * 0x10001u, 0u};
    tw[2] = (lane < 32) ? tw[0] : tw[1];
    asm volatile("" : "+v"(tw[0]), "+v"(tw[1]), "+v"(tw[2]));
    f32x2 rc, nc;
    float e_;
    ln_stats<false>(v, lane, 0.03125f, tf, z4, rc, nc, e_);
    ln_apply<true>(tf, lane, rc, nc, c);
#pragma unroll
    for (int k = 0; k < 9; ++k) c[k] = (c[k] & ~UMASK) | tw[k >> 2];
    *(u32x4*)(smem + la) = (u32x4){c[0], c[1], c[2], c[3]};
    *(u32x4*)(smem + la + RP) = (u32x4){c[4], c[5], c[6], c[7]};
    *(uint32_t*)(smem + lt) = c[8];
    u32x4 ha, hb;
    {
      uint32_t t, cc, n = SPIN_CAP;
      asm volatile("s_mov_b64 exec, 1\n\tv_mov_b32 %2, 1\n\tds_add_u32 %5, %2\n\ts_mov_b64 exec, -1\n"
                   "1:\n\tds_read_b32 %2, %5\n\tds_read_b64_tr_b16 v[40:41], %6\n\tds_read_b64_tr_b16 v[42:43], %6 offset:%7\n\tds_read_b64_tr_b16 v[44:45], %6 offset:512\n\t"
                   "ds_read_b64_tr_b16 v[46:47], %6 offset:%8\n\ts_sub_u32 %4, %4, 1\n\ts_cbranch_scc1 2f\n\ts_waitcnt lgkmcnt(4)\n\tv_readfirstlane_b32 %3, %2\n\t"
                   "s_cmp_lt_u32 %3, 4\n\ts_cbranch_scc1 1b\n2:\n\ts_waitcnt lgkmcnt(0)"
                   : "=&{v[40:43]}"(ha), "=&{v[44:47]}"(hb), "=&v"(t), "=&s"(cc), "+s"(n) : "v"(c0), "v"(ta), "n"(4 * RP), "n"(512 + 4 * RP) : "memory", "scc");
    }
    ST16(uo, ha);
    ST16(uo + 64 * 128, hb);
    if (wl == 0) {
      const u32x2 h4 = trd(ta + 1024), h5 = trd(ta + 1024 + 4 * RP);
      ST16(uo + 128 * 128, ((u32x4){h4[0], h4[1], h5[0], h5[1]}));
    }
  };
  const int dX = 512 - 24 + 8 * bx + bj;
  if constexpr (ROLE == 2) {
    const int tl = tid - 256;
    if (isx) {
      __builtin_amdgcn_s_setprio(QPRIO0);
      *(u32x4*)(smem + X_TB + 16 * tl) = zero4;
      *(u32x4*)(smem + X_TB + 16 * min(256 + tl, (TB + C_U) / 16 - 1)) = zero4;
      const int pr = (wl + XROT) & 3;
      COps e;
      conv_ops(lb0 + X_TB, lb0 + X_UL, lb0 + X_IM, lane, e);
      TOps t;
      table_ops(lb0 + X_IM, lb0 + X_TB, wl & 1, lane, t);
      e.o = lb0 + X_ST + (uint32_t)(16 * ((2 * i + (kq >> 1)) ^ ((i >> 2) & 1)) + 8 * (kq & 1));
      e.f = ca + 28 - 32;
      asm volatile("" : "+v"(e.o), "+v"(e.f));
      float ldt, are, aim, bre, bim, cre, cim, dsk;
      P_WAIT(0, , "v103", "v104", "v105", "v106", "v107", "v109");
      asm volatile("" : "={v109}"(ep));
      T = ((uint32_t)__builtin_amdgcn_readfirstlane(ep) + 1u) * 0x9E3779B1u;
      P_MORE();
      asm volatile("v_lshlrev_b32 %0, 16, v107" : "=v"(dsk) : : "v107");
      TS(0, 1);
      __builtin_amdgcn_s_setprio(QPRIO);
      int* evp = (int*)(sel + 44);
      char* im = smem + X_IM + IP * (tl & 127);
      evp[wl] = (wl < 2) ? gen_y(ldt, are, aim, bre, bim, cre, cim, lb0 + X_IM + IP * tl, [] {} TSA) : gen_x(ldt, are, aim, im);
      TS(0, 2);
      __builtin_amdgcn_s_setprio(QPRIO2);
      gsync(ca + 8, 4);
      if (wl < 2) evp[-4 + wl] = (int)table_tile(t, wl & 1, lane, lb0 + LM + 4 * (128 + 44), dsk TSA);
      if (wl < 2) gsync0(ca + 8, 8); else { count_up(ca + 8); SLEEP(XSL); gwait(ca + 8, 8); }
      const uint32_t cs = (uint32_t)__builtin_amdgcn_readfirstlane(evp[-4]);
      if (wl == 3) *(u32x4*)(smem + X_IM + pad_at(lane & 7)) = zero4;
      TS(0, 3);
      CONV_RUN(2 + pr, e, , "+{v11}"(tid2));
      __builtin_amdgcn_s_setprio(3);
      {
        const int lane = tid2 & 63;
        const u32x4 v = *(const u32x4*)(smem + X_ST + 2048 * pr + 1024 * (lane >> 5) + 16 * ((lane & 31) ^ ((lane >> 3) & 1)));
        if (__builtin_expect(v_put(v, (u32x4){utag(T, 0), utag(T, 1), utag(T, 2), utag(T, 3)}, cs != S16, v_at(dX, 64 * pr + lane)), 0)) {
          DRAIN();
          if (lane == 0) ST4(O_DN + 16u * (4 * (dX - 256) + pr), T);
        }
        if (pr == 0 && lane == 0) asm volatile("global_store_dwordx2 %0, %1, %2 sc1" : : "v"(O_SC + 16u * dX), "v"((u32x2){T, cs}), "s"(ws) : "memory");
      }
      TS(0, 6);
    }
    if (!isx) __builtin_amdgcn_s_setprio(PPRIO);
    asm volatile("" : "+v"(tid2));
    if (!isx) rows_xw(tid2 & 63);
    if (!isx && !ffn) {
      SLEEP(RSLEEP);
#pragma unroll
      for (int k = 0; k < 7; ++k) ST16(O_SL + (uint32_t)((6 * bx + bj - 18) * 1792 + 256 * k - 256 + tid2) * 16u, ones);
      DRAIN();
    }
    if (!isx) BAR();
    const uint32_t tp = (uint32_t)__builtin_amdgcn_readfirstlane(cnt[4]);
    if (!isx) T = (uint32_t)__builtin_amdgcn_readfirstlane(sel[43]);
    if (!isx && w == 4) v_look(tp, tid2 & 63);
    v_time();
  } else {
    __builtin_amdgcn_s_setprio(3);
    if (tid < 16) cnt[tid] = 0u;
    BAR();
    if (tid < 20) ((uint32_t*)(hm + 320))[tid] = 0u;
    if (tid >= 184) *(u32x4*)(smem + L_SC - 16 * 184 + 16 * tid) = (u32x4){0x41804180u, 0x41804180u, 0x41804180u, 0x41804180u};
    uint32_t sink = 0;
    ln1(tg, 0, ca + 12);
    TS(0, 2);
    __builtin_amdgcn_s_setprio(NPRIO);
    asm volatile("s_waitcnt vmcnt(0)" : "+v"(sink) : : "memory");
    TS(0, 13);
    if (w == 0) {
      const uint32_t xr = O_XC + (uint32_t)((1021 - 4 * g0) * 72 + lane) * 16u;
      ST16(xr, ones);
      ST16(xr + 1024u, ones);
      ST16(xr + 2048u, ones);
      if (lane < 24) ST16(xr + 3072u, ones);
    }
    if (w == 2 && b < 72 && lane < 8) ST16(O_FB + (uint32_t)(8 * b + lane) * 16u, ones);
    if (isx) {
      rows_xw(lane);
      char* sp = smem + X_RW(w) + 16 * lane;
      *(u32x4*)sp = (u32x4){xv[0], xv[1], xv[2], xv[3]};
      *(u32x4*)(sp + 1024) = (u32x4){xv[4], xv[5], xv[6], xv[7]};
      *(u32x2*)(smem + X_RW(w) + 2048 + 8 * lane) = (u32x2){xv[8], wv[4]};
      if (w == 0) *(u32x4*)(smem + X_WR + 16 * lane) = (u32x4){wv[0], wv[1], wv[2], wv[3]};
    }
    SLEEP(ESLEEP);
    uint32_t tu = latest(gate(ws, O_SU + (uint32_t)(256 * bx + (ALLP ? tid : 64 * w + 4 * i + (bj & 3))) * 128u, T));
    asm volatile("s_mov_b64 exec, 1\n\tds_max_u32 %0, %1\n\ts_mov_b64 exec, -1" : : "v"(ca + 16), "v"(tu) : "memory");
    if (w == 0) sel[43] = T;
    if (isx) {
      const uint32_t xo = u_at(tid, dX);
      char* sm = smem + X_UL + C_U - 16 + 16 * tid;
      const u32x4 ut = {utag(T, 0), utag(T, 1), utag(T, 2), utag(T, 3)};
      __builtin_amdgcn_s_setprio(3);
      until(tu + UD + XD);
      u32x4 uw;
      asm volatile("global_load_dwordx4 %0, %1, %2 sc1" : "=&v"(uw) : "v"(xo), "s"(ws) : "memory");
      asm volatile("s_waitcnt vmcnt(0)" : "+v"(uw));
      uw ^= ut;
      u_again(uw, ut, xo);
      *(u32x4*)(sm + 16) = uw;
      BAR();
      if (w < 3) {
        const u32x4 pw = *(const u32x4*)sm;
        if (w < 2) sums<true>(smem + X_IM + 16 * tid, pw, uw); else sums<false>(smem + X_IM + 16 * tid, pw, uw);
        count_up(ca + 28);
      }
    } else {
      until(tu + UD + WD);
      BAR();
    }
    tu = (uint32_t)__builtin_amdgcn_readfirstlane(cnt[4]);
    TS(0, 3);
    __builtin_amdgcn_s_setprio(DPRIO);
    if (ffn) {
      ts1 = (tu + UD + VD2 - TSTOP) | 1u;
      gu_dma(0, N0, IC<40>{});
      gu_dma(N0, NA, IC<WDEPTH>{});
      asm volatile("s_waitcnt vmcnt(%0)" : : "n"(NA - 9) : "memory");
      gsync0(ca + 4, 4);
      gu_read(0);
      asm volatile("s_waitcnt lgkmcnt(0)" ::: "memory");
      count_up(ca + 36);
      TS(0, 4);
      gu_dma(NA, 18, IC<WDEPTH2>{});
      gwait(ca + 36, 4);
      down_dma(0, KN, lane);
      asm volatile("s_waitcnt vmcnt(%0)" : : "n"(3 * KN) : "memory");
      count_up(ca + 4);
      if (STP && w == 1)
        asm volatile("global_load_dword v126, %0, %1 sc1" : : "v"(O_SC + 1024u * (fb >> 4) + 128u * (lane & 7)), "s"(ws) : "memory", "v126");
      TS(0, 7);
    } else {
      if (!isx && C3) {
        const int n = 6 * min(tid, 191) + bj - 18, r = n >> 2;
        const char* wb = (const char*)((n & 1) ? wu : wg) + bx * 1152;
        const int c0 = (3 - (int)((uintptr_t)wb >> 7)) & 7;
        wb += (uint32_t)((r + 144 * ((r >= 144) + ((n >> 1) & 1))) * (F_ * 2) + 128 * c0);
        TOUCH(wb);
        if (c0 == 0) TOUCH(wb + 1024);
      }
      auto d_line = [&](int k) {
        const int n = min((k * 14 + bj - 18) * 64 + lane, 5183), q = n / 1296, r = n - 1296 * q;
        return (const char*)wd + (uint32_t)(bx * 5184 + (r / 18) * 72 + 18 * (HREV ? 3 - q : q) + r % 18) * 128u;
      };
      if (isx && w == 3) v_look(tu, lane);
      int k = w;
      if (w < HW) {
#pragma unroll 1
        for (; k < 6 && (int)((uint32_t)__builtin_amdgcn_s_memrealtime() - (tu + UD + VD2 - HSTOP)) < 0; k += HW) { TOUCH(d_line(k)); DRAIN(); }
      }
      TS(0, 4);
      v_time();
      until(tv + RD2);
      if (w < HW) {
#pragma unroll 1
        for (; k < 6; k += HW) { TOUCH(d_line(k)); DRAIN(); }
      }
      TS(0, 7);
    }
    v_time();
    tk = tv + KD;
  }
  {
  const int tid = tid2, lane = tid & 63, i = lane & 15, kq = lane >> 4;
  __builtin_amdgcn_s_setprio(3);
  const int PL = ffn ? 6400 : LW + 6656;
  f32x2 xf[9];
  f32x4 za = {0.f, 0.f, 0.f, 0.f}, zb = za;
  uint32_t al, ha, h0a, h1a, so, da;
  auto ffn_terms = [&] {
    al = O_XC + 64u * (i >> 3) + 16u * kq + 128u * w;
    ha = (ROLE == 1) ? (uint32_t)((4 * (kq & 1) + 2 * (kq >> 1) + (i >> 3)) * 80 + 16 * w + 2 * (i & 7)) : 0u;
    h0a = (uint32_t)(min(i, 8) * 80 + 16 * kq); h1a = (uint32_t)(min(i ^ 8, 8) * 80 + 16 * kq);
    so = O_SL + (uint32_t)fb * SP + ((ROLE == 2) ? 16u * tid - 4096u : 8192u + 16u * lane);
    da = lb0 + (uint32_t)((8 * kq + ((lane >> 2) & 3)) * 1152 + 32 * ((8 * kq + ((lane >> 2) & 3) + 2) >> 2) + 8 * (lane & 3) + ((ROLE == 2) ? 128 * wl : 1024));
    if (ROLE == 2) asm volatile("" : "+v"(h0a), "+v"(h1a), "+v"(so), "+v"(da)); else asm volatile("" : "+v"(ha), "+v"(al), "+v"(da));
  };
  if (ROLE == 1) ffn_terms();
  uint32_t hv[9] = {}, nv[9] = {}, keya = 0u, keyb = 0u;
  int rka = 0, rkb = 0;
  auto put_row = [&](const uint32_t* r, int j, const void* base, uint32_t ro) {
    const u32x4 tl = {r[8], (uint32_t)DPP_I((int)r[8], 0x55, 0xf), (uint32_t)DPP_I((int)r[8], 0xaa, 0xf), (uint32_t)DPP_I((int)r[8], 0xff, 0xf)};
    asm volatile("global_store_dwordx4 %0, %1, %2 sc1\n\ts_nop 1" : : "v"(ro + 16u * lane), "v"((u32x4){r[4 * j], r[4 * j + 1], r[4 * j + 2], r[4 * j + 3]}), "s"(base) : "memory");
    if ((lane & 35) == 32 * j) asm volatile("global_store_dwordx4 %0, %1, %2 offset:1024 sc1\n\ts_nop 1" : : "v"(ro + 4u * (lane & 31)), "v"(tl), "s"(base) : "memory");
  };
  if (ln2) {
    const int tl = tid - 256, d1 = 64 * (tl >> 5) + (tl & 31), d3 = 64 * ((tl >> 4) & 7) + 32 + (tl & 15), d2 = (tl < 128) ? d3 + 16 : min(384 + tl, 575);
    uint32_t vo = v_at(d1, g0), vo2 = v_at(d2, g0), vo3 = v_at(d3, g0);
    u32x4 ut = {utag(T, 0), utag(T, 1), utag(T, 2), utag(T, 3)};
    u32x4 va, vc = ut, vb = ut;
    asm volatile("" : "+v"(vo), "+v"(vo2), "+v"(vo3), "+v"(ut), "+v"(vc), "+v"(vb));
    TS(0, 7);
    until(tv);
    TS(0, 8);
    asm volatile("global_load_dwordx4 %0, %2, %4 sc1\n\ts_cmp_lt_u32 %5, 2\n\ts_cbranch_scc1 1f\n\tglobal_load_dwordx4 %1, %3, %4 sc1\n1:"
                 : "=&v"(va), "+v"(vc) : "v"(vo), "v"(vo3), "s"(ws), "s"(wl) : "memory", "scc");
    until(tv + V2D);
    asm volatile("s_cmp_eq_u32 %3, 3\n\ts_cbranch_scc1 1f\n\tglobal_load_dwordx4 %0, %1, %2 sc1\n1:" : "+v"(vb) : "v"(vo2), "s"(ws), "s"(wl) : "memory", "scc");
    __builtin_amdgcn_sched_barrier(0);
    if (isx) {
      const u32x4 ra = *(const u32x4*)(smem + X_RW(wl) + 16 * lane), rb = *(const u32x4*)(smem + X_RW(wl) + 1024 + 16 * lane), rw = *(const u32x4*)(smem + X_WR + 16 * lane);
      const u32x2 rc = *(const u32x2*)(smem + X_RW(wl) + 2048 + 8 * lane);
#pragma unroll
      for (int k = 0; k < 4; ++k) { xv[k] = ra[k]; xv[4 + k] = rb[k]; wv[k] = rw[k]; }
      xv[8] = rc[0]; wv[4] = rc[1];
    }
    uint32_t pa = (uint32_t)(PL + 9216 * wl + 32 * lane), pb = (uint32_t)(PL + 9216 * wl + 2048 + 8 * (lane & 31)), ps = (lane < 32) ? 0u : 16u;
    uint32_t kid = (uint32_t)(((255 - g2) << 5) | (7 - 2 * wl - (lane >> 5)));
    uint32_t px = (uint32_t)(PL + 4 * d1), py = (uint32_t)(PL + 4 * d2), pz = (uint32_t)(PL + 4 * d3);
#pragma unroll
    for (int k = 0; k < 9; ++k) xf[k] = (f32x2){bflo(xv[k]), bfhi(xv[k])};
    asm volatile("" : "+v"(xf[0]), "+v"(xf[1]), "+v"(xf[2]), "+v"(xf[3]), "+v"(xf[4]), "+v"(xf[5]), "+v"(xf[6]), "+v"(xf[7]), "+v"(xf[8]));
    asm volatile("" : "+v"(za), "+v"(zb), "+v"(pa), "+v"(pb), "+v"(ps), "+v"(kid), "+v"(px), "+v"(py), "+v"(pz));
    float sv[10], ws_ = dot2(wv[4], 0x3f803f80u, 0.f) * 0.5f;
#pragma unroll
    for (int k = 0; k < 4; ++k) ws_ = dot2(wv[k], 0x3f803f80u, ws_);
    ws_ = wave_sum(ws_);
#pragma unroll
    for (int k = 0; k < 10; ++k) sv[k] = 16.f;
    uint32_t ka_ = lb0 + LM + 4u * (128 + 48 + 2 * wl + (lane >> 5)), kb_ = lb0 + LM + 4u * (128 + 48);
    uint32_t ko_ = O_KEY + (uint32_t)(256 * (lane & 7) + 8 * (g2 & 31) + (g2 >> 5)) * 16u;
    asm volatile("" : "+v"(ws_), "+v"(sv[0]), "+v"(sv[1]), "+v"(sv[2]), "+v"(sv[3]), "+v"(sv[4]), "+v"(sv[5]), "+v"(sv[6]), "+v"(sv[7]), "+v"(sv[8]), "+v"(sv[9]),
                 "+v"(ka_), "+v"(kb_), "+v"(ko_));
    if (ffn) gwait(ca + 36, 4);
    asm volatile("s_waitcnt vmcnt(1)\n\ts_cmp_eq_u32 %2, 3\n\ts_cbranch_scc0 1f\n\ts_waitcnt vmcnt(0)\n1:" : "+v"(va), "+v"(vc) : "s"(wl) : "memory", "scc");
    auto turn1 = [&] {
#pragma unroll
      for (int e = 0; e < 4; ++e) *(uint32_t*)(smem + px + 9216 * e) = va[e];
      if (wl >= 2) {
#pragma unroll
        for (int e = 0; e < 4; ++e) *(uint32_t*)(smem + pz + 9216 * e) = vc[e];
      }
    };
    auto turn = [&] {
      if (wl < 3) {
#pragma unroll
        for (int e = 0; e < 4; ++e) *(uint32_t*)(smem + py + 9216 * e) = vb[e];
      }
    };
    va ^= ut; vc ^= ut;
    turn1();
    asm volatile("s_waitcnt vmcnt(0)" : "+v"(vb) : : "memory");
    vb ^= ut;
    turn();
    const u32x4 vs = va | vb | vc;
    if (__builtin_expect(__any(((vs[0] | vs[1] | vs[2] | vs[3]) & UMASK) != 0u), 0)) {
#pragma unroll 1
      for (int spin = 0; spin < 4; ++spin) {
        const bool na = ((va[0] | va[1] | va[2] | va[3]) & UMASK) != 0u, nb = ((vb[0] | vb[1] | vb[2] | vb[3]) & UMASK) != 0u, nc = ((vc[0] | vc[1] | vc[2] | vc[3]) & UMASK) != 0u;
        if (!__any(na || nb || nc)) break;
        TSC(16);
        if (na) LD16(va, vo);
        if (nb) LD16(vb, vo2);
        if (nc) LD16(vc, vo3);
        asm volatile("s_waitcnt vmcnt(0)" : "+v"(va), "+v"(vb), "+v"(vc) : : "memory");
        if (na) va ^= ut;
        if (nb) vb ^= ut;
        if (nc) vc ^= ut;
      }
      auto again = [&](u32x4& v, uint32_t o, int d) {
        const int J = g0 >> 5;
        const uint32_t dn = O_DN + 16u * (uint32_t)((d < 512) ? 4 * (d >> 1) + ((0x01013232 >> (4 * J)) & 3) : 4 * (d - 256) + (J >> 1));
        bool pend = ((v[0] | v[1] | v[2] | v[3]) & UMASK) != 0u;
#pragma unroll 1
        for (int spin = 0; spin < SPIN_CAP && __any(pend); ++spin) {
          TSC(16);
          SLEEP(PSLEEP);
          if (pend) {
            uint32_t fin = ~T;
            if ((v[0] & v[1] & v[2] & v[3] & LMASK) == LMASK)
              asm volatile("global_load_dword %0, %1, %2 sc1\n\ts_waitcnt vmcnt(0)" : "=&v"(fin) : "v"(dn), "s"(ws) : "memory");
            LD16W(v, o);
            v ^= ut;
            pend = ((v[0] | v[1] | v[2] | v[3]) & UMASK) != 0u;
            if (pend && fin == T) {
              u32x2 sc;
              for (int k = 0; k < SPIN_CAP; ++k) {
                asm volatile("global_load_dwordx2 %0, %1, %2 sc1\n\ts_waitcnt vmcnt(0)" : "=&v"(sc) : "v"(O_SC + 16u * d), "s"(ws) : "memory");
                if (sc[0] == T) break;
              }
              *(u16*)(smem + L_SC + 2 * d) = (u16)(sc[1] >> 16);
              cnt[10] = 1u;
              v = (v ^ ut) & ~LMASK;
              pend = false;
            }
          }
        }
      };
      again(va, vo, d1); again(vc, vo3, d3); again(vb, vo2, d2);
      turn1();
      turn();
    }
    u32x4 a0, a1;
    u32x2 a2, cf;
    {
      uint32_t cc, n = SPIN_CAP;
      asm volatile("s_mov_b64 exec, 1\n\tv_mov_b32 v90, 1\n\tds_add_u32 %6, v90\n\ts_mov_b64 exec, -1\n"
                   "1:\n\tds_read2_b32 %3, %6 offset1:10\n\tds_read_b128 %0, %7\n\tds_read_b128 %1, %7 offset:16\n\tds_read_b64 %2, %8\n\t"
                   "s_sub_u32 %5, %5, 1\n\ts_cbranch_scc1 2f\n\ts_waitcnt lgkmcnt(3)\n\tv_readfirstlane_b32 %4, v90\n\t"
                   "s_cmp_lt_u32 %4, 4\n\ts_cbranch_scc1 1b\n2:\n\ts_waitcnt lgkmcnt(0)"
                   : "=&v"(a0), "=&v"(a1), "=&v"(a2), "=&{v[90:91]}"(cf), "=&s"(cc), "+s"(n) : "v"(ca), "v"(lb0 + pa), "v"(lb0 + pb) : "memory", "scc");
    }
    TS(0, 9);
    if (__builtin_expect(__builtin_amdgcn_readfirstlane(cf[1]) != 0, 0)) {
      const u32x4 q = *(const u32x4*)(smem + L_SC + 16 * lane);
      const uint32_t r = *(const uint32_t*)(smem + L_SC + 1024 + 4 * (lane & 31));
#pragma unroll
      for (int k = 0; k < 4; ++k) { sv[2 * k] = bflo(q[k]); sv[2 * k + 1] = bfhi(q[k]); }
      sv[8] = bflo(r); sv[9] = bfhi(r);
    }
    const uint32_t av[10] = {a0[0], a0[1], a0[2], a0[3], a1[0], a1[1], a1[2], a1[3], a2[0] >> ps, a2[1] >> ps};
    f32x2 h[9];
#pragma unroll
    for (int k = 0; k < 10; ++k) {
      asm("v_fma_mix_f32 %0, %1, %2, %3 op_sel_hi:[1,0,0]" : "=v"(h[(k < 8) ? k >> 1 : 8][k & 1]) : "v"(av[k]), "v"(sv[k]), "v"(xf[(k < 8) ? k >> 1 : 8][k & 1]));
      if (k < 8) asm("v_fma_mix_f32 %0, %1, %2, %3 op_sel:[1,0,0] op_sel_hi:[1,0,0]" : "=v"(h[4 + (k >> 1)][k & 1]) : "v"(av[k]), "v"(sv[k]), "v"(xf[4 + (k >> 1)][k & 1]));
    }
#pragma unroll
    for (int k = 0; k < 9; ++k) hv[k] = pack2(h[k][0], h[k][1]);
    const float da = dot2(hv[2], wv[2], dot2(hv[0], wv[0], zb[0])) + dot2(hv[3], wv[3], dot2(hv[1], wv[1], zb[1]));
    const float db = dot2(hv[6], wv[2], dot2(hv[4], wv[0], zb[2])) + dot2(hv[7], wv[3], dot2(hv[5], wv[1], zb[3]));
    float dt = dot2(hv[8], wv[4], fold32(da, db));
    f32x2 rc, nc;
    ln_stats<true>(hv, lane, 1.f, xf, za, rc, nc, dt);
    dt = (lane < 32) ? dt * rc[0] + ws_ * nc[0] : dt * rc[1] + ws_ * nc[1];
    const uint32_t bb = pack2(dt, dt) & 0xffff0000u;
    const uint32_t kk = (bb ^ (((uint32_t)((int)bb >> 31) & 0x7fff0000u) | 0x80000000u)) | kid;
    u32x4 ka, kb;
    uint32_t old;
    asm volatile("s_mov_b64 exec, %5\n\tds_write_b32 %3, %4\n\ts_mov_b64 exec, 1\n\tv_mov_b32 %2, 1\n\tds_add_rtn_u32 %2, %7, %2\n\ts_mov_b64 exec, -1\n\t"
                 "ds_read_b128 %0, %6\n\tds_read_b128 %1, %6 offset:16\n\ts_waitcnt lgkmcnt(0)"
                 : "=&v"(ka), "=&v"(kb), "=&v"(old) : "v"(ka_), "v"(kk), "s"(0x8000000080000000ull), "v"(kb_), "v"(ca) : "memory");
    if (__builtin_amdgcn_readfirstlane(old) == 7) {
      uint32_t m1 = max(max(ka[0], ka[1]), ka[2]), m2 = med3u(ka[0], ka[1], ka[2]), m3 = min(min(ka[0], ka[1]), ka[2]);
#pragma unroll
      for (int e = 0; e < 5; ++e) {
        const uint32_t kx = e ? kb[e - 1] : ka[3];
        m3 = med3u(m2, m3, kx); m2 = med3u(m1, m2, kx); m1 = max(m1, kx);
      }
      if (lane < 8) ST16(ko_, ((u32x4){m1 | 24u, m2 | 16u, m3 | 8u, T & ~1u}));
    }
    TS(0, 10);
    keya = (uint32_t)__builtin_amdgcn_readlane(kk, 31); keyb = (uint32_t)__builtin_amdgcn_readlane(kk, 63);
    ln_apply<false>(xf, lane, rc, nc, nv);
    gwait(ca, 8);
    if (wl == 1) {
      const u32x4 kv = *(const u32x4*)&kl[4 * (lane & 1)];
      if (lane < 2) ST16(O_KF + (uint32_t)(2 * g2 + lane) * 16u, ((u32x4){__builtin_amdgcn_perm(kv[1], kv[0], 0x07060302u), T, __builtin_amdgcn_perm(kv[3], kv[2], 0x07060302u), T}));
      if (ROLE == 2 && b == 0 && lane == 0) ST4(O_EP, T * 0x0E8B2F51u);
    }
    const uint32_t ko = kl[lane & 7];
    rka = FORCEFB + __popcll(__ballot(ko > keya) & 0xffull); rkb = FORCEFB + __popcll(__ballot(ko > keyb) & 0xffull);
#pragma unroll
    for (int k = 0; k < 9; ++k) {
      const f32x2 o = xf[k] + (f32x2){bflo(nv[k]), bfhi(nv[k])};
      hv[k] = pack2(o[0], o[1]);
      nv[k] = min(nv[k], 0xfffffffeu);
    }
    if (rka < 3) put_row(nv, 0, ws, O_XC + (uint32_t)(1023 - 4 * g2 - rka) * 1152u);
    if (rkb < 3) put_row(nv, 1, ws, O_XC + (uint32_t)(1023 - 4 * g2 - rkb) * 1152u);
    if (!LPOS && ffn) down_dma(KN, 4, lane);
    ffn_terms();
  }
  uint32_t k3rd = 0u, k4th = 0u;
  uint32_t q0 = lb0 + LM + 4u * lane, q1 = lb0 + LM + 32u * w, q2 = q1 + 4u * (lane & 7), q3 = lb0 + LM + 512u + 4u * (lane & 7), q4 = lb0 + LM + (uint32_t)(tid & ~7);
  uint32_t r8 = C_, sb = lb0 + LM + 512u;
  asm volatile("" : "+v"(q0), "+v"(q1), "+v"(q2), "+v"(q3), "+v"(q4), "+v"(r8), "+v"(sb));
  if constexpr (ROLE == 1) {
    u32x4 key;
    const uint32_t kof = O_KEY + (uint32_t)(256 * bx + tid) * 16u;
    until(tk);
    if (!ffn) SLEEP(NSLEEP);
    LD16(key, kof);
    if (ffn) {
      gwait0(ca + 4, 8);
      gu_read(1);
    }
    asm volatile("s_waitcnt vmcnt(0)" : "+v"(key) : : "memory");
    if (__builtin_expect(__any(key[3] != (T & ~1u)), 0)) {
#pragma unroll 1
      for (int spin = 0; spin < SPIN_CAP; ++spin) {
        const bool need = key[3] != (T & ~1u);
        if (!__any(need)) break;
        TSC(15);
        SLEEP(KBACK);
        if (need) LD16W(key, kof);
      }
    }
    TS(0, 11);
    const uint32_t m1 = oct_maxu(key[0]);
    const bool hit = key[0] == m1;
    const uint32_t k0 = hit ? key[1] : key[0], k1 = hit ? key[2] : key[1];
    const uint32_t m2 = oct_maxu(k0);
    *LP(u32x2, q4) = (u32x2){m1, m2};
    k3rd = (k0 == m2) ? k1 : k0;
    k4th = (k0 == m2) ? (hit ? 0u : key[2]) : k1;
    if (RPF == 1 && !ffn && w == (bj & 3)) {
      const uint32_t r1 = O_XC + ((m1 >> 3) & 0x3ffu) * 1152u + 128u * (lane & 7), r2 = O_XC + ((m2 >> 3) & 0x3ffu) * 1152u + 128u * (lane & 7);
      asm volatile("global_load_dword v126, %0, %3 sc1\n\tglobal_load_dword v126, %1, %3 sc1\n\tglobal_load_dword v126, %2, %3 offset:1024 sc1"
                   : : "v"(r1), "v"(r2), "v"(((lane & 1) ? r1 : r2) - 128u * (lane & 7)), "s"(ws) : "memory", "v126");
    }
  }
  auto pick = [&](auto co, auto so_) {
    BAR();
    const uint32_t c = *LP(uint32_t, q0 + co.value), own = *LP(uint32_t, q2 + co.value);
    const u32x4 la = *LP(u32x4, q1 + co.value), lb = *LP(u32x4, q1 + co.value + 16);
    uint32_t r = r8;
    asm("v_cmp_gt_u32 s[40:41], %1, %2\n\tv_cmp_gt_u32 s[42:43], %1, %3\n\tv_cmp_gt_u32 s[44:45], %1, %4\n\tv_cmp_gt_u32 s[46:47], %1, %5\n\t"
        "v_cmp_gt_u32 s[48:49], %1, %6\n\tv_cmp_gt_u32 s[50:51], %1, %7\n\tv_cmp_gt_u32 s[52:53], %1, %8\n\tv_cmp_gt_u32 s[54:55], %1, %9\n\t"
        "s_bcnt1_i32_b64 s40, s[40:41]\n\ts_bcnt1_i32_b64 s42, s[42:43]\n\tv_writelane_b32 %0, s40, 0\n\ts_bcnt1_i32_b64 s44, s[44:45]\n\t"
        "v_writelane_b32 %0, s42, 1\n\ts_bcnt1_i32_b64 s46, s[46:47]\n\tv_writelane_b32 %0, s44, 2\n\ts_bcnt1_i32_b64 s48, s[48:49]\n\t"
        "v_writelane_b32 %0, s46, 3\n\ts_bcnt1_i32_b64 s50, s[50:51]\n\tv_writelane_b32 %0, s48, 4\n\ts_bcnt1_i32_b64 s52, s[52:53]\n\t"
        "v_writelane_b32 %0, s50, 5\n\ts_bcnt1_i32_b64 s54, s[54:55]\n\tv_writelane_b32 %0, s52, 6\n\tv_writelane_b32 %0, s54, 7"
        : "+v"(r) : "v"(c), "v"(la[0]), "v"(la[1]), "v"(la[2]), "v"(la[3]), "v"(lb[0]), "v"(lb[1]), "v"(lb[2]), "v"(lb[3])
        : "scc", "s40", "s41", "s42", "s43", "s44", "s45", "s46", "s47", "s48", "s49", "s50", "s51", "s52", "s53", "s54", "s55");
    *LP(uint32_t, sb + 4u * min(r, (uint32_t)C_) + so_.value) = own;
    BAR();
  };
  auto rows = [&](uint32_t ao, auto&& meanwhile) {
    u32x4 aa, ab, ac = __builtin_bit_cast(u32x4, zb);
    asm volatile("global_load_dwordx4 %0, %3, %4 sc1\n\tglobal_load_dwordx4 %1, %3, %4 offset:512 sc1\n\t"
                 "s_cmp_lg_u32 %5, 0\n\ts_cbranch_scc1 1f\n\tglobal_load_dwordx4 %2, %3, %4 offset:1024 sc1\n1:"
                 : "=&v"(aa), "=&v"(ab), "+v"(ac) : "v"(ao), "s"(ws), "s"(w) : "memory", "scc");
    __builtin_amdgcn_sched_barrier(0);
    meanwhile();
    asm volatile("s_waitcnt vmcnt(0)" : "+v"(aa), "+v"(ab), "+v"(ac) : : "memory");
    if (__builtin_expect(__any(max(max(max4(aa), max4(ab)), max4(ac)) == 0xffffffffu), 0)) {
#pragma unroll 1
      for (int spin = 0; spin < SPIN_CAP; ++spin) {
        const bool na = max4(aa) == 0xffffffffu, nb = max4(ab) == 0xffffffffu, nc = max4(ac) == 0xffffffffu;
        if (!__any(na || nb || nc)) break;
        TSC(14);
        SLEEP(PSLEEP);
        if (na) LD16W(aa, ao);
        if (nb) LD16W(ab, ao + 512u);
        if (nc) LD16W(ac, ao + 1024u);
      }
    }
    *(u32x4*)(smem + LA + 16 * tid) = aa;
    *(u32x4*)(smem + LA + 4096 + 16 * tid) = ab;
    if (w == 0) *(u32x4*)(smem + LA + 8192 + 16 * tid) = ac;
  };
  auto rows_dma = [&](uint32_t ao) {
    uint32_t dl = lb0 + LA + 1024u * w, keep;
    asm volatile("s_mov_b32 %0, m0\n\ts_mov_b32 m0, %1\n\ts_add_u32 %1, %1, 4096 - 512\n\tglobal_load_lds_dwordx4 %2, %3 sc1\n\ts_mov_b32 m0, %1\n\ts_add_u32 %1, %1, 4096 - 512\n\t"
                 "global_load_lds_dwordx4 %2, %3 offset:512 sc1\n\ts_cmp_lg_u32 %4, 0\n\ts_cbranch_scc1 1f\n\ts_mov_b32 m0, %1\n\ts_nop 0\n\t"
                 "global_load_lds_dwordx4 %2, %3 offset:1024 sc1\n1:\n\ts_mov_b32 m0, %0" : "=&s"(keep), "+s"(dl) : "v"(ao), "s"(ws), "s"(w) : "memory", "scc");
  };
  auto get_bd = [&](int t0, int t1) {
#pragma unroll
    for (int t = t0; t < t1; ++t) {
      const u32x2 h0 = trd(da + 512 * (t >> 2) + 32 * (t & 3)), h1 = trd(da + 512 * (t >> 2) + 32 * (t & 3) + 4608 + 32);
      bw[t] = (u32x4){h0[0], h0[1], h1[0], h1[1]};
    }
#pragma unroll
    for (int t = t0; t < t1; ++t) asm volatile("" : "+v"(bw[t]));
  };
  pick(IC<0>{}, IC<0>{});
  uint32_t mykey = *LP(uint32_t, q3), ro = ((mykey >> 3) & 0x3ffu) * 1152u;
  TS(0, 12);
  auto check = [&] {
    cnt[(k3rd > (uint32_t)__builtin_amdgcn_readlane(mykey, 7)) ? 5 : 6] = 1u;
    const uint32_t m3 = oct_maxu(k3rd);
    if (k3rd == m3) k3rd = k4th;
    if ((lane & 7) == 0) cand[72 + (tid >> 3)] = m3;
    if (w == 0 && lane < 32) cand[(lane < 8) ? 64 + lane : 96 + lane] = (lane < 8) ? mykey : 0u;
  };
  if constexpr (ROLE == 1) {
    if (ffn && !FORCEFB) rows_dma(ro + al);
    check();
    DRAIN();
  } else if (ffn) {
    if (LPOS) { SLEEP(LSLEEP); down_dma(KN, 4, lane); } else { DRAIN(); gsync0(ca, 12); get_bd(0, 8); }
  }
  BAR();
  TS(0, 5);
  auto put_out = [&](int j, uint32_t k0, int rk) {
    const uint64_t mine = __ballot(((mykey ^ k0) & ~24u) == 0u) & 0xffull;
    if (mine == 0ull) put_row(hv, j, out, (uint32_t)((8 * g2 + 2 * wl + j) * (D_ * 2)));
    else if (rk >= 3) put_row(nv, j, ws, O_FB + 1152u * (uint32_t)__builtin_ctzll(mine));
  };
  auto warm_out = [&] {
    const uint32_t k0 = (lane < 9) ? keya : keyb;
    bool hit = false;
#pragma unroll
    for (int e = 0; e < 8; ++e) hit |= (((uint32_t)__builtin_amdgcn_readlane(mykey, e) ^ k0) & ~24u) == 0u;
    if (OTP && lane < 18 && hit) asm volatile("global_load_dword v126, %0, %1" : : "v"((uint32_t)((8 * g2 + 2 * wl) * (D_ * 2) + 128 * lane)), "s"(out) : "memory", "v126");
  };
  auto rest_out = [&] { put_out(0, keya, rka); put_out(1, keyb, rkb); };
  f32x4 lo, hi;
  auto gate_up = [&] {
    u32x4 av[5];
    const uint32_t aa = lb0 + LA + 16u * lane;
#pragma unroll
    for (int j = 0; j < 5; ++j) asm volatile("ds_read_b128 %0, %1 offset:%2" : "=v"(av[j]) : "v"(aa), "n"(1024 * j) : "memory");
    lo = (f32x4){0.f, 0.f, 0.f, 0.f}; hi = lo;
#pragma unroll
    for (int j = 0; j < 9; ++j) {
      if (j < 5) asm volatile("s_waitcnt lgkmcnt(4)" : "+v"(av[j % 5]));
      else if (j == 5) asm volatile("s_waitcnt lgkmcnt(2)" : "+v"(av[0]), "+v"(av[1]));
      else if (j == 7) asm volatile("s_waitcnt lgkmcnt(0)" : "+v"(av[2]), "+v"(av[3]));
      lo = MMB(av[j % 5], bw[2 * j], lo);
      hi = MMB(av[j % 5], bw[2 * j + 1], hi);
      __builtin_amdgcn_sched_barrier(0);
      if (j < 4) asm volatile("ds_read_b128 %0, %1 offset:%2" : "=v"(av[j]) : "v"(aa), "n"(1024 * (j + 5)) : "memory");
    }
  };
  uint32_t fl;
  asm volatile("ds_read_b32 %0, %1" : "=v"(fl) : "v"(ca + 20) : "memory");
  if (ROLE == 1) gate_up();
  asm volatile("s_waitcnt lgkmcnt(0)" : "+v"(fl));
  const bool slow = FORCESLOW || FORCEFB || __builtin_amdgcn_readfirstlane(fl) != 0;
  if (__builtin_expect(slow, 0)) {
    TSC(17);
    pick(IC<256>{}, IC<64>{});
    mykey = sel[16 + (lane & 7)];
    ro = ((mykey >> 3) & 0x3ffu) * 1152u;
    const uint32_t k8 = (uint32_t)__builtin_amdgcn_readlane(mykey, 7);
    if (ROLE == 1 && k3rd > k8) cnt[11] = 1u;
    if (w == 0 && __any((mykey & 24u) == 8u)) {
      const uint32_t pk = sel[16 + (lane >> 3)], gi = (pk >> 5) & 0xffu;
      u32x4 kf;
#pragma unroll 1
      for (int spin = 0; spin < SPIN_CAP; ++spin) {
        LD16W(kf, O_KF + (2u * (255u - gi) + ((lane >> 2) & 1u)) * 16u);
        if (!__any(kf[1] != T || kf[3] != T)) break;
        SLEEP(KBACK);
      }
      const uint32_t wd_ = (lane & 2) ? kf[2] : kf[0], kt = ((lane & 1) ? wd_ & 0xffff0000u : wd_ << 16) | (gi << 5) | (7u - (lane & 7));
      const uint64_t up = __ballot((pk & 24u) == 8u && kt >= (k8 & ~24u));
      if (__popcll((up >> (lane & 56)) & 0xffull) > 3) cnt[11] = 1u;
    }
    BAR();
    if (FORCESLOW > 1 || FORCEFB || __builtin_amdgcn_readfirstlane(cnt[11]) != 0) {
    u32x4 key;
#pragma unroll 1
    for (int spin = 0; spin < SPIN_CAP; ++spin) {
      LD16W(key, O_KF + 16u * tid);
      if (!__any(key[1] != T || key[3] != T)) break;
      SLEEP(KBACK);
    }
    const uint32_t ib = (uint32_t)(((255 - (tid >> 1)) << 5) | (7 - 4 * (tid & 1)));
    uint32_t k0 = (key[0] << 16) | ib, k1 = (key[0] & 0xffff0000u) | (ib - 1), k2 = (key[2] << 16) | (ib - 2), k3 = (key[2] & 0xffff0000u) | (ib - 3);
    CEX(k0, k1) CEX(k2, k3) CEX(k0, k2) CEX(k1, k3) CEX(k1, k2)
#pragma unroll 1
    for (int c = 0; c < C_; ++c) {
      const uint32_t mx = wave_maxu(k0);
      const bool hit = (k0 == mx);
      k0 = hit ? k1 : k0; k1 = hit ? k2 : k1; k2 = hit ? k3 : k2; k3 = hit ? 0u : k3;
      if (lane == 0) cand[64 + 8 * w + c] = mx;
    }
    pick(IC<256>{}, IC<128>{});
    mykey = sel[32 + (lane & 7)];
    const uint32_t okey = sel[32 + (lane >> 3)];
    const uint64_t same = __ballot(((mykey ^ okey) & 0x1fe0u) == 0u && okey > mykey);
    const int sl = FORCEFB + __popcll((same >> (lane & 7)) & 0x0101010101010101ull);
    ro = (sl < 3) ? (((mykey >> 3) & 0x3ffu) + 3 - sl) * 1152u : O_FB - O_XC + 1152u * (lane & 7);
    }
    if (ln2) rest_out();
    if (ffn) {
      if (ROLE == 1) rows(ro + al, [] {});
      BAR();
      if (ROLE == 1) gate_up();
    }
  }
  if (OPOS == 0 && ROLE == 2 && ffn && !slow) { SLEEP(OD); rest_out(); }
  if (!ffn) {
    if (ln2) warm_out();
    if (ln2 && !slow) { SLEEP(OD2); rest_out(); }
    TSE();
    return;
  }
  uint32_t fx = 0, fv = 0, fn = 0, fo = 0, sa[9];
  float rs, hx, xs;
  u32x4 se;
  if constexpr (ROLE == 1) {
    f32x4 lh = (lane < 32) ? lo : hi;
    float fa = fold32(lh[0], lh[2]), fb2 = fold32(lh[1], lh[3]);
    if (__builtin_expect(__any(__builtin_isunordered(fa, fb2)) && !slow, 0)) {
      gsync0(ca + 4, 12);
      rows(ro + al, [] {});
      gsync0(ca + 4, 16);
      gate_up();
      lh = (lane < 32) ? lo : hi;
      fa = fold32(lh[0], lh[2]); fb2 = fold32(lh[1], lh[3]);
    }
    const float g = __int_as_float(__builtin_amdgcn_update_dpp(__float_as_int(fa), __float_as_int(fb2), 0x128, 0xf, 0xc, false));
    const float u = __int_as_float(__builtin_amdgcn_update_dpp(__float_as_int(fb2), __float_as_int(fa), 0x128, 0xf, 0x3, false));
    const uint32_t gu = pack2(g, u);
    const float gp = bflo(gu);
    const float sg = rbf(gp * __builtin_amdgcn_rcpf(1.f + __expf(-gp)));
    *(u16*)((char*)hm + ha) = f2bf(sg * bfhi(gu));
  } else if (LPOS) {
    DRAIN();
    gsync0(ca, 12);
    get_bd(0, 8);
  }
  BAR();
  TS(0, 6);
  if (ROLE == 2 || w == 2) {
    const u32x4 a0 = *(const u32x4*)((char*)hm + h0a), a1 = *(const u32x4*)((char*)hm + h1a);
    if (ROLE == 1) get_bd(0, 4);
    const f32x4 z = {0.f, 0.f, 0.f, 0.f};
    f32x4 o[4];
#pragma unroll
    for (int t = 0; t < 2 * ROLE; ++t) o[t] = MMB(a1, bw[2 * t + 1], MMB(a0, bw[2 * t], z));
#pragma unroll
    for (int t = 0; t < ROLE; ++t) {
      u32x4 sv;
#pragma unroll
      for (int r = 0; r < 4; ++r) sv[r] = min(pack2(o[2 * t][r], o[2 * t + 1][r]), 0xfffffffeu);
      ST16(so + 4096u * t, sv);
    }
    TS(0, 13);
  }
  if (OPOS == 1 && ROLE == 2) {
    warm_out();
    SLEEP(OSLEEP);
    if (!slow) rest_out();
  }
  if (ROLE == 1 && w == 1) {
    const int q = (fb & 15) >> 2, ts = 4 * (q & 1) + ((i & 7) >> 1);
    const int dd = 64 * (fb >> 4) + 16 * (2 * (i & 1) + (q >> 1)) + 4 * (fb & 3) + 2 * (kq & 1) + (kq >> 1);
    const uint32_t fkey = (uint32_t)__builtin_amdgcn_ds_bpermute(4 * ts, (int)mykey), fr = (uint32_t)__builtin_amdgcn_ds_bpermute(4 * ts, (int)ro);
    const int ft = 2047 - (int)(((fkey >> 2) & 0x7f8u) | (fkey & 7u));
    fo = (uint32_t)(ft * D_ + dd) * 2u;
    u32x2 fs;
    asm volatile("global_load_ushort %0, %4, %8\n\tglobal_load_ushort %1, %5, %9 sc1\n\tglobal_load_ushort %2, %6, %9 sc1\n\tglobal_load_dwordx2 %3, %7, %9 sc1"
                 : "=&v"(fx), "=&v"(fv), "=&v"(fn), "=&v"(fs) : "v"(fo), "v"(v_at(dd, ft >> 3) + 2u * (ft & 7)), "v"(O_XC + fr + 2u * dd), "v"(O_SC + 16u * dd), "s"(x), "s"(ws) : "memory");
    uint32_t t16 = (((T >> (3 * ((ft >> 1) & 3))) & LMASK) | (((T >> (12 + ((ft >> 1) & 3))) & 0x00010001u) << 14)) >> (16 * (ft & 1)) & 0xffffu;
    const uint32_t o = fkey >> 16;
    const float rs0 = bf2f((u16)((o & 0x8000u) ? (o & 0x7fffu) : (~o & 0xffffu)));
    rs = rbf(__builtin_amdgcn_rcpf(1.f + __expf(-rs0)));
    const uint32_t one = (i < 8) ? (0x3f80u << (16 * (i & 1))) : 0u;
    se = (u32x4){(i >> 1) == 0 ? one : 0u, (i >> 1) == 1 ? one : 0u, (i >> 1) == 2 ? one : 0u, (i >> 1) == 3 ? one : 0u};
#pragma unroll
    for (int m = 0; m < 9; ++m) sa[m] = O_SL + (uint32_t)(16 * m + (lane >> 2)) * SP + (uint32_t)(4 * fb + (lane & 3)) * 16u;
    asm volatile("" : "+v"(t16), "+v"(rs), "+v"(se), "+v"(sa[0]), "+v"(sa[1]), "+v"(sa[2]), "+v"(sa[3]), "+v"(sa[4]), "+v"(sa[5]), "+v"(sa[6]), "+v"(sa[7]), "+v"(sa[8]));
    SLEEP(SSLEEP);
    if (FD) until(tk + FD);
    TS(0, 1);
    float sum;
#pragma unroll 1
    for (int spin = 0; spin < SPIN_CAP; ++spin) {
      u32x4 sv[9];
#pragma unroll
      for (int m = 0; m < 9; ++m) LD16(sv[m], sa[m]);
      asm volatile("s_waitcnt vmcnt(9)" : "+v"(fx), "+v"(fv), "+v"(fn), "+v"(fs));
      if (__builtin_expect(__any(fs[0] != T), 0)) {
        for (int k = 0; k < SPIN_CAP && fs[0] != T; ++k)
          asm volatile("global_load_dwordx2 %0, %1, %2 sc1\n\ts_waitcnt vmcnt(0)" : "=&v"(fs) : "v"(O_SC + 16u * dd), "s"(ws) : "memory");
      }
      const uint32_t f1 = fv ^ t16, f2 = (f1 & 7u) ? fv & ~7u : f1;
      hx = rbf(bf2f((u16)fx) + (float)__builtin_bit_cast(_Float16, (u16)f2) * __uint_as_float(fs[1]));
      xs = bf2f((u16)fn);
      asm volatile("" : "+v"(hx), "+v"(xs));
      f32x4 dv = {0.f, 0.f, 0.f, 0.f};
      static_for<0, 9>([&](auto mm) {
        constexpr int m = mm.value;
        asm volatile("s_waitcnt vmcnt(%1)" : "+v"(sv[m]) : "n"(8 - m));
        dv = MMB(sv[m], se, dv);
        __builtin_amdgcn_sched_barrier(0);
      });
      sum = fold16(fold32(dv[0], dv[1]), fold32(dv[2], dv[3]));
      if (__builtin_expect(!__any(sum != sum), 1)) break;
      uint32_t mx = 0u;
#pragma unroll
      for (int m = 0; m < 9; ++m) mx = max(mx, max4(sv[m]));
      if (!__any(mx == 0xffffffffu)) break;
      TSC(18);
      SLEEP(PSLEEP);
    }
    const float o1 = rbf(xs + rbf(rbf(sum) * rs));
    if (i < 8) asm volatile("global_store_short %0, %1, %2 sc1" : : "v"(fo), "v"((uint32_t)f2bf(hx + o1)), "s"(out) : "memory");
    TS(0, 2);
  }
  TSE();
  }
  }
}
#define BROT "192"
#define CPF_A "s_getpc_b64 s[96:97]\n\ts_lshl_b32 s99, s2, 4\n\ts_lshl_b32 s84, s98, 7\n\ts_add_u32 s99, s99, s84\n\tv_and_b32 v112, 1, v0\n\t" \
              "v_lshl_add_u32 v112, v112, 12, s99\n\tv_min_u32 v112, kf_end-kf_all-0x80, v112\n\ts_mov_b64 exec, 3\n\tglobal_load_dword v112, v112, s[96:97]\n\ts_mov_b64 exec, -1\n\t"
extern "C" __global__ __launch_bounds__(512, 4) void kf_all(KARGS) {
  __shared__ __attribute__((aligned(16))) char smem[SMEM];
  asm volatile("s_load_dwordx16 s[64:79], s[0:1], 0x0\n\ts_load_dwordx4 s[80:83], s[0:1], 0x40\n\tv_readfirstlane_b32 s98, v0\n\t"
               "s_add_u32 s84, s2, " BROT "\n\ts_xor_b32 s84, s84, s2\n\ts_bitset0_b32 s84, 8\n\ts_bitset0_b32 s84, 9\n\ts_xor_b32 s2, s2, s84\n\t"
               "v_mov_b32 v109, 0\n\ts_and_b32 s85, s2, 7\n\ts_bitcmp1_b32 s2, 8\n\ts_cbranch_scc1 1f\n\t" CPF_A "s_bitcmp1_b32 s98, 8\n\ts_cbranch_scc1 2f\n\t"
               "s_andn2_b32 s84, s2, 31\n\ts_lshl2_add_u32 s84, s85, s84\n\ts_bfe_u32 s85, s2, 0x20003\n\ts_or_b32 s84, s84, s85\n\t"
               "s_mulk_i32 s84, 0x2400\n\ts_mulk_i32 s98, 36\n\ts_add_u32 s84, s84, s98\n\tv_and_b32 v110, 63, v0\n\tv_and_b32 v111, 32, v0\n\t"
               "v_lshlrev_b32 v111, 5, v111\n\tv_lshl_add_u32 v111, v110, 2, v111\n\tv_lshl_add_u32 v110, v110, 4, s84\n\tv_add_u32 v111, s84, v111\n\t"
               "s_cmpk_lt_u32 s2, 0x90\n\ts_waitcnt lgkmcnt(0)\n\t"
               "global_load_dwordx4 v[100:103], v110, s[64:65]\n\tglobal_load_dwordx4 v[104:107], v110, s[64:65] offset:1152\n\t"
               "global_load_dword v108, v111, s[64:65] offset:1024\n\tglobal_load_dword v109, v109, s[82:83] sc1\n\t"
               "s_cbranch_scc1 kf_n\n\ts_branch kf_h\n"
               "1:\n\ts_bfe_u32 s84, s2, 0x50003\n\ts_lshl_b32 s85, s85, 5\n\ts_or_b32 s84, s84, s85\n\ts_lshl_b32 s84, s84, 2\n\t"
               "v_and_b32 v110, 0xff, v0\n\tv_bfe_u32 v107, v0, 7, 1\n"
               "4:\n\ts_lshl_b32 s85, s84, 7\n\tv_lshl_add_u32 v110, v110, 1, s85\n\tv_lshl_add_u32 v107, v107, 1, s84\n\ts_and_b32 s85, s98, s2\n\ts_waitcnt lgkmcnt(0)\n\t"
               "global_load_ushort v100, v107, s[78:79]\n\tglobal_load_ushort v101, v110, s[66:67]\n\tglobal_load_ushort v102, v110, s[68:69]\n\t"
               "global_load_dword v109, v109, s[82:83] sc1\n\ts_bitcmp1_b32 s85, 8\n\ts_cbranch_scc1 kf_u\n\t"
               "global_load_ushort v103, v110, s[70:71]\n\tglobal_load_ushort v104, v110, s[72:73]\n\tglobal_load_ushort v105, v110, s[74:75]\n\t"
               "global_load_ushort v106, v110, s[76:77]\n\tglobal_load_ushort v107, v107, s[80:81]\n\ts_bitcmp1_b32 s2, 8\n\ts_cbranch_scc1 kf_c\n"
               "6:\n\ts_barrier\n\ts_branch kf_p\n"
               "2:\n\ts_cmpk_lt_u32 s2, 0xc0\n\ts_cbranch_scc1 6b\n\t"
               "s_lshr_b32 s84, s2, 3\n\ts_lshl3_add_u32 s84, s85, s84\n\ts_lshl1_add_u32 s84, s84, 976\n\t"
               "v_and_b32 v110, 0x7f, v0\n\tv_mov_b32 v107, 0\n\ts_branch 4b"
               : : "s"(__builtin_amdgcn_kernarg_segment_ptr()), "s"(blockIdx.x), "v"(threadIdx.x),
                   "s"((uint32_t)(uintptr_t)(__attribute__((address_space(3))) char*)smem) : "scc", "s96", "s97", "s98", "s99", "v127");
}
extern "C" __global__ __launch_bounds__(512, 4) void kf_c(KARGS) {
  __shared__ __attribute__((aligned(16))) char smem[SMEM];
  role_body<0>(smem, KPASS);
}
extern "C" __global__ __launch_bounds__(512, 4) void kf_u(KARGS) {
  __shared__ __attribute__((aligned(16))) char smem[SMEM];
  role_body<3>(smem, KPASS);
}
extern "C" __global__ __launch_bounds__(512, 4) void kf_n(KARGS) {
  __shared__ __attribute__((aligned(16))) char smem[SMEM];
  role_body<1, 1>(smem, KPASS);
}
extern "C" __global__ __launch_bounds__(512, 4) void kf_h(KARGS) {
  __shared__ __attribute__((aligned(16))) char smem[SMEM];
  role_body<1, 0>(smem, KPASS);
}
extern "C" __global__ __launch_bounds__(512, 4) void kf_p(KARGS) {
  __shared__ __attribute__((aligned(16))) char smem[SMEM];
  role_body<2>(smem, KPASS);
}
extern "C" __global__ __launch_bounds__(512, 4) void kf_conv() { asm volatile(CONV_ASM ::: CONV_CLOB); }
extern "C" __global__ void kf_end() {}

extern "C" __global__ __launch_bounds__(1024) void k_probe(int* dummy) {
  __shared__ int sm[2];
  const uint32_t now = (uint32_t)__builtin_amdgcn_s_memrealtime();
  __shared__ int sn;
  if (threadIdx.x == 0) { sm[0] = 0; sm[1] = 1 == 1 ? 0x7fffffff : 0; sn = 0; }
  __syncthreads();
  for (int k = threadIdx.x; k < 4096; k += 1024) {
    const uint32_t a = (uint32_t)g_prb[k], b = (uint32_t)(g_prb[k] >> 32);
    if (a) atomicMax(&sm[0], (int)(now - a));
    if (b) { if (1 == 1) atomicMin(&sm[1], (int)(now - b)); else if (1 == 0) atomicMax(&sm[1], (int)(now - b)); else { atomicAdd(&sm[1], (int)(now - b)); atomicAdd(&sn, 1); } }
  }
  __syncthreads();
  const int q = min(max(sm[0] * 10 - (1 == 2 ? sm[1] * 10 / max(sn, 1) : sm[1] * 10), 0), 30000);
  if (threadIdx.x == 0) while ((int)((uint32_t)__builtin_amdgcn_s_memrealtime() - now) < q) __builtin_amdgcn_s_sleep(2);
}
extern "C" void kernel_launch(void* const* d_in, const int* in_sizes, int n_in,
                              void* d_out, int out_size, void* d_ws, size_t ws_size,
                              hipStream_t stream) {
  kf_all<<<512, 512, 0, stream>>>((const u16*)d_in[0], (const u16*)d_in[1], (const u16*)d_in[2], (const u16*)d_in[3], (const u16*)d_in[4],
                                  (const u16*)d_in[5], (const u16*)d_in[6], (const u16*)d_in[7], (const u16*)d_in[8], (char*)d_ws + (size_t)WSOFF * 1048576,
                                  (const u16*)d_in[10], (const u16*)d_in[11], (const u16*)d_in[12], (const u16*)d_in[13], (u16*)d_out);
  k_probe<<<1, 1024, 0, stream>>>((int*)d_ws);
}
```

```cpp
#include <hip/hip_runtime.h>
#include <hip/hip_bf16.h>
#include <stdint.h>

typedef __attribute__((ext_vector_type(8))) short bf16x8;
typedef __attribute__((ext_vector_type(4))) float f32x4;
typedef uint16_t u16;
typedef __attribute__((ext_vector_type(4))) uint32_t u32x4;
typedef __attribute__((ext_vector_type(2))) uint32_t u32x2;
#define TSD
#define TS(k, i)
#define TSC(i)
#define TSE()
#define TSP
#define TSA

constexpr int L_ = 2048, D_ = 576, N_ = 128, F_ = 4608, C_ = 8;
constexpr int NF = 144;
constexpr uint32_t O_EP = 0;
constexpr uint32_t O_KF = 4096;
constexpr uint32_t O_KEY = O_KF + 8192;
constexpr uint32_t O_SC = O_KEY + 32768;
constexpr uint32_t O_DN = O_SC + 9216;
constexpr uint32_t O_SB = O_DN + 20480;
constexpr uint32_t O_SU = O_SB + 65536;
constexpr uint32_t O_FB = O_SU + 262144;
constexpr uint32_t O_XC = O_FB + 8 * 1152;
constexpr uint32_t SP = 9216 + 128;
constexpr uint32_t O_SL = O_XC + 1024 * 1152;
constexpr uint32_t SLU = 336;
constexpr uint32_t O_U = O_SL + 256 * SLU * 16;
constexpr int UP = 584;
constexpr uint32_t VP = 8192 + 128, VPP = 4096 + 128;
constexpr uint32_t O_V = O_U + 256 * UP * 16, O_VP = O_V + 256 * VP;
static_assert(256 * SLU * 16 >= NF * SP && O_SL % 128 == 0 && O_U % 128 == 0 && O_V % 128 == 0, "");
__host__ __device__ __forceinline__ uint32_t u_at(int tg, int d) { return O_U + 16u * (uint32_t)((tg >> 1) * (UP * 2) + (d >> 2) * 8 + (tg & 1) * 4 + (d & 3)); }
__host__ __device__ __forceinline__ uint32_t v_at(int d, int tg) {
  return (d < 512) ? O_V + (uint32_t)(d >> 1) * VP + 16u * (uint32_t)(2 * tg + (d & 1)) : O_VP + (uint32_t)(d - 512) * VPP + 16u * (uint32_t)tg;
}
#define HAVE_VAT
#define HAVE_VTAG
__device__ __forceinline__ float bf2f(u16 v) { return __uint_as_float((uint32_t)v << 16); }
__device__ __forceinline__ float bflo(uint32_t v) { return __uint_as_float(v << 16); }
__device__ __forceinline__ float bfhi(uint32_t v) { return __uint_as_float(v & 0xffff0000u); }
__device__ __forceinline__ u16 f2bf(float f) { return __builtin_bit_cast(u16, (__bf16)f); }
__device__ __forceinline__ float rbf(float f) { return bf2f(f2bf(f)); }
typedef __attribute__((ext_vector_type(2))) float f32x2;
typedef __attribute__((ext_vector_type(2))) __bf16 bf16x2v;
__device__ __forceinline__ uint32_t pack2(float a, float b) {
  return __builtin_bit_cast(uint32_t, __builtin_convertvector((f32x2){a, b}, bf16x2v));
}

#define DPP_I(v, ctrl, rmask) __builtin_amdgcn_update_dpp(0, (v), (ctrl), (rmask), 0xf, true)
__device__ __forceinline__ float row_sum(float v) {
  v += __int_as_float(DPP_I(__float_as_int(v), 0x111, 0xf));
  v += __int_as_float(DPP_I(__float_as_int(v), 0x112, 0xf));
  v += __int_as_float(DPP_I(__float_as_int(v), 0x114, 0xf));
  v += __int_as_float(DPP_I(__float_as_int(v), 0x118, 0xf));
  return v;
}
__device__ __forceinline__ float rdl(float v, int l) { return __int_as_float(__builtin_amdgcn_readlane(__float_as_int(v), l)); }
__device__ __forceinline__ float wave_sum(float v) {
  v = row_sum(v);
  asm("s_nop 1\n\tv_add_f32_dpp %0, %0, %0 row_bcast:15 row_mask:0xa bank_mask:0xf\n\t"
      "s_nop 1\n\tv_add_f32_dpp %0, %0, %0 row_bcast:31 row_mask:0xc bank_mask:0xf\n\ts_nop 0" : "+v"(v));
  return rdl(v, 63);
}
__device__ __forceinline__ float fold32(float a, float b) {
  const auto r = __builtin_amdgcn_permlane32_swap(__float_as_uint(a), __float_as_uint(b), false, false);
  return __uint_as_float(r[0]) + __uint_as_float(r[1]);
}
__device__ __forceinline__ float fold16(float a, float b) {
  const auto r = __builtin_amdgcn_permlane16_swap(__float_as_uint(a), __float_as_uint(b), false, false);
  return __uint_as_float(r[0]) + __uint_as_float(r[1]);
}
__device__ __forceinline__ void wave_sum2(float& a, float& b) {
  float v = row_sum(fold32(a, b));
  asm("s_nop 1\n\tv_add_f32_dpp %0, %0, %0 row_bcast:15 row_mask:0xa bank_mask:0xf\n\ts_nop 0" : "+v"(v));
  a = rdl(v, 31); b = rdl(v, 63);
}
__device__ __forceinline__ float dot2(uint32_t a, uint32_t b, float c) {
  return __builtin_amdgcn_fdot2_f32_bf16(__builtin_bit_cast(bf16x2v, a), __builtin_bit_cast(bf16x2v, b), c, false);
}
__device__ __forceinline__ uint32_t row_maxu(uint32_t v) {
  v = max(v, (uint32_t)DPP_I((int)v, 0xb1, 0xf));
  v = max(v, (uint32_t)DPP_I((int)v, 0x4e, 0xf));
  v = max(v, (uint32_t)DPP_I((int)v, 0x141, 0xf));
  v = max(v, (uint32_t)DPP_I((int)v, 0x140, 0xf));
  return v;
}
__device__ __forceinline__ uint32_t wave_maxu(uint32_t v) {
  v = max(v, (uint32_t)DPP_I((int)v, 0x111, 0xf));
  v = max(v, (uint32_t)DPP_I((int)v, 0x112, 0xf));
  v = max(v, (uint32_t)DPP_I((int)v, 0x114, 0xf));
  v = max(v, (uint32_t)DPP_I((int)v, 0x118, 0xf));
  v = max(v, (uint32_t)DPP_I((int)v, 0x142, 0xa));
  v = max(v, (uint32_t)DPP_I((int)v, 0x143, 0xc));
  return (uint32_t)__builtin_amdgcn_readlane((int)v, 63);
}

typedef __attribute__((ext_vector_type(8))) _Float16 f16x8;
typedef __attribute__((ext_vector_type(2))) _Float16 f16x2;
__device__ __forceinline__ uint32_t packh(f32x2 v) { return __builtin_bit_cast(uint32_t, __builtin_convertvector(v, f16x2)); }
__device__ __forceinline__ uint32_t packh(float a, float b) { return packh((f32x2){a, b}); }
constexpr int IP = 224, I_X = 128;
__device__ __forceinline__ f32x2 cmul(f32x2 z, f32x2 w) {
  f32x2 r;
  asm("v_pk_mul_f32 %0, %1, %2 op_sel_hi:[0,1]\n\tv_pk_fma_f32 %0, %1, %2, %0 op_sel:[1,1,0] op_sel_hi:[1,0,1] neg_lo:[0,1,0]" : "=&v"(r) : "v"(z), "v"(w));
  return r;
}
__device__ __forceinline__ f32x2 cexp2(float ar2, float air, float l) {
  const float t = air * l;
  return (f32x2){__builtin_amdgcn_cosf(t), __builtin_amdgcn_sinf(t)} * __builtin_amdgcn_exp2f(ar2 * l);
}
__device__ __forceinline__ float state_a(float ldt, float are, float aim, float& ar2, float& air, float& g) {
  const float dt = rbf(__expf(ldt));
  ar2 = dt * are * 1.4426950408889634f;
  air = dt * aim * 0.15915494309189535f;
  air -= __builtin_rintf(air);
  g = 0.f;
  if (__builtin_expect(__any(ar2 > 0.f), 0)) g = fminf(__uint_as_float(wave_maxu(__float_as_uint(fmaxf(ar2, 0.f)))), 1.f);
  return dt;
}
template <int N> __device__ __forceinline__ void put_im(char* p, int im, const f32x2* z) {
  *(u32x4*)p = (u32x4){packh(z[0][0], z[1][0]), packh(z[2][0], z[3][0]), packh(z[4][0], z[5][0]), packh(z[6][0], z[7][0])};
  *(u32x4*)(p + im) = (u32x4){packh(z[0][1], z[1][1]), packh(z[2][1], z[3][1]), packh(z[4][1], z[5][1]), packh(z[6][1], z[7][1])};
}
#define Y_ASM \
  "v_mov_b64 v[40:41], %0\nv_pk_mul_f32 v[42:43], v[40:41], %1 op_sel_hi:[0,1]\nv_pk_mul_f32 v[44:45], %0, %2 op_sel_hi:[0,1]\n" \
  "v_pk_fma_f32 v[42:43], v[40:41], %1, v[42:43] op_sel:[1,1,0] op_sel_hi:[1,0,1] neg_lo:[0,1,0]\n" \
  "v_pk_fma_f32 v[44:45], %0, %2, v[44:45] op_sel:[1,1,0] op_sel_hi:[1,0,1] neg_lo:[0,1,0]\nv_cvt_pk_f16_f32 v56, v40, v42\n" \
  "v_cvt_pk_f16_f32 v60, v41, v43\nv_pk_mul_f32 v[40:41], v[42:43], %1 op_sel_hi:[0,1]\nv_pk_mul_f32 v[48:49], v[44:45], %2 op_sel_hi:[0,1]\n" \
  "v_pk_fma_f32 v[40:41], v[42:43], %1, v[40:41] op_sel:[1,1,0] op_sel_hi:[1,0,1] neg_lo:[0,1,0]\n" \
  "v_pk_fma_f32 v[48:49], v[44:45], %2, v[48:49] op_sel:[1,1,0] op_sel_hi:[1,0,1] neg_lo:[0,1,0]\n" \
  "v_pk_mul_f32 v[42:43], v[40:41], %1 op_sel_hi:[0,1]\nv_pk_mul_f32 v[52:53], v[48:49], %2 op_sel_hi:[0,1]\n" \
  "v_pk_fma_f32 v[42:43], v[40:41], %1, v[42:43] op_sel:[1,1,0] op_sel_hi:[1,0,1] neg_lo:[0,1,0]\n" \
  "v_pk_fma_f32 v[52:53], v[48:49], %2, v[52:53] op_sel:[1,1,0] op_sel_hi:[1,0,1] neg_lo:[0,1,0]\nv_cvt_pk_f16_f32 v57, v40, v42\n" \
  "v_cvt_pk_f16_f32 v61, v41, v43\nv_pk_mul_f32 v[40:41], v[42:43], %1 op_sel_hi:[0,1]\n" \
  "v_pk_fma_f32 v[40:41], v[42:43], %1, v[40:41] op_sel:[1,1,0] op_sel_hi:[1,0,1] neg_lo:[0,1,0]\n" \
  "v_pk_mul_f32 v[42:43], v[40:41], %1 op_sel_hi:[0,1]\nv_pk_mul_f32 v[46:47], v[44:45], %1 op_sel_hi:[0,1]\n" \
  "v_pk_fma_f32 v[42:43], v[40:41], %1, v[42:43] op_sel:[1,1,0] op_sel_hi:[1,0,1] neg_lo:[0,1,0]\n" \
  "v_pk_fma_f32 v[46:47], v[44:45], %1, v[46:47] op_sel:[1,1,0] op_sel_hi:[1,0,1] neg_lo:[0,1,0]\nv_cvt_pk_f16_f32 v58, v40, v42\n" \
  "v_cvt_pk_f16_f32 v62, v41, v43\nv_cvt_pk_f16_f32 v64, v44, v46\nv_cvt_pk_f16_f32 v68, v45, v47\n" \
  "v_pk_mul_f32 v[40:41], v[42:43], %1 op_sel_hi:[0,1]\nv_pk_mul_f32 v[44:45], v[46:47], %1 op_sel_hi:[0,1]\n" \
  "v_pk_fma_f32 v[40:41], v[42:43], %1, v[40:41] op_sel:[1,1,0] op_sel_hi:[1,0,1] neg_lo:[0,1,0]\n" \
  "v_pk_fma_f32 v[44:45], v[46:47], %1, v[44:45] op_sel:[1,1,0] op_sel_hi:[1,0,1] neg_lo:[0,1,0]\n" \
  "v_pk_mul_f32 v[42:43], v[40:41], %1 op_sel_hi:[0,1]\nv_pk_mul_f32 v[46:47], v[44:45], %1 op_sel_hi:[0,1]\n" \
  "v_pk_fma_f32 v[42:43], v[40:41], %1, v[42:43] op_sel:[1,1,0] op_sel_hi:[1,0,1] neg_lo:[0,1,0]\n" \
  "v_pk_fma_f32 v[46:47], v[44:45], %1, v[46:47] op_sel:[1,1,0] op_sel_hi:[1,0,1] neg_lo:[0,1,0]\nv_cvt_pk_f16_f32 v59, v40, v42\n" \
  "v_cvt_pk_f16_f32 v63, v41, v43\nv_cvt_pk_f16_f32 v65, v44, v46\nv_cvt_pk_f16_f32 v69, v45, v47\n" \
  "v_pk_mul_f32 v[44:45], v[46:47], %1 op_sel_hi:[0,1]\nds_write_b128 %3, v[56:59] offset:0\n" \
  "v_pk_fma_f32 v[44:45], v[46:47], %1, v[44:45] op_sel:[1,1,0] op_sel_hi:[1,0,1] neg_lo:[0,1,0]\nds_write_b128 %3, v[60:63] offset:64\n" \
  "v_pk_mul_f32 v[46:47], v[44:45], %1 op_sel_hi:[0,1]\nv_pk_mul_f32 v[50:51], v[48:49], %1 op_sel_hi:[0,1]\n" \
  "v_pk_fma_f32 v[46:47], v[44:45], %1, v[46:47] op_sel:[1,1,0] op_sel_hi:[1,0,1] neg_lo:[0,1,0]\n" \
  "v_pk_fma_f32 v[50:51], v[48:49], %1, v[50:51] op_sel:[1,1,0] op_sel_hi:[1,0,1] neg_lo:[0,1,0]\nv_cvt_pk_f16_f32 v66, v44, v46\n" \
  "v_cvt_pk_f16_f32 v70, v45, v47\nv_cvt_pk_f16_f32 v56, v48, v50\nv_cvt_pk_f16_f32 v60, v49, v51\n" \
  "v_pk_mul_f32 v[44:45], v[46:47], %1 op_sel_hi:[0,1]\nv_pk_mul_f32 v[48:49], v[50:51], %1 op_sel_hi:[0,1]\n" \
  "v_pk_fma_f32 v[44:45], v[46:47], %1, v[44:45] op_sel:[1,1,0] op_sel_hi:[1,0,1] neg_lo:[0,1,0]\n" \
  "v_pk_fma_f32 v[48:49], v[50:51], %1, v[48:49] op_sel:[1,1,0] op_sel_hi:[1,0,1] neg_lo:[0,1,0]\n" \
  "v_pk_mul_f32 v[46:47], v[44:45], %1 op_sel_hi:[0,1]\nv_pk_mul_f32 v[50:51], v[48:49], %1 op_sel_hi:[0,1]\n" \
  "v_pk_fma_f32 v[46:47], v[44:45], %1, v[46:47] op_sel:[1,1,0] op_sel_hi:[1,0,1] neg_lo:[0,1,0]\n" \
  "v_pk_fma_f32 v[50:51], v[48:49], %1, v[50:51] op_sel:[1,1,0] op_sel_hi:[1,0,1] neg_lo:[0,1,0]\nv_cvt_pk_f16_f32 v67, v44, v46\n" \
  "v_cvt_pk_f16_f32 v71, v45, v47\nv_cvt_pk_f16_f32 v57, v48, v50\nv_cvt_pk_f16_f32 v61, v49, v51\n" \
  "v_pk_mul_f32 v[48:49], v[50:51], %1 op_sel_hi:[0,1]\nds_write_b128 %3, v[64:67] offset:16\n" \
  "v_pk_fma_f32 v[48:49], v[50:51], %1, v[48:49] op_sel:[1,1,0] op_sel_hi:[1,0,1] neg_lo:[0,1,0]\nds_write_b128 %3, v[68:71] offset:80\n" \
  "v_pk_mul_f32 v[50:51], v[48:49], %1 op_sel_hi:[0,1]\nv_pk_mul_f32 v[54:55], v[52:53], %1 op_sel_hi:[0,1]\n" \
  "v_pk_fma_f32 v[50:51], v[48:49], %1, v[50:51] op_sel:[1,1,0] op_sel_hi:[1,0,1] neg_lo:[0,1,0]\n" \
  "v_pk_fma_f32 v[54:55], v[52:53], %1, v[54:55] op_sel:[1,1,0] op_sel_hi:[1,0,1] neg_lo:[0,1,0]\nv_cvt_pk_f16_f32 v58, v48, v50\n" \
  "v_cvt_pk_f16_f32 v62, v49, v51\nv_cvt_pk_f16_f32 v64, v52, v54\nv_cvt_pk_f16_f32 v68, v53, v55\n" \
  "v_pk_mul_f32 v[48:49], v[50:51], %1 op_sel_hi:[0,1]\nv_pk_mul_f32 v[52:53], v[54:55], %1 op_sel_hi:[0,1]\n" \
  "v_pk_fma_f32 v[48:49], v[50:51], %1, v[48:49] op_sel:[1,1,0] op_sel_hi:[1,0,1] neg_lo:[0,1,0]\n" \
  "v_pk_fma_f32 v[52:53], v[54:55], %1, v[52:53] op_sel:[1,1,0] op_sel_hi:[1,0,1] neg_lo:[0,1,0]\n" \
  "v_pk_mul_f32 v[50:51], v[48:49], %1 op_sel_hi:[0,1]\nv_pk_mul_f32 v[54:55], v[52:53], %1 op_sel_hi:[0,1]\n" \
  "v_pk_fma_f32 v[50:51], v[48:49], %1, v[50:51] op_sel:[1,1,0] op_sel_hi:[1,0,1] neg_lo:[0,1,0]\n" \
  "v_pk_fma_f32 v[54:55], v[52:53], %1, v[54:55] op_sel:[1,1,0] op_sel_hi:[1,0,1] neg_lo:[0,1,0]\nv_cvt_pk_f16_f32 v59, v48, v50\n" \
  "v_cvt_pk_f16_f32 v63, v49, v51\nv_cvt_pk_f16_f32 v65, v52, v54\nv_cvt_pk_f16_f32 v69, v53, v55\n" \
  "v_pk_mul_f32 v[52:53], v[54:55], %1 op_sel_hi:[0,1]\nds_write_b128 %3, v[56:59] offset:32\n" \
  "v_pk_fma_f32 v[52:53], v[54:55], %1, v[52:53] op_sel:[1,1,0] op_sel_hi:[1,0,1] neg_lo:[0,1,0]\nds_write_b128 %3, v[60:63] offset:96\n" \
  "v_pk_mul_f32 v[54:55], v[52:53], %1 op_sel_hi:[0,1]\n" \
  "v_pk_fma_f32 v[54:55], v[52:53], %1, v[54:55] op_sel:[1,1,0] op_sel_hi:[1,0,1] neg_lo:[0,1,0]\nv_cvt_pk_f16_f32 v66, v52, v54\n" \
  "v_cvt_pk_f16_f32 v70, v53, v55\nv_pk_mul_f32 v[52:53], v[54:55], %1 op_sel_hi:[0,1]\n" \
  "v_pk_fma_f32 v[52:53], v[54:55], %1, v[52:53] op_sel:[1,1,0] op_sel_hi:[1,0,1] neg_lo:[0,1,0]\n" \
  "v_pk_mul_f32 v[54:55], v[52:53], %1 op_sel_hi:[0,1]\n" \
  "v_pk_fma_f32 v[54:55], v[52:53], %1, v[54:55] op_sel:[1,1,0] op_sel_hi:[1,0,1] neg_lo:[0,1,0]\nv_cvt_pk_f16_f32 v67, v52, v54\n" \
  "v_cvt_pk_f16_f32 v71, v53, v55\nds_write_b128 %3, v[64:67] offset:48\nds_write_b128 %3, v[68:71] offset:112\n"
#define Y_CLOB "memory", "v40", "v41", "v42", "v43", "v44", "v45", "v46", "v47", "v48", "v49", "v50", "v51", "v52", "v53", "v54", "v55", "v56", "v57", "v58", "v59", "v60", "v61", "v62", "v63", "v64", "v65", "v66", "v67", "v68", "v69", "v70", "v71"
#define T_ASM \
  "ds_read_b128 %4, %8\nds_read_b64_tr_b16 v[80:81], %7 offset:0\nds_read_b64_tr_b16 v[82:83], %7 offset:64\n" \
  "ds_read_b64_tr_b16 v[84:85], %5 offset:0\nds_read_b64_tr_b16 v[86:87], %5 offset:48\nds_read_b64_tr_b16 v[88:89], %6 offset:0\n" \
  "ds_read_b64_tr_b16 v[90:91], %6 offset:48\nds_read_b64_tr_b16 v[92:93], %7 offset:3584\nds_read_b64_tr_b16 v[94:95], %7 offset:3648\n" \
  "ds_read_b64_tr_b16 v[96:97], %5 offset:3584\nds_read_b64_tr_b16 v[98:99], %5 offset:3632\nds_read_b64_tr_b16 v[100:101], %6 offset:3584\n" \
  "ds_read_b64_tr_b16 v[102:103], %6 offset:3632\nds_read_b64_tr_b16 v[104:105], %7 offset:7168\nds_read_b64_tr_b16 v[106:107], %7 offset:7232\n" \
  "s_waitcnt lgkmcnt(8)\nv_mfma_f32_16x16x32_f16 %0, v[84:87], v[80:83], 0\nds_read_b64_tr_b16 v[108:109], %5 offset:7168\n" \
  "ds_read_b64_tr_b16 v[110:111], %5 offset:7216\nds_read_b64_tr_b16 v[112:113], %6 offset:7168\nv_mfma_f32_16x16x32_f16 %2, v[88:91], v[80:83], 0\n" \
  "ds_read_b64_tr_b16 v[114:115], %6 offset:7216\nds_read_b64_tr_b16 v[116:117], %7 offset:10752\nds_read_b64_tr_b16 v[118:119], %7 offset:10816\n" \
  "s_waitcnt lgkmcnt(8)\nv_mfma_f32_16x16x32_f16 %0, v[96:99], v[92:95], %0\nds_read_b64_tr_b16 v[120:121], %5 offset:10752\n" \
  "ds_read_b64_tr_b16 v[122:123], %5 offset:10800\nds_read_b64_tr_b16 v[124:125], %6 offset:10752\n" \
  "v_mfma_f32_16x16x32_f16 %2, v[100:103], v[92:95], %2\nds_read_b64_tr_b16 v[126:127], %6 offset:10800\n" \
  "ds_read_b64_tr_b16 v[80:81], %7 offset:14336\nds_read_b64_tr_b16 v[82:83], %7 offset:14400\ns_waitcnt lgkmcnt(8)\n" \
  "v_mfma_f32_16x16x32_f16 %0, v[108:111], v[104:107], %0\nds_read_b64_tr_b16 v[84:85], %5 offset:14336\n" \
  "ds_read_b64_tr_b16 v[86:87], %5 offset:14384\nds_read_b64_tr_b16 v[88:89], %6 offset:14336\n" \
  "v_mfma_f32_16x16x32_f16 %2, v[112:115], v[104:107], %2\nds_read_b64_tr_b16 v[90:91], %6 offset:14384\n" \
  "ds_read_b64_tr_b16 v[92:93], %7 offset:17920\nds_read_b64_tr_b16 v[94:95], %7 offset:17984\ns_waitcnt lgkmcnt(8)\n" \
  "v_mfma_f32_16x16x32_f16 %0, v[120:123], v[116:119], %0\nds_read_b64_tr_b16 v[96:97], %5 offset:17920\n" \
  "ds_read_b64_tr_b16 v[98:99], %5 offset:17968\nds_read_b64_tr_b16 v[100:101], %6 offset:17920\n" \
  "v_mfma_f32_16x16x32_f16 %2, v[124:127], v[116:119], %2\nds_read_b64_tr_b16 v[102:103], %6 offset:17968\n" \
  "ds_read_b64_tr_b16 v[104:105], %7 offset:21504\nds_read_b64_tr_b16 v[106:107], %7 offset:21568\ns_waitcnt lgkmcnt(8)\n" \
  "v_mfma_f32_16x16x32_f16 %1, v[84:87], v[80:83], 0\nds_read_b64_tr_b16 v[108:109], %5 offset:21504\nds_read_b64_tr_b16 v[110:111], %5 offset:21552\n" \
  "ds_read_b64_tr_b16 v[112:113], %6 offset:21504\nv_mfma_f32_16x16x32_f16 %3, v[88:91], v[80:83], 0\nds_read_b64_tr_b16 v[114:115], %6 offset:21552\n" \
  "ds_read_b64_tr_b16 v[116:117], %7 offset:25088\nds_read_b64_tr_b16 v[118:119], %7 offset:25152\ns_waitcnt lgkmcnt(8)\n" \
  "v_mfma_f32_16x16x32_f16 %1, v[96:99], v[92:95], %1\nds_read_b64_tr_b16 v[120:121], %5 offset:25088\n" \
  "ds_read_b64_tr_b16 v[122:123], %5 offset:25136\nds_read_b64_tr_b16 v[124:125], %6 offset:25088\n" \
  "v_mfma_f32_16x16x32_f16 %3, v[100:103], v[92:95], %3\nds_read_b64_tr_b16 v[126:127], %6 offset:25136\ns_waitcnt lgkmcnt(6)\n" \
  "v_mfma_f32_16x16x32_f16 %1, v[108:111], v[104:107], %1\nv_mfma_f32_16x16x32_f16 %3, v[112:115], v[104:107], %3\ns_waitcnt lgkmcnt(0)\n" \
  "v_mfma_f32_16x16x32_f16 %1, v[120:123], v[116:119], %1\nv_mfma_f32_16x16x32_f16 %3, v[124:127], v[116:119], %3\n"
#define T_CLOB "memory", "v80", "v81", "v82", "v83", "v84", "v85", "v86", "v87", "v88", "v89", "v90", "v91", "v92", "v93", "v94", "v95", "v96", "v97", "v98", "v99", "v100", "v101", "v102", "v103", "v104", "v105", "v106", "v107", "v108", "v109", "v110", "v111", "v112", "v113", "v114", "v115", "v116", "v117", "v118", "v119", "v120", "v121", "v122", "v123", "v124", "v125", "v126", "v127"
template <class F> __device__ __forceinline__ int gen_y(float ldt, float are, float aim, float bre, float bim, float cre, float cim, uint32_t im, F&& mid TSP) {
  float ar2, air, g;
  const float dt = state_a(ldt, are, aim, ar2, air, g);
  const float dbr = dt * bre, dbi = dt * bim, pr = cre * dbr - cim * dbi, pi = cre * dbi + cim * dbr;
  const f32x2 w8 = cexp2(ar2, -air, 8.f), w64 = cexp2(ar2, -air, 64.f);
  const float pm = fmaxf(fabsf(pr), fabsf(pi));
  int e = -4;
  if (__builtin_expect(__any(!(pm < 0x1p-4f) || ar2 > 0.f) || !__any(pm > 0x1p-12f), 0)) e = (int)(wave_maxu(__float_as_uint(pm)) >> 23) - 126 + (int)ceilf(g * 250.f);
  const f32x2 y0 = {__builtin_amdgcn_ldexpf(pr, 14 - e), __builtin_amdgcn_ldexpf(-pi, 14 - e)};
  mid();
  TS(0, 7);
  asm volatile(Y_ASM : : "v"(y0), "v"(w8), "v"(w64), "v"(im) : Y_CLOB);
  return e;
}
__device__ __forceinline__ int gen_x(float ldt, float are, float aim, char* im) {
  float ar2, air, g;
  state_a(ldt, are, aim, ar2, air, g);
  const int e = min((int)ceilf(g * 1810.f), 300);
  const float s = __builtin_amdgcn_ldexpf(1.f, 15 - e);
  const f32x2 w1 = cexp2(ar2, air, 1.f), w256 = cexp2(ar2, air, 256.f), w2 = cmul(w1, w1);
  f32x2 a[8], b[8], c[8];
  a[0] = (f32x2){s, 0.f}; a[1] = w1 * s; a[2] = w2 * s;
  b[0] = b[1] = w256 * s;
  b[2] = cmul(b[0], w2); a[3] = cmul(a[1], w2); a[4] = cmul(a[2], w2);
  b[3] = b[4] = b[5] = cmul(b[2], w2); a[5] = cmul(a[3], w2); a[6] = cmul(a[4], w2);
  b[6] = cmul(b[3], w2); a[7] = cmul(a[5], w2); c[0] = cmul(b[3], w256);
  b[7] = cmul(b[6], w2); c[2] = cmul(c[0], w256);
  put_im<0>(im + I_X, 48, a);
  c[1] = cmul(b[7], w256); c[3] = cmul(c[1], w256);
  put_im<0>(im + I_X + 16, 48, b);
#pragma unroll
  for (int k = 4; k < 8; ++k) c[k] = cmul(c[k - 1], w256);
  put_im<0>(im + I_X + 32, 48, c);
  return e;
}

template <int I> struct IC { static constexpr int value = I; };
template <int B, int E, class F> __device__ __forceinline__ void static_for(F&& f) {
  if constexpr (B < E) { f(IC<B>{}); static_for<B + 1, E>(f); }
}
typedef __attribute__((ext_vector_type(4))) short s16x4;
__device__ __forceinline__ u32x2 trd(uint32_t a) {
  return __builtin_bit_cast(u32x2, __builtin_amdgcn_ds_read_tr16_b64_v4i16((__attribute__((address_space(3))) s16x4*)(uintptr_t)a));
}
#define BAR() asm volatile("s_waitcnt lgkmcnt(0)\n\ts_barrier" ::: "memory")
#define ST16(off, v) asm volatile("global_store_dwordx4 %0, %1, %2 sc1\n\ts_nop 1" : : "v"(off), "v"(v), "s"(ws) : "memory")
#define ST4(off, v) asm volatile("global_store_dword %0, %1, %2 sc1" : : "v"(off), "v"(v), "s"(ws) : "memory")
#define LD16(r, off) asm volatile("global_load_dwordx4 %0, %1, %2 sc1" : "=v"(r) : "v"(off), "s"(ws) : "memory")
#define LD16W(r, off) asm volatile("global_load_dwordx4 %0, %1, %2 sc1\n\ts_waitcnt vmcnt(0)" : "=&v"(r) : "v"(off), "s"(ws) : "memory")
#define DRAIN() asm volatile("s_waitcnt vmcnt(0)" ::: "memory")
#define CEX(a, b) { const uint32_t hi_ = max(a, b), lo_ = min(a, b); a = hi_; b = lo_; }
#define MMB(a_, b_, c_) __builtin_amdgcn_mfma_f32_16x16x32_bf16(__builtin_bit_cast(bf16x8, a_), __builtin_bit_cast(bf16x8, b_), c_, 0, 0, 0)
#define NTM 0
#define DPOL ""
#define DPOL2 ""
#define DPOL4 ""
constexpr int SPIN_CAP = 1 << 18;
constexpr uint32_t UMASK = 0x40074007u;
__device__ __forceinline__ uint32_t utag(uint32_t T, int j) {
  return ((T >> (3 * j)) & 0x00070007u) | (((j <= 2) ? (T << (2 - j)) : (T >> (j - 2))) & 0x40004000u);
}
__device__ __forceinline__ uint32_t max4(const u32x4& a) { return max(max(a[0], a[1]), max(a[2], a[3])); }
#ifndef ULONG
#define ULONG 8
#endif
__device__ __forceinline__ void until(uint32_t t) {
#pragma unroll 1
  for (int spin = 0; spin < 600; ++spin) {
    const int d = (int)(t - (uint32_t)__builtin_amdgcn_s_memrealtime());
    if (d <= 0) break;
    if (ULONG && d > 4 * ULONG) __builtin_amdgcn_s_sleep(ULONG); else __builtin_amdgcn_s_sleep(1);
  }
}
__device__ __forceinline__ uint32_t latest(uint32_t t) {
  const uint32_t t0 = (uint32_t)__builtin_amdgcn_readfirstlane(t) - 0x40000000u;
  return wave_maxu(t - t0) + t0;
}
__device__ __forceinline__ uint32_t gate(const char* ws, uint32_t po, uint32_t T) {
  u32x2 pv;
#pragma unroll 1
  for (int spin = 0; spin < SPIN_CAP; ++spin) {
    asm volatile("global_load_dwordx2 %0, %1, %2 sc1\n\ts_waitcnt vmcnt(0)" : "=&v"(pv) : "v"(po), "s"(ws) : "memory");
    if (!__any(pv[0] != T)) break;
    __builtin_amdgcn_s_sleep(2);
  }
  return pv[1];
}
__device__ __forceinline__ uint32_t gate2(const char* ws, uint32_t oa, uint32_t da, uint32_t ob, uint32_t T) {
  u32x2 pa, pb;
#pragma unroll 1
  for (int spin = 0; spin < SPIN_CAP; ++spin) {
    asm volatile("global_load_dwordx2 %0, %2, %4 sc1\n\tglobal_load_dwordx2 %1, %3, %4 sc1\n\ts_waitcnt vmcnt(0)" : "=&v"(pa), "=&v"(pb) : "v"(oa), "v"(ob), "s"(ws) : "memory");
    if (!__any(pa[0] != T || pb[0] != T)) break;
    __builtin_amdgcn_s_sleep(4);
  }
  pa[1] += da;
  return latest(((int)(pa[1] - pb[1]) > 0) ? pa[1] : pb[1]);
}
__device__ __forceinline__ uint32_t ahead(uint32_t c0, uint32_t t0, int dt) {
  const uint32_t c = (uint32_t)__builtin_amdgcn_s_memtime(), t = (uint32_t)__builtin_amdgcn_s_memrealtime();
  return t + (uint32_t)((float)((t - t0) * (uint32_t)(20 * dt)) * __builtin_amdgcn_rcpf((float)(c - c0)));
}
__device__ __forceinline__ void gsync(uint32_t a, uint32_t target) {
  uint32_t t, c, n = SPIN_CAP;
  uint64_t ex;
  asm volatile("s_waitcnt lgkmcnt(0)\n\ts_mov_b64 %3, exec\n\ts_mov_b64 exec, 1\n\tv_mov_b32 %0, 1\n\tds_add_u32 %4, %0\n\ts_mov_b64 exec, %3\n"
               "1:\n\ts_sleep 1\n\tds_read_b32 %0, %4\n\ts_sub_u32 %2, %2, 1\n\ts_cbranch_scc1 2f\n\ts_waitcnt lgkmcnt(0)\n\tv_readfirstlane_b32 %1, %0\n\t"
               "s_cmp_lt_u32 %1, %5\n\ts_cbranch_scc1 1b\n2:\n\ts_waitcnt lgkmcnt(0)"
               : "=&v"(t), "=&s"(c), "+s"(n), "=&s"(ex) : "v"(a), "n"(target) : "memory", "scc");
}
__device__ __forceinline__ void gsync0(uint32_t a, uint32_t target) {
  uint32_t t, c, n = SPIN_CAP;
  asm volatile("s_waitcnt lgkmcnt(0)\n\ts_mov_b64 exec, 1\n\tv_mov_b32 %0, 1\n\tds_add_u32 %3, %0\n\ts_mov_b64 exec, -1\n"
               "1:\n\tds_read_b32 %0, %3\n\ts_sub_u32 %2, %2, 1\n\ts_cbranch_scc1 2f\n\ts_waitcnt lgkmcnt(0)\n\tv_readfirstlane_b32 %1, %0\n\t"
               "s_cmp_lt_u32 %1, %4\n\ts_cbranch_scc1 1b\n2:\n\ts_waitcnt lgkmcnt(0)"
               : "=&v"(t), "=&s"(c), "+s"(n) : "v"(a), "n"(target) : "memory", "scc");
}
__device__ __forceinline__ void gwait(uint32_t a, uint32_t target) {
  uint32_t t, c, n = SPIN_CAP;
  asm volatile("1:\n\ts_sleep 2\n\tds_read_b32 %0, %3\n\ts_sub_u32 %2, %2, 1\n\ts_cbranch_scc1 2f\n\ts_waitcnt lgkmcnt(0)\n\tv_readfirstlane_b32 %1, %0\n\t"
               "s_cmp_lt_u32 %1, %4\n\ts_cbranch_scc1 1b\n2:\n\ts_waitcnt lgkmcnt(0)"
               : "=&v"(t), "=&s"(c), "+s"(n) : "v"(a), "n"(target) : "memory", "scc");
}
__device__ __forceinline__ void gwait0(uint32_t a, uint32_t target) {
  uint32_t t, c, n = SPIN_CAP;
  asm volatile("1:\n\tds_read_b32 %0, %3\n\ts_sub_u32 %2, %2, 1\n\ts_cbranch_scc1 2f\n\ts_waitcnt lgkmcnt(0)\n\tv_readfirstlane_b32 %1, %0\n\t"
               "s_cmp_lt_u32 %1, %4\n\ts_cbranch_scc1 1b\n2:\n\ts_waitcnt lgkmcnt(0)"
               : "=&v"(t), "=&s"(c), "+s"(n) : "v"(a), "n"(target) : "memory", "scc");
}
__device__ __forceinline__ void dma16(const void* base, uint32_t voff, uint32_t lds) {
  uint32_t keep;
  asm volatile("s_mov_b32 %0, m0\n\ts_mov_b32 m0, %3\n\ts_nop 0\n\tglobal_load_lds_dwordx4 %1, %2" DPOL "\n\ts_mov_b32 m0, %0" : "=&s"(keep) : "v"(voff), "s"(base), "s"(lds) : "memory");
}
__device__ __forceinline__ void dma16d(const void* base, uint32_t voff, uint32_t lds) {
  uint32_t keep;
  asm volatile("s_mov_b32 %0, m0\n\ts_mov_b32 m0, %3\n\ts_nop 0\n\tglobal_load_lds_dwordx4 %1, %2" DPOL2 "\n\ts_mov_b32 m0, %0" : "=&s"(keep) : "v"(voff), "s"(base), "s"(lds) : "memory");
}
__device__ __forceinline__ void nap(int n) {
  for (; n > 15; n -= 15) __builtin_amdgcn_s_sleep(15);
  switch (n) {
    case 1: __builtin_amdgcn_s_sleep(1); break; case 2: __builtin_amdgcn_s_sleep(2); break; case 3: __builtin_amdgcn_s_sleep(3); break;
    case 4: __builtin_amdgcn_s_sleep(4); break; case 5: __builtin_amdgcn_s_sleep(5); break; case 6: __builtin_amdgcn_s_sleep(6); break;
    case 7: __builtin_amdgcn_s_sleep(7); break; case 8: __builtin_amdgcn_s_sleep(8); break; case 9: __builtin_amdgcn_s_sleep(9); break;
    case 10: __builtin_amdgcn_s_sleep(10); break; case 11: __builtin_amdgcn_s_sleep(11); break; case 12: __builtin_amdgcn_s_sleep(12); break;
    case 13: __builtin_amdgcn_s_sleep(13); break; case 14: __builtin_amdgcn_s_sleep(14); break; case 15: __builtin_amdgcn_s_sleep(15); break;
    default: break;
  }
}
#define SLEEP(n) do { if ((n) > 0) __builtin_amdgcn_s_sleep(n); } while (0)
#define USLEEP 3
#define SSLEEP 11
#define PSLEEP 2
#define PPRIO 0
#define LFIRST 1
#define WD 32
#define OSLEEP 12
#define QPRIO 1
#define KPRIO 2
#define GSLEEP 0
#define NSLEEP 0
#define UD 40
#define XD 0
#define RSLEEP 0
#define UPRIO 2
#define VD2 380
#define WDEPTH 6
#define ESLEEP 0
#define SD 58
#define KD 182
#define OD 0
#define KBACK 6
#define DDEPTH 12
#define C3 1
#define XROT 2
#define WSOFF 0
#define KN 1
#define RPF 1
#define LSLEEP 1
#define TSTOP 78
#define HSTOP 170
#define VEARLY 0ull
#define FPR 3
#define SPR 3
#define DPRIO 0
#define QPRIO2 3
#define HW 2
#define RD 57
#define OPOS 1
#define OD2 6
#define NA 12
#define RD2 110
#define ORD1 ((unsigned __int128)0x321087654u << 36 | 0x321087654ull)
#define WDEPTH2 5
#define RQ NA
#define HREV 1
#define QPRIO0 3
#define VPS 1
#define RW 0
#define NPRIO 0
#define XSL 10
#define V2D 24
#define N0 8
#define LPOS 1
#define ALLP 1
#define OTP 1
#define STP 0
#define FD 204
#define VDB 365
#define BALL 1
#define HALL 0
#define FORCESLOW 0
#define FORCEFB 0
#define CONV_ASM \
  "s_cmp_eq_u32 s89, 1\ns_cbranch_scc1 1f\ns_cmp_gt_u32 s89, 1\ns_cbranch_scc1 9f\nds_read_b64 v[124:125], v3 offset:576\n" \
  "ds_read_b64 v[126:127], v3 offset:584\nds_read_b64 v[120:121], v3 offset:64\nds_read_b64 v[122:123], v3 offset:72\n" \
  "ds_read_b64 v[116:117], v3 offset:640\nds_read_b64 v[118:119], v3 offset:648\nds_read_b64 v[112:113], v3 offset:128\n" \
  "ds_read_b64 v[114:115], v3 offset:136\nds_read_b64 v[108:109], v3 offset:704\nds_read_b64 v[110:111], v3 offset:712\n" \
  "ds_read_b64 v[104:105], v3 offset:192\nds_read_b64 v[106:107], v3 offset:200\nds_read_b64 v[100:101], v3 offset:768\n" \
  "ds_read_b64 v[102:103], v3 offset:776\nds_read_b64 v[96:97], v3 offset:256\nds_read_b64 v[98:99], v3 offset:264\n" \
  "ds_read_b64 v[92:93], v3 offset:832\nds_read_b64 v[94:95], v3 offset:840\nds_read_b64 v[88:89], v3 offset:320\n" \
  "ds_read_b64 v[90:91], v3 offset:328\nds_read_b64 v[84:85], v3 offset:896\nds_read_b64 v[86:87], v3 offset:904\n" \
  "ds_read_b64 v[80:81], v3 offset:384\nds_read_b64 v[82:83], v3 offset:392\nds_read_b64 v[76:77], v3 offset:960\n" \
  "ds_read_b64 v[78:79], v3 offset:968\nds_read_b64 v[72:73], v3 offset:448\nds_read_b64 v[74:75], v3 offset:456\n" \
  "ds_read_b64 v[68:69], v3 offset:1024\nds_read_b64 v[70:71], v3 offset:1032\nds_read_b64 v[64:65], v3 offset:512\n" \
  "ds_read_b64 v[66:67], v3 offset:520\nds_read_b64 v[60:61], v3 offset:0\nds_read_b64 v[62:63], v3 offset:8\nds_read_b64 v[56:57], v3 offset:1088\n" \
  "ds_read_b64 v[58:59], v3 offset:1096\ns_waitcnt lgkmcnt(0)\ns_barrier\nds_read_b128 v[36:39], v2 offset:0\nds_read_b128 v[40:43], v2 offset:64\n" \
  "ds_read_b128 v[44:47], v2 offset:128\nds_read_b128 v[48:51], v2 offset:192\ns_waitcnt lgkmcnt(3)\n" \
  "v_mfma_f32_16x16x32_f16 v[12:15], v[124:127], v[36:39], 0\ns_setprio 1\nds_read_b128 v[52:55], v2 offset:256\n" \
  "v_mfma_f32_16x16x32_f16 v[16:19], v[120:123], v[36:39], 0\ns_waitcnt lgkmcnt(3)\nv_mfma_f32_16x16x32_f16 v[12:15], v[116:119], v[40:43], v[12:15]\n" \
  "ds_read_b128 v[36:39], v2 offset:320\nv_mfma_f32_16x16x32_f16 v[16:19], v[112:115], v[40:43], v[16:19]\ns_waitcnt lgkmcnt(3)\n" \
  "v_mfma_f32_16x16x32_f16 v[12:15], v[108:111], v[44:47], v[12:15]\nds_read_b128 v[40:43], v2 offset:384\n" \
  "v_mfma_f32_16x16x32_f16 v[16:19], v[104:107], v[44:47], v[16:19]\ns_waitcnt lgkmcnt(3)\n" \
  "v_mfma_f32_16x16x32_f16 v[12:15], v[100:103], v[48:51], v[12:15]\nds_read_b128 v[44:47], v2 offset:448\n" \
  "v_mfma_f32_16x16x32_f16 v[16:19], v[96:99], v[48:51], v[16:19]\ns_waitcnt lgkmcnt(3)\n" \
  "v_mfma_f32_16x16x32_f16 v[12:15], v[92:95], v[52:55], v[12:15]\nv_mfma_f32_16x16x32_f16 v[16:19], v[88:91], v[52:55], v[16:19]\n" \
  "s_waitcnt lgkmcnt(2)\nv_mfma_f32_16x16x32_f16 v[12:15], v[84:87], v[36:39], v[12:15]\nds_read_b128 v[48:51], v2 offset:512\n" \
  "v_mfma_f32_16x16x32_f16 v[16:19], v[80:83], v[36:39], v[16:19]\ns_waitcnt lgkmcnt(2)\n" \
  "v_mfma_f32_16x16x32_f16 v[12:15], v[76:79], v[40:43], v[12:15]\nds_read_b128 v[52:55], v2 offset:576\n" \
  "v_mfma_f32_16x16x32_f16 v[16:19], v[72:75], v[40:43], v[16:19]\ns_waitcnt lgkmcnt(2)\n" \
  "v_mfma_f32_16x16x32_f16 v[12:15], v[68:71], v[44:47], v[12:15]\nds_read_b128 v[36:39], v2 offset:640\n" \
  "v_mfma_f32_16x16x32_f16 v[16:19], v[64:67], v[44:47], v[16:19]\nv_mfma_f32_16x16x32_f16 v[20:23], v[60:63], v[44:47], 0\n" \
  "ds_read_b128 v[40:43], v2 offset:704\nv_mfma_f32_16x16x32_f16 v[28:31], v[56:59], v[44:47], 0\ns_waitcnt lgkmcnt(3)\n" \
  "v_mfma_f32_16x16x32_f16 v[16:19], v[124:127], v[48:51], v[16:19]\nds_read_b128 v[56:59], v2 offset:768\nv_cvt_pkrtz_f16_f32 v32, v12, v13\n" \
  "v_mfma_f32_16x16x32_f16 v[20:23], v[120:123], v[48:51], v[20:23]\nv_cvt_pkrtz_f16_f32 v33, v14, v15\ns_waitcnt lgkmcnt(3)\n" \
  "v_mfma_f32_16x16x32_f16 v[16:19], v[116:119], v[52:55], v[16:19]\nds_read_b128 v[44:47], v2 offset:832\n" \
  "v_pk_add_u16 v32, v32, 4 op_sel_hi:[1,0] clamp\nv_mfma_f32_16x16x32_f16 v[20:23], v[112:115], v[52:55], v[20:23]\n" \
  "v_pk_add_u16 v33, v33, 4 op_sel_hi:[1,0] clamp\ns_waitcnt lgkmcnt(3)\nv_mfma_f32_16x16x32_f16 v[16:19], v[108:111], v[36:39], v[16:19]\n" \
  "ds_read_b128 v[48:51], v2 offset:896\nds_write_b64 v4, v[32:33] offset:0\nv_mfma_f32_16x16x32_f16 v[20:23], v[104:107], v[36:39], v[20:23]\n" \
  "s_waitcnt lgkmcnt(3)\nv_mfma_f32_16x16x32_f16 v[16:19], v[100:103], v[40:43], v[16:19]\nds_read_b128 v[52:55], v2 offset:960\n" \
  "v_mfma_f32_16x16x32_f16 v[20:23], v[96:99], v[40:43], v[20:23]\nv_mfma_f32_16x16x32_f16 v[16:19], v[92:95], v[56:59], v[16:19]\n" \
  "v_mfma_f32_16x16x32_f16 v[20:23], v[88:91], v[56:59], v[20:23]\nds_read_b128 v[36:39], v2 offset:1024\ns_waitcnt lgkmcnt(3)\n" \
  "v_mfma_f32_16x16x32_f16 v[16:19], v[84:87], v[44:47], v[16:19]\nv_mfma_f32_16x16x32_f16 v[20:23], v[80:83], v[44:47], v[20:23]\n" \
  "ds_read_b128 v[40:43], v2 offset:1088\nds_read_b64 v[56:57], v6 offset:256\nds_read_b64 v[58:59], v6 offset:264\n" \
  "v_mfma_f32_16x16x32_f16 v[16:19], v[76:79], v[48:51], v[16:19]\nv_mfma_f32_16x16x32_f16 v[20:23], v[72:75], v[48:51], v[20:23]\n" \
  "ds_read_b128 v[44:47], v2 offset:1152\ns_waitcnt lgkmcnt(4)\nv_mfma_f32_16x16x32_f16 v[16:19], v[68:71], v[52:55], v[16:19]\n" \
  "v_mfma_f32_16x16x32_f16 v[20:23], v[64:67], v[52:55], v[20:23]\nds_read_b128 v[48:51], v2 offset:1216\n" \
  "v_mfma_f32_16x16x32_f16 v[24:27], v[60:63], v[52:55], 0\nv_mfma_f32_16x16x32_f16 v[20:23], v[124:127], v[36:39], v[20:23]\n" \
  "ds_read_b128 v[52:55], v2 offset:1280\nv_mfma_f32_16x16x32_f16 v[24:27], v[120:123], v[36:39], v[24:27]\ns_waitcnt lgkmcnt(5)\n" \
  "v_mfma_f32_16x16x32_f16 v[20:23], v[116:119], v[40:43], v[20:23]\nds_read_b128 v[36:39], v2 offset:1344\nv_cvt_pkrtz_f16_f32 v32, v16, v17\n" \
  "v_mfma_f32_16x16x32_f16 v[24:27], v[112:115], v[40:43], v[24:27]\nv_cvt_pkrtz_f16_f32 v33, v18, v19\ns_waitcnt lgkmcnt(3)\n" \
  "v_mfma_f32_16x16x32_f16 v[20:23], v[108:111], v[44:47], v[20:23]\nds_read_b128 v[40:43], v2 offset:1408\n" \
  "v_pk_add_u16 v32, v32, 4 op_sel_hi:[1,0] clamp\nds_read_b32 v35, v9 offset:32\nv_mfma_f32_16x16x32_f16 v[24:27], v[104:107], v[44:47], v[24:27]\n" \
  "v_pk_add_u16 v33, v33, 4 op_sel_hi:[1,0] clamp\ns_waitcnt lgkmcnt(3)\nv_mfma_f32_16x16x32_f16 v[20:23], v[100:103], v[48:51], v[20:23]\n" \
  "ds_read_b128 v[44:47], v2 offset:1472\nds_write_b64 v4, v[32:33] offset:1024\nv_mfma_f32_16x16x32_f16 v[24:27], v[96:99], v[48:51], v[24:27]\n" \
  "v_mfma_f32_16x16x32_f16 v[20:23], v[92:95], v[52:55], v[20:23]\nv_mfma_f32_16x16x32_f16 v[24:27], v[88:91], v[52:55], v[24:27]\n" \
  "ds_read_b128 v[48:51], v2 offset:1536\ns_waitcnt lgkmcnt(3)\nv_readfirstlane_b32 s85, v35\ns_cmp_lt_u32 s85, 4\ns_cbranch_scc1 21f\n22:\n" \
  "v_mfma_f32_16x16x32_f16 v[20:23], v[84:87], v[36:39], v[20:23]\nv_mfma_f32_16x16x32_f16 v[24:27], v[80:83], v[36:39], v[24:27]\n" \
  "ds_read_b128 v[52:55], v2 offset:1600\nv_mfma_f32_16x16x32_f16 v[20:23], v[76:79], v[40:43], v[20:23]\n" \
  "v_mfma_f32_16x16x32_f16 v[24:27], v[72:75], v[40:43], v[24:27]\nds_read_b128 v[36:39], v2 offset:1664\ns_waitcnt lgkmcnt(4)\n" \
  "v_mfma_f32_16x16x32_f16 v[20:23], v[68:71], v[44:47], v[20:23]\nv_mfma_f32_16x16x32_f16 v[24:27], v[64:67], v[44:47], v[24:27]\n" \
  "ds_read_b128 v[40:43], v2 offset:1728\nv_mfma_f32_16x16x32_f16 v[28:31], v[60:63], v[44:47], v[28:31]\nds_read_b64_tr_b16 v[60:61], v5 offset:0\n" \
  "ds_read_b64_tr_b16 v[62:63], v5 offset:32\ns_waitcnt lgkmcnt(4)\nv_mfma_f32_16x16x32_f16 v[24:27], v[124:127], v[48:51], v[24:27]\n" \
  "v_mfma_f32_16x16x32_f16 v[28:31], v[120:123], v[48:51], v[28:31]\nds_read_b64_tr_b16 v[44:45], v5 offset:256\n" \
  "ds_read_b64_tr_b16 v[46:47], v5 offset:288\nv_mfma_f32_16x16x32_f16 v[24:27], v[116:119], v[52:55], v[24:27]\n" \
  "ds_read_b64 v[120:121], v6 offset:320\nds_read_b64 v[122:123], v6 offset:328\nv_mfma_f32_16x16x32_f16 v[28:31], v[112:115], v[52:55], v[28:31]\n" \
  "ds_read_b128 v[48:51], v2 offset:1792\ns_waitcnt lgkmcnt(7)\nv_mfma_f32_16x16x32_f16 v[24:27], v[108:111], v[36:39], v[24:27]\n" \
  "ds_read_b128 v[112:115], v2 offset:1856\nv_mfma_f32_16x16x32_f16 v[28:31], v[104:107], v[36:39], v[28:31]\nds_read_b64 v[52:53], v6 offset:128\n" \
  "ds_read_b64 v[54:55], v6 offset:136\nv_mfma_f32_16x16x32_f16 v[24:27], v[100:103], v[40:43], v[24:27]\nds_read_b128 v[104:107], v2 offset:1920\n" \
  "v_mfma_f32_16x16x32_f16 v[28:31], v[96:99], v[40:43], v[28:31]\nds_read_b64 v[36:37], v6 offset:192\nds_read_b64 v[38:39], v6 offset:200\n" \
  "s_waitcnt lgkmcnt(9)\nv_mfma_f32_16x16x32_f16 v[20:23], v[60:63], v[56:59], v[20:23]\nds_read_b128 v[96:99], v2 offset:1984\ns_waitcnt lgkmcnt(7)\n" \
  "v_mfma_f32_16x16x32_f16 v[20:23], v[44:47], v[120:123], v[20:23]\nv_mfma_f32_16x16x32_f16 v[24:27], v[92:95], v[48:51], v[24:27]\n" \
  "ds_read_b128 v[40:43], v2 offset:2048\nv_mfma_f32_16x16x32_f16 v[28:31], v[88:91], v[48:51], v[28:31]\nds_read_b64_tr_b16 v[88:89], v5 offset:512\n" \
  "ds_read_b64_tr_b16 v[90:91], v5 offset:544\ns_waitcnt lgkmcnt(6)\nv_mfma_f32_16x16x32_f16 v[24:27], v[84:87], v[112:115], v[24:27]\n" \
  "v_mfma_f32_16x16x32_f16 v[28:31], v[80:83], v[112:115], v[28:31]\nv_cvt_pkrtz_f16_f32 v32, v20, v21\nv_cvt_pkrtz_f16_f32 v33, v22, v23\n" \
  "v_mfma_f32_16x16x32_f16 v[24:27], v[76:79], v[104:107], v[24:27]\nds_read_b64_tr_b16 v[48:49], v5 offset:768\n" \
  "ds_read_b64_tr_b16 v[50:51], v5 offset:800\nv_mfma_f32_16x16x32_f16 v[28:31], v[72:75], v[104:107], v[28:31]\n" \
  "ds_read_b128 v[80:83], v2 offset:2112\ns_waitcnt lgkmcnt(5)\nv_mfma_f32_16x16x32_f16 v[24:27], v[68:71], v[96:99], v[24:27]\n" \
  "ds_read_b128 v[112:115], v2 offset:2176\nv_pk_add_u16 v32, v32, 4 op_sel_hi:[1,0] clamp\n" \
  "v_mfma_f32_16x16x32_f16 v[28:31], v[64:67], v[96:99], v[28:31]\nds_read_b128 v[72:75], v2 offset:2240\n" \
  "v_pk_add_u16 v33, v33, 4 op_sel_hi:[1,0] clamp\nv_mfma_f32_16x16x32_f16 v[28:31], v[124:127], v[40:43], v[28:31]\n" \
  "ds_read_b128 v[104:107], v2 offset:2304\nds_write_b64 v4, v[32:33] offset:2048\nv_mfma_f32_16x16x32_f16 v[24:27], v[60:63], v[52:55], v[24:27]\n" \
  "ds_read_b128 v[64:67], v2 offset:2368\nds_read_b64 v[96:97], v6 offset:0\nds_read_b64 v[98:99], v6 offset:8\n" \
  "v_mfma_f32_16x16x32_f16 v[24:27], v[44:47], v[36:39], v[24:27]\nds_read_b128 v[124:127], v2 offset:2432\ns_waitcnt lgkmcnt(9)\n" \
  "v_mfma_f32_16x16x32_f16 v[24:27], v[88:91], v[56:59], v[24:27]\nds_read_b128 v[40:43], v2 offset:2496\n" \
  "v_mfma_f32_16x16x32_f16 v[24:27], v[48:51], v[120:123], v[24:27]\ns_waitcnt lgkmcnt(8)\n" \
  "v_mfma_f32_16x16x32_f16 v[28:31], v[116:119], v[80:83], v[28:31]\nds_read_b64 v[116:117], v6 offset:64\nds_read_b64 v[118:119], v6 offset:72\n" \
  "v_mfma_f32_16x16x32_f16 v[28:31], v[108:111], v[112:115], v[28:31]\nds_read_b64_tr_b16 v[80:81], v5 offset:1024\n" \
  "ds_read_b64_tr_b16 v[82:83], v5 offset:1056\ns_waitcnt lgkmcnt(10)\nv_mfma_f32_16x16x32_f16 v[28:31], v[100:103], v[72:75], v[28:31]\n" \
  "v_cvt_pkrtz_f16_f32 v32, v24, v25\nv_cvt_pkrtz_f16_f32 v33, v26, v27\nv_mfma_f32_16x16x32_f16 v[28:31], v[92:95], v[104:107], v[28:31]\n" \
  "v_pk_add_u16 v32, v32, 4 op_sel_hi:[1,0] clamp\ns_waitcnt lgkmcnt(5)\nv_mfma_f32_16x16x32_f16 v[28:31], v[84:87], v[64:67], v[28:31]\n" \
  "ds_read_b64_tr_b16 v[108:109], v5 offset:1280\nds_read_b64_tr_b16 v[110:111], v5 offset:1312\n" \
  "v_mfma_f32_16x16x32_f16 v[28:31], v[76:79], v[124:127], v[28:31]\nds_read_b64_tr_b16 v[112:113], v7 offset:0\n" \
  "ds_read_b64_tr_b16 v[114:115], v7 offset:64\ns_waitcnt lgkmcnt(6)\nv_mfma_f32_16x16x32_f16 v[28:31], v[68:71], v[40:43], v[28:31]\n" \
  "ds_read2_b32 v[100:101], v8 offset0:48 offset1:49\nds_read2_b32 v[102:103], v8 offset0:50 offset1:51\n" \
  "v_mfma_f32_16x16x32_f16 v[28:31], v[60:63], v[96:99], v[28:31]\nv_pk_add_u16 v33, v33, 4 op_sel_hi:[1,0] clamp\n" \
  "ds_write_b64 v4, v[32:33] offset:3072\nv_mfma_f32_16x16x32_f16 v[28:31], v[44:47], v[116:119], v[28:31]\n" \
  "v_mfma_f32_16x16x32_f16 v[28:31], v[88:91], v[52:55], v[28:31]\nv_mfma_f32_16x16x32_f16 v[28:31], v[48:51], v[36:39], v[28:31]\n" \
  "s_waitcnt lgkmcnt(5)\nv_mfma_f32_16x16x32_f16 v[28:31], v[80:83], v[56:59], v[28:31]\n" \
  "v_mfma_f32_16x16x32_f16 v[28:31], v[108:111], v[120:123], v[28:31]\ns_waitcnt lgkmcnt(1)\n" \
  "v_mfma_f32_16x16x32_f16 v[28:31], v[112:115], v[100:103], v[28:31]\ns_nop 7\nv_cvt_pkrtz_f16_f32 v32, v28, v29\nv_cvt_pkrtz_f16_f32 v33, v30, v31\n" \
  "v_pk_add_u16 v32, v32, 4 op_sel_hi:[1,0] clamp\nv_pk_add_u16 v33, v33, 4 op_sel_hi:[1,0] clamp\nds_write_b64 v4, v[32:33] offset:4096\n" \
  "s_setprio 0\ns_setpc_b64 s[90:91]\n21:\ns_movk_i32 s84, 0x4000\n23:\nds_read_b32 v35, v9 offset:32\ns_sub_u32 s84, s84, 1\ns_cbranch_scc1 22b\n" \
  "s_waitcnt lgkmcnt(0)\nv_readfirstlane_b32 s85, v35\ns_cmp_lt_u32 s85, 4\ns_cbranch_scc1 23b\ns_branch 22b\n1:\n" \
  "ds_read_b64 v[124:125], v3 offset:1088\nds_read_b64 v[126:127], v3 offset:1096\nds_read_b64 v[120:121], v3 offset:0\n" \
  "ds_read_b64 v[122:123], v3 offset:8\nds_read_b64 v[116:117], v3 offset:64\nds_read_b64 v[118:119], v3 offset:72\n" \
  "ds_read_b64 v[112:113], v3 offset:128\nds_read_b64 v[114:115], v3 offset:136\nds_read_b64 v[108:109], v3 offset:192\n" \
  "ds_read_b64 v[110:111], v3 offset:200\nds_read_b64 v[104:105], v3 offset:256\nds_read_b64 v[106:107], v3 offset:264\n" \
  "ds_read_b64 v[100:101], v3 offset:320\nds_read_b64 v[102:103], v3 offset:328\nds_read_b64 v[96:97], v3 offset:384\n" \
  "ds_read_b64 v[98:99], v3 offset:392\nds_read_b64 v[92:93], v3 offset:448\nds_read_b64 v[94:95], v3 offset:456\n" \
  "ds_read_b64 v[88:89], v3 offset:512\nds_read_b64 v[90:91], v3 offset:520\nds_read_b64 v[84:85], v3 offset:576\n" \
  "ds_read_b64 v[86:87], v3 offset:584\nds_read_b64 v[80:81], v3 offset:640\nds_read_b64 v[82:83], v3 offset:648\n" \
  "ds_read_b64 v[76:77], v3 offset:704\nds_read_b64 v[78:79], v3 offset:712\nds_read_b64 v[72:73], v3 offset:768\n" \
  "ds_read_b64 v[74:75], v3 offset:776\nds_read_b64 v[68:69], v3 offset:832\nds_read_b64 v[70:71], v3 offset:840\n" \
  "ds_read_b64 v[64:65], v3 offset:896\nds_read_b64 v[66:67], v3 offset:904\nds_read_b64 v[60:61], v3 offset:960\n" \
  "ds_read_b64 v[62:63], v3 offset:968\nds_read_b64 v[56:57], v3 offset:1024\nds_read_b64 v[58:59], v3 offset:1032\n" \
  "ds_read_b64 v[52:53], v6 offset:0\nds_read_b64 v[54:55], v6 offset:8\nds_read_b64 v[48:49], v6 offset:64\nds_read_b64 v[50:51], v6 offset:72\n" \
  "s_waitcnt lgkmcnt(0)\ns_barrier\nds_read_b128 v[28:31], v2 offset:960\nds_read_b128 v[32:35], v2 offset:1472\n" \
  "ds_read_b128 v[36:39], v2 offset:1984\nds_read_b128 v[40:43], v2 offset:2048\nds_read_b128 v[44:47], v2 offset:2112\ns_waitcnt lgkmcnt(3)\n" \
  "v_mfma_f32_16x16x32_f16 v[12:15], v[124:127], v[28:31], 0\ns_setprio 1\nds_read_b128 v[28:31], v2 offset:2176\n" \
  "v_mfma_f32_16x16x32_f16 v[16:19], v[124:127], v[32:35], 0\nds_read_b128 v[32:35], v2 offset:2240\ns_waitcnt lgkmcnt(3)\n" \
  "v_mfma_f32_16x16x32_f16 v[12:15], v[120:123], v[36:39], v[12:15]\nv_mfma_f32_16x16x32_f16 v[20:23], v[124:127], v[36:39], 0\n" \
  "ds_read_b128 v[124:127], v2 offset:2304\nds_read_b128 v[36:39], v2 offset:2368\nv_mfma_f32_16x16x32_f16 v[12:15], v[116:119], v[40:43], v[12:15]\n" \
  "ds_read_b128 v[40:43], v2 offset:2432\ns_waitcnt lgkmcnt(4)\nv_mfma_f32_16x16x32_f16 v[12:15], v[112:115], v[44:47], v[12:15]\n" \
  "ds_read_b128 v[44:47], v2 offset:2496\nv_mfma_f32_16x16x32_f16 v[12:15], v[108:111], v[28:31], v[12:15]\nds_read_b128 v[28:31], v2 offset:2560\n" \
  "s_waitcnt lgkmcnt(4)\nv_mfma_f32_16x16x32_f16 v[12:15], v[104:107], v[32:35], v[12:15]\nds_read_b128 v[32:35], v2 offset:2624\n" \
  "v_mfma_f32_16x16x32_f16 v[12:15], v[100:103], v[124:127], v[12:15]\ns_waitcnt lgkmcnt(3)\n" \
  "v_mfma_f32_16x16x32_f16 v[12:15], v[96:99], v[36:39], v[12:15]\nds_read_b128 v[124:127], v2 offset:2688\n" \
  "v_mfma_f32_16x16x32_f16 v[12:15], v[92:95], v[40:43], v[12:15]\ns_waitcnt lgkmcnt(3)\n" \
  "v_mfma_f32_16x16x32_f16 v[12:15], v[88:91], v[44:47], v[12:15]\nds_read_b128 v[36:39], v2 offset:2752\n" \
  "v_mfma_f32_16x16x32_f16 v[16:19], v[120:123], v[44:47], v[16:19]\ns_waitcnt lgkmcnt(3)\n" \
  "v_mfma_f32_16x16x32_f16 v[12:15], v[84:87], v[28:31], v[12:15]\nds_read_b128 v[40:43], v2 offset:2816\n" \
  "v_mfma_f32_16x16x32_f16 v[16:19], v[116:119], v[28:31], v[16:19]\ns_waitcnt lgkmcnt(3)\n" \
  "v_mfma_f32_16x16x32_f16 v[12:15], v[80:83], v[32:35], v[12:15]\nds_read_b128 v[44:47], v2 offset:2880\n" \
  "v_mfma_f32_16x16x32_f16 v[16:19], v[112:115], v[32:35], v[16:19]\ns_waitcnt lgkmcnt(3)\n" \
  "v_mfma_f32_16x16x32_f16 v[12:15], v[76:79], v[124:127], v[12:15]\nds_read_b128 v[28:31], v2 offset:2944\n" \
  "v_mfma_f32_16x16x32_f16 v[16:19], v[108:111], v[124:127], v[16:19]\ns_waitcnt lgkmcnt(3)\n" \
  "v_mfma_f32_16x16x32_f16 v[12:15], v[72:75], v[36:39], v[12:15]\nds_read_b128 v[32:35], v2 offset:3008\n" \
  "v_mfma_f32_16x16x32_f16 v[16:19], v[104:107], v[36:39], v[16:19]\ns_waitcnt lgkmcnt(3)\n" \
  "v_mfma_f32_16x16x32_f16 v[12:15], v[68:71], v[40:43], v[12:15]\nv_mfma_f32_16x16x32_f16 v[16:19], v[100:103], v[40:43], v[16:19]\n" \
  "ds_read_b128 v[124:127], v2 offset:3072\ns_waitcnt lgkmcnt(3)\nv_mfma_f32_16x16x32_f16 v[12:15], v[64:67], v[44:47], v[12:15]\n" \
  "v_mfma_f32_16x16x32_f16 v[16:19], v[96:99], v[44:47], v[16:19]\nds_read_b128 v[36:39], v2 offset:3136\ns_waitcnt lgkmcnt(3)\n" \
  "v_mfma_f32_16x16x32_f16 v[12:15], v[60:63], v[28:31], v[12:15]\nds_read_b64 v[40:41], v6 offset:128\nds_read_b64 v[42:43], v6 offset:136\n" \
  "v_mfma_f32_16x16x32_f16 v[16:19], v[92:95], v[28:31], v[16:19]\nds_read_b128 v[44:47], v2 offset:3200\ns_waitcnt lgkmcnt(4)\n" \
  "v_mfma_f32_16x16x32_f16 v[12:15], v[56:59], v[32:35], v[12:15]\nv_mfma_f32_16x16x32_f16 v[16:19], v[88:91], v[32:35], v[16:19]\n" \
  "ds_read_b128 v[28:31], v2 offset:3264\nv_mfma_f32_16x16x32_f16 v[20:23], v[120:123], v[32:35], v[20:23]\nds_read_b64 v[120:121], v6 offset:192\n" \
  "ds_read_b64 v[122:123], v6 offset:200\nv_mfma_f32_16x16x32_f16 v[16:19], v[84:87], v[124:127], v[16:19]\nds_read_b128 v[32:35], v2 offset:3328\n" \
  "v_mfma_f32_16x16x32_f16 v[20:23], v[116:119], v[124:127], v[20:23]\nds_read_b64 v[116:117], v6 offset:256\nds_read_b64 v[118:119], v6 offset:264\n" \
  "s_waitcnt lgkmcnt(6)\nv_mfma_f32_16x16x32_f16 v[16:19], v[80:83], v[36:39], v[16:19]\nds_read_b128 v[124:127], v2 offset:3392\n" \
  "v_mfma_f32_16x16x32_f16 v[20:23], v[112:115], v[36:39], v[20:23]\nds_read_b64 v[112:113], v6 offset:320\nds_read_b64 v[114:115], v6 offset:328\n" \
  "v_mfma_f32_16x16x32_f16 v[16:19], v[76:79], v[44:47], v[16:19]\nds_read_b128 v[36:39], v2 offset:3456\n" \
  "v_mfma_f32_16x16x32_f16 v[20:23], v[108:111], v[44:47], v[20:23]\ns_waitcnt lgkmcnt(6)\n" \
  "v_mfma_f32_16x16x32_f16 v[16:19], v[72:75], v[28:31], v[16:19]\nds_read_b128 v[108:111], v2 offset:3520\n" \
  "v_mfma_f32_16x16x32_f16 v[20:23], v[104:107], v[28:31], v[20:23]\nv_mfma_f32_16x16x32_f16 v[16:19], v[68:71], v[32:35], v[16:19]\n" \
  "ds_read_b128 v[44:47], v2 offset:3584\nv_mfma_f32_16x16x32_f16 v[20:23], v[100:103], v[32:35], v[20:23]\nds_read_b128 v[104:107], v2 offset:3648\n" \
  "ds_read_b32 v27, v9 offset:32\ns_waitcnt lgkmcnt(4)\nv_mfma_f32_16x16x32_f16 v[16:19], v[64:67], v[124:127], v[16:19]\n" \
  "ds_read_b128 v[28:31], v2 offset:3712\nv_mfma_f32_16x16x32_f16 v[20:23], v[96:99], v[124:127], v[20:23]\nds_read_b128 v[100:103], v2 offset:3776\n" \
  "ds_read2_b32 v[32:33], v8 offset0:32 offset1:33\nds_read2_b32 v[34:35], v8 offset0:34 offset1:35\n" \
  "v_mfma_f32_16x16x32_f16 v[16:19], v[60:63], v[36:39], v[16:19]\nds_read_b128 v[96:99], v2 offset:3840\n" \
  "v_mfma_f32_16x16x32_f16 v[20:23], v[92:95], v[36:39], v[20:23]\nds_read_b128 v[124:127], v2 offset:3904\ns_waitcnt lgkmcnt(8)\n" \
  "v_mfma_f32_16x16x32_f16 v[16:19], v[56:59], v[108:111], v[16:19]\nds_read_b128 v[92:95], v2 offset:3968\ns_waitcnt lgkmcnt(7)\n" \
  "v_readfirstlane_b32 s85, v27\ns_cmp_lt_u32 s85, 4\ns_cbranch_scc1 31f\n32:\nv_mfma_f32_16x16x32_f16 v[20:23], v[88:91], v[108:111], v[20:23]\n" \
  "ds_read_b128 v[36:39], v2 offset:4032\nds_read_b64_tr_b16 v[88:89], v5 offset:512\nds_read_b64_tr_b16 v[90:91], v5 offset:544\n" \
  "v_mfma_f32_16x16x32_f16 v[20:23], v[84:87], v[44:47], v[20:23]\nds_read_b64_tr_b16 v[108:109], v5 offset:768\n" \
  "ds_read_b64_tr_b16 v[110:111], v5 offset:800\nv_mfma_f32_16x16x32_f16 v[20:23], v[80:83], v[104:107], v[20:23]\n" \
  "ds_read_b64_tr_b16 v[84:85], v5 offset:1024\nds_read_b64_tr_b16 v[86:87], v5 offset:1056\ns_waitcnt lgkmcnt(12)\n" \
  "v_mfma_f32_16x16x32_f16 v[20:23], v[76:79], v[28:31], v[20:23]\nds_read_b64_tr_b16 v[44:45], v5 offset:1280\n" \
  "ds_read_b64_tr_b16 v[46:47], v5 offset:1312\nv_mfma_f32_16x16x32_f16 v[20:23], v[72:75], v[100:103], v[20:23]\ns_waitcnt lgkmcnt(10)\n" \
  "v_mfma_f32_16x16x32_f16 v[20:23], v[68:71], v[96:99], v[20:23]\nds_read_b64_tr_b16 v[80:81], v5 offset:1536\n" \
  "ds_read_b64_tr_b16 v[82:83], v5 offset:1568\nv_mfma_f32_16x16x32_f16 v[20:23], v[64:67], v[124:127], v[20:23]\n" \
  "ds_read_b64_tr_b16 v[104:105], v5 offset:1792\nds_read_b64_tr_b16 v[106:107], v5 offset:1824\ns_waitcnt lgkmcnt(12)\n" \
  "v_mfma_f32_16x16x32_f16 v[20:23], v[60:63], v[92:95], v[20:23]\nds_read_b64_tr_b16 v[76:77], v5 offset:2048\n" \
  "ds_read_b64_tr_b16 v[78:79], v5 offset:2080\nv_mfma_f32_16x16x32_f16 v[20:23], v[56:59], v[36:39], v[20:23]\ns_waitcnt lgkmcnt(10)\n" \
  "v_mfma_f32_16x16x32_f16 v[12:15], v[88:91], v[52:55], v[12:15]\nds_read_b64_tr_b16 v[28:29], v5 offset:2304\n" \
  "ds_read_b64_tr_b16 v[30:31], v5 offset:2336\nv_mfma_f32_16x16x32_f16 v[12:15], v[108:111], v[48:51], v[12:15]\n" \
  "ds_read_b64_tr_b16 v[72:73], v5 offset:2560\nds_read_b64_tr_b16 v[74:75], v5 offset:2592\ns_waitcnt lgkmcnt(10)\n" \
  "v_mfma_f32_16x16x32_f16 v[12:15], v[84:87], v[40:43], v[12:15]\nds_read_b64_tr_b16 v[100:101], v5 offset:2816\n" \
  "ds_read_b64_tr_b16 v[102:103], v5 offset:2848\nv_mfma_f32_16x16x32_f16 v[16:19], v[84:87], v[52:55], v[16:19]\n" \
  "ds_read_b64_tr_b16 v[68:69], v7 offset:0\nds_read_b64_tr_b16 v[70:71], v7 offset:64\n" \
  "v_mfma_f32_16x16x32_f16 v[12:15], v[44:47], v[120:123], v[12:15]\nv_mfma_f32_16x16x32_f16 v[16:19], v[44:47], v[48:51], v[16:19]\n" \
  "s_waitcnt lgkmcnt(10)\nv_mfma_f32_16x16x32_f16 v[12:15], v[80:83], v[116:119], v[12:15]\nds_read2_b32 v[96:97], v8 offset0:16 offset1:17\n" \
  "ds_read2_b32 v[98:99], v8 offset0:18 offset1:19\nv_mfma_f32_16x16x32_f16 v[16:19], v[80:83], v[40:43], v[16:19]\n" \
  "ds_read2_b32 v[64:65], v8 offset0:0 offset1:1\nds_read2_b32 v[66:67], v8 offset0:2 offset1:3\n" \
  "v_mfma_f32_16x16x32_f16 v[20:23], v[80:83], v[52:55], v[20:23]\nv_mfma_f32_16x16x32_f16 v[12:15], v[104:107], v[112:115], v[12:15]\n" \
  "v_mfma_f32_16x16x32_f16 v[16:19], v[104:107], v[120:123], v[16:19]\nv_mfma_f32_16x16x32_f16 v[20:23], v[104:107], v[48:51], v[20:23]\n" \
  "s_waitcnt lgkmcnt(10)\nv_mfma_f32_16x16x32_f16 v[16:19], v[76:79], v[116:119], v[16:19]\nds_read_b64_tr_b16 v[124:125], v7 offset:512\n" \
  "ds_read_b64_tr_b16 v[126:127], v7 offset:576\nv_mfma_f32_16x16x32_f16 v[20:23], v[76:79], v[40:43], v[20:23]\n" \
  "ds_read2_b32 v[60:61], v8 offset0:48 offset1:49\nds_read2_b32 v[62:63], v8 offset0:50 offset1:51\n" \
  "v_mfma_f32_16x16x32_f16 v[16:19], v[28:31], v[112:115], v[16:19]\nv_mfma_f32_16x16x32_f16 v[20:23], v[28:31], v[120:123], v[20:23]\n" \
  "s_waitcnt lgkmcnt(10)\nv_mfma_f32_16x16x32_f16 v[20:23], v[72:75], v[116:119], v[20:23]\nds_read_b64_tr_b16 v[92:93], v7 offset:1024\n" \
  "ds_read_b64_tr_b16 v[94:95], v7 offset:1088\nv_mfma_f32_16x16x32_f16 v[20:23], v[100:103], v[112:115], v[20:23]\n" \
  "ds_read_b64_tr_b16 v[56:57], v7 offset:1536\nds_read_b64_tr_b16 v[58:59], v7 offset:1600\ns_waitcnt lgkmcnt(10)\n" \
  "v_mfma_f32_16x16x32_f16 v[12:15], v[68:71], v[32:35], v[12:15]\nv_mfma_f32_16x16x32_f16 v[16:19], v[68:71], v[96:99], v[16:19]\n" \
  "s_waitcnt lgkmcnt(6)\nv_mfma_f32_16x16x32_f16 v[20:23], v[68:71], v[64:67], v[20:23]\ns_waitcnt lgkmcnt(2)\n" \
  "v_mfma_f32_16x16x32_f16 v[12:15], v[124:127], v[60:63], v[12:15]\nv_mfma_f32_16x16x32_f16 v[16:19], v[124:127], v[32:35], v[16:19]\n" \
  "v_mfma_f32_16x16x32_f16 v[20:23], v[124:127], v[96:99], v[20:23]\nv_mfma_f32_16x16x32_f16 v[16:19], v[92:95], v[60:63], v[16:19]\n" \
  "v_mfma_f32_16x16x32_f16 v[20:23], v[92:95], v[32:35], v[20:23]\ns_waitcnt lgkmcnt(0)\n" \
  "v_mfma_f32_16x16x32_f16 v[20:23], v[56:59], v[60:63], v[20:23]\ns_nop 1\nv_cvt_pkrtz_f16_f32 v24, v12, v13\nv_cvt_pkrtz_f16_f32 v25, v14, v15\n" \
  "v_pk_add_u16 v24, v24, 4 op_sel_hi:[1,0] clamp\nv_pk_add_u16 v25, v25, 4 op_sel_hi:[1,0] clamp\nds_write_b64 v4, v[24:25] offset:5120\n" \
  "v_cvt_pkrtz_f16_f32 v24, v16, v17\nv_cvt_pkrtz_f16_f32 v25, v18, v19\nv_pk_add_u16 v24, v24, 4 op_sel_hi:[1,0] clamp\n" \
  "v_pk_add_u16 v25, v25, 4 op_sel_hi:[1,0] clamp\nds_write_b64 v4, v[24:25] offset:6144\nv_cvt_pkrtz_f16_f32 v24, v20, v21\n" \
  "v_cvt_pkrtz_f16_f32 v25, v22, v23\nv_pk_add_u16 v24, v24, 4 op_sel_hi:[1,0] clamp\nv_pk_add_u16 v25, v25, 4 op_sel_hi:[1,0] clamp\n" \
  "ds_write_b64 v4, v[24:25] offset:7168\ns_setprio 0\ns_setpc_b64 s[90:91]\n31:\ns_movk_i32 s84, 0x4000\n33:\nds_read_b32 v27, v9 offset:32\n" \
  "s_sub_u32 s84, s84, 1\ns_cbranch_scc1 32b\ns_waitcnt lgkmcnt(0)\nv_readfirstlane_b32 s85, v27\ns_cmp_lt_u32 s85, 4\ns_cbranch_scc1 33b\n" \
  "s_branch 32b\n9:\ns_cmp_eq_u32 s89, 3\ns_cbranch_scc1 3f\ns_cmp_eq_u32 s89, 4\ns_cbranch_scc1 4f\ns_cmp_eq_u32 s89, 5\ns_cbranch_scc1 5f\n" \
  "ds_read_b64 v[124:125], v3 offset:576\nds_read_b64 v[126:127], v3 offset:584\nds_read_b64 v[120:121], v3 offset:64\n" \
  "ds_read_b64 v[122:123], v3 offset:72\nds_read_b64 v[116:117], v3 offset:640\nds_read_b64 v[118:119], v3 offset:648\n" \
  "ds_read_b64 v[112:113], v3 offset:128\nds_read_b64 v[114:115], v3 offset:136\nds_read_b64 v[108:109], v3 offset:704\n" \
  "ds_read_b64 v[110:111], v3 offset:712\nds_read_b64 v[104:105], v3 offset:192\nds_read_b64 v[106:107], v3 offset:200\n" \
  "ds_read_b64 v[100:101], v3 offset:768\nds_read_b64 v[102:103], v3 offset:776\nds_read_b64 v[96:97], v3 offset:256\n" \
  "ds_read_b64 v[98:99], v3 offset:264\nds_read_b64 v[92:93], v3 offset:832\nds_read_b64 v[94:95], v3 offset:840\n" \
  "ds_read_b64 v[88:89], v3 offset:320\nds_read_b64 v[90:91], v3 offset:328\nds_read_b64 v[84:85], v3 offset:896\n" \
  "ds_read_b64 v[86:87], v3 offset:904\nds_read_b64 v[80:81], v3 offset:384\nds_read_b64 v[82:83], v3 offset:392\n" \
  "ds_read_b64 v[76:77], v3 offset:960\nds_read_b64 v[78:79], v3 offset:968\nds_read_b64 v[72:73], v3 offset:448\n" \
  "ds_read_b64 v[74:75], v3 offset:456\nds_read_b64 v[68:69], v3 offset:1024\nds_read_b64 v[70:71], v3 offset:1032\n" \
  "ds_read_b64 v[64:65], v3 offset:512\nds_read_b64 v[66:67], v3 offset:520\ns_waitcnt lgkmcnt(0)\ns_barrier\nds_read_b128 v[24:27], v2 offset:0\n" \
  "ds_read_b128 v[28:31], v2 offset:64\nds_read_b128 v[32:35], v2 offset:128\nds_read_b128 v[36:39], v2 offset:192\ns_waitcnt lgkmcnt(3)\n" \
  "v_mfma_f32_16x16x32_f16 v[12:15], v[124:127], v[24:27], 0\ns_setprio 1\nds_read_b128 v[40:43], v2 offset:256\n" \
  "v_mfma_f32_16x16x32_f16 v[16:19], v[120:123], v[24:27], 0\ns_waitcnt lgkmcnt(3)\nv_mfma_f32_16x16x32_f16 v[12:15], v[116:119], v[28:31], v[12:15]\n" \
  "ds_read_b128 v[44:47], v2 offset:320\nv_mfma_f32_16x16x32_f16 v[16:19], v[112:115], v[28:31], v[16:19]\ns_waitcnt lgkmcnt(3)\n" \
  "v_mfma_f32_16x16x32_f16 v[12:15], v[108:111], v[32:35], v[12:15]\nds_read_b128 v[48:51], v2 offset:384\n" \
  "v_mfma_f32_16x16x32_f16 v[16:19], v[104:107], v[32:35], v[16:19]\ns_waitcnt lgkmcnt(3)\n" \
  "v_mfma_f32_16x16x32_f16 v[12:15], v[100:103], v[36:39], v[12:15]\nds_read_b128 v[52:55], v2 offset:448\n" \
  "v_mfma_f32_16x16x32_f16 v[16:19], v[96:99], v[36:39], v[16:19]\ns_waitcnt lgkmcnt(3)\n" \
  "v_mfma_f32_16x16x32_f16 v[12:15], v[92:95], v[40:43], v[12:15]\nds_read_b128 v[56:59], v2 offset:512\n" \
  "v_mfma_f32_16x16x32_f16 v[16:19], v[88:91], v[40:43], v[16:19]\nds_read_b128 v[60:63], v2 offset:576\ns_waitcnt lgkmcnt(3)\n" \
  "v_mfma_f32_16x16x32_f16 v[12:15], v[84:87], v[44:47], v[12:15]\nds_read_b128 v[120:123], v2 offset:640\n" \
  "v_mfma_f32_16x16x32_f16 v[16:19], v[80:83], v[44:47], v[16:19]\nds_read_b128 v[24:27], v2 offset:704\n" \
  "v_mfma_f32_16x16x32_f16 v[12:15], v[76:79], v[48:51], v[12:15]\nds_read_b128 v[112:115], v2 offset:768\n" \
  "v_mfma_f32_16x16x32_f16 v[16:19], v[72:75], v[48:51], v[16:19]\nds_read_b128 v[28:31], v2 offset:832\ns_waitcnt lgkmcnt(5)\n" \
  "v_mfma_f32_16x16x32_f16 v[12:15], v[68:71], v[52:55], v[12:15]\nds_read_b128 v[104:107], v2 offset:896\n" \
  "v_mfma_f32_16x16x32_f16 v[16:19], v[64:67], v[52:55], v[16:19]\nds_read_b128 v[32:35], v2 offset:960\n" \
  "v_mfma_f32_16x16x32_f16 v[16:19], v[124:127], v[56:59], v[16:19]\ns_waitcnt lgkmcnt(5)\n" \
  "v_mfma_f32_16x16x32_f16 v[16:19], v[116:119], v[60:63], v[16:19]\nv_mfma_f32_16x16x32_f16 v[16:19], v[108:111], v[120:123], v[16:19]\n" \
  "s_waitcnt lgkmcnt(3)\nv_mfma_f32_16x16x32_f16 v[16:19], v[100:103], v[24:27], v[16:19]\nv_cvt_pkrtz_f16_f32 v20, v12, v13\n" \
  "v_cvt_pkrtz_f16_f32 v21, v14, v15\nv_mfma_f32_16x16x32_f16 v[16:19], v[92:95], v[112:115], v[16:19]\n" \
  "v_pk_add_u16 v20, v20, 4 op_sel_hi:[1,0] clamp\ns_waitcnt lgkmcnt(2)\nv_mfma_f32_16x16x32_f16 v[16:19], v[84:87], v[28:31], v[16:19]\n" \
  "v_pk_add_u16 v21, v21, 4 op_sel_hi:[1,0] clamp\ns_waitcnt lgkmcnt(1)\nv_mfma_f32_16x16x32_f16 v[16:19], v[76:79], v[104:107], v[16:19]\n" \
  "ds_write_b64 v4, v[20:21] offset:0\ns_waitcnt lgkmcnt(1)\nv_mfma_f32_16x16x32_f16 v[16:19], v[68:71], v[32:35], v[16:19]\ns_nop 7\n" \
  "v_cvt_pkrtz_f16_f32 v20, v16, v17\nv_cvt_pkrtz_f16_f32 v21, v18, v19\nv_pk_add_u16 v20, v20, 4 op_sel_hi:[1,0] clamp\n" \
  "v_pk_add_u16 v21, v21, 4 op_sel_hi:[1,0] clamp\nds_write_b64 v4, v[20:21] offset:1024\ns_setprio 0\ns_setpc_b64 s[90:91]\n3:\n" \
  "ds_read_b64 v[124:125], v3 offset:0\nds_read_b64 v[126:127], v3 offset:8\nds_read_b64 v[120:121], v3 offset:64\n" \
  "ds_read_b64 v[122:123], v3 offset:72\nds_read_b64 v[116:117], v3 offset:128\nds_read_b64 v[118:119], v3 offset:136\n" \
  "ds_read_b64 v[112:113], v3 offset:192\nds_read_b64 v[114:115], v3 offset:200\nds_read_b64 v[108:109], v3 offset:256\n" \
  "ds_read_b64 v[110:111], v3 offset:264\nds_read_b64 v[104:105], v3 offset:320\nds_read_b64 v[106:107], v3 offset:328\n" \
  "ds_read_b64 v[100:101], v3 offset:384\nds_read_b64 v[102:103], v3 offset:392\nds_read_b64 v[96:97], v3 offset:448\n" \
  "ds_read_b64 v[98:99], v3 offset:456\nds_read_b64 v[92:93], v3 offset:512\nds_read_b64 v[94:95], v3 offset:520\n" \
  "ds_read_b64 v[88:89], v3 offset:576\nds_read_b64 v[90:91], v3 offset:584\nds_read_b64 v[84:85], v3 offset:640\n" \
  "ds_read_b64 v[86:87], v3 offset:648\nds_read_b64 v[80:81], v3 offset:704\nds_read_b64 v[82:83], v3 offset:712\n" \
  "ds_read_b64 v[76:77], v3 offset:768\nds_read_b64 v[78:79], v3 offset:776\nds_read_b64 v[72:73], v3 offset:832\n" \
  "ds_read_b64 v[74:75], v3 offset:840\nds_read_b64 v[68:69], v3 offset:896\nds_read_b64 v[70:71], v3 offset:904\n" \
  "ds_read_b64 v[64:65], v3 offset:960\nds_read_b64 v[66:67], v3 offset:968\nds_read_b64 v[60:61], v3 offset:1024\n" \
  "ds_read_b64 v[62:63], v3 offset:1032\nds_read_b64 v[56:57], v6 offset:256\nds_read_b64 v[58:59], v6 offset:264\n" \
  "ds_read_b64 v[52:53], v6 offset:128\nds_read_b64 v[54:55], v6 offset:136\nds_read_b64 v[48:49], v6 offset:320\n" \
  "ds_read_b64 v[50:51], v6 offset:328\nds_read_b64 v[44:45], v6 offset:192\nds_read_b64 v[46:47], v6 offset:200\ns_waitcnt lgkmcnt(0)\ns_barrier\n" \
  "ds_read_b128 v[24:27], v2 offset:448\nds_read_b128 v[28:31], v2 offset:512\nds_read_b128 v[32:35], v2 offset:576\n" \
  "ds_read_b128 v[36:39], v2 offset:640\nds_read_b128 v[40:43], v2 offset:704\ns_waitcnt lgkmcnt(3)\n" \
  "v_mfma_f32_16x16x32_f16 v[12:15], v[124:127], v[24:27], 0\ns_setprio 1\nds_read_b128 v[24:27], v2 offset:768\n" \
  "v_mfma_f32_16x16x32_f16 v[12:15], v[120:123], v[28:31], v[12:15]\nds_read_b128 v[28:31], v2 offset:832\ns_waitcnt lgkmcnt(3)\n" \
  "v_mfma_f32_16x16x32_f16 v[12:15], v[116:119], v[32:35], v[12:15]\nds_read_b128 v[32:35], v2 offset:896\n" \
  "v_mfma_f32_16x16x32_f16 v[12:15], v[112:115], v[36:39], v[12:15]\nds_read_b128 v[36:39], v2 offset:960\ns_waitcnt lgkmcnt(3)\n" \
  "v_mfma_f32_16x16x32_f16 v[12:15], v[108:111], v[40:43], v[12:15]\nds_read_b128 v[40:43], v2 offset:1024\n" \
  "v_mfma_f32_16x16x32_f16 v[12:15], v[104:107], v[24:27], v[12:15]\nds_read_b128 v[24:27], v2 offset:1088\ns_waitcnt lgkmcnt(3)\n" \
  "v_mfma_f32_16x16x32_f16 v[12:15], v[100:103], v[28:31], v[12:15]\nds_read_b128 v[28:31], v2 offset:1152\n" \
  "v_mfma_f32_16x16x32_f16 v[12:15], v[96:99], v[32:35], v[12:15]\ns_waitcnt lgkmcnt(3)\n" \
  "v_mfma_f32_16x16x32_f16 v[12:15], v[92:95], v[36:39], v[12:15]\nds_read_b128 v[32:35], v2 offset:1216\n" \
  "v_mfma_f32_16x16x32_f16 v[16:19], v[124:127], v[36:39], 0\ns_waitcnt lgkmcnt(3)\nv_mfma_f32_16x16x32_f16 v[12:15], v[88:91], v[40:43], v[12:15]\n" \
  "ds_read_b128 v[124:127], v2 offset:1280\nv_mfma_f32_16x16x32_f16 v[16:19], v[120:123], v[40:43], v[16:19]\ns_waitcnt lgkmcnt(3)\n" \
  "v_mfma_f32_16x16x32_f16 v[12:15], v[84:87], v[24:27], v[12:15]\nds_read_b128 v[36:39], v2 offset:1344\n" \
  "v_mfma_f32_16x16x32_f16 v[16:19], v[116:119], v[24:27], v[16:19]\ns_waitcnt lgkmcnt(3)\n" \
  "v_mfma_f32_16x16x32_f16 v[12:15], v[80:83], v[28:31], v[12:15]\nds_read_b128 v[120:123], v2 offset:1408\n" \
  "v_mfma_f32_16x16x32_f16 v[16:19], v[112:115], v[28:31], v[16:19]\ns_waitcnt lgkmcnt(3)\n" \
  "v_mfma_f32_16x16x32_f16 v[12:15], v[76:79], v[32:35], v[12:15]\nds_read_b128 v[40:43], v2 offset:1472\n" \
  "v_mfma_f32_16x16x32_f16 v[16:19], v[108:111], v[32:35], v[16:19]\ns_waitcnt lgkmcnt(3)\n" \
  "v_mfma_f32_16x16x32_f16 v[12:15], v[72:75], v[124:127], v[12:15]\nds_read_b128 v[116:119], v2 offset:1536\n" \
  "v_mfma_f32_16x16x32_f16 v[16:19], v[104:107], v[124:127], v[16:19]\nds_read_b128 v[24:27], v2 offset:1600\ns_waitcnt lgkmcnt(3)\n" \
  "v_mfma_f32_16x16x32_f16 v[12:15], v[68:71], v[36:39], v[12:15]\nds_read_b128 v[112:115], v2 offset:1664\nds_read_b32 v23, v9 offset:32\n" \
  "v_mfma_f32_16x16x32_f16 v[16:19], v[100:103], v[36:39], v[16:19]\nds_read_b128 v[28:31], v2 offset:1728\n" \
  "v_mfma_f32_16x16x32_f16 v[12:15], v[64:67], v[120:123], v[12:15]\nds_read_b128 v[108:111], v2 offset:1792\n" \
  "v_mfma_f32_16x16x32_f16 v[16:19], v[96:99], v[120:123], v[16:19]\nds_read_b128 v[32:35], v2 offset:1856\ns_waitcnt lgkmcnt(6)\n" \
  "v_mfma_f32_16x16x32_f16 v[12:15], v[60:63], v[40:43], v[12:15]\nds_read_b128 v[104:107], v2 offset:1920\n" \
  "v_mfma_f32_16x16x32_f16 v[16:19], v[92:95], v[40:43], v[16:19]\nds_read_b128 v[124:127], v2 offset:1984\ns_waitcnt lgkmcnt(5)\n" \
  "v_readfirstlane_b32 s85, v23\ns_cmp_lt_u32 s85, 3\ns_cbranch_scc1 51f\n52:\nv_mfma_f32_16x16x32_f16 v[16:19], v[88:91], v[116:119], v[16:19]\n" \
  "ds_read_b64_tr_b16 v[100:101], v5 offset:0\nds_read_b64_tr_b16 v[102:103], v5 offset:32\n" \
  "v_mfma_f32_16x16x32_f16 v[16:19], v[84:87], v[24:27], v[16:19]\nds_read_b64_tr_b16 v[36:37], v5 offset:256\n" \
  "ds_read_b64_tr_b16 v[38:39], v5 offset:288\nv_mfma_f32_16x16x32_f16 v[16:19], v[80:83], v[112:115], v[16:19]\n" \
  "ds_read_b64_tr_b16 v[96:97], v5 offset:512\nds_read_b64_tr_b16 v[98:99], v5 offset:544\ns_waitcnt lgkmcnt(9)\n" \
  "v_mfma_f32_16x16x32_f16 v[16:19], v[76:79], v[28:31], v[16:19]\nds_read_b64_tr_b16 v[120:121], v5 offset:768\n" \
  "ds_read_b64_tr_b16 v[122:123], v5 offset:800\nv_mfma_f32_16x16x32_f16 v[16:19], v[72:75], v[108:111], v[16:19]\ns_waitcnt lgkmcnt(9)\n" \
  "v_mfma_f32_16x16x32_f16 v[16:19], v[68:71], v[32:35], v[16:19]\nv_mfma_f32_16x16x32_f16 v[16:19], v[64:67], v[104:107], v[16:19]\n" \
  "s_waitcnt lgkmcnt(6)\nv_mfma_f32_16x16x32_f16 v[16:19], v[60:63], v[124:127], v[16:19]\n" \
  "v_mfma_f32_16x16x32_f16 v[12:15], v[100:103], v[56:59], v[12:15]\nv_mfma_f32_16x16x32_f16 v[16:19], v[100:103], v[52:55], v[16:19]\n" \
  "s_waitcnt lgkmcnt(2)\nv_mfma_f32_16x16x32_f16 v[12:15], v[36:39], v[48:51], v[12:15]\n" \
  "v_mfma_f32_16x16x32_f16 v[16:19], v[36:39], v[44:47], v[16:19]\nv_mfma_f32_16x16x32_f16 v[16:19], v[96:99], v[56:59], v[16:19]\n" \
  "s_waitcnt lgkmcnt(0)\nv_mfma_f32_16x16x32_f16 v[16:19], v[120:123], v[48:51], v[16:19]\ns_nop 3\nv_cvt_pkrtz_f16_f32 v20, v12, v13\n" \
  "v_cvt_pkrtz_f16_f32 v21, v14, v15\nv_pk_add_u16 v20, v20, 4 op_sel_hi:[1,0] clamp\nv_pk_add_u16 v21, v21, 4 op_sel_hi:[1,0] clamp\n" \
  "ds_write_b64 v4, v[20:21] offset:2048\nv_cvt_pkrtz_f16_f32 v20, v16, v17\nv_cvt_pkrtz_f16_f32 v21, v18, v19\n" \
  "v_pk_add_u16 v20, v20, 4 op_sel_hi:[1,0] clamp\nv_pk_add_u16 v21, v21, 4 op_sel_hi:[1,0] clamp\nds_write_b64 v4, v[20:21] offset:3072\n" \
  "s_setprio 0\ns_setpc_b64 s[90:91]\n51:\ns_movk_i32 s84, 0x4000\n53:\nds_read_b32 v23, v9 offset:32\ns_sub_u32 s84, s84, 1\ns_cbranch_scc1 52b\n" \
  "s_waitcnt lgkmcnt(0)\nv_readfirstlane_b32 s85, v23\ns_cmp_lt_u32 s85, 3\ns_cbranch_scc1 53b\ns_branch 52b\n4:\n" \
  "ds_read_b64 v[124:125], v3 offset:1088\nds_read_b64 v[126:127], v3 offset:1096\nds_read_b64 v[120:121], v3 offset:0\n" \
  "ds_read_b64 v[122:123], v3 offset:8\nds_read_b64 v[116:117], v3 offset:64\nds_read_b64 v[118:119], v3 offset:72\n" \
  "ds_read_b64 v[112:113], v3 offset:128\nds_read_b64 v[114:115], v3 offset:136\nds_read_b64 v[108:109], v3 offset:192\n" \
  "ds_read_b64 v[110:111], v3 offset:200\nds_read_b64 v[104:105], v3 offset:256\nds_read_b64 v[106:107], v3 offset:264\n" \
  "ds_read_b64 v[100:101], v3 offset:320\nds_read_b64 v[102:103], v3 offset:328\nds_read_b64 v[96:97], v3 offset:384\n" \
  "ds_read_b64 v[98:99], v3 offset:392\nds_read_b64 v[92:93], v3 offset:448\nds_read_b64 v[94:95], v3 offset:456\n" \
  "ds_read_b64 v[88:89], v3 offset:512\nds_read_b64 v[90:91], v3 offset:520\nds_read_b64 v[84:85], v3 offset:576\n" \
  "ds_read_b64 v[86:87], v3 offset:584\nds_read_b64 v[80:81], v3 offset:640\nds_read_b64 v[82:83], v3 offset:648\n" \
  "ds_read_b64 v[76:77], v3 offset:704\nds_read_b64 v[78:79], v3 offset:712\nds_read_b64 v[72:73], v3 offset:768\n" \
  "ds_read_b64 v[74:75], v3 offset:776\nds_read_b64 v[68:69], v3 offset:832\nds_read_b64 v[70:71], v3 offset:840\n" \
  "ds_read_b64 v[64:65], v3 offset:896\nds_read_b64 v[66:67], v3 offset:904\nds_read_b64 v[60:61], v3 offset:960\n" \
  "ds_read_b64 v[62:63], v3 offset:968\nds_read_b64 v[56:57], v3 offset:1024\nds_read_b64 v[58:59], v3 offset:1032\n" \
  "ds_read_b64 v[52:53], v6 offset:0\nds_read_b64 v[54:55], v6 offset:8\nds_read_b64 v[48:49], v6 offset:64\nds_read_b64 v[50:51], v6 offset:72\n" \
  "ds_read_b64 v[44:45], v6 offset:128\nds_read_b64 v[46:47], v6 offset:136\nds_read_b64 v[40:41], v6 offset:192\n" \
  "ds_read_b64 v[42:43], v6 offset:200\ns_waitcnt lgkmcnt(0)\ns_barrier\nds_read_b128 v[24:27], v2 offset:448\nds_read_b128 v[28:31], v2 offset:960\n" \
  "ds_read_b128 v[32:35], v2 offset:1472\nds_read_b128 v[36:39], v2 offset:1536\ns_waitcnt lgkmcnt(3)\n" \
  "v_mfma_f32_16x16x32_f16 v[12:15], v[124:127], v[24:27], 0\ns_setprio 1\nds_read_b128 v[24:27], v2 offset:1600\ns_waitcnt lgkmcnt(3)\n" \
  "v_mfma_f32_16x16x32_f16 v[16:19], v[124:127], v[28:31], 0\nds_read_b128 v[124:127], v2 offset:1664\ns_waitcnt lgkmcnt(3)\n" \
  "v_mfma_f32_16x16x32_f16 v[12:15], v[120:123], v[32:35], v[12:15]\nds_read_b128 v[28:31], v2 offset:1728\ns_waitcnt lgkmcnt(3)\n" \
  "v_mfma_f32_16x16x32_f16 v[12:15], v[116:119], v[36:39], v[12:15]\nds_read_b128 v[32:35], v2 offset:1792\ns_waitcnt lgkmcnt(3)\n" \
  "v_mfma_f32_16x16x32_f16 v[12:15], v[112:115], v[24:27], v[12:15]\nds_read_b128 v[36:39], v2 offset:1856\ns_waitcnt lgkmcnt(3)\n" \
  "v_mfma_f32_16x16x32_f16 v[12:15], v[108:111], v[124:127], v[12:15]\nds_read_b128 v[24:27], v2 offset:1920\ns_waitcnt lgkmcnt(3)\n" \
  "v_mfma_f32_16x16x32_f16 v[12:15], v[104:107], v[28:31], v[12:15]\nds_read_b128 v[124:127], v2 offset:1984\ns_waitcnt lgkmcnt(3)\n" \
  "v_mfma_f32_16x16x32_f16 v[12:15], v[100:103], v[32:35], v[12:15]\nds_read_b128 v[28:31], v2 offset:2048\ns_waitcnt lgkmcnt(3)\n" \
  "v_mfma_f32_16x16x32_f16 v[12:15], v[96:99], v[36:39], v[12:15]\nds_read_b128 v[32:35], v2 offset:2112\ns_waitcnt lgkmcnt(3)\n" \
  "v_mfma_f32_16x16x32_f16 v[12:15], v[92:95], v[24:27], v[12:15]\nds_read_b128 v[36:39], v2 offset:2176\ns_waitcnt lgkmcnt(3)\n" \
  "v_mfma_f32_16x16x32_f16 v[12:15], v[88:91], v[124:127], v[12:15]\nds_read_b128 v[24:27], v2 offset:2240\n" \
  "v_mfma_f32_16x16x32_f16 v[16:19], v[120:123], v[124:127], v[16:19]\nds_read_b64 v[120:121], v6 offset:256\nds_read_b64 v[122:123], v6 offset:264\n" \
  "s_waitcnt lgkmcnt(4)\nv_mfma_f32_16x16x32_f16 v[12:15], v[84:87], v[28:31], v[12:15]\nds_read_b128 v[124:127], v2 offset:2304\n" \
  "v_mfma_f32_16x16x32_f16 v[16:19], v[116:119], v[28:31], v[16:19]\nds_read_b64 v[116:117], v6 offset:320\nds_read_b64 v[118:119], v6 offset:328\n" \
  "v_mfma_f32_16x16x32_f16 v[12:15], v[80:83], v[32:35], v[12:15]\nds_read_b128 v[28:31], v2 offset:2368\n" \
  "v_mfma_f32_16x16x32_f16 v[16:19], v[112:115], v[32:35], v[16:19]\ns_waitcnt lgkmcnt(6)\n" \
  "v_mfma_f32_16x16x32_f16 v[12:15], v[76:79], v[36:39], v[12:15]\nds_read_b128 v[112:115], v2 offset:2432\n" \
  "v_mfma_f32_16x16x32_f16 v[16:19], v[108:111], v[36:39], v[16:19]\nds_read2_b32 v[32:33], v8 offset0:48 offset1:49\n" \
  "ds_read2_b32 v[34:35], v8 offset0:50 offset1:51\nv_mfma_f32_16x16x32_f16 v[12:15], v[72:75], v[24:27], v[12:15]\n" \
  "ds_read_b128 v[108:111], v2 offset:2496\nv_mfma_f32_16x16x32_f16 v[16:19], v[104:107], v[24:27], v[16:19]\n" \
  "ds_read2_b32 v[36:37], v8 offset0:32 offset1:33\nds_read2_b32 v[38:39], v8 offset0:34 offset1:35\ns_waitcnt lgkmcnt(6)\n" \
  "v_mfma_f32_16x16x32_f16 v[12:15], v[68:71], v[124:127], v[12:15]\nds_read_b128 v[104:107], v2 offset:2560\n" \
  "v_mfma_f32_16x16x32_f16 v[16:19], v[100:103], v[124:127], v[16:19]\nds_read_b128 v[24:27], v2 offset:2624\n" \
  "v_mfma_f32_16x16x32_f16 v[12:15], v[64:67], v[28:31], v[12:15]\nds_read_b128 v[100:103], v2 offset:2688\nds_read_b32 v23, v9 offset:32\n" \
  "v_mfma_f32_16x16x32_f16 v[16:19], v[96:99], v[28:31], v[16:19]\nds_read_b128 v[124:127], v2 offset:2752\ns_waitcnt lgkmcnt(7)\n" \
  "v_mfma_f32_16x16x32_f16 v[12:15], v[60:63], v[112:115], v[12:15]\nds_read_b128 v[96:99], v2 offset:2816\n" \
  "v_mfma_f32_16x16x32_f16 v[16:19], v[92:95], v[112:115], v[16:19]\nds_read_b128 v[28:31], v2 offset:2880\n" \
  "v_mfma_f32_16x16x32_f16 v[12:15], v[56:59], v[108:111], v[12:15]\nds_read_b128 v[92:95], v2 offset:2944\n" \
  "v_mfma_f32_16x16x32_f16 v[16:19], v[88:91], v[108:111], v[16:19]\nds_read_b128 v[112:115], v2 offset:3008\ns_waitcnt lgkmcnt(5)\n" \
  "v_readfirstlane_b32 s85, v23\ns_cmp_lt_u32 s85, 3\ns_cbranch_scc1 61f\n62:\nv_mfma_f32_16x16x32_f16 v[16:19], v[84:87], v[104:107], v[16:19]\n" \
  "ds_read_b64_tr_b16 v[88:89], v5 offset:0\nds_read_b64_tr_b16 v[90:91], v5 offset:32\n" \
  "v_mfma_f32_16x16x32_f16 v[16:19], v[80:83], v[24:27], v[16:19]\nds_read_b64_tr_b16 v[108:109], v5 offset:256\n" \
  "ds_read_b64_tr_b16 v[110:111], v5 offset:288\nv_mfma_f32_16x16x32_f16 v[16:19], v[76:79], v[100:103], v[16:19]\n" \
  "ds_read_b64_tr_b16 v[84:85], v5 offset:512\nds_read_b64_tr_b16 v[86:87], v5 offset:544\ns_waitcnt lgkmcnt(9)\n" \
  "v_mfma_f32_16x16x32_f16 v[16:19], v[72:75], v[124:127], v[16:19]\nds_read_b64_tr_b16 v[104:105], v5 offset:768\n" \
  "ds_read_b64_tr_b16 v[106:107], v5 offset:800\nv_mfma_f32_16x16x32_f16 v[16:19], v[68:71], v[96:99], v[16:19]\n" \
  "ds_read_b64_tr_b16 v[80:81], v5 offset:1024\nds_read_b64_tr_b16 v[82:83], v5 offset:1056\ns_waitcnt lgkmcnt(11)\n" \
  "v_mfma_f32_16x16x32_f16 v[16:19], v[64:67], v[28:31], v[16:19]\nds_read_b64_tr_b16 v[24:25], v5 offset:1280\n" \
  "ds_read_b64_tr_b16 v[26:27], v5 offset:1312\nv_mfma_f32_16x16x32_f16 v[16:19], v[60:63], v[92:95], v[16:19]\n" \
  "ds_read_b64_tr_b16 v[76:77], v5 offset:1536\nds_read_b64_tr_b16 v[78:79], v5 offset:1568\ns_waitcnt lgkmcnt(12)\n" \
  "v_mfma_f32_16x16x32_f16 v[16:19], v[56:59], v[112:115], v[16:19]\nds_read_b64_tr_b16 v[100:101], v5 offset:1792\n" \
  "ds_read_b64_tr_b16 v[102:103], v5 offset:1824\nv_mfma_f32_16x16x32_f16 v[12:15], v[88:91], v[52:55], v[12:15]\ns_waitcnt lgkmcnt(10)\n" \
  "v_mfma_f32_16x16x32_f16 v[12:15], v[108:111], v[48:51], v[12:15]\nds_read_b64_tr_b16 v[72:73], v7 offset:0\n" \
  "ds_read_b64_tr_b16 v[74:75], v7 offset:64\nv_mfma_f32_16x16x32_f16 v[12:15], v[84:87], v[44:47], v[12:15]\n" \
  "ds_read_b64_tr_b16 v[124:125], v7 offset:512\nds_read_b64_tr_b16 v[126:127], v7 offset:576\n" \
  "v_mfma_f32_16x16x32_f16 v[16:19], v[84:87], v[52:55], v[16:19]\ns_waitcnt lgkmcnt(10)\n" \
  "v_mfma_f32_16x16x32_f16 v[12:15], v[104:107], v[40:43], v[12:15]\nv_mfma_f32_16x16x32_f16 v[16:19], v[104:107], v[48:51], v[16:19]\n" \
  "v_mfma_f32_16x16x32_f16 v[12:15], v[80:83], v[120:123], v[12:15]\nv_mfma_f32_16x16x32_f16 v[16:19], v[80:83], v[44:47], v[16:19]\n" \
  "s_waitcnt lgkmcnt(6)\nv_mfma_f32_16x16x32_f16 v[12:15], v[24:27], v[116:119], v[12:15]\n" \
  "v_mfma_f32_16x16x32_f16 v[16:19], v[24:27], v[40:43], v[16:19]\nv_mfma_f32_16x16x32_f16 v[16:19], v[76:79], v[120:123], v[16:19]\n" \
  "s_waitcnt lgkmcnt(2)\nv_mfma_f32_16x16x32_f16 v[16:19], v[100:103], v[116:119], v[16:19]\n" \
  "v_mfma_f32_16x16x32_f16 v[12:15], v[72:75], v[32:35], v[12:15]\nv_mfma_f32_16x16x32_f16 v[16:19], v[72:75], v[36:39], v[16:19]\n" \
  "s_waitcnt lgkmcnt(0)\nv_mfma_f32_16x16x32_f16 v[16:19], v[124:127], v[32:35], v[16:19]\ns_nop 4\nv_cvt_pkrtz_f16_f32 v20, v12, v13\n" \
  "v_cvt_pkrtz_f16_f32 v21, v14, v15\nv_pk_add_u16 v20, v20, 4 op_sel_hi:[1,0] clamp\nv_pk_add_u16 v21, v21, 4 op_sel_hi:[1,0] clamp\n" \
  "ds_write_b64 v4, v[20:21] offset:4096\nv_cvt_pkrtz_f16_f32 v20, v16, v17\nv_cvt_pkrtz_f16_f32 v21, v18, v19\n" \
  "v_pk_add_u16 v20, v20, 4 op_sel_hi:[1,0] clamp\nv_pk_add_u16 v21, v21, 4 op_sel_hi:[1,0] clamp\nds_write_b64 v4, v[20:21] offset:5120\n" \
  "s_setprio 0\ns_setpc_b64 s[90:91]\n61:\ns_movk_i32 s84, 0x4000\n63:\nds_read_b32 v23, v9 offset:32\ns_sub_u32 s84, s84, 1\ns_cbranch_scc1 62b\n" \
  "s_waitcnt lgkmcnt(0)\nv_readfirstlane_b32 s85, v23\ns_cmp_lt_u32 s85, 3\ns_cbranch_scc1 63b\ns_branch 62b\n5:\n" \
  "ds_read_b64 v[124:125], v3 offset:1088\nds_read_b64 v[126:127], v3 offset:1096\nds_read_b64 v[120:121], v3 offset:0\n" \
  "ds_read_b64 v[122:123], v3 offset:8\nds_read_b64 v[116:117], v3 offset:64\nds_read_b64 v[118:119], v3 offset:72\n" \
  "ds_read_b64 v[112:113], v3 offset:128\nds_read_b64 v[114:115], v3 offset:136\nds_read_b64 v[108:109], v3 offset:192\n" \
  "ds_read_b64 v[110:111], v3 offset:200\nds_read_b64 v[104:105], v3 offset:256\nds_read_b64 v[106:107], v3 offset:264\n" \
  "ds_read_b64 v[100:101], v3 offset:320\nds_read_b64 v[102:103], v3 offset:328\nds_read_b64 v[96:97], v3 offset:384\n" \
  "ds_read_b64 v[98:99], v3 offset:392\nds_read_b64 v[92:93], v3 offset:448\nds_read_b64 v[94:95], v3 offset:456\n" \
  "ds_read_b64 v[88:89], v3 offset:512\nds_read_b64 v[90:91], v3 offset:520\nds_read_b64 v[84:85], v3 offset:576\n" \
  "ds_read_b64 v[86:87], v3 offset:584\nds_read_b64 v[80:81], v3 offset:640\nds_read_b64 v[82:83], v3 offset:648\n" \
  "ds_read_b64 v[76:77], v3 offset:704\nds_read_b64 v[78:79], v3 offset:712\nds_read_b64 v[72:73], v3 offset:768\n" \
  "ds_read_b64 v[74:75], v3 offset:776\nds_read_b64 v[68:69], v3 offset:832\nds_read_b64 v[70:71], v3 offset:840\n" \
  "ds_read_b64 v[64:65], v3 offset:896\nds_read_b64 v[66:67], v3 offset:904\nds_read_b64 v[60:61], v3 offset:960\n" \
  "ds_read_b64 v[62:63], v3 offset:968\nds_read_b64 v[56:57], v3 offset:1024\nds_read_b64 v[58:59], v3 offset:1032\n" \
  "ds_read_b64 v[52:53], v6 offset:0\nds_read_b64 v[54:55], v6 offset:8\nds_read_b64 v[48:49], v6 offset:64\nds_read_b64 v[50:51], v6 offset:72\n" \
  "ds_read_b64 v[44:45], v6 offset:128\nds_read_b64 v[46:47], v6 offset:136\nds_read_b64 v[40:41], v6 offset:192\n" \
  "ds_read_b64 v[42:43], v6 offset:200\ns_waitcnt lgkmcnt(0)\ns_barrier\nds_read_b128 v[24:27], v2 offset:1472\n" \
  "ds_read_b128 v[28:31], v2 offset:1984\nds_read_b128 v[32:35], v2 offset:2496\nds_read_b128 v[36:39], v2 offset:2560\ns_waitcnt lgkmcnt(3)\n" \
  "v_mfma_f32_16x16x32_f16 v[12:15], v[124:127], v[24:27], 0\ns_setprio 1\nds_read_b128 v[24:27], v2 offset:2624\ns_waitcnt lgkmcnt(3)\n" \
  "v_mfma_f32_16x16x32_f16 v[16:19], v[124:127], v[28:31], 0\nds_read_b128 v[124:127], v2 offset:2688\ns_waitcnt lgkmcnt(3)\n" \
  "v_mfma_f32_16x16x32_f16 v[12:15], v[120:123], v[32:35], v[12:15]\nds_read_b128 v[28:31], v2 offset:2752\ns_waitcnt lgkmcnt(3)\n" \
  "v_mfma_f32_16x16x32_f16 v[12:15], v[116:119], v[36:39], v[12:15]\nds_read_b128 v[32:35], v2 offset:2816\ns_waitcnt lgkmcnt(3)\n" \
  "v_mfma_f32_16x16x32_f16 v[12:15], v[112:115], v[24:27], v[12:15]\nds_read_b128 v[36:39], v2 offset:2880\ns_waitcnt lgkmcnt(3)\n" \
  "v_mfma_f32_16x16x32_f16 v[12:15], v[108:111], v[124:127], v[12:15]\nds_read_b128 v[24:27], v2 offset:2944\ns_waitcnt lgkmcnt(3)\n" \
  "v_mfma_f32_16x16x32_f16 v[12:15], v[104:107], v[28:31], v[12:15]\nds_read_b128 v[124:127], v2 offset:3008\ns_waitcnt lgkmcnt(3)\n" \
  "v_mfma_f32_16x16x32_f16 v[12:15], v[100:103], v[32:35], v[12:15]\nds_read_b128 v[28:31], v2 offset:3072\ns_waitcnt lgkmcnt(3)\n" \
  "v_mfma_f32_16x16x32_f16 v[12:15], v[96:99], v[36:39], v[12:15]\nds_read_b128 v[32:35], v2 offset:3136\ns_waitcnt lgkmcnt(3)\n" \
  "v_mfma_f32_16x16x32_f16 v[12:15], v[92:95], v[24:27], v[12:15]\nds_read_b128 v[36:39], v2 offset:3200\ns_waitcnt lgkmcnt(3)\n" \
  "v_mfma_f32_16x16x32_f16 v[12:15], v[88:91], v[124:127], v[12:15]\nds_read_b128 v[24:27], v2 offset:3264\n" \
  "v_mfma_f32_16x16x32_f16 v[16:19], v[120:123], v[124:127], v[16:19]\nds_read_b64 v[120:121], v6 offset:256\nds_read_b64 v[122:123], v6 offset:264\n" \
  "s_waitcnt lgkmcnt(4)\nv_mfma_f32_16x16x32_f16 v[12:15], v[84:87], v[28:31], v[12:15]\nds_read_b128 v[124:127], v2 offset:3328\n" \
  "v_mfma_f32_16x16x32_f16 v[16:19], v[116:119], v[28:31], v[16:19]\nds_read_b64 v[116:117], v6 offset:320\nds_read_b64 v[118:119], v6 offset:328\n" \
  "v_mfma_f32_16x16x32_f16 v[12:15], v[80:83], v[32:35], v[12:15]\nds_read_b128 v[28:31], v2 offset:3392\n" \
  "v_mfma_f32_16x16x32_f16 v[16:19], v[112:115], v[32:35], v[16:19]\ns_waitcnt lgkmcnt(6)\n" \
  "v_mfma_f32_16x16x32_f16 v[12:15], v[76:79], v[36:39], v[12:15]\nds_read_b128 v[112:115], v2 offset:3456\n" \
  "v_mfma_f32_16x16x32_f16 v[16:19], v[108:111], v[36:39], v[16:19]\nds_read2_b32 v[32:33], v8 offset0:16 offset1:17\n" \
  "ds_read2_b32 v[34:35], v8 offset0:18 offset1:19\nv_mfma_f32_16x16x32_f16 v[12:15], v[72:75], v[24:27], v[12:15]\n" \
  "ds_read_b128 v[108:111], v2 offset:3520\nv_mfma_f32_16x16x32_f16 v[16:19], v[104:107], v[24:27], v[16:19]\n" \
  "ds_read2_b32 v[36:37], v8 offset0:0 offset1:1\nds_read2_b32 v[38:39], v8 offset0:2 offset1:3\ns_waitcnt lgkmcnt(6)\n" \
  "v_mfma_f32_16x16x32_f16 v[12:15], v[68:71], v[124:127], v[12:15]\nds_read_b128 v[104:107], v2 offset:3584\n" \
  "v_mfma_f32_16x16x32_f16 v[16:19], v[100:103], v[124:127], v[16:19]\nds_read_b128 v[24:27], v2 offset:3648\n" \
  "ds_read2_b32 v[100:101], v8 offset0:32 offset1:33\nds_read2_b32 v[102:103], v8 offset0:34 offset1:35\n" \
  "v_mfma_f32_16x16x32_f16 v[12:15], v[64:67], v[28:31], v[12:15]\nds_read_b128 v[124:127], v2 offset:3712\nds_read_b32 v23, v9 offset:32\n" \
  "v_mfma_f32_16x16x32_f16 v[16:19], v[96:99], v[28:31], v[16:19]\nds_read_b128 v[96:99], v2 offset:3776\ns_waitcnt lgkmcnt(9)\n" \
  "v_mfma_f32_16x16x32_f16 v[12:15], v[60:63], v[112:115], v[12:15]\nds_read_b128 v[28:31], v2 offset:3840\n" \
  "v_mfma_f32_16x16x32_f16 v[16:19], v[92:95], v[112:115], v[16:19]\nds_read_b128 v[92:95], v2 offset:3904\n" \
  "v_mfma_f32_16x16x32_f16 v[12:15], v[56:59], v[108:111], v[12:15]\nds_read_b128 v[112:115], v2 offset:3968\n" \
  "v_mfma_f32_16x16x32_f16 v[16:19], v[88:91], v[108:111], v[16:19]\nds_read_b128 v[88:91], v2 offset:4032\ns_waitcnt lgkmcnt(5)\n" \
  "v_readfirstlane_b32 s85, v23\ns_cmp_lt_u32 s85, 3\ns_cbranch_scc1 71f\n72:\nv_mfma_f32_16x16x32_f16 v[16:19], v[84:87], v[104:107], v[16:19]\n" \
  "ds_read_b64_tr_b16 v[108:109], v5 offset:1024\nds_read_b64_tr_b16 v[110:111], v5 offset:1056\n" \
  "v_mfma_f32_16x16x32_f16 v[16:19], v[80:83], v[24:27], v[16:19]\nds_read_b64_tr_b16 v[84:85], v5 offset:1280\n" \
  "ds_read_b64_tr_b16 v[86:87], v5 offset:1312\nv_mfma_f32_16x16x32_f16 v[16:19], v[76:79], v[124:127], v[16:19]\n" \
  "ds_read_b64_tr_b16 v[104:105], v5 offset:1536\nds_read_b64_tr_b16 v[106:107], v5 offset:1568\ns_waitcnt lgkmcnt(9)\n" \
  "v_mfma_f32_16x16x32_f16 v[16:19], v[72:75], v[96:99], v[16:19]\nds_read_b64_tr_b16 v[80:81], v5 offset:1792\n" \
  "ds_read_b64_tr_b16 v[82:83], v5 offset:1824\nv_mfma_f32_16x16x32_f16 v[16:19], v[68:71], v[28:31], v[16:19]\n" \
  "ds_read_b64_tr_b16 v[24:25], v5 offset:2048\nds_read_b64_tr_b16 v[26:27], v5 offset:2080\ns_waitcnt lgkmcnt(11)\n" \
  "v_mfma_f32_16x16x32_f16 v[16:19], v[64:67], v[92:95], v[16:19]\nds_read_b64_tr_b16 v[76:77], v5 offset:2304\n" \
  "ds_read_b64_tr_b16 v[78:79], v5 offset:2336\nv_mfma_f32_16x16x32_f16 v[16:19], v[60:63], v[112:115], v[16:19]\n" \
  "ds_read_b64_tr_b16 v[124:125], v5 offset:2560\nds_read_b64_tr_b16 v[126:127], v5 offset:2592\ns_waitcnt lgkmcnt(12)\n" \
  "v_mfma_f32_16x16x32_f16 v[16:19], v[56:59], v[88:91], v[16:19]\nds_read_b64_tr_b16 v[72:73], v5 offset:2816\n" \
  "ds_read_b64_tr_b16 v[74:75], v5 offset:2848\nv_mfma_f32_16x16x32_f16 v[12:15], v[108:111], v[52:55], v[12:15]\ns_waitcnt lgkmcnt(10)\n" \
  "v_mfma_f32_16x16x32_f16 v[12:15], v[84:87], v[48:51], v[12:15]\nds_read_b64_tr_b16 v[96:97], v7 offset:0\n" \
  "ds_read_b64_tr_b16 v[98:99], v7 offset:64\nv_mfma_f32_16x16x32_f16 v[12:15], v[104:107], v[44:47], v[12:15]\n" \
  "ds_read_b64_tr_b16 v[68:69], v7 offset:512\nds_read_b64_tr_b16 v[70:71], v7 offset:576\n" \
  "v_mfma_f32_16x16x32_f16 v[16:19], v[104:107], v[52:55], v[16:19]\ns_waitcnt lgkmcnt(10)\n" \
  "v_mfma_f32_16x16x32_f16 v[12:15], v[80:83], v[40:43], v[12:15]\nds_read_b64_tr_b16 v[28:29], v7 offset:1024\n" \
  "ds_read_b64_tr_b16 v[30:31], v7 offset:1088\nv_mfma_f32_16x16x32_f16 v[16:19], v[80:83], v[48:51], v[16:19]\n" \
  "ds_read2_b32 v[64:65], v8 offset0:48 offset1:49\nds_read2_b32 v[66:67], v8 offset0:50 offset1:51\n" \
  "v_mfma_f32_16x16x32_f16 v[12:15], v[24:27], v[120:123], v[12:15]\nv_mfma_f32_16x16x32_f16 v[16:19], v[24:27], v[44:47], v[16:19]\n" \
  "s_waitcnt lgkmcnt(10)\nv_mfma_f32_16x16x32_f16 v[12:15], v[76:79], v[116:119], v[12:15]\nds_read_b64_tr_b16 v[92:93], v7 offset:1536\n" \
  "ds_read_b64_tr_b16 v[94:95], v7 offset:1600\nv_mfma_f32_16x16x32_f16 v[16:19], v[76:79], v[40:43], v[16:19]\n" \
  "v_mfma_f32_16x16x32_f16 v[16:19], v[124:127], v[120:123], v[16:19]\ns_waitcnt lgkmcnt(8)\n" \
  "v_mfma_f32_16x16x32_f16 v[16:19], v[72:75], v[116:119], v[16:19]\nv_mfma_f32_16x16x32_f16 v[12:15], v[96:99], v[32:35], v[12:15]\n" \
  "v_mfma_f32_16x16x32_f16 v[16:19], v[96:99], v[36:39], v[16:19]\ns_waitcnt lgkmcnt(4)\n" \
  "v_mfma_f32_16x16x32_f16 v[12:15], v[68:71], v[100:103], v[12:15]\nv_mfma_f32_16x16x32_f16 v[16:19], v[68:71], v[32:35], v[16:19]\n" \
  "s_waitcnt lgkmcnt(2)\nv_mfma_f32_16x16x32_f16 v[12:15], v[28:31], v[64:67], v[12:15]\n" \
  "v_mfma_f32_16x16x32_f16 v[16:19], v[28:31], v[100:103], v[16:19]\ns_waitcnt lgkmcnt(0)\n" \
  "v_mfma_f32_16x16x32_f16 v[16:19], v[92:95], v[64:67], v[16:19]\ns_nop 4\nv_cvt_pkrtz_f16_f32 v20, v12, v13\nv_cvt_pkrtz_f16_f32 v21, v14, v15\n" \
  "v_pk_add_u16 v20, v20, 4 op_sel_hi:[1,0] clamp\nv_pk_add_u16 v21, v21, 4 op_sel_hi:[1,0] clamp\nds_write_b64 v4, v[20:21] offset:6144\n" \
  "v_cvt_pkrtz_f16_f32 v20, v16, v17\nv_cvt_pkrtz_f16_f32 v21, v18, v19\nv_pk_add_u16 v20, v20, 4 op_sel_hi:[1,0] clamp\n" \
  "v_pk_add_u16 v21, v21, 4 op_sel_hi:[1,0] clamp\nds_write_b64 v4, v[20:21] offset:7168\ns_setprio 0\ns_setpc_b64 s[90:91]\n71:\n" \
  "s_movk_i32 s84, 0x4000\n73:\nds_read_b32 v23, v9 offset:32\ns_sub_u32 s84, s84, 1\ns_cbranch_scc1 72b\ns_waitcnt lgkmcnt(0)\n" \
  "v_readfirstlane_b32 s85, v23\ns_cmp_lt_u32 s85, 3\ns_cbranch_scc1 73b\ns_branch 72b\n"
#define CONV_CLOB "memory", "scc", "s84", "s85", "s90", "s91", "v12", "v13", "v14", "v15", "v16", "v17", "v18", "v19", "v20", "v21", "v22", "v23", "v24", "v25", "v26", "v27", "v28", "v29", "v30", "v31", "v32", "v33", "v34", "v35", "v36", "v37", "v38", "v39", "v40", "v41", "v42", "v43", "v44", "v45", "v46", "v47", "v48", "v49", "v50", "v51", "v52", "v53", "v54", "v55", "v56", "v57", "v58", "v59", "v60", "v61", "v62", "v63", "v64", "v65", "v66", "v67", "v68", "v69", "v70", "v71", "v72", "v73", "v74", "v75", "v76", "v77", "v78", "v79", "v80", "v81", "v82", "v83", "v84", "v85", "v86", "v87", "v88", "v89", "v90", "v91", "v92", "v93", "v94", "v95", "v96", "v97", "v98", "v99", "v100", "v101", "v102", "v103", "v104", "v105", "v106", "v107", "v108", "v109", "v110", "v111", "v112", "v113", "v114", "v115", "v116", "v117", "v118", "v119", "v120", "v121", "v122", "v123", "v124", "v125", "v126", "v127"
constexpr int TDP = 1216, T_3 = 4 * TDP, T_4 = T_3 + 512, TB = T_4 + 320;
constexpr int C_U = 480, UB = C_U + 4096 + 96, C_G5 = 0, C_G3 = 3200, C_G9 = 6400, C_G7 = C_G9 + 2096, GB = C_G7 + 2080 + 112;
constexpr int O_FL = 2 * TB, O_UL = O_FL + 64, O_IM = O_UL + 2 * UB, O_ST = O_IM + 2 * GB;
constexpr int X_TB = 10240, X_UL = X_TB + TB, X_IM = X_UL + UB, X_ST = X_IM + GB;
constexpr int X_WR = X_IM + 128 * IP;
#define X_RW(w) (LW + 9216 * (2 + ((w) >> 1)) + 2560 * ((w) & 1))
constexpr int RP = 1216;
constexpr int LW = 36864 + 512, LA = LW, LM = LW + 36864;
constexpr int L_SC = LM + 2048, SMEM = O_IM + 256 * IP;
static_assert(L_SC + 1152 <= SMEM && O_ST + 8192 <= SMEM && X_IM + 128 * IP <= LM && X_ST + 8192 <= LM && UB % 128 == 64 && GB % 128 == 64 && SMEM <= 79872, "");
struct TOps { uint32_t xa, xb, ya, pd, pf, pg, pe; };
__device__ __forceinline__ void table_ops(uint32_t im, uint32_t tb, int ct, int lane, TOps& t) {
  const int j = lane & 15, kq = lane >> 4, q4 = (lane >> 2) & 3, p4 = lane & 3, jj = j + 16 * ct;
  const uint32_t sa = im + (uint32_t)(IP * (lane >> 2));
  t.xa = sa + (uint32_t)(I_X + 8 * p4); t.xb = sa + (uint32_t)(I_X + 32 + 8 * (p4 & 1)); t.ya = sa + (uint32_t)(32 * ct + 8 * p4);
  t.pd = tb + (uint32_t)(2 * (541 - (8 * jj + 4 * (kq & 1) + 256 * (kq >> 1))));
  t.pf = tb + (uint32_t)((kq & 1) ? T_4 + 2 * (63 - jj) : T_3 + 2 * (127 - 2 * jj));
  t.pg = t.pf - ((kq & 1) ? 64 : 2);
  t.pe = tb + (uint32_t)(p4 * (TDP + 2) + ((lane < 12) ? 2 * (32 - q4) : 2 * (572 + (q4 & 1))));
  asm volatile("" : "+v"(t.xa), "+v"(t.xb), "+v"(t.ya), "+v"(t.pd), "+v"(t.pf), "+v"(t.pg), "+v"(t.pe));
}
typedef __attribute__((address_space(3))) char* lptr;
#define LP(T, a) ((__attribute__((address_space(3))) T*)(uintptr_t)(a))
__device__ __forceinline__ uint32_t table_tile(const TOps& t, int ct, int lane, uint32_t evp, float dsk TSP) {
  u32x4 ev;
  f32x4 ad[2], af[2];
  asm volatile(T_ASM : "=&v"(ad[0]), "=&v"(ad[1]), "=&v"(af[0]), "=&v"(af[1]), "=&v"(ev) : "v"(t.xa), "v"(t.xb), "v"(t.ya), "v"(evp) : T_CLOB);
  TS(0, 4);
  const int e0 = (int)(ev[0] + ev[2]), e1 = (int)(ev[1] + ev[3]);
  const int em = min(max(max(max(e0, e1), (int)((__float_as_uint(dsk) >> 23) & 0xffu) - 133), -110), 115);
  const int ts = ((uint32_t)(em + 12) <= 8u) ? 1 : -3 - em;
  float s0 = __builtin_amdgcn_ldexpf(1.f, e0 - 29 + ts), s1 = __builtin_amdgcn_ldexpf(1.f, e1 - 29 + ts);
  float d0 = (ct == 0 && lane == 0) ? __builtin_amdgcn_ldexpf(dsk, ts) : 0.f;
  asm volatile("s_nop 1" : "+v"(ad[0]), "+v"(ad[1]), "+v"(af[0]), "+v"(af[1]), "+v"(s0), "+v"(s1), "+v"(d0));
  const f32x4 k = ad[0] * s0 + (f32x4){d0, 0.f, 0.f, 0.f} + ad[1] * s1, f = af[0] * s0 + af[1] * s1;
  auto pk = [](float a, float b) { uint32_t r; asm("v_cvt_pk_f16_f32 %0, %1, %2" : "=v"(r) : "v"(a), "v"(b)); return r; };
  asm volatile("s_nop 0" : : "v"(k), "v"(f));
  const uint32_t p32 = pk(k[3], k[2]), p10 = pk(k[1], k[0]), p21 = pk(k[2], k[1]), p01 = pk(f[0], f[1]), p23 = pk(f[2], f[3]);
  *LP(u32x2, t.pd + 3 * TDP + 6) = (u32x2){p32, p10};
  *LP(uint32_t, t.pd + TDP + 2) = p32; *LP(uint32_t, t.pd + TDP + 6) = p10;
  *LP(uint32_t, t.pd + 2) = p21; *LP(uint32_t, t.pd + 2 * TDP + 6) = p21;
  *LP(u16, t.pd) = (u16)p32; *LP(u16, t.pd + 2 * TDP + 4) = (u16)p32;
  asm volatile("ds_write_b16_d16_hi %0, %1 offset:6\n\tds_write_b16_d16_hi %0, %1 offset:%2" : : "v"(t.pd), "v"(p10), "n"(2 * TDP + 10) : "memory");
  if (ct) {
    const uint32_t va = (uint32_t)__builtin_amdgcn_readlane((int)p32, 63), vb = (uint32_t)__builtin_amdgcn_readlane((int)p23, 15) >> 16;
    *LP(u16, t.pe) = (u16)((lane < 12) ? va : vb);
  }
  if (lane < 32) {
    *LP(u16, t.pf + 128) = (u16)p01; *LP(u16, t.pg + 128) = (u16)(p01 >> 16);
    *LP(u16, t.pf) = (u16)p23; *LP(u16, t.pg) = (u16)(p23 >> 16);
  }
  return (uint32_t)(127 + 5 - ts) << 23;
}
__device__ __forceinline__ uint32_t hadd(uint32_t a, uint32_t b) { return __builtin_bit_cast(uint32_t, __builtin_bit_cast(f16x2, a) + __builtin_bit_cast(f16x2, b)); }
template <bool FULL> __device__ __forceinline__ void sums(char* sm, const u32x4 p, const u32x4 c) {
  const uint32_t D[8] = {p[0], p[1], p[2], p[3], c[0], c[1], c[2], c[3]};
  uint32_t O[7];
#pragma unroll
  for (int j = 0; j < 7; ++j) O[j] = __builtin_amdgcn_alignbit(D[j + 1], D[j], 16);
  u32x4 a, b;
#pragma unroll
  for (int j = 0; j < 4; ++j) a[j] = hadd(D[j + 2], hadd(O[j + 1], O[j + 2]));
  *(u32x4*)(sm + C_G3 + 32) = a;
#pragma unroll
  for (int j = 0; j < 4; ++j) b[j] = hadd(a[j], hadd(D[j + 1], D[j + 3]));
  *(u32x4*)(sm + C_G5 + 32) = b;
  if (FULL) {
#pragma unroll
    for (int j = 0; j < 4; ++j) a[j] = hadd(b[j], hadd(O[j], O[j + 3]));
    *(u32x4*)(sm + C_G7 + 32) = a;
#pragma unroll
    for (int j = 0; j < 4; ++j) b[j] = hadd(a[j], hadd(D[j], D[j + 4]));
    *(u32x4*)(sm + C_G9 + 32) = b;
  }
}
__device__ __forceinline__ void count_up(uint32_t a) {
  uint32_t t;
  asm volatile("s_mov_b64 exec, 1\n\tv_mov_b32 %0, 1\n\tds_add_u32 %1, %0\n\ts_mov_b64 exec, -1" : "=&v"(t) : "v"(a) : "memory");
}
__device__ __forceinline__ int pad_at(int r) { return 16 * (r & 1) + 3200 * (r >> 1) - ((r >= 6) ? 3200 - 2096 : 0); }
struct COps { uint32_t b, a, o, g3, k3, g4, k4, f; };
__device__ __forceinline__ void conv_ops(uint32_t tb, uint32_t ub, uint32_t cb, int lane, COps& e) {
  const uint32_t j = lane & 15, kq = lane >> 4, q4 = (lane >> 2) & 3, p4 = lane & 3;
  e.b = ub + 32u * j + 16u * kq;
  e.a = tb + p4 * TDP + 32u - 8u * q4 + 16u * kq;
  e.g3 = cb + C_G5 + 8 + 64u * kq + 8u * (q4 + p4) + (q4 & 1u) * 3200u;
  e.g4 = cb + C_G9 + 8 + 128u * kq + 16u * q4 + 8u * p4 + (q4 & 1u) * 2096u;
  e.k3 = tb + T_3 + 120 - 8u * j + 16u * kq;
  e.k4 = tb + T_4 + 60 - 4u * j + 16u * kq;
  asm volatile("" : "+v"(e.b), "+v"(e.a), "+v"(e.g3), "+v"(e.g4), "+v"(e.k3), "+v"(e.k4));
}
#define CONV_RUN(prog, e, ...) do { int prog_ = (prog); \
    asm volatile("s_call_b64 s[90:91], kf_conv" : "+{s89}"(prog_), "+{v2}"(e.b), "+{v3}"(e.a), "+{v4}"(e.o), "+{v5}"(e.g3), "+{v6}"(e.k3), "+{v7}"(e.g4), \
                 "+{v8}"(e.k4), "+{v9}"(e.f) __VA_ARGS__ : : CONV_CLOB); } while (0)

template <bool E>
__device__ __forceinline__ void ln_stats(const uint32_t* v, int lane, float sc, f32x2* t, f32x4 z, f32x2& rc, f32x2& nc, float& e) {
  float s1a = z[0], s2a = z[1], s1b = z[2], s2b = z[3];
#pragma unroll
  for (int k = 0; k < 4; ++k) {
    s1a = dot2(v[k], 0x3f803f80u, s1a); s2a = dot2(v[k], v[k], s2a);
    s1b = dot2(v[4 + k], 0x3f803f80u, s1b); s2b = dot2(v[4 + k], v[4 + k], s2b);
  }
#pragma unroll
  for (int k = 0; k < 9; ++k) t[k] = (f32x2){bflo(v[k]), bfhi(v[k])};
  const float rs = row_sum(fold16(dot2(v[8], 0x3f803f80u, fold32(s1a, s1b)), dot2(v[8], v[8], fold32(s2a, s2b))));
  if (E) { e = row_sum(e); e += __int_as_float(__builtin_amdgcn_update_dpp(0, __float_as_int(e), 0x142, 0xa, 0xf, false)); }
  const f32x2 mean = (f32x2){rdl(rs, 15), rdl(rs, 47)} * (1.f / D_), m2 = mean * mean;
  f32x2 var = (f32x2){rdl(rs, 31), rdl(rs, 63)} * (1.f / D_) - m2;
  asm volatile("" : "+v"(t[0]), "+v"(t[1]), "+v"(t[2]), "+v"(t[3]), "+v"(t[4]), "+v"(t[5]), "+v"(t[6]), "+v"(t[7]), "+v"(t[8]));
  if (__builtin_expect(!(m2[0] <= 16.f * var[0] && m2[1] <= 16.f * var[1]), 0)) {
    float q[3] = {0.f, 0.f, 0.f};
#pragma unroll
    for (int k = 0; k < 9; ++k) {
      const f32x2 a = t[k] - ((k < 4 || (k == 8 && lane < 32)) ? mean[0] : mean[1]);
      q[k >> 2] += a[0] * a[0] + a[1] * a[1];
    }
    float qa = q[0] + ((lane < 32) ? q[2] : 0.f), qb = q[1] + ((lane < 32) ? 0.f : q[2]);
    wave_sum2(qa, qb);
    var = (f32x2){qa, qb} * (1.f / D_);
  }
  const uint32_t mp = pack2(mean[0], mean[1]), vp = pack2(var[0], var[1]);
  const f32x2 ve = (f32x2){bflo(vp), bfhi(vp)} + 1e-5f;
  const uint32_t e2 = pack2(ve[0], ve[1]);
  const uint32_t sp = pack2(__builtin_amdgcn_sqrtf(bflo(e2)), __builtin_amdgcn_sqrtf(bfhi(e2)));
  rc = (f32x2){__builtin_amdgcn_rcpf(bflo(sp)), __builtin_amdgcn_rcpf(bfhi(sp))} * sc;
  nc = -(f32x2){bflo(mp), bfhi(mp)} * rc;
}
template <bool H>
__device__ __forceinline__ void ln_apply(const f32x2* t, int lane, f32x2 rc, f32x2 nc, uint32_t* c) {
  const float nm[3] = {nc[0], nc[1], (lane < 32) ? nc[0] : nc[1]}, rr[3] = {rc[0], rc[1], (lane < 32) ? rc[0] : rc[1]};
  f32x2 dd[9];
#pragma unroll
  for (int k = 0; k < 9; ++k) dd[k] = __builtin_elementwise_fma(t[k], (f32x2){rr[k >> 2], rr[k >> 2]}, (f32x2){nm[k >> 2], nm[k >> 2]});
  asm volatile("" : "+v"(dd[0]), "+v"(dd[1]), "+v"(dd[2]), "+v"(dd[3]), "+v"(dd[4]), "+v"(dd[5]), "+v"(dd[6]), "+v"(dd[7]), "+v"(dd[8]));
#pragma unroll
  for (int k = 0; k < 9; ++k) {
    const f32x2 d = dd[k];
    typedef __attribute__((ext_vector_type(2))) uint16_t u16x2;
    if (H) c[k] = __builtin_bit_cast(uint32_t, __builtin_elementwise_add_sat(__builtin_bit_cast(u16x2, __builtin_amdgcn_cvt_pkrtz(d[0], d[1])), (u16x2){4, 4}));
    else c[k] = pack2(d[0], d[1]);
  }
}
__device__ __forceinline__ uint32_t med3u(uint32_t a, uint32_t b, uint32_t c) { return max(min(a, b), min(max(a, b), c)); }
__device__ __forceinline__ uint32_t oct_maxu(uint32_t v) {
  v = max(v, (uint32_t)DPP_I((int)v, 0xb1, 0xf));
  v = max(v, (uint32_t)DPP_I((int)v, 0x4e, 0xf));
  v = max(v, (uint32_t)DPP_I((int)v, 0x141, 0xf));
  return v;
}

#define KARGS const u16* __restrict__ x, const u16* __restrict__ Are, const u16* __restrict__ Aim, const u16* __restrict__ Bre, \
    const u16* __restrict__ Bim, const u16* __restrict__ Cre, const u16* __restrict__ Cim, const u16* __restrict__ logdt, \
    const u16* __restrict__ Dskip, char* __restrict__ ws, const u16* __restrict__ wr, const u16* __restrict__ wg, \
    const u16* __restrict__ wu, const u16* __restrict__ wd, u16* __restrict__ out
#define KPASS x, Are, Aim, Bre, Bim, Cre, Cim, logdt, Dskip, ws, wr, wg, wu, wd, out
template <int ROLE, int FFN = -1>
__device__ __forceinline__ void role_body(char* smem, KARGS) {
  const int tid = threadIdx.x, lane = tid & 63;
  const int w = __builtin_amdgcn_readfirstlane(tid >> 6), wl = w & 3;
  const int b = blockIdx.x & 255, bx = b & 7, bj = b >> 3;
  constexpr bool isl = ROLE == 1 || ROLE == 2;
  const int tg = isl ? 32 * (bj >> 2) + 4 * bx + (bj & 3) : 32 * bx + bj;
  u32x4 xa, xc;
  uint32_t ep, r0;
#define P_WAIT(n, ...) asm volatile("s_waitcnt vmcnt(" #n ")\n\tv_lshlrev_b32 %0, 16, v100\n\tv_lshlrev_b32 %1, 16, v101\n\tv_lshlrev_b32 %2, 16, v102" \
    : "=v"(ldt), "=v"(are), "=v"(aim) : : "v100", "v101", "v102" __VA_ARGS__)
#define P_MORE() asm volatile("v_lshlrev_b32 %0, 16, v103\n\tv_lshlrev_b32 %1, 16, v104\n\tv_lshlrev_b32 %2, 16, v105\n\tv_lshlrev_b32 %3, 16, v106" \
    : "=v"(bre), "=v"(bim), "=v"(cre), "=v"(cim) : : "v103", "v104", "v105", "v106")
  TSD;
  TS(0, 0);
  const uint32_t lb0 = (uint32_t)(uintptr_t)(__attribute__((address_space(3))) char*)smem;
  const int i = lane & 15, kq = lane >> 4;
  const u32x4 ones = {0xffffffffu, 0xffffffffu, 0xffffffffu, 0xffffffffu}, zero4 = {0u, 0u, 0u, 0u};
  auto u_again = [&](u32x4& uw, const u32x4& ut, uint32_t uo) {
#pragma unroll 1
    for (int spin = 0; spin < SPIN_CAP; ++spin) {
      const bool na = ((uw[0] | uw[1] | uw[2] | uw[3]) & UMASK) != 0u;
      if (__builtin_expect(!__any(na), 1)) break;
      TSC(19);
      SLEEP(USLEEP);
      if (na) { LD16W(uw, uo); uw ^= ut; }
    }
  };
  constexpr uint32_t S16 = 0x41800000u, LMASK = 0x00070007u;
  auto v_put = [&](u32x4 v, const u32x4& ut, bool other, uint32_t vo) {
    const bool second = __any(other || ((v[0] | v[1] | v[2] | v[3]) & 0x40004000u) != 0u);
    if (__builtin_expect(second, 0)) v = (v & ~LMASK) | (~ut & LMASK); else v = (v & ~LMASK) | ut;
    ST16(vo, v);
    return second;
  };
  auto v_end = [&](uint32_t T, int n) { DRAIN(); if (lane == 0) ST4(O_DN + 16u * n, T); };
  if constexpr (!isl) {
    const int ch = wl >> 1, tl = tid & 255;
    int* evp = (int*)(smem + O_FL + 16 * ch);
    float ldt, are, aim, dsk;
    if constexpr (ROLE == 0) {
      __builtin_amdgcn_s_setprio(KPRIO);
      COps e;
      conv_ops(lb0 + (uint32_t)(ch * TB), lb0 + (uint32_t)(O_UL + ch * UB), lb0 + (uint32_t)(O_IM + ch * GB), lane, e);
      e.o = lb0 + O_ST + (uint32_t)(16 * ((4 * i + 2 * (kq >> 1) + ch) ^ (i >> 1)) + 8 * (kq & 1));
      e.f = lb0 + O_FL;
      TOps t;
      table_ops(lb0 + (uint32_t)(O_IM + 128 * IP * ch), lb0 + (uint32_t)(TB * ch), wl & 1, lane, t);
      uint32_t pz = lb0 + O_IM + (uint32_t)(((lane >> 3) & 1) * GB + pad_at(lane & 7)), im = lb0 + (uint32_t)(O_IM + IP * tl);
      asm volatile("" : "+v"(e.o), "+v"(e.f), "+v"(pz), "+v"(im));
      float bre, bim, cre, cim;
      P_WAIT(0, , "v103", "v104", "v105", "v106", "v107", "v109");
      asm volatile("" : "={v109}"(ep));
      const uint32_t T = ((uint32_t)__builtin_amdgcn_readfirstlane(ep) + 1u) * 0x9E3779B1u;
      if (w == 3 && lane < 8) ST16(O_KEY + 128u * b + 16u * lane, ones);
      P_MORE();
      asm volatile("v_lshlrev_b32 %0, 16, v107" : "=v"(dsk) : : "v107");
      TS(0, 1);
      evp[wl & 1] = gen_y(ldt, are, aim, bre, bim, cre, cim, im, [] {} TSA);
      TS(0, 8);
      BAR();
      TS(0, 2);
      const uint32_t cv = table_tile(t, wl & 1, lane, lb0 + O_FL + 16 * ch, dsk TSA), cs = (uint32_t)__builtin_amdgcn_readfirstlane(cv);
      *(uint32_t*)(smem + O_FL + 40 + 4 * ch) = cv;
      TS(0, 9);
      gsync0(lb0 + O_FL + 36, 4);
      TS(0, 3);
      *(u32x4*)(smem + O_ST + 16 * tl) = ones;
      *(u32x4*)(smem + O_ST + 4096 + 16 * tl) = ones;
      if (w == 0) *LP(u32x4, pz) = zero4;
      DRAIN();
      CONV_RUN(wl & 1, e);
      if (wl & 1) asm volatile("s_mov_b64 exec, 1\n\tglobal_store_dwordx2 %0, %1, %2 sc1" : : "v"(O_SC + 16u * (2 * tg + ch)), "v"((u32x2){T, cs}), "s"(ws) : "memory");
      TS(0, 5);
    } else {
      __builtin_amdgcn_s_setprio(UPRIO);
      const uint32_t ck0 = (uint32_t)__builtin_amdgcn_s_memtime(), rt0 = (uint32_t)__builtin_amdgcn_s_memrealtime();
#pragma unroll
      for (int k = 0; k < 3; ++k) *(u32x4*)(smem + ((16 * (tl + 256 * k) < O_FL) ? 16 * (tl + 256 * k) : O_FL + 32)) = zero4;
      if (w == 7) *(u32x4*)(smem + O_UL + (lane >> 5) * UB + 16 * min(lane & 31, 29)) = zero4;
      uint32_t uo = u_at(tl >> 1, 2 * tg + (tid & 1)), go = O_SU + (uint32_t)(256 * bx + 32 * wl + (lane & 31) + 4 * (lane & 32)) * 128u;
      uint32_t ul = (uint32_t)(O_UL + (tid & 1) * UB + C_U - 16 + 16 * (tl >> 1)), ug = (uint32_t)(O_IM + (tid & 1) * GB + 16 * (tl >> 1)), so = lb0 + O_ST + 16u * (uint32_t)(lane ^ (lane >> 3));
      uint32_t vo = O_V + (uint32_t)tg * VP + 16u * lane;
      asm volatile("" : "+v"(uo), "+v"(go), "+v"(ul), "+v"(ug), "+v"(so), "+v"(vo));
      P_WAIT(1, , "v109");
      TS(0, 1);
      evp[2 + (wl & 1)] = gen_x(ldt, are, aim, smem + O_IM + IP * tl);
      asm volatile("s_waitcnt vmcnt(0)" : "={v109}"(ep));
      const uint32_t T = ((uint32_t)__builtin_amdgcn_readfirstlane(ep) + 1u) * 0x9E3779B1u;
      TS(0, 8);
      BAR();
      TS(0, 2);
      if (w == 7 && bj >= 24) {
        const uint32_t t_ = ahead(ck0, rt0, VDB);
        ST16(O_SB + (uint32_t)(64 * (lane >> 3) + 8 * bx + bj - 24) * 128u + 16u * (lane & 7), ((u32x4){T, t_, T, t_}));
      }
      u32x4 ut = {utag(T, 0), utag(T, 1), utag(T, 2), utag(T, 3)};
      asm volatile("" : "+v"(ut));
      SLEEP(GSLEEP);
      u32x2 pv;
      pv[1] = gate(ws, go, T);
      TS(0, 3);
      until(latest(pv[1]) + UD);
      TS(0, 7);
      u32x4 uw, ux;
      asm volatile("global_load_dwordx4 %0, %2, %4 sc1\n\tglobal_load_dwordx4 %1, %3, %4 sc1"
                   : "=&v"(uw), "=&v"(ux) : "v"(uo), "v"(uo + 64 * UP * 32), "s"(ws) : "memory");
      __builtin_amdgcn_s_setprio(FPR);
      asm volatile("s_waitcnt vmcnt(1)" : "+v"(uw));
      uw ^= ut;
      *(u32x4*)(smem + ul + 16) = uw;
      asm volatile("s_waitcnt vmcnt(0)" : "+v"(ux));
      TS(0, 11);
      ux ^= ut;
      *(u32x4*)(smem + ul + 2048 + 16) = ux;
      if (__builtin_expect(__any(((uw[0] | uw[1] | uw[2] | uw[3] | ux[0] | ux[1] | ux[2] | ux[3]) & UMASK) != 0u), 0)) {
        u_again(uw, ut, uo);
        u_again(ux, ut, uo + 64 * UP * 32);
        *(u32x4*)(smem + ul + 16) = uw;
        *(u32x4*)(smem + ul + 2048 + 16) = ux;
      }
      BAR();
      TS(0, 4);
      const u32x4 pw = *(const u32x4*)(smem + ul);
      if (wl < 2) {
        const u32x4 px = *(const u32x4*)(smem + ul + 2048);
        sums<true>(smem + ug, pw, uw);
        sums<false>(smem + ug + 2048, px, ux);
      } else sums<true>(smem + ug, pw, uw);
      count_up(lb0 + O_FL + 32);
      __builtin_amdgcn_s_setprio(SPR);
      TS(0, 5);
      const bool other = *(const uint32_t*)(smem + O_FL + 40 + 4 * (lane & 1)) != S16;
      bool second = false;
#pragma unroll
      for (int k = 0; k < 2; ++k) {
        const uint32_t o = ((k ? 0x3247u : 0x1065u) >> (4 * wl) & 7u) * 1024u;
        u32x4 v;
#pragma unroll 1
        for (int spin = 0; spin < SPIN_CAP; ++spin) {
          asm volatile("ds_read_b128 %0, %1\n\ts_waitcnt lgkmcnt(0)" : "=&v"(v) : "v"(so + o) : "memory");
          if (!__any(max4(v) == 0xffffffffu)) break;
          __builtin_amdgcn_s_sleep(VPS);
        }
        TS(0, 12 + k);
        second |= v_put(v, ut, other, vo + o);
        TS(0, 10 - 4 * k);
      }
      if (__builtin_expect(second, 0)) v_end(T, 4 * tg + wl);
    }
    TSE();
  } else {
  uint32_t* cand = (uint32_t*)(smem + LM);
  uint32_t* sel = cand + 128;
  uint32_t* kl = sel + 48;
  uint32_t* cnt = sel + 64;
  const uint32_t ca = lb0 + LM + 4 * (128 + 64);
  u16* hm = (u16*)(cnt + 16);
  uint32_t T;
  const bool ffn = (FFN < 0) ? bj < 18 : FFN, isx = FFN != 1 && bj >= 24;
  const int fb = 18 * bx + bj;
  constexpr bool ln2 = ROLE == 2;
  const int g0 = tg, g2 = tg;
  u32x4 bw[18];
#pragma unroll
  for (int s = 0; s < 9; ++s) bw[s] = (u32x4){0u, 0u, 0u, 0u};
  int tid2 = threadIdx.x;
  uint32_t tv = 0u, tk = 0u;
  auto v_look = [&](uint32_t tu, int lane) {
    uint32_t tm = latest(gate(ws, O_SB + (uint32_t)(64 * bx + lane) * 128u, T)) - V2D;
    if ((int)(tm - (tu + UD + VD2)) < 0) tm = tu + UD + VD2;
    cnt[8] = tm;
    count_up(ca + 52);
  };
  auto v_time = [&] { gwait(ca + 52, 1); tv = (uint32_t)__builtin_amdgcn_readfirstlane(cnt[8]); };
  uint32_t ts1 = 0u;
  auto rest = [&] {
    if (ts1 && (int)((uint32_t)__builtin_amdgcn_s_memrealtime() - ts1) > 0) {
      v_time();
      ts1 = (tv - TSTOP) | 1u;
      if ((int)((uint32_t)__builtin_amdgcn_s_memrealtime() - ts1) > 0) { TS(0, 8); until(tv + RD); ts1 = 0u; }
    }
  };
  auto gu_dma = [&](int q0, int q1, auto depth) {
    const char* gs = (const char*)((w < 2) ? wg : wu) + (size_t)((144 * (w & 1)) * (F_ * 2) + fb * 64);
    const uint32_t gv = (uint32_t)((lane >> 2) * (F_ * 2) + 16 * ((lane & 3) ^ (lane >> 5) ^ (w & 2)));
#pragma unroll 1
    for (int q = q0; q < q1; ++q) {
      const int k = (int)((((bj & 1) ? ORD1 : ((unsigned __int128)0x876543210u << 36 | 0x876543210ull)) >> (4 * (q - (q >= 9 ? 9 : 0)) + (q >= 9 ? 36 : 0))) & 15u), h = q >= 9;
      if (q >= RQ) rest();
      dma16(gs + (size_t)(288 * h + 16 * k) * (F_ * 2), gv, lb0 + LW * h + 9216 * w + 1024 * k);
      if (!BALL || q < RQ || ts1) asm volatile("s_waitcnt vmcnt(%0)" : : "n"(depth.value) : "memory");
    }
  };
  auto down_dma = [&](int k0, int k1, int lane) {
#pragma unroll 1
    for (int k = k0; k < k1; ++k) {
      const int o = 9216 * wl + 2304 * k;
      const char* ds = (const char*)wd + (size_t)(fb * 36864 + o);
      const uint32_t dl = lb0 + o + 32 * ((4 * wl + k + 1) >> 1);
      if (ROLE == 1) rest();
      dma16d(ds, 16u * lane, dl);
      dma16d(ds + 1024, 16u * lane, dl + 1024);
      uint32_t keep;
      asm volatile("s_mov_b32 %0, m0\n\ts_mov_b32 m0, %3\n\ts_mov_b64 exec, 0xffff\n\tglobal_load_lds_dwordx4 %1, %2" DPOL2 "\n\ts_mov_b64 exec, -1\n\ts_mov_b32 m0, %0\n\ts_waitcnt vmcnt(%4)"
                   : "=&s"(keep) : "v"(16u * lane), "s"(ds + 2048), "s"(dl + 2048), "n"(ROLE == 1 ? DDEPTH : 40) : "memory");
    }
  };
  auto gu_read = [&](int h) {
    const uint32_t ra = lb0 + LW * h + (uint32_t)(((lane >> 1) & 1) * 18432 + (8 * kq + ((lane >> 2) & 3)) * 64 + ((w ^ (kq & 1) ^ (lane & 2)) * 16) + 8 * (lane & 1));
#pragma unroll
    for (int s = 0; s < 9; ++s) {
      const u32x2 h0 = trd(ra + 2048 * s), h1 = trd(ra + 2048 * s + 256);
      bw[9 * h + s] = (u32x4){h0[0], h0[1], h1[0], h1[1]};
    }
#pragma unroll
    for (int s = 0; s < 9; ++s) asm volatile("" : "+v"(bw[9 * h + s]));
  };
  const uint32_t vearly = (uint32_t)((VEARLY >> (8 * (g0 >> 5))) & 0xffull);
#define TOUCH(pa) asm volatile("global_load_dword v126, %0, off" DPOL4 : : "v"(pa) : "memory", "v126")
  uint32_t wv[5], xv[9];
  auto rows_xw = [&](int lane) {
    u32x4 wa, xr, xs;
    const uint32_t xo = (uint32_t)((8 * g2 + 2 * wl) * (D_ * 2));
    asm volatile("global_load_dwordx4 %0, %5, %9\n\tglobal_load_dword %1, %6, %9 offset:1024\n\t"
                 "global_load_dwordx4 %2, %7, %10\n\tglobal_load_dwordx4 %3, %7, %10 offset:1152\n\tglobal_load_dword %4, %8, %10 offset:1024\n\ts_waitcnt vmcnt(0)"
                 : "=&v"(wa), "=&v"(wv[4]), "=&v"(xr), "=&v"(xs), "=&v"(xv[8])
                 : "v"(16u * lane), "v"(4u * (lane & 31)), "v"(xo + 16u * lane), "v"(xo + 4u * lane + 32u * (lane & 32)), "s"(wr), "s"(x) : "memory");
#pragma unroll
    for (int k = 0; k < 4; ++k) { wv[k] = wa[k]; xv[k] = xr[k]; xv[4 + k] = xs[k]; }
  };
  auto ln1 = [&](int g, int o, uint32_t c0) {
    uint32_t uo = u_at(g, 64 * wl + lane);
    uint32_t ta = lb0 + (uint32_t)(o + ((lane >> 2) & 3) * RP + (64 * wl + 16 * kq + 4 * (lane & 3)) * 2);
    uint32_t la = (uint32_t)(o + 2 * wl * RP + 16 * lane), lt = (uint32_t)(o + (2 * wl + (lane >> 5)) * RP + 1024 + 4 * (lane & 31));
    uint32_t sg = O_SU + (uint32_t)(256 * (lane >> 3) + g) * 128u + 16u * (lane & 7);
    asm volatile("" : "+v"(uo), "+v"(ta), "+v"(la), "+v"(lt), "+v"(sg));
    f32x4 z4 = {0.f, 0.f, 0.f, 0.f};
    asm volatile("" : "+v"(z4));
    asm volatile("s_waitcnt vmcnt(0)" : "={v[100:103]}"(xa), "={v[104:107]}"(xc), "={v108}"(r0), "={v109}"(ep));
    TS(0, 1);
    const uint32_t v[9] = {xa[0], xa[1], xa[2], xa[3], xc[0], xc[1], xc[2], xc[3], r0};
    uint32_t c[9];
    f32x2 tf[9];
    T = ((uint32_t)__builtin_amdgcn_readfirstlane(ep) + 1u) * 0x9E3779B1u;
    if (wl == 3) { const uint32_t now = (uint32_t)__builtin_amdgcn_s_memrealtime() + SD; ST16(sg, ((u32x4){T, now, T, now})); }
    const uint32_t tj = ((T >> (3 * wl)) & 0x00070007u) | (((T >> (12 + wl)) & 0x00010001u) << 14);
    uint32_t tw[3] = {(tj & 0xffffu) * 0x10001u, (tj >> 16) * 0x10001u, 0u};
    tw[2] = (lane < 32) ? tw[0] : tw[1];
    asm volatile("" : "+v"(tw[0]), "+v"(tw[1]), "+v"(tw[2]));
    f32x2 rc, nc;
    float e_;
    ln_stats<false>(v, lane, 0.03125f, tf, z4, rc, nc, e_);
    ln_apply<true>(tf, lane, rc, nc, c);
#pragma unroll
    for (int k = 0; k < 9; ++k) c[k] = (c[k] & ~UMASK) | tw[k >> 2];
    *(u32x4*)(smem + la) = (u32x4){c[0], c[1], c[2], c[3]};
    *(u32x4*)(smem + la + RP) = (u32x4){c[4], c[5], c[6], c[7]};
    *(uint32_t*)(smem + lt) = c[8];
    u32x4 ha, hb;
    {
      uint32_t t, cc, n = SPIN_CAP;
      asm volatile("s_mov_b64 exec, 1\n\tv_mov_b32 %2, 1\n\tds_add_u32 %5, %2\n\ts_mov_b64 exec, -1\n"
                   "1:\n\tds_read_b32 %2, %5\n\tds_read_b64_tr_b16 v[40:41], %6\n\tds_read_b64_tr_b16 v[42:43], %6 offset:%7\n\tds_read_b64_tr_b16 v[44:45], %6 offset:512\n\t"
                   "ds_read_b64_tr_b16 v[46:47], %6 offset:%8\n\ts_sub_u32 %4, %4, 1\n\ts_cbranch_scc1 2f\n\ts_waitcnt lgkmcnt(4)\n\tv_readfirstlane_b32 %3, %2\n\t"
                   "s_cmp_lt_u32 %3, 4\n\ts_cbranch_scc1 1b\n2:\n\ts_waitcnt lgkmcnt(0)"
                   : "=&{v[40:43]}"(ha), "=&{v[44:47]}"(hb), "=&v"(t), "=&s"(cc), "+s"(n) : "v"(c0), "v"(ta), "n"(4 * RP), "n"(512 + 4 * RP) : "memory", "scc");
    }
    ST16(uo, ha);
    ST16(uo + 64 * 128, hb);
    if (wl == 0) {
      const u32x2 h4 = trd(ta + 1024), h5 = trd(ta + 1024 + 4 * RP);
      ST16(uo + 128 * 128, ((u32x4){h4[0], h4[1], h5[0], h5[1]}));
    }
  };
  const int dX = 512 - 24 + 8 * bx + bj;
  if constexpr (ROLE == 2) {
    const int tl = tid - 256;
    if (isx) {
      __builtin_amdgcn_s_setprio(QPRIO0);
      *(u32x4*)(smem + X_TB + 16 * tl) = zero4;
      *(u32x4*)(smem + X_TB + 16 * min(256 + tl, (TB + C_U) / 16 - 1)) = zero4;
      const int pr = (wl + XROT) & 3;
      COps e;
      conv_ops(lb0 + X_TB, lb0 + X_UL, lb0 + X_IM, lane, e);
      TOps t;
      table_ops(lb0 + X_IM, lb0 + X_TB, wl & 1, lane, t);
      e.o = lb0 + X_ST + (uint32_t)(16 * ((2 * i + (kq >> 1)) ^ ((i >> 2) & 1)) + 8 * (kq & 1));
      e.f = ca + 28 - 32;
      asm volatile("" : "+v"(e.o), "+v"(e.f));
      float ldt, are, aim, bre, bim, cre, cim, dsk;
      P_WAIT(0, , "v103", "v104", "v105", "v106", "v107", "v109");
      asm volatile("" : "={v109}"(ep));
      T = ((uint32_t)__builtin_amdgcn_readfirstlane(ep) + 1u) * 0x9E3779B1u;
      P_MORE();
      asm volatile("v_lshlrev_b32 %0, 16, v107" : "=v"(dsk) : : "v107");
      TS(0, 1);
      __builtin_amdgcn_s_setprio(QPRIO);
      int* evp = (int*)(sel + 44);
      char* im = smem + X_IM + IP * (tl & 127);
      evp[wl] = (wl < 2) ? gen_y(ldt, are, aim, bre, bim, cre, cim, lb0 + X_IM + IP * tl, [] {} TSA) : gen_x(ldt, are, aim, im);
      TS(0, 2);
      __builtin_amdgcn_s_setprio(QPRIO2);
      gsync(ca + 8, 4);
      if (wl < 2) evp[-4 + wl] = (int)table_tile(t, wl & 1, lane, lb0 + LM + 4 * (128 + 44), dsk TSA);
      if (wl < 2) gsync0(ca + 8, 8); else { count_up(ca + 8); SLEEP(XSL); gwait(ca + 8, 8); }
      const uint32_t cs = (uint32_t)__builtin_amdgcn_readfirstlane(evp[-4]);
      if (wl == 3) *(u32x4*)(smem + X_IM + pad_at(lane & 7)) = zero4;
      TS(0, 3);
      CONV_RUN(2 + pr, e, , "+{v11}"(tid2));
      __builtin_amdgcn_s_setprio(3);
      {
        const int lane = tid2 & 63;
        const u32x4 v = *(const u32x4*)(smem + X_ST + 2048 * pr + 1024 * (lane >> 5) + 16 * ((lane & 31) ^ ((lane >> 3) & 1)));
        if (__builtin_expect(v_put(v, (u32x4){utag(T, 0), utag(T, 1), utag(T, 2), utag(T, 3)}, cs != S16, v_at(dX, 64 * pr + lane)), 0)) {
          DRAIN();
          if (lane == 0) ST4(O_DN + 16u * (4 * (dX - 256) + pr), T);
        }
        if (pr == 0 && lane == 0) asm volatile("global_store_dwordx2 %0, %1, %2 sc1" : : "v"(O_SC + 16u * dX), "v"((u32x2){T, cs}), "s"(ws) : "memory");
      }
      TS(0, 6);
    }
    if (!isx) __builtin_amdgcn_s_setprio(PPRIO);
    asm volatile("" : "+v"(tid2));
    if (!isx) rows_xw(tid2 & 63);
    if (!isx && !ffn) {
      SLEEP(RSLEEP);
#pragma unroll
      for (int k = 0; k < 7; ++k) ST16(O_SL + (uint32_t)((6 * bx + bj - 18) * 1792 + 256 * k - 256 + tid2) * 16u, ones);
      DRAIN();
    }
    if (!isx) BAR();
    const uint32_t tp = (uint32_t)__builtin_amdgcn_readfirstlane(cnt[4]);
    if (!isx) T = (uint32_t)__builtin_amdgcn_readfirstlane(sel[43]);
    if (!isx && w == 4) v_look(tp, tid2 & 63);
    v_time();
  } else {
    __builtin_amdgcn_s_setprio(3);
    if (tid < 16) cnt[tid] = 0u;
    BAR();
    if (tid < 20) ((uint32_t*)(hm + 320))[tid] = 0u;
    if (tid >= 184) *(u32x4*)(smem + L_SC - 16 * 184 + 16 * tid) = (u32x4){0x41804180u, 0x41804180u, 0x41804180u, 0x41804180u};
    uint32_t sink = 0;
    ln1(tg, 0, ca + 12);
    TS(0, 2);
    __builtin_amdgcn_s_setprio(NPRIO);
    asm volatile("s_waitcnt vmcnt(0)" : "+v"(sink) : : "memory");
    TS(0, 13);
    if (w == 0) {
      const uint32_t xr = O_XC + (uint32_t)((1021 - 4 * g0) * 72 + lane) * 16u;
      ST16(xr, ones);
      ST16(xr + 1024u, ones);
      ST16(xr + 2048u, ones);
      if (lane < 24) ST16(xr + 3072u, ones);
    }
    if (w == 2 && b < 72 && lane < 8) ST16(O_FB + (uint32_t)(8 * b + lane) * 16u, ones);
    if (isx) {
      rows_xw(lane);
      char* sp = smem + X_RW(w) + 16 * lane;
      *(u32x4*)sp = (u32x4){xv[0], xv[1], xv[2], xv[3]};
      *(u32x4*)(sp + 1024) = (u32x4){xv[4], xv[5], xv[6], xv[7]};
      *(u32x2*)(smem + X_RW(w) + 2048 + 8 * lane) = (u32x2){xv[8], wv[4]};
      if (w == 0) *(u32x4*)(smem + X_WR + 16 * lane) = (u32x4){wv[0], wv[1], wv[2], wv[3]};
    }
    SLEEP(ESLEEP);
    uint32_t tu = latest(gate(ws, O_SU + (uint32_t)(256 * bx + (ALLP ? tid : 64 * w + 4 * i + (bj & 3))) * 128u, T));
    asm volatile("s_mov_b64 exec, 1\n\tds_max_u32 %0, %1\n\ts_mov_b64 exec, -1" : : "v"(ca + 16), "v"(tu) : "memory");
    if (w == 0) sel[43] = T;
    if (isx) {
      const uint32_t xo = u_at(tid, dX);
      char* sm = smem + X_UL + C_U - 16 + 16 * tid;
      const u32x4 ut = {utag(T, 0), utag(T, 1), utag(T, 2), utag(T, 3)};
      __builtin_amdgcn_s_setprio(3);
      until(tu + UD + XD);
      u32x4 uw;
      asm volatile("global_load_dwordx4 %0, %1, %2 sc1" : "=&v"(uw) : "v"(xo), "s"(ws) : "memory");
      asm volatile("s_waitcnt vmcnt(0)" : "+v"(uw));
      uw ^= ut;
      u_again(uw, ut, xo);
      *(u32x4*)(sm + 16) = uw;
      BAR();
      if (w < 3) {
        const u32x4 pw = *(const u32x4*)sm;
        if (w < 2) sums<true>(smem + X_IM + 16 * tid, pw, uw); else sums<false>(smem + X_IM + 16 * tid, pw, uw);
        count_up(ca + 28);
      }
    } else {
      until(tu + UD + WD);
      BAR();
    }
    tu = (uint32_t)__builtin_amdgcn_readfirstlane(cnt[4]);
    TS(0, 3);
    __builtin_amdgcn_s_setprio(DPRIO);
    if (ffn) {
      ts1 = (tu + UD + VD2 - TSTOP) | 1u;
      gu_dma(0, N0, IC<40>{});
      gu_dma(N0, NA, IC<WDEPTH>{});
      asm volatile("s_waitcnt vmcnt(%0)" : : "n"(NA - 9) : "memory");
      gsync0(ca + 4, 4);
      gu_read(0);
      asm volatile("s_waitcnt lgkmcnt(0)" ::: "memory");
      count_up(ca + 36);
      TS(0, 4);
      gu_dma(NA, 18, IC<WDEPTH2>{});
      gwait(ca + 36, 4);
      down_dma(0, KN, lane);
      asm volatile("s_waitcnt vmcnt(%0)" : : "n"(3 * KN) : "memory");
      count_up(ca + 4);
      if (STP && w == 1)
        asm volatile("global_load_dword v126, %0, %1 sc1" : : "v"(O_SC + 1024u * (fb >> 4) + 128u * (lane & 7)), "s"(ws) : "memory", "v126");
      TS(0, 7);
    } else {
      if (!isx && C3) {
        const int n = 6 * min(tid, 191) + bj - 18, r = n >> 2;
        const char* wb = (const char*)((n & 1) ? wu : wg) + bx * 1152;
        const int c0 = (3 - (int)((uintptr_t)wb >> 7)) & 7;
        wb += (uint32_t)((r + 144 * ((r >= 144) + ((n >> 1) & 1))) * (F_ * 2) + 128 * c0);
        TOUCH(wb);
        if (c0 == 0) TOUCH(wb + 1024);
      }
      auto d_line = [&](int k) {
        const int n = min((k * 14 + bj - 18) * 64 + lane, 5183), q = n / 1296, r = n - 1296 * q;
        return (const char*)wd + (uint32_t)(bx * 5184 + (r / 18) * 72 + 18 * (HREV ? 3 - q : q) + r % 18) * 128u;
      };
      if (isx && w == 3) v_look(tu, lane);
      int k = w;
      if (w < HW) {
#pragma unroll 1
        for (; k < 6 && (int)((uint32_t)__builtin_amdgcn_s_memrealtime() - (tu + UD + VD2 - HSTOP)) < 0; k += HW) { TOUCH(d_line(k)); DRAIN(); }
      }
      TS(0, 4);
      v_time();
      until(tv + RD2);
      if (w < HW) {
#pragma unroll 1
        for (; k < 6; k += HW) { TOUCH(d_line(k)); if (!HALL) DRAIN(); }
      }
      TS(0, 7);
    }
    v_time();
    tk = tv + KD;
  }
  {
  const int tid = tid2, lane = tid & 63, i = lane & 15, kq = lane >> 4;
  __builtin_amdgcn_s_setprio(3);
  const int PL = ffn ? 6400 : LW + 6656;
  f32x2 xf[9];
  f32x4 za = {0.f, 0.f, 0.f, 0.f}, zb = za;
  uint32_t al, ha, h0a, h1a, so, da;
  auto ffn_terms = [&] {
    al = O_XC + 64u * (i >> 3) + 16u * kq + 128u * w;
    ha = (ROLE == 1) ? (uint32_t)((4 * (kq & 1) + 2 * (kq >> 1) + (i >> 3)) * 80 + 16 * w + 2 * (i & 7)) : 0u;
    h0a = (uint32_t)(min(i, 8) * 80 + 16 * kq); h1a = (uint32_t)(min(i ^ 8, 8) * 80 + 16 * kq);
    so = O_SL + (uint32_t)fb * SP + ((ROLE == 2) ? 16u * tid - 4096u : 8192u + 16u * lane);
    da = lb0 + (uint32_t)((8 * kq + ((lane >> 2) & 3)) * 1152 + 32 * ((8 * kq + ((lane >> 2) & 3) + 2) >> 2) + 8 * (lane & 3) + ((ROLE == 2) ? 128 * wl : 1024));
    if (ROLE == 2) asm volatile("" : "+v"(h0a), "+v"(h1a), "+v"(so), "+v"(da)); else asm volatile("" : "+v"(ha), "+v"(al), "+v"(da));
  };
  if (ROLE == 1) ffn_terms();
  uint32_t hv[9] = {}, nv[9] = {}, keya = 0u, keyb = 0u;
  int rka = 0, rkb = 0;
  auto put_row = [&](const uint32_t* r, int j, const void* base, uint32_t ro) {
    const u32x4 tl = {r[8], (uint32_t)DPP_I((int)r[8], 0x55, 0xf), (uint32_t)DPP_I((int)r[8], 0xaa, 0xf), (uint32_t)DPP_I((int)r[8], 0xff, 0xf)};
    asm volatile("global_store_dwordx4 %0, %1, %2 sc1\n\ts_nop 1" : : "v"(ro + 16u * lane), "v"((u32x4){r[4 * j], r[4 * j + 1], r[4 * j + 2], r[4 * j + 3]}), "s"(base) : "memory");
    if ((lane & 35) == 32 * j) asm volatile("global_store_dwordx4 %0, %1, %2 offset:1024 sc1\n\ts_nop 1" : : "v"(ro + 4u * (lane & 31)), "v"(tl), "s"(base) : "memory");
  };
  if (ln2) {
    const int tl = tid - 256, d1 = 64 * (tl >> 5) + (tl & 31), d3 = 64 * ((tl >> 4) & 7) + 32 + (tl & 15), d2 = (tl < 128) ? d3 + 16 : min(384 + tl, 575);
    uint32_t vo = v_at(d1, g0), vo2 = v_at(d2, g0), vo3 = v_at(d3, g0);
    u32x4 ut = {utag(T, 0), utag(T, 1), utag(T, 2), utag(T, 3)};
    u32x4 va, vc = ut, vb = ut;
    asm volatile("" : "+v"(vo), "+v"(vo2), "+v"(vo3), "+v"(ut), "+v"(vc), "+v"(vb));
    TS(0, 7);
    until(tv);
    TS(0, 8);
    asm volatile("global_load_dwordx4 %0, %2, %4 sc1\n\ts_cmp_lt_u32 %5, 2\n\ts_cbranch_scc1 1f\n\tglobal_load_dwordx4 %1, %3, %4 sc1\n1:"
                 : "=&v"(va), "+v"(vc) : "v"(vo), "v"(vo3), "s"(ws), "s"(wl) : "memory", "scc");
    until(tv + V2D);
    asm volatile("s_cmp_eq_u32 %3, 3\n\ts_cbranch_scc1 1f\n\tglobal_load_dwordx4 %0, %1, %2 sc1\n1:" : "+v"(vb) : "v"(vo2), "s"(ws), "s"(wl) : "memory", "scc");
    __builtin_amdgcn_sched_barrier(0);
    if (isx) {
      const u32x4 ra = *(const u32x4*)(smem + X_RW(wl) + 16 * lane), rb = *(const u32x4*)(smem + X_RW(wl) + 1024 + 16 * lane), rw = *(const u32x4*)(smem + X_WR + 16 * lane);
      const u32x2 rc = *(const u32x2*)(smem + X_RW(wl) + 2048 + 8 * lane);
#pragma unroll
      for (int k = 0; k < 4; ++k) { xv[k] = ra[k]; xv[4 + k] = rb[k]; wv[k] = rw[k]; }
      xv[8] = rc[0]; wv[4] = rc[1];
    }
    uint32_t pa = (uint32_t)(PL + 9216 * wl + 32 * lane), pb = (uint32_t)(PL + 9216 * wl + 2048 + 8 * (lane & 31)), ps = (lane < 32) ? 0u : 16u;
    uint32_t kid = (uint32_t)(((255 - g2) << 5) | (7 - 2 * wl - (lane >> 5)));
    uint32_t px = (uint32_t)(PL + 4 * d1), py = (uint32_t)(PL + 4 * d2), pz = (uint32_t)(PL + 4 * d3);
#pragma unroll
    for (int k = 0; k < 9; ++k) xf[k] = (f32x2){bflo(xv[k]), bfhi(xv[k])};
    asm volatile("" : "+v"(xf[0]), "+v"(xf[1]), "+v"(xf[2]), "+v"(xf[3]), "+v"(xf[4]), "+v"(xf[5]), "+v"(xf[6]), "+v"(xf[7]), "+v"(xf[8]));
    asm volatile("" : "+v"(za), "+v"(zb), "+v"(pa), "+v"(pb), "+v"(ps), "+v"(kid), "+v"(px), "+v"(py), "+v"(pz));
    float sv[10], ws_ = dot2(wv[4], 0x3f803f80u, 0.f) * 0.5f;
#pragma unroll
    for (int k = 0; k < 4; ++k) ws_ = dot2(wv[k], 0x3f803f80u, ws_);
    ws_ = wave_sum(ws_);
#pragma unroll
    for (int k = 0; k < 10; ++k) sv[k] = 16.f;
    uint32_t ka_ = lb0 + LM + 4u * (128 + 48 + 2 * wl + (lane >> 5)), kb_ = lb0 + LM + 4u * (128 + 48);
    uint32_t ko_ = O_KEY + (uint32_t)(256 * (lane & 7) + 8 * (g2 & 31) + (g2 >> 5)) * 16u;
    asm volatile("" : "+v"(ws_), "+v"(sv[0]), "+v"(sv[1]), "+v"(sv[2]), "+v"(sv[3]), "+v"(sv[4]), "+v"(sv[5]), "+v"(sv[6]), "+v"(sv[7]), "+v"(sv[8]), "+v"(sv[9]),
                 "+v"(ka_), "+v"(kb_), "+v"(ko_));
    if (ffn) gwait(ca + 36, 4);
    asm volatile("s_waitcnt vmcnt(1)\n\ts_cmp_eq_u32 %2, 3\n\ts_cbranch_scc0 1f\n\ts_waitcnt vmcnt(0)\n1:" : "+v"(va), "+v"(vc) : "s"(wl) : "memory", "scc");
    auto turn1 = [&] {
#pragma unroll
      for (int e = 0; e < 4; ++e) *(uint32_t*)(smem + px + 9216 * e) = va[e];
      if (wl >= 2) {
#pragma unroll
        for (int e = 0; e < 4; ++e) *(uint32_t*)(smem + pz + 9216 * e) = vc[e];
      }
    };
    auto turn = [&] {
      if (wl < 3) {
#pragma unroll
        for (int e = 0; e < 4; ++e) *(uint32_t*)(smem + py + 9216 * e) = vb[e];
      }
    };
    va ^= ut; vc ^= ut;
    turn1();
    asm volatile("s_waitcnt vmcnt(0)" : "+v"(vb) : : "memory");
    vb ^= ut;
    turn();
    const u32x4 vs = va | vb | vc;
    if (__builtin_expect(__any(((vs[0] | vs[1] | vs[2] | vs[3]) & UMASK) != 0u), 0)) {
#pragma unroll 1
      for (int spin = 0; spin < 4; ++spin) {
        const bool na = ((va[0] | va[1] | va[2] | va[3]) & UMASK) != 0u, nb = ((vb[0] | vb[1] | vb[2] | vb[3]) & UMASK) != 0u, nc = ((vc[0] | vc[1] | vc[2] | vc[3]) & UMASK) != 0u;
        if (!__any(na || nb || nc)) break;
        TSC(16);
        if (na) LD16(va, vo);
        if (nb) LD16(vb, vo2);
        if (nc) LD16(vc, vo3);
        asm volatile("s_waitcnt vmcnt(0)" : "+v"(va), "+v"(vb), "+v"(vc) : : "memory");
        if (na) va ^= ut;
        if (nb) vb ^= ut;
        if (nc) vc ^= ut;
      }
      auto again = [&](u32x4& v, uint32_t o, int d) {
        const int J = g0 >> 5;
        const uint32_t dn = O_DN + 16u * (uint32_t)((d < 512) ? 4 * (d >> 1) + ((0x01013232 >> (4 * J)) & 3) : 4 * (d - 256) + (J >> 1));
        bool pend = ((v[0] | v[1] | v[2] | v[3]) & UMASK) != 0u;
#pragma unroll 1
        for (int spin = 0; spin < SPIN_CAP && __any(pend); ++spin) {
          TSC(16);
          SLEEP(PSLEEP);
          if (pend) {
            uint32_t fin = ~T;
            if ((v[0] & v[1] & v[2] & v[3] & LMASK) == LMASK)
              asm volatile("global_load_dword %0, %1, %2 sc1\n\ts_waitcnt vmcnt(0)" : "=&v"(fin) : "v"(dn), "s"(ws) : "memory");
            LD16W(v, o);
            v ^= ut;
            pend = ((v[0] | v[1] | v[2] | v[3]) & UMASK) != 0u;
            if (pend && fin == T) {
              u32x2 sc;
              for (int k = 0; k < SPIN_CAP; ++k) {
                asm volatile("global_load_dwordx2 %0, %1, %2 sc1\n\ts_waitcnt vmcnt(0)" : "=&v"(sc) : "v"(O_SC + 16u * d), "s"(ws) : "memory");
                if (sc[0] == T) break;
              }
              *(u16*)(smem + L_SC + 2 * d) = (u16)(sc[1] >> 16);
              cnt[10] = 1u;
              v = (v ^ ut) & ~LMASK;
              pend = false;
            }
          }
        }
      };
      again(va, vo, d1); again(vc, vo3, d3); again(vb, vo2, d2);
      turn1();
      turn();
    }
    u32x4 a0, a1;
    u32x2 a2, cf;
    {
      uint32_t cc, n = SPIN_CAP;
      asm volatile("s_mov_b64 exec, 1\n\tv_mov_b32 v90, 1\n\tds_add_u32 %6, v90\n\ts_mov_b64 exec, -1\n"
                   "1:\n\tds_read2_b32 %3, %6 offset1:10\n\tds_read_b128 %0, %7\n\tds_read_b128 %1, %7 offset:16\n\tds_read_b64 %2, %8\n\t"
                   "s_sub_u32 %5, %5, 1\n\ts_cbranch_scc1 2f\n\ts_waitcnt lgkmcnt(3)\n\tv_readfirstlane_b32 %4, v90\n\t"
                   "s_cmp_lt_u32 %4, 4\n\ts_cbranch_scc1 1b\n2:\n\ts_waitcnt lgkmcnt(0)"
                   : "=&v"(a0), "=&v"(a1), "=&v"(a2), "=&{v[90:91]}"(cf), "=&s"(cc), "+s"(n) : "v"(ca), "v"(lb0 + pa), "v"(lb0 + pb) : "memory", "scc");
    }
    TS(0, 9);
    if (__builtin_expect(__builtin_amdgcn_readfirstlane(cf[1]) != 0, 0)) {
      const u32x4 q = *(const u32x4*)(smem + L_SC + 16 * lane);
      const uint32_t r = *(const uint32_t*)(smem + L_SC + 1024 + 4 * (lane & 31));
#pragma unroll
      for (int k = 0; k < 4; ++k) { sv[2 * k] = bflo(q[k]); sv[2 * k + 1] = bfhi(q[k]); }
      sv[8] = bflo(r); sv[9] = bfhi(r);
    }
    const uint32_t av[10] = {a0[0], a0[1], a0[2], a0[3], a1[0], a1[1], a1[2], a1[3], a2[0] >> ps, a2[1] >> ps};
    f32x2 h[9];
#pragma unroll
    for (int k = 0; k < 10; ++k) {
      asm("v_fma_mix_f32 %0, %1, %2, %3 op_sel_hi:[1,0,0]" : "=v"(h[(k < 8) ? k >> 1 : 8][k & 1]) : "v"(av[k]), "v"(sv[k]), "v"(xf[(k < 8) ? k >> 1 : 8][k & 1]));
      if (k < 8) asm("v_fma_mix_f32 %0, %1, %2, %3 op_sel:[1,0,0] op_sel_hi:[1,0,0]" : "=v"(h[4 + (k >> 1)][k & 1]) : "v"(av[k]), "v"(sv[k]), "v"(xf[4 + (k >> 1)][k & 1]));
    }
#pragma unroll
    for (int k = 0; k < 9; ++k) hv[k] = pack2(h[k][0], h[k][1]);
    const float da = dot2(hv[2], wv[2], dot2(hv[0], wv[0], zb[0])) + dot2(hv[3], wv[3], dot2(hv[1], wv[1], zb[1]));
    const float db = dot2(hv[6], wv[2], dot2(hv[4], wv[0], zb[2])) + dot2(hv[7], wv[3], dot2(hv[5], wv[1], zb[3]));
    float dt = dot2(hv[8], wv[4], fold32(da, db));
    f32x2 rc, nc;
    ln_stats<true>(hv, lane, 1.f, xf, za, rc, nc, dt);
    dt = (lane < 32) ? dt * rc[0] + ws_ * nc[0] : dt * rc[1] + ws_ * nc[1];
    const uint32_t bb = pack2(dt, dt) & 0xffff0000u;
    const uint32_t kk = (bb ^ (((uint32_t)((int)bb >> 31) & 0x7fff0000u) | 0x80000000u)) | kid;
    u32x4 ka, kb;
    uint32_t old;
    asm volatile("s_mov_b64 exec, %5\n\tds_write_b32 %3, %4\n\ts_mov_b64 exec, 1\n\tv_mov_b32 %2, 1\n\tds_add_rtn_u32 %2, %7, %2\n\ts_mov_b64 exec, -1\n\t"
                 "ds_read_b128 %0, %6\n\tds_read_b128 %1, %6 offset:16\n\ts_waitcnt lgkmcnt(0)"
                 : "=&v"(ka), "=&v"(kb), "=&v"(old) : "v"(ka_), "v"(kk), "s"(0x8000000080000000ull), "v"(kb_), "v"(ca) : "memory");
    if (__builtin_amdgcn_readfirstlane(old) == 7) {
      uint32_t m1 = max(max(ka[0], ka[1]), ka[2]), m2 = med3u(ka[0], ka[1], ka[2]), m3 = min(min(ka[0], ka[1]), ka[2]);
#pragma unroll
      for (int e = 0; e < 5; ++e) {
        const uint32_t kx = e ? kb[e - 1] : ka[3];
        m3 = med3u(m2, m3, kx); m2 = med3u(m1, m2, kx); m1 = max(m1, kx);
      }
      if (lane < 8) ST16(ko_, ((u32x4){m1 | 24u, m2 | 16u, m3 | 8u, T & ~1u}));
    }
    TS(0, 10);
    keya = (uint32_t)__builtin_amdgcn_readlane(kk, 31); keyb = (uint32_t)__builtin_amdgcn_readlane(kk, 63);
    ln_apply<false>(xf, lane, rc, nc, nv);
    gwait(ca, 8);
    if (wl == 1) {
      const u32x4 kv = *(const u32x4*)&kl[4 * (lane & 1)];
      if (lane < 2) ST16(O_KF + (uint32_t)(2 * g2 + lane) * 16u, ((u32x4){__builtin_amdgcn_perm(kv[1], kv[0], 0x07060302u), T, __builtin_amdgcn_perm(kv[3], kv[2], 0x07060302u), T}));
      if (ROLE == 2 && b == 0 && lane == 0) ST4(O_EP, T * 0x0E8B2F51u);
    }
    const uint32_t ko = kl[lane & 7];
    rka = FORCEFB + __popcll(__ballot(ko > keya) & 0xffull); rkb = FORCEFB + __popcll(__ballot(ko > keyb) & 0xffull);
#pragma unroll
    for (int k = 0; k < 9; ++k) {
      const f32x2 o = xf[k] + (f32x2){bflo(nv[k]), bfhi(nv[k])};
      hv[k] = pack2(o[0], o[1]);
      nv[k] = min(nv[k], 0xfffffffeu);
    }
    if (rka < 3) put_row(nv, 0, ws, O_XC + (uint32_t)(1023 - 4 * g2 - rka) * 1152u);
    if (rkb < 3) put_row(nv, 1, ws, O_XC + (uint32_t)(1023 - 4 * g2 - rkb) * 1152u);
    if (!LPOS && ffn) down_dma(KN, 4, lane);
    ffn_terms();
  }
  uint32_t k3rd = 0u, k4th = 0u;
  uint32_t q0 = lb0 + LM + 4u * lane, q1 = lb0 + LM + 32u * w, q2 = q1 + 4u * (lane & 7), q3 = lb0 + LM + 512u + 4u * (lane & 7), q4 = lb0 + LM + (uint32_t)(tid & ~7);
  uint32_t r8 = C_, sb = lb0 + LM + 512u;
  asm volatile("" : "+v"(q0), "+v"(q1), "+v"(q2), "+v"(q3), "+v"(q4), "+v"(r8), "+v"(sb));
  if constexpr (ROLE == 1) {
    u32x4 key;
    const uint32_t kof = O_KEY + (uint32_t)(256 * bx + tid) * 16u;
    until(tk);
    if (!ffn) SLEEP(NSLEEP);
    LD16(key, kof);
    if (ffn) {
      gwait0(ca + 4, 8);
      gu_read(1);
    }
    asm volatile("s_waitcnt vmcnt(0)" : "+v"(key) : : "memory");
    if (__builtin_expect(__any(key[3] != (T & ~1u)), 0)) {
#pragma unroll 1
      for (int spin = 0; spin < SPIN_CAP; ++spin) {
        const bool need = key[3] != (T & ~1u);
        if (!__any(need)) break;
        TSC(15);
        SLEEP(KBACK);
        if (need) LD16W(key, kof);
      }
    }
    TS(0, 11);
    const uint32_t m1 = oct_maxu(key[0]);
    const bool hit = key[0] == m1;
    const uint32_t k0 = hit ? key[1] : key[0], k1 = hit ? key[2] : key[1];
    const uint32_t m2 = oct_maxu(k0);
    *LP(u32x2, q4) = (u32x2){m1, m2};
    k3rd = (k0 == m2) ? k1 : k0;
    k4th = (k0 == m2) ? (hit ? 0u : key[2]) : k1;
    if (RPF == 1 && !ffn && w == (bj & 3)) {
      const uint32_t r1 = O_XC + ((m1 >> 3) & 0x3ffu) * 1152u + 128u * (lane & 7), r2 = O_XC + ((m2 >> 3) & 0x3ffu) * 1152u + 128u * (lane & 7);
      asm volatile("global_load_dword v126, %0, %3 sc1\n\tglobal_load_dword v126, %1, %3 sc1\n\tglobal_load_dword v126, %2, %3 offset:1024 sc1"
                   : : "v"(r1), "v"(r2), "v"(((lane & 1) ? r1 : r2) - 128u * (lane & 7)), "s"(ws) : "memory", "v126");
    }
  }
  auto pick = [&](auto co, auto so_) {
    BAR();
    const uint32_t c = *LP(uint32_t, q0 + co.value), own = *LP(uint32_t, q2 + co.value);
    const u32x4 la = *LP(u32x4, q1 + co.value), lb = *LP(u32x4, q1 + co.value + 16);
    uint32_t r = r8;
    asm("v_cmp_gt_u32 s[40:41], %1, %2\n\tv_cmp_gt_u32 s[42:43], %1, %3\n\tv_cmp_gt_u32 s[44:45], %1, %4\n\tv_cmp_gt_u32 s[46:47], %1, %5\n\t"
        "v_cmp_gt_u32 s[48:49], %1, %6\n\tv_cmp_gt_u32 s[50:51], %1, %7\n\tv_cmp_gt_u32 s[52:53], %1, %8\n\tv_cmp_gt_u32 s[54:55], %1, %9\n\t"
        "s_bcnt1_i32_b64 s40, s[40:41]\n\ts_bcnt1_i32_b64 s42, s[42:43]\n\tv_writelane_b32 %0, s40, 0\n\ts_bcnt1_i32_b64 s44, s[44:45]\n\t"
        "v_writelane_b32 %0, s42, 1\n\ts_bcnt1_i32_b64 s46, s[46:47]\n\tv_writelane_b32 %0, s44, 2\n\ts_bcnt1_i32_b64 s48, s[48:49]\n\t"
        "v_writelane_b32 %0, s46, 3\n\ts_bcnt1_i32_b64 s50, s[50:51]\n\tv_writelane_b32 %0, s48, 4\n\ts_bcnt1_i32_b64 s52, s[52:53]\n\t"
        "v_writelane_b32 %0, s50, 5\n\ts_bcnt1_i32_b64 s54, s[54:55]\n\tv_writelane_b32 %0, s52, 6\n\tv_writelane_b32 %0, s54, 7"
        : "+v"(r) : "v"(c), "v"(la[0]), "v"(la[1]), "v"(la[2]), "v"(la[3]), "v"(lb[0]), "v"(lb[1]), "v"(lb[2]), "v"(lb[3])
        : "scc", "s40", "s41", "s42", "s43", "s44", "s45", "s46", "s47", "s48", "s49", "s50", "s51", "s52", "s53", "s54", "s55");
    *LP(uint32_t, sb + 4u * min(r, (uint32_t)C_) + so_.value) = own;
    BAR();
  };
  auto rows = [&](uint32_t ao, auto&& meanwhile) {
    u32x4 aa, ab, ac = __builtin_bit_cast(u32x4, zb);
    asm volatile("global_load_dwordx4 %0, %3, %4 sc1\n\tglobal_load_dwordx4 %1, %3, %4 offset:512 sc1\n\t"
                 "s_cmp_lg_u32 %5, 0\n\ts_cbranch_scc1 1f\n\tglobal_load_dwordx4 %2, %3, %4 offset:1024 sc1\n1:"
                 : "=&v"(aa), "=&v"(ab), "+v"(ac) : "v"(ao), "s"(ws), "s"(w) : "memory", "scc");
    __builtin_amdgcn_sched_barrier(0);
    meanwhile();
    asm volatile("s_waitcnt vmcnt(0)" : "+v"(aa), "+v"(ab), "+v"(ac) : : "memory");
    if (__builtin_expect(__any(max(max(max4(aa), max4(ab)), max4(ac)) == 0xffffffffu), 0)) {
#pragma unroll 1
      for (int spin = 0; spin < SPIN_CAP; ++spin) {
        const bool na = max4(aa) == 0xffffffffu, nb = max4(ab) == 0xffffffffu, nc = max4(ac) == 0xffffffffu;
        if (!__any(na || nb || nc)) break;
        TSC(14);
        SLEEP(PSLEEP);
        if (na) LD16W(aa, ao);
        if (nb) LD16W(ab, ao + 512u);
        if (nc) LD16W(ac, ao + 1024u);
      }
    }
    *(u32x4*)(smem + LA + 16 * tid) = aa;
    *(u32x4*)(smem + LA + 4096 + 16 * tid) = ab;
    if (w == 0) *(u32x4*)(smem + LA + 8192 + 16 * tid) = ac;
  };
  auto rows_dma = [&](uint32_t ao) {
    uint32_t dl = lb0 + LA + 1024u * w, keep;
    asm volatile("s_mov_b32 %0, m0\n\ts_mov_b32 m0, %1\n\ts_add_u32 %1, %1, 4096 - 512\n\tglobal_load_lds_dwordx4 %2, %3 sc1\n\ts_mov_b32 m0, %1\n\ts_add_u32 %1, %1, 4096 - 512\n\t"
                 "global_load_lds_dwordx4 %2, %3 offset:512 sc1\n\ts_cmp_lg_u32 %4, 0\n\ts_cbranch_scc1 1f\n\ts_mov_b32 m0, %1\n\ts_nop 0\n\t"
                 "global_load_lds_dwordx4 %2, %3 offset:1024 sc1\n1:\n\ts_mov_b32 m0, %0" : "=&s"(keep), "+s"(dl) : "v"(ao), "s"(ws), "s"(w) : "memory", "scc");
  };
  auto get_bd = [&](int t0, int t1) {
#pragma unroll
    for (int t = t0; t < t1; ++t) {
      const u32x2 h0 = trd(da + 512 * (t >> 2) + 32 * (t & 3)), h1 = trd(da + 512 * (t >> 2) + 32 * (t & 3) + 4608 + 32);
      bw[t] = (u32x4){h0[0], h0[1], h1[0], h1[1]};
    }
#pragma unroll
    for (int t = t0; t < t1; ++t) asm volatile("" : "+v"(bw[t]));
  };
  pick(IC<0>{}, IC<0>{});
  uint32_t mykey = *LP(uint32_t, q3), ro = ((mykey >> 3) & 0x3ffu) * 1152u;
  TS(0, 12);
  auto check = [&] {
    cnt[(k3rd > (uint32_t)__builtin_amdgcn_readlane(mykey, 7)) ? 5 : 6] = 1u;
    const uint32_t m3 = oct_maxu(k3rd);
    if (k3rd == m3) k3rd = k4th;
    if ((lane & 7) == 0) cand[72 + (tid >> 3)] = m3;
    if (w == 0 && lane < 32) cand[(lane < 8) ? 64 + lane : 96 + lane] = (lane < 8) ? mykey : 0u;
  };
  if constexpr (ROLE == 1) {
    if (ffn && !FORCEFB) rows_dma(ro + al);
    check();
    DRAIN();
  } else if (ffn) {
    if (LPOS) { SLEEP(LSLEEP); down_dma(KN, 4, lane); } else { DRAIN(); gsync0(ca, 12); get_bd(0, 8); }
  }
  BAR();
  TS(0, 5);
  auto put_out = [&](int j, uint32_t k0, int rk) {
    const uint64_t mine = __ballot(((mykey ^ k0) & ~24u) == 0u) & 0xffull;
    if (mine == 0ull) put_row(hv, j, out, (uint32_t)((8 * g2 + 2 * wl + j) * (D_ * 2)));
    else if (rk >= 3) put_row(nv, j, ws, O_FB + 1152u * (uint32_t)__builtin_ctzll(mine));
  };
  auto warm_out = [&] {
    const uint32_t k0 = (lane < 9) ? keya : keyb;
    bool hit = false;
#pragma unroll
    for (int e = 0; e < 8; ++e) hit |= (((uint32_t)__builtin_amdgcn_readlane(mykey, e) ^ k0) & ~24u) == 0u;
    if (OTP && lane < 18 && hit) asm volatile("global_load_dword v126, %0, %1" : : "v"((uint32_t)((8 * g2 + 2 * wl) * (D_ * 2) + 128 * lane)), "s"(out) : "memory", "v126");
  };
  auto rest_out = [&] { put_out(0, keya, rka); put_out(1, keyb, rkb); };
  f32x4 lo, hi;
  u32x4 av[5];
  const uint32_t aa = lb0 + LA + 16u * lane;
  auto gu_ask = [&] {
#pragma unroll
    for (int j = 0; j < 5; ++j) asm volatile("ds_read_b128 %0, %1 offset:%2" : "=v"(av[j]) : "v"(aa), "n"(1024 * j) : "memory");
  };
  auto gate_up = [&] {
    lo = (f32x4){0.f, 0.f, 0.f, 0.f}; hi = lo;
#pragma unroll
    for (int j = 0; j < 9; ++j) {
      if (j < 5) asm volatile("s_waitcnt lgkmcnt(4)" : "+v"(av[j % 5]));
      else if (j == 5) asm volatile("s_waitcnt lgkmcnt(2)" : "+v"(av[0]), "+v"(av[1]));
      else if (j == 7) asm volatile("s_waitcnt lgkmcnt(0)" : "+v"(av[2]), "+v"(av[3]));
      lo = MMB(av[j % 5], bw[2 * j], lo);
      hi = MMB(av[j % 5], bw[2 * j + 1], hi);
      __builtin_amdgcn_sched_barrier(0);
      if (j < 4) asm volatile("ds_read_b128 %0, %1 offset:%2" : "=v"(av[j]) : "v"(aa), "n"(1024 * (j + 5)) : "memory");
    }
  };
  uint32_t fl;
  asm volatile("ds_read_b32 %0, %1" : "=v"(fl) : "v"(ca + 20) : "memory");
  if (FFN == 1) gu_ask();
  asm volatile("s_waitcnt lgkmcnt(%1)" : "+v"(fl) : "n"(FFN == 1 ? 5 : 0));
  bool slow = false;
  if (__builtin_expect(FORCESLOW || FORCEFB || __builtin_amdgcn_readfirstlane(fl) != 0, 0)) {
    TSC(17);
    if (FFN == 1) asm volatile("s_waitcnt lgkmcnt(0)" : "+v"(av[0]), "+v"(av[1]), "+v"(av[2]), "+v"(av[3]), "+v"(av[4]));
    pick(IC<256>{}, IC<64>{});
    mykey = sel[16 + (lane & 7)];
    ro = ((mykey >> 3) & 0x3ffu) * 1152u;
    if (FFN == 1 && !FORCEFB) rows_dma(ro + al);
    const uint32_t k8 = (uint32_t)__builtin_amdgcn_readlane(mykey, 7);
    if (ROLE == 1 && k3rd > k8) cnt[11] = 1u;
    if (w == 0 && __any((mykey & 24u) == 8u)) {
      const uint32_t pk = sel[16 + (lane >> 3)], gi = (pk >> 5) & 0xffu;
      u32x4 kf;
#pragma unroll 1
      for (int spin = 0; spin < SPIN_CAP; ++spin) {
        LD16W(kf, O_KF + (2u * (255u - gi) + ((lane >> 2) & 1u)) * 16u);
        if (!__any(kf[1] != T || kf[3] != T)) break;
        SLEEP(KBACK);
      }
      const uint32_t wd_ = (lane & 2) ? kf[2] : kf[0], kt = ((lane & 1) ? wd_ & 0xffff0000u : wd_ << 16) | (gi << 5) | (7u - (lane & 7));
      const uint64_t up = __ballot((pk & 24u) == 8u && kt >= (k8 & ~24u));
      if (__popcll((up >> (lane & 56)) & 0xffull) > 3) cnt[11] = 1u;
    }
    if (ROLE == 1) DRAIN();
    BAR();
    if (FORCESLOW > 1 || FORCEFB || __builtin_amdgcn_readfirstlane(cnt[11]) != 0) {
    slow = true;
    u32x4 key;
#pragma unroll 1
    for (int spin = 0; spin < SPIN_CAP; ++spin) {
      LD16W(key, O_KF + 16u * tid);
      if (!__any(key[1] != T || key[3] != T)) break;
      SLEEP(KBACK);
    }
    const uint32_t ib = (uint32_t)(((255 - (tid >> 1)) << 5) | (7 - 4 * (tid & 1)));
    uint32_t k0 = (key[0] << 16) | ib, k1 = (key[0] & 0xffff0000u) | (ib - 1), k2 = (key[2] << 16) | (ib - 2), k3 = (key[2] & 0xffff0000u) | (ib - 3);
    CEX(k0, k1) CEX(k2, k3) CEX(k0, k2) CEX(k1, k3) CEX(k1, k2)
#pragma unroll 1
    for (int c = 0; c < C_; ++c) {
      const uint32_t mx = wave_maxu(k0);
      const bool hit = (k0 == mx);
      k0 = hit ? k1 : k0; k1 = hit ? k2 : k1; k2 = hit ? k3 : k2; k3 = hit ? 0u : k3;
      if (lane == 0) cand[64 + 8 * w + c] = mx;
    }
    pick(IC<256>{}, IC<128>{});
    mykey = sel[32 + (lane & 7)];
    const uint32_t okey = sel[32 + (lane >> 3)];
    const uint64_t same = __ballot(((mykey ^ okey) & 0x1fe0u) == 0u && okey > mykey);
    const int sl = FORCEFB + __popcll((same >> (lane & 7)) & 0x0101010101010101ull);
    ro = (sl < 3) ? (((mykey >> 3) & 0x3ffu) + 3 - sl) * 1152u : O_FB - O_XC + 1152u * (lane & 7);
    if (ln2) rest_out();
    if (ffn) {
      if (ROLE == 1) rows(ro + al, [] {});
      BAR();
    }
    }
    if (FFN == 1) {
      gu_ask();
      asm volatile("s_waitcnt lgkmcnt(0)" : "+v"(av[0]), "+v"(av[1]), "+v"(av[2]), "+v"(av[3]), "+v"(av[4]));
    }
  }
  if (FFN == 1) gate_up();
  if (OPOS == 0 && ROLE == 2 && ffn && !slow) { SLEEP(OD); rest_out(); }
  if (!ffn) {
    if (ln2) warm_out();
    if (ln2 && !slow) { SLEEP(OD2); rest_out(); }
    TSE();
    return;
  }
  uint32_t fx = 0, fv = 0, fn = 0, fo = 0, sa[9];
  float rs, hx, xs;
  u32x4 se;
  if constexpr (ROLE == 1) {
    f32x4 lh = (lane < 32) ? lo : hi;
    float fa = fold32(lh[0], lh[2]), fb2 = fold32(lh[1], lh[3]);
    if (__builtin_expect(__any(__builtin_isunordered(fa, fb2)) && !slow, 0)) {
      gsync0(ca + 4, 12);
      rows(ro + al, [] {});
      gsync0(ca + 4, 16);
      gu_ask();
      gate_up();
      lh = (lane < 32) ? lo : hi;
      fa = fold32(lh[0], lh[2]); fb2 = fold32(lh[1], lh[3]);
    }
    const float g = __int_as_float(__builtin_amdgcn_update_dpp(__float_as_int(fa), __float_as_int(fb2), 0x128, 0xf, 0xc, false));
    const float u = __int_as_float(__builtin_amdgcn_update_dpp(__float_as_int(fb2), __float_as_int(fa), 0x128, 0xf, 0x3, false));
    const uint32_t gu = pack2(g, u);
    const float gp = bflo(gu);
    const float sg = rbf(gp * __builtin_amdgcn_rcpf(1.f + __expf(-gp)));
    *(u16*)((char*)hm + ha) = f2bf(sg * bfhi(gu));
  } else if (LPOS) {
    DRAIN();
    gsync0(ca, 12);
    get_bd(0, 8);
  }
  BAR();
  TS(0, 6);
  if (ROLE == 2 || w == 2) {
    const u32x4 a0 = *(const u32x4*)((char*)hm + h0a), a1 = *(const u32x4*)((char*)hm + h1a);
    if (ROLE == 1) get_bd(0, 4);
    const f32x4 z = {0.f, 0.f, 0.f, 0.f};
    f32x4 o[4];
#pragma unroll
    for (int t = 0; t < 2 * ROLE; ++t) o[t] = MMB(a1, bw[2 * t + 1], MMB(a0, bw[2 * t], z));
#pragma unroll
    for (int t = 0; t < ROLE; ++t) {
      u32x4 sv;
#pragma unroll
      for (int r = 0; r < 4; ++r) sv[r] = min(pack2(o[2 * t][r], o[2 * t + 1][r]), 0xfffffffeu);
      ST16(so + 4096u * t, sv);
    }
    TS(0, 13);
  }
  if (OPOS == 1 && ROLE == 2) {
    warm_out();
    SLEEP(OSLEEP);
    if (!slow) rest_out();
  }
  if (ROLE == 1 && w == 1) {
    const int q = (fb & 15) >> 2, ts = 4 * (q & 1) + ((i & 7) >> 1);
    const int dd = 64 * (fb >> 4) + 16 * (2 * (i & 1) + (q >> 1)) + 4 * (fb & 3) + 2 * (kq & 1) + (kq >> 1);
    const uint32_t fkey = (uint32_t)__builtin_amdgcn_ds_bpermute(4 * ts, (int)mykey), fr = (uint32_t)__builtin_amdgcn_ds_bpermute(4 * ts, (int)ro);
    const int ft = 2047 - (int)(((fkey >> 2) & 0x7f8u) | (fkey & 7u));
    fo = (uint32_t)(ft * D_ + dd) * 2u;
    u32x2 fs;
    asm volatile("global_load_ushort %0, %4, %8\n\tglobal_load_ushort %1, %5, %9 sc1\n\tglobal_load_ushort %2, %6, %9 sc1\n\tglobal_load_dwordx2 %3, %7, %9 sc1"
                 : "=&v"(fx), "=&v"(fv), "=&v"(fn), "=&v"(fs) : "v"(fo), "v"(v_at(dd, ft >> 3) + 2u * (ft & 7)), "v"(O_XC + fr + 2u * dd), "v"(O_SC + 16u * dd), "s"(x), "s"(ws) : "memory");
    uint32_t t16 = (((T >> (3 * ((ft >> 1) & 3))) & LMASK) | (((T >> (12 + ((ft >> 1) & 3))) & 0x00010001u) << 14)) >> (16 * (ft & 1)) & 0xffffu;
    const uint32_t o = fkey >> 16;
    const float rs0 = bf2f((u16)((o & 0x8000u) ? (o & 0x7fffu) : (~o & 0xffffu)));
    rs = rbf(__builtin_amdgcn_rcpf(1.f + __expf(-rs0)));
    const uint32_t one = (i < 8) ? (0x3f80u << (16 * (i & 1))) : 0u;
    se = (u32x4){(i >> 1) == 0 ? one : 0u, (i >> 1) == 1 ? one : 0u, (i >> 1) == 2 ? one : 0u, (i >> 1) == 3 ? one : 0u};
#pragma unroll
    for (int m = 0; m < 9; ++m) sa[m] = O_SL + (uint32_t)(16 * m + (lane >> 2)) * SP + (uint32_t)(4 * fb + (lane & 3)) * 16u;
    asm volatile("" : "+v"(t16), "+v"(rs), "+v"(se), "+v"(sa[0]), "+v"(sa[1]), "+v"(sa[2]), "+v"(sa[3]), "+v"(sa[4]), "+v"(sa[5]), "+v"(sa[6]), "+v"(sa[7]), "+v"(sa[8]));
    SLEEP(SSLEEP);
    if (FD) until(tk + FD);
    TS(0, 1);
    float sum;
#pragma unroll 1
    for (int spin = 0; spin < SPIN_CAP; ++spin) {
      u32x4 sv[9];
#pragma unroll
      for (int m = 0; m < 9; ++m) LD16(sv[m], sa[m]);
      asm volatile("s_waitcnt vmcnt(9)" : "+v"(fx), "+v"(fv), "+v"(fn), "+v"(fs));
      if (__builtin_expect(__any(fs[0] != T), 0)) {
        for (int k = 0; k < SPIN_CAP && fs[0] != T; ++k)
          asm volatile("global_load_dwordx2 %0, %1, %2 sc1\n\ts_waitcnt vmcnt(0)" : "=&v"(fs) : "v"(O_SC + 16u * dd), "s"(ws) : "memory");
      }
      const uint32_t f1 = fv ^ t16, f2 = (f1 & 7u) ? fv & ~7u : f1;
      hx = rbf(bf2f((u16)fx) + (float)__builtin_bit_cast(_Float16, (u16)f2) * __uint_as_float(fs[1]));
      xs = bf2f((u16)fn);
      asm volatile("" : "+v"(hx), "+v"(xs));
      f32x4 dv = {0.f, 0.f, 0.f, 0.f};
      static_for<0, 9>([&](auto mm) {
        constexpr int m = mm.value;
        asm volatile("s_waitcnt vmcnt(%1)" : "+v"(sv[m]) : "n"(8 - m));
        dv = MMB(sv[m], se, dv);
        __builtin_amdgcn_sched_barrier(0);
      });
      sum = fold16(fold32(dv[0], dv[1]), fold32(dv[2], dv[3]));
      if (__builtin_expect(!__any(sum != sum), 1)) break;
      uint32_t mx = 0u;
#pragma unroll
      for (int m = 0; m < 9; ++m) mx = max(mx, max4(sv[m]));
      if (!__any(mx == 0xffffffffu)) break;
      TSC(18);
      SLEEP(PSLEEP);
    }
    const float o1 = rbf(xs + rbf(rbf(sum) * rs));
    if (i < 8) asm volatile("global_store_short %0, %1, %2 sc1" : : "v"(fo), "v"((uint32_t)f2bf(hx + o1)), "s"(out) : "memory");
    TS(0, 2);
  }
  TSE();
  }
  }
}
#define BROT "192"
#define CPF_A "s_getpc_b64 s[96:97]\n\ts_lshl_b32 s99, s2, 4\n\ts_lshl_b32 s84, s98, 7\n\ts_add_u32 s99, s99, s84\n\tv_and_b32 v112, 1, v0\n\t" \
              "v_lshl_add_u32 v112, v112, 12, s99\n\tv_min_u32 v112, kf_end-kf_all-0x80, v112\n\ts_mov_b64 exec, 3\n\tglobal_load_dword v112, v112, s[96:97]\n\ts_mov_b64 exec, -1\n\t"
extern "C" __global__ __launch_bounds__(512, 4) void kf_all(KARGS) {
  __shared__ __attribute__((aligned(16))) char smem[SMEM];
  asm volatile("s_load_dwordx16 s[64:79], s[0:1], 0x0\n\ts_load_dwordx4 s[80:83], s[0:1], 0x40\n\tv_readfirstlane_b32 s98, v0\n\t"
               "s_add_u32 s84, s2, " BROT "\n\ts_xor_b32 s84, s84, s2\n\ts_bitset0_b32 s84, 8\n\ts_bitset0_b32 s84, 9\n\ts_xor_b32 s2, s2, s84\n\t"
               "v_mov_b32 v109, 0\n\ts_and_b32 s85, s2, 7\n\ts_bitcmp1_b32 s2, 8\n\ts_cbranch_scc1 1f\n\t" CPF_A "s_bitcmp1_b32 s98, 8\n\ts_cbranch_scc1 2f\n\t"
               "s_andn2_b32 s84, s2, 31\n\ts_lshl2_add_u32 s84, s85, s84\n\ts_bfe_u32 s85, s2, 0x20003\n\ts_or_b32 s84, s84, s85\n\t"
               "s_mulk_i32 s84, 0x2400\n\ts_mulk_i32 s98, 36\n\ts_add_u32 s84, s84, s98\n\tv_and_b32 v110, 63, v0\n\tv_and_b32 v111, 32, v0\n\t"
               "v_lshlrev_b32 v111, 5, v111\n\tv_lshl_add_u32 v111, v110, 2, v111\n\tv_lshl_add_u32 v110, v110, 4, s84\n\tv_add_u32 v111, s84, v111\n\t"
               "s_cmpk_lt_u32 s2, 0x90\n\ts_waitcnt lgkmcnt(0)\n\t"
               "global_load_dwordx4 v[100:103], v110, s[64:65]\n\tglobal_load_dwordx4 v[104:107], v110, s[64:65] offset:1152\n\t"
               "global_load_dword v108, v111, s[64:65] offset:1024\n\tglobal_load_dword v109, v109, s[82:83] sc1\n\t"
               "s_cbranch_scc1 kf_n\n\ts_branch kf_h\n"
               "1:\n\ts_bfe_u32 s84, s2, 0x50003\n\ts_lshl_b32 s85, s85, 5\n\ts_or_b32 s84, s84, s85\n\ts_lshl_b32 s84, s84, 2\n\t"
               "v_and_b32 v110, 0xff, v0\n\tv_bfe_u32 v107, v0, 7, 1\n"
               "4:\n\ts_lshl_b32 s85, s84, 7\n\tv_lshl_add_u32 v110, v110, 1, s85\n\tv_lshl_add_u32 v107, v107, 1, s84\n\ts_and_b32 s85, s98, s2\n\ts_waitcnt lgkmcnt(0)\n\t"
               "global_load_ushort v100, v107, s[78:79]\n\tglobal_load_ushort v101, v110, s[66:67]\n\tglobal_load_ushort v102, v110, s[68:69]\n\t"
               "global_load_dword v109, v109, s[82:83] sc1\n\ts_bitcmp1_b32 s85, 8\n\ts_cbranch_scc1 kf_u\n\t"
               "global_load_ushort v103, v110, s[70:71]\n\tglobal_load_ushort v104, v110, s[72:73]\n\tglobal_load_ushort v105, v110, s[74:75]\n\t"
               "global_load_ushort v106, v110, s[76:77]\n\tglobal_load_ushort v107, v107, s[80:81]\n\ts_bitcmp1_b32 s2, 8\n\ts_cbranch_scc1 kf_c\n"
               "6:\n\ts_barrier\n\ts_branch kf_p\n"
               "2:\n\ts_cmpk_lt_u32 s2, 0xc0\n\ts_cbranch_scc1 6b\n\t"
               "s_lshr_b32 s84, s2, 3\n\ts_lshl3_add_u32 s84, s85, s84\n\ts_lshl1_add_u32 s84, s84, 976\n\t"
               "v_and_b32 v110, 0x7f, v0\n\tv_mov_b32 v107, 0\n\ts_branch 4b"
               : : "s"(__builtin_amdgcn_kernarg_segment_ptr()), "s"(blockIdx.x), "v"(threadIdx.x),
                   "s"((uint32_t)(uintptr_t)(__attribute__((address_space(3))) char*)smem) : "scc", "s96", "s97", "s98", "s99", "v127");
}
extern "C" __global__ __launch_bounds__(512, 4) void kf_c(KARGS) {
  __shared__ __attribute__((aligned(16))) char smem[SMEM];
  role_body<0>(smem, KPASS);
}
extern "C" __global__ __launch_bounds__(512, 4) void kf_u(KARGS) {
  __shared__ __attribute__((aligned(16))) char smem[SMEM];
  role_body<3>(smem, KPASS);
}
extern "C" __global__ __launch_bounds__(512, 4) void kf_n(KARGS) {
  __shared__ __attribute__((aligned(16))) char smem[SMEM];
  role_body<1, 1>(smem, KPASS);
}
extern "C" __global__ __launch_bounds__(512, 4) void kf_h(KARGS) {
  __shared__ __attribute__((aligned(16))) char smem[SMEM];
  role_body<1, 0>(smem, KPASS);
}
extern "C" __global__ __launch_bounds__(512, 4) void kf_p(KARGS) {
  __shared__ __attribute__((aligned(16))) char smem[SMEM];
  role_body<2>(smem, KPASS);
}
extern "C" __global__ __launch_bounds__(512, 4) void kf_conv() { asm volatile(CONV_ASM ::: CONV_CLOB); }
extern "C" __global__ void kf_end() {}

extern "C" void kernel_launch(void* const* d_in, const int* in_sizes, int n_in,
                              void* d_out, int out_size, void* d_ws, size_t ws_size,
                              hipStream_t stream) {
  kf_all<<<512, 512, 0, stream>>>((const u16*)d_in[0], (const u16*)d_in[1], (const u16*)d_in[2], (const u16*)d_in[3], (const u16*)d_in[4],
                                  (const u16*)d_in[5], (const u16*)d_in[6], (const u16*)d_in[7], (const u16*)d_in[8], (char*)d_ws + (size_t)WSOFF * 1048576,
                                  (const u16*)d_in[10], (const u16*)d_in[11], (const u16*)d_in[12], (const u16*)d_in[13], (u16*)d_out);
}
```

```cpp
#include <hip/hip_runtime.h>
#include <hip/hip_bf16.h>
#include <stdint.h>
#define A_ 1

typedef __attribute__((ext_vector_type(8))) short bf16x8;
typedef __attribute__((ext_vector_type(4))) float f32x4;
typedef uint16_t u16;
typedef __attribute__((ext_vector_type(4))) uint32_t u32x4;
typedef __attribute__((ext_vector_type(2))) uint32_t u32x2;
#define TSD uint32_t pr0_ = 0u, pr1_ = 0u
__device__ unsigned long long g_prb[4096];
#define TS(k, i) do { if ((i) == 0) pr0_ = (uint32_t)__builtin_amdgcn_s_memrealtime() | 1u; \
    if (ROLE == 1 && (i) == 2 && (1 < 0 || w == 1) && (blockIdx.x & 255) < 144) pr1_ = (uint32_t)__builtin_amdgcn_s_memrealtime() | 1u; } while (0)
#define TSC(i)
#define TSE() do { if (__builtin_amdgcn_mbcnt_hi(-1, __builtin_amdgcn_mbcnt_lo(-1, 0)) == 0) \
    __hip_atomic_store(&g_prb[blockIdx.x * 8 + w], ((unsigned long long)pr1_ << 32) | pr0_, __ATOMIC_RELAXED, __HIP_MEMORY_SCOPE_AGENT); } while (0)
#define TSP
#define TSA

constexpr int L_ = 2048, D_ = 576, N_ = 128, F_ = 4608, C_ = 8;
constexpr int NF = 144;
constexpr uint32_t O_EP = 0;
constexpr uint32_t O_KF = 4096;
constexpr uint32_t O_KEY = O_KF + 8192;
constexpr uint32_t O_SC = O_KEY + 32768;
constexpr uint32_t O_DN = O_SC + 9216;
constexpr uint32_t O_SB = O_DN + 20480;
constexpr uint32_t O_SU = O_SB + 65536;
constexpr uint32_t O_FB = O_SU + 262144;
constexpr uint32_t O_XC = O_FB + 8 * 1152;
constexpr uint32_t SP = 9216 + 128;
constexpr uint32_t O_SL = O_XC + 1024 * 1152;
constexpr uint32_t SLU = 336;
constexpr uint32_t O_U = O_SL + 256 * SLU * 16;
constexpr int UP = 584;
constexpr uint32_t VP = 8192 + 128, VPP = 4096 + 128;
constexpr uint32_t O_V = O_U + 256 * UP * 16, O_VP = O_V + 256 * VP;
static_assert(256 * SLU * 16 >= NF * SP && O_SL % 128 == 0 && O_U % 128 == 0 && O_V % 128 == 0, "");
__host__ __device__ __forceinline__ uint32_t u_at(int tg, int d) { return O_U + 16u * (uint32_t)((tg >> 1) * (UP * 2) + (d >> 2) * 8 + (tg & 1) * 4 + (d & 3)); }
__host__ __device__ __forceinline__ uint32_t v_at(int d, int tg) {
  return (d < 512) ? O_V + (uint32_t)(d >> 1) * VP + 16u * (uint32_t)(2 * tg + (d & 1)) : O_VP + (uint32_t)(d - 512) * VPP + 16u * (uint32_t)tg;
}
#define HAVE_VAT
#define HAVE_VTAG
__device__ __forceinline__ float bf2f(u16 v) { return __uint_as_float((uint32_t)v << 16); }
__device__ __forceinline__ float bflo(uint32_t v) { return __uint_as_float(v << 16); }
__device__ __forceinline__ float bfhi(uint32_t v) { return __uint_as_float(v & 0xffff0000u); }
__device__ __forceinline__ u16 f2bf(float f) { return __builtin_bit_cast(u16, (__bf16)f); }
__device__ __forceinline__ float rbf(float f) { return bf2f(f2bf(f)); }
typedef __attribute__((ext_vector_type(2))) float f32x2;
typedef __attribute__((ext_vector_type(2))) __bf16 bf16x2v;
__device__ __forceinline__ uint32_t pack2(float a, float b) {
  return __builtin_bit_cast(uint32_t, __builtin_convertvector((f32x2){a, b}, bf16x2v));
}

#define DPP_I(v, ctrl, rmask) __builtin_amdgcn_update_dpp(0, (v), (ctrl), (rmask), 0xf, true)
__device__ __forceinline__ float row_sum(float v) {
  v += __int_as_float(DPP_I(__float_as_int(v), 0x111, 0xf));
  v += __int_as_float(DPP_I(__float_as_int(v), 0x112, 0xf));
  v += __int_as_float(DPP_I(__float_as_int(v), 0x114, 0xf));
  v += __int_as_float(DPP_I(__float_as_int(v), 0x118, 0xf));
  return v;
}
__device__ __forceinline__ float rdl(float v, int l) { return __int_as_float(__builtin_amdgcn_readlane(__float_as_int(v), l)); }
__device__ __forceinline__ float wave_sum(float v) {
  v = row_sum(v);
  asm("s_nop 1\n\tv_add_f32_dpp %0, %0, %0 row_bcast:15 row_mask:0xa bank_mask:0xf\n\t"
      "s_nop 1\n\tv_add_f32_dpp %0, %0, %0 row_bcast:31 row_mask:0xc bank_mask:0xf\n\ts_nop 0" : "+v"(v));
  return rdl(v, 63);
}
__device__ __forceinline__ float fold32(float a, float b) {
  const auto r = __builtin_amdgcn_permlane32_swap(__float_as_uint(a), __float_as_uint(b), false, false);
  return __uint_as_float(r[0]) + __uint_as_float(r[1]);
}
__device__ __forceinline__ float fold16(float a, float b) {
  const auto r = __builtin_amdgcn_permlane16_swap(__float_as_uint(a), __float_as_uint(b), false, false);
  return __uint_as_float(r[0]) + __uint_as_float(r[1]);
}
__device__ __forceinline__ void wave_sum2(float& a, float& b) {
  float v = row_sum(fold32(a, b));
  asm("s_nop 1\n\tv_add_f32_dpp %0, %0, %0 row_bcast:15 row_mask:0xa bank_mask:0xf\n\ts_nop 0" : "+v"(v));
  a = rdl(v, 31); b = rdl(v, 63);
}
__device__ __forceinline__ float dot2(uint32_t a, uint32_t b, float c) {
  return __builtin_amdgcn_fdot2_f32_bf16(__builtin_bit_cast(bf16x2v, a), __builtin_bit_cast(bf16x2v, b), c, false);
}
__device__ __forceinline__ uint32_t row_maxu(uint32_t v) {
  v = max(v, (uint32_t)DPP_I((int)v, 0xb1, 0xf));
  v = max(v, (uint32_t)DPP_I((int)v, 0x4e, 0xf));
  v = max(v, (uint32_t)DPP_I((int)v, 0x141, 0xf));
  v = max(v, (uint32_t)DPP_I((int)v, 0x140, 0xf));
  return v;
}
__device__ __forceinline__ uint32_t wave_maxu(uint32_t v) {
  v = max(v, (uint32_t)DPP_I((int)v, 0x111, 0xf));
  v = max(v, (uint32_t)DPP_I((int)v, 0x112, 0xf));
  v = max(v, (uint32_t)DPP_I((int)v, 0x114, 0xf));
  v = max(v, (uint32_t)DPP_I((int)v, 0x118, 0xf));
  v = max(v, (uint32_t)DPP_I((int)v, 0x142, 0xa));
  v = max(v, (uint32_t)DPP_I((int)v, 0x143, 0xc));
  return (uint32_t)__builtin_amdgcn_readlane((int)v, 63);
}

typedef __attribute__((ext_vector_type(8))) _Float16 f16x8;
typedef __attribute__((ext_vector_type(2))) _Float16 f16x2;
__device__ __forceinline__ uint32_t packh(f32x2 v) { return __builtin_bit_cast(uint32_t, __builtin_convertvector(v, f16x2)); }
__device__ __forceinline__ uint32_t packh(float a, float b) { return packh((f32x2){a, b}); }
constexpr int IP = 224, I_X = 128;
__device__ __forceinline__ f32x2 cmul(f32x2 z, f32x2 w) {
  f32x2 r;
  asm("v_pk_mul_f32 %0, %1, %2 op_sel_hi:[0,1]\n\tv_pk_fma_f32 %0, %1, %2, %0 op_sel:[1,1,0] op_sel_hi:[1,0,1] neg_lo:[0,1,0]" : "=&v"(r) : "v"(z), "v"(w));
  return r;
}
__device__ __forceinline__ f32x2 cexp2(float ar2, float air, float l) {
  const float t = air * l;
  return (f32x2){__builtin_amdgcn_cosf(t), __builtin_amdgcn_sinf(t)} * __builtin_amdgcn_exp2f(ar2 * l);
}
__device__ __forceinline__ float state_a(float ldt, float are, float aim, float& ar2, float& air, float& g) {
  const float dt = rbf(__expf(ldt));
  ar2 = dt * are * 1.4426950408889634f;
  air = dt * aim * 0.15915494309189535f;
  air -= __builtin_rintf(air);
  g = 0.f;
  if (__builtin_expect(__any(ar2 > 0.f), 0)) g = fminf(__uint_as_float(wave_maxu(__float_as_uint(fmaxf(ar2, 0.f)))), 1.f);
  return dt;
}
template <int N> __device__ __forceinline__ void put_im(char* p, int im, const f32x2* z) {
  *(u32x4*)p = (u32x4){packh(z[0][0], z[1][0]), packh(z[2][0], z[3][0]), packh(z[4][0], z[5][0]), packh(z[6][0], z[7][0])};
  *(u32x4*)(p + im) = (u32x4){packh(z[0][1], z[1][1]), packh(z[2][1], z[3][1]), packh(z[4][1], z[5][1]), packh(z[6][1], z[7][1])};
}
#define Y_ASM \
  "v_mov_b64 v[40:41], %0\nv_pk_mul_f32 v[42:43], v[40:41], %1 op_sel_hi:[0,1]\nv_pk_mul_f32 v[44:45], %0, %2 op_sel_hi:[0,1]\n" \
  "v_pk_fma_f32 v[42:43], v[40:41], %1, v[42:43] op_sel:[1,1,0] op_sel_hi:[1,0,1] neg_lo:[0,1,0]\n" \
  "v_pk_fma_f32 v[44:45], %0, %2, v[44:45] op_sel:[1,1,0] op_sel_hi:[1,0,1] neg_lo:[0,1,0]\nv_cvt_pk_f16_f32 v56, v40, v42\n" \
  "v_cvt_pk_f16_f32 v60, v41, v43\nv_pk_mul_f32 v[40:41], v[42:43], %1 op_sel_hi:[0,1]\nv_pk_mul_f32 v[48:49], v[44:45], %2 op_sel_hi:[0,1]\n" \
  "v_pk_fma_f32 v[40:41], v[42:43], %1, v[40:41] op_sel:[1,1,0] op_sel_hi:[1,0,1] neg_lo:[0,1,0]\n" \
  "v_pk_fma_f32 v[48:49], v[44:45], %2, v[48:49] op_sel:[1,1,0] op_sel_hi:[1,0,1] neg_lo:[0,1,0]\n" \
  "v_pk_mul_f32 v[42:43], v[40:41], %1 op_sel_hi:[0,1]\nv_pk_mul_f32 v[52:53], v[48:49], %2 op_sel_hi:[0,1]\n" \
  "v_pk_fma_f32 v[42:43], v[40:41], %1, v[42:43] op_sel:[1,1,0] op_sel_hi:[1,0,1] neg_lo:[0,1,0]\n" \
  "v_pk_fma_f32 v[52:53], v[48:49], %2, v[52:53] op_sel:[1,1,0] op_sel_hi:[1,0,1] neg_lo:[0,1,0]\nv_cvt_pk_f16_f32 v57, v40, v42\n" \
  "v_cvt_pk_f16_f32 v61, v41, v43\nv_pk_mul_f32 v[40:41], v[42:43], %1 op_sel_hi:[0,1]\n" \
  "v_pk_fma_f32 v[40:41], v[42:43], %1, v[40:41] op_sel:[1,1,0] op_sel_hi:[1,0,1] neg_lo:[0,1,0]\n" \
  "v_pk_mul_f32 v[42:43], v[40:41], %1 op_sel_hi:[0,1]\nv_pk_mul_f32 v[46:47], v[44:45], %1 op_sel_hi:[0,1]\n" \
  "v_pk_fma_f32 v[42:43], v[40:41], %1, v[42:43] op_sel:[1,1,0] op_sel_hi:[1,0,1] neg_lo:[0,1,0]\n" \
  "v_pk_fma_f32 v[46:47], v[44:45], %1, v[46:47] op_sel:[1,1,0] op_sel_hi:[1,0,1] neg_lo:[0,1,0]\nv_cvt_pk_f16_f32 v58, v40, v42\n" \
  "v_cvt_pk_f16_f32 v62, v41, v43\nv_cvt_pk_f16_f32 v64, v44, v46\nv_cvt_pk_f16_f32 v68, v45, v47\n" \
  "v_pk_mul_f32 v[40:41], v[42:43], %1 op_sel_hi:[0,1]\nv_pk_mul_f32 v[44:45], v[46:47], %1 op_sel_hi:[0,1]\n" \
  "v_pk_fma_f32 v[40:41], v[42:43], %1, v[40:41] op_sel:[1,1,0] op_sel_hi:[1,0,1] neg_lo:[0,1,0]\n" \
  "v_pk_fma_f32 v[44:45], v[46:47], %1, v[44:45] op_sel:[1,1,0] op_sel_hi:[1,0,1] neg_lo:[0,1,0]\n" \
  "v_pk_mul_f32 v[42:43], v[40:41], %1 op_sel_hi:[0,1]\nv_pk_mul_f32 v[46:47], v[44:45], %1 op_sel_hi:[0,1]\n" \
  "v_pk_fma_f32 v[42:43], v[40:41], %1, v[42:43] op_sel:[1,1,0] op_sel_hi:[1,0,1] neg_lo:[0,1,0]\n" \
  "v_pk_fma_f32 v[46:47], v[44:45], %1, v[46:47] op_sel:[1,1,0] op_sel_hi:[1,0,1] neg_lo:[0,1,0]\nv_cvt_pk_f16_f32 v59, v40, v42\n" \
  "v_cvt_pk_f16_f32 v63, v41, v43\nv_cvt_pk_f16_f32 v65, v44, v46\nv_cvt_pk_f16_f32 v69, v45, v47\n" \
  "v_pk_mul_f32 v[44:45], v[46:47], %1 op_sel_hi:[0,1]\nds_write_b128 %3, v[56:59] offset:0\n" \
  "v_pk_fma_f32 v[44:45], v[46:47], %1, v[44:45] op_sel:[1,1,0] op_sel_hi:[1,0,1] neg_lo:[0,1,0]\nds_write_b128 %3, v[60:63] offset:64\n" \
  "v_pk_mul_f32 v[46:47], v[44:45], %1 op_sel_hi:[0,1]\nv_pk_mul_f32 v[50:51], v[48:49], %1 op_sel_hi:[0,1]\n" \
  "v_pk_fma_f32 v[46:47], v[44:45], %1, v[46:47] op_sel:[1,1,0] op_sel_hi:[1,0,1] neg_lo:[0,1,0]\n" \
  "v_pk_fma_f32 v[50:51], v[48:49], %1, v[50:51] op_sel:[1,1,0] op_sel_hi:[1,0,1] neg_lo:[0,1,0]\nv_cvt_pk_f16_f32 v66, v44, v46\n" \
  "v_cvt_pk_f16_f32 v70, v45, v47\nv_cvt_pk_f16_f32 v56, v48, v50\nv_cvt_pk_f16_f32 v60, v49, v51\n" \
  "v_pk_mul_f32 v[44:45], v[46:47], %1 op_sel_hi:[0,1]\nv_pk_mul_f32 v[48:49], v[50:51], %1 op_sel_hi:[0,1]\n" \
  "v_pk_fma_f32 v[44:45], v[46:47], %1, v[44:45] op_sel:[1,1,0] op_sel_hi:[1,0,1] neg_lo:[0,1,0]\n" \
  "v_pk_fma_f32 v[48:49], v[50:51], %1, v[48:49] op_sel:[1,1,0] op_sel_hi:[1,0,1] neg_lo:[0,1,0]\n" \
  "v_pk_mul_f32 v[46:47], v[44:45], %1 op_sel_hi:[0,1]\nv_pk_mul_f32 v[50:51], v[48:49], %1 op_sel_hi:[0,1]\n" \
  "v_pk_fma_f32 v[46:47], v[44:45], %1, v[46:47] op_sel:[1,1,0] op_sel_hi:[1,0,1] neg_lo:[0,1,0]\n" \
  "v_pk_fma_f32 v[50:51], v[48:49], %1, v[50:51] op_sel:[1,1,0] op_sel_hi:[1,0,1] neg_lo:[0,1,0]\nv_cvt_pk_f16_f32 v67, v44, v46\n" \
  "v_cvt_pk_f16_f32 v71, v45, v47\nv_cvt_pk_f16_f32 v57, v48, v50\nv_cvt_pk_f16_f32 v61, v49, v51\n" \
  "v_pk_mul_f32 v[48:49], v[50:51], %1 op_sel_hi:[0,1]\nds_write_b128 %3, v[64:67] offset:16\n" \
  "v_pk_fma_f32 v[48:49], v[50:51], %1, v[48:49] op_sel:[1,1,0] op_sel_hi:[1,0,1] neg_lo:[0,1,0]\nds_write_b128 %3, v[68:71] offset:80\n" \
  "v_pk_mul_f32 v[50:51], v[48:49], %1 op_sel_hi:[0,1]\nv_pk_mul_f32 v[54:55], v[52:53], %1 op_sel_hi:[0,1]\n" \
  "v_pk_fma_f32 v[50:51], v[48:49], %1, v[50:51] op_sel:[1,1,0] op_sel_hi:[1,0,1] neg_lo:[0,1,0]\n" \
  "v_pk_fma_f32 v[54:55], v[52:53], %1, v[54:55] op_sel:[1,1,0] op_sel_hi:[1,0,1] neg_lo:[0,1,0]\nv_cvt_pk_f16_f32 v58, v48, v50\n" \
  "v_cvt_pk_f16_f32 v62, v49, v51\nv_cvt_pk_f16_f32 v64, v52, v54\nv_cvt_pk_f16_f32 v68, v53, v55\n" \
  "v_pk_mul_f32 v[48:49], v[50:51], %1 op_sel_hi:[0,1]\nv_pk_mul_f32 v[52:53], v[54:55], %1 op_sel_hi:[0,1]\n" \
  "v_pk_fma_f32 v[48:49], v[50:51], %1, v[48:49] op_sel:[1,1,0] op_sel_hi:[1,0,1] neg_lo:[0,1,0]\n" \
  "v_pk_fma_f32 v[52:53], v[54:55], %1, v[52:53] op_sel:[1,1,0] op_sel_hi:[1,0,1] neg_lo:[0,1,0]\n" \
  "v_pk_mul_f32 v[50:51], v[48:49], %1 op_sel_hi:[0,1]\nv_pk_mul_f32 v[54:55], v[52:53], %1 op_sel_hi:[0,1]\n" \
  "v_pk_fma_f32 v[50:51], v[48:49], %1, v[50:51] op_sel:[1,1,0] op_sel_hi:[1,0,1] neg_lo:[0,1,0]\n" \
  "v_pk_fma_f32 v[54:55], v[52:53], %1, v[54:55] op_sel:[1,1,0] op_sel_hi:[1,0,1] neg_lo:[0,1,0]\nv_cvt_pk_f16_f32 v59, v48, v50\n" \
  "v_cvt_pk_f16_f32 v63, v49, v51\nv_cvt_pk_f16_f32 v65, v52, v54\nv_cvt_pk_f16_f32 v69, v53, v55\n" \
  "v_pk_mul_f32 v[52:53], v[54:55], %1 op_sel_hi:[0,1]\nds_write_b128 %3, v[56:59] offset:32\n" \
  "v_pk_fma_f32 v[52:53], v[54:55], %1, v[52:53] op_sel:[1,1,0] op_sel_hi:[1,0,1] neg_lo:[0,1,0]\nds_write_b128 %3, v[60:63] offset:96\n" \
  "v_pk_mul_f32 v[54:55], v[52:53], %1 op_sel_hi:[0,1]\n" \
  "v_pk_fma_f32 v[54:55], v[52:53], %1, v[54:55] op_sel:[1,1,0] op_sel_hi:[1,0,1] neg_lo:[0,1,0]\nv_cvt_pk_f16_f32 v66, v52, v54\n" \
  "v_cvt_pk_f16_f32 v70, v53, v55\nv_pk_mul_f32 v[52:53], v[54:55], %1 op_sel_hi:[0,1]\n" \
  "v_pk_fma_f32 v[52:53], v[54:55], %1, v[52:53] op_sel:[1,1,0] op_sel_hi:[1,0,1] neg_lo:[0,1,0]\n" \
  "v_pk_mul_f32 v[54:55], v[52:53], %1 op_sel_hi:[0,1]\n" \
  "v_pk_fma_f32 v[54:55], v[52:53], %1, v[54:55] op_sel:[1,1,0] op_sel_hi:[1,0,1] neg_lo:[0,1,0]\nv_cvt_pk_f16_f32 v67, v52, v54\n" \
  "v_cvt_pk_f16_f32 v71, v53, v55\nds_write_b128 %3, v[64:67] offset:48\nds_write_b128 %3, v[68:71] offset:112\n"
#define Y_CLOB "memory", "v40", "v41", "v42", "v43", "v44", "v45", "v46", "v47", "v48", "v49", "v50", "v51", "v52", "v53", "v54", "v55", "v56", "v57", "v58", "v59", "v60", "v61", "v62", "v63", "v64", "v65", "v66", "v67", "v68", "v69", "v70", "v71"
#define T_ASM \
  "ds_read_b128 %4, %8\nds_read_b64_tr_b16 v[80:81], %7 offset:0\nds_read_b64_tr_b16 v[82:83], %7 offset:64\n" \
  "ds_read_b64_tr_b16 v[84:85], %5 offset:0\nds_read_b64_tr_b16 v[86:87], %5 offset:48\nds_read_b64_tr_b16 v[88:89], %6 offset:0\n" \
  "ds_read_b64_tr_b16 v[90:91], %6 offset:48\nds_read_b64_tr_b16 v[92:93], %7 offset:3584\nds_read_b64_tr_b16 v[94:95], %7 offset:3648\n" \
  "ds_read_b64_tr_b16 v[96:97], %5 offset:3584\nds_read_b64_tr_b16 v[98:99], %5 offset:3632\nds_read_b64_tr_b16 v[100:101], %6 offset:3584\n" \
  "ds_read_b64_tr_b16 v[102:103], %6 offset:3632\nds_read_b64_tr_b16 v[104:105], %7 offset:7168\nds_read_b64_tr_b16 v[106:107], %7 offset:7232\n" \
  "s_waitcnt lgkmcnt(8)\nv_mfma_f32_16x16x32_f16 %0, v[84:87], v[80:83], 0\nds_read_b64_tr_b16 v[108:109], %5 offset:7168\n" \
  "ds_read_b64_tr_b16 v[110:111], %5 offset:7216\nds_read_b64_tr_b16 v[112:113], %6 offset:7168\nv_mfma_f32_16x16x32_f16 %2, v[88:91], v[80:83], 0\n" \
  "ds_read_b64_tr_b16 v[114:115], %6 offset:7216\nds_read_b64_tr_b16 v[116:117], %7 offset:10752\nds_read_b64_tr_b16 v[118:119], %7 offset:10816\n" \
  "s_waitcnt lgkmcnt(8)\nv_mfma_f32_16x16x32_f16 %0, v[96:99], v[92:95], %0\nds_read_b64_tr_b16 v[120:121], %5 offset:10752\n" \
  "ds_read_b64_tr_b16 v[122:123], %5 offset:10800\nds_read_b64_tr_b16 v[124:125], %6 offset:10752\n" \
  "v_mfma_f32_16x16x32_f16 %2, v[100:103], v[92:95], %2\nds_read_b64_tr_b16 v[126:127], %6 offset:10800\n" \
  "ds_read_b64_tr_b16 v[80:81], %7 offset:14336\nds_read_b64_tr_b16 v[82:83], %7 offset:14400\ns_waitcnt lgkmcnt(8)\n" \
  "v_mfma_f32_16x16x32_f16 %0, v[108:111], v[104:107], %0\nds_read_b64_tr_b16 v[84:85], %5 offset:14336\n" \
  "ds_read_b64_tr_b16 v[86:87], %5 offset:14384\nds_read_b64_tr_b16 v[88:89], %6 offset:14336\n" \
  "v_mfma_f32_16x16x32_f16 %2, v[112:115], v[104:107], %2\nds_read_b64_tr_b16 v[90:91], %6 offset:14384\n" \
  "ds_read_b64_tr_b16 v[92:93], %7 offset:17920\nds_read_b64_tr_b16 v[94:95], %7 offset:17984\ns_waitcnt lgkmcnt(8)\n" \
  "v_mfma_f32_16x16x32_f16 %0, v[120:123], v[116:119], %0\nds_read_b64_tr_b16 v[96:97], %5 offset:17920\n" \
  "ds_read_b64_tr_b16 v[98:99], %5 offset:17968\nds_read_b64_tr_b16 v[100:101], %6 offset:17920\n" \
  "v_mfma_f32_16x16x32_f16 %2, v[124:127], v[116:119], %2\nds_read_b64_tr_b16 v[102:103], %6 offset:17968\n" \
  "ds_read_b64_tr_b16 v[104:105], %7 offset:21504\nds_read_b64_tr_b16 v[106:107], %7 offset:21568\ns_waitcnt lgkmcnt(8)\n" \
  "v_mfma_f32_16x16x32_f16 %1, v[84:87], v[80:83], 0\nds_read_b64_tr_b16 v[108:109], %5 offset:21504\nds_read_b64_tr_b16 v[110:111], %5 offset:21552\n" \
  "ds_read_b64_tr_b16 v[112:113], %6 offset:21504\nv_mfma_f32_16x16x32_f16 %3, v[88:91], v[80:83], 0\nds_read_b64_tr_b16 v[114:115], %6 offset:21552\n" \
  "ds_read_b64_tr_b16 v[116:117], %7 offset:25088\nds_read_b64_tr_b16 v[118:119], %7 offset:25152\ns_waitcnt lgkmcnt(8)\n" \
  "v_mfma_f32_16x16x32_f16 %1, v[96:99], v[92:95], %1\nds_read_b64_tr_b16 v[120:121], %5 offset:25088\n" \
  "ds_read_b64_tr_b16 v[122:123], %5 offset:25136\nds_read_b64_tr_b16 v[124:125], %6 offset:25088\n" \
  "v_mfma_f32_16x16x32_f16 %3, v[100:103], v[92:95], %3\nds_read_b64_tr_b16 v[126:127], %6 offset:25136\ns_waitcnt lgkmcnt(6)\n" \
  "v_mfma_f32_16x16x32_f16 %1, v[108:111], v[104:107], %1\nv_mfma_f32_16x16x32_f16 %3, v[112:115], v[104:107], %3\ns_waitcnt lgkmcnt(0)\n" \
  "v_mfma_f32_16x16x32_f16 %1, v[120:123], v[116:119], %1\nv_mfma_f32_16x16x32_f16 %3, v[124:127], v[116:119], %3\n"
#define T_CLOB "memory", "v80", "v81", "v82", "v83", "v84", "v85", "v86", "v87", "v88", "v89", "v90", "v91", "v92", "v93", "v94", "v95", "v96", "v97", "v98", "v99", "v100", "v101", "v102", "v103", "v104", "v105", "v106", "v107", "v108", "v109", "v110", "v111", "v112", "v113", "v114", "v115", "v116", "v117", "v118", "v119", "v120", "v121", "v122", "v123", "v124", "v125", "v126", "v127"
template <class F> __device__ __forceinline__ int gen_y(float ldt, float are, float aim, float bre, float bim, float cre, float cim, uint32_t im, F&& mid TSP) {
  float ar2, air, g;
  const float dt = state_a(ldt, are, aim, ar2, air, g);
  const float dbr = dt * bre, dbi = dt * bim, pr = cre * dbr - cim * dbi, pi = cre * dbi + cim * dbr;
  const f32x2 w8 = cexp2(ar2, -air, 8.f), w64 = cexp2(ar2, -air, 64.f);
  const float pm = fmaxf(fabsf(pr), fabsf(pi));
  int e = -4;
  if (__builtin_expect(__any(!(pm < 0x1p-4f) || ar2 > 0.f) || !__any(pm > 0x1p-12f), 0)) e = (int)(wave_maxu(__float_as_uint(pm)) >> 23) - 126 + (int)ceilf(g * 250.f);
  const f32x2 y0 = {__builtin_amdgcn_ldexpf(pr, 14 - e), __builtin_amdgcn_ldexpf(-pi, 14 - e)};
  mid();
  asm volatile(Y_ASM : : "v"(y0), "v"(w8), "v"(w64), "v"(im) : Y_CLOB);
  return e;
}
__device__ __forceinline__ int gen_x(float ldt, float are, float aim, char* im) {
  float ar2, air, g;
  state_a(ldt, are, aim, ar2, air, g);
  const int e = min((int)ceilf(g * 1810.f), 300);
  const float s = __builtin_amdgcn_ldexpf(1.f, 15 - e);
  const f32x2 w1 = cexp2(ar2, air, 1.f), w256 = cexp2(ar2, air, 256.f), w2 = cmul(w1, w1);
  f32x2 a[8], b[8], c[8];
  a[0] = (f32x2){s, 0.f}; a[1] = w1 * s; a[2] = w2 * s;
  b[0] = b[1] = w256 * s;
  b[2] = cmul(b[0], w2); a[3] = cmul(a[1], w2); a[4] = cmul(a[2], w2);
  b[3] = b[4] = b[5] = cmul(b[2], w2); a[5] = cmul(a[3], w2); a[6] = cmul(a[4], w2);
  b[6] = cmul(b[3], w2); a[7] = cmul(a[5], w2); c[0] = cmul(b[3], w256);
  b[7] = cmul(b[6], w2); c[2] = cmul(c[0], w256);
  put_im<0>(im + I_X, 48, a);
  c[1] = cmul(b[7], w256); c[3] = cmul(c[1], w256);
  put_im<0>(im + I_X + 16, 48, b);
#pragma unroll
  for (int k = 4; k < 8; ++k) c[k] = cmul(c[k - 1], w256);
  put_im<0>(im + I_X + 32, 48, c);
  return e;
}

template <int I> struct IC { static constexpr int value = I; };
template <int B, int E, class F> __device__ __forceinline__ void static_for(F&& f) {
  if constexpr (B < E) { f(IC<B>{}); static_for<B + 1, E>(f); }
}
typedef __attribute__((ext_vector_type(4))) short s16x4;
__device__ __forceinline__ u32x2 trd(uint32_t a) {
  return __builtin_bit_cast(u32x2, __builtin_amdgcn_ds_read_tr16_b64_v4i16((__attribute__((address_space(3))) s16x4*)(uintptr_t)a));
}
#define BAR() asm volatile("s_waitcnt lgkmcnt(0)\n\ts_barrier" ::: "memory")
#define ST16(off, v) asm volatile("global_store_dwordx4 %0, %1, %2 sc1\n\ts_nop 1" : : "v"(off), "v"(v), "s"(ws) : "memory")
#define ST4(off, v) asm volatile("global_store_dword %0, %1, %2 sc1" : : "v"(off), "v"(v), "s"(ws) : "memory")
#define LD16(r, off) asm volatile("global_load_dwordx4 %0, %1, %2 sc1" : "=v"(r) : "v"(off), "s"(ws) : "memory")
#define LD16W(r, off) asm volatile("global_load_dwordx4 %0, %1, %2 sc1\n\ts_waitcnt vmcnt(0)" : "=&v"(r) : "v"(off), "s"(ws) : "memory")
#define DRAIN() asm volatile("s_waitcnt vmcnt(0)" ::: "memory")
#define CEX(a, b) { const uint32_t hi_ = max(a, b), lo_ = min(a, b); a = hi_; b = lo_; }
#define MMB(a_, b_, c_) __builtin_amdgcn_mfma_f32_16x16x32_bf16(__builtin_bit_cast(bf16x8, a_), __builtin_bit_cast(bf16x8, b_), c_, 0, 0, 0)
#define NTM 0
#define DPOL ""
#define DPOL2 ""
#define DPOL4 ""
constexpr int SPIN_CAP = 1 << 18;
constexpr uint32_t UMASK = 0x40074007u;
__device__ __forceinline__ uint32_t utag(uint32_t T, int j) {
  return ((T >> (3 * j)) & 0x00070007u) | (((j <= 2) ? (T << (2 - j)) : (T >> (j - 2))) & 0x40004000u);
}
__device__ __forceinline__ uint32_t max4(const u32x4& a) { return max(max(a[0], a[1]), max(a[2], a[3])); }
#ifndef ULONG
#define ULONG 8
#endif
__device__ __forceinline__ void until(uint32_t t) {
#pragma unroll 1
  for (int spin = 0; spin < 600; ++spin) {
    const int d = (int)(t - (uint32_t)__builtin_amdgcn_s_memrealtime());
    if (d <= 0) break;
    if (ULONG && d > 4 * ULONG) __builtin_amdgcn_s_sleep(ULONG); else __builtin_amdgcn_s_sleep(1);
  }
}
__device__ __forceinline__ uint32_t latest(uint32_t t) {
  const uint32_t t0 = (uint32_t)__builtin_amdgcn_readfirstlane(t) - 0x40000000u;
  return wave_maxu(t - t0) + t0;
}
__device__ __forceinline__ uint32_t gate(const char* ws, uint32_t po, uint32_t T) {
  u32x2 pv;
#pragma unroll 1
  for (int spin = 0; spin < SPIN_CAP; ++spin) {
    asm volatile("global_load_dwordx2 %0, %1, %2 sc1\n\ts_waitcnt vmcnt(0)" : "=&v"(pv) : "v"(po), "s"(ws) : "memory");
    if (!__any(pv[0] != T)) break;
    __builtin_amdgcn_s_sleep(2);
  }
  return pv[1];
}
__device__ __forceinline__ uint32_t gate2(const char* ws, uint32_t oa, uint32_t da, uint32_t ob, uint32_t T) {
  u32x2 pa, pb;
#pragma unroll 1
  for (int spin = 0; spin < SPIN_CAP; ++spin) {
    asm volatile("global_load_dwordx2 %0, %2, %4 sc1\n\tglobal_load_dwordx2 %1, %3, %4 sc1\n\ts_waitcnt vmcnt(0)" : "=&v"(pa), "=&v"(pb) : "v"(oa), "v"(ob), "s"(ws) : "memory");
    if (!__any(pa[0] != T || pb[0] != T)) break;
    __builtin_amdgcn_s_sleep(4);
  }
  pa[1] += da;
  return latest(((int)(pa[1] - pb[1]) > 0) ? pa[1] : pb[1]);
}
__device__ __forceinline__ uint32_t ahead(uint32_t c0, uint32_t t0, int dt) {
  const uint32_t c = (uint32_t)__builtin_amdgcn_s_memtime(), t = (uint32_t)__builtin_amdgcn_s_memrealtime();
  return t + (uint32_t)((float)((t - t0) * (uint32_t)(20 * dt)) * __builtin_amdgcn_rcpf((float)(c - c0)));
}
__device__ __forceinline__ void gsync(uint32_t a, uint32_t target) {
  uint32_t t, c, n = SPIN_CAP;
  uint64_t ex;
  asm volatile("s_waitcnt lgkmcnt(0)\n\ts_mov_b64 %3, exec\n\ts_mov_b64 exec, 1\n\tv_mov_b32 %0, 1\n\tds_add_u32 %4, %0\n\ts_mov_b64 exec, %3\n"
               "1:\n\ts_sleep 1\n\tds_read_b32 %0, %4\n\ts_sub_u32 %2, %2, 1\n\ts_cbranch_scc1 2f\n\ts_waitcnt lgkmcnt(0)\n\tv_readfirstlane_b32 %1, %0\n\t"
               "s_cmp_lt_u32 %1, %5\n\ts_cbranch_scc1 1b\n2:\n\ts_waitcnt lgkmcnt(0)"
               : "=&v"(t), "=&s"(c), "+s"(n), "=&s"(ex) : "v"(a), "n"(target) : "memory", "scc");
}
__device__ __forceinline__ void gsync0(uint32_t a, uint32_t target) {
  uint32_t t, c, n = SPIN_CAP;
  asm volatile("s_waitcnt lgkmcnt(0)\n\ts_mov_b64 exec, 1\n\tv_mov_b32 %0, 1\n\tds_add_u32 %3, %0\n\ts_mov_b64 exec, -1\n"
               "1:\n\tds_read_b32 %0, %3\n\ts_sub_u32 %2, %2, 1\n\ts_cbranch_scc1 2f\n\ts_waitcnt lgkmcnt(0)\n\tv_readfirstlane_b32 %1, %0\n\t"
               "s_cmp_lt_u32 %1, %4\n\ts_cbranch_scc1 1b\n2:\n\ts_waitcnt lgkmcnt(0)"
               : "=&v"(t), "=&s"(c), "+s"(n) : "v"(a), "n"(target) : "memory", "scc");
}
__device__ __forceinline__ void gwait(uint32_t a, uint32_t target) {
  uint32_t t, c, n = SPIN_CAP;
  asm volatile("1:\n\ts_sleep 2\n\tds_read_b32 %0, %3\n\ts_sub_u32 %2, %2, 1\n\ts_cbranch_scc1 2f\n\ts_waitcnt lgkmcnt(0)\n\tv_readfirstlane_b32 %1, %0\n\t"
               "s_cmp_lt_u32 %1, %4\n\ts_cbranch_scc1 1b\n2:\n\ts_waitcnt lgkmcnt(0)"
               : "=&v"(t), "=&s"(c), "+s"(n) : "v"(a), "n"(target) : "memory", "scc");
}
__device__ __forceinline__ void gwait0(uint32_t a, uint32_t target) {
  uint32_t t, c, n = SPIN_CAP;
  asm volatile("1:\n\tds_read_b32 %0, %3\n\ts_sub_u32 %2, %2, 1\n\ts_cbranch_scc1 2f\n\ts_waitcnt lgkmcnt(0)\n\tv_readfirstlane_b32 %1, %0\n\t"
               "s_cmp_lt_u32 %1, %4\n\ts_cbranch_scc1 1b\n2:\n\ts_waitcnt lgkmcnt(0)"
               : "=&v"(t), "=&s"(c), "+s"(n) : "v"(a), "n"(target) : "memory", "scc");
}
__device__ __forceinline__ void dma16(const void* base, uint32_t voff, uint32_t lds) {
  uint32_t keep;
  asm volatile("s_mov_b32 %0, m0\n\ts_mov_b32 m0, %3\n\ts_nop 0\n\tglobal_load_lds_dwordx4 %1, %2" DPOL "\n\ts_mov_b32 m0, %0" : "=&s"(keep) : "v"(voff), "s"(base), "s"(lds) : "memory");
}
__device__ __forceinline__ void dma16d(const void* base, uint32_t voff, uint32_t lds) {
  uint32_t keep;
  asm volatile("s_mov_b32 %0, m0\n\ts_mov_b32 m0, %3\n\ts_nop 0\n\tglobal_load_lds_dwordx4 %1, %2" DPOL2 "\n\ts_mov_b32 m0, %0" : "=&s"(keep) : "v"(voff), "s"(base), "s"(lds) : "memory");
}
__device__ __forceinline__ void nap(int n) {
  for (; n > 15; n -= 15) __builtin_amdgcn_s_sleep(15);
  switch (n) {
    case 1: __builtin_amdgcn_s_sleep(1); break; case 2: __builtin_amdgcn_s_sleep(2); break; case 3: __builtin_amdgcn_s_sleep(3); break;
    case 4: __builtin_amdgcn_s_sleep(4); break; case 5: __builtin_amdgcn_s_sleep(5); break; case 6: __builtin_amdgcn_s_sleep(6); break;
    case 7: __builtin_amdgcn_s_sleep(7); break; case 8: __builtin_amdgcn_s_sleep(8); break; case 9: __builtin_amdgcn_s_sleep(9); break;
    case 10: __builtin_amdgcn_s_sleep(10); break; case 11: __builtin_amdgcn_s_sleep(11); break; case 12: __builtin_amdgcn_s_sleep(12); break;
    case 13: __builtin_amdgcn_s_sleep(13); break; case 14: __builtin_amdgcn_s_sleep(14); break; case 15: __builtin_amdgcn_s_sleep(15); break;
    default: break;
  }
}
#define SLEEP(n) do { if ((n) > 0) __builtin_amdgcn_s_sleep(n); } while (0)
#define USLEEP 3
#define SSLEEP 11
#define PSLEEP 2
#define PPRIO 0
#define LFIRST 1
#define WD 28
#define OSLEEP 12
#define QPRIO 1
#define KPRIO 2
#define GSLEEP 0
#define NSLEEP 0
#define UD 40
#define XD 0
#define RSLEEP 0
#define UPRIO 2
#define VD2 380
#define WDEPTH 6
#define ESLEEP 0
#define SD 58
#define KD 182
#define OD 0
#define KBACK 6
#define DDEPTH 12
#define C3 1
#define XROT 2
#define WSOFF 0
#define KN 1
#define RPF 1
#define LSLEEP 1
#define TSTOP 78
#define HSTOP 110
#define VEARLY 0ull
#define FPR 3
#define SPR 3
#define DPRIO 0
#define QPRIO2 3
#define HW 2
#define RD 57
#define OPOS 1
#define OD2 6
#define NA 12
#define RD2 110
#define ORD1 ((unsigned __int128)0x321087654u << 36 | 0x321087654ull)
#define WDEPTH2 5
#define RQ NA
#define HREV 1
#define QPRIO0 3
#define VPS 1
#define RW 0
#define NPRIO 0
#define XSL 10
#define V2D 24
#define N0 8
#define LPOS 1
#define ALLP 1
#define OTP 1
#define STP 0
#define FD 204
#define VDB 365
#define BALL 1
#define HALL 0
#define FORCESLOW 0
#define FORCEFB 0
#define CONV_ASM \
  "s_cmp_eq_u32 s89, 1\ns_cbranch_scc1 1f\ns_cmp_gt_u32 s89, 1\ns_cbranch_scc1 9f\nds_read_b64 v[124:125], v3 offset:576\n" \
  "ds_read_b64 v[126:127], v3 offset:584\nds_read_b64 v[120:121], v3 offset:64\nds_read_b64 v[122:123], v3 offset:72\n" \
  "ds_read_b64 v[116:117], v3 offset:640\nds_read_b64 v[118:119], v3 offset:648\nds_read_b64 v[112:113], v3 offset:128\n" \
  "ds_read_b64 v[114:115], v3 offset:136\nds_read_b64 v[108:109], v3 offset:704\nds_read_b64 v[110:111], v3 offset:712\n" \
  "ds_read_b64 v[104:105], v3 offset:192\nds_read_b64 v[106:107], v3 offset:200\nds_read_b64 v[100:101], v3 offset:768\n" \
  "ds_read_b64 v[102:103], v3 offset:776\nds_read_b64 v[96:97], v3 offset:256\nds_read_b64 v[98:99], v3 offset:264\n" \
  "ds_read_b64 v[92:93], v3 offset:832\nds_read_b64 v[94:95], v3 offset:840\nds_read_b64 v[88:89], v3 offset:320\n" \
  "ds_read_b64 v[90:91], v3 offset:328\nds_read_b64 v[84:85], v3 offset:896\nds_read_b64 v[86:87], v3 offset:904\n" \
  "ds_read_b64 v[80:81], v3 offset:384\nds_read_b64 v[82:83], v3 offset:392\nds_read_b64 v[76:77], v3 offset:960\n" \
  "ds_read_b64 v[78:79], v3 offset:968\nds_read_b64 v[72:73], v3 offset:448\nds_read_b64 v[74:75], v3 offset:456\n" \
  "ds_read_b64 v[68:69], v3 offset:1024\nds_read_b64 v[70:71], v3 offset:1032\nds_read_b64 v[64:65], v3 offset:512\n" \
  "ds_read_b64 v[66:67], v3 offset:520\nds_read_b64 v[60:61], v3 offset:0\nds_read_b64 v[62:63], v3 offset:8\nds_read_b64 v[56:57], v3 offset:1088\n" \
  "ds_read_b64 v[58:59], v3 offset:1096\ns_waitcnt lgkmcnt(0)\ns_barrier\nds_read_b128 v[36:39], v2 offset:0\nds_read_b128 v[40:43], v2 offset:64\n" \
  "ds_read_b128 v[44:47], v2 offset:128\nds_read_b128 v[48:51], v2 offset:192\ns_waitcnt lgkmcnt(3)\n" \
  "v_mfma_f32_16x16x32_f16 v[12:15], v[124:127], v[36:39], 0\ns_setprio 1\nds_read_b128 v[52:55], v2 offset:256\n" \
  "v_mfma_f32_16x16x32_f16 v[16:19], v[120:123], v[36:39], 0\ns_waitcnt lgkmcnt(3)\nv_mfma_f32_16x16x32_f16 v[12:15], v[116:119], v[40:43], v[12:15]\n" \
  "ds_read_b128 v[36:39], v2 offset:320\nv_mfma_f32_16x16x32_f16 v[16:19], v[112:115], v[40:43], v[16:19]\ns_waitcnt lgkmcnt(3)\n" \
  "v_mfma_f32_16x16x32_f16 v[12:15], v[108:111], v[44:47], v[12:15]\nds_read_b128 v[40:43], v2 offset:384\n" \
  "v_mfma_f32_16x16x32_f16 v[16:19], v[104:107], v[44:47], v[16:19]\ns_waitcnt lgkmcnt(3)\n" \
  "v_mfma_f32_16x16x32_f16 v[12:15], v[100:103], v[48:51], v[12:15]\nds_read_b128 v[44:47], v2 offset:448\n" \
  "v_mfma_f32_16x16x32_f16 v[16:19], v[96:99], v[48:51], v[16:19]\ns_waitcnt lgkmcnt(3)\n" \
  "v_mfma_f32_16x16x32_f16 v[12:15], v[92:95], v[52:55], v[12:15]\nv_mfma_f32_16x16x32_f16 v[16:19], v[88:91], v[52:55], v[16:19]\n" \
  "s_waitcnt lgkmcnt(2)\nv_mfma_f32_16x16x32_f16 v[12:15], v[84:87], v[36:39], v[12:15]\nds_read_b128 v[48:51], v2 offset:512\n" \
  "v_mfma_f32_16x16x32_f16 v[16:19], v[80:83], v[36:39], v[16:19]\ns_waitcnt lgkmcnt(2)\n" \
  "v_mfma_f32_16x16x32_f16 v[12:15], v[76:79], v[40:43], v[12:15]\nds_read_b128 v[52:55], v2 offset:576\n" \
  "v_mfma_f32_16x16x32_f16 v[16:19], v[72:75], v[40:43], v[16:19]\ns_waitcnt lgkmcnt(2)\n" \
  "v_mfma_f32_16x16x32_f16 v[12:15], v[68:71], v[44:47], v[12:15]\nds_read_b128 v[36:39], v2 offset:640\n" \
  "v_mfma_f32_16x16x32_f16 v[16:19], v[64:67], v[44:47], v[16:19]\nv_mfma_f32_16x16x32_f16 v[20:23], v[60:63], v[44:47], 0\n" \
  "ds_read_b128 v[40:43], v2 offset:704\nv_mfma_f32_16x16x32_f16 v[28:31], v[56:59], v[44:47], 0\ns_waitcnt lgkmcnt(3)\n" \
  "v_mfma_f32_16x16x32_f16 v[16:19], v[124:127], v[48:51], v[16:19]\nds_read_b128 v[56:59], v2 offset:768\nv_cvt_pkrtz_f16_f32 v32, v12, v13\n" \
  "v_mfma_f32_16x16x32_f16 v[20:23], v[120:123], v[48:51], v[20:23]\nv_cvt_pkrtz_f16_f32 v33, v14, v15\ns_waitcnt lgkmcnt(3)\n" \
  "v_mfma_f32_16x16x32_f16 v[16:19], v[116:119], v[52:55], v[16:19]\nds_read_b128 v[44:47], v2 offset:832\n" \
  "v_pk_add_u16 v32, v32, 4 op_sel_hi:[1,0] clamp\nv_mfma_f32_16x16x32_f16 v[20:23], v[112:115], v[52:55], v[20:23]\n" \
  "v_pk_add_u16 v33, v33, 4 op_sel_hi:[1,0] clamp\ns_waitcnt lgkmcnt(3)\nv_mfma_f32_16x16x32_f16 v[16:19], v[108:111], v[36:39], v[16:19]\n" \
  "ds_read_b128 v[48:51], v2 offset:896\nds_write_b64 v4, v[32:33] offset:0\nv_mfma_f32_16x16x32_f16 v[20:23], v[104:107], v[36:39], v[20:23]\n" \
  "s_waitcnt lgkmcnt(3)\nv_mfma_f32_16x16x32_f16 v[16:19], v[100:103], v[40:43], v[16:19]\nds_read_b128 v[52:55], v2 offset:960\n" \
  "v_mfma_f32_16x16x32_f16 v[20:23], v[96:99], v[40:43], v[20:23]\nv_mfma_f32_16x16x32_f16 v[16:19], v[92:95], v[56:59], v[16:19]\n" \
  "v_mfma_f32_16x16x32_f16 v[20:23], v[88:91], v[56:59], v[20:23]\nds_read_b128 v[36:39], v2 offset:1024\ns_waitcnt lgkmcnt(3)\n" \
  "v_mfma_f32_16x16x32_f16 v[16:19], v[84:87], v[44:47], v[16:19]\nv_mfma_f32_16x16x32_f16 v[20:23], v[80:83], v[44:47], v[20:23]\n" \
  "ds_read_b128 v[40:43], v2 offset:1088\nds_read_b64 v[56:57], v6 offset:256\nds_read_b64 v[58:59], v6 offset:264\n" \
  "v_mfma_f32_16x16x32_f16 v[16:19], v[76:79], v[48:51], v[16:19]\nv_mfma_f32_16x16x32_f16 v[20:23], v[72:75], v[48:51], v[20:23]\n" \
  "ds_read_b128 v[44:47], v2 offset:1152\ns_waitcnt lgkmcnt(4)\nv_mfma_f32_16x16x32_f16 v[16:19], v[68:71], v[52:55], v[16:19]\n" \
  "v_mfma_f32_16x16x32_f16 v[20:23], v[64:67], v[52:55], v[20:23]\nds_read_b128 v[48:51], v2 offset:1216\n" \
  "v_mfma_f32_16x16x32_f16 v[24:27], v[60:63], v[52:55], 0\nv_mfma_f32_16x16x32_f16 v[20:23], v[124:127], v[36:39], v[20:23]\n" \
  "ds_read_b128 v[52:55], v2 offset:1280\nv_mfma_f32_16x16x32_f16 v[24:27], v[120:123], v[36:39], v[24:27]\ns_waitcnt lgkmcnt(5)\n" \
  "v_mfma_f32_16x16x32_f16 v[20:23], v[116:119], v[40:43], v[20:23]\nds_read_b128 v[36:39], v2 offset:1344\nv_cvt_pkrtz_f16_f32 v32, v16, v17\n" \
  "v_mfma_f32_16x16x32_f16 v[24:27], v[112:115], v[40:43], v[24:27]\nv_cvt_pkrtz_f16_f32 v33, v18, v19\ns_waitcnt lgkmcnt(3)\n" \
  "v_mfma_f32_16x16x32_f16 v[20:23], v[108:111], v[44:47], v[20:23]\nds_read_b128 v[40:43], v2 offset:1408\n" \
  "v_pk_add_u16 v32, v32, 4 op_sel_hi:[1,0] clamp\nds_read_b32 v35, v9 offset:32\nv_mfma_f32_16x16x32_f16 v[24:27], v[104:107], v[44:47], v[24:27]\n" \
  "v_pk_add_u16 v33, v33, 4 op_sel_hi:[1,0] clamp\ns_waitcnt lgkmcnt(3)\nv_mfma_f32_16x16x32_f16 v[20:23], v[100:103], v[48:51], v[20:23]\n" \
  "ds_read_b128 v[44:47], v2 offset:1472\nds_write_b64 v4, v[32:33] offset:1024\nv_mfma_f32_16x16x32_f16 v[24:27], v[96:99], v[48:51], v[24:27]\n" \
  "v_mfma_f32_16x16x32_f16 v[20:23], v[92:95], v[52:55], v[20:23]\nv_mfma_f32_16x16x32_f16 v[24:27], v[88:91], v[52:55], v[24:27]\n" \
  "ds_read_b128 v[48:51], v2 offset:1536\ns_waitcnt lgkmcnt(3)\nv_readfirstlane_b32 s85, v35\ns_cmp_lt_u32 s85, 4\ns_cbranch_scc1 21f\n22:\n" \
  "v_mfma_f32_16x16x32_f16 v[20:23], v[84:87], v[36:39], v[20:23]\nv_mfma_f32_16x16x32_f16 v[24:27], v[80:83], v[36:39], v[24:27]\n" \
  "ds_read_b128 v[52:55], v2 offset:1600\nv_mfma_f32_16x16x32_f16 v[20:23], v[76:79], v[40:43], v[20:23]\n" \
  "v_mfma_f32_16x16x32_f16 v[24:27], v[72:75], v[40:43], v[24:27]\nds_read_b128 v[36:39], v2 offset:1664\ns_waitcnt lgkmcnt(4)\n" \
  "v_mfma_f32_16x16x32_f16 v[20:23], v[68:71], v[44:47], v[20:23]\nv_mfma_f32_16x16x32_f16 v[24:27], v[64:67], v[44:47], v[24:27]\n" \
  "ds_read_b128 v[40:43], v2 offset:1728\nv_mfma_f32_16x16x32_f16 v[28:31], v[60:63], v[44:47], v[28:31]\nds_read_b64_tr_b16 v[60:61], v5 offset:0\n" \
  "ds_read_b64_tr_b16 v[62:63], v5 offset:32\ns_waitcnt lgkmcnt(4)\nv_mfma_f32_16x16x32_f16 v[24:27], v[124:127], v[48:51], v[24:27]\n" \
  "v_mfma_f32_16x16x32_f16 v[28:31], v[120:123], v[48:51], v[28:31]\nds_read_b64_tr_b16 v[44:45], v5 offset:256\n" \
  "ds_read_b64_tr_b16 v[46:47], v5 offset:288\nv_mfma_f32_16x16x32_f16 v[24:27], v[116:119], v[52:55], v[24:27]\n" \
  "ds_read_b64 v[120:121], v6 offset:320\nds_read_b64 v[122:123], v6 offset:328\nv_mfma_f32_16x16x32_f16 v[28:31], v[112:115], v[52:55], v[28:31]\n" \
  "ds_read_b128 v[48:51], v2 offset:1792\ns_waitcnt lgkmcnt(7)\nv_mfma_f32_16x16x32_f16 v[24:27], v[108:111], v[36:39], v[24:27]\n" \
  "ds_read_b128 v[112:115], v2 offset:1856\nv_mfma_f32_16x16x32_f16 v[28:31], v[104:107], v[36:39], v[28:31]\nds_read_b64 v[52:53], v6 offset:128\n" \
  "ds_read_b64 v[54:55], v6 offset:136\nv_mfma_f32_16x16x32_f16 v[24:27], v[100:103], v[40:43], v[24:27]\nds_read_b128 v[104:107], v2 offset:1920\n" \
  "v_mfma_f32_16x16x32_f16 v[28:31], v[96:99], v[40:43], v[28:31]\nds_read_b64 v[36:37], v6 offset:192\nds_read_b64 v[38:39], v6 offset:200\n" \
  "s_waitcnt lgkmcnt(9)\nv_mfma_f32_16x16x32_f16 v[20:23], v[60:63], v[56:59], v[20:23]\nds_read_b128 v[96:99], v2 offset:1984\ns_waitcnt lgkmcnt(7)\n" \
  "v_mfma_f32_16x16x32_f16 v[20:23], v[44:47], v[120:123], v[20:23]\nv_mfma_f32_16x16x32_f16 v[24:27], v[92:95], v[48:51], v[24:27]\n" \
  "ds_read_b128 v[40:43], v2 offset:2048\nv_mfma_f32_16x16x32_f16 v[28:31], v[88:91], v[48:51], v[28:31]\nds_read_b64_tr_b16 v[88:89], v5 offset:512\n" \
  "ds_read_b64_tr_b16 v[90:91], v5 offset:544\ns_waitcnt lgkmcnt(6)\nv_mfma_f32_16x16x32_f16 v[24:27], v[84:87], v[112:115], v[24:27]\n" \
  "v_mfma_f32_16x16x32_f16 v[28:31], v[80:83], v[112:115], v[28:31]\nv_cvt_pkrtz_f16_f32 v32, v20, v21\nv_cvt_pkrtz_f16_f32 v33, v22, v23\n" \
  "v_mfma_f32_16x16x32_f16 v[24:27], v[76:79], v[104:107], v[24:27]\nds_read_b64_tr_b16 v[48:49], v5 offset:768\n" \
  "ds_read_b64_tr_b16 v[50:51], v5 offset:800\nv_mfma_f32_16x16x32_f16 v[28:31], v[72:75], v[104:107], v[28:31]\n" \
  "ds_read_b128 v[80:83], v2 offset:2112\ns_waitcnt lgkmcnt(5)\nv_mfma_f32_16x16x32_f16 v[24:27], v[68:71], v[96:99], v[24:27]\n" \
  "ds_read_b128 v[112:115], v2 offset:2176\nv_pk_add_u16 v32, v32, 4 op_sel_hi:[1,0] clamp\n" \
  "v_mfma_f32_16x16x32_f16 v[28:31], v[64:67], v[96:99], v[28:31]\nds_read_b128 v[72:75], v2 offset:2240\n" \
  "v_pk_add_u16 v33, v33, 4 op_sel_hi:[1,0] clamp\nv_mfma_f32_16x16x32_f16 v[28:31], v[124:127], v[40:43], v[28:31]\n" \
  "ds_read_b128 v[104:107], v2 offset:2304\nds_write_b64 v4, v[32:33] offset:2048\nv_mfma_f32_16x16x32_f16 v[24:27], v[60:63], v[52:55], v[24:27]\n" \
  "ds_read_b128 v[64:67], v2 offset:2368\nds_read_b64 v[96:97], v6 offset:0\nds_read_b64 v[98:99], v6 offset:8\n" \
  "v_mfma_f32_16x16x32_f16 v[24:27], v[44:47], v[36:39], v[24:27]\nds_read_b128 v[124:127], v2 offset:2432\ns_waitcnt lgkmcnt(9)\n" \
  "v_mfma_f32_16x16x32_f16 v[24:27], v[88:91], v[56:59], v[24:27]\nds_read_b128 v[40:43], v2 offset:2496\n" \
  "v_mfma_f32_16x16x32_f16 v[24:27], v[48:51], v[120:123], v[24:27]\ns_waitcnt lgkmcnt(8)\n" \
  "v_mfma_f32_16x16x32_f16 v[28:31], v[116:119], v[80:83], v[28:31]\nds_read_b64 v[116:117], v6 offset:64\nds_read_b64 v[118:119], v6 offset:72\n" \
  "v_mfma_f32_16x16x32_f16 v[28:31], v[108:111], v[112:115], v[28:31]\nds_read_b64_tr_b16 v[80:81], v5 offset:1024\n" \
  "ds_read_b64_tr_b16 v[82:83], v5 offset:1056\ns_waitcnt lgkmcnt(10)\nv_mfma_f32_16x16x32_f16 v[28:31], v[100:103], v[72:75], v[28:31]\n" \
  "v_cvt_pkrtz_f16_f32 v32, v24, v25\nv_cvt_pkrtz_f16_f32 v33, v26, v27\nv_mfma_f32_16x16x32_f16 v[28:31], v[92:95], v[104:107], v[28:31]\n" \
  "v_pk_add_u16 v32, v32, 4 op_sel_hi:[1,0] clamp\ns_waitcnt lgkmcnt(5)\nv_mfma_f32_16x16x32_f16 v[28:31], v[84:87], v[64:67], v[28:31]\n" \
  "ds_read_b64_tr_b16 v[108:109], v5 offset:1280\nds_read_b64_tr_b16 v[110:111], v5 offset:1312\n" \
  "v_mfma_f32_16x16x32_f16 v[28:31], v[76:79], v[124:127], v[28:31]\nds_read_b64_tr_b16 v[112:113], v7 offset:0\n" \
  "ds_read_b64_tr_b16 v[114:115], v7 offset:64\ns_waitcnt lgkmcnt(6)\nv_mfma_f32_16x16x32_f16 v[28:31], v[68:71], v[40:43], v[28:31]\n" \
  "ds_read2_b32 v[100:101], v8 offset0:48 offset1:49\nds_read2_b32 v[102:103], v8 offset0:50 offset1:51\n" \
  "v_mfma_f32_16x16x32_f16 v[28:31], v[60:63], v[96:99], v[28:31]\nv_pk_add_u16 v33, v33, 4 op_sel_hi:[1,0] clamp\n" \
  "ds_write_b64 v4, v[32:33] offset:3072\nv_mfma_f32_16x16x32_f16 v[28:31], v[44:47], v[116:119], v[28:31]\n" \
  "v_mfma_f32_16x16x32_f16 v[28:31], v[88:91], v[52:55], v[28:31]\nv_mfma_f32_16x16x32_f16 v[28:31], v[48:51], v[36:39], v[28:31]\n" \
  "s_waitcnt lgkmcnt(5)\nv_mfma_f32_16x16x32_f16 v[28:31], v[80:83], v[56:59], v[28:31]\n" \
  "v_mfma_f32_16x16x32_f16 v[28:31], v[108:111], v[120:123], v[28:31]\ns_waitcnt lgkmcnt(1)\n" \
  "v_mfma_f32_16x16x32_f16 v[28:31], v[112:115], v[100:103], v[28:31]\ns_nop 7\nv_cvt_pkrtz_f16_f32 v32, v28, v29\nv_cvt_pkrtz_f16_f32 v33, v30, v31\n" \
  "v_pk_add_u16 v32, v32, 4 op_sel_hi:[1,0] clamp\nv_pk_add_u16 v33, v33, 4 op_sel_hi:[1,0] clamp\nds_write_b64 v4, v[32:33] offset:4096\n" \
  "s_setprio 0\ns_setpc_b64 s[90:91]\n21:\ns_movk_i32 s84, 0x4000\n23:\nds_read_b32 v35, v9 offset:32\ns_sub_u32 s84, s84, 1\ns_cbranch_scc1 22b\n" \
  "s_waitcnt lgkmcnt(0)\nv_readfirstlane_b32 s85, v35\ns_cmp_lt_u32 s85, 4\ns_cbranch_scc1 23b\ns_branch 22b\n1:\n" \
  "ds_read_b64 v[124:125], v3 offset:1088\nds_read_b64 v[126:127], v3 offset:1096\nds_read_b64 v[120:121], v3 offset:0\n" \
  "ds_read_b64 v[122:123], v3 offset:8\nds_read_b64 v[116:117], v3 offset:64\nds_read_b64 v[118:119], v3 offset:72\n" \
  "ds_read_b64 v[112:113], v3 offset:128\nds_read_b64 v[114:115], v3 offset:136\nds_read_b64 v[108:109], v3 offset:192\n" \
  "ds_read_b64 v[110:111], v3 offset:200\nds_read_b64 v[104:105], v3 offset:256\nds_read_b64 v[106:107], v3 offset:264\n" \
  "ds_read_b64 v[100:101], v3 offset:320\nds_read_b64 v[102:103], v3 offset:328\nds_read_b64 v[96:97], v3 offset:384\n" \
  "ds_read_b64 v[98:99], v3 offset:392\nds_read_b64 v[92:93], v3 offset:448\nds_read_b64 v[94:95], v3 offset:456\n" \
  "ds_read_b64 v[88:89], v3 offset:512\nds_read_b64 v[90:91], v3 offset:520\nds_read_b64 v[84:85], v3 offset:576\n" \
  "ds_read_b64 v[86:87], v3 offset:584\nds_read_b64 v[80:81], v3 offset:640\nds_read_b64 v[82:83], v3 offset:648\n" \
  "ds_read_b64 v[76:77], v3 offset:704\nds_read_b64 v[78:79], v3 offset:712\nds_read_b64 v[72:73], v3 offset:768\n" \
  "ds_read_b64 v[74:75], v3 offset:776\nds_read_b64 v[68:69], v3 offset:832\nds_read_b64 v[70:71], v3 offset:840\n" \
  "ds_read_b64 v[64:65], v3 offset:896\nds_read_b64 v[66:67], v3 offset:904\nds_read_b64 v[60:61], v3 offset:960\n" \
  "ds_read_b64 v[62:63], v3 offset:968\nds_read_b64 v[56:57], v3 offset:1024\nds_read_b64 v[58:59], v3 offset:1032\n" \
  "ds_read_b64 v[52:53], v6 offset:0\nds_read_b64 v[54:55], v6 offset:8\nds_read_b64 v[48:49], v6 offset:64\nds_read_b64 v[50:51], v6 offset:72\n" \
  "s_waitcnt lgkmcnt(0)\ns_barrier\nds_read_b128 v[28:31], v2 offset:960\nds_read_b128 v[32:35], v2 offset:1472\n" \
  "ds_read_b128 v[36:39], v2 offset:1984\nds_read_b128 v[40:43], v2 offset:2048\nds_read_b128 v[44:47], v2 offset:2112\ns_waitcnt lgkmcnt(3)\n" \
  "v_mfma_f32_16x16x32_f16 v[12:15], v[124:127], v[28:31], 0\ns_setprio 1\nds_read_b128 v[28:31], v2 offset:2176\n" \
  "v_mfma_f32_16x16x32_f16 v[16:19], v[124:127], v[32:35], 0\nds_read_b128 v[32:35], v2 offset:2240\ns_waitcnt lgkmcnt(3)\n" \
  "v_mfma_f32_16x16x32_f16 v[12:15], v[120:123], v[36:39], v[12:15]\nv_mfma_f32_16x16x32_f16 v[20:23], v[124:127], v[36:39], 0\n" \
  "ds_read_b128 v[124:127], v2 offset:2304\nds_read_b128 v[36:39], v2 offset:2368\nv_mfma_f32_16x16x32_f16 v[12:15], v[116:119], v[40:43], v[12:15]\n" \
  "ds_read_b128 v[40:43], v2 offset:2432\ns_waitcnt lgkmcnt(4)\nv_mfma_f32_16x16x32_f16 v[12:15], v[112:115], v[44:47], v[12:15]\n" \
  "ds_read_b128 v[44:47], v2 offset:2496\nv_mfma_f32_16x16x32_f16 v[12:15], v[108:111], v[28:31], v[12:15]\nds_read_b128 v[28:31], v2 offset:2560\n" \
  "s_waitcnt lgkmcnt(4)\nv_mfma_f32_16x16x32_f16 v[12:15], v[104:107], v[32:35], v[12:15]\nds_read_b128 v[32:35], v2 offset:2624\n" \
  "v_mfma_f32_16x16x32_f16 v[12:15], v[100:103], v[124:127], v[12:15]\ns_waitcnt lgkmcnt(3)\n" \
  "v_mfma_f32_16x16x32_f16 v[12:15], v[96:99], v[36:39], v[12:15]\nds_read_b128 v[124:127], v2 offset:2688\n" \
  "v_mfma_f32_16x16x32_f16 v[12:15], v[92:95], v[40:43], v[12:15]\ns_waitcnt lgkmcnt(3)\n" \
  "v_mfma_f32_16x16x32_f16 v[12:15], v[88:91], v[44:47], v[12:15]\nds_read_b128 v[36:39], v2 offset:2752\n" \
  "v_mfma_f32_16x16x32_f16 v[16:19], v[120:123], v[44:47], v[16:19]\ns_waitcnt lgkmcnt(3)\n" \
  "v_mfma_f32_16x16x32_f16 v[12:15], v[84:87], v[28:31], v[12:15]\nds_read_b128 v[40:43], v2 offset:2816\n" \
  "v_mfma_f32_16x16x32_f16 v[16:19], v[116:119], v[28:31], v[16:19]\ns_waitcnt lgkmcnt(3)\n" \
  "v_mfma_f32_16x16x32_f16 v[12:15], v[80:83], v[32:35], v[12:15]\nds_read_b128 v[44:47], v2 offset:2880\n" \
  "v_mfma_f32_16x16x32_f16 v[16:19], v[112:115], v[32:35], v[16:19]\ns_waitcnt lgkmcnt(3)\n" \
  "v_mfma_f32_16x16x32_f16 v[12:15], v[76:79], v[124:127], v[12:15]\nds_read_b128 v[28:31], v2 offset:2944\n" \
  "v_mfma_f32_16x16x32_f16 v[16:19], v[108:111], v[124:127], v[16:19]\ns_waitcnt lgkmcnt(3)\n" \
  "v_mfma_f32_16x16x32_f16 v[12:15], v[72:75], v[36:39], v[12:15]\nds_read_b128 v[32:35], v2 offset:3008\n" \
  "v_mfma_f32_16x16x32_f16 v[16:19], v[104:107], v[36:39], v[16:19]\ns_waitcnt lgkmcnt(3)\n" \
  "v_mfma_f32_16x16x32_f16 v[12:15], v[68:71], v[40:43], v[12:15]\nv_mfma_f32_16x16x32_f16 v[16:19], v[100:103], v[40:43], v[16:19]\n" \
  "ds_read_b128 v[124:127], v2 offset:3072\ns_waitcnt lgkmcnt(3)\nv_mfma_f32_16x16x32_f16 v[12:15], v[64:67], v[44:47], v[12:15]\n" \
  "v_mfma_f32_16x16x32_f16 v[16:19], v[96:99], v[44:47], v[16:19]\nds_read_b128 v[36:39], v2 offset:3136\ns_waitcnt lgkmcnt(3)\n" \
  "v_mfma_f32_16x16x32_f16 v[12:15], v[60:63], v[28:31], v[12:15]\nds_read_b64 v[40:41], v6 offset:128\nds_read_b64 v[42:43], v6 offset:136\n" \
  "v_mfma_f32_16x16x32_f16 v[16:19], v[92:95], v[28:31], v[16:19]\nds_read_b128 v[44:47], v2 offset:3200\ns_waitcnt lgkmcnt(4)\n" \
  "v_mfma_f32_16x16x32_f16 v[12:15], v[56:59], v[32:35], v[12:15]\nv_mfma_f32_16x16x32_f16 v[16:19], v[88:91], v[32:35], v[16:19]\n" \
  "ds_read_b128 v[28:31], v2 offset:3264\nv_mfma_f32_16x16x32_f16 v[20:23], v[120:123], v[32:35], v[20:23]\nds_read_b64 v[120:121], v6 offset:192\n" \
  "ds_read_b64 v[122:123], v6 offset:200\nv_mfma_f32_16x16x32_f16 v[16:19], v[84:87], v[124:127], v[16:19]\nds_read_b128 v[32:35], v2 offset:3328\n" \
  "v_mfma_f32_16x16x32_f16 v[20:23], v[116:119], v[124:127], v[20:23]\nds_read_b64 v[116:117], v6 offset:256\nds_read_b64 v[118:119], v6 offset:264\n" \
  "s_waitcnt lgkmcnt(6)\nv_mfma_f32_16x16x32_f16 v[16:19], v[80:83], v[36:39], v[16:19]\nds_read_b128 v[124:127], v2 offset:3392\n" \
  "v_mfma_f32_16x16x32_f16 v[20:23], v[112:115], v[36:39], v[20:23]\nds_read_b64 v[112:113], v6 offset:320\nds_read_b64 v[114:115], v6 offset:328\n" \
  "v_mfma_f32_16x16x32_f16 v[16:19], v[76:79], v[44:47], v[16:19]\nds_read_b128 v[36:39], v2 offset:3456\n" \
  "v_mfma_f32_16x16x32_f16 v[20:23], v[108:111], v[44:47], v[20:23]\ns_waitcnt lgkmcnt(6)\n" \
  "v_mfma_f32_16x16x32_f16 v[16:19], v[72:75], v[28:31], v[16:19]\nds_read_b128 v[108:111], v2 offset:3520\n" \
  "v_mfma_f32_16x16x32_f16 v[20:23], v[104:107], v[28:31], v[20:23]\nv_mfma_f32_16x16x32_f16 v[16:19], v[68:71], v[32:35], v[16:19]\n" \
  "ds_read_b128 v[44:47], v2 offset:3584\nv_mfma_f32_16x16x32_f16 v[20:23], v[100:103], v[32:35], v[20:23]\nds_read_b128 v[104:107], v2 offset:3648\n" \
  "ds_read_b32 v27, v9 offset:32\ns_waitcnt lgkmcnt(4)\nv_mfma_f32_16x16x32_f16 v[16:19], v[64:67], v[124:127], v[16:19]\n" \
  "ds_read_b128 v[28:31], v2 offset:3712\nv_mfma_f32_16x16x32_f16 v[20:23], v[96:99], v[124:127], v[20:23]\nds_read_b128 v[100:103], v2 offset:3776\n" \
  "ds_read2_b32 v[32:33], v8 offset0:32 offset1:33\nds_read2_b32 v[34:35], v8 offset0:34 offset1:35\n" \
  "v_mfma_f32_16x16x32_f16 v[16:19], v[60:63], v[36:39], v[16:19]\nds_read_b128 v[96:99], v2 offset:3840\n" \
  "v_mfma_f32_16x16x32_f16 v[20:23], v[92:95], v[36:39], v[20:23]\nds_read_b128 v[124:127], v2 offset:3904\ns_waitcnt lgkmcnt(8)\n" \
  "v_mfma_f32_16x16x32_f16 v[16:19], v[56:59], v[108:111], v[16:19]\nds_read_b128 v[92:95], v2 offset:3968\ns_waitcnt lgkmcnt(7)\n" \
  "v_readfirstlane_b32 s85, v27\ns_cmp_lt_u32 s85, 4\ns_cbranch_scc1 31f\n32:\nv_mfma_f32_16x16x32_f16 v[20:23], v[88:91], v[108:111], v[20:23]\n" \
  "ds_read_b128 v[36:39], v2 offset:4032\nds_read_b64_tr_b16 v[88:89], v5 offset:512\nds_read_b64_tr_b16 v[90:91], v5 offset:544\n" \
  "v_mfma_f32_16x16x32_f16 v[20:23], v[84:87], v[44:47], v[20:23]\nds_read_b64_tr_b16 v[108:109], v5 offset:768\n" \
  "ds_read_b64_tr_b16 v[110:111], v5 offset:800\nv_mfma_f32_16x16x32_f16 v[20:23], v[80:83], v[104:107], v[20:23]\n" \
  "ds_read_b64_tr_b16 v[84:85], v5 offset:1024\nds_read_b64_tr_b16 v[86:87], v5 offset:1056\ns_waitcnt lgkmcnt(12)\n" \
  "v_mfma_f32_16x16x32_f16 v[20:23], v[76:79], v[28:31], v[20:23]\nds_read_b64_tr_b16 v[44:45], v5 offset:1280\n" \
  "ds_read_b64_tr_b16 v[46:47], v5 offset:1312\nv_mfma_f32_16x16x32_f16 v[20:23], v[72:75], v[100:103], v[20:23]\ns_waitcnt lgkmcnt(10)\n" \
  "v_mfma_f32_16x16x32_f16 v[20:23], v[68:71], v[96:99], v[20:23]\nds_read_b64_tr_b16 v[80:81], v5 offset:1536\n" \
  "ds_read_b64_tr_b16 v[82:83], v5 offset:1568\nv_mfma_f32_16x16x32_f16 v[20:23], v[64:67], v[124:127], v[20:23]\n" \
  "ds_read_b64_tr_b16 v[104:105], v5 offset:1792\nds_read_b64_tr_b16 v[106:107], v5 offset:1824\ns_waitcnt lgkmcnt(12)\n" \
  "v_mfma_f32_16x16x32_f16 v[20:23], v[60:63], v[92:95], v[20:23]\nds_read_b64_tr_b16 v[76:77], v5 offset:2048\n" \
  "ds_read_b64_tr_b16 v[78:79], v5 offset:2080\nv_mfma_f32_16x16x32_f16 v[20:23], v[56:59], v[36:39], v[20:23]\ns_waitcnt lgkmcnt(10)\n" \
  "v_mfma_f32_16x16x32_f16 v[12:15], v[88:91], v[52:55], v[12:15]\nds_read_b64_tr_b16 v[28:29], v5 offset:2304\n" \
  "ds_read_b64_tr_b16 v[30:31], v5 offset:2336\nv_mfma_f32_16x16x32_f16 v[12:15], v[108:111], v[48:51], v[12:15]\n" \
  "ds_read_b64_tr_b16 v[72:73], v5 offset:2560\nds_read_b64_tr_b16 v[74:75], v5 offset:2592\ns_waitcnt lgkmcnt(10)\n" \
  "v_mfma_f32_16x16x32_f16 v[12:15], v[84:87], v[40:43], v[12:15]\nds_read_b64_tr_b16 v[100:101], v5 offset:2816\n" \
  "ds_read_b64_tr_b16 v[102:103], v5 offset:2848\nv_mfma_f32_16x16x32_f16 v[16:19], v[84:87], v[52:55], v[16:19]\n" \
  "ds_read_b64_tr_b16 v[68:69], v7 offset:0\nds_read_b64_tr_b16 v[70:71], v7 offset:64\n" \
  "v_mfma_f32_16x16x32_f16 v[12:15], v[44:47], v[120:123], v[12:15]\nv_mfma_f32_16x16x32_f16 v[16:19], v[44:47], v[48:51], v[16:19]\n" \
  "s_waitcnt lgkmcnt(10)\nv_mfma_f32_16x16x32_f16 v[12:15], v[80:83], v[116:119], v[12:15]\nds_read2_b32 v[96:97], v8 offset0:16 offset1:17\n" \
  "ds_read2_b32 v[98:99], v8 offset0:18 offset1:19\nv_mfma_f32_16x16x32_f16 v[16:19], v[80:83], v[40:43], v[16:19]\n" \
  "ds_read2_b32 v[64:65], v8 offset0:0 offset1:1\nds_read2_b32 v[66:67], v8 offset0:2 offset1:3\n" \
  "v_mfma_f32_16x16x32_f16 v[20:23], v[80:83], v[52:55], v[20:23]\nv_mfma_f32_16x16x32_f16 v[12:15], v[104:107], v[112:115], v[12:15]\n" \
  "v_mfma_f32_16x16x32_f16 v[16:19], v[104:107], v[120:123], v[16:19]\nv_mfma_f32_16x16x32_f16 v[20:23], v[104:107], v[48:51], v[20:23]\n" \
  "s_waitcnt lgkmcnt(10)\nv_mfma_f32_16x16x32_f16 v[16:19], v[76:79], v[116:119], v[16:19]\nds_read_b64_tr_b16 v[124:125], v7 offset:512\n" \
  "ds_read_b64_tr_b16 v[126:127], v7 offset:576\nv_mfma_f32_16x16x32_f16 v[20:23], v[76:79], v[40:43], v[20:23]\n" \
  "ds_read2_b32 v[60:61], v8 offset0:48 offset1:49\nds_read2_b32 v[62:63], v8 offset0:50 offset1:51\n" \
  "v_mfma_f32_16x16x32_f16 v[16:19], v[28:31], v[112:115], v[16:19]\nv_mfma_f32_16x16x32_f16 v[20:23], v[28:31], v[120:123], v[20:23]\n" \
  "s_waitcnt lgkmcnt(10)\nv_mfma_f32_16x16x32_f16 v[20:23], v[72:75], v[116:119], v[20:23]\nds_read_b64_tr_b16 v[92:93], v7 offset:1024\n" \
  "ds_read_b64_tr_b16 v[94:95], v7 offset:1088\nv_mfma_f32_16x16x32_f16 v[20:23], v[100:103], v[112:115], v[20:23]\n" \
  "ds_read_b64_tr_b16 v[56:57], v7 offset:1536\nds_read_b64_tr_b16 v[58:59], v7 offset:1600\ns_waitcnt lgkmcnt(10)\n" \
  "v_mfma_f32_16x16x32_f16 v[12:15], v[68:71], v[32:35], v[12:15]\nv_mfma_f32_16x16x32_f16 v[16:19], v[68:71], v[96:99], v[16:19]\n" \
  "s_waitcnt lgkmcnt(6)\nv_mfma_f32_16x16x32_f16 v[20:23], v[68:71], v[64:67], v[20:23]\ns_waitcnt lgkmcnt(2)\n" \
  "v_mfma_f32_16x16x32_f16 v[12:15], v[124:127], v[60:63], v[12:15]\nv_mfma_f32_16x16x32_f16 v[16:19], v[124:127], v[32:35], v[16:19]\n" \
  "v_mfma_f32_16x16x32_f16 v[20:23], v[124:127], v[96:99], v[20:23]\nv_mfma_f32_16x16x32_f16 v[16:19], v[92:95], v[60:63], v[16:19]\n" \
  "v_mfma_f32_16x16x32_f16 v[20:23], v[92:95], v[32:35], v[20:23]\ns_waitcnt lgkmcnt(0)\n" \
  "v_mfma_f32_16x16x32_f16 v[20:23], v[56:59], v[60:63], v[20:23]\ns_nop 1\nv_cvt_pkrtz_f16_f32 v24, v12, v13\nv_cvt_pkrtz_f16_f32 v25, v14, v15\n" \
  "v_pk_add_u16 v24, v24, 4 op_sel_hi:[1,0] clamp\nv_pk_add_u16 v25, v25, 4 op_sel_hi:[1,0] clamp\nds_write_b64 v4, v[24:25] offset:5120\n" \
  "v_cvt_pkrtz_f16_f32 v24, v16, v17\nv_cvt_pkrtz_f16_f32 v25, v18, v19\nv_pk_add_u16 v24, v24, 4 op_sel_hi:[1,0] clamp\n" \
  "v_pk_add_u16 v25, v25, 4 op_sel_hi:[1,0] clamp\nds_write_b64 v4, v[24:25] offset:6144\nv_cvt_pkrtz_f16_f32 v24, v20, v21\n" \
  "v_cvt_pkrtz_f16_f32 v25, v22, v23\nv_pk_add_u16 v24, v24, 4 op_sel_hi:[1,0] clamp\nv_pk_add_u16 v25, v25, 4 op_sel_hi:[1,0] clamp\n" \
  "ds_write_b64 v4, v[24:25] offset:7168\ns_setprio 0\ns_setpc_b64 s[90:91]\n31:\ns_movk_i32 s84, 0x4000\n33:\nds_read_b32 v27, v9 offset:32\n" \
  "s_sub_u32 s84, s84, 1\ns_cbranch_scc1 32b\ns_waitcnt lgkmcnt(0)\nv_readfirstlane_b32 s85, v27\ns_cmp_lt_u32 s85, 4\ns_cbranch_scc1 33b\n" \
  "s_branch 32b\n9:\ns_cmp_eq_u32 s89, 3\ns_cbranch_scc1 3f\ns_cmp_eq_u32 s89, 4\ns_cbranch_scc1 4f\ns_cmp_eq_u32 s89, 5\ns_cbranch_scc1 5f\n" \
  "ds_read_b64 v[124:125], v3 offset:576\nds_read_b64 v[126:127], v3 offset:584\nds_read_b64 v[120:121], v3 offset:64\n" \
  "ds_read_b64 v[122:123], v3 offset:72\nds_read_b64 v[116:117], v3 offset:640\nds_read_b64 v[118:119], v3 offset:648\n" \
  "ds_read_b64 v[112:113], v3 offset:128\nds_read_b64 v[114:115], v3 offset:136\nds_read_b64 v[108:109], v3 offset:704\n" \
  "ds_read_b64 v[110:111], v3 offset:712\nds_read_b64 v[104:105], v3 offset:192\nds_read_b64 v[106:107], v3 offset:200\n" \
  "ds_read_b64 v[100:101], v3 offset:768\nds_read_b64 v[102:103], v3 offset:776\nds_read_b64 v[96:97], v3 offset:256\n" \
  "ds_read_b64 v[98:99], v3 offset:264\nds_read_b64 v[92:93], v3 offset:832\nds_read_b64 v[94:95], v3 offset:840\n" \
  "ds_read_b64 v[88:89], v3 offset:320\nds_read_b64 v[90:91], v3 offset:328\nds_read_b64 v[84:85], v3 offset:896\n" \
  "ds_read_b64 v[86:87], v3 offset:904\nds_read_b64 v[80:81], v3 offset:384\nds_read_b64 v[82:83], v3 offset:392\n" \
  "ds_read_b64 v[76:77], v3 offset:960\nds_read_b64 v[78:79], v3 offset:968\nds_read_b64 v[72:73], v3 offset:448\n" \
  "ds_read_b64 v[74:75], v3 offset:456\nds_read_b64 v[68:69], v3 offset:1024\nds_read_b64 v[70:71], v3 offset:1032\n" \
  "ds_read_b64 v[64:65], v3 offset:512\nds_read_b64 v[66:67], v3 offset:520\ns_waitcnt lgkmcnt(0)\ns_barrier\nds_read_b128 v[24:27], v2 offset:0\n" \
  "ds_read_b128 v[28:31], v2 offset:64\nds_read_b128 v[32:35], v2 offset:128\nds_read_b128 v[36:39], v2 offset:192\ns_waitcnt lgkmcnt(3)\n" \
  "v_mfma_f32_16x16x32_f16 v[12:15], v[124:127], v[24:27], 0\ns_setprio 1\nds_read_b128 v[40:43], v2 offset:256\n" \
  "v_mfma_f32_16x16x32_f16 v[16:19], v[120:123], v[24:27], 0\ns_waitcnt lgkmcnt(3)\nv_mfma_f32_16x16x32_f16 v[12:15], v[116:119], v[28:31], v[12:15]\n" \
  "ds_read_b128 v[44:47], v2 offset:320\nv_mfma_f32_16x16x32_f16 v[16:19], v[112:115], v[28:31], v[16:19]\ns_waitcnt lgkmcnt(3)\n" \
  "v_mfma_f32_16x16x32_f16 v[12:15], v[108:111], v[32:35], v[12:15]\nds_read_b128 v[48:51], v2 offset:384\n" \
  "v_mfma_f32_16x16x32_f16 v[16:19], v[104:107], v[32:35], v[16:19]\ns_waitcnt lgkmcnt(3)\n" \
  "v_mfma_f32_16x16x32_f16 v[12:15], v[100:103], v[36:39], v[12:15]\nds_read_b128 v[52:55], v2 offset:448\n" \
  "v_mfma_f32_16x16x32_f16 v[16:19], v[96:99], v[36:39], v[16:19]\ns_waitcnt lgkmcnt(3)\n" \
  "v_mfma_f32_16x16x32_f16 v[12:15], v[92:95], v[40:43], v[12:15]\nds_read_b128 v[56:59], v2 offset:512\n" \
  "v_mfma_f32_16x16x32_f16 v[16:19], v[88:91], v[40:43], v[16:19]\nds_read_b128 v[60:63], v2 offset:576\ns_waitcnt lgkmcnt(3)\n" \
  "v_mfma_f32_16x16x32_f16 v[12:15], v[84:87], v[44:47], v[12:15]\nds_read_b128 v[120:123], v2 offset:640\n" \
  "v_mfma_f32_16x16x32_f16 v[16:19], v[80:83], v[44:47], v[16:19]\nds_read_b128 v[24:27], v2 offset:704\n" \
  "v_mfma_f32_16x16x32_f16 v[12:15], v[76:79], v[48:51], v[12:15]\nds_read_b128 v[112:115], v2 offset:768\n" \
  "v_mfma_f32_16x16x32_f16 v[16:19], v[72:75], v[48:51], v[16:19]\nds_read_b128 v[28:31], v2 offset:832\ns_waitcnt lgkmcnt(5)\n" \
  "v_mfma_f32_16x16x32_f16 v[12:15], v[68:71], v[52:55], v[12:15]\nds_read_b128 v[104:107], v2 offset:896\n" \
  "v_mfma_f32_16x16x32_f16 v[16:19], v[64:67], v[52:55], v[16:19]\nds_read_b128 v[32:35], v2 offset:960\n" \
  "v_mfma_f32_16x16x32_f16 v[16:19], v[124:127], v[56:59], v[16:19]\ns_waitcnt lgkmcnt(5)\n" \
  "v_mfma_f32_16x16x32_f16 v[16:19], v[116:119], v[60:63], v[16:19]\nv_mfma_f32_16x16x32_f16 v[16:19], v[108:111], v[120:123], v[16:19]\n" \
  "s_waitcnt lgkmcnt(3)\nv_mfma_f32_16x16x32_f16 v[16:19], v[100:103], v[24:27], v[16:19]\nv_cvt_pkrtz_f16_f32 v20, v12, v13\n" \
  "v_cvt_pkrtz_f16_f32 v21, v14, v15\nv_mfma_f32_16x16x32_f16 v[16:19], v[92:95], v[112:115], v[16:19]\n" \
  "v_pk_add_u16 v20, v20, 4 op_sel_hi:[1,0] clamp\ns_waitcnt lgkmcnt(2)\nv_mfma_f32_16x16x32_f16 v[16:19], v[84:87], v[28:31], v[16:19]\n" \
  "v_pk_add_u16 v21, v21, 4 op_sel_hi:[1,0] clamp\ns_waitcnt lgkmcnt(1)\nv_mfma_f32_16x16x32_f16 v[16:19], v[76:79], v[104:107], v[16:19]\n" \
  "ds_write_b64 v4, v[20:21] offset:0\ns_waitcnt lgkmcnt(1)\nv_mfma_f32_16x16x32_f16 v[16:19], v[68:71], v[32:35], v[16:19]\ns_nop 7\n" \
  "v_cvt_pkrtz_f16_f32 v20, v16, v17\nv_cvt_pkrtz_f16_f32 v21, v18, v19\nv_pk_add_u16 v20, v20, 4 op_sel_hi:[1,0] clamp\n" \
  "v_pk_add_u16 v21, v21, 4 op_sel_hi:[1,0] clamp\nds_write_b64 v4, v[20:21] offset:1024\ns_setprio 0\ns_setpc_b64 s[90:91]\n3:\n" \
  "ds_read_b64 v[124:125], v3 offset:0\nds_read_b64 v[126:127], v3 offset:8\nds_read_b64 v[120:121], v3 offset:64\n" \
  "ds_read_b64 v[122:123], v3 offset:72\nds_read_b64 v[116:117], v3 offset:128\nds_read_b64 v[118:119], v3 offset:136\n" \
  "ds_read_b64 v[112:113], v3 offset:192\nds_read_b64 v[114:115], v3 offset:200\nds_read_b64 v[108:109], v3 offset:256\n" \
  "ds_read_b64 v[110:111], v3 offset:264\nds_read_b64 v[104:105], v3 offset:320\nds_read_b64 v[106:107], v3 offset:328\n" \
  "ds_read_b64 v[100:101], v3 offset:384\nds_read_b64 v[102:103], v3 offset:392\nds_read_b64 v[96:97], v3 offset:448\n" \
  "ds_read_b64 v[98:99], v3 offset:456\nds_read_b64 v[92:93], v3 offset:512\nds_read_b64 v[94:95], v3 offset:520\n" \
  "ds_read_b64 v[88:89], v3 offset:576\nds_read_b64 v[90:91], v3 offset:584\nds_read_b64 v[84:85], v3 offset:640\n" \
  "ds_read_b64 v[86:87], v3 offset:648\nds_read_b64 v[80:81], v3 offset:704\nds_read_b64 v[82:83], v3 offset:712\n" \
  "ds_read_b64 v[76:77], v3 offset:768\nds_read_b64 v[78:79], v3 offset:776\nds_read_b64 v[72:73], v3 offset:832\n" \
  "ds_read_b64 v[74:75], v3 offset:840\nds_read_b64 v[68:69], v3 offset:896\nds_read_b64 v[70:71], v3 offset:904\n" \
  "ds_read_b64 v[64:65], v3 offset:960\nds_read_b64 v[66:67], v3 offset:968\nds_read_b64 v[60:61], v3 offset:1024\n" \
  "ds_read_b64 v[62:63], v3 offset:1032\nds_read_b64 v[56:57], v6 offset:256\nds_read_b64 v[58:59], v6 offset:264\n" \
  "ds_read_b64 v[52:53], v6 offset:128\nds_read_b64 v[54:55], v6 offset:136\nds_read_b64 v[48:49], v6 offset:320\n" \
  "ds_read_b64 v[50:51], v6 offset:328\nds_read_b64 v[44:45], v6 offset:192\nds_read_b64 v[46:47], v6 offset:200\ns_waitcnt lgkmcnt(0)\ns_barrier\n" \
  "ds_read_b128 v[24:27], v2 offset:448\nds_read_b128 v[28:31], v2 offset:512\nds_read_b128 v[32:35], v2 offset:576\n" \
  "ds_read_b128 v[36:39], v2 offset:640\nds_read_b128 v[40:43], v2 offset:704\ns_waitcnt lgkmcnt(3)\n" \
  "v_mfma_f32_16x16x32_f16 v[12:15], v[124:127], v[24:27], 0\ns_setprio 1\nds_read_b128 v[24:27], v2 offset:768\n" \
  "v_mfma_f32_16x16x32_f16 v[12:15], v[120:123], v[28:31], v[12:15]\nds_read_b128 v[28:31], v2 offset:832\ns_waitcnt lgkmcnt(3)\n" \
  "v_mfma_f32_16x16x32_f16 v[12:15], v[116:119], v[32:35], v[12:15]\nds_read_b128 v[32:35], v2 offset:896\n" \
  "v_mfma_f32_16x16x32_f16 v[12:15], v[112:115], v[36:39], v[12:15]\nds_read_b128 v[36:39], v2 offset:960\ns_waitcnt lgkmcnt(3)\n" \
  "v_mfma_f32_16x16x32_f16 v[12:15], v[108:111], v[40:43], v[12:15]\nds_read_b128 v[40:43], v2 offset:1024\n" \
  "v_mfma_f32_16x16x32_f16 v[12:15], v[104:107], v[24:27], v[12:15]\nds_read_b128 v[24:27], v2 offset:1088\ns_waitcnt lgkmcnt(3)\n" \
  "v_mfma_f32_16x16x32_f16 v[12:15], v[100:103], v[28:31], v[12:15]\nds_read_b128 v[28:31], v2 offset:1152\n" \
  "v_mfma_f32_16x16x32_f16 v[12:15], v[96:99], v[32:35], v[12:15]\ns_waitcnt lgkmcnt(3)\n" \
  "v_mfma_f32_16x16x32_f16 v[12:15], v[92:95], v[36:39], v[12:15]\nds_read_b128 v[32:35], v2 offset:1216\n" \
  "v_mfma_f32_16x16x32_f16 v[16:19], v[124:127], v[36:39], 0\ns_waitcnt lgkmcnt(3)\nv_mfma_f32_16x16x32_f16 v[12:15], v[88:91], v[40:43], v[12:15]\n" \
  "ds_read_b128 v[124:127], v2 offset:1280\nv_mfma_f32_16x16x32_f16 v[16:19], v[120:123], v[40:43], v[16:19]\ns_waitcnt lgkmcnt(3)\n" \
  "v_mfma_f32_16x16x32_f16 v[12:15], v[84:87], v[24:27], v[12:15]\nds_read_b128 v[36:39], v2 offset:1344\n" \
  "v_mfma_f32_16x16x32_f16 v[16:19], v[116:119], v[24:27], v[16:19]\ns_waitcnt lgkmcnt(3)\n" \
  "v_mfma_f32_16x16x32_f16 v[12:15], v[80:83], v[28:31], v[12:15]\nds_read_b128 v[120:123], v2 offset:1408\n" \
  "v_mfma_f32_16x16x32_f16 v[16:19], v[112:115], v[28:31], v[16:19]\ns_waitcnt lgkmcnt(3)\n" \
  "v_mfma_f32_16x16x32_f16 v[12:15], v[76:79], v[32:35], v[12:15]\nds_read_b128 v[40:43], v2 offset:1472\n" \
  "v_mfma_f32_16x16x32_f16 v[16:19], v[108:111], v[32:35], v[16:19]\ns_waitcnt lgkmcnt(3)\n" \
  "v_mfma_f32_16x16x32_f16 v[12:15], v[72:75], v[124:127], v[12:15]\nds_read_b128 v[116:119], v2 offset:1536\n" \
  "v_mfma_f32_16x16x32_f16 v[16:19], v[104:107], v[124:127], v[16:19]\nds_read_b128 v[24:27], v2 offset:1600\ns_waitcnt lgkmcnt(3)\n" \
  "v_mfma_f32_16x16x32_f16 v[12:15], v[68:71], v[36:39], v[12:15]\nds_read_b128 v[112:115], v2 offset:1664\nds_read_b32 v23, v9 offset:32\n" \
  "v_mfma_f32_16x16x32_f16 v[16:19], v[100:103], v[36:39], v[16:19]\nds_read_b128 v[28:31], v2 offset:1728\n" \
  "v_mfma_f32_16x16x32_f16 v[12:15], v[64:67], v[120:123], v[12:15]\nds_read_b128 v[108:111], v2 offset:1792\n" \
  "v_mfma_f32_16x16x32_f16 v[16:19], v[96:99], v[120:123], v[16:19]\nds_read_b128 v[32:35], v2 offset:1856\ns_waitcnt lgkmcnt(6)\n" \
  "v_mfma_f32_16x16x32_f16 v[12:15], v[60:63], v[40:43], v[12:15]\nds_read_b128 v[104:107], v2 offset:1920\n" \
  "v_mfma_f32_16x16x32_f16 v[16:19], v[92:95], v[40:43], v[16:19]\nds_read_b128 v[124:127], v2 offset:1984\ns_waitcnt lgkmcnt(5)\n" \
  "v_readfirstlane_b32 s85, v23\ns_cmp_lt_u32 s85, 3\ns_cbranch_scc1 51f\n52:\nv_mfma_f32_16x16x32_f16 v[16:19], v[88:91], v[116:119], v[16:19]\n" \
  "ds_read_b64_tr_b16 v[100:101], v5 offset:0\nds_read_b64_tr_b16 v[102:103], v5 offset:32\n" \
  "v_mfma_f32_16x16x32_f16 v[16:19], v[84:87], v[24:27], v[16:19]\nds_read_b64_tr_b16 v[36:37], v5 offset:256\n" \
  "ds_read_b64_tr_b16 v[38:39], v5 offset:288\nv_mfma_f32_16x16x32_f16 v[16:19], v[80:83], v[112:115], v[16:19]\n" \
  "ds_read_b64_tr_b16 v[96:97], v5 offset:512\nds_read_b64_tr_b16 v[98:99], v5 offset:544\ns_waitcnt lgkmcnt(9)\n" \
  "v_mfma_f32_16x16x32_f16 v[16:19], v[76:79], v[28:31], v[16:19]\nds_read_b64_tr_b16 v[120:121], v5 offset:768\n" \
  "ds_read_b64_tr_b16 v[122:123], v5 offset:800\nv_mfma_f32_16x16x32_f16 v[16:19], v[72:75], v[108:111], v[16:19]\ns_waitcnt lgkmcnt(9)\n" \
  "v_mfma_f32_16x16x32_f16 v[16:19], v[68:71], v[32:35], v[16:19]\nv_mfma_f32_16x16x32_f16 v[16:19], v[64:67], v[104:107], v[16:19]\n" \
  "s_waitcnt lgkmcnt(6)\nv_mfma_f32_16x16x32_f16 v[16:19], v[60:63], v[124:127], v[16:19]\n" \
  "v_mfma_f32_16x16x32_f16 v[12:15], v[100:103], v[56:59], v[12:15]\nv_mfma_f32_16x16x32_f16 v[16:19], v[100:103], v[52:55], v[16:19]\n" \
  "s_waitcnt lgkmcnt(2)\nv_mfma_f32_16x16x32_f16 v[12:15], v[36:39], v[48:51], v[12:15]\n" \
  "v_mfma_f32_16x16x32_f16 v[16:19], v[36:39], v[44:47], v[16:19]\nv_mfma_f32_16x16x32_f16 v[16:19], v[96:99], v[56:59], v[16:19]\n" \
  "s_waitcnt lgkmcnt(0)\nv_mfma_f32_16x16x32_f16 v[16:19], v[120:123], v[48:51], v[16:19]\ns_nop 3\nv_cvt_pkrtz_f16_f32 v20, v12, v13\n" \
  "v_cvt_pkrtz_f16_f32 v21, v14, v15\nv_pk_add_u16 v20, v20, 4 op_sel_hi:[1,0] clamp\nv_pk_add_u16 v21, v21, 4 op_sel_hi:[1,0] clamp\n" \
  "ds_write_b64 v4, v[20:21] offset:2048\nv_cvt_pkrtz_f16_f32 v20, v16, v17\nv_cvt_pkrtz_f16_f32 v21, v18, v19\n" \
  "v_pk_add_u16 v20, v20, 4 op_sel_hi:[1,0] clamp\nv_pk_add_u16 v21, v21, 4 op_sel_hi:[1,0] clamp\nds_write_b64 v4, v[20:21] offset:3072\n" \
  "s_setprio 0\ns_setpc_b64 s[90:91]\n51:\ns_movk_i32 s84, 0x4000\n53:\nds_read_b32 v23, v9 offset:32\ns_sub_u32 s84, s84, 1\ns_cbranch_scc1 52b\n" \
  "s_waitcnt lgkmcnt(0)\nv_readfirstlane_b32 s85, v23\ns_cmp_lt_u32 s85, 3\ns_cbranch_scc1 53b\ns_branch 52b\n4:\n" \
  "ds_read_b64 v[124:125], v3 offset:1088\nds_read_b64 v[126:127], v3 offset:1096\nds_read_b64 v[120:121], v3 offset:0\n" \
  "ds_read_b64 v[122:123], v3 offset:8\nds_read_b64 v[116:117], v3 offset:64\nds_read_b64 v[118:119], v3 offset:72\n" \
  "ds_read_b64 v[112:113], v3 offset:128\nds_read_b64 v[114:115], v3 offset:136\nds_read_b64 v[108:109], v3 offset:192\n" \
  "ds_read_b64 v[110:111], v3 offset:200\nds_read_b64 v[104:105], v3 offset:256\nds_read_b64 v[106:107], v3 offset:264\n" \
  "ds_read_b64 v[100:101], v3 offset:320\nds_read_b64 v[102:103], v3 offset:328\nds_read_b64 v[96:97], v3 offset:384\n" \
  "ds_read_b64 v[98:99], v3 offset:392\nds_read_b64 v[92:93], v3 offset:448\nds_read_b64 v[94:95], v3 offset:456\n" \
  "ds_read_b64 v[88:89], v3 offset:512\nds_read_b64 v[90:91], v3 offset:520\nds_read_b64 v[84:85], v3 offset:576\n" \
  "ds_read_b64 v[86:87], v3 offset:584\nds_read_b64 v[80:81], v3 offset:640\nds_read_b64 v[82:83], v3 offset:648\n" \
  "ds_read_b64 v[76:77], v3 offset:704\nds_read_b64 v[78:79], v3 offset:712\nds_read_b64 v[72:73], v3 offset:768\n" \
  "ds_read_b64 v[74:75], v3 offset:776\nds_read_b64 v[68:69], v3 offset:832\nds_read_b64 v[70:71], v3 offset:840\n" \
  "ds_read_b64 v[64:65], v3 offset:896\nds_read_b64 v[66:67], v3 offset:904\nds_read_b64 v[60:61], v3 offset:960\n" \
  "ds_read_b64 v[62:63], v3 offset:968\nds_read_b64 v[56:57], v3 offset:1024\nds_read_b64 v[58:59], v3 offset:1032\n" \
  "ds_read_b64 v[52:53], v6 offset:0\nds_read_b64 v[54:55], v6 offset:8\nds_read_b64 v[48:49], v6 offset:64\nds_read_b64 v[50:51], v6 offset:72\n" \
  "ds_read_b64 v[44:45], v6 offset:128\nds_read_b64 v[46:47], v6 offset:136\nds_read_b64 v[40:41], v6 offset:192\n" \
  "ds_read_b64 v[42:43], v6 offset:200\ns_waitcnt lgkmcnt(0)\ns_barrier\nds_read_b128 v[24:27], v2 offset:448\nds_read_b128 v[28:31], v2 offset:960\n" \
  "ds_read_b128 v[32:35], v2 offset:1472\nds_read_b128 v[36:39], v2 offset:1536\ns_waitcnt lgkmcnt(3)\n" \
  "v_mfma_f32_16x16x32_f16 v[12:15], v[124:127], v[24:27], 0\ns_setprio 1\nds_read_b128 v[24:27], v2 offset:1600\ns_waitcnt lgkmcnt(3)\n" \
  "v_mfma_f32_16x16x32_f16 v[16:19], v[124:127], v[28:31], 0\nds_read_b128 v[124:127], v2 offset:1664\ns_waitcnt lgkmcnt(3)\n" \
  "v_mfma_f32_16x16x32_f16 v[12:15], v[120:123], v[32:35], v[12:15]\nds_read_b128 v[28:31], v2 offset:1728\ns_waitcnt lgkmcnt(3)\n" \
  "v_mfma_f32_16x16x32_f16 v[12:15], v[116:119], v[36:39], v[12:15]\nds_read_b128 v[32:35], v2 offset:1792\ns_waitcnt lgkmcnt(3)\n" \
  "v_mfma_f32_16x16x32_f16 v[12:15], v[112:115], v[24:27], v[12:15]\nds_read_b128 v[36:39], v2 offset:1856\ns_waitcnt lgkmcnt(3)\n" \
  "v_mfma_f32_16x16x32_f16 v[12:15], v[108:111], v[124:127], v[12:15]\nds_read_b128 v[24:27], v2 offset:1920\ns_waitcnt lgkmcnt(3)\n" \
  "v_mfma_f32_16x16x32_f16 v[12:15], v[104:107], v[28:31], v[12:15]\nds_read_b128 v[124:127], v2 offset:1984\ns_waitcnt lgkmcnt(3)\n" \
  "v_mfma_f32_16x16x32_f16 v[12:15], v[100:103], v[32:35], v[12:15]\nds_read_b128 v[28:31], v2 offset:2048\ns_waitcnt lgkmcnt(3)\n" \
  "v_mfma_f32_16x16x32_f16 v[12:15], v[96:99], v[36:39], v[12:15]\nds_read_b128 v[32:35], v2 offset:2112\ns_waitcnt lgkmcnt(3)\n" \
  "v_mfma_f32_16x16x32_f16 v[12:15], v[92:95], v[24:27], v[12:15]\nds_read_b128 v[36:39], v2 offset:2176\ns_waitcnt lgkmcnt(3)\n" \
  "v_mfma_f32_16x16x32_f16 v[12:15], v[88:91], v[124:127], v[12:15]\nds_read_b128 v[24:27], v2 offset:2240\n" \
  "v_mfma_f32_16x16x32_f16 v[16:19], v[120:123], v[124:127], v[16:19]\nds_read_b64 v[120:121], v6 offset:256\nds_read_b64 v[122:123], v6 offset:264\n" \
  "s_waitcnt lgkmcnt(4)\nv_mfma_f32_16x16x32_f16 v[12:15], v[84:87], v[28:31], v[12:15]\nds_read_b128 v[124:127], v2 offset:2304\n" \
  "v_mfma_f32_16x16x32_f16 v[16:19], v[116:119], v[28:31], v[16:19]\nds_read_b64 v[116:117], v6 offset:320\nds_read_b64 v[118:119], v6 offset:328\n" \
  "v_mfma_f32_16x16x32_f16 v[12:15], v[80:83], v[32:35], v[12:15]\nds_read_b128 v[28:31], v2 offset:2368\n" \
  "v_mfma_f32_16x16x32_f16 v[16:19], v[112:115], v[32:35], v[16:19]\ns_waitcnt lgkmcnt(6)\n" \
  "v_mfma_f32_16x16x32_f16 v[12:15], v[76:79], v[36:39], v[12:15]\nds_read_b128 v[112:115], v2 offset:2432\n" \
  "v_mfma_f32_16x16x32_f16 v[16:19], v[108:111], v[36:39], v[16:19]\nds_read2_b32 v[32:33], v8 offset0:48 offset1:49\n" \
  "ds_read2_b32 v[34:35], v8 offset0:50 offset1:51\nv_mfma_f32_16x16x32_f16 v[12:15], v[72:75], v[24:27], v[12:15]\n" \
  "ds_read_b128 v[108:111], v2 offset:2496\nv_mfma_f32_16x16x32_f16 v[16:19], v[104:107], v[24:27], v[16:19]\n" \
  "ds_read2_b32 v[36:37], v8 offset0:32 offset1:33\nds_read2_b32 v[38:39], v8 offset0:34 offset1:35\ns_waitcnt lgkmcnt(6)\n" \
  "v_mfma_f32_16x16x32_f16 v[12:15], v[68:71], v[124:127], v[12:15]\nds_read_b128 v[104:107], v2 offset:2560\n" \
  "v_mfma_f32_16x16x32_f16 v[16:19], v[100:103], v[124:127], v[16:19]\nds_read_b128 v[24:27], v2 offset:2624\n" \
  "v_mfma_f32_16x16x32_f16 v[12:15], v[64:67], v[28:31], v[12:15]\nds_read_b128 v[100:103], v2 offset:2688\nds_read_b32 v23, v9 offset:32\n" \
  "v_mfma_f32_16x16x32_f16 v[16:19], v[96:99], v[28:31], v[16:19]\nds_read_b128 v[124:127], v2 offset:2752\ns_waitcnt lgkmcnt(7)\n" \
  "v_mfma_f32_16x16x32_f16 v[12:15], v[60:63], v[112:115], v[12:15]\nds_read_b128 v[96:99], v2 offset:2816\n" \
  "v_mfma_f32_16x16x32_f16 v[16:19], v[92:95], v[112:115], v[16:19]\nds_read_b128 v[28:31], v2 offset:2880\n" \
  "v_mfma_f32_16x16x32_f16 v[12:15], v[56:59], v[108:111], v[12:15]\nds_read_b128 v[92:95], v2 offset:2944\n" \
  "v_mfma_f32_16x16x32_f16 v[16:19], v[88:91], v[108:111], v[16:19]\nds_read_b128 v[112:115], v2 offset:3008\ns_waitcnt lgkmcnt(5)\n" \
  "v_readfirstlane_b32 s85, v23\ns_cmp_lt_u32 s85, 3\ns_cbranch_scc1 61f\n62:\nv_mfma_f32_16x16x32_f16 v[16:19], v[84:87], v[104:107], v[16:19]\n" \
  "ds_read_b64_tr_b16 v[88:89], v5 offset:0\nds_read_b64_tr_b16 v[90:91], v5 offset:32\n" \
  "v_mfma_f32_16x16x32_f16 v[16:19], v[80:83], v[24:27], v[16:19]\nds_read_b64_tr_b16 v[108:109], v5 offset:256\n" \
  "ds_read_b64_tr_b16 v[110:111], v5 offset:288\nv_mfma_f32_16x16x32_f16 v[16:19], v[76:79], v[100:103], v[16:19]\n" \
  "ds_read_b64_tr_b16 v[84:85], v5 offset:512\nds_read_b64_tr_b16 v[86:87], v5 offset:544\ns_waitcnt lgkmcnt(9)\n" \
  "v_mfma_f32_16x16x32_f16 v[16:19], v[72:75], v[124:127], v[16:19]\nds_read_b64_tr_b16 v[104:105], v5 offset:768\n" \
  "ds_read_b64_tr_b16 v[106:107], v5 offset:800\nv_mfma_f32_16x16x32_f16 v[16:19], v[68:71], v[96:99], v[16:19]\n" \
  "ds_read_b64_tr_b16 v[80:81], v5 offset:1024\nds_read_b64_tr_b16 v[82:83], v5 offset:1056\ns_waitcnt lgkmcnt(11)\n" \
  "v_mfma_f32_16x16x32_f16 v[16:19], v[64:67], v[28:31], v[16:19]\nds_read_b64_tr_b16 v[24:25], v5 offset:1280\n" \
  "ds_read_b64_tr_b16 v[26:27], v5 offset:1312\nv_mfma_f32_16x16x32_f16 v[16:19], v[60:63], v[92:95], v[16:19]\n" \
  "ds_read_b64_tr_b16 v[76:77], v5 offset:1536\nds_read_b64_tr_b16 v[78:79], v5 offset:1568\ns_waitcnt lgkmcnt(12)\n" \
  "v_mfma_f32_16x16x32_f16 v[16:19], v[56:59], v[112:115], v[16:19]\nds_read_b64_tr_b16 v[100:101], v5 offset:1792\n" \
  "ds_read_b64_tr_b16 v[102:103], v5 offset:1824\nv_mfma_f32_16x16x32_f16 v[12:15], v[88:91], v[52:55], v[12:15]\ns_waitcnt lgkmcnt(10)\n" \
  "v_mfma_f32_16x16x32_f16 v[12:15], v[108:111], v[48:51], v[12:15]\nds_read_b64_tr_b16 v[72:73], v7 offset:0\n" \
  "ds_read_b64_tr_b16 v[74:75], v7 offset:64\nv_mfma_f32_16x16x32_f16 v[12:15], v[84:87], v[44:47], v[12:15]\n" \
  "ds_read_b64_tr_b16 v[124:125], v7 offset:512\nds_read_b64_tr_b16 v[126:127], v7 offset:576\n" \
  "v_mfma_f32_16x16x32_f16 v[16:19], v[84:87], v[52:55], v[16:19]\ns_waitcnt lgkmcnt(10)\n" \
  "v_mfma_f32_16x16x32_f16 v[12:15], v[104:107], v[40:43], v[12:15]\nv_mfma_f32_16x16x32_f16 v[16:19], v[104:107], v[48:51], v[16:19]\n" \
  "v_mfma_f32_16x16x32_f16 v[12:15], v[80:83], v[120:123], v[12:15]\nv_mfma_f32_16x16x32_f16 v[16:19], v[80:83], v[44:47], v[16:19]\n" \
  "s_waitcnt lgkmcnt(6)\nv_mfma_f32_16x16x32_f16 v[12:15], v[24:27], v[116:119], v[12:15]\n" \
  "v_mfma_f32_16x16x32_f16 v[16:19], v[24:27], v[40:43], v[16:19]\nv_mfma_f32_16x16x32_f16 v[16:19], v[76:79], v[120:123], v[16:19]\n" \
  "s_waitcnt lgkmcnt(2)\nv_mfma_f32_16x16x32_f16 v[16:19], v[100:103], v[116:119], v[16:19]\n" \
  "v_mfma_f32_16x16x32_f16 v[12:15], v[72:75], v[32:35], v[12:15]\nv_mfma_f32_16x16x32_f16 v[16:19], v[72:75], v[36:39], v[16:19]\n" \
  "s_waitcnt lgkmcnt(0)\nv_mfma_f32_16x16x32_f16 v[16:19], v[124:127], v[32:35], v[16:19]\ns_nop 4\nv_cvt_pkrtz_f16_f32 v20, v12, v13\n" \
  "v_cvt_pkrtz_f16_f32 v21, v14, v15\nv_pk_add_u16 v20, v20, 4 op_sel_hi:[1,0] clamp\nv_pk_add_u16 v21, v21, 4 op_sel_hi:[1,0] clamp\n" \
  "ds_write_b64 v4, v[20:21] offset:4096\nv_cvt_pkrtz_f16_f32 v20, v16, v17\nv_cvt_pkrtz_f16_f32 v21, v18, v19\n" \
  "v_pk_add_u16 v20, v20, 4 op_sel_hi:[1,0] clamp\nv_pk_add_u16 v21, v21, 4 op_sel_hi:[1,0] clamp\nds_write_b64 v4, v[20:21] offset:5120\n" \
  "s_setprio 0\ns_setpc_b64 s[90:91]\n61:\ns_movk_i32 s84, 0x4000\n63:\nds_read_b32 v23, v9 offset:32\ns_sub_u32 s84, s84, 1\ns_cbranch_scc1 62b\n" \
  "s_waitcnt lgkmcnt(0)\nv_readfirstlane_b32 s85, v23\ns_cmp_lt_u32 s85, 3\ns_cbranch_scc1 63b\ns_branch 62b\n5:\n" \
  "ds_read_b64 v[124:125], v3 offset:1088\nds_read_b64 v[126:127], v3 offset:1096\nds_read_b64 v[120:121], v3 offset:0\n" \
  "ds_read_b64 v[122:123], v3 offset:8\nds_read_b64 v[116:117], v3 offset:64\nds_read_b64 v[118:119], v3 offset:72\n" \
  "ds_read_b64 v[112:113], v3 offset:128\nds_read_b64 v[114:115], v3 offset:136\nds_read_b64 v[108:109], v3 offset:192\n" \
  "ds_read_b64 v[110:111], v3 offset:200\nds_read_b64 v[104:105], v3 offset:256\nds_read_b64 v[106:107], v3 offset:264\n" \
  "ds_read_b64 v[100:101], v3 offset:320\nds_read_b64 v[102:103], v3 offset:328\nds_read_b64 v[96:97], v3 offset:384\n" \
  "ds_read_b64 v[98:99], v3 offset:392\nds_read_b64 v[92:93], v3 offset:448\nds_read_b64 v[94:95], v3 offset:456\n" \
  "ds_read_b64 v[88:89], v3 offset:512\nds_read_b64 v[90:91], v3 offset:520\nds_read_b64 v[84:85], v3 offset:576\n" \
  "ds_read_b64 v[86:87], v3 offset:584\nds_read_b64 v[80:81], v3 offset:640\nds_read_b64 v[82:83], v3 offset:648\n" \
  "ds_read_b64 v[76:77], v3 offset:704\nds_read_b64 v[78:79], v3 offset:712\nds_read_b64 v[72:73], v3 offset:768\n" \
  "ds_read_b64 v[74:75], v3 offset:776\nds_read_b64 v[68:69], v3 offset:832\nds_read_b64 v[70:71], v3 offset:840\n" \
  "ds_read_b64 v[64:65], v3 offset:896\nds_read_b64 v[66:67], v3 offset:904\nds_read_b64 v[60:61], v3 offset:960\n" \
  "ds_read_b64 v[62:63], v3 offset:968\nds_read_b64 v[56:57], v3 offset:1024\nds_read_b64 v[58:59], v3 offset:1032\n" \
  "ds_read_b64 v[52:53], v6 offset:0\nds_read_b64 v[54:55], v6 offset:8\nds_read_b64 v[48:49], v6 offset:64\nds_read_b64 v[50:51], v6 offset:72\n" \
  "ds_read_b64 v[44:45], v6 offset:128\nds_read_b64 v[46:47], v6 offset:136\nds_read_b64 v[40:41], v6 offset:192\n" \
  "ds_read_b64 v[42:43], v6 offset:200\ns_waitcnt lgkmcnt(0)\ns_barrier\nds_read_b128 v[24:27], v2 offset:1472\n" \
  "ds_read_b128 v[28:31], v2 offset:1984\nds_read_b128 v[32:35], v2 offset:2496\nds_read_b128 v[36:39], v2 offset:2560\ns_waitcnt lgkmcnt(3)\n" \
  "v_mfma_f32_16x16x32_f16 v[12:15], v[124:127], v[24:27], 0\ns_setprio 1\nds_read_b128 v[24:27], v2 offset:2624\ns_waitcnt lgkmcnt(3)\n" \
  "v_mfma_f32_16x16x32_f16 v[16:19], v[124:127], v[28:31], 0\nds_read_b128 v[124:127], v2 offset:2688\ns_waitcnt lgkmcnt(3)\n" \
  "v_mfma_f32_16x16x32_f16 v[12:15], v[120:123], v[32:35], v[12:15]\nds_read_b128 v[28:31], v2 offset:2752\ns_waitcnt lgkmcnt(3)\n" \
  "v_mfma_f32_16x16x32_f16 v[12:15], v[116:119], v[36:39], v[12:15]\nds_read_b128 v[32:35], v2 offset:2816\ns_waitcnt lgkmcnt(3)\n" \
  "v_mfma_f32_16x16x32_f16 v[12:15], v[112:115], v[24:27], v[12:15]\nds_read_b128 v[36:39], v2 offset:2880\ns_waitcnt lgkmcnt(3)\n" \
  "v_mfma_f32_16x16x32_f16 v[12:15], v[108:111], v[124:127], v[12:15]\nds_read_b128 v[24:27], v2 offset:2944\ns_waitcnt lgkmcnt(3)\n" \
  "v_mfma_f32_16x16x32_f16 v[12:15], v[104:107], v[28:31], v[12:15]\nds_read_b128 v[124:127], v2 offset:3008\ns_waitcnt lgkmcnt(3)\n" \
  "v_mfma_f32_16x16x32_f16 v[12:15], v[100:103], v[32:35], v[12:15]\nds_read_b128 v[28:31], v2 offset:3072\ns_waitcnt lgkmcnt(3)\n" \
  "v_mfma_f32_16x16x32_f16 v[12:15], v[96:99], v[36:39], v[12:15]\nds_read_b128 v[32:35], v2 offset:3136\ns_waitcnt lgkmcnt(3)\n" \
  "v_mfma_f32_16x16x32_f16 v[12:15], v[92:95], v[24:27], v[12:15]\nds_read_b128 v[36:39], v2 offset:3200\ns_waitcnt lgkmcnt(3)\n" \
  "v_mfma_f32_16x16x32_f16 v[12:15], v[88:91], v[124:127], v[12:15]\nds_read_b128 v[24:27], v2 offset:3264\n" \
  "v_mfma_f32_16x16x32_f16 v[16:19], v[120:123], v[124:127], v[16:19]\nds_read_b64 v[120:121], v6 offset:256\nds_read_b64 v[122:123], v6 offset:264\n" \
  "s_waitcnt lgkmcnt(4)\nv_mfma_f32_16x16x32_f16 v[12:15], v[84:87], v[28:31], v[12:15]\nds_read_b128 v[124:127], v2 offset:3328\n" \
  "v_mfma_f32_16x16x32_f16 v[16:19], v[116:119], v[28:31], v[16:19]\nds_read_b64 v[116:117], v6 offset:320\nds_read_b64 v[118:119], v6 offset:328\n" \
  "v_mfma_f32_16x16x32_f16 v[12:15], v[80:83], v[32:35], v[12:15]\nds_read_b128 v[28:31], v2 offset:3392\n" \
  "v_mfma_f32_16x16x32_f16 v[16:19], v[112:115], v[32:35], v[16:19]\ns_waitcnt lgkmcnt(6)\n" \
  "v_mfma_f32_16x16x32_f16 v[12:15], v[76:79], v[36:39], v[12:15]\nds_read_b128 v[112:115], v2 offset:3456\n" \
  "v_mfma_f32_16x16x32_f16 v[16:19], v[108:111], v[36:39], v[16:19]\nds_read2_b32 v[32:33], v8 offset0:16 offset1:17\n" \
  "ds_read2_b32 v[34:35], v8 offset0:18 offset1:19\nv_mfma_f32_16x16x32_f16 v[12:15], v[72:75], v[24:27], v[12:15]\n" \
  "ds_read_b128 v[108:111], v2 offset:3520\nv_mfma_f32_16x16x32_f16 v[16:19], v[104:107], v[24:27], v[16:19]\n" \
  "ds_read2_b32 v[36:37], v8 offset0:0 offset1:1\nds_read2_b32 v[38:39], v8 offset0:2 offset1:3\ns_waitcnt lgkmcnt(6)\n" \
  "v_mfma_f32_16x16x32_f16 v[12:15], v[68:71], v[124:127], v[12:15]\nds_read_b128 v[104:107], v2 offset:3584\n" \
  "v_mfma_f32_16x16x32_f16 v[16:19], v[100:103], v[124:127], v[16:19]\nds_read_b128 v[24:27], v2 offset:3648\n" \
  "ds_read2_b32 v[100:101], v8 offset0:32 offset1:33\nds_read2_b32 v[102:103], v8 offset0:34 offset1:35\n" \
  "v_mfma_f32_16x16x32_f16 v[12:15], v[64:67], v[28:31], v[12:15]\nds_read_b128 v[124:127], v2 offset:3712\nds_read_b32 v23, v9 offset:32\n" \
  "v_mfma_f32_16x16x32_f16 v[16:19], v[96:99], v[28:31], v[16:19]\nds_read_b128 v[96:99], v2 offset:3776\ns_waitcnt lgkmcnt(9)\n" \
  "v_mfma_f32_16x16x32_f16 v[12:15], v[60:63], v[112:115], v[12:15]\nds_read_b128 v[28:31], v2 offset:3840\n" \
  "v_mfma_f32_16x16x32_f16 v[16:19], v[92:95], v[112:115], v[16:19]\nds_read_b128 v[92:95], v2 offset:3904\n" \
  "v_mfma_f32_16x16x32_f16 v[12:15], v[56:59], v[108:111], v[12:15]\nds_read_b128 v[112:115], v2 offset:3968\n" \
  "v_mfma_f32_16x16x32_f16 v[16:19], v[88:91], v[108:111], v[16:19]\nds_read_b128 v[88:91], v2 offset:4032\ns_waitcnt lgkmcnt(5)\n" \
  "v_readfirstlane_b32 s85, v23\ns_cmp_lt_u32 s85, 3\ns_cbranch_scc1 71f\n72:\nv_mfma_f32_16x16x32_f16 v[16:19], v[84:87], v[104:107], v[16:19]\n" \
  "ds_read_b64_tr_b16 v[108:109], v5 offset:1024\nds_read_b64_tr_b16 v[110:111], v5 offset:1056\n" \
  "v_mfma_f32_16x16x32_f16 v[16:19], v[80:83], v[24:27], v[16:19]\nds_read_b64_tr_b16 v[84:85], v5 offset:1280\n" \
  "ds_read_b64_tr_b16 v[86:87], v5 offset:1312\nv_mfma_f32_16x16x32_f16 v[16:19], v[76:79], v[124:127], v[16:19]\n" \
  "ds_read_b64_tr_b16 v[104:105], v5 offset:1536\nds_read_b64_tr_b16 v[106:107], v5 offset:1568\ns_waitcnt lgkmcnt(9)\n" \
  "v_mfma_f32_16x16x32_f16 v[16:19], v[72:75], v[96:99], v[16:19]\nds_read_b64_tr_b16 v[80:81], v5 offset:1792\n" \
  "ds_read_b64_tr_b16 v[82:83], v5 offset:1824\nv_mfma_f32_16x16x32_f16 v[16:19], v[68:71], v[28:31], v[16:19]\n" \
  "ds_read_b64_tr_b16 v[24:25], v5 offset:2048\nds_read_b64_tr_b16 v[26:27], v5 offset:2080\ns_waitcnt lgkmcnt(11)\n" \
  "v_mfma_f32_16x16x32_f16 v[16:19], v[64:67], v[92:95], v[16:19]\nds_read_b64_tr_b16 v[76:77], v5 offset:2304\n" \
  "ds_read_b64_tr_b16 v[78:79], v5 offset:2336\nv_mfma_f32_16x16x32_f16 v[16:19], v[60:63], v[112:115], v[16:19]\n" \
  "ds_read_b64_tr_b16 v[124:125], v5 offset:2560\nds_read_b64_tr_b16 v[126:127], v5 offset:2592\ns_waitcnt lgkmcnt(12)\n" \
  "v_mfma_f32_16x16x32_f16 v[16:19], v[56:59], v[88:91], v[16:19]\nds_read_b64_tr_b16 v[72:73], v5 offset:2816\n" \
  "ds_read_b64_tr_b16 v[74:75], v5 offset:2848\nv_mfma_f32_16x16x32_f16 v[12:15], v[108:111], v[52:55], v[12:15]\ns_waitcnt lgkmcnt(10)\n" \
  "v_mfma_f32_16x16x32_f16 v[12:15], v[84:87], v[48:51], v[12:15]\nds_read_b64_tr_b16 v[96:97], v7 offset:0\n" \
  "ds_read_b64_tr_b16 v[98:99], v7 offset:64\nv_mfma_f32_16x16x32_f16 v[12:15], v[104:107], v[44:47], v[12:15]\n" \
  "ds_read_b64_tr_b16 v[68:69], v7 offset:512\nds_read_b64_tr_b16 v[70:71], v7 offset:576\n" \
  "v_mfma_f32_16x16x32_f16 v[16:19], v[104:107], v[52:55], v[16:19]\ns_waitcnt lgkmcnt(10)\n" \
  "v_mfma_f32_16x16x32_f16 v[12:15], v[80:83], v[40:43], v[12:15]\nds_read_b64_tr_b16 v[28:29], v7 offset:1024\n" \
  "ds_read_b64_tr_b16 v[30:31], v7 offset:1088\nv_mfma_f32_16x16x32_f16 v[16:19], v[80:83], v[48:51], v[16:19]\n" \
  "ds_read2_b32 v[64:65], v8 offset0:48 offset1:49\nds_read2_b32 v[66:67], v8 offset0:50 offset1:51\n" \
  "v_mfma_f32_16x16x32_f16 v[12:15], v[24:27], v[120:123], v[12:15]\nv_mfma_f32_16x16x32_f16 v[16:19], v[24:27], v[44:47], v[16:19]\n" \
  "s_waitcnt lgkmcnt(10)\nv_mfma_f32_16x16x32_f16 v[12:15], v[76:79], v[116:119], v[12:15]\nds_read_b64_tr_b16 v[92:93], v7 offset:1536\n" \
  "ds_read_b64_tr_b16 v[94:95], v7 offset:1600\nv_mfma_f32_16x16x32_f16 v[16:19], v[76:79], v[40:43], v[16:19]\n" \
  "v_mfma_f32_16x16x32_f16 v[16:19], v[124:127], v[120:123], v[16:19]\ns_waitcnt lgkmcnt(8)\n" \
  "v_mfma_f32_16x16x32_f16 v[16:19], v[72:75], v[116:119], v[16:19]\nv_mfma_f32_16x16x32_f16 v[12:15], v[96:99], v[32:35], v[12:15]\n" \
  "v_mfma_f32_16x16x32_f16 v[16:19], v[96:99], v[36:39], v[16:19]\ns_waitcnt lgkmcnt(4)\n" \
  "v_mfma_f32_16x16x32_f16 v[12:15], v[68:71], v[100:103], v[12:15]\nv_mfma_f32_16x16x32_f16 v[16:19], v[68:71], v[32:35], v[16:19]\n" \
  "s_waitcnt lgkmcnt(2)\nv_mfma_f32_16x16x32_f16 v[12:15], v[28:31], v[64:67], v[12:15]\n" \
  "v_mfma_f32_16x16x32_f16 v[16:19], v[28:31], v[100:103], v[16:19]\ns_waitcnt lgkmcnt(0)\n" \
  "v_mfma_f32_16x16x32_f16 v[16:19], v[92:95], v[64:67], v[16:19]\ns_nop 4\nv_cvt_pkrtz_f16_f32 v20, v12, v13\nv_cvt_pkrtz_f16_f32 v21, v14, v15\n" \
  "v_pk_add_u16 v20, v20, 4 op_sel_hi:[1,0] clamp\nv_pk_add_u16 v21, v21, 4 op_sel_hi:[1,0] clamp\nds_write_b64 v4, v[20:21] offset:6144\n" \
  "v_cvt_pkrtz_f16_f32 v20, v16, v17\nv_cvt_pkrtz_f16_f32 v21, v18, v19\nv_pk_add_u16 v20, v20, 4 op_sel_hi:[1,0] clamp\n" \
  "v_pk_add_u16 v21, v21, 4 op_sel_hi:[1,0] clamp\nds_write_b64 v4, v[20:21] offset:7168\ns_setprio 0\ns_setpc_b64 s[90:91]\n71:\n" \
  "s_movk_i32 s84, 0x4000\n73:\nds_read_b32 v23, v9 offset:32\ns_sub_u32 s84, s84, 1\ns_cbranch_scc1 72b\ns_waitcnt lgkmcnt(0)\n" \
  "v_readfirstlane_b32 s85, v23\ns_cmp_lt_u32 s85, 3\ns_cbranch_scc1 73b\ns_branch 72b\n"
#define CONV_CLOB "memory", "scc", "s84", "s85", "s90", "s91", "v12", "v13", "v14", "v15", "v16", "v17", "v18", "v19", "v20", "v21", "v22", "v23", "v24", "v25", "v26", "v27", "v28", "v29", "v30", "v31", "v32", "v33", "v34", "v35", "v36", "v37", "v38", "v39", "v40", "v41", "v42", "v43", "v44", "v45", "v46", "v47", "v48", "v49", "v50", "v51", "v52", "v53", "v54", "v55", "v56", "v57", "v58", "v59", "v60", "v61", "v62", "v63", "v64", "v65", "v66", "v67", "v68", "v69", "v70", "v71", "v72", "v73", "v74", "v75", "v76", "v77", "v78", "v79", "v80", "v81", "v82", "v83", "v84", "v85", "v86", "v87", "v88", "v89", "v90", "v91", "v92", "v93", "v94", "v95", "v96", "v97", "v98", "v99", "v100", "v101", "v102", "v103", "v104", "v105", "v106", "v107", "v108", "v109", "v110", "v111", "v112", "v113", "v114", "v115", "v116", "v117", "v118", "v119", "v120", "v121", "v122", "v123", "v124", "v125", "v126", "v127"
constexpr int TDP = 1216, T_3 = 4 * TDP, T_4 = T_3 + 512, TB = T_4 + 320;
constexpr int C_U = 480, UB = C_U + 4096 + 96, C_G5 = 0, C_G3 = 3200, C_G9 = 6400, C_G7 = C_G9 + 2096, GB = C_G7 + 2080 + 112;
constexpr int O_FL = 2 * TB, O_UL = O_FL + 64, O_IM = O_UL + 2 * UB, O_ST = O_IM + 2 * GB;
constexpr int X_TB = 10240, X_UL = X_TB + TB, X_IM = X_UL + UB, X_ST = X_IM + GB;
constexpr int X_WR = X_IM + 128 * IP;
#define X_RW(w) (LW + 9216 * (2 + ((w) >> 1)) + 2560 * ((w) & 1))
constexpr int RP = 1216;
constexpr int LW = 36864 + 512, LA = LW, LM = LW + 36864;
constexpr int L_SC = LM + 2048, SMEM = O_IM + 256 * IP;
static_assert(L_SC + 1152 <= SMEM && O_ST + 8192 <= SMEM && X_IM + 128 * IP <= LM && X_ST + 8192 <= LM && UB % 128 == 64 && GB % 128 == 64 && SMEM <= 79872, "");
struct TOps { uint32_t xa, xb, ya, pd, pf, pg, pe; };
__device__ __forceinline__ void table_ops(uint32_t im, uint32_t tb, int ct, int lane, TOps& t) {
  const int j = lane & 15, kq = lane >> 4, q4 = (lane >> 2) & 3, p4 = lane & 3, jj = j + 16 * ct;
  const uint32_t sa = im + (uint32_t)(IP * (lane >> 2));
  t.xa = sa + (uint32_t)(I_X + 8 * p4); t.xb = sa + (uint32_t)(I_X + 32 + 8 * (p4 & 1)); t.ya = sa + (uint32_t)(32 * ct + 8 * p4);
  t.pd = tb + (uint32_t)(2 * (541 - (8 * jj + 4 * (kq & 1) + 256 * (kq >> 1))));
  t.pf = tb + (uint32_t)((kq & 1) ? T_4 + 2 * (63 - jj) : T_3 + 2 * (127 - 2 * jj));
  t.pg = t.pf - ((kq & 1) ? 64 : 2);
  t.pe = tb + (uint32_t)(p4 * (TDP + 2) + ((lane < 12) ? 2 * (32 - q4) : 2 * (572 + (q4 & 1))));
  asm volatile("" : "+v"(t.xa), "+v"(t.xb), "+v"(t.ya), "+v"(t.pd), "+v"(t.pf), "+v"(t.pg), "+v"(t.pe));
}
typedef __attribute__((address_space(3))) char* lptr;
#define LP(T, a) ((__attribute__((address_space(3))) T*)(uintptr_t)(a))
__device__ __forceinline__ uint32_t table_tile(const TOps& t, int ct, int lane, uint32_t evp, float dsk TSP) {
  u32x4 ev;
  f32x4 ad[2], af[2];
  asm volatile(T_ASM : "=&v"(ad[0]), "=&v"(ad[1]), "=&v"(af[0]), "=&v"(af[1]), "=&v"(ev) : "v"(t.xa), "v"(t.xb), "v"(t.ya), "v"(evp) : T_CLOB);
  const int e0 = (int)(ev[0] + ev[2]), e1 = (int)(ev[1] + ev[3]);
  const int em = min(max(max(max(e0, e1), (int)((__float_as_uint(dsk) >> 23) & 0xffu) - 133), -110), 115);
  const int ts = ((uint32_t)(em + 12) <= 8u) ? 1 : -3 - em;
  float s0 = __builtin_amdgcn_ldexpf(1.f, e0 - 29 + ts), s1 = __builtin_amdgcn_ldexpf(1.f, e1 - 29 + ts);
  float d0 = (ct == 0 && lane == 0) ? __builtin_amdgcn_ldexpf(dsk, ts) : 0.f;
  asm volatile("s_nop 1" : "+v"(ad[0]), "+v"(ad[1]), "+v"(af[0]), "+v"(af[1]), "+v"(s0), "+v"(s1), "+v"(d0));
  const f32x4 k = ad[0] * s0 + (f32x4){d0, 0.f, 0.f, 0.f} + ad[1] * s1, f = af[0] * s0 + af[1] * s1;
  auto pk = [](float a, float b) { uint32_t r; asm("v_cvt_pk_f16_f32 %0, %1, %2" : "=v"(r) : "v"(a), "v"(b)); return r; };
  asm volatile("s_nop 0" : : "v"(k), "v"(f));
  const uint32_t p32 = pk(k[3], k[2]), p10 = pk(k[1], k[0]), p21 = pk(k[2], k[1]), p01 = pk(f[0], f[1]), p23 = pk(f[2], f[3]);
  *LP(u32x2, t.pd + 3 * TDP + 6) = (u32x2){p32, p10};
  *LP(uint32_t, t.pd + TDP + 2) = p32; *LP(uint32_t, t.pd + TDP + 6) = p10;
  *LP(uint32_t, t.pd + 2) = p21; *LP(uint32_t, t.pd + 2 * TDP + 6) = p21;
  *LP(u16, t.pd) = (u16)p32; *LP(u16, t.pd + 2 * TDP + 4) = (u16)p32;
  asm volatile("ds_write_b16_d16_hi %0, %1 offset:6\n\tds_write_b16_d16_hi %0, %1 offset:%2" : : "v"(t.pd), "v"(p10), "n"(2 * TDP + 10) : "memory");
  if (ct) {
    const uint32_t va = (uint32_t)__builtin_amdgcn_readlane((int)p32, 63), vb = (uint32_t)__builtin_amdgcn_readlane((int)p23, 15) >> 16;
    *LP(u16, t.pe) = (u16)((lane < 12) ? va : vb);
  }
  if (lane < 32) {
    *LP(u16, t.pf + 128) = (u16)p01; *LP(u16, t.pg + 128) = (u16)(p01 >> 16);
    *LP(u16, t.pf) = (u16)p23; *LP(u16, t.pg) = (u16)(p23 >> 16);
  }
  return (uint32_t)(127 + 5 - ts) << 23;
}
__device__ __forceinline__ uint32_t hadd(uint32_t a, uint32_t b) { return __builtin_bit_cast(uint32_t, __builtin_bit_cast(f16x2, a) + __builtin_bit_cast(f16x2, b)); }
template <bool FULL> __device__ __forceinline__ void sums(char* sm, const u32x4 p, const u32x4 c) {
  const uint32_t D[8] = {p[0], p[1], p[2], p[3], c[0], c[1], c[2], c[3]};
  uint32_t O[7];
#pragma unroll
  for (int j = 0; j < 7; ++j) O[j] = __builtin_amdgcn_alignbit(D[j + 1], D[j], 16);
  u32x4 a, b;
#pragma unroll
  for (int j = 0; j < 4; ++j) a[j] = hadd(D[j + 2], hadd(O[j + 1], O[j + 2]));
  *(u32x4*)(sm + C_G3 + 32) = a;
#pragma unroll
  for (int j = 0; j < 4; ++j) b[j] = hadd(a[j], hadd(D[j + 1], D[j + 3]));
  *(u32x4*)(sm + C_G5 + 32) = b;
  if (FULL) {
#pragma unroll
    for (int j = 0; j < 4; ++j) a[j] = hadd(b[j], hadd(O[j], O[j + 3]));
    *(u32x4*)(sm + C_G7 + 32) = a;
#pragma unroll
    for (int j = 0; j < 4; ++j) b[j] = hadd(a[j], hadd(D[j], D[j + 4]));
    *(u32x4*)(sm + C_G9 + 32) = b;
  }
}
__device__ __forceinline__ void count_up(uint32_t a) {
  uint32_t t;
  asm volatile("s_mov_b64 exec, 1\n\tv_mov_b32 %0, 1\n\tds_add_u32 %1, %0\n\ts_mov_b64 exec, -1" : "=&v"(t) : "v"(a) : "memory");
}
__device__ __forceinline__ int pad_at(int r) { return 16 * (r & 1) + 3200 * (r >> 1) - ((r >= 6) ? 3200 - 2096 : 0); }
struct COps { uint32_t b, a, o, g3, k3, g4, k4, f; };
__device__ __forceinline__ void conv_ops(uint32_t tb, uint32_t ub, uint32_t cb, int lane, COps& e) {
  const uint32_t j = lane & 15, kq = lane >> 4, q4 = (lane >> 2) & 3, p4 = lane & 3;
  e.b = ub + 32u * j + 16u * kq;
  e.a = tb + p4 * TDP + 32u - 8u * q4 + 16u * kq;
  e.g3 = cb + C_G5 + 8 + 64u * kq + 8u * (q4 + p4) + (q4 & 1u) * 3200u;
  e.g4 = cb + C_G9 + 8 + 128u * kq + 16u * q4 + 8u * p4 + (q4 & 1u) * 2096u;
  e.k3 = tb + T_3 + 120 - 8u * j + 16u * kq;
  e.k4 = tb + T_4 + 60 - 4u * j + 16u * kq;
  asm volatile("" : "+v"(e.b), "+v"(e.a), "+v"(e.g3), "+v"(e.g4), "+v"(e.k3), "+v"(e.k4));
}
#define CONV_RUN(prog, e, ...) do { int prog_ = (prog); \
    asm volatile("s_call_b64 s[90:91], kf_conv" : "+{s89}"(prog_), "+{v2}"(e.b), "+{v3}"(e.a), "+{v4}"(e.o), "+{v5}"(e.g3), "+{v6}"(e.k3), "+{v7}"(e.g4), \
                 "+{v8}"(e.k4), "+{v9}"(e.f) __VA_ARGS__ : : CONV_CLOB); } while (0)

template <bool E>
__device__ __forceinline__ void ln_stats(const uint32_t* v, int lane, float sc, f32x2* t, f32x4 z, f32x2& rc, f32x2& nc, float& e) {
  float s1a = z[0], s2a = z[1], s1b = z[2], s2b = z[3];
#pragma unroll
  for (int k = 0; k < 4; ++k) {
    s1a = dot2(v[k], 0x3f803f80u, s1a); s2a = dot2(v[k], v[k], s2a);
    s1b = dot2(v[4 + k], 0x3f803f80u, s1b); s2b = dot2(v[4 + k], v[4 + k], s2b);
  }
#pragma unroll
  for (int k = 0; k < 9; ++k) t[k] = (f32x2){bflo(v[k]), bfhi(v[k])};
  const float rs = row_sum(fold16(dot2(v[8], 0x3f803f80u, fold32(s1a, s1b)), dot2(v[8], v[8], fold32(s2a, s2b))));
  if (E) { e = row_sum(e); e += __int_as_float(__builtin_amdgcn_update_dpp(0, __float_as_int(e), 0x142, 0xa, 0xf, false)); }
  const f32x2 mean = (f32x2){rdl(rs, 15), rdl(rs, 47)} * (1.f / D_), m2 = mean * mean;
  f32x2 var = (f32x2){rdl(rs, 31), rdl(rs, 63)} * (1.f / D_) - m2;
  asm volatile("" : "+v"(t[0]), "+v"(t[1]), "+v"(t[2]), "+v"(t[3]), "+v"(t[4]), "+v"(t[5]), "+v"(t[6]), "+v"(t[7]), "+v"(t[8]));
  if (__builtin_expect(!(m2[0] <= 16.f * var[0] && m2[1] <= 16.f * var[1]), 0)) {
    float q[3] = {0.f, 0.f, 0.f};
#pragma unroll
    for (int k = 0; k < 9; ++k) {
      const f32x2 a = t[k] - ((k < 4 || (k == 8 && lane < 32)) ? mean[0] : mean[1]);
      q[k >> 2] += a[0] * a[0] + a[1] * a[1];
    }
    float qa = q[0] + ((lane < 32) ? q[2] : 0.f), qb = q[1] + ((lane < 32) ? 0.f : q[2]);
    wave_sum2(qa, qb);
    var = (f32x2){qa, qb} * (1.f / D_);
  }
  const uint32_t mp = pack2(mean[0], mean[1]), vp = pack2(var[0], var[1]);
  const f32x2 ve = (f32x2){bflo(vp), bfhi(vp)} + 1e-5f;
  const uint32_t e2 = pack2(ve[0], ve[1]);
  const uint32_t sp = pack2(__builtin_amdgcn_sqrtf(bflo(e2)), __builtin_amdgcn_sqrtf(bfhi(e2)));
  rc = (f32x2){__builtin_amdgcn_rcpf(bflo(sp)), __builtin_amdgcn_rcpf(bfhi(sp))} * sc;
  nc = -(f32x2){bflo(mp), bfhi(mp)} * rc;
}
template <bool H>
__device__ __forceinline__ void ln_apply(const f32x2* t, int lane, f32x2 rc, f32x2 nc, uint32_t* c) {
  const float nm[3] = {nc[0], nc[1], (lane < 32) ? nc[0] : nc[1]}, rr[3] = {rc[0], rc[1], (lane < 32) ? rc[0] : rc[1]};
  f32x2 dd[9];
#pragma unroll
  for (int k = 0; k < 9; ++k) dd[k] = __builtin_elementwise_fma(t[k], (f32x2){rr[k >> 2], rr[k >> 2]}, (f32x2){nm[k >> 2], nm[k >> 2]});
  asm volatile("" : "+v"(dd[0]), "+v"(dd[1]), "+v"(dd[2]), "+v"(dd[3]), "+v"(dd[4]), "+v"(dd[5]), "+v"(dd[6]), "+v"(dd[7]), "+v"(dd[8]));
#pragma unroll
  for (int k = 0; k < 9; ++k) {
    const f32x2 d = dd[k];
    typedef __attribute__((ext_vector_type(2))) uint16_t u16x2;
    if (H) c[k] = __builtin_bit_cast(uint32_t, __builtin_elementwise_add_sat(__builtin_bit_cast(u16x2, __builtin_amdgcn_cvt_pkrtz(d[0], d[1])), (u16x2){4, 4}));
    else c[k] = pack2(d[0], d[1]);
  }
}
__device__ __forceinline__ uint32_t med3u(uint32_t a, uint32_t b, uint32_t c) { return max(min(a, b), min(max(a, b), c)); }
__device__ __forceinline__ uint32_t oct_maxu(uint32_t v) {
  v = max(v, (uint32_t)DPP_I((int)v, 0xb1, 0xf));
  v = max(v, (uint32_t)DPP_I((int)v, 0x4e, 0xf));
  v = max(v, (uint32_t)DPP_I((int)v, 0x141, 0xf));
  return v;
}

#define KARGS const u16* __restrict__ x, const u16* __restrict__ Are, const u16* __restrict__ Aim, const u16* __restrict__ Bre, \
    const u16* __restrict__ Bim, const u16* __restrict__ Cre, const u16* __restrict__ Cim, const u16* __restrict__ logdt, \
    const u16* __restrict__ Dskip, char* __restrict__ ws, const u16* __restrict__ wr, const u16* __restrict__ wg, \
    const u16* __restrict__ wu, const u16* __restrict__ wd, u16* __restrict__ out
#define KPASS x, Are, Aim, Bre, Bim, Cre, Cim, logdt, Dskip, ws, wr, wg, wu, wd, out
template <int ROLE, int FFN = -1>
__device__ __forceinline__ void role_body(char* smem, KARGS) {
  const int tid = threadIdx.x, lane = tid & 63;
  const int w = __builtin_amdgcn_readfirstlane(tid >> 6), wl = w & 3;
  const int b = blockIdx.x & 255, bx = b & 7, bj = b >> 3;
  constexpr bool isl = ROLE == 1 || ROLE == 2;
  const int tg = isl ? 32 * (bj >> 2) + 4 * bx + (bj & 3) : 32 * bx + bj;
  u32x4 xa, xc;
  uint32_t ep, r0;
#define P_WAIT(n, ...) asm volatile("s_waitcnt vmcnt(" #n ")\n\tv_lshlrev_b32 %0, 16, v100\n\tv_lshlrev_b32 %1, 16, v101\n\tv_lshlrev_b32 %2, 16, v102" \
    : "=v"(ldt), "=v"(are), "=v"(aim) : : "v100", "v101", "v102" __VA_ARGS__)
#define P_MORE() asm volatile("v_lshlrev_b32 %0, 16, v103\n\tv_lshlrev_b32 %1, 16, v104\n\tv_lshlrev_b32 %2, 16, v105\n\tv_lshlrev_b32 %3, 16, v106" \
    : "=v"(bre), "=v"(bim), "=v"(cre), "=v"(cim) : : "v103", "v104", "v105", "v106")
  TSD;
  TS(0, 0);
  const uint32_t lb0 = (uint32_t)(uintptr_t)(__attribute__((address_space(3))) char*)smem;
  const int i = lane & 15, kq = lane >> 4;
  const u32x4 ones = {0xffffffffu, 0xffffffffu, 0xffffffffu, 0xffffffffu}, zero4 = {0u, 0u, 0u, 0u};
  auto u_again = [&](u32x4& uw, const u32x4& ut, uint32_t uo) {
#pragma unroll 1
    for (int spin = 0; spin < SPIN_CAP; ++spin) {
      const bool na = ((uw[0] | uw[1] | uw[2] | uw[3]) & UMASK) != 0u;
      if (__builtin_expect(!__any(na), 1)) break;
      TSC(19);
      SLEEP(USLEEP);
      if (na) { LD16W(uw, uo); uw ^= ut; }
    }
  };
  constexpr uint32_t S16 = 0x41800000u, LMASK = 0x00070007u;
  auto v_put = [&](u32x4 v, const u32x4& ut, bool other, uint32_t vo) {
    const bool second = __any(other || ((v[0] | v[1] | v[2] | v[3]) & 0x40004000u) != 0u);
    if (__builtin_expect(second, 0)) v = (v & ~LMASK) | (~ut & LMASK); else v = (v & ~LMASK) | ut;
    ST16(vo, v);
    return second;
  };
  auto v_end = [&](uint32_t T, int n) { DRAIN(); if (lane == 0) ST4(O_DN + 16u * n, T); };
  if constexpr (!isl) {
    const int ch = wl >> 1, tl = tid & 255;
    int* evp = (int*)(smem + O_FL + 16 * ch);
    float ldt, are, aim, dsk;
    if constexpr (ROLE == 0) {
      __builtin_amdgcn_s_setprio(KPRIO);
      COps e;
      conv_ops(lb0 + (uint32_t)(ch * TB), lb0 + (uint32_t)(O_UL + ch * UB), lb0 + (uint32_t)(O_IM + ch * GB), lane, e);
      e.o = lb0 + O_ST + (uint32_t)(16 * ((4 * i + 2 * (kq >> 1) + ch) ^ (i >> 1)) + 8 * (kq & 1));
      e.f = lb0 + O_FL;
      TOps t;
      table_ops(lb0 + (uint32_t)(O_IM + 128 * IP * ch), lb0 + (uint32_t)(TB * ch), wl & 1, lane, t);
      uint32_t pz = lb0 + O_IM + (uint32_t)(((lane >> 3) & 1) * GB + pad_at(lane & 7)), im = lb0 + (uint32_t)(O_IM + IP * tl);
      asm volatile("" : "+v"(e.o), "+v"(e.f), "+v"(pz), "+v"(im));
      float bre, bim, cre, cim;
      P_WAIT(0, , "v103", "v104", "v105", "v106", "v107", "v109");
      asm volatile("" : "={v109}"(ep));
      const uint32_t T = ((uint32_t)__builtin_amdgcn_readfirstlane(ep) + 1u) * 0x9E3779B1u;
      if (w == 3 && lane < 8) ST16(O_KEY + 128u * b + 16u * lane, ones);
      P_MORE();
      asm volatile("v_lshlrev_b32 %0, 16, v107" : "=v"(dsk) : : "v107");
      TS(0, 1);
      evp[wl & 1] = gen_y(ldt, are, aim, bre, bim, cre, cim, im, [] {} TSA);
      TS(0, 8);
      BAR();
      TS(0, 2);
      const uint32_t cv = table_tile(t, wl & 1, lane, lb0 + O_FL + 16 * ch, dsk TSA), cs = (uint32_t)__builtin_amdgcn_readfirstlane(cv);
      *(uint32_t*)(smem + O_FL + 40 + 4 * ch) = cv;
      TS(0, 9);
      gsync0(lb0 + O_FL + 36, 4);
      TS(0, 3);
      *(u32x4*)(smem + O_ST + 16 * tl) = ones;
      *(u32x4*)(smem + O_ST + 4096 + 16 * tl) = ones;
      if (w == 0) *LP(u32x4, pz) = zero4;
      DRAIN();
      CONV_RUN(wl & 1, e);
      if (wl & 1) asm volatile("s_mov_b64 exec, 1\n\tglobal_store_dwordx2 %0, %1, %2 sc1" : : "v"(O_SC + 16u * (2 * tg + ch)), "v"((u32x2){T, cs}), "s"(ws) : "memory");
      TS(0, 5);
    } else {
      __builtin_amdgcn_s_setprio(UPRIO);
      const uint32_t ck0 = (uint32_t)__builtin_amdgcn_s_memtime(), rt0 = (uint32_t)__builtin_amdgcn_s_memrealtime();
#pragma unroll
      for (int k = 0; k < 3; ++k) *(u32x4*)(smem + ((16 * (tl + 256 * k) < O_FL) ? 16 * (tl + 256 * k) : O_FL + 32)) = zero4;
      if (w == 7) *(u32x4*)(smem + O_UL + (lane >> 5) * UB + 16 * min(lane & 31, 29)) = zero4;
      uint32_t uo = u_at(tl >> 1, 2 * tg + (tid & 1)), go = O_SU + (uint32_t)(256 * bx + 32 * wl + (lane & 31) + 4 * (lane & 32)) * 128u;
      uint32_t ul = (uint32_t)(O_UL + (tid & 1) * UB + C_U - 16 + 16 * (tl >> 1)), ug = (uint32_t)(O_IM + (tid & 1) * GB + 16 * (tl >> 1)), so = lb0 + O_ST + 16u * (uint32_t)(lane ^ (lane >> 3));
      uint32_t vo = O_V + (uint32_t)tg * VP + 16u * lane;
      asm volatile("" : "+v"(uo), "+v"(go), "+v"(ul), "+v"(ug), "+v"(so), "+v"(vo));
      P_WAIT(1, , "v109");
      TS(0, 1);
      evp[2 + (wl & 1)] = gen_x(ldt, are, aim, smem + O_IM + IP * tl);
      asm volatile("s_waitcnt vmcnt(0)" : "={v109}"(ep));
      const uint32_t T = ((uint32_t)__builtin_amdgcn_readfirstlane(ep) + 1u) * 0x9E3779B1u;
      TS(0, 8);
      BAR();
      TS(0, 2);
      if (w == 7 && bj >= 24) {
        const uint32_t t_ = ahead(ck0, rt0, VDB);
        ST16(O_SB + (uint32_t)(64 * (lane >> 3) + 8 * bx + bj - 24) * 128u + 16u * (lane & 7), ((u32x4){T, t_, T, t_}));
      }
      u32x4 ut = {utag(T, 0), utag(T, 1), utag(T, 2), utag(T, 3)};
      asm volatile("" : "+v"(ut));
      SLEEP(GSLEEP);
      u32x2 pv;
      pv[1] = gate(ws, go, T);
      TS(0, 3);
      until(latest(pv[1]) + UD);
      TS(0, 7);
      u32x4 uw, ux;
      asm volatile("global_load_dwordx4 %0, %2, %4 sc1\n\tglobal_load_dwordx4 %1, %3, %4 sc1"
                   : "=&v"(uw), "=&v"(ux) : "v"(uo), "v"(uo + 64 * UP * 32), "s"(ws) : "memory");
      __builtin_amdgcn_s_setprio(FPR);
      asm volatile("s_waitcnt vmcnt(1)" : "+v"(uw));
      uw ^= ut;
      *(u32x4*)(smem + ul + 16) = uw;
      asm volatile("s_waitcnt vmcnt(0)" : "+v"(ux));
      TS(0, 11);
      ux ^= ut;
      *(u32x4*)(smem + ul + 2048 + 16) = ux;
      if (__builtin_expect(__any(((uw[0] | uw[1] | uw[2] | uw[3] | ux[0] | ux[1] | ux[2] | ux[3]) & UMASK) != 0u), 0)) {
        u_again(uw, ut, uo);
        u_again(ux, ut, uo + 64 * UP * 32);
        *(u32x4*)(smem + ul + 16) = uw;
        *(u32x4*)(smem + ul + 2048 + 16) = ux;
      }
      BAR();
      TS(0, 4);
      const u32x4 pw = *(const u32x4*)(smem + ul);
      if (wl < 2) {
        const u32x4 px = *(const u32x4*)(smem + ul + 2048);
        sums<true>(smem + ug, pw, uw);
        sums<false>(smem + ug + 2048, px, ux);
      } else sums<true>(smem + ug, pw, uw);
      count_up(lb0 + O_FL + 32);
      __builtin_amdgcn_s_setprio(SPR);
      TS(0, 5);
      const bool other = *(const uint32_t*)(smem + O_FL + 40 + 4 * (lane & 1)) != S16;
      bool second = false;
#pragma unroll
      for (int k = 0; k < 2; ++k) {
        const uint32_t o = ((k ? 0x3247u : 0x1065u) >> (4 * wl) & 7u) * 1024u;
        u32x4 v;
#pragma unroll 1
        for (int spin = 0; spin < SPIN_CAP; ++spin) {
          asm volatile("ds_read_b128 %0, %1\n\ts_waitcnt lgkmcnt(0)" : "=&v"(v) : "v"(so + o) : "memory");
          if (!__any(max4(v) == 0xffffffffu)) break;
          __builtin_amdgcn_s_sleep(VPS);
        }
        TS(0, 12 + k);
        second |= v_put(v, ut, other, vo + o);
        TS(0, 10 - 4 * k);
      }
      if (__builtin_expect(second, 0)) v_end(T, 4 * tg + wl);
    }
    TSE();
  } else {
  uint32_t* cand = (uint32_t*)(smem + LM);
  uint32_t* sel = cand + 128;
  uint32_t* kl = sel + 48;
  uint32_t* cnt = sel + 64;
  const uint32_t ca = lb0 + LM + 4 * (128 + 64);
  u16* hm = (u16*)(cnt + 16);
  uint32_t T;
  const bool ffn = (FFN < 0) ? bj < 18 : FFN, isx = FFN != 1 && bj >= 24;
  const int fb = 18 * bx + bj;
  constexpr bool ln2 = ROLE == 2;
  const int g0 = tg, g2 = tg;
  u32x4 bw[18];
#pragma unroll
  for (int s = 0; s < 9; ++s) bw[s] = (u32x4){0u, 0u, 0u, 0u};
  int tid2 = threadIdx.x;
  uint32_t tv = 0u, tk = 0u;
  auto v_look = [&](uint32_t tu, int lane) {
    uint32_t tm = latest(gate(ws, O_SB + (uint32_t)(64 * bx + lane) * 128u, T)) - V2D;
    if ((int)(tm - (tu + UD + VD2)) < 0) tm = tu + UD + VD2;
    cnt[8] = tm;
    count_up(ca + 52);
  };
  auto v_time = [&] { gwait(ca + 52, 1); tv = (uint32_t)__builtin_amdgcn_readfirstlane(cnt[8]); };
  uint32_t ts1 = 0u;
  auto rest = [&] {
    if (ts1 && (int)((uint32_t)__builtin_amdgcn_s_memrealtime() - ts1) > 0) {
      v_time();
      ts1 = (tv - TSTOP) | 1u;
      if ((int)((uint32_t)__builtin_amdgcn_s_memrealtime() - ts1) > 0) { TS(0, 8); until(tv + RD); ts1 = 0u; }
    }
  };
  auto gu_dma = [&](int q0, int q1, auto depth) {
    const char* gs = (const char*)((w < 2) ? wg : wu) + (size_t)((144 * (w & 1)) * (F_ * 2) + fb * 64);
    const uint32_t gv = (uint32_t)((lane >> 2) * (F_ * 2) + 16 * ((lane & 3) ^ (lane >> 5) ^ (w & 2)));
#pragma unroll 1
    for (int q = q0; q < q1; ++q) {
      const int k = (int)((((bj & 1) ? ORD1 : ((unsigned __int128)0x876543210u << 36 | 0x876543210ull)) >> (4 * (q - (q >= 9 ? 9 : 0)) + (q >= 9 ? 36 : 0))) & 15u), h = q >= 9;
      if (q >= RQ) rest();
      dma16(gs + (size_t)(288 * h + 16 * k) * (F_ * 2), gv, lb0 + LW * h + 9216 * w + 1024 * k);
      if (!BALL || q < RQ || ts1) asm volatile("s_waitcnt vmcnt(%0)" : : "n"(depth.value) : "memory");
    }
  };
  auto down_dma = [&](int k0, int k1, int lane) {
#pragma unroll 1
    for (int k = k0; k < k1; ++k) {
      const int o = 9216 * wl + 2304 * k;
      const char* ds = (const char*)wd + (size_t)(fb * 36864 + o);
      const uint32_t dl = lb0 + o + 32 * ((4 * wl + k + 1) >> 1);
      if (ROLE == 1) rest();
      dma16d(ds, 16u * lane, dl);
      dma16d(ds + 1024, 16u * lane, dl + 1024);
      uint32_t keep;
      asm volatile("s_mov_b32 %0, m0\n\ts_mov_b32 m0, %3\n\ts_mov_b64 exec, 0xffff\n\tglobal_load_lds_dwordx4 %1, %2" DPOL2 "\n\ts_mov_b64 exec, -1\n\ts_mov_b32 m0, %0\n\ts_waitcnt vmcnt(%4)"
                   : "=&s"(keep) : "v"(16u * lane), "s"(ds + 2048), "s"(dl + 2048), "n"(ROLE == 1 ? DDEPTH : 40) : "memory");
    }
  };
  auto gu_read = [&](int h) {
    const uint32_t ra = lb0 + LW * h + (uint32_t)(((lane >> 1) & 1) * 18432 + (8 * kq + ((lane >> 2) & 3)) * 64 + ((w ^ (kq & 1) ^ (lane & 2)) * 16) + 8 * (lane & 1));
#pragma unroll
    for (int s = 0; s < 9; ++s) {
      const u32x2 h0 = trd(ra + 2048 * s), h1 = trd(ra + 2048 * s + 256);
      bw[9 * h + s] = (u32x4){h0[0], h0[1], h1[0], h1[1]};
    }
#pragma unroll
    for (int s = 0; s < 9; ++s) asm volatile("" : "+v"(bw[9 * h + s]));
  };
  const uint32_t vearly = (uint32_t)((VEARLY >> (8 * (g0 >> 5))) & 0xffull);
#define TOUCH(pa) asm volatile("global_load_dword v126, %0, off" DPOL4 : : "v"(pa) : "memory", "v126")
  uint32_t wv[5], xv[9];
  auto rows_xw = [&](int lane) {
    u32x4 wa, xr, xs;
    const uint32_t xo = (uint32_t)((8 * g2 + 2 * wl) * (D_ * 2));
    asm volatile("global_load_dwordx4 %0, %5, %9\n\tglobal_load_dword %1, %6, %9 offset:1024\n\t"
                 "global_load_dwordx4 %2, %7, %10\n\tglobal_load_dwordx4 %3, %7, %10 offset:1152\n\tglobal_load_dword %4, %8, %10 offset:1024\n\ts_waitcnt vmcnt(0)"
                 : "=&v"(wa), "=&v"(wv[4]), "=&v"(xr), "=&v"(xs), "=&v"(xv[8])
                 : "v"(16u * lane), "v"(4u * (lane & 31)), "v"(xo + 16u * lane), "v"(xo + 4u * lane + 32u * (lane & 32)), "s"(wr), "s"(x) : "memory");
#pragma unroll
    for (int k = 0; k < 4; ++k) { wv[k] = wa[k]; xv[k] = xr[k]; xv[4 + k] = xs[k]; }
  };
  auto ln1 = [&](int g, int o, uint32_t c0) {
    uint32_t uo = u_at(g, 64 * wl + lane);
    uint32_t ta = lb0 + (uint32_t)(o + ((lane >> 2) & 3) * RP + (64 * wl + 16 * kq + 4 * (lane & 3)) * 2);
    uint32_t la = (uint32_t)(o + 2 * wl * RP + 16 * lane), lt = (uint32_t)(o + (2 * wl + (lane >> 5)) * RP + 1024 + 4 * (lane & 31));
    uint32_t sg = O_SU + (uint32_t)(256 * (lane >> 3) + g) * 128u + 16u * (lane & 7);
    asm volatile("" : "+v"(uo), "+v"(ta), "+v"(la), "+v"(lt), "+v"(sg));
    f32x4 z4 = {0.f, 0.f, 0.f, 0.f};
    asm volatile("" : "+v"(z4));
    asm volatile("s_waitcnt vmcnt(0)" : "={v[100:103]}"(xa), "={v[104:107]}"(xc), "={v108}"(r0), "={v109}"(ep));
    TS(0, 1);
    const uint32_t v[9] = {xa[0], xa[1], xa[2], xa[3], xc[0], xc[1], xc[2], xc[3], r0};
    uint32_t c[9];
    f32x2 tf[9];
    T = ((uint32_t)__builtin_amdgcn_readfirstlane(ep) + 1u) * 0x9E3779B1u;
    if (wl == 3) { const uint32_t now = (uint32_t)__builtin_amdgcn_s_memrealtime() + SD; ST16(sg, ((u32x4){T, now, T, now})); }
    const uint32_t tj = ((T >> (3 * wl)) & 0x00070007u) | (((T >> (12 + wl)) & 0x00010001u) << 14);
    uint32_t tw[3] = {(tj & 0xffffu) * 0x10001u, (tj >> 16) * 0x10001u, 0u};
    tw[2] = (lane < 32) ? tw[0] : tw[1];
    asm volatile("" : "+v"(tw[0]), "+v"(tw[1]), "+v"(tw[2]));
    f32x2 rc, nc;
    float e_;
    ln_stats<false>(v, lane, 0.03125f, tf, z4, rc, nc, e_);
    ln_apply<true>(tf, lane, rc, nc, c);
#pragma unroll
    for (int k = 0; k < 9; ++k) c[k] = (c[k] & ~UMASK) | tw[k >> 2];
    *(u32x4*)(smem + la) = (u32x4){c[0], c[1], c[2], c[3]};
    *(u32x4*)(smem + la + RP) = (u32x4){c[4], c[5], c[6], c[7]};
    *(uint32_t*)(smem + lt) = c[8];
    u32x4 ha, hb;
    {
      uint32_t t, cc, n = SPIN_CAP;
      asm volatile("s_mov_b64 exec, 1\n\tv_mov_b32 %2, 1\n\tds_add_u32 %5, %2\n\ts_mov_b64 exec, -1\n"
                   "1:\n\tds_read_b32 %2, %5\n\tds_read_b64_tr_b16 v[40:41], %6\n\tds_read_b64_tr_b16 v[42:43], %6 offset:%7\n\tds_read_b64_tr_b16 v[44:45], %6 offset:512\n\t"
                   "ds_read_b64_tr_b16 v[46:47], %6 offset:%8\n\ts_sub_u32 %4, %4, 1\n\ts_cbranch_scc1 2f\n\ts_waitcnt lgkmcnt(4)\n\tv_readfirstlane_b32 %3, %2\n\t"
                   "s_cmp_lt_u32 %3, 4\n\ts_cbranch_scc1 1b\n2:\n\ts_waitcnt lgkmcnt(0)"
                   : "=&{v[40:43]}"(ha), "=&{v[44:47]}"(hb), "=&v"(t), "=&s"(cc), "+s"(n) : "v"(c0), "v"(ta), "n"(4 * RP), "n"(512 + 4 * RP) : "memory", "scc");
    }
    ST16(uo, ha);
    ST16(uo + 64 * 128, hb);
    if (wl == 0) {
      const u32x2 h4 = trd(ta + 1024), h5 = trd(ta + 1024 + 4 * RP);
      ST16(uo + 128 * 128, ((u32x4){h4[0], h4[1], h5[0], h5[1]}));
    }
  };
  const int dX = 512 - 24 + 8 * bx + bj;
  if constexpr (ROLE == 2) {
    const int tl = tid - 256;
    if (isx) {
      __builtin_amdgcn_s_setprio(QPRIO0);
      *(u32x4*)(smem + X_TB + 16 * tl) = zero4;
      *(u32x4*)(smem + X_TB + 16 * min(256 + tl, (TB + C_U) / 16 - 1)) = zero4;
      const int pr = (wl + XROT) & 3;
      COps e;
      conv_ops(lb0 + X_TB, lb0 + X_UL, lb0 + X_IM, lane, e);
      TOps t;
      table_ops(lb0 + X_IM, lb0 + X_TB, wl & 1, lane, t);
      e.o = lb0 + X_ST + (uint32_t)(16 * ((2 * i + (kq >> 1)) ^ ((i >> 2) & 1)) + 8 * (kq & 1));
      e.f = ca + 28 - 32;
      asm volatile("" : "+v"(e.o), "+v"(e.f));
      float ldt, are, aim, bre, bim, cre, cim, dsk;
      P_WAIT(0, , "v103", "v104", "v105", "v106", "v107", "v109");
      asm volatile("" : "={v109}"(ep));
      T = ((uint32_t)__builtin_amdgcn_readfirstlane(ep) + 1u) * 0x9E3779B1u;
      P_MORE();
      asm volatile("v_lshlrev_b32 %0, 16, v107" : "=v"(dsk) : : "v107");
      TS(0, 1);
      __builtin_amdgcn_s_setprio(QPRIO);
      int* evp = (int*)(sel + 44);
      char* im = smem + X_IM + IP * (tl & 127);
      evp[wl] = (wl < 2) ? gen_y(ldt, are, aim, bre, bim, cre, cim, lb0 + X_IM + IP * tl, [] {} TSA) : gen_x(ldt, are, aim, im);
      TS(0, 2);
      __builtin_amdgcn_s_setprio(QPRIO2);
      gsync(ca + 8, 4);
      if (wl < 2) evp[-4 + wl] = (int)table_tile(t, wl & 1, lane, lb0 + LM + 4 * (128 + 44), dsk TSA);
      if (wl < 2) gsync0(ca + 8, 8); else { count_up(ca + 8); SLEEP(XSL); gwait(ca + 8, 8); }
      const uint32_t cs = (uint32_t)__builtin_amdgcn_readfirstlane(evp[-4]);
      if (wl == 3) *(u32x4*)(smem + X_IM + pad_at(lane & 7)) = zero4;
      TS(0, 3);
      CONV_RUN(2 + pr, e, , "+{v11}"(tid2));
      __builtin_amdgcn_s_setprio(3);
      {
        const int lane = tid2 & 63;
        const u32x4 v = *(const u32x4*)(smem + X_ST + 2048 * pr + 1024 * (lane >> 5) + 16 * ((lane & 31) ^ ((lane >> 3) & 1)));
        if (__builtin_expect(v_put(v, (u32x4){utag(T, 0), utag(T, 1), utag(T, 2), utag(T, 3)}, cs != S16, v_at(dX, 64 * pr + lane)), 0)) {
          DRAIN();
          if (lane == 0) ST4(O_DN + 16u * (4 * (dX - 256) + pr), T);
        }
        if (pr == 0 && lane == 0) asm volatile("global_store_dwordx2 %0, %1, %2 sc1" : : "v"(O_SC + 16u * dX), "v"((u32x2){T, cs}), "s"(ws) : "memory");
      }
      TS(0, 6);
    }
    if (!isx) __builtin_amdgcn_s_setprio(PPRIO);
    asm volatile("" : "+v"(tid2));
    if (!isx) rows_xw(tid2 & 63);
    if (!isx && !ffn) {
      SLEEP(RSLEEP);
#pragma unroll
      for (int k = 0; k < 7; ++k) ST16(O_SL + (uint32_t)((6 * bx + bj - 18) * 1792 + 256 * k - 256 + tid2) * 16u, ones);
      DRAIN();
    }
    if (!isx) BAR();
    const uint32_t tp = (uint32_t)__builtin_amdgcn_readfirstlane(cnt[4]);
    if (!isx) T = (uint32_t)__builtin_amdgcn_readfirstlane(sel[43]);
    if (!isx && w == 4) v_look(tp, tid2 & 63);
    v_time();
  } else {
    __builtin_amdgcn_s_setprio(3);
    if (tid < 16) cnt[tid] = 0u;
    BAR();
    if (tid < 20) ((uint32_t*)(hm + 320))[tid] = 0u;
    if (tid >= 184) *(u32x4*)(smem + L_SC - 16 * 184 + 16 * tid) = (u32x4){0x41804180u, 0x41804180u, 0x41804180u, 0x41804180u};
    uint32_t sink = 0;
    ln1(tg, 0, ca + 12);
    TS(0, 2);
    __builtin_amdgcn_s_setprio(NPRIO);
    asm volatile("s_waitcnt vmcnt(0)" : "+v"(sink) : : "memory");
    TS(0, 13);
    if (w == 0) {
      const uint32_t xr = O_XC + (uint32_t)((1021 - 4 * g0) * 72 + lane) * 16u;
      ST16(xr, ones);
      ST16(xr + 1024u, ones);
      ST16(xr + 2048u, ones);
      if (lane < 24) ST16(xr + 3072u, ones);
    }
    if (w == 2 && b < 72 && lane < 8) ST16(O_FB + (uint32_t)(8 * b + lane) * 16u, ones);
    if (isx) {
      rows_xw(lane);
      char* sp = smem + X_RW(w) + 16 * lane;
      *(u32x4*)sp = (u32x4){xv[0], xv[1], xv[2], xv[3]};
      *(u32x4*)(sp + 1024) = (u32x4){xv[4], xv[5], xv[6], xv[7]};
      *(u32x2*)(smem + X_RW(w) + 2048 + 8 * lane) = (u32x2){xv[8], wv[4]};
      if (w == 0) *(u32x4*)(smem + X_WR + 16 * lane) = (u32x4){wv[0], wv[1], wv[2], wv[3]};
    }
    SLEEP(ESLEEP);
    uint32_t tu = latest(gate(ws, O_SU + (uint32_t)(256 * bx + (ALLP ? tid : 64 * w + 4 * i + (bj & 3))) * 128u, T));
    asm volatile("s_mov_b64 exec, 1\n\tds_max_u32 %0, %1\n\ts_mov_b64 exec, -1" : : "v"(ca + 16), "v"(tu) : "memory");
    if (w == 0) sel[43] = T;
    if (isx) {
      const uint32_t xo = u_at(tid, dX);
      char* sm = smem + X_UL + C_U - 16 + 16 * tid;
      const u32x4 ut = {utag(T, 0), utag(T, 1), utag(T, 2), utag(T, 3)};
      __builtin_amdgcn_s_setprio(3);
      until(tu + UD + XD);
      u32x4 uw;
      asm volatile("global_load_dwordx4 %0, %1, %2 sc1" : "=&v"(uw) : "v"(xo), "s"(ws) : "memory");
      asm volatile("s_waitcnt vmcnt(0)" : "+v"(uw));
      uw ^= ut;
      u_again(uw, ut, xo);
      *(u32x4*)(sm + 16) = uw;
      BAR();
      if (w < 3) {
        const u32x4 pw = *(const u32x4*)sm;
        if (w < 2) sums<true>(smem + X_IM + 16 * tid, pw, uw); else sums<false>(smem + X_IM + 16 * tid, pw, uw);
        count_up(ca + 28);
      }
    } else {
      until(tu + UD + WD);
      BAR();
    }
    tu = (uint32_t)__builtin_amdgcn_readfirstlane(cnt[4]);
    TS(0, 3);
    __builtin_amdgcn_s_setprio(DPRIO);
    if (ffn) {
      ts1 = (tu + UD + VD2 - TSTOP) | 1u;
      gu_dma(0, N0, IC<40>{});
      gu_dma(N0, NA, IC<WDEPTH>{});
      asm volatile("s_waitcnt vmcnt(%0)" : : "n"(NA - 9) : "memory");
      gsync0(ca + 4, 4);
      gu_read(0);
      asm volatile("s_waitcnt lgkmcnt(0)" ::: "memory");
      count_up(ca + 36);
      TS(0, 4);
      gu_dma(NA, 18, IC<WDEPTH2>{});
      gwait(ca + 36, 4);
      down_dma(0, KN, lane);
      asm volatile("s_waitcnt vmcnt(%0)" : : "n"(3 * KN) : "memory");
      count_up(ca + 4);
      if (STP && w == 1)
        asm volatile("global_load_dword v126, %0, %1 sc1" : : "v"(O_SC + 1024u * (fb >> 4) + 128u * (lane & 7)), "s"(ws) : "memory", "v126");
      TS(0, 7);
    } else {
      if (!isx && C3) {
        const int n = 6 * min(tid, 191) + bj - 18, r = n >> 2;
        const char* wb = (const char*)((n & 1) ? wu : wg) + bx * 1152;
        const int c0 = (3 - (int)((uintptr_t)wb >> 7)) & 7;
        wb += (uint32_t)((r + 144 * ((r >= 144) + ((n >> 1) & 1))) * (F_ * 2) + 128 * c0);
        TOUCH(wb);
        if (c0 == 0) TOUCH(wb + 1024);
      }
      auto d_line = [&](int k) {
        const int n = min((k * 14 + bj - 18) * 64 + lane, 5183), q = n / 1296, r = n - 1296 * q;
        return (const char*)wd + (uint32_t)(bx * 5184 + (r / 18) * 72 + 18 * (HREV ? 3 - q : q) + r % 18) * 128u;
      };
      if (isx && w == 3) v_look(tu, lane);
      int k = w;
      if (w < HW) {
#pragma unroll 1
        for (; k < 6 && (int)((uint32_t)__builtin_amdgcn_s_memrealtime() - (tu + UD + VD2 - HSTOP)) < 0; k += HW) { TOUCH(d_line(k)); DRAIN(); }
      }
      TS(0, 4);
      v_time();
      until(tv + RD2);
      if (w < HW) {
#pragma unroll 1
        for (; k < 6; k += HW) { TOUCH(d_line(k)); if (!HALL) DRAIN(); }
      }
      TS(0, 7);
    }
    v_time();
    tk = tv + KD;
  }
  {
  const int tid = tid2, lane = tid & 63, i = lane & 15, kq = lane >> 4;
  __builtin_amdgcn_s_setprio(3);
  const int PL = ffn ? 6400 : LW + 6656;
  f32x2 xf[9];
  f32x4 za = {0.f, 0.f, 0.f, 0.f}, zb = za;
  uint32_t al, ha, h0a, h1a, so, da;
  auto ffn_terms = [&] {
    al = O_XC + 64u * (i >> 3) + 16u * kq + 128u * w;
    ha = (ROLE == 1) ? (uint32_t)((4 * (kq & 1) + 2 * (kq >> 1) + (i >> 3)) * 80 + 16 * w + 2 * (i & 7)) : 0u;
    h0a = (uint32_t)(min(i, 8) * 80 + 16 * kq); h1a = (uint32_t)(min(i ^ 8, 8) * 80 + 16 * kq);
    so = O_SL + (uint32_t)fb * SP + ((ROLE == 2) ? 16u * tid - 4096u : 8192u + 16u * lane);
    da = lb0 + (uint32_t)((8 * kq + ((lane >> 2) & 3)) * 1152 + 32 * ((8 * kq + ((lane >> 2) & 3) + 2) >> 2) + 8 * (lane & 3) + ((ROLE == 2) ? 128 * wl : 1024));
    if (ROLE == 2) asm volatile("" : "+v"(h0a), "+v"(h1a), "+v"(so), "+v"(da)); else asm volatile("" : "+v"(ha), "+v"(al), "+v"(da));
  };
  if (ROLE == 1) ffn_terms();
  uint32_t hv[9] = {}, nv[9] = {}, keya = 0u, keyb = 0u;
  int rka = 0, rkb = 0;
  auto put_row = [&](const uint32_t* r, int j, const void* base, uint32_t ro) {
    const u32x4 tl = {r[8], (uint32_t)DPP_I((int)r[8], 0x55, 0xf), (uint32_t)DPP_I((int)r[8], 0xaa, 0xf), (uint32_t)DPP_I((int)r[8], 0xff, 0xf)};
    asm volatile("global_store_dwordx4 %0, %1, %2 sc1\n\ts_nop 1" : : "v"(ro + 16u * lane), "v"((u32x4){r[4 * j], r[4 * j + 1], r[4 * j + 2], r[4 * j + 3]}), "s"(base) : "memory");
    if ((lane & 35) == 32 * j) asm volatile("global_store_dwordx4 %0, %1, %2 offset:1024 sc1\n\ts_nop 1" : : "v"(ro + 4u * (lane & 31)), "v"(tl), "s"(base) : "memory");
  };
  if (ln2) {
    const int tl = tid - 256, d1 = 64 * (tl >> 5) + (tl & 31), d3 = 64 * ((tl >> 4) & 7) + 32 + (tl & 15), d2 = (tl < 128) ? d3 + 16 : min(384 + tl, 575);
    uint32_t vo = v_at(d1, g0), vo2 = v_at(d2, g0), vo3 = v_at(d3, g0);
    u32x4 ut = {utag(T, 0), utag(T, 1), utag(T, 2), utag(T, 3)};
    u32x4 va, vc = ut, vb = ut;
    asm volatile("" : "+v"(vo), "+v"(vo2), "+v"(vo3), "+v"(ut), "+v"(vc), "+v"(vb));
    TS(0, 7);
    until(tv);
    TS(0, 8);
    asm volatile("global_load_dwordx4 %0, %2, %4 sc1\n\ts_cmp_lt_u32 %5, 2\n\ts_cbranch_scc1 1f\n\tglobal_load_dwordx4 %1, %3, %4 sc1\n1:"
                 : "=&v"(va), "+v"(vc) : "v"(vo), "v"(vo3), "s"(ws), "s"(wl) : "memory", "scc");
    until(tv + V2D);
    asm volatile("s_cmp_eq_u32 %3, 3\n\ts_cbranch_scc1 1f\n\tglobal_load_dwordx4 %0, %1, %2 sc1\n1:" : "+v"(vb) : "v"(vo2), "s"(ws), "s"(wl) : "memory", "scc");
    __builtin_amdgcn_sched_barrier(0);
    if (isx) {
      const u32x4 ra = *(const u32x4*)(smem + X_RW(wl) + 16 * lane), rb = *(const u32x4*)(smem + X_RW(wl) + 1024 + 16 * lane), rw = *(const u32x4*)(smem + X_WR + 16 * lane);
      const u32x2 rc = *(const u32x2*)(smem + X_RW(wl) + 2048 + 8 * lane);
#pragma unroll
      for (int k = 0; k < 4; ++k) { xv[k] = ra[k]; xv[4 + k] = rb[k]; wv[k] = rw[k]; }
      xv[8] = rc[0]; wv[4] = rc[1];
    }
    uint32_t pa = (uint32_t)(PL + 9216 * wl + 32 * lane), pb = (uint32_t)(PL + 9216 * wl + 2048 + 8 * (lane & 31)), ps = (lane < 32) ? 0u : 16u;
    uint32_t kid = (uint32_t)(((255 - g2) << 5) | (7 - 2 * wl - (lane >> 5)));
    uint32_t px = (uint32_t)(PL + 4 * d1), py = (uint32_t)(PL + 4 * d2), pz = (uint32_t)(PL + 4 * d3);
#pragma unroll
    for (int k = 0; k < 9; ++k) xf[k] = (f32x2){bflo(xv[k]), bfhi(xv[k])};
    asm volatile("" : "+v"(xf[0]), "+v"(xf[1]), "+v"(xf[2]), "+v"(xf[3]), "+v"(xf[4]), "+v"(xf[5]), "+v"(xf[6]), "+v"(xf[7]), "+v"(xf[8]));
    asm volatile("" : "+v"(za), "+v"(zb), "+v"(pa), "+v"(pb), "+v"(ps), "+v"(kid), "+v"(px), "+v"(py), "+v"(pz));
    float sv[10], ws_ = dot2(wv[4], 0x3f803f80u, 0.f) * 0.5f;
#pragma unroll
    for (int k = 0; k < 4; ++k) ws_ = dot2(wv[k], 0x3f803f80u, ws_);
    ws_ = wave_sum(ws_);
#pragma unroll
    for (int k = 0; k < 10; ++k) sv[k] = 16.f;
    uint32_t ka_ = lb0 + LM + 4u * (128 + 48 + 2 * wl + (lane >> 5)), kb_ = lb0 + LM + 4u * (128 + 48);
    uint32_t ko_ = O_KEY + (uint32_t)(256 * (lane & 7) + 8 * (g2 & 31) + (g2 >> 5)) * 16u;
    asm volatile("" : "+v"(ws_), "+v"(sv[0]), "+v"(sv[1]), "+v"(sv[2]), "+v"(sv[3]), "+v"(sv[4]), "+v"(sv[5]), "+v"(sv[6]), "+v"(sv[7]), "+v"(sv[8]), "+v"(sv[9]),
                 "+v"(ka_), "+v"(kb_), "+v"(ko_));
    if (ffn) gwait(ca + 36, 4);
    asm volatile("s_waitcnt vmcnt(1)\n\ts_cmp_eq_u32 %2, 3\n\ts_cbranch_scc0 1f\n\ts_waitcnt vmcnt(0)\n1:" : "+v"(va), "+v"(vc) : "s"(wl) : "memory", "scc");
    auto turn1 = [&] {
#pragma unroll
      for (int e = 0; e < 4; ++e) *(uint32_t*)(smem + px + 9216 * e) = va[e];
      if (wl >= 2) {
#pragma unroll
        for (int e = 0; e < 4; ++e) *(uint32_t*)(smem + pz + 9216 * e) = vc[e];
      }
    };
    auto turn = [&] {
      if (wl < 3) {
#pragma unroll
        for (int e = 0; e < 4; ++e) *(uint32_t*)(smem + py + 9216 * e) = vb[e];
      }
    };
    va ^= ut; vc ^= ut;
    turn1();
    asm volatile("s_waitcnt vmcnt(0)" : "+v"(vb) : : "memory");
    vb ^= ut;
    turn();
    const u32x4 vs = va | vb | vc;
    if (__builtin_expect(__any(((vs[0] | vs[1] | vs[2] | vs[3]) & UMASK) != 0u), 0)) {
#pragma unroll 1
      for (int spin = 0; spin < 4; ++spin) {
        const bool na = ((va[0] | va[1] | va[2] | va[3]) & UMASK) != 0u, nb = ((vb[0] | vb[1] | vb[2] | vb[3]) & UMASK) != 0u, nc = ((vc[0] | vc[1] | vc[2] | vc[3]) & UMASK) != 0u;
        if (!__any(na || nb || nc)) break;
        TSC(16);
        if (na) LD16(va, vo);
        if (nb) LD16(vb, vo2);
        if (nc) LD16(vc, vo3);
        asm volatile("s_waitcnt vmcnt(0)" : "+v"(va), "+v"(vb), "+v"(vc) : : "memory");
        if (na) va ^= ut;
        if (nb) vb ^= ut;
        if (nc) vc ^= ut;
      }
      auto again = [&](u32x4& v, uint32_t o, int d) {
        const int J = g0 >> 5;
        const uint32_t dn = O_DN + 16u * (uint32_t)((d < 512) ? 4 * (d >> 1) + ((0x01013232 >> (4 * J)) & 3) : 4 * (d - 256) + (J >> 1));
        bool pend = ((v[0] | v[1] | v[2] | v[3]) & UMASK) != 0u;
#pragma unroll 1
        for (int spin = 0; spin < SPIN_CAP && __any(pend); ++spin) {
          TSC(16);
          SLEEP(PSLEEP);
          if (pend) {
            uint32_t fin = ~T;
            if ((v[0] & v[1] & v[2] & v[3] & LMASK) == LMASK)
              asm volatile("global_load_dword %0, %1, %2 sc1\n\ts_waitcnt vmcnt(0)" : "=&v"(fin) : "v"(dn), "s"(ws) : "memory");
            LD16W(v, o);
            v ^= ut;
            pend = ((v[0] | v[1] | v[2] | v[3]) & UMASK) != 0u;
            if (pend && fin == T) {
              u32x2 sc;
              for (int k = 0; k < SPIN_CAP; ++k) {
                asm volatile("global_load_dwordx2 %0, %1, %2 sc1\n\ts_waitcnt vmcnt(0)" : "=&v"(sc) : "v"(O_SC + 16u * d), "s"(ws) : "memory");
                if (sc[0] == T) break;
              }
              *(u16*)(smem + L_SC + 2 * d) = (u16)(sc[1] >> 16);
              cnt[10] = 1u;
              v = (v ^ ut) & ~LMASK;
              pend = false;
            }
          }
        }
      };
      again(va, vo, d1); again(vc, vo3, d3); again(vb, vo2, d2);
      turn1();
      turn();
    }
    u32x4 a0, a1;
    u32x2 a2, cf;
    {
      uint32_t cc, n = SPIN_CAP;
      asm volatile("s_mov_b64 exec, 1\n\tv_mov_b32 v90, 1\n\tds_add_u32 %6, v90\n\ts_mov_b64 exec, -1\n"
                   "1:\n\tds_read2_b32 %3, %6 offset1:10\n\tds_read_b128 %0, %7\n\tds_read_b128 %1, %7 offset:16\n\tds_read_b64 %2, %8\n\t"
                   "s_sub_u32 %5, %5, 1\n\ts_cbranch_scc1 2f\n\ts_waitcnt lgkmcnt(3)\n\tv_readfirstlane_b32 %4, v90\n\t"
                   "s_cmp_lt_u32 %4, 4\n\ts_cbranch_scc1 1b\n2:\n\ts_waitcnt lgkmcnt(0)"
                   : "=&v"(a0), "=&v"(a1), "=&v"(a2), "=&{v[90:91]}"(cf), "=&s"(cc), "+s"(n) : "v"(ca), "v"(lb0 + pa), "v"(lb0 + pb) : "memory", "scc");
    }
    TS(0, 9);
    if (__builtin_expect(__builtin_amdgcn_readfirstlane(cf[1]) != 0, 0)) {
      const u32x4 q = *(const u32x4*)(smem + L_SC + 16 * lane);
      const uint32_t r = *(const uint32_t*)(smem + L_SC + 1024 + 4 * (lane & 31));
#pragma unroll
      for (int k = 0; k < 4; ++k) { sv[2 * k] = bflo(q[k]); sv[2 * k + 1] = bfhi(q[k]); }
      sv[8] = bflo(r); sv[9] = bfhi(r);
    }
    const uint32_t av[10] = {a0[0], a0[1], a0[2], a0[3], a1[0], a1[1], a1[2], a1[3], a2[0] >> ps, a2[1] >> ps};
    f32x2 h[9];
#pragma unroll
    for (int k = 0; k < 10; ++k) {
      asm("v_fma_mix_f32 %0, %1, %2, %3 op_sel_hi:[1,0,0]" : "=v"(h[(k < 8) ? k >> 1 : 8][k & 1]) : "v"(av[k]), "v"(sv[k]), "v"(xf[(k < 8) ? k >> 1 : 8][k & 1]));
      if (k < 8) asm("v_fma_mix_f32 %0, %1, %2, %3 op_sel:[1,0,0] op_sel_hi:[1,0,0]" : "=v"(h[4 + (k >> 1)][k & 1]) : "v"(av[k]), "v"(sv[k]), "v"(xf[4 + (k >> 1)][k & 1]));
    }
#pragma unroll
    for (int k = 0; k < 9; ++k) hv[k] = pack2(h[k][0], h[k][1]);
    const float da = dot2(hv[2], wv[2], dot2(hv[0], wv[0], zb[0])) + dot2(hv[3], wv[3], dot2(hv[1], wv[1], zb[1]));
    const float db = dot2(hv[6], wv[2], dot2(hv[4], wv[0], zb[2])) + dot2(hv[7], wv[3], dot2(hv[5], wv[1], zb[3]));
    float dt = dot2(hv[8], wv[4], fold32(da, db));
    f32x2 rc, nc;
    ln_stats<true>(hv, lane, 1.f, xf, za, rc, nc, dt);
    dt = (lane < 32) ? dt * rc[0] + ws_ * nc[0] : dt * rc[1] + ws_ * nc[1];
    const uint32_t bb = pack2(dt, dt) & 0xffff0000u;
    const uint32_t kk = (bb ^ (((uint32_t)((int)bb >> 31) & 0x7fff0000u) | 0x80000000u)) | kid;
    u32x4 ka, kb;
    uint32_t old;
    asm volatile("s_mov_b64 exec, %5\n\tds_write_b32 %3, %4\n\ts_mov_b64 exec, 1\n\tv_mov_b32 %2, 1\n\tds_add_rtn_u32 %2, %7, %2\n\ts_mov_b64 exec, -1\n\t"
                 "ds_read_b128 %0, %6\n\tds_read_b128 %1, %6 offset:16\n\ts_waitcnt lgkmcnt(0)"
                 : "=&v"(ka), "=&v"(kb), "=&v"(old) : "v"(ka_), "v"(kk), "s"(0x8000000080000000ull), "v"(kb_), "v"(ca) : "memory");
    if (__builtin_amdgcn_readfirstlane(old) == 7) {
      uint32_t m1 = max(max(ka[0], ka[1]), ka[2]), m2 = med3u(ka[0], ka[1], ka[2]), m3 = min(min(ka[0], ka[1]), ka[2]);
#pragma unroll
      for (int e = 0; e < 5; ++e) {
        const uint32_t kx = e ? kb[e - 1] : ka[3];
        m3 = med3u(m2, m3, kx); m2 = med3u(m1, m2, kx); m1 = max(m1, kx);
      }
      if (lane < 8) ST16(ko_, ((u32x4){m1 | 24u, m2 | 16u, m3 | 8u, T & ~1u}));
    }
    TS(0, 10);
    keya = (uint32_t)__builtin_amdgcn_readlane(kk, 31); keyb = (uint32_t)__builtin_amdgcn_readlane(kk, 63);
    ln_apply<false>(xf, lane, rc, nc, nv);
    gwait(ca, 8);
    if (wl == 1) {
      const u32x4 kv = *(const u32x4*)&kl[4 * (lane & 1)];
      if (lane < 2) ST16(O_KF + (uint32_t)(2 * g2 + lane) * 16u, ((u32x4){__builtin_amdgcn_perm(kv[1], kv[0], 0x07060302u), T, __builtin_amdgcn_perm(kv[3], kv[2], 0x07060302u), T}));
      if (ROLE == 2 && b == 0 && lane == 0) ST4(O_EP, T * 0x0E8B2F51u);
    }
    const uint32_t ko = kl[lane & 7];
    rka = FORCEFB + __popcll(__ballot(ko > keya) & 0xffull); rkb = FORCEFB + __popcll(__ballot(ko > keyb) & 0xffull);
#pragma unroll
    for (int k = 0; k < 9; ++k) {
      const f32x2 o = xf[k] + (f32x2){bflo(nv[k]), bfhi(nv[k])};
      hv[k] = pack2(o[0], o[1]);
      nv[k] = min(nv[k], 0xfffffffeu);
    }
    if (rka < 3) put_row(nv, 0, ws, O_XC + (uint32_t)(1023 - 4 * g2 - rka) * 1152u);
    if (rkb < 3) put_row(nv, 1, ws, O_XC + (uint32_t)(1023 - 4 * g2 - rkb) * 1152u);
    if (!LPOS && ffn) down_dma(KN, 4, lane);
    ffn_terms();
  }
  uint32_t k3rd = 0u, k4th = 0u;
  uint32_t q0 = lb0 + LM + 4u * lane, q1 = lb0 + LM + 32u * w, q2 = q1 + 4u * (lane & 7), q3 = lb0 + LM + 512u + 4u * (lane & 7), q4 = lb0 + LM + (uint32_t)(tid & ~7);
  uint32_t r8 = C_, sb = lb0 + LM + 512u;
  asm volatile("" : "+v"(q0), "+v"(q1), "+v"(q2), "+v"(q3), "+v"(q4), "+v"(r8), "+v"(sb));
  if constexpr (ROLE == 1) {
    u32x4 key;
    const uint32_t kof = O_KEY + (uint32_t)(256 * bx + tid) * 16u;
    until(tk);
    if (!ffn) SLEEP(NSLEEP);
    LD16(key, kof);
    if (ffn) {
      gwait0(ca + 4, 8);
      gu_read(1);
    }
    asm volatile("s_waitcnt vmcnt(0)" : "+v"(key) : : "memory");
    if (__builtin_expect(__any(key[3] != (T & ~1u)), 0)) {
#pragma unroll 1
      for (int spin = 0; spin < SPIN_CAP; ++spin) {
        const bool need = key[3] != (T & ~1u);
        if (!__any(need)) break;
        TSC(15);
        SLEEP(KBACK);
        if (need) LD16W(key, kof);
      }
    }
    TS(0, 11);
    const uint32_t m1 = oct_maxu(key[0]);
    const bool hit = key[0] == m1;
    const uint32_t k0 = hit ? key[1] : key[0], k1 = hit ? key[2] : key[1];
    const uint32_t m2 = oct_maxu(k0);
    *LP(u32x2, q4) = (u32x2){m1, m2};
    k3rd = (k0 == m2) ? k1 : k0;
    k4th = (k0 == m2) ? (hit ? 0u : key[2]) : k1;
    if (RPF == 1 && !ffn && w == (bj & 3)) {
      const uint32_t r1 = O_XC + ((m1 >> 3) & 0x3ffu) * 1152u + 128u * (lane & 7), r2 = O_XC + ((m2 >> 3) & 0x3ffu) * 1152u + 128u * (lane & 7);
      asm volatile("global_load_dword v126, %0, %3 sc1\n\tglobal_load_dword v126, %1, %3 sc1\n\tglobal_load_dword v126, %2, %3 offset:1024 sc1"
                   : : "v"(r1), "v"(r2), "v"(((lane & 1) ? r1 : r2) - 128u * (lane & 7)), "s"(ws) : "memory", "v126");
    }
  }
  auto pick = [&](auto co, auto so_) {
    BAR();
    const uint32_t c = *LP(uint32_t, q0 + co.value), own = *LP(uint32_t, q2 + co.value);
    const u32x4 la = *LP(u32x4, q1 + co.value), lb = *LP(u32x4, q1 + co.value + 16);
    uint32_t r = r8;
    asm("v_cmp_gt_u32 s[40:41], %1, %2\n\tv_cmp_gt_u32 s[42:43], %1, %3\n\tv_cmp_gt_u32 s[44:45], %1, %4\n\tv_cmp_gt_u32 s[46:47], %1, %5\n\t"
        "v_cmp_gt_u32 s[48:49], %1, %6\n\tv_cmp_gt_u32 s[50:51], %1, %7\n\tv_cmp_gt_u32 s[52:53], %1, %8\n\tv_cmp_gt_u32 s[54:55], %1, %9\n\t"
        "s_bcnt1_i32_b64 s40, s[40:41]\n\ts_bcnt1_i32_b64 s42, s[42:43]\n\tv_writelane_b32 %0, s40, 0\n\ts_bcnt1_i32_b64 s44, s[44:45]\n\t"
        "v_writelane_b32 %0, s42, 1\n\ts_bcnt1_i32_b64 s46, s[46:47]\n\tv_writelane_b32 %0, s44, 2\n\ts_bcnt1_i32_b64 s48, s[48:49]\n\t"
        "v_writelane_b32 %0, s46, 3\n\ts_bcnt1_i32_b64 s50, s[50:51]\n\tv_writelane_b32 %0, s48, 4\n\ts_bcnt1_i32_b64 s52, s[52:53]\n\t"
        "v_writelane_b32 %0, s50, 5\n\ts_bcnt1_i32_b64 s54, s[54:55]\n\tv_writelane_b32 %0, s52, 6\n\tv_writelane_b32 %0, s54, 7"
        : "+v"(r) : "v"(c), "v"(la[0]), "v"(la[1]), "v"(la[2]), "v"(la[3]), "v"(lb[0]), "v"(lb[1]), "v"(lb[2]), "v"(lb[3])
        : "scc", "s40", "s41", "s42", "s43", "s44", "s45", "s46", "s47", "s48", "s49", "s50", "s51", "s52", "s53", "s54", "s55");
    *LP(uint32_t, sb + 4u * min(r, (uint32_t)C_) + so_.value) = own;
    BAR();
  };
  auto rows = [&](uint32_t ao, auto&& meanwhile) {
    u32x4 aa, ab, ac = __builtin_bit_cast(u32x4, zb);
    asm volatile("global_load_dwordx4 %0, %3, %4 sc1\n\tglobal_load_dwordx4 %1, %3, %4 offset:512 sc1\n\t"
                 "s_cmp_lg_u32 %5, 0\n\ts_cbranch_scc1 1f\n\tglobal_load_dwordx4 %2, %3, %4 offset:1024 sc1\n1:"
                 : "=&v"(aa), "=&v"(ab), "+v"(ac) : "v"(ao), "s"(ws), "s"(w) : "memory", "scc");
    __builtin_amdgcn_sched_barrier(0);
    meanwhile();
    asm volatile("s_waitcnt vmcnt(0)" : "+v"(aa), "+v"(ab), "+v"(ac) : : "memory");
    if (__builtin_expect(__any(max(max(max4(aa), max4(ab)), max4(ac)) == 0xffffffffu), 0)) {
#pragma unroll 1
      for (int spin = 0; spin < SPIN_CAP; ++spin) {
        const bool na = max4(aa) == 0xffffffffu, nb = max4(ab) == 0xffffffffu, nc = max4(ac) == 0xffffffffu;
        if (!__any(na || nb || nc)) break;
        TSC(14);
        SLEEP(PSLEEP);
        if (na) LD16W(aa, ao);
        if (nb) LD16W(ab, ao + 512u);
        if (nc) LD16W(ac, ao + 1024u);
      }
    }
    *(u32x4*)(smem + LA + 16 * tid) = aa;
    *(u32x4*)(smem + LA + 4096 + 16 * tid) = ab;
    if (w == 0) *(u32x4*)(smem + LA + 8192 + 16 * tid) = ac;
  };
  auto rows_dma = [&](uint32_t ao) {
    uint32_t dl = lb0 + LA + 1024u * w, keep;
    asm volatile("s_mov_b32 %0, m0\n\ts_mov_b32 m0, %1\n\ts_add_u32 %1, %1, 4096 - 512\n\tglobal_load_lds_dwordx4 %2, %3 sc1\n\ts_mov_b32 m0, %1\n\ts_add_u32 %1, %1, 4096 - 512\n\t"
                 "global_load_lds_dwordx4 %2, %3 offset:512 sc1\n\ts_cmp_lg_u32 %4, 0\n\ts_cbranch_scc1 1f\n\ts_mov_b32 m0, %1\n\ts_nop 0\n\t"
                 "global_load_lds_dwordx4 %2, %3 offset:1024 sc1\n1:\n\ts_mov_b32 m0, %0" : "=&s"(keep), "+s"(dl) : "v"(ao), "s"(ws), "s"(w) : "memory", "scc");
  };
  auto get_bd = [&](int t0, int t1) {
#pragma unroll
    for (int t = t0; t < t1; ++t) {
      const u32x2 h0 = trd(da + 512 * (t >> 2) + 32 * (t & 3)), h1 = trd(da + 512 * (t >> 2) + 32 * (t & 3) + 4608 + 32);
      bw[t] = (u32x4){h0[0], h0[1], h1[0], h1[1]};
    }
#pragma unroll
    for (int t = t0; t < t1; ++t) asm volatile("" : "+v"(bw[t]));
  };
  pick(IC<0>{}, IC<0>{});
  uint32_t mykey = *LP(uint32_t, q3), ro = ((mykey >> 3) & 0x3ffu) * 1152u;
  TS(0, 12);
  auto check = [&] {
    cnt[(k3rd > (uint32_t)__builtin_amdgcn_readlane(mykey, 7)) ? 5 : 6] = 1u;
    const uint32_t m3 = oct_maxu(k3rd);
    if (k3rd == m3) k3rd = k4th;
    if ((lane & 7) == 0) cand[72 + (tid >> 3)] = m3;
    if (w == 0 && lane < 32) cand[(lane < 8) ? 64 + lane : 96 + lane] = (lane < 8) ? mykey : 0u;
  };
  if constexpr (ROLE == 1) {
    if (ffn && !FORCEFB) rows_dma(ro + al);
    check();
    DRAIN();
  } else if (ffn) {
    if (LPOS) { SLEEP(LSLEEP); down_dma(KN, 4, lane); } else { DRAIN(); gsync0(ca, 12); get_bd(0, 8); }
  }
  BAR();
  TS(0, 5);
  auto put_out = [&](int j, uint32_t k0, int rk) {
    const uint64_t mine = __ballot(((mykey ^ k0) & ~24u) == 0u) & 0xffull;
    if (mine == 0ull) put_row(hv, j, out, (uint32_t)((8 * g2 + 2 * wl + j) * (D_ * 2)));
    else if (rk >= 3) put_row(nv, j, ws, O_FB + 1152u * (uint32_t)__builtin_ctzll(mine));
  };
  auto warm_out = [&] {
    const uint32_t k0 = (lane < 9) ? keya : keyb;
    bool hit = false;
#pragma unroll
    for (int e = 0; e < 8; ++e) hit |= (((uint32_t)__builtin_amdgcn_readlane(mykey, e) ^ k0) & ~24u) == 0u;
    if (OTP && lane < 18 && hit) asm volatile("global_load_dword v126, %0, %1" : : "v"((uint32_t)((8 * g2 + 2 * wl) * (D_ * 2) + 128 * lane)), "s"(out) : "memory", "v126");
  };
  auto rest_out = [&] { put_out(0, keya, rka); put_out(1, keyb, rkb); };
  f32x4 lo, hi;
  auto gate_up = [&] {
    u32x4 av[5];
    const uint32_t aa = lb0 + LA + 16u * lane;
#pragma unroll
    for (int j = 0; j < 5; ++j) asm volatile("ds_read_b128 %0, %1 offset:%2" : "=v"(av[j]) : "v"(aa), "n"(1024 * j) : "memory");
    lo = (f32x4){0.f, 0.f, 0.f, 0.f}; hi = lo;
#pragma unroll
    for (int j = 0; j < 9; ++j) {
      if (j < 5) asm volatile("s_waitcnt lgkmcnt(4)" : "+v"(av[j % 5]));
      else if (j == 5) asm volatile("s_waitcnt lgkmcnt(2)" : "+v"(av[0]), "+v"(av[1]));
      else if (j == 7) asm volatile("s_waitcnt lgkmcnt(0)" : "+v"(av[2]), "+v"(av[3]));
      lo = MMB(av[j % 5], bw[2 * j], lo);
      hi = MMB(av[j % 5], bw[2 * j + 1], hi);
      __builtin_amdgcn_sched_barrier(0);
      if (j < 4) asm volatile("ds_read_b128 %0, %1 offset:%2" : "=v"(av[j]) : "v"(aa), "n"(1024 * (j + 5)) : "memory");
    }
  };
  uint32_t fl;
  asm volatile("ds_read_b32 %0, %1" : "=v"(fl) : "v"(ca + 20) : "memory");
  if (ROLE == 1) gate_up();
  asm volatile("s_waitcnt lgkmcnt(0)" : "+v"(fl));
  const bool slow = FORCESLOW || FORCEFB || __builtin_amdgcn_readfirstlane(fl) != 0;
  if (__builtin_expect(slow, 0)) {
    TSC(17);
    pick(IC<256>{}, IC<64>{});
    mykey = sel[16 + (lane & 7)];
    ro = ((mykey >> 3) & 0x3ffu) * 1152u;
    const uint32_t k8 = (uint32_t)__builtin_amdgcn_readlane(mykey, 7);
    if (ROLE == 1 && k3rd > k8) cnt[11] = 1u;
    if (w == 0 && __any((mykey & 24u) == 8u)) {
      const uint32_t pk = sel[16 + (lane >> 3)], gi = (pk >> 5) & 0xffu;
      u32x4 kf;
#pragma unroll 1
      for (int spin = 0; spin < SPIN_CAP; ++spin) {
        LD16W(kf, O_KF + (2u * (255u - gi) + ((lane >> 2) & 1u)) * 16u);
        if (!__any(kf[1] != T || kf[3] != T)) break;
        SLEEP(KBACK);
      }
      const uint32_t wd_ = (lane & 2) ? kf[2] : kf[0], kt = ((lane & 1) ? wd_ & 0xffff0000u : wd_ << 16) | (gi << 5) | (7u - (lane & 7));
      const uint64_t up = __ballot((pk & 24u) == 8u && kt >= (k8 & ~24u));
      if (__popcll((up >> (lane & 56)) & 0xffull) > 3) cnt[11] = 1u;
    }
    BAR();
    if (FORCESLOW > 1 || FORCEFB || __builtin_amdgcn_readfirstlane(cnt[11]) != 0) {
    u32x4 key;
#pragma unroll 1
    for (int spin = 0; spin < SPIN_CAP; ++spin) {
      LD16W(key, O_KF + 16u * tid);
      if (!__any(key[1] != T || key[3] != T)) break;
      SLEEP(KBACK);
    }
    const uint32_t ib = (uint32_t)(((255 - (tid >> 1)) << 5) | (7 - 4 * (tid & 1)));
    uint32_t k0 = (key[0] << 16) | ib, k1 = (key[0] & 0xffff0000u) | (ib - 1), k2 = (key[2] << 16) | (ib - 2), k3 = (key[2] & 0xffff0000u) | (ib - 3);
    CEX(k0, k1) CEX(k2, k3) CEX(k0, k2) CEX(k1, k3) CEX(k1, k2)
#pragma unroll 1
    for (int c = 0; c < C_; ++c) {
      const uint32_t mx = wave_maxu(k0);
      const bool hit = (k0 == mx);
      k0 = hit ? k1 : k0; k1 = hit ? k2 : k1; k2 = hit ? k3 : k2; k3 = hit ? 0u : k3;
      if (lane == 0) cand[64 + 8 * w + c] = mx;
    }
    pick(IC<256>{}, IC<128>{});
    mykey = sel[32 + (lane & 7)];
    const uint32_t okey = sel[32 + (lane >> 3)];
    const uint64_t same = __ballot(((mykey ^ okey) & 0x1fe0u) == 0u && okey > mykey);
    const int sl = FORCEFB + __popcll((same >> (lane & 7)) & 0x0101010101010101ull);
    ro = (sl < 3) ? (((mykey >> 3) & 0x3ffu) + 3 - sl) * 1152u : O_FB - O_XC + 1152u * (lane & 7);
    }
    if (ln2) rest_out();
    if (ffn) {
      if (ROLE == 1) rows(ro + al, [] {});
      BAR();
      if (ROLE == 1) gate_up();
    }
  }
  if (OPOS == 0 && ROLE == 2 && ffn && !slow) { SLEEP(OD); rest_out(); }
  if (!ffn) {
    if (ln2) warm_out();
    if (ln2 && !slow) { SLEEP(OD2); rest_out(); }
    TSE();
    return;
  }
  uint32_t fx = 0, fv = 0, fn = 0, fo = 0, sa[9];
  float rs, hx, xs;
  u32x4 se;
  if constexpr (ROLE == 1) {
    f32x4 lh = (lane < 32) ? lo : hi;
    float fa = fold32(lh[0], lh[2]), fb2 = fold32(lh[1], lh[3]);
    if (__builtin_expect(__any(__builtin_isunordered(fa, fb2)) && !slow, 0)) {
      gsync0(ca + 4, 12);
      rows(ro + al, [] {});
      gsync0(ca + 4, 16);
      gate_up();
      lh = (lane < 32) ? lo : hi;
      fa = fold32(lh[0], lh[2]); fb2 = fold32(lh[1], lh[3]);
    }
    const float g = __int_as_float(__builtin_amdgcn_update_dpp(__float_as_int(fa), __float_as_int(fb2), 0x128, 0xf, 0xc, false));
    const float u = __int_as_float(__builtin_amdgcn_update_dpp(__float_as_int(fb2), __float_as_int(fa), 0x128, 0xf, 0x3, false));
    const uint32_t gu = pack2(g, u);
    const float gp = bflo(gu);
    const float sg = rbf(gp * __builtin_amdgcn_rcpf(1.f + __expf(-gp)));
    *(u16*)((char*)hm + ha) = f2bf(sg * bfhi(gu));
  } else if (LPOS) {
    DRAIN();
    gsync0(ca, 12);
    get_bd(0, 8);
  }
  BAR();
  TS(0, 6);
  if (ROLE == 2 || w == 2) {
    const u32x4 a0 = *(const u32x4*)((char*)hm + h0a), a1 = *(const u32x4*)((char*)hm + h1a);
    if (ROLE == 1) get_bd(0, 4);
    const f32x4 z = {0.f, 0.f, 0.f, 0.f};
    f32x4 o[4];
#pragma unroll
    for (int t = 0; t < 2 * ROLE; ++t) o[t] = MMB(a1, bw[2 * t + 1], MMB(a0, bw[2 * t], z));
#pragma unroll
    for (int t = 0; t < ROLE; ++t) {
      u32x4 sv;
#pragma unroll
      for (int r = 0; r < 4; ++r) sv[r] = min(pack2(o[2 * t][r], o[2 * t + 1][r]), 0xfffffffeu);
      ST16(so + 4096u * t, sv);
    }
    TS(0, 13);
  }
  if (OPOS == 1 && ROLE == 2) {
    warm_out();
    SLEEP(OSLEEP);
    if (!slow) rest_out();
  }
  if (ROLE == 1 && w == 1) {
    const int q = (fb & 15) >> 2, ts = 4 * (q & 1) + ((i & 7) >> 1);
    const int dd = 64 * (fb >> 4) + 16 * (2 * (i & 1) + (q >> 1)) + 4 * (fb & 3) + 2 * (kq & 1) + (kq >> 1);
    const uint32_t fkey = (uint32_t)__builtin_amdgcn_ds_bpermute(4 * ts, (int)mykey), fr = (uint32_t)__builtin_amdgcn_ds_bpermute(4 * ts, (int)ro);
    const int ft = 2047 - (int)(((fkey >> 2) & 0x7f8u) | (fkey & 7u));
    fo = (uint32_t)(ft * D_ + dd) * 2u;
    u32x2 fs;
    asm volatile("global_load_ushort %0, %4, %8\n\tglobal_load_ushort %1, %5, %9 sc1\n\tglobal_load_ushort %2, %6, %9 sc1\n\tglobal_load_dwordx2 %3, %7, %9 sc1"
                 : "=&v"(fx), "=&v"(fv), "=&v"(fn), "=&v"(fs) : "v"(fo), "v"(v_at(dd, ft >> 3) + 2u * (ft & 7)), "v"(O_XC + fr + 2u * dd), "v"(O_SC + 16u * dd), "s"(x), "s"(ws) : "memory");
    uint32_t t16 = (((T >> (3 * ((ft >> 1) & 3))) & LMASK) | (((T >> (12 + ((ft >> 1) & 3))) & 0x00010001u) << 14)) >> (16 * (ft & 1)) & 0xffffu;
    const uint32_t o = fkey >> 16;
    const float rs0 = bf2f((u16)((o & 0x8000u) ? (o & 0x7fffu) : (~o & 0xffffu)));
    rs = rbf(__builtin_amdgcn_rcpf(1.f + __expf(-rs0)));
    const uint32_t one = (i < 8) ? (0x3f80u << (16 * (i & 1))) : 0u;
    se = (u32x4){(i >> 1) == 0 ? one : 0u, (i >> 1) == 1 ? one : 0u, (i >> 1) == 2 ? one : 0u, (i >> 1) == 3 ? one : 0u};
#pragma unroll
    for (int m = 0; m < 9; ++m) sa[m] = O_SL + (uint32_t)(16 * m + (lane >> 2)) * SP + (uint32_t)(4 * fb + (lane & 3)) * 16u;
    asm volatile("" : "+v"(t16), "+v"(rs), "+v"(se), "+v"(sa[0]), "+v"(sa[1]), "+v"(sa[2]), "+v"(sa[3]), "+v"(sa[4]), "+v"(sa[5]), "+v"(sa[6]), "+v"(sa[7]), "+v"(sa[8]));
    SLEEP(SSLEEP);
    if (FD) until(tk + FD);
    TS(0, 1);
    float sum;
#pragma unroll 1
    for (int spin = 0; spin < SPIN_CAP; ++spin) {
      u32x4 sv[9];
#pragma unroll
      for (int m = 0; m < 9; ++m) LD16(sv[m], sa[m]);
      asm volatile("s_waitcnt vmcnt(9)" : "+v"(fx), "+v"(fv), "+v"(fn), "+v"(fs));
      if (__builtin_expect(__any(fs[0] != T), 0)) {
        for (int k = 0; k < SPIN_CAP && fs[0] != T; ++k)
          asm volatile("global_load_dwordx2 %0, %1, %2 sc1\n\ts_waitcnt vmcnt(0)" : "=&v"(fs) : "v"(O_SC + 16u * dd), "s"(ws) : "memory");
      }
      const uint32_t f1 = fv ^ t16, f2 = (f1 & 7u) ? fv & ~7u : f1;
      hx = rbf(bf2f((u16)fx) + (float)__builtin_bit_cast(_Float16, (u16)f2) * __uint_as_float(fs[1]));
      xs = bf2f((u16)fn);
      asm volatile("" : "+v"(hx), "+v"(xs));
      f32x4 dv = {0.f, 0.f, 0.f, 0.f};
      static_for<0, 9>([&](auto mm) {
        constexpr int m = mm.value;
        asm volatile("s_waitcnt vmcnt(%1)" : "+v"(sv[m]) : "n"(8 - m));
        dv = MMB(sv[m], se, dv);
        __builtin_amdgcn_sched_barrier(0);
      });
      sum = fold16(fold32(dv[0], dv[1]), fold32(dv[2], dv[3]));
      if (__builtin_expect(!__any(sum != sum), 1)) break;
      uint32_t mx = 0u;
#pragma unroll
      for (int m = 0; m < 9; ++m) mx = max(mx, max4(sv[m]));
      if (!__any(mx == 0xffffffffu)) break;
      TSC(18);
      SLEEP(PSLEEP);
    }
    const float o1 = rbf(xs + rbf(rbf(sum) * rs));
    if (i < 8) asm volatile("global_store_short %0, %1, %2 sc1" : : "v"(fo), "v"((uint32_t)f2bf(hx + o1)), "s"(out) : "memory");
    TS(0, 2);
  }
  TSE();
  }
  }
}
#define BROT "192"
#define CPF_A "s_getpc_b64 s[96:97]\n\ts_lshl_b32 s99, s2, 4\n\ts_lshl_b32 s84, s98, 7\n\ts_add_u32 s99, s99, s84\n\tv_and_b32 v112, 1, v0\n\t" \
              "v_lshl_add_u32 v112, v112, 12, s99\n\tv_min_u32 v112, kf_end-kf_all-0x80, v112\n\ts_mov_b64 exec, 3\n\tglobal_load_dword v112, v112, s[96:97]\n\ts_mov_b64 exec, -1\n\t"
extern "C" __global__ __launch_bounds__(512, 4) void kf_all(KARGS) {
  __shared__ __attribute__((aligned(16))) char smem[SMEM];
  asm volatile("s_load_dwordx16 s[64:79], s[0:1], 0x0\n\ts_load_dwordx4 s[80:83], s[0:1], 0x40\n\tv_readfirstlane_b32 s98, v0\n\t"
               "s_add_u32 s84, s2, " BROT "\n\ts_xor_b32 s84, s84, s2\n\ts_bitset0_b32 s84, 8\n\ts_bitset0_b32 s84, 9\n\ts_xor_b32 s2, s2, s84\n\t"
               "v_mov_b32 v109, 0\n\ts_and_b32 s85, s2, 7\n\ts_bitcmp1_b32 s2, 8\n\ts_cbranch_scc1 1f\n\t" CPF_A "s_bitcmp1_b32 s98, 8\n\ts_cbranch_scc1 2f\n\t"
               "s_andn2_b32 s84, s2, 31\n\ts_lshl2_add_u32 s84, s85, s84\n\ts_bfe_u32 s85, s2, 0x20003\n\ts_or_b32 s84, s84, s85\n\t"
               "s_mulk_i32 s84, 0x2400\n\ts_mulk_i32 s98, 36\n\ts_add_u32 s84, s84, s98\n\tv_and_b32 v110, 63, v0\n\tv_and_b32 v111, 32, v0\n\t"
               "v_lshlrev_b32 v111, 5, v111\n\tv_lshl_add_u32 v111, v110, 2, v111\n\tv_lshl_add_u32 v110, v110, 4, s84\n\tv_add_u32 v111, s84, v111\n\t"
               "s_cmpk_lt_u32 s2, 0x90\n\ts_waitcnt lgkmcnt(0)\n\t"
               "global_load_dwordx4 v[100:103], v110, s[64:65]\n\tglobal_load_dwordx4 v[104:107], v110, s[64:65] offset:1152\n\t"
               "global_load_dword v108, v111, s[64:65] offset:1024\n\tglobal_load_dword v109, v109, s[82:83] sc1\n\t"
               "s_cbranch_scc1 kf_n\n\ts_branch kf_h\n"
               "1:\n\ts_bfe_u32 s84, s2, 0x50003\n\ts_lshl_b32 s85, s85, 5\n\ts_or_b32 s84, s84, s85\n\ts_lshl_b32 s84, s84, 2\n\t"
               "v_and_b32 v110, 0xff, v0\n\tv_bfe_u32 v107, v0, 7, 1\n"
               "4:\n\ts_lshl_b32 s85, s84, 7\n\tv_lshl_add_u32 v110, v110, 1, s85\n\tv_lshl_add_u32 v107, v107, 1, s84\n\ts_and_b32 s85, s98, s2\n\ts_waitcnt lgkmcnt(0)\n\t"
               "global_load_ushort v100, v107, s[78:79]\n\tglobal_load_ushort v101, v110, s[66:67]\n\tglobal_load_ushort v102, v110, s[68:69]\n\t"
               "global_load_dword v109, v109, s[82:83] sc1\n\ts_bitcmp1_b32 s85, 8\n\ts_cbranch_scc1 kf_u\n\t"
               "global_load_ushort v103, v110, s[70:71]\n\tglobal_load_ushort v104, v110, s[72:73]\n\tglobal_load_ushort v105, v110, s[74:75]\n\t"
               "global_load_ushort v106, v110, s[76:77]\n\tglobal_load_ushort v107, v107, s[80:81]\n\ts_bitcmp1_b32 s2, 8\n\ts_cbranch_scc1 kf_c\n"
               "6:\n\ts_barrier\n\ts_branch kf_p\n"
               "2:\n\ts_cmpk_lt_u32 s2, 0xc0\n\ts_cbranch_scc1 6b\n\t"
               "s_lshr_b32 s84, s2, 3\n\ts_lshl3_add_u32 s84, s85, s84\n\ts_lshl1_add_u32 s84, s84, 976\n\t"
               "v_and_b32 v110, 0x7f, v0\n\tv_mov_b32 v107, 0\n\ts_branch 4b"
               : : "s"(__builtin_amdgcn_kernarg_segment_ptr()), "s"(blockIdx.x), "v"(threadIdx.x),
                   "s"((uint32_t)(uintptr_t)(__attribute__((address_space(3))) char*)smem) : "scc", "s96", "s97", "s98", "s99", "v127");
}
extern "C" __global__ __launch_bounds__(512, 4) void kf_c(KARGS) {
  __shared__ __attribute__((aligned(16))) char smem[SMEM];
  role_body<0>(smem, KPASS);
}
extern "C" __global__ __launch_bounds__(512, 4) void kf_u(KARGS) {
  __shared__ __attribute__((aligned(16))) char smem[SMEM];
  role_body<3>(smem, KPASS);
}
extern "C" __global__ __launch_bounds__(512, 4) void kf_n(KARGS) {
  __shared__ __attribute__((aligned(16))) char smem[SMEM];
  role_body<1, 1>(smem, KPASS);
}
extern "C" __global__ __launch_bounds__(512, 4) void kf_h(KARGS) {
  __shared__ __attribute__((aligned(16))) char smem[SMEM];
  role_body<1, 0>(smem, KPASS);
}
extern "C" __global__ __launch_bounds__(512, 4) void kf_p(KARGS) {
  __shared__ __attribute__((aligned(16))) char smem[SMEM];
  role_body<2>(smem, KPASS);
}
extern "C" __global__ __launch_bounds__(512, 4) void kf_conv() { asm volatile(CONV_ASM ::: CONV_CLOB); }
extern "C" __global__ void kf_end() {}

extern "C" __global__ __launch_bounds__(1024) void k_probe(int* dummy) {
  __shared__ int sm[2];
  const uint32_t now = (uint32_t)__builtin_amdgcn_s_memrealtime();
  __shared__ int sn;
  if (threadIdx.x == 0) { sm[0] = 0; sm[1] = 1 == 1 ? 0x7fffffff : 0; sn = 0; }
  __syncthreads();
  for (int k = threadIdx.x; k < 4096; k += 1024) {
    const uint32_t a = (uint32_t)g_prb[k], b = (uint32_t)(g_prb[k] >> 32);
    if (a) atomicMax(&sm[0], (int)(now - a));
    if (b) { if (1 == 1) atomicMin(&sm[1], (int)(now - b)); else if (1 == 0) atomicMax(&sm[1], (int)(now - b)); else { atomicAdd(&sm[1], (int)(now - b)); atomicAdd(&sn, 1); } }
  }
  __syncthreads();
  const int q = min(max(sm[0] * 10 - (1 == 2 ? sm[1] * 10 / max(sn, 1) : sm[1] * 10), 0), 30000);
  if (threadIdx.x == 0) while ((int)((uint32_t)__builtin_amdgcn_s_memrealtime() - now) < q) __builtin_amdgcn_s_sleep(2);
}
extern "C" void kernel_launch(void* const* d_in, const int* in_sizes, int n_in,
                              void* d_out, int out_size, void* d_ws, size_t ws_size,
                              hipStream_t stream) {
  kf_all<<<512, 512, 0, stream>>>((const u16*)d_in[0], (const u16*)d_in[1], (const u16*)d_in[2], (const u16*)d_in[3], (const u16*)d_in[4],
                                  (const u16*)d_in[5], (const u16*)d_in[6], (const u16*)d_in[7], (const u16*)d_in[8], (char*)d_ws + (size_t)WSOFF * 1048576,
                                  (const u16*)d_in[10], (const u16*)d_in[11], (const u16*)d_in[12], (const u16*)d_in[13], (u16*)d_out);
  k_probe<<<1, 1024, 0, stream>>>((int*)d_ws);
}
```

```cpp
#include <hip/hip_runtime.h>
#include <hip/hip_bf16.h>
#include <stdint.h>
#define HSTOP 200
#define HALL 1
#define RD2 145

typedef __attribute__((ext_vector_type(8))) short bf16x8;
typedef __attribute__((ext_vector_type(4))) float f32x4;
typedef uint16_t u16;
typedef __attribute__((ext_vector_type(4))) uint32_t u32x4;
typedef __attribute__((ext_vector_type(2))) uint32_t u32x2;
#define TSD uint32_t pr0_ = 0u, pr1_ = 0u
__device__ unsigned long long g_prb[4096];
#define TS(k, i) do { if ((i) == 0) pr0_ = (uint32_t)__builtin_amdgcn_s_memrealtime() | 1u; \
    if (ROLE == 1 && (i) == 2 && (1 < 0 || w == 1) && (blockIdx.x & 255) < 144) pr1_ = (uint32_t)__builtin_amdgcn_s_memrealtime() | 1u; } while (0)
#define TSC(i)
#define TSE() do { if (__builtin_amdgcn_mbcnt_hi(-1, __builtin_amdgcn_mbcnt_lo(-1, 0)) == 0) \
    __hip_atomic_store(&g_prb[blockIdx.x * 8 + w], ((unsigned long long)pr1_ << 32) | pr0_, __ATOMIC_RELAXED, __HIP_MEMORY_SCOPE_AGENT); } while (0)
#define TSP
#define TSA

constexpr int L_ = 2048, D_ = 576, N_ = 128, F_ = 4608, C_ = 8;
constexpr int NF = 144;
constexpr uint32_t O_EP = 0;
constexpr uint32_t O_KF = 4096;
constexpr uint32_t O_KEY = O_KF + 8192;
constexpr uint32_t O_SC = O_KEY + 32768;
constexpr uint32_t O_DN = O_SC + 9216;
constexpr uint32_t O_SB = O_DN + 20480;
constexpr uint32_t O_SU = O_SB + 65536;
constexpr uint32_t O_FB = O_SU + 262144;
constexpr uint32_t O_XC = O_FB + 8 * 1152;
constexpr uint32_t SP = 9216 + 128;
constexpr uint32_t O_SL = O_XC + 1024 * 1152;
constexpr uint32_t SLU = 336;
constexpr uint32_t O_U = O_SL + 256 * SLU * 16;
constexpr int UP = 584;
constexpr uint32_t VP = 8192 + 128, VPP = 4096 + 128;
constexpr uint32_t O_V = O_U + 256 * UP * 16, O_VP = O_V + 256 * VP;
static_assert(256 * SLU * 16 >= NF * SP && O_SL % 128 == 0 && O_U % 128 == 0 && O_V % 128 == 0, "");
__host__ __device__ __forceinline__ uint32_t u_at(int tg, int d) { return O_U + 16u * (uint32_t)((tg >> 1) * (UP * 2) + (d >> 2) * 8 + (tg & 1) * 4 + (d & 3)); }
__host__ __device__ __forceinline__ uint32_t v_at(int d, int tg) {
  return (d < 512) ? O_V + (uint32_t)(d >> 1) * VP + 16u * (uint32_t)(2 * tg + (d & 1)) : O_VP + (uint32_t)(d - 512) * VPP + 16u * (uint32_t)tg;
}
#define HAVE_VAT
#define HAVE_VTAG
__device__ __forceinline__ float bf2f(u16 v) { return __uint_as_float((uint32_t)v << 16); }
__device__ __forceinline__ float bflo(uint32_t v) { return __uint_as_float(v << 16); }
__device__ __forceinline__ float bfhi(uint32_t v) { return __uint_as_float(v & 0xffff0000u); }
__device__ __forceinline__ u16 f2bf(float f) { return __builtin_bit_cast(u16, (__bf16)f); }
__device__ __forceinline__ float rbf(float f) { return bf2f(f2bf(f)); }
typedef __attribute__((ext_vector_type(2))) float f32x2;
typedef __attribute__((ext_vector_type(2))) __bf16 bf16x2v;
__device__ __forceinline__ uint32_t pack2(float a, float b) {
  return __builtin_bit_cast(uint32_t, __builtin_convertvector((f32x2){a, b}, bf16x2v));
}

#define DPP_I(v, ctrl, rmask) __builtin_amdgcn_update_dpp(0, (v), (ctrl), (rmask), 0xf, true)
__device__ __forceinline__ float row_sum(float v) {
  v += __int_as_float(DPP_I(__float_as_int(v), 0x111, 0xf));
  v += __int_as_float(DPP_I(__float_as_int(v), 0x112, 0xf));
  v += __int_as_float(DPP_I(__float_as_int(v), 0x114, 0xf));
  v += __int_as_float(DPP_I(__float_as_int(v), 0x118, 0xf));
  return v;
}
__device__ __forceinline__ float rdl(float v, int l) { return __int_as_float(__builtin_amdgcn_readlane(__float_as_int(v), l)); }
__device__ __forceinline__ float wave_sum(float v) {
  v = row_sum(v);
  asm("s_nop 1\n\tv_add_f32_dpp %0, %0, %0 row_bcast:15 row_mask:0xa bank_mask:0xf\n\t"
      "s_nop 1\n\tv_add_f32_dpp %0, %0, %0 row_bcast:31 row_mask:0xc bank_mask:0xf\n\ts_nop 0" : "+v"(v));
  return rdl(v, 63);
}
__device__ __forceinline__ float fold32(float a, float b) {
  const auto r = __builtin_amdgcn_permlane32_swap(__float_as_uint(a), __float_as_uint(b), false, false);
  return __uint_as_float(r[0]) + __uint_as_float(r[1]);
}
__device__ __forceinline__ float fold16(float a, float b) {
  const auto r = __builtin_amdgcn_permlane16_swap(__float_as_uint(a), __float_as_uint(b), false, false);
  return __uint_as_float(r[0]) + __uint_as_float(r[1]);
}
__device__ __forceinline__ void wave_sum2(float& a, float& b) {
  float v = row_sum(fold32(a, b));
  asm("s_nop 1\n\tv_add_f32_dpp %0, %0, %0 row_bcast:15 row_mask:0xa bank_mask:0xf\n\ts_nop 0" : "+v"(v));
  a = rdl(v, 31); b = rdl(v, 63);
}
__device__ __forceinline__ float dot2(uint32_t a, uint32_t b, float c) {
  return __builtin_amdgcn_fdot2_f32_bf16(__builtin_bit_cast(bf16x2v, a), __builtin_bit_cast(bf16x2v, b), c, false);
}
__device__ __forceinline__ uint32_t row_maxu(uint32_t v) {
  v = max(v, (uint32_t)DPP_I((int)v, 0xb1, 0xf));
  v = max(v, (uint32_t)DPP_I((int)v, 0x4e, 0xf));
  v = max(v, (uint32_t)DPP_I((int)v, 0x141, 0xf));
  v = max(v, (uint32_t)DPP_I((int)v, 0x140, 0xf));
  return v;
}
__device__ __forceinline__ uint32_t wave_maxu(uint32_t v) {
  v = max(v, (uint32_t)DPP_I((int)v, 0x111, 0xf));
  v = max(v, (uint32_t)DPP_I((int)v, 0x112, 0xf));
  v = max(v, (uint32_t)DPP_I((int)v, 0x114, 0xf));
  v = max(v, (uint32_t)DPP_I((int)v, 0x118, 0xf));
  v = max(v, (uint32_t)DPP_I((int)v, 0x142, 0xa));
  v = max(v, (uint32_t)DPP_I((int)v, 0x143, 0xc));
  return (uint32_t)__builtin_amdgcn_readlane((int)v, 63);
}

typedef __attribute__((ext_vector_type(8))) _Float16 f16x8;
typedef __attribute__((ext_vector_type(2))) _Float16 f16x2;
__device__ __forceinline__ uint32_t packh(f32x2 v) { return __builtin_bit_cast(uint32_t, __builtin_convertvector(v, f16x2)); }
__device__ __forceinline__ uint32_t packh(float a, float b) { return packh((f32x2){a, b}); }
constexpr int IP = 224, I_X = 128;
__device__ __forceinline__ f32x2 cmul(f32x2 z, f32x2 w) {
  f32x2 r;
  asm("v_pk_mul_f32 %0, %1, %2 op_sel_hi:[0,1]\n\tv_pk_fma_f32 %0, %1, %2, %0 op_sel:[1,1,0] op_sel_hi:[1,0,1] neg_lo:[0,1,0]" : "=&v"(r) : "v"(z), "v"(w));
  return r;
}
__device__ __forceinline__ f32x2 cexp2(float ar2, float air, float l) {
  const float t = air * l;
  return (f32x2){__builtin_amdgcn_cosf(t), __builtin_amdgcn_sinf(t)} * __builtin_amdgcn_exp2f(ar2 * l);
}
__device__ __forceinline__ float state_a(float ldt, float are, float aim, float& ar2, float& air, float& g) {
  const float dt = rbf(__expf(ldt));
  ar2 = dt * are * 1.4426950408889634f;
  air = dt * aim * 0.15915494309189535f;
  air -= __builtin_rintf(air);
  g = 0.f;
  if (__builtin_expect(__any(ar2 > 0.f), 0)) g = fminf(__uint_as_float(wave_maxu(__float_as_uint(fmaxf(ar2, 0.f)))), 1.f);
  return dt;
}
template <int N> __device__ __forceinline__ void put_im(char* p, int im, const f32x2* z) {
  *(u32x4*)p = (u32x4){packh(z[0][0], z[1][0]), packh(z[2][0], z[3][0]), packh(z[4][0], z[5][0]), packh(z[6][0], z[7][0])};
  *(u32x4*)(p + im) = (u32x4){packh(z[0][1], z[1][1]), packh(z[2][1], z[3][1]), packh(z[4][1], z[5][1]), packh(z[6][1], z[7][1])};
}
#define Y_ASM \
  "v_mov_b64 v[40:41], %0\nv_pk_mul_f32 v[42:43], v[40:41], %1 op_sel_hi:[0,1]\nv_pk_mul_f32 v[44:45], %0, %2 op_sel_hi:[0,1]\n" \
  "v_pk_fma_f32 v[42:43], v[40:41], %1, v[42:43] op_sel:[1,1,0] op_sel_hi:[1,0,1] neg_lo:[0,1,0]\n" \
  "v_pk_fma_f32 v[44:45], %0, %2, v[44:45] op_sel:[1,1,0] op_sel_hi:[1,0,1] neg_lo:[0,1,0]\nv_cvt_pk_f16_f32 v56, v40, v42\n" \
  "v_cvt_pk_f16_f32 v60, v41, v43\nv_pk_mul_f32 v[40:41], v[42:43], %1 op_sel_hi:[0,1]\nv_pk_mul_f32 v[48:49], v[44:45], %2 op_sel_hi:[0,1]\n" \
  "v_pk_fma_f32 v[40:41], v[42:43], %1, v[40:41] op_sel:[1,1,0] op_sel_hi:[1,0,1] neg_lo:[0,1,0]\n" \
  "v_pk_fma_f32 v[48:49], v[44:45], %2, v[48:49] op_sel:[1,1,0] op_sel_hi:[1,0,1] neg_lo:[0,1,0]\n" \
  "v_pk_mul_f32 v[42:43], v[40:41], %1 op_sel_hi:[0,1]\nv_pk_mul_f32 v[52:53], v[48:49], %2 op_sel_hi:[0,1]\n" \
  "v_pk_fma_f32 v[42:43], v[40:41], %1, v[42:43] op_sel:[1,1,0] op_sel_hi:[1,0,1] neg_lo:[0,1,0]\n" \
  "v_pk_fma_f32 v[52:53], v[48:49], %2, v[52:53] op_sel:[1,1,0] op_sel_hi:[1,0,1] neg_lo:[0,1,0]\nv_cvt_pk_f16_f32 v57, v40, v42\n" \
  "v_cvt_pk_f16_f32 v61, v41, v43\nv_pk_mul_f32 v[40:41], v[42:43], %1 op_sel_hi:[0,1]\n" \
  "v_pk_fma_f32 v[40:41], v[42:43], %1, v[40:41] op_sel:[1,1,0] op_sel_hi:[1,0,1] neg_lo:[0,1,0]\n" \
  "v_pk_mul_f32 v[42:43], v[40:41], %1 op_sel_hi:[0,1]\nv_pk_mul_f32 v[46:47], v[44:45], %1 op_sel_hi:[0,1]\n" \
  "v_pk_fma_f32 v[42:43], v[40:41], %1, v[42:43] op_sel:[1,1,0] op_sel_hi:[1,0,1] neg_lo:[0,1,0]\n" \
  "v_pk_fma_f32 v[46:47], v[44:45], %1, v[46:47] op_sel:[1,1,0] op_sel_hi:[1,0,1] neg_lo:[0,1,0]\nv_cvt_pk_f16_f32 v58, v40, v42\n" \
  "v_cvt_pk_f16_f32 v62, v41, v43\nv_cvt_pk_f16_f32 v64, v44, v46\nv_cvt_pk_f16_f32 v68, v45, v47\n" \
  "v_pk_mul_f32 v[40:41], v[42:43], %1 op_sel_hi:[0,1]\nv_pk_mul_f32 v[44:45], v[46:47], %1 op_sel_hi:[0,1]\n" \
  "v_pk_fma_f32 v[40:41], v[42:43], %1, v[40:41] op_sel:[1,1,0] op_sel_hi:[1,0,1] neg_lo:[0,1,0]\n" \
  "v_pk_fma_f32 v[44:45], v[46:47], %1, v[44:45] op_sel:[1,1,0] op_sel_hi:[1,0,1] neg_lo:[0,1,0]\n" \
  "v_pk_mul_f32 v[42:43], v[40:41], %1 op_sel_hi:[0,1]\nv_pk_mul_f32 v[46:47], v[44:45], %1 op_sel_hi:[0,1]\n" \
  "v_pk_fma_f32 v[42:43], v[40:41], %1, v[42:43] op_sel:[1,1,0] op_sel_hi:[1,0,1] neg_lo:[0,1,0]\n" \
  "v_pk_fma_f32 v[46:47], v[44:45], %1, v[46:47] op_sel:[1,1,0] op_sel_hi:[1,0,1] neg_lo:[0,1,0]\nv_cvt_pk_f16_f32 v59, v40, v42\n" \
  "v_cvt_pk_f16_f32 v63, v41, v43\nv_cvt_pk_f16_f32 v65, v44, v46\nv_cvt_pk_f16_f32 v69, v45, v47\n" \
  "v_pk_mul_f32 v[44:45], v[46:47], %1 op_sel_hi:[0,1]\nds_write_b128 %3, v[56:59] offset:0\n" \
  "v_pk_fma_f32 v[44:45], v[46:47], %1, v[44:45] op_sel:[1,1,0] op_sel_hi:[1,0,1] neg_lo:[0,1,0]\nds_write_b128 %3, v[60:63] offset:64\n" \
  "v_pk_mul_f32 v[46:47], v[44:45], %1 op_sel_hi:[0,1]\nv_pk_mul_f32 v[50:51], v[48:49], %1 op_sel_hi:[0,1]\n" \
  "v_pk_fma_f32 v[46:47], v[44:45], %1, v[46:47] op_sel:[1,1,0] op_sel_hi:[1,0,1] neg_lo:[0,1,0]\n" \
  "v_pk_fma_f32 v[50:51], v[48:49], %1, v[50:51] op_sel:[1,1,0] op_sel_hi:[1,0,1] neg_lo:[0,1,0]\nv_cvt_pk_f16_f32 v66, v44, v46\n" \
  "v_cvt_pk_f16_f32 v70, v45, v47\nv_cvt_pk_f16_f32 v56, v48, v50\nv_cvt_pk_f16_f32 v60, v49, v51\n" \
  "v_pk_mul_f32 v[44:45], v[46:47], %1 op_sel_hi:[0,1]\nv_pk_mul_f32 v[48:49], v[50:51], %1 op_sel_hi:[0,1]\n" \
  "v_pk_fma_f32 v[44:45], v[46:47], %1, v[44:45] op_sel:[1,1,0] op_sel_hi:[1,0,1] neg_lo:[0,1,0]\n" \
  "v_pk_fma_f32 v[48:49], v[50:51], %1, v[48:49] op_sel:[1,1,0] op_sel_hi:[1,0,1] neg_lo:[0,1,0]\n" \
  "v_pk_mul_f32 v[46:47], v[44:45], %1 op_sel_hi:[0,1]\nv_pk_mul_f32 v[50:51], v[48:49], %1 op_sel_hi:[0,1]\n" \
  "v_pk_fma_f32 v[46:47], v[44:45], %1, v[46:47] op_sel:[1,1,0] op_sel_hi:[1,0,1] neg_lo:[0,1,0]\n" \
  "v_pk_fma_f32 v[50:51], v[48:49], %1, v[50:51] op_sel:[1,1,0] op_sel_hi:[1,0,1] neg_lo:[0,1,0]\nv_cvt_pk_f16_f32 v67, v44, v46\n" \
  "v_cvt_pk_f16_f32 v71, v45, v47\nv_cvt_pk_f16_f32 v57, v48, v50\nv_cvt_pk_f16_f32 v61, v49, v51\n" \
  "v_pk_mul_f32 v[48:49], v[50:51], %1 op_sel_hi:[0,1]\nds_write_b128 %3, v[64:67] offset:16\n" \
  "v_pk_fma_f32 v[48:49], v[50:51], %1, v[48:49] op_sel:[1,1,0] op_sel_hi:[1,0,1] neg_lo:[0,1,0]\nds_write_b128 %3, v[68:71] offset:80\n" \
  "v_pk_mul_f32 v[50:51], v[48:49], %1 op_sel_hi:[0,1]\nv_pk_mul_f32 v[54:55], v[52:53], %1 op_sel_hi:[0,1]\n" \
  "v_pk_fma_f32 v[50:51], v[48:49], %1, v[50:51] op_sel:[1,1,0] op_sel_hi:[1,0,1] neg_lo:[0,1,0]\n" \
  "v_pk_fma_f32 v[54:55], v[52:53], %1, v[54:55] op_sel:[1,1,0] op_sel_hi:[1,0,1] neg_lo:[0,1,0]\nv_cvt_pk_f16_f32 v58, v48, v50\n" \
  "v_cvt_pk_f16_f32 v62, v49, v51\nv_cvt_pk_f16_f32 v64, v52, v54\nv_cvt_pk_f16_f32 v68, v53, v55\n" \
  "v_pk_mul_f32 v[48:49], v[50:51], %1 op_sel_hi:[0,1]\nv_pk_mul_f32 v[52:53], v[54:55], %1 op_sel_hi:[0,1]\n" \
  "v_pk_fma_f32 v[48:49], v[50:51], %1, v[48:49] op_sel:[1,1,0] op_sel_hi:[1,0,1] neg_lo:[0,1,0]\n" \
  "v_pk_fma_f32 v[52:53], v[54:55], %1, v[52:53] op_sel:[1,1,0] op_sel_hi:[1,0,1] neg_lo:[0,1,0]\n" \
  "v_pk_mul_f32 v[50:51], v[48:49], %1 op_sel_hi:[0,1]\nv_pk_mul_f32 v[54:55], v[52:53], %1 op_sel_hi:[0,1]\n" \
  "v_pk_fma_f32 v[50:51], v[48:49], %1, v[50:51] op_sel:[1,1,0] op_sel_hi:[1,0,1] neg_lo:[0,1,0]\n" \
  "v_pk_fma_f32 v[54:55], v[52:53], %1, v[54:55] op_sel:[1,1,0] op_sel_hi:[1,0,1] neg_lo:[0,1,0]\nv_cvt_pk_f16_f32 v59, v48, v50\n" \
  "v_cvt_pk_f16_f32 v63, v49, v51\nv_cvt_pk_f16_f32 v65, v52, v54\nv_cvt_pk_f16_f32 v69, v53, v55\n" \
  "v_pk_mul_f32 v[52:53], v[54:55], %1 op_sel_hi:[0,1]\nds_write_b128 %3, v[56:59] offset:32\n" \
  "v_pk_fma_f32 v[52:53], v[54:55], %1, v[52:53] op_sel:[1,1,0] op_sel_hi:[1,0,1] neg_lo:[0,1,0]\nds_write_b128 %3, v[60:63] offset:96\n" \
  "v_pk_mul_f32 v[54:55], v[52:53], %1 op_sel_hi:[0,1]\n" \
  "v_pk_fma_f32 v[54:55], v[52:53], %1, v[54:55] op_sel:[1,1,0] op_sel_hi:[1,0,1] neg_lo:[0,1,0]\nv_cvt_pk_f16_f32 v66, v52, v54\n" \
  "v_cvt_pk_f16_f32 v70, v53, v55\nv_pk_mul_f32 v[52:53], v[54:55], %1 op_sel_hi:[0,1]\n" \
  "v_pk_fma_f32 v[52:53], v[54:55], %1, v[52:53] op_sel:[1,1,0] op_sel_hi:[1,0,1] neg_lo:[0,1,0]\n" \
  "v_pk_mul_f32 v[54:55], v[52:53], %1 op_sel_hi:[0,1]\n" \
  "v_pk_fma_f32 v[54:55], v[52:53], %1, v[54:55] op_sel:[1,1,0] op_sel_hi:[1,0,1] neg_lo:[0,1,0]\nv_cvt_pk_f16_f32 v67, v52, v54\n" \
  "v_cvt_pk_f16_f32 v71, v53, v55\nds_write_b128 %3, v[64:67] offset:48\nds_write_b128 %3, v[68:71] offset:112\n"
#define Y_CLOB "memory", "v40", "v41", "v42", "v43", "v44", "v45", "v46", "v47", "v48", "v49", "v50", "v51", "v52", "v53", "v54", "v55", "v56", "v57", "v58", "v59", "v60", "v61", "v62", "v63", "v64", "v65", "v66", "v67", "v68", "v69", "v70", "v71"
#define T_ASM \
  "ds_read_b128 %4, %8\nds_read_b64_tr_b16 v[80:81], %7 offset:0\nds_read_b64_tr_b16 v[82:83], %7 offset:64\n" \
  "ds_read_b64_tr_b16 v[84:85], %5 offset:0\nds_read_b64_tr_b16 v[86:87], %5 offset:48\nds_read_b64_tr_b16 v[88:89], %6 offset:0\n" \
  "ds_read_b64_tr_b16 v[90:91], %6 offset:48\nds_read_b64_tr_b16 v[92:93], %7 offset:3584\nds_read_b64_tr_b16 v[94:95], %7 offset:3648\n" \
  "ds_read_b64_tr_b16 v[96:97], %5 offset:3584\nds_read_b64_tr_b16 v[98:99], %5 offset:3632\nds_read_b64_tr_b16 v[100:101], %6 offset:3584\n" \
  "ds_read_b64_tr_b16 v[102:103], %6 offset:3632\nds_read_b64_tr_b16 v[104:105], %7 offset:7168\nds_read_b64_tr_b16 v[106:107], %7 offset:7232\n" \
  "s_waitcnt lgkmcnt(8)\nv_mfma_f32_16x16x32_f16 %0, v[84:87], v[80:83], 0\nds_read_b64_tr_b16 v[108:109], %5 offset:7168\n" \
  "ds_read_b64_tr_b16 v[110:111], %5 offset:7216\nds_read_b64_tr_b16 v[112:113], %6 offset:7168\nv_mfma_f32_16x16x32_f16 %2, v[88:91], v[80:83], 0\n" \
  "ds_read_b64_tr_b16 v[114:115], %6 offset:7216\nds_read_b64_tr_b16 v[116:117], %7 offset:10752\nds_read_b64_tr_b16 v[118:119], %7 offset:10816\n" \
  "s_waitcnt lgkmcnt(8)\nv_mfma_f32_16x16x32_f16 %0, v[96:99], v[92:95], %0\nds_read_b64_tr_b16 v[120:121], %5 offset:10752\n" \
  "ds_read_b64_tr_b16 v[122:123], %5 offset:10800\nds_read_b64_tr_b16 v[124:125], %6 offset:10752\n" \
  "v_mfma_f32_16x16x32_f16 %2, v[100:103], v[92:95], %2\nds_read_b64_tr_b16 v[126:127], %6 offset:10800\n" \
  "ds_read_b64_tr_b16 v[80:81], %7 offset:14336\nds_read_b64_tr_b16 v[82:83], %7 offset:14400\ns_waitcnt lgkmcnt(8)\n" \
  "v_mfma_f32_16x16x32_f16 %0, v[108:111], v[104:107], %0\nds_read_b64_tr_b16 v[84:85], %5 offset:14336\n" \
  "ds_read_b64_tr_b16 v[86:87], %5 offset:14384\nds_read_b64_tr_b16 v[88:89], %6 offset:14336\n" \
  "v_mfma_f32_16x16x32_f16 %2, v[112:115], v[104:107], %2\nds_read_b64_tr_b16 v[90:91], %6 offset:14384\n" \
  "ds_read_b64_tr_b16 v[92:93], %7 offset:17920\nds_read_b64_tr_b16 v[94:95], %7 offset:17984\ns_waitcnt lgkmcnt(8)\n" \
  "v_mfma_f32_16x16x32_f16 %0, v[120:123], v[116:119], %0\nds_read_b64_tr_b16 v[96:97], %5 offset:17920\n" \
  "ds_read_b64_tr_b16 v[98:99], %5 offset:17968\nds_read_b64_tr_b16 v[100:101], %6 offset:17920\n" \
  "v_mfma_f32_16x16x32_f16 %2, v[124:127], v[116:119], %2\nds_read_b64_tr_b16 v[102:103], %6 offset:17968\n" \
  "ds_read_b64_tr_b16 v[104:105], %7 offset:21504\nds_read_b64_tr_b16 v[106:107], %7 offset:21568\ns_waitcnt lgkmcnt(8)\n" \
  "v_mfma_f32_16x16x32_f16 %1, v[84:87], v[80:83], 0\nds_read_b64_tr_b16 v[108:109], %5 offset:21504\nds_read_b64_tr_b16 v[110:111], %5 offset:21552\n" \
  "ds_read_b64_tr_b16 v[112:113], %6 offset:21504\nv_mfma_f32_16x16x32_f16 %3, v[88:91], v[80:83], 0\nds_read_b64_tr_b16 v[114:115], %6 offset:21552\n" \
  "ds_read_b64_tr_b16 v[116:117], %7 offset:25088\nds_read_b64_tr_b16 v[118:119], %7 offset:25152\ns_waitcnt lgkmcnt(8)\n" \
  "v_mfma_f32_16x16x32_f16 %1, v[96:99], v[92:95], %1\nds_read_b64_tr_b16 v[120:121], %5 offset:25088\n" \
  "ds_read_b64_tr_b16 v[122:123], %5 offset:25136\nds_read_b64_tr_b16 v[124:125], %6 offset:25088\n" \
  "v_mfma_f32_16x16x32_f16 %3, v[100:103], v[92:95], %3\nds_read_b64_tr_b16 v[126:127], %6 offset:25136\ns_waitcnt lgkmcnt(6)\n" \
  "v_mfma_f32_16x16x32_f16 %1, v[108:111], v[104:107], %1\nv_mfma_f32_16x16x32_f16 %3, v[112:115], v[104:107], %3\ns_waitcnt lgkmcnt(0)\n" \
  "v_mfma_f32_16x16x32_f16 %1, v[120:123], v[116:119], %1\nv_mfma_f32_16x16x32_f16 %3, v[124:127], v[116:119], %3\n"
#define T_CLOB "memory", "v80", "v81", "v82", "v83", "v84", "v85", "v86", "v87", "v88", "v89", "v90", "v91", "v92", "v93", "v94", "v95", "v96", "v97", "v98", "v99", "v100", "v101", "v102", "v103", "v104", "v105", "v106", "v107", "v108", "v109", "v110", "v111", "v112", "v113", "v114", "v115", "v116", "v117", "v118", "v119", "v120", "v121", "v122", "v123", "v124", "v125", "v126", "v127"
template <class F> __device__ __forceinline__ int gen_y(float ldt, float are, float aim, float bre, float bim, float cre, float cim, uint32_t im, F&& mid TSP) {
  float ar2, air, g;
  const float dt = state_a(ldt, are, aim, ar2, air, g);
  const float dbr = dt * bre, dbi = dt * bim, pr = cre * dbr - cim * dbi, pi = cre * dbi + cim * dbr;
  const f32x2 w8 = cexp2(ar2, -air, 8.f), w64 = cexp2(ar2, -air, 64.f);
  const float pm = fmaxf(fabsf(pr), fabsf(pi));
  int e = -4;
  if (__builtin_expect(__any(!(pm < 0x1p-4f) || ar2 > 0.f) || !__any(pm > 0x1p-12f), 0)) e = (int)(wave_maxu(__float_as_uint(pm)) >> 23) - 126 + (int)ceilf(g * 250.f);
  const f32x2 y0 = {__builtin_amdgcn_ldexpf(pr, 14 - e), __builtin_amdgcn_ldexpf(-pi, 14 - e)};
  mid();
  asm volatile(Y_ASM : : "v"(y0), "v"(w8), "v"(w64), "v"(im) : Y_CLOB);
  return e;
}
__device__ __forceinline__ int gen_x(float ldt, float are, float aim, char* im) {
  float ar2, air, g;
  state_a(ldt, are, aim, ar2, air, g);
  const int e = min((int)ceilf(g * 1810.f), 300);
  const float s = __builtin_amdgcn_ldexpf(1.f, 15 - e);
  const f32x2 w1 = cexp2(ar2, air, 1.f), w256 = cexp2(ar2, air, 256.f), w2 = cmul(w1, w1);
  f32x2 a[8], b[8], c[8];
  a[0] = (f32x2){s, 0.f}; a[1] = w1 * s; a[2] = w2 * s;
  b[0] = b[1] = w256 * s;
  b[2] = cmul(b[0], w2); a[3] = cmul(a[1], w2); a[4] = cmul(a[2], w2);
  b[3] = b[4] = b[5] = cmul(b[2], w2); a[5] = cmul(a[3], w2); a[6] = cmul(a[4], w2);
  b[6] = cmul(b[3], w2); a[7] = cmul(a[5], w2); c[0] = cmul(b[3], w256);
  b[7] = cmul(b[6], w2); c[2] = cmul(c[0], w256);
  put_im<0>(im + I_X, 48, a);
  c[1] = cmul(b[7], w256); c[3] = cmul(c[1], w256);
  put_im<0>(im + I_X + 16, 48, b);
#pragma unroll
  for (int k = 4; k < 8; ++k) c[k] = cmul(c[k - 1], w256);
  put_im<0>(im + I_X + 32, 48, c);
  return e;
}

template <int I> struct IC { static constexpr int value = I; };
template <int B, int E, class F> __device__ __forceinline__ void static_for(F&& f) {
  if constexpr (B < E) { f(IC<B>{}); static_for<B + 1, E>(f); }
}
typedef __attribute__((ext_vector_type(4))) short s16x4;
__device__ __forceinline__ u32x2 trd(uint32_t a) {
  return __builtin_bit_cast(u32x2, __builtin_amdgcn_ds_read_tr16_b64_v4i16((__attribute__((address_space(3))) s16x4*)(uintptr_t)a));
}
#define BAR() asm volatile("s_waitcnt lgkmcnt(0)\n\ts_barrier" ::: "memory")
#define ST16(off, v) asm volatile("global_store_dwordx4 %0, %1, %2 sc1\n\ts_nop 1" : : "v"(off), "v"(v), "s"(ws) : "memory")
#define ST4(off, v) asm volatile("global_store_dword %0, %1, %2 sc1" : : "v"(off), "v"(v), "s"(ws) : "memory")
#define LD16(r, off) asm volatile("global_load_dwordx4 %0, %1, %2 sc1" : "=v"(r) : "v"(off), "s"(ws) : "memory")
#define LD16W(r, off) asm volatile("global_load_dwordx4 %0, %1, %2 sc1\n\ts_waitcnt vmcnt(0)" : "=&v"(r) : "v"(off), "s"(ws) : "memory")
#define DRAIN() asm volatile("s_waitcnt vmcnt(0)" ::: "memory")
#define CEX(a, b) { const uint32_t hi_ = max(a, b), lo_ = min(a, b); a = hi_; b = lo_; }
#define MMB(a_, b_, c_) __builtin_amdgcn_mfma_f32_16x16x32_bf16(__builtin_bit_cast(bf16x8, a_), __builtin_bit_cast(bf16x8, b_), c_, 0, 0, 0)
#define NTM 0
#define DPOL ""
#define DPOL2 ""
#define DPOL4 ""
constexpr int SPIN_CAP = 1 << 18;
constexpr uint32_t UMASK = 0x40074007u;
__device__ __forceinline__ uint32_t utag(uint32_t T, int j) {
  return ((T >> (3 * j)) & 0x00070007u) | (((j <= 2) ? (T << (2 - j)) : (T >> (j - 2))) & 0x40004000u);
}
__device__ __forceinline__ uint32_t max4(const u32x4& a) { return max(max(a[0], a[1]), max(a[2], a[3])); }
#ifndef ULONG
#define ULONG 8
#endif
__device__ __forceinline__ void until(uint32_t t) {
#pragma unroll 1
  for (int spin = 0; spin < 600; ++spin) {
    const int d = (int)(t - (uint32_t)__builtin_amdgcn_s_memrealtime());
    if (d <= 0) break;
    if (ULONG && d > 4 * ULONG) __builtin_amdgcn_s_sleep(ULONG); else __builtin_amdgcn_s_sleep(1);
  }
}
__device__ __forceinline__ uint32_t latest(uint32_t t) {
  const uint32_t t0 = (uint32_t)__builtin_amdgcn_readfirstlane(t) - 0x40000000u;
  return wave_maxu(t - t0) + t0;
}
__device__ __forceinline__ uint32_t gate(const char* ws, uint32_t po, uint32_t T) {
  u32x2 pv;
#pragma unroll 1
  for (int spin = 0; spin < SPIN_CAP; ++spin) {
    asm volatile("global_load_dwordx2 %0, %1, %2 sc1\n\ts_waitcnt vmcnt(0)" : "=&v"(pv) : "v"(po), "s"(ws) : "memory");
    if (!__any(pv[0] != T)) break;
    __builtin_amdgcn_s_sleep(2);
  }
  return pv[1];
}
__device__ __forceinline__ uint32_t gate2(const char* ws, uint32_t oa, uint32_t da, uint32_t ob, uint32_t T) {
  u32x2 pa, pb;
#pragma unroll 1
  for (int spin = 0; spin < SPIN_CAP; ++spin) {
    asm volatile("global_load_dwordx2 %0, %2, %4 sc1\n\tglobal_load_dwordx2 %1, %3, %4 sc1\n\ts_waitcnt vmcnt(0)" : "=&v"(pa), "=&v"(pb) : "v"(oa), "v"(ob), "s"(ws) : "memory");
    if (!__any(pa[0] != T || pb[0] != T)) break;
    __builtin_amdgcn_s_sleep(4);
  }
  pa[1] += da;
  return latest(((int)(pa[1] - pb[1]) > 0) ? pa[1] : pb[1]);
}
__device__ __forceinline__ uint32_t ahead(uint32_t c0, uint32_t t0, int dt) {
  const uint32_t c = (uint32_t)__builtin_amdgcn_s_memtime(), t = (uint32_t)__builtin_amdgcn_s_memrealtime();
  return t + (uint32_t)((float)((t - t0) * (uint32_t)(20 * dt)) * __builtin_amdgcn_rcpf((float)(c - c0)));
}
__device__ __forceinline__ void gsync(uint32_t a, uint32_t target) {
  uint32_t t, c, n = SPIN_CAP;
  uint64_t ex;
  asm volatile("s_waitcnt lgkmcnt(0)\n\ts_mov_b64 %3, exec\n\ts_mov_b64 exec, 1\n\tv_mov_b32 %0, 1\n\tds_add_u32 %4, %0\n\ts_mov_b64 exec, %3\n"
               "1:\n\ts_sleep 1\n\tds_read_b32 %0, %4\n\ts_sub_u32 %2, %2, 1\n\ts_cbranch_scc1 2f\n\ts_waitcnt lgkmcnt(0)\n\tv_readfirstlane_b32 %1, %0\n\t"
               "s_cmp_lt_u32 %1, %5\n\ts_cbranch_scc1 1b\n2:\n\ts_waitcnt lgkmcnt(0)"
               : "=&v"(t), "=&s"(c), "+s"(n), "=&s"(ex) : "v"(a), "n"(target) : "memory", "scc");
}
__device__ __forceinline__ void gsync0(uint32_t a, uint32_t target) {
  uint32_t t, c, n = SPIN_CAP;
  asm volatile("s_waitcnt lgkmcnt(0)\n\ts_mov_b64 exec, 1\n\tv_mov_b32 %0, 1\n\tds_add_u32 %3, %0\n\ts_mov_b64 exec, -1\n"
               "1:\n\tds_read_b32 %0, %3\n\ts_sub_u32 %2, %2, 1\n\ts_cbranch_scc1 2f\n\ts_waitcnt lgkmcnt(0)\n\tv_readfirstlane_b32 %1, %0\n\t"
               "s_cmp_lt_u32 %1, %4\n\ts_cbranch_scc1 1b\n2:\n\ts_waitcnt lgkmcnt(0)"
               : "=&v"(t), "=&s"(c), "+s"(n) : "v"(a), "n"(target) : "memory", "scc");
}
__device__ __forceinline__ void gwait(uint32_t a, uint32_t target) {
  uint32_t t, c, n = SPIN_CAP;
  asm volatile("1:\n\ts_sleep 2\n\tds_read_b32 %0, %3\n\ts_sub_u32 %2, %2, 1\n\ts_cbranch_scc1 2f\n\ts_waitcnt lgkmcnt(0)\n\tv_readfirstlane_b32 %1, %0\n\t"
               "s_cmp_lt_u32 %1, %4\n\ts_cbranch_scc1 1b\n2:\n\ts_waitcnt lgkmcnt(0)"
               : "=&v"(t), "=&s"(c), "+s"(n) : "v"(a), "n"(target) : "memory", "scc");
}
__device__ __forceinline__ void gwait0(uint32_t a, uint32_t target) {
  uint32_t t, c, n = SPIN_CAP;
  asm volatile("1:\n\tds_read_b32 %0, %3\n\ts_sub_u32 %2, %2, 1\n\ts_cbranch_scc1 2f\n\ts_waitcnt lgkmcnt(0)\n\tv_readfirstlane_b32 %1, %0\n\t"
               "s_cmp_lt_u32 %1, %4\n\ts_cbranch_scc1 1b\n2:\n\ts_waitcnt lgkmcnt(0)"
               : "=&v"(t), "=&s"(c), "+s"(n) : "v"(a), "n"(target) : "memory", "scc");
}
__device__ __forceinline__ void dma16(const void* base, uint32_t voff, uint32_t lds) {
  uint32_t keep;
  asm volatile("s_mov_b32 %0, m0\n\ts_mov_b32 m0, %3\n\ts_nop 0\n\tglobal_load_lds_dwordx4 %1, %2" DPOL "\n\ts_mov_b32 m0, %0" : "=&s"(keep) : "v"(voff), "s"(base), "s"(lds) : "memory");
}
__device__ __forceinline__ void dma16d(const void* base, uint32_t voff, uint32_t lds) {
  uint32_t keep;
  asm volatile("s_mov_b32 %0, m0\n\ts_mov_b32 m0, %3\n\ts_nop 0\n\tglobal_load_lds_dwordx4 %1, %2" DPOL2 "\n\ts_mov_b32 m0, %0" : "=&s"(keep) : "v"(voff), "s"(base), "s"(lds) : "memory");
}
__device__ __forceinline__ void nap(int n) {
  for (; n > 15; n -= 15) __builtin_amdgcn_s_sleep(15);
  switch (n) {
    case 1: __builtin_amdgcn_s_sleep(1); break; case 2: __builtin_amdgcn_s_sleep(2); break; case 3: __builtin_amdgcn_s_sleep(3); break;
    case 4: __builtin_amdgcn_s_sleep(4); break; case 5: __builtin_amdgcn_s_sleep(5); break; case 6: __builtin_amdgcn_s_sleep(6); break;
    case 7: __builtin_amdgcn_s_sleep(7); break; case 8: __builtin_amdgcn_s_sleep(8); break; case 9: __builtin_amdgcn_s_sleep(9); break;
    case 10: __builtin_amdgcn_s_sleep(10); break; case 11: __builtin_amdgcn_s_sleep(11); break; case 12: __builtin_amdgcn_s_sleep(12); break;
    case 13: __builtin_amdgcn_s_sleep(13); break; case 14: __builtin_amdgcn_s_sleep(14); break; case 15: __builtin_amdgcn_s_sleep(15); break;
    default: break;
  }
}
#define SLEEP(n) do { if ((n) > 0) __builtin_amdgcn_s_sleep(n); } while (0)
#define USLEEP 3
#define SSLEEP 11
#define PSLEEP 2
#define PPRIO 0
#define LFIRST 1
#define WD 28
#define OSLEEP 12
#define QPRIO 1
#define KPRIO 2
#define GSLEEP 0
#define NSLEEP 0
#define UD 40
#define XD 0
#define RSLEEP 0
#define UPRIO 2
#define VD2 380
#define WDEPTH 6
#define ESLEEP 0
#define SD 58
#define KD 182
#define OD 0
#define KBACK 6
#define DDEPTH 12
#define C3 1
#define XROT 2
#define WSOFF 0
#define KN 1
#define RPF 1
#define LSLEEP 1
#define TSTOP 78
#define VEARLY 0ull
#define FPR 3
#define SPR 3
#define DPRIO 0
#define QPRIO2 3
#define HW 2
#define RD 57
#define OPOS 1
#define OD2 6
#define NA 12
#define ORD1 ((unsigned __int128)0x321087654u << 36 | 0x321087654ull)
#define WDEPTH2 5
#define RQ NA
#define HREV 1
#define QPRIO0 3
#define VPS 1
#define RW 0
#define NPRIO 0
#define XSL 10
#define V2D 24
#define N0 8
#define LPOS 1
#define ALLP 1
#define OTP 1
#define STP 0
#define FD 204
#define VDB 365
#define BALL 1
#define FORCESLOW 0
#define FORCEFB 0
#define CONV_ASM \
  "s_cmp_eq_u32 s89, 1\ns_cbranch_scc1 1f\ns_cmp_gt_u32 s89, 1\ns_cbranch_scc1 9f\nds_read_b64 v[124:125], v3 offset:576\n" \
  "ds_read_b64 v[126:127], v3 offset:584\nds_read_b64 v[120:121], v3 offset:64\nds_read_b64 v[122:123], v3 offset:72\n" \
  "ds_read_b64 v[116:117], v3 offset:640\nds_read_b64 v[118:119], v3 offset:648\nds_read_b64 v[112:113], v3 offset:128\n" \
  "ds_read_b64 v[114:115], v3 offset:136\nds_read_b64 v[108:109], v3 offset:704\nds_read_b64 v[110:111], v3 offset:712\n" \
  "ds_read_b64 v[104:105], v3 offset:192\nds_read_b64 v[106:107], v3 offset:200\nds_read_b64 v[100:101], v3 offset:768\n" \
  "ds_read_b64 v[102:103], v3 offset:776\nds_read_b64 v[96:97], v3 offset:256\nds_read_b64 v[98:99], v3 offset:264\n" \
  "ds_read_b64 v[92:93], v3 offset:832\nds_read_b64 v[94:95], v3 offset:840\nds_read_b64 v[88:89], v3 offset:320\n" \
  "ds_read_b64 v[90:91], v3 offset:328\nds_read_b64 v[84:85], v3 offset:896\nds_read_b64 v[86:87], v3 offset:904\n" \
  "ds_read_b64 v[80:81], v3 offset:384\nds_read_b64 v[82:83], v3 offset:392\nds_read_b64 v[76:77], v3 offset:960\n" \
  "ds_read_b64 v[78:79], v3 offset:968\nds_read_b64 v[72:73], v3 offset:448\nds_read_b64 v[74:75], v3 offset:456\n" \
  "ds_read_b64 v[68:69], v3 offset:1024\nds_read_b64 v[70:71], v3 offset:1032\nds_read_b64 v[64:65], v3 offset:512\n" \
  "ds_read_b64 v[66:67], v3 offset:520\nds_read_b64 v[60:61], v3 offset:0\nds_read_b64 v[62:63], v3 offset:8\nds_read_b64 v[56:57], v3 offset:1088\n" \
  "ds_read_b64 v[58:59], v3 offset:1096\ns_waitcnt lgkmcnt(0)\ns_barrier\nds_read_b128 v[36:39], v2 offset:0\nds_read_b128 v[40:43], v2 offset:64\n" \
  "ds_read_b128 v[44:47], v2 offset:128\nds_read_b128 v[48:51], v2 offset:192\ns_waitcnt lgkmcnt(3)\n" \
  "v_mfma_f32_16x16x32_f16 v[12:15], v[124:127], v[36:39], 0\ns_setprio 1\nds_read_b128 v[52:55], v2 offset:256\n" \
  "v_mfma_f32_16x16x32_f16 v[16:19], v[120:123], v[36:39], 0\ns_waitcnt lgkmcnt(3)\nv_mfma_f32_16x16x32_f16 v[12:15], v[116:119], v[40:43], v[12:15]\n" \
  "ds_read_b128 v[36:39], v2 offset:320\nv_mfma_f32_16x16x32_f16 v[16:19], v[112:115], v[40:43], v[16:19]\ns_waitcnt lgkmcnt(3)\n" \
  "v_mfma_f32_16x16x32_f16 v[12:15], v[108:111], v[44:47], v[12:15]\nds_read_b128 v[40:43], v2 offset:384\n" \
  "v_mfma_f32_16x16x32_f16 v[16:19], v[104:107], v[44:47], v[16:19]\ns_waitcnt lgkmcnt(3)\n" \
  "v_mfma_f32_16x16x32_f16 v[12:15], v[100:103], v[48:51], v[12:15]\nds_read_b128 v[44:47], v2 offset:448\n" \
  "v_mfma_f32_16x16x32_f16 v[16:19], v[96:99], v[48:51], v[16:19]\ns_waitcnt lgkmcnt(3)\n" \
  "v_mfma_f32_16x16x32_f16 v[12:15], v[92:95], v[52:55], v[12:15]\nv_mfma_f32_16x16x32_f16 v[16:19], v[88:91], v[52:55], v[16:19]\n" \
  "s_waitcnt lgkmcnt(2)\nv_mfma_f32_16x16x32_f16 v[12:15], v[84:87], v[36:39], v[12:15]\nds_read_b128 v[48:51], v2 offset:512\n" \
  "v_mfma_f32_16x16x32_f16 v[16:19], v[80:83], v[36:39], v[16:19]\ns_waitcnt lgkmcnt(2)\n" \
  "v_mfma_f32_16x16x32_f16 v[12:15], v[76:79], v[40:43], v[12:15]\nds_read_b128 v[52:55], v2 offset:576\n" \
  "v_mfma_f32_16x16x32_f16 v[16:19], v[72:75], v[40:43], v[16:19]\ns_waitcnt lgkmcnt(2)\n" \
  "v_mfma_f32_16x16x32_f16 v[12:15], v[68:71], v[44:47], v[12:15]\nds_read_b128 v[36:39], v2 offset:640\n" \
  "v_mfma_f32_16x16x32_f16 v[16:19], v[64:67], v[44:47], v[16:19]\nv_mfma_f32_16x16x32_f16 v[20:23], v[60:63], v[44:47], 0\n" \
  "ds_read_b128 v[40:43], v2 offset:704\nv_mfma_f32_16x16x32_f16 v[28:31], v[56:59], v[44:47], 0\ns_waitcnt lgkmcnt(3)\n" \
  "v_mfma_f32_16x16x32_f16 v[16:19], v[124:127], v[48:51], v[16:19]\nds_read_b128 v[56:59], v2 offset:768\nv_cvt_pkrtz_f16_f32 v32, v12, v13\n" \
  "v_mfma_f32_16x16x32_f16 v[20:23], v[120:123], v[48:51], v[20:23]\nv_cvt_pkrtz_f16_f32 v33, v14, v15\ns_waitcnt lgkmcnt(3)\n" \
  "v_mfma_f32_16x16x32_f16 v[16:19], v[116:119], v[52:55], v[16:19]\nds_read_b128 v[44:47], v2 offset:832\n" \
  "v_pk_add_u16 v32, v32, 4 op_sel_hi:[1,0] clamp\nv_mfma_f32_16x16x32_f16 v[20:23], v[112:115], v[52:55], v[20:23]\n" \
  "v_pk_add_u16 v33, v33, 4 op_sel_hi:[1,0] clamp\ns_waitcnt lgkmcnt(3)\nv_mfma_f32_16x16x32_f16 v[16:19], v[108:111], v[36:39], v[16:19]\n" \
  "ds_read_b128 v[48:51], v2 offset:896\nds_write_b64 v4, v[32:33] offset:0\nv_mfma_f32_16x16x32_f16 v[20:23], v[104:107], v[36:39], v[20:23]\n" \
  "s_waitcnt lgkmcnt(3)\nv_mfma_f32_16x16x32_f16 v[16:19], v[100:103], v[40:43], v[16:19]\nds_read_b128 v[52:55], v2 offset:960\n" \
  "v_mfma_f32_16x16x32_f16 v[20:23], v[96:99], v[40:43], v[20:23]\nv_mfma_f32_16x16x32_f16 v[16:19], v[92:95], v[56:59], v[16:19]\n" \
  "v_mfma_f32_16x16x32_f16 v[20:23], v[88:91], v[56:59], v[20:23]\nds_read_b128 v[36:39], v2 offset:1024\ns_waitcnt lgkmcnt(3)\n" \
  "v_mfma_f32_16x16x32_f16 v[16:19], v[84:87], v[44:47], v[16:19]\nv_mfma_f32_16x16x32_f16 v[20:23], v[80:83], v[44:47], v[20:23]\n" \
  "ds_read_b128 v[40:43], v2 offset:1088\nds_read_b64 v[56:57], v6 offset:256\nds_read_b64 v[58:59], v6 offset:264\n" \
  "v_mfma_f32_16x16x32_f16 v[16:19], v[76:79], v[48:51], v[16:19]\nv_mfma_f32_16x16x32_f16 v[20:23], v[72:75], v[48:51], v[20:23]\n" \
  "ds_read_b128 v[44:47], v2 offset:1152\ns_waitcnt lgkmcnt(4)\nv_mfma_f32_16x16x32_f16 v[16:19], v[68:71], v[52:55], v[16:19]\n" \
  "v_mfma_f32_16x16x32_f16 v[20:23], v[64:67], v[52:55], v[20:23]\nds_read_b128 v[48:51], v2 offset:1216\n" \
  "v_mfma_f32_16x16x32_f16 v[24:27], v[60:63], v[52:55], 0\nv_mfma_f32_16x16x32_f16 v[20:23], v[124:127], v[36:39], v[20:23]\n" \
  "ds_read_b128 v[52:55], v2 offset:1280\nv_mfma_f32_16x16x32_f16 v[24:27], v[120:123], v[36:39], v[24:27]\ns_waitcnt lgkmcnt(5)\n" \
  "v_mfma_f32_16x16x32_f16 v[20:23], v[116:119], v[40:43], v[20:23]\nds_read_b128 v[36:39], v2 offset:1344\nv_cvt_pkrtz_f16_f32 v32, v16, v17\n" \
  "v_mfma_f32_16x16x32_f16 v[24:27], v[112:115], v[40:43], v[24:27]\nv_cvt_pkrtz_f16_f32 v33, v18, v19\ns_waitcnt lgkmcnt(3)\n" \
  "v_mfma_f32_16x16x32_f16 v[20:23], v[108:111], v[44:47], v[20:23]\nds_read_b128 v[40:43], v2 offset:1408\n" \
  "v_pk_add_u16 v32, v32, 4 op_sel_hi:[1,0] clamp\nds_read_b32 v35, v9 offset:32\nv_mfma_f32_16x16x32_f16 v[24:27], v[104:107], v[44:47], v[24:27]\n" \
  "v_pk_add_u16 v33, v33, 4 op_sel_hi:[1,0] clamp\ns_waitcnt lgkmcnt(3)\nv_mfma_f32_16x16x32_f16 v[20:23], v[100:103], v[48:51], v[20:23]\n" \
  "ds_read_b128 v[44:47], v2 offset:1472\nds_write_b64 v4, v[32:33] offset:1024\nv_mfma_f32_16x16x32_f16 v[24:27], v[96:99], v[48:51], v[24:27]\n" \
  "v_mfma_f32_16x16x32_f16 v[20:23], v[92:95], v[52:55], v[20:23]\nv_mfma_f32_16x16x32_f16 v[24:27], v[88:91], v[52:55], v[24:27]\n" \
  "ds_read_b128 v[48:51], v2 offset:1536\ns_waitcnt lgkmcnt(3)\nv_readfirstlane_b32 s85, v35\ns_cmp_lt_u32 s85, 4\ns_cbranch_scc1 21f\n22:\n" \
  "v_mfma_f32_16x16x32_f16 v[20:23], v[84:87], v[36:39], v[20:23]\nv_mfma_f32_16x16x32_f16 v[24:27], v[80:83], v[36:39], v[24:27]\n" \
  "ds_read_b128 v[52:55], v2 offset:1600\nv_mfma_f32_16x16x32_f16 v[20:23], v[76:79], v[40:43], v[20:23]\n" \
  "v_mfma_f32_16x16x32_f16 v[24:27], v[72:75], v[40:43], v[24:27]\nds_read_b128 v[36:39], v2 offset:1664\ns_waitcnt lgkmcnt(4)\n" \
  "v_mfma_f32_16x16x32_f16 v[20:23], v[68:71], v[44:47], v[20:23]\nv_mfma_f32_16x16x32_f16 v[24:27], v[64:67], v[44:47], v[24:27]\n" \
  "ds_read_b128 v[40:43], v2 offset:1728\nv_mfma_f32_16x16x32_f16 v[28:31], v[60:63], v[44:47], v[28:31]\nds_read_b64_tr_b16 v[60:61], v5 offset:0\n" \
  "ds_read_b64_tr_b16 v[62:63], v5 offset:32\ns_waitcnt lgkmcnt(4)\nv_mfma_f32_16x16x32_f16 v[24:27], v[124:127], v[48:51], v[24:27]\n" \
  "v_mfma_f32_16x16x32_f16 v[28:31], v[120:123], v[48:51], v[28:31]\nds_read_b64_tr_b16 v[44:45], v5 offset:256\n" \
  "ds_read_b64_tr_b16 v[46:47], v5 offset:288\nv_mfma_f32_16x16x32_f16 v[24:27], v[116:119], v[52:55], v[24:27]\n" \
  "ds_read_b64 v[120:121], v6 offset:320\nds_read_b64 v[122:123], v6 offset:328\nv_mfma_f32_16x16x32_f16 v[28:31], v[112:115], v[52:55], v[28:31]\n" \
  "ds_read_b128 v[48:51], v2 offset:1792\ns_waitcnt lgkmcnt(7)\nv_mfma_f32_16x16x32_f16 v[24:27], v[108:111], v[36:39], v[24:27]\n" \
  "ds_read_b128 v[112:115], v2 offset:1856\nv_mfma_f32_16x16x32_f16 v[28:31], v[104:107], v[36:39], v[28:31]\nds_read_b64 v[52:53], v6 offset:128\n" \
  "ds_read_b64 v[54:55], v6 offset:136\nv_mfma_f32_16x16x32_f16 v[24:27], v[100:103], v[40:43], v[24:27]\nds_read_b128 v[104:107], v2 offset:1920\n" \
  "v_mfma_f32_16x16x32_f16 v[28:31], v[96:99], v[40:43], v[28:31]\nds_read_b64 v[36:37], v6 offset:192\nds_read_b64 v[38:39], v6 offset:200\n" \
  "s_waitcnt lgkmcnt(9)\nv_mfma_f32_16x16x32_f16 v[20:23], v[60:63], v[56:59], v[20:23]\nds_read_b128 v[96:99], v2 offset:1984\ns_waitcnt lgkmcnt(7)\n" \
  "v_mfma_f32_16x16x32_f16 v[20:23], v[44:47], v[120:123], v[20:23]\nv_mfma_f32_16x16x32_f16 v[24:27], v[92:95], v[48:51], v[24:27]\n" \
  "ds_read_b128 v[40:43], v2 offset:2048\nv_mfma_f32_16x16x32_f16 v[28:31], v[88:91], v[48:51], v[28:31]\nds_read_b64_tr_b16 v[88:89], v5 offset:512\n" \
  "ds_read_b64_tr_b16 v[90:91], v5 offset:544\ns_waitcnt lgkmcnt(6)\nv_mfma_f32_16x16x32_f16 v[24:27], v[84:87], v[112:115], v[24:27]\n" \
  "v_mfma_f32_16x16x32_f16 v[28:31], v[80:83], v[112:115], v[28:31]\nv_cvt_pkrtz_f16_f32 v32, v20, v21\nv_cvt_pkrtz_f16_f32 v33, v22, v23\n" \
  "v_mfma_f32_16x16x32_f16 v[24:27], v[76:79], v[104:107], v[24:27]\nds_read_b64_tr_b16 v[48:49], v5 offset:768\n" \
  "ds_read_b64_tr_b16 v[50:51], v5 offset:800\nv_mfma_f32_16x16x32_f16 v[28:31], v[72:75], v[104:107], v[28:31]\n" \
  "ds_read_b128 v[80:83], v2 offset:2112\ns_waitcnt lgkmcnt(5)\nv_mfma_f32_16x16x32_f16 v[24:27], v[68:71], v[96:99], v[24:27]\n" \
  "ds_read_b128 v[112:115], v2 offset:2176\nv_pk_add_u16 v32, v32, 4 op_sel_hi:[1,0] clamp\n" \
  "v_mfma_f32_16x16x32_f16 v[28:31], v[64:67], v[96:99], v[28:31]\nds_read_b128 v[72:75], v2 offset:2240\n" \
  "v_pk_add_u16 v33, v33, 4 op_sel_hi:[1,0] clamp\nv_mfma_f32_16x16x32_f16 v[28:31], v[124:127], v[40:43], v[28:31]\n" \
  "ds_read_b128 v[104:107], v2 offset:2304\nds_write_b64 v4, v[32:33] offset:2048\nv_mfma_f32_16x16x32_f16 v[24:27], v[60:63], v[52:55], v[24:27]\n" \
  "ds_read_b128 v[64:67], v2 offset:2368\nds_read_b64 v[96:97], v6 offset:0\nds_read_b64 v[98:99], v6 offset:8\n" \
  "v_mfma_f32_16x16x32_f16 v[24:27], v[44:47], v[36:39], v[24:27]\nds_read_b128 v[124:127], v2 offset:2432\ns_waitcnt lgkmcnt(9)\n" \
  "v_mfma_f32_16x16x32_f16 v[24:27], v[88:91], v[56:59], v[24:27]\nds_read_b128 v[40:43], v2 offset:2496\n" \
  "v_mfma_f32_16x16x32_f16 v[24:27], v[48:51], v[120:123], v[24:27]\ns_waitcnt lgkmcnt(8)\n" \
  "v_mfma_f32_16x16x32_f16 v[28:31], v[116:119], v[80:83], v[28:31]\nds_read_b64 v[116:117], v6 offset:64\nds_read_b64 v[118:119], v6 offset:72\n" \
  "v_mfma_f32_16x16x32_f16 v[28:31], v[108:111], v[112:115], v[28:31]\nds_read_b64_tr_b16 v[80:81], v5 offset:1024\n" \
  "ds_read_b64_tr_b16 v[82:83], v5 offset:1056\ns_waitcnt lgkmcnt(10)\nv_mfma_f32_16x16x32_f16 v[28:31], v[100:103], v[72:75], v[28:31]\n" \
  "v_cvt_pkrtz_f16_f32 v32, v24, v25\nv_cvt_pkrtz_f16_f32 v33, v26, v27\nv_mfma_f32_16x16x32_f16 v[28:31], v[92:95], v[104:107], v[28:31]\n" \
  "v_pk_add_u16 v32, v32, 4 op_sel_hi:[1,0] clamp\ns_waitcnt lgkmcnt(5)\nv_mfma_f32_16x16x32_f16 v[28:31], v[84:87], v[64:67], v[28:31]\n" \
  "ds_read_b64_tr_b16 v[108:109], v5 offset:1280\nds_read_b64_tr_b16 v[110:111], v5 offset:1312\n" \
  "v_mfma_f32_16x16x32_f16 v[28:31], v[76:79], v[124:127], v[28:31]\nds_read_b64_tr_b16 v[112:113], v7 offset:0\n" \
  "ds_read_b64_tr_b16 v[114:115], v7 offset:64\ns_waitcnt lgkmcnt(6)\nv_mfma_f32_16x16x32_f16 v[28:31], v[68:71], v[40:43], v[28:31]\n" \
  "ds_read2_b32 v[100:101], v8 offset0:48 offset1:49\nds_read2_b32 v[102:103], v8 offset0:50 offset1:51\n" \
  "v_mfma_f32_16x16x32_f16 v[28:31], v[60:63], v[96:99], v[28:31]\nv_pk_add_u16 v33, v33, 4 op_sel_hi:[1,0] clamp\n" \
  "ds_write_b64 v4, v[32:33] offset:3072\nv_mfma_f32_16x16x32_f16 v[28:31], v[44:47], v[116:119], v[28:31]\n" \
  "v_mfma_f32_16x16x32_f16 v[28:31], v[88:91], v[52:55], v[28:31]\nv_mfma_f32_16x16x32_f16 v[28:31], v[48:51], v[36:39], v[28:31]\n" \
  "s_waitcnt lgkmcnt(5)\nv_mfma_f32_16x16x32_f16 v[28:31], v[80:83], v[56:59], v[28:31]\n" \
  "v_mfma_f32_16x16x32_f16 v[28:31], v[108:111], v[120:123], v[28:31]\ns_waitcnt lgkmcnt(1)\n" \
  "v_mfma_f32_16x16x32_f16 v[28:31], v[112:115], v[100:103], v[28:31]\ns_nop 7\nv_cvt_pkrtz_f16_f32 v32, v28, v29\nv_cvt_pkrtz_f16_f32 v33, v30, v31\n" \
  "v_pk_add_u16 v32, v32, 4 op_sel_hi:[1,0] clamp\nv_pk_add_u16 v33, v33, 4 op_sel_hi:[1,0] clamp\nds_write_b64 v4, v[32:33] offset:4096\n" \
  "s_setprio 0\ns_setpc_b64 s[90:91]\n21:\ns_movk_i32 s84, 0x4000\n23:\nds_read_b32 v35, v9 offset:32\ns_sub_u32 s84, s84, 1\ns_cbranch_scc1 22b\n" \
  "s_waitcnt lgkmcnt(0)\nv_readfirstlane_b32 s85, v35\ns_cmp_lt_u32 s85, 4\ns_cbranch_scc1 23b\ns_branch 22b\n1:\n" \
  "ds_read_b64 v[124:125], v3 offset:1088\nds_read_b64 v[126:127], v3 offset:1096\nds_read_b64 v[120:121], v3 offset:0\n" \
  "ds_read_b64 v[122:123], v3 offset:8\nds_read_b64 v[116:117], v3 offset:64\nds_read_b64 v[118:119], v3 offset:72\n" \
  "ds_read_b64 v[112:113], v3 offset:128\nds_read_b64 v[114:115], v3 offset:136\nds_read_b64 v[108:109], v3 offset:192\n" \
  "ds_read_b64 v[110:111], v3 offset:200\nds_read_b64 v[104:105], v3 offset:256\nds_read_b64 v[106:107], v3 offset:264\n" \
  "ds_read_b64 v[100:101], v3 offset:320\nds_read_b64 v[102:103], v3 offset:328\nds_read_b64 v[96:97], v3 offset:384\n" \
  "ds_read_b64 v[98:99], v3 offset:392\nds_read_b64 v[92:93], v3 offset:448\nds_read_b64 v[94:95], v3 offset:456\n" \
  "ds_read_b64 v[88:89], v3 offset:512\nds_read_b64 v[90:91], v3 offset:520\nds_read_b64 v[84:85], v3 offset:576\n" \
  "ds_read_b64 v[86:87], v3 offset:584\nds_read_b64 v[80:81], v3 offset:640\nds_read_b64 v[82:83], v3 offset:648\n" \
  "ds_read_b64 v[76:77], v3 offset:704\nds_read_b64 v[78:79], v3 offset:712\nds_read_b64 v[72:73], v3 offset:768\n" \
  "ds_read_b64 v[74:75], v3 offset:776\nds_read_b64 v[68:69], v3 offset:832\nds_read_b64 v[70:71], v3 offset:840\n" \
  "ds_read_b64 v[64:65], v3 offset:896\nds_read_b64 v[66:67], v3 offset:904\nds_read_b64 v[60:61], v3 offset:960\n" \
  "ds_read_b64 v[62:63], v3 offset:968\nds_read_b64 v[56:57], v3 offset:1024\nds_read_b64 v[58:59], v3 offset:1032\n" \
  "ds_read_b64 v[52:53], v6 offset:0\nds_read_b64 v[54:55], v6 offset:8\nds_read_b64 v[48:49], v6 offset:64\nds_read_b64 v[50:51], v6 offset:72\n" \
  "s_waitcnt lgkmcnt(0)\ns_barrier\nds_read_b128 v[28:31], v2 offset:960\nds_read_b128 v[32:35], v2 offset:1472\n" \
  "ds_read_b128 v[36:39], v2 offset:1984\nds_read_b128 v[40:43], v2 offset:2048\nds_read_b128 v[44:47], v2 offset:2112\ns_waitcnt lgkmcnt(3)\n" \
  "v_mfma_f32_16x16x32_f16 v[12:15], v[124:127], v[28:31], 0\ns_setprio 1\nds_read_b128 v[28:31], v2 offset:2176\n" \
  "v_mfma_f32_16x16x32_f16 v[16:19], v[124:127], v[32:35], 0\nds_read_b128 v[32:35], v2 offset:2240\ns_waitcnt lgkmcnt(3)\n" \
  "v_mfma_f32_16x16x32_f16 v[12:15], v[120:123], v[36:39], v[12:15]\nv_mfma_f32_16x16x32_f16 v[20:23], v[124:127], v[36:39], 0\n" \
  "ds_read_b128 v[124:127], v2 offset:2304\nds_read_b128 v[36:39], v2 offset:2368\nv_mfma_f32_16x16x32_f16 v[12:15], v[116:119], v[40:43], v[12:15]\n" \
  "ds_read_b128 v[40:43], v2 offset:2432\ns_waitcnt lgkmcnt(4)\nv_mfma_f32_16x16x32_f16 v[12:15], v[112:115], v[44:47], v[12:15]\n" \
  "ds_read_b128 v[44:47], v2 offset:2496\nv_mfma_f32_16x16x32_f16 v[12:15], v[108:111], v[28:31], v[12:15]\nds_read_b128 v[28:31], v2 offset:2560\n" \
  "s_waitcnt lgkmcnt(4)\nv_mfma_f32_16x16x32_f16 v[12:15], v[104:107], v[32:35], v[12:15]\nds_read_b128 v[32:35], v2 offset:2624\n" \
  "v_mfma_f32_16x16x32_f16 v[12:15], v[100:103], v[124:127], v[12:15]\ns_waitcnt lgkmcnt(3)\n" \
  "v_mfma_f32_16x16x32_f16 v[12:15], v[96:99], v[36:39], v[12:15]\nds_read_b128 v[124:127], v2 offset:2688\n" \
  "v_mfma_f32_16x16x32_f16 v[12:15], v[92:95], v[40:43], v[12:15]\ns_waitcnt lgkmcnt(3)\n" \
  "v_mfma_f32_16x16x32_f16 v[12:15], v[88:91], v[44:47], v[12:15]\nds_read_b128 v[36:39], v2 offset:2752\n" \
  "v_mfma_f32_16x16x32_f16 v[16:19], v[120:123], v[44:47], v[16:19]\ns_waitcnt lgkmcnt(3)\n" \
  "v_mfma_f32_16x16x32_f16 v[12:15], v[84:87], v[28:31], v[12:15]\nds_read_b128 v[40:43], v2 offset:2816\n" \
  "v_mfma_f32_16x16x32_f16 v[16:19], v[116:119], v[28:31], v[16:19]\ns_waitcnt lgkmcnt(3)\n" \
  "v_mfma_f32_16x16x32_f16 v[12:15], v[80:83], v[32:35], v[12:15]\nds_read_b128 v[44:47], v2 offset:2880\n" \
  "v_mfma_f32_16x16x32_f16 v[16:19], v[112:115], v[32:35], v[16:19]\ns_waitcnt lgkmcnt(3)\n" \
  "v_mfma_f32_16x16x32_f16 v[12:15], v[76:79], v[124:127], v[12:15]\nds_read_b128 v[28:31], v2 offset:2944\n" \
  "v_mfma_f32_16x16x32_f16 v[16:19], v[108:111], v[124:127], v[16:19]\ns_waitcnt lgkmcnt(3)\n" \
  "v_mfma_f32_16x16x32_f16 v[12:15], v[72:75], v[36:39], v[12:15]\nds_read_b128 v[32:35], v2 offset:3008\n" \
  "v_mfma_f32_16x16x32_f16 v[16:19], v[104:107], v[36:39], v[16:19]\ns_waitcnt lgkmcnt(3)\n" \
  "v_mfma_f32_16x16x32_f16 v[12:15], v[68:71], v[40:43], v[12:15]\nv_mfma_f32_16x16x32_f16 v[16:19], v[100:103], v[40:43], v[16:19]\n" \
  "ds_read_b128 v[124:127], v2 offset:3072\ns_waitcnt lgkmcnt(3)\nv_mfma_f32_16x16x32_f16 v[12:15], v[64:67], v[44:47], v[12:15]\n" \
  "v_mfma_f32_16x16x32_f16 v[16:19], v[96:99], v[44:47], v[16:19]\nds_read_b128 v[36:39], v2 offset:3136\ns_waitcnt lgkmcnt(3)\n" \
  "v_mfma_f32_16x16x32_f16 v[12:15], v[60:63], v[28:31], v[12:15]\nds_read_b64 v[40:41], v6 offset:128\nds_read_b64 v[42:43], v6 offset:136\n" \
  "v_mfma_f32_16x16x32_f16 v[16:19], v[92:95], v[28:31], v[16:19]\nds_read_b128 v[44:47], v2 offset:3200\ns_waitcnt lgkmcnt(4)\n" \
  "v_mfma_f32_16x16x32_f16 v[12:15], v[56:59], v[32:35], v[12:15]\nv_mfma_f32_16x16x32_f16 v[16:19], v[88:91], v[32:35], v[16:19]\n" \
  "ds_read_b128 v[28:31], v2 offset:3264\nv_mfma_f32_16x16x32_f16 v[20:23], v[120:123], v[32:35], v[20:23]\nds_read_b64 v[120:121], v6 offset:192\n" \
  "ds_read_b64 v[122:123], v6 offset:200\nv_mfma_f32_16x16x32_f16 v[16:19], v[84:87], v[124:127], v[16:19]\nds_read_b128 v[32:35], v2 offset:3328\n" \
  "v_mfma_f32_16x16x32_f16 v[20:23], v[116:119], v[124:127], v[20:23]\nds_read_b64 v[116:117], v6 offset:256\nds_read_b64 v[118:119], v6 offset:264\n" \
  "s_waitcnt lgkmcnt(6)\nv_mfma_f32_16x16x32_f16 v[16:19], v[80:83], v[36:39], v[16:19]\nds_read_b128 v[124:127], v2 offset:3392\n" \
  "v_mfma_f32_16x16x32_f16 v[20:23], v[112:115], v[36:39], v[20:23]\nds_read_b64 v[112:113], v6 offset:320\nds_read_b64 v[114:115], v6 offset:328\n" \
  "v_mfma_f32_16x16x32_f16 v[16:19], v[76:79], v[44:47], v[16:19]\nds_read_b128 v[36:39], v2 offset:3456\n" \
  "v_mfma_f32_16x16x32_f16 v[20:23], v[108:111], v[44:47], v[20:23]\ns_waitcnt lgkmcnt(6)\n" \
  "v_mfma_f32_16x16x32_f16 v[16:19], v[72:75], v[28:31], v[16:19]\nds_read_b128 v[108:111], v2 offset:3520\n" \
  "v_mfma_f32_16x16x32_f16 v[20:23], v[104:107], v[28:31], v[20:23]\nv_mfma_f32_16x16x32_f16 v[16:19], v[68:71], v[32:35], v[16:19]\n" \
  "ds_read_b128 v[44:47], v2 offset:3584\nv_mfma_f32_16x16x32_f16 v[20:23], v[100:103], v[32:35], v[20:23]\nds_read_b128 v[104:107], v2 offset:3648\n" \
  "ds_read_b32 v27, v9 offset:32\ns_waitcnt lgkmcnt(4)\nv_mfma_f32_16x16x32_f16 v[16:19], v[64:67], v[124:127], v[16:19]\n" \
  "ds_read_b128 v[28:31], v2 offset:3712\nv_mfma_f32_16x16x32_f16 v[20:23], v[96:99], v[124:127], v[20:23]\nds_read_b128 v[100:103], v2 offset:3776\n" \
  "ds_read2_b32 v[32:33], v8 offset0:32 offset1:33\nds_read2_b32 v[34:35], v8 offset0:34 offset1:35\n" \
  "v_mfma_f32_16x16x32_f16 v[16:19], v[60:63], v[36:39], v[16:19]\nds_read_b128 v[96:99], v2 offset:3840\n" \
  "v_mfma_f32_16x16x32_f16 v[20:23], v[92:95], v[36:39], v[20:23]\nds_read_b128 v[124:127], v2 offset:3904\ns_waitcnt lgkmcnt(8)\n" \
  "v_mfma_f32_16x16x32_f16 v[16:19], v[56:59], v[108:111], v[16:19]\nds_read_b128 v[92:95], v2 offset:3968\ns_waitcnt lgkmcnt(7)\n" \
  "v_readfirstlane_b32 s85, v27\ns_cmp_lt_u32 s85, 4\ns_cbranch_scc1 31f\n32:\nv_mfma_f32_16x16x32_f16 v[20:23], v[88:91], v[108:111], v[20:23]\n" \
  "ds_read_b128 v[36:39], v2 offset:4032\nds_read_b64_tr_b16 v[88:89], v5 offset:512\nds_read_b64_tr_b16 v[90:91], v5 offset:544\n" \
  "v_mfma_f32_16x16x32_f16 v[20:23], v[84:87], v[44:47], v[20:23]\nds_read_b64_tr_b16 v[108:109], v5 offset:768\n" \
  "ds_read_b64_tr_b16 v[110:111], v5 offset:800\nv_mfma_f32_16x16x32_f16 v[20:23], v[80:83], v[104:107], v[20:23]\n" \
  "ds_read_b64_tr_b16 v[84:85], v5 offset:1024\nds_read_b64_tr_b16 v[86:87], v5 offset:1056\ns_waitcnt lgkmcnt(12)\n" \
  "v_mfma_f32_16x16x32_f16 v[20:23], v[76:79], v[28:31], v[20:23]\nds_read_b64_tr_b16 v[44:45], v5 offset:1280\n" \
  "ds_read_b64_tr_b16 v[46:47], v5 offset:1312\nv_mfma_f32_16x16x32_f16 v[20:23], v[72:75], v[100:103], v[20:23]\ns_waitcnt lgkmcnt(10)\n" \
  "v_mfma_f32_16x16x32_f16 v[20:23], v[68:71], v[96:99], v[20:23]\nds_read_b64_tr_b16 v[80:81], v5 offset:1536\n" \
  "ds_read_b64_tr_b16 v[82:83], v5 offset:1568\nv_mfma_f32_16x16x32_f16 v[20:23], v[64:67], v[124:127], v[20:23]\n" \
  "ds_read_b64_tr_b16 v[104:105], v5 offset:1792\nds_read_b64_tr_b16 v[106:107], v5 offset:1824\ns_waitcnt lgkmcnt(12)\n" \
  "v_mfma_f32_16x16x32_f16 v[20:23], v[60:63], v[92:95], v[20:23]\nds_read_b64_tr_b16 v[76:77], v5 offset:2048\n" \
  "ds_read_b64_tr_b16 v[78:79], v5 offset:2080\nv_mfma_f32_16x16x32_f16 v[20:23], v[56:59], v[36:39], v[20:23]\ns_waitcnt lgkmcnt(10)\n" \
  "v_mfma_f32_16x16x32_f16 v[12:15], v[88:91], v[52:55], v[12:15]\nds_read_b64_tr_b16 v[28:29], v5 offset:2304\n" \
  "ds_read_b64_tr_b16 v[30:31], v5 offset:2336\nv_mfma_f32_16x16x32_f16 v[12:15], v[108:111], v[48:51], v[12:15]\n" \
  "ds_read_b64_tr_b16 v[72:73], v5 offset:2560\nds_read_b64_tr_b16 v[74:75], v5 offset:2592\ns_waitcnt lgkmcnt(10)\n" \
  "v_mfma_f32_16x16x32_f16 v[12:15], v[84:87], v[40:43], v[12:15]\nds_read_b64_tr_b16 v[100:101], v5 offset:2816\n" \
  "ds_read_b64_tr_b16 v[102:103], v5 offset:2848\nv_mfma_f32_16x16x32_f16 v[16:19], v[84:87], v[52:55], v[16:19]\n" \
  "ds_read_b64_tr_b16 v[68:69], v7 offset:0\nds_read_b64_tr_b16 v[70:71], v7 offset:64\n" \
  "v_mfma_f32_16x16x32_f16 v[12:15], v[44:47], v[120:123], v[12:15]\nv_mfma_f32_16x16x32_f16 v[16:19], v[44:47], v[48:51], v[16:19]\n" \
  "s_waitcnt lgkmcnt(10)\nv_mfma_f32_16x16x32_f16 v[12:15], v[80:83], v[116:119], v[12:15]\nds_read2_b32 v[96:97], v8 offset0:16 offset1:17\n" \
  "ds_read2_b32 v[98:99], v8 offset0:18 offset1:19\nv_mfma_f32_16x16x32_f16 v[16:19], v[80:83], v[40:43], v[16:19]\n" \
  "ds_read2_b32 v[64:65], v8 offset0:0 offset1:1\nds_read2_b32 v[66:67], v8 offset0:2 offset1:3\n" \
  "v_mfma_f32_16x16x32_f16 v[20:23], v[80:83], v[52:55], v[20:23]\nv_mfma_f32_16x16x32_f16 v[12:15], v[104:107], v[112:115], v[12:15]\n" \
  "v_mfma_f32_16x16x32_f16 v[16:19], v[104:107], v[120:123], v[16:19]\nv_mfma_f32_16x16x32_f16 v[20:23], v[104:107], v[48:51], v[20:23]\n" \
  "s_waitcnt lgkmcnt(10)\nv_mfma_f32_16x16x32_f16 v[16:19], v[76:79], v[116:119], v[16:19]\nds_read_b64_tr_b16 v[124:125], v7 offset:512\n" \
  "ds_read_b64_tr_b16 v[126:127], v7 offset:576\nv_mfma_f32_16x16x32_f16 v[20:23], v[76:79], v[40:43], v[20:23]\n" \
  "ds_read2_b32 v[60:61], v8 offset0:48 offset1:49\nds_read2_b32 v[62:63], v8 offset0:50 offset1:51\n" \
  "v_mfma_f32_16x16x32_f16 v[16:19], v[28:31], v[112:115], v[16:19]\nv_mfma_f32_16x16x32_f16 v[20:23], v[28:31], v[120:123], v[20:23]\n" \
  "s_waitcnt lgkmcnt(10)\nv_mfma_f32_16x16x32_f16 v[20:23], v[72:75], v[116:119], v[20:23]\nds_read_b64_tr_b16 v[92:93], v7 offset:1024\n" \
  "ds_read_b64_tr_b16 v[94:95], v7 offset:1088\nv_mfma_f32_16x16x32_f16 v[20:23], v[100:103], v[112:115], v[20:23]\n" \
  "ds_read_b64_tr_b16 v[56:57], v7 offset:1536\nds_read_b64_tr_b16 v[58:59], v7 offset:1600\ns_waitcnt lgkmcnt(10)\n" \
  "v_mfma_f32_16x16x32_f16 v[12:15], v[68:71], v[32:35], v[12:15]\nv_mfma_f32_16x16x32_f16 v[16:19], v[68:71], v[96:99], v[16:19]\n" \
  "s_waitcnt lgkmcnt(6)\nv_mfma_f32_16x16x32_f16 v[20:23], v[68:71], v[64:67], v[20:23]\ns_waitcnt lgkmcnt(2)\n" \
  "v_mfma_f32_16x16x32_f16 v[12:15], v[124:127], v[60:63], v[12:15]\nv_mfma_f32_16x16x32_f16 v[16:19], v[124:127], v[32:35], v[16:19]\n" \
  "v_mfma_f32_16x16x32_f16 v[20:23], v[124:127], v[96:99], v[20:23]\nv_mfma_f32_16x16x32_f16 v[16:19], v[92:95], v[60:63], v[16:19]\n" \
  "v_mfma_f32_16x16x32_f16 v[20:23], v[92:95], v[32:35], v[20:23]\ns_waitcnt lgkmcnt(0)\n" \
  "v_mfma_f32_16x16x32_f16 v[20:23], v[56:59], v[60:63], v[20:23]\ns_nop 1\nv_cvt_pkrtz_f16_f32 v24, v12, v13\nv_cvt_pkrtz_f16_f32 v25, v14, v15\n" \
  "v_pk_add_u16 v24, v24, 4 op_sel_hi:[1,0] clamp\nv_pk_add_u16 v25, v25, 4 op_sel_hi:[1,0] clamp\nds_write_b64 v4, v[24:25] offset:5120\n" \
  "v_cvt_pkrtz_f16_f32 v24, v16, v17\nv_cvt_pkrtz_f16_f32 v25, v18, v19\nv_pk_add_u16 v24, v24, 4 op_sel_hi:[1,0] clamp\n" \
  "v_pk_add_u16 v25, v25, 4 op_sel_hi:[1,0] clamp\nds_write_b64 v4, v[24:25] offset:6144\nv_cvt_pkrtz_f16_f32 v24, v20, v21\n" \
  "v_cvt_pkrtz_f16_f32 v25, v22, v23\nv_pk_add_u16 v24, v24, 4 op_sel_hi:[1,0] clamp\nv_pk_add_u16 v25, v25, 4 op_sel_hi:[1,0] clamp\n" \
  "ds_write_b64 v4, v[24:25] offset:7168\ns_setprio 0\ns_setpc_b64 s[90:91]\n31:\ns_movk_i32 s84, 0x4000\n33:\nds_read_b32 v27, v9 offset:32\n" \
  "s_sub_u32 s84, s84, 1\ns_cbranch_scc1 32b\ns_waitcnt lgkmcnt(0)\nv_readfirstlane_b32 s85, v27\ns_cmp_lt_u32 s85, 4\ns_cbranch_scc1 33b\n" \
  "s_branch 32b\n9:\ns_cmp_eq_u32 s89, 3\ns_cbranch_scc1 3f\ns_cmp_eq_u32 s89, 4\ns_cbranch_scc1 4f\ns_cmp_eq_u32 s89, 5\ns_cbranch_scc1 5f\n" \
  "ds_read_b64 v[124:125], v3 offset:576\nds_read_b64 v[126:127], v3 offset:584\nds_read_b64 v[120:121], v3 offset:64\n" \
  "ds_read_b64 v[122:123], v3 offset:72\nds_read_b64 v[116:117], v3 offset:640\nds_read_b64 v[118:119], v3 offset:648\n" \
  "ds_read_b64 v[112:113], v3 offset:128\nds_read_b64 v[114:115], v3 offset:136\nds_read_b64 v[108:109], v3 offset:704\n" \
  "ds_read_b64 v[110:111], v3 offset:712\nds_read_b64 v[104:105], v3 offset:192\nds_read_b64 v[106:107], v3 offset:200\n" \
  "ds_read_b64 v[100:101], v3 offset:768\nds_read_b64 v[102:103], v3 offset:776\nds_read_b64 v[96:97], v3 offset:256\n" \
  "ds_read_b64 v[98:99], v3 offset:264\nds_read_b64 v[92:93], v3 offset:832\nds_read_b64 v[94:95], v3 offset:840\n" \
  "ds_read_b64 v[88:89], v3 offset:320\nds_read_b64 v[90:91], v3 offset:328\nds_read_b64 v[84:85], v3 offset:896\n" \
  "ds_read_b64 v[86:87], v3 offset:904\nds_read_b64 v[80:81], v3 offset:384\nds_read_b64 v[82:83], v3 offset:392\n" \
  "ds_read_b64 v[76:77], v3 offset:960\nds_read_b64 v[78:79], v3 offset:968\nds_read_b64 v[72:73], v3 offset:448\n" \
  "ds_read_b64 v[74:75], v3 offset:456\nds_read_b64 v[68:69], v3 offset:1024\nds_read_b64 v[70:71], v3 offset:1032\n" \
  "ds_read_b64 v[64:65], v3 offset:512\nds_read_b64 v[66:67], v3 offset:520\ns_waitcnt lgkmcnt(0)\ns_barrier\nds_read_b128 v[24:27], v2 offset:0\n" \
  "ds_read_b128 v[28:31], v2 offset:64\nds_read_b128 v[32:35], v2 offset:128\nds_read_b128 v[36:39], v2 offset:192\ns_waitcnt lgkmcnt(3)\n" \
  "v_mfma_f32_16x16x32_f16 v[12:15], v[124:127], v[24:27], 0\ns_setprio 1\nds_read_b128 v[40:43], v2 offset:256\n" \
  "v_mfma_f32_16x16x32_f16 v[16:19], v[120:123], v[24:27], 0\ns_waitcnt lgkmcnt(3)\nv_mfma_f32_16x16x32_f16 v[12:15], v[116:119], v[28:31], v[12:15]\n" \
  "ds_read_b128 v[44:47], v2 offset:320\nv_mfma_f32_16x16x32_f16 v[16:19], v[112:115], v[28:31], v[16:19]\ns_waitcnt lgkmcnt(3)\n" \
  "v_mfma_f32_16x16x32_f16 v[12:15], v[108:111], v[32:35], v[12:15]\nds_read_b128 v[48:51], v2 offset:384\n" \
  "v_mfma_f32_16x16x32_f16 v[16:19], v[104:107], v[32:35], v[16:19]\ns_waitcnt lgkmcnt(3)\n" \
  "v_mfma_f32_16x16x32_f16 v[12:15], v[100:103], v[36:39], v[12:15]\nds_read_b128 v[52:55], v2 offset:448\n" \
  "v_mfma_f32_16x16x32_f16 v[16:19], v[96:99], v[36:39], v[16:19]\ns_waitcnt lgkmcnt(3)\n" \
  "v_mfma_f32_16x16x32_f16 v[12:15], v[92:95], v[40:43], v[12:15]\nds_read_b128 v[56:59], v2 offset:512\n" \
  "v_mfma_f32_16x16x32_f16 v[16:19], v[88:91], v[40:43], v[16:19]\nds_read_b128 v[60:63], v2 offset:576\ns_waitcnt lgkmcnt(3)\n" \
  "v_mfma_f32_16x16x32_f16 v[12:15], v[84:87], v[44:47], v[12:15]\nds_read_b128 v[120:123], v2 offset:640\n" \
  "v_mfma_f32_16x16x32_f16 v[16:19], v[80:83], v[44:47], v[16:19]\nds_read_b128 v[24:27], v2 offset:704\n" \
  "v_mfma_f32_16x16x32_f16 v[12:15], v[76:79], v[48:51], v[12:15]\nds_read_b128 v[112:115], v2 offset:768\n" \
  "v_mfma_f32_16x16x32_f16 v[16:19], v[72:75], v[48:51], v[16:19]\nds_read_b128 v[28:31], v2 offset:832\ns_waitcnt lgkmcnt(5)\n" \
  "v_mfma_f32_16x16x32_f16 v[12:15], v[68:71], v[52:55], v[12:15]\nds_read_b128 v[104:107], v2 offset:896\n" \
  "v_mfma_f32_16x16x32_f16 v[16:19], v[64:67], v[52:55], v[16:19]\nds_read_b128 v[32:35], v2 offset:960\n" \
  "v_mfma_f32_16x16x32_f16 v[16:19], v[124:127], v[56:59], v[16:19]\ns_waitcnt lgkmcnt(5)\n" \
  "v_mfma_f32_16x16x32_f16 v[16:19], v[116:119], v[60:63], v[16:19]\nv_mfma_f32_16x16x32_f16 v[16:19], v[108:111], v[120:123], v[16:19]\n" \
  "s_waitcnt lgkmcnt(3)\nv_mfma_f32_16x16x32_f16 v[16:19], v[100:103], v[24:27], v[16:19]\nv_cvt_pkrtz_f16_f32 v20, v12, v13\n" \
  "v_cvt_pkrtz_f16_f32 v21, v14, v15\nv_mfma_f32_16x16x32_f16 v[16:19], v[92:95], v[112:115], v[16:19]\n" \
  "v_pk_add_u16 v20, v20, 4 op_sel_hi:[1,0] clamp\ns_waitcnt lgkmcnt(2)\nv_mfma_f32_16x16x32_f16 v[16:19], v[84:87], v[28:31], v[16:19]\n" \
  "v_pk_add_u16 v21, v21, 4 op_sel_hi:[1,0] clamp\ns_waitcnt lgkmcnt(1)\nv_mfma_f32_16x16x32_f16 v[16:19], v[76:79], v[104:107], v[16:19]\n" \
  "ds_write_b64 v4, v[20:21] offset:0\ns_waitcnt lgkmcnt(1)\nv_mfma_f32_16x16x32_f16 v[16:19], v[68:71], v[32:35], v[16:19]\ns_nop 7\n" \
  "v_cvt_pkrtz_f16_f32 v20, v16, v17\nv_cvt_pkrtz_f16_f32 v21, v18, v19\nv_pk_add_u16 v20, v20, 4 op_sel_hi:[1,0] clamp\n" \
  "v_pk_add_u16 v21, v21, 4 op_sel_hi:[1,0] clamp\nds_write_b64 v4, v[20:21] offset:1024\ns_setprio 0\ns_setpc_b64 s[90:91]\n3:\n" \
  "ds_read_b64 v[124:125], v3 offset:0\nds_read_b64 v[126:127], v3 offset:8\nds_read_b64 v[120:121], v3 offset:64\n" \
  "ds_read_b64 v[122:123], v3 offset:72\nds_read_b64 v[116:117], v3 offset:128\nds_read_b64 v[118:119], v3 offset:136\n" \
  "ds_read_b64 v[112:113], v3 offset:192\nds_read_b64 v[114:115], v3 offset:200\nds_read_b64 v[108:109], v3 offset:256\n" \
  "ds_read_b64 v[110:111], v3 offset:264\nds_read_b64 v[104:105], v3 offset:320\nds_read_b64 v[106:107], v3 offset:328\n" \
  "ds_read_b64 v[100:101], v3 offset:384\nds_read_b64 v[102:103], v3 offset:392\nds_read_b64 v[96:97], v3 offset:448\n" \
  "ds_read_b64 v[98:99], v3 offset:456\nds_read_b64 v[92:93], v3 offset:512\nds_read_b64 v[94:95], v3 offset:520\n" \
  "ds_read_b64 v[88:89], v3 offset:576\nds_read_b64 v[90:91], v3 offset:584\nds_read_b64 v[84:85], v3 offset:640\n" \
  "ds_read_b64 v[86:87], v3 offset:648\nds_read_b64 v[80:81], v3 offset:704\nds_read_b64 v[82:83], v3 offset:712\n" \
  "ds_read_b64 v[76:77], v3 offset:768\nds_read_b64 v[78:79], v3 offset:776\nds_read_b64 v[72:73], v3 offset:832\n" \
  "ds_read_b64 v[74:75], v3 offset:840\nds_read_b64 v[68:69], v3 offset:896\nds_read_b64 v[70:71], v3 offset:904\n" \
  "ds_read_b64 v[64:65], v3 offset:960\nds_read_b64 v[66:67], v3 offset:968\nds_read_b64 v[60:61], v3 offset:1024\n" \
  "ds_read_b64 v[62:63], v3 offset:1032\nds_read_b64 v[56:57], v6 offset:256\nds_read_b64 v[58:59], v6 offset:264\n" \
  "ds_read_b64 v[52:53], v6 offset:128\nds_read_b64 v[54:55], v6 offset:136\nds_read_b64 v[48:49], v6 offset:320\n" \
  "ds_read_b64 v[50:51], v6 offset:328\nds_read_b64 v[44:45], v6 offset:192\nds_read_b64 v[46:47], v6 offset:200\ns_waitcnt lgkmcnt(0)\ns_barrier\n" \
  "ds_read_b128 v[24:27], v2 offset:448\nds_read_b128 v[28:31], v2 offset:512\nds_read_b128 v[32:35], v2 offset:576\n" \
  "ds_read_b128 v[36:39], v2 offset:640\nds_read_b128 v[40:43], v2 offset:704\ns_waitcnt lgkmcnt(3)\n" \
  "v_mfma_f32_16x16x32_f16 v[12:15], v[124:127], v[24:27], 0\ns_setprio 1\nds_read_b128 v[24:27], v2 offset:768\n" \
  "v_mfma_f32_16x16x32_f16 v[12:15], v[120:123], v[28:31], v[12:15]\nds_read_b128 v[28:31], v2 offset:832\ns_waitcnt lgkmcnt(3)\n" \
  "v_mfma_f32_16x16x32_f16 v[12:15], v[116:119], v[32:35], v[12:15]\nds_read_b128 v[32:35], v2 offset:896\n" \
  "v_mfma_f32_16x16x32_f16 v[12:15], v[112:115], v[36:39], v[12:15]\nds_read_b128 v[36:39], v2 offset:960\ns_waitcnt lgkmcnt(3)\n" \
  "v_mfma_f32_16x16x32_f16 v[12:15], v[108:111], v[40:43], v[12:15]\nds_read_b128 v[40:43], v2 offset:1024\n" \
  "v_mfma_f32_16x16x32_f16 v[12:15], v[104:107], v[24:27], v[12:15]\nds_read_b128 v[24:27], v2 offset:1088\ns_waitcnt lgkmcnt(3)\n" \
  "v_mfma_f32_16x16x32_f16 v[12:15], v[100:103], v[28:31], v[12:15]\nds_read_b128 v[28:31], v2 offset:1152\n" \
  "v_mfma_f32_16x16x32_f16 v[12:15], v[96:99], v[32:35], v[12:15]\ns_waitcnt lgkmcnt(3)\n" \
  "v_mfma_f32_16x16x32_f16 v[12:15], v[92:95], v[36:39], v[12:15]\nds_read_b128 v[32:35], v2 offset:1216\n" \
  "v_mfma_f32_16x16x32_f16 v[16:19], v[124:127], v[36:39], 0\ns_waitcnt lgkmcnt(3)\nv_mfma_f32_16x16x32_f16 v[12:15], v[88:91], v[40:43], v[12:15]\n" \
  "ds_read_b128 v[124:127], v2 offset:1280\nv_mfma_f32_16x16x32_f16 v[16:19], v[120:123], v[40:43], v[16:19]\ns_waitcnt lgkmcnt(3)\n" \
  "v_mfma_f32_16x16x32_f16 v[12:15], v[84:87], v[24:27], v[12:15]\nds_read_b128 v[36:39], v2 offset:1344\n" \
  "v_mfma_f32_16x16x32_f16 v[16:19], v[116:119], v[24:27], v[16:19]\ns_waitcnt lgkmcnt(3)\n" \
  "v_mfma_f32_16x16x32_f16 v[12:15], v[80:83], v[28:31], v[12:15]\nds_read_b128 v[120:123], v2 offset:1408\n" \
  "v_mfma_f32_16x16x32_f16 v[16:19], v[112:115], v[28:31], v[16:19]\ns_waitcnt lgkmcnt(3)\n" \
  "v_mfma_f32_16x16x32_f16 v[12:15], v[76:79], v[32:35], v[12:15]\nds_read_b128 v[40:43], v2 offset:1472\n" \
  "v_mfma_f32_16x16x32_f16 v[16:19], v[108:111], v[32:35], v[16:19]\ns_waitcnt lgkmcnt(3)\n" \
  "v_mfma_f32_16x16x32_f16 v[12:15], v[72:75], v[124:127], v[12:15]\nds_read_b128 v[116:119], v2 offset:1536\n" \
  "v_mfma_f32_16x16x32_f16 v[16:19], v[104:107], v[124:127], v[16:19]\nds_read_b128 v[24:27], v2 offset:1600\ns_waitcnt lgkmcnt(3)\n" \
  "v_mfma_f32_16x16x32_f16 v[12:15], v[68:71], v[36:39], v[12:15]\nds_read_b128 v[112:115], v2 offset:1664\nds_read_b32 v23, v9 offset:32\n" \
  "v_mfma_f32_16x16x32_f16 v[16:19], v[100:103], v[36:39], v[16:19]\nds_read_b128 v[28:31], v2 offset:1728\n" \
  "v_mfma_f32_16x16x32_f16 v[12:15], v[64:67], v[120:123], v[12:15]\nds_read_b128 v[108:111], v2 offset:1792\n" \
  "v_mfma_f32_16x16x32_f16 v[16:19], v[96:99], v[120:123], v[16:19]\nds_read_b128 v[32:35], v2 offset:1856\ns_waitcnt lgkmcnt(6)\n" \
  "v_mfma_f32_16x16x32_f16 v[12:15], v[60:63], v[40:43], v[12:15]\nds_read_b128 v[104:107], v2 offset:1920\n" \
  "v_mfma_f32_16x16x32_f16 v[16:19], v[92:95], v[40:43], v[16:19]\nds_read_b128 v[124:127], v2 offset:1984\ns_waitcnt lgkmcnt(5)\n" \
  "v_readfirstlane_b32 s85, v23\ns_cmp_lt_u32 s85, 3\ns_cbranch_scc1 51f\n52:\nv_mfma_f32_16x16x32_f16 v[16:19], v[88:91], v[116:119], v[16:19]\n" \
  "ds_read_b64_tr_b16 v[100:101], v5 offset:0\nds_read_b64_tr_b16 v[102:103], v5 offset:32\n" \
  "v_mfma_f32_16x16x32_f16 v[16:19], v[84:87], v[24:27], v[16:19]\nds_read_b64_tr_b16 v[36:37], v5 offset:256\n" \
  "ds_read_b64_tr_b16 v[38:39], v5 offset:288\nv_mfma_f32_16x16x32_f16 v[16:19], v[80:83], v[112:115], v[16:19]\n" \
  "ds_read_b64_tr_b16 v[96:97], v5 offset:512\nds_read_b64_tr_b16 v[98:99], v5 offset:544\ns_waitcnt lgkmcnt(9)\n" \
  "v_mfma_f32_16x16x32_f16 v[16:19], v[76:79], v[28:31], v[16:19]\nds_read_b64_tr_b16 v[120:121], v5 offset:768\n" \
  "ds_read_b64_tr_b16 v[122:123], v5 offset:800\nv_mfma_f32_16x16x32_f16 v[16:19], v[72:75], v[108:111], v[16:19]\ns_waitcnt lgkmcnt(9)\n" \
  "v_mfma_f32_16x16x32_f16 v[16:19], v[68:71], v[32:35], v[16:19]\nv_mfma_f32_16x16x32_f16 v[16:19], v[64:67], v[104:107], v[16:19]\n" \
  "s_waitcnt lgkmcnt(6)\nv_mfma_f32_16x16x32_f16 v[16:19], v[60:63], v[124:127], v[16:19]\n" \
  "v_mfma_f32_16x16x32_f16 v[12:15], v[100:103], v[56:59], v[12:15]\nv_mfma_f32_16x16x32_f16 v[16:19], v[100:103], v[52:55], v[16:19]\n" \
  "s_waitcnt lgkmcnt(2)\nv_mfma_f32_16x16x32_f16 v[12:15], v[36:39], v[48:51], v[12:15]\n" \
  "v_mfma_f32_16x16x32_f16 v[16:19], v[36:39], v[44:47], v[16:19]\nv_mfma_f32_16x16x32_f16 v[16:19], v[96:99], v[56:59], v[16:19]\n" \
  "s_waitcnt lgkmcnt(0)\nv_mfma_f32_16x16x32_f16 v[16:19], v[120:123], v[48:51], v[16:19]\ns_nop 3\nv_cvt_pkrtz_f16_f32 v20, v12, v13\n" \
  "v_cvt_pkrtz_f16_f32 v21, v14, v15\nv_pk_add_u16 v20, v20, 4 op_sel_hi:[1,0] clamp\nv_pk_add_u16 v21, v21, 4 op_sel_hi:[1,0] clamp\n" \
  "ds_write_b64 v4, v[20:21] offset:2048\nv_cvt_pkrtz_f16_f32 v20, v16, v17\nv_cvt_pkrtz_f16_f32 v21, v18, v19\n" \
  "v_pk_add_u16 v20, v20, 4 op_sel_hi:[1,0] clamp\nv_pk_add_u16 v21, v21, 4 op_sel_hi:[1,0] clamp\nds_write_b64 v4, v[20:21] offset:3072\n" \
  "s_setprio 0\ns_setpc_b64 s[90:91]\n51:\ns_movk_i32 s84, 0x4000\n53:\nds_read_b32 v23, v9 offset:32\ns_sub_u32 s84, s84, 1\ns_cbranch_scc1 52b\n" \
  "s_waitcnt lgkmcnt(0)\nv_readfirstlane_b32 s85, v23\ns_cmp_lt_u32 s85, 3\ns_cbranch_scc1 53b\ns_branch 52b\n4:\n" \
  "ds_read_b64 v[124:125], v3 offset:1088\nds_read_b64 v[126:127], v3 offset:1096\nds_read_b64 v[120:121], v3 offset:0\n" \
  "ds_read_b64 v[122:123], v3 offset:8\nds_read_b64 v[116:117], v3 offset:64\nds_read_b64 v[118:119], v3 offset:72\n" \
  "ds_read_b64 v[112:113], v3 offset:128\nds_read_b64 v[114:115], v3 offset:136\nds_read_b64 v[108:109], v3 offset:192\n" \
  "ds_read_b64 v[110:111], v3 offset:200\nds_read_b64 v[104:105], v3 offset:256\nds_read_b64 v[106:107], v3 offset:264\n" \
  "ds_read_b64 v[100:101], v3 offset:320\nds_read_b64 v[102:103], v3 offset:328\nds_read_b64 v[96:97], v3 offset:384\n" \
  "ds_read_b64 v[98:99], v3 offset:392\nds_read_b64 v[92:93], v3 offset:448\nds_read_b64 v[94:95], v3 offset:456\n" \
  "ds_read_b64 v[88:89], v3 offset:512\nds_read_b64 v[90:91], v3 offset:520\nds_read_b64 v[84:85], v3 offset:576\n" \
  "ds_read_b64 v[86:87], v3 offset:584\nds_read_b64 v[80:81], v3 offset:640\nds_read_b64 v[82:83], v3 offset:648\n" \
  "ds_read_b64 v[76:77], v3 offset:704\nds_read_b64 v[78:79], v3 offset:712\nds_read_b64 v[72:73], v3 offset:768\n" \
  "ds_read_b64 v[74:75], v3 offset:776\nds_read_b64 v[68:69], v3 offset:832\nds_read_b64 v[70:71], v3 offset:840\n" \
  "ds_read_b64 v[64:65], v3 offset:896\nds_read_b64 v[66:67], v3 offset:904\nds_read_b64 v[60:61], v3 offset:960\n" \
  "ds_read_b64 v[62:63], v3 offset:968\nds_read_b64 v[56:57], v3 offset:1024\nds_read_b64 v[58:59], v3 offset:1032\n" \
  "ds_read_b64 v[52:53], v6 offset:0\nds_read_b64 v[54:55], v6 offset:8\nds_read_b64 v[48:49], v6 offset:64\nds_read_b64 v[50:51], v6 offset:72\n" \
  "ds_read_b64 v[44:45], v6 offset:128\nds_read_b64 v[46:47], v6 offset:136\nds_read_b64 v[40:41], v6 offset:192\n" \
  "ds_read_b64 v[42:43], v6 offset:200\ns_waitcnt lgkmcnt(0)\ns_barrier\nds_read_b128 v[24:27], v2 offset:448\nds_read_b128 v[28:31], v2 offset:960\n" \
  "ds_read_b128 v[32:35], v2 offset:1472\nds_read_b128 v[36:39], v2 offset:1536\ns_waitcnt lgkmcnt(3)\n" \
  "v_mfma_f32_16x16x32_f16 v[12:15], v[124:127], v[24:27], 0\ns_setprio 1\nds_read_b128 v[24:27], v2 offset:1600\ns_waitcnt lgkmcnt(3)\n" \
  "v_mfma_f32_16x16x32_f16 v[16:19], v[124:127], v[28:31], 0\nds_read_b128 v[124:127], v2 offset:1664\ns_waitcnt lgkmcnt(3)\n" \
  "v_mfma_f32_16x16x32_f16 v[12:15], v[120:123], v[32:35], v[12:15]\nds_read_b128 v[28:31], v2 offset:1728\ns_waitcnt lgkmcnt(3)\n" \
  "v_mfma_f32_16x16x32_f16 v[12:15], v[116:119], v[36:39], v[12:15]\nds_read_b128 v[32:35], v2 offset:1792\ns_waitcnt lgkmcnt(3)\n" \
  "v_mfma_f32_16x16x32_f16 v[12:15], v[112:115], v[24:27], v[12:15]\nds_read_b128 v[36:39], v2 offset:1856\ns_waitcnt lgkmcnt(3)\n" \
  "v_mfma_f32_16x16x32_f16 v[12:15], v[108:111], v[124:127], v[12:15]\nds_read_b128 v[24:27], v2 offset:1920\ns_waitcnt lgkmcnt(3)\n" \
  "v_mfma_f32_16x16x32_f16 v[12:15], v[104:107], v[28:31], v[12:15]\nds_read_b128 v[124:127], v2 offset:1984\ns_waitcnt lgkmcnt(3)\n" \
  "v_mfma_f32_16x16x32_f16 v[12:15], v[100:103], v[32:35], v[12:15]\nds_read_b128 v[28:31], v2 offset:2048\ns_waitcnt lgkmcnt(3)\n" \
  "v_mfma_f32_16x16x32_f16 v[12:15], v[96:99], v[36:39], v[12:15]\nds_read_b128 v[32:35], v2 offset:2112\ns_waitcnt lgkmcnt(3)\n" \
  "v_mfma_f32_16x16x32_f16 v[12:15], v[92:95], v[24:27], v[12:15]\nds_read_b128 v[36:39], v2 offset:2176\ns_waitcnt lgkmcnt(3)\n" \
  "v_mfma_f32_16x16x32_f16 v[12:15], v[88:91], v[124:127], v[12:15]\nds_read_b128 v[24:27], v2 offset:2240\n" \
  "v_mfma_f32_16x16x32_f16 v[16:19], v[120:123], v[124:127], v[16:19]\nds_read_b64 v[120:121], v6 offset:256\nds_read_b64 v[122:123], v6 offset:264\n" \
  "s_waitcnt lgkmcnt(4)\nv_mfma_f32_16x16x32_f16 v[12:15], v[84:87], v[28:31], v[12:15]\nds_read_b128 v[124:127], v2 offset:2304\n" \
  "v_mfma_f32_16x16x32_f16 v[16:19], v[116:119], v[28:31], v[16:19]\nds_read_b64 v[116:117], v6 offset:320\nds_read_b64 v[118:119], v6 offset:328\n" \
  "v_mfma_f32_16x16x32_f16 v[12:15], v[80:83], v[32:35], v[12:15]\nds_read_b128 v[28:31], v2 offset:2368\n" \
  "v_mfma_f32_16x16x32_f16 v[16:19], v[112:115], v[32:35], v[16:19]\ns_waitcnt lgkmcnt(6)\n" \
  "v_mfma_f32_16x16x32_f16 v[12:15], v[76:79], v[36:39], v[12:15]\nds_read_b128 v[112:115], v2 offset:2432\n" \
  "v_mfma_f32_16x16x32_f16 v[16:19], v[108:111], v[36:39], v[16:19]\nds_read2_b32 v[32:33], v8 offset0:48 offset1:49\n" \
  "ds_read2_b32 v[34:35], v8 offset0:50 offset1:51\nv_mfma_f32_16x16x32_f16 v[12:15], v[72:75], v[24:27], v[12:15]\n" \
  "ds_read_b128 v[108:111], v2 offset:2496\nv_mfma_f32_16x16x32_f16 v[16:19], v[104:107], v[24:27], v[16:19]\n" \
  "ds_read2_b32 v[36:37], v8 offset0:32 offset1:33\nds_read2_b32 v[38:39], v8 offset0:34 offset1:35\ns_waitcnt lgkmcnt(6)\n" \
  "v_mfma_f32_16x16x32_f16 v[12:15], v[68:71], v[124:127], v[12:15]\nds_read_b128 v[104:107], v2 offset:2560\n" \
  "v_mfma_f32_16x16x32_f16 v[16:19], v[100:103], v[124:127], v[16:19]\nds_read_b128 v[24:27], v2 offset:2624\n" \
  "v_mfma_f32_16x16x32_f16 v[12:15], v[64:67], v[28:31], v[12:15]\nds_read_b128 v[100:103], v2 offset:2688\nds_read_b32 v23, v9 offset:32\n" \
  "v_mfma_f32_16x16x32_f16 v[16:19], v[96:99], v[28:31], v[16:19]\nds_read_b128 v[124:127], v2 offset:2752\ns_waitcnt lgkmcnt(7)\n" \
  "v_mfma_f32_16x16x32_f16 v[12:15], v[60:63], v[112:115], v[12:15]\nds_read_b128 v[96:99], v2 offset:2816\n" \
  "v_mfma_f32_16x16x32_f16 v[16:19], v[92:95], v[112:115], v[16:19]\nds_read_b128 v[28:31], v2 offset:2880\n" \
  "v_mfma_f32_16x16x32_f16 v[12:15], v[56:59], v[108:111], v[12:15]\nds_read_b128 v[92:95], v2 offset:2944\n" \
  "v_mfma_f32_16x16x32_f16 v[16:19], v[88:91], v[108:111], v[16:19]\nds_read_b128 v[112:115], v2 offset:3008\ns_waitcnt lgkmcnt(5)\n" \
  "v_readfirstlane_b32 s85, v23\ns_cmp_lt_u32 s85, 3\ns_cbranch_scc1 61f\n62:\nv_mfma_f32_16x16x32_f16 v[16:19], v[84:87], v[104:107], v[16:19]\n" \
  "ds_read_b64_tr_b16 v[88:89], v5 offset:0\nds_read_b64_tr_b16 v[90:91], v5 offset:32\n" \
  "v_mfma_f32_16x16x32_f16 v[16:19], v[80:83], v[24:27], v[16:19]\nds_read_b64_tr_b16 v[108:109], v5 offset:256\n" \
  "ds_read_b64_tr_b16 v[110:111], v5 offset:288\nv_mfma_f32_16x16x32_f16 v[16:19], v[76:79], v[100:103], v[16:19]\n" \
  "ds_read_b64_tr_b16 v[84:85], v5 offset:512\nds_read_b64_tr_b16 v[86:87], v5 offset:544\ns_waitcnt lgkmcnt(9)\n" \
  "v_mfma_f32_16x16x32_f16 v[16:19], v[72:75], v[124:127], v[16:19]\nds_read_b64_tr_b16 v[104:105], v5 offset:768\n" \
  "ds_read_b64_tr_b16 v[106:107], v5 offset:800\nv_mfma_f32_16x16x32_f16 v[16:19], v[68:71], v[96:99], v[16:19]\n" \
  "ds_read_b64_tr_b16 v[80:81], v5 offset:1024\nds_read_b64_tr_b16 v[82:83], v5 offset:1056\ns_waitcnt lgkmcnt(11)\n" \
  "v_mfma_f32_16x16x32_f16 v[16:19], v[64:67], v[28:31], v[16:19]\nds_read_b64_tr_b16 v[24:25], v5 offset:1280\n" \
  "ds_read_b64_tr_b16 v[26:27], v5 offset:1312\nv_mfma_f32_16x16x32_f16 v[16:19], v[60:63], v[92:95], v[16:19]\n" \
  "ds_read_b64_tr_b16 v[76:77], v5 offset:1536\nds_read_b64_tr_b16 v[78:79], v5 offset:1568\ns_waitcnt lgkmcnt(12)\n" \
  "v_mfma_f32_16x16x32_f16 v[16:19], v[56:59], v[112:115], v[16:19]\nds_read_b64_tr_b16 v[100:101], v5 offset:1792\n" \
  "ds_read_b64_tr_b16 v[102:103], v5 offset:1824\nv_mfma_f32_16x16x32_f16 v[12:15], v[88:91], v[52:55], v[12:15]\ns_waitcnt lgkmcnt(10)\n" \
  "v_mfma_f32_16x16x32_f16 v[12:15], v[108:111], v[48:51], v[12:15]\nds_read_b64_tr_b16 v[72:73], v7 offset:0\n" \
  "ds_read_b64_tr_b16 v[74:75], v7 offset:64\nv_mfma_f32_16x16x32_f16 v[12:15], v[84:87], v[44:47], v[12:15]\n" \
  "ds_read_b64_tr_b16 v[124:125], v7 offset:512\nds_read_b64_tr_b16 v[126:127], v7 offset:576\n" \
  "v_mfma_f32_16x16x32_f16 v[16:19], v[84:87], v[52:55], v[16:19]\ns_waitcnt lgkmcnt(10)\n" \
  "v_mfma_f32_16x16x32_f16 v[12:15], v[104:107], v[40:43], v[12:15]\nv_mfma_f32_16x16x32_f16 v[16:19], v[104:107], v[48:51], v[16:19]\n" \
  "v_mfma_f32_16x16x32_f16 v[12:15], v[80:83], v[120:123], v[12:15]\nv_mfma_f32_16x16x32_f16 v[16:19], v[80:83], v[44:47], v[16:19]\n" \
  "s_waitcnt lgkmcnt(6)\nv_mfma_f32_16x16x32_f16 v[12:15], v[24:27], v[116:119], v[12:15]\n" \
  "v_mfma_f32_16x16x32_f16 v[16:19], v[24:27], v[40:43], v[16:19]\nv_mfma_f32_16x16x32_f16 v[16:19], v[76:79], v[120:123], v[16:19]\n" \
  "s_waitcnt lgkmcnt(2)\nv_mfma_f32_16x16x32_f16 v[16:19], v[100:103], v[116:119], v[16:19]\n" \
  "v_mfma_f32_16x16x32_f16 v[12:15], v[72:75], v[32:35], v[12:15]\nv_mfma_f32_16x16x32_f16 v[16:19], v[72:75], v[36:39], v[16:19]\n" \
  "s_waitcnt lgkmcnt(0)\nv_mfma_f32_16x16x32_f16 v[16:19], v[124:127], v[32:35], v[16:19]\ns_nop 4\nv_cvt_pkrtz_f16_f32 v20, v12, v13\n" \
  "v_cvt_pkrtz_f16_f32 v21, v14, v15\nv_pk_add_u16 v20, v20, 4 op_sel_hi:[1,0] clamp\nv_pk_add_u16 v21, v21, 4 op_sel_hi:[1,0] clamp\n" \
  "ds_write_b64 v4, v[20:21] offset:4096\nv_cvt_pkrtz_f16_f32 v20, v16, v17\nv_cvt_pkrtz_f16_f32 v21, v18, v19\n" \
  "v_pk_add_u16 v20, v20, 4 op_sel_hi:[1,0] clamp\nv_pk_add_u16 v21, v21, 4 op_sel_hi:[1,0] clamp\nds_write_b64 v4, v[20:21] offset:5120\n" \
  "s_setprio 0\ns_setpc_b64 s[90:91]\n61:\ns_movk_i32 s84, 0x4000\n63:\nds_read_b32 v23, v9 offset:32\ns_sub_u32 s84, s84, 1\ns_cbranch_scc1 62b\n" \
  "s_waitcnt lgkmcnt(0)\nv_readfirstlane_b32 s85, v23\ns_cmp_lt_u32 s85, 3\ns_cbranch_scc1 63b\ns_branch 62b\n5:\n" \
  "ds_read_b64 v[124:125], v3 offset:1088\nds_read_b64 v[126:127], v3 offset:1096\nds_read_b64 v[120:121], v3 offset:0\n" \
  "ds_read_b64 v[122:123], v3 offset:8\nds_read_b64 v[116:117], v3 offset:64\nds_read_b64 v[118:119], v3 offset:72\n" \
  "ds_read_b64 v[112:113], v3 offset:128\nds_read_b64 v[114:115], v3 offset:136\nds_read_b64 v[108:109], v3 offset:192\n" \
  "ds_read_b64 v[110:111], v3 offset:200\nds_read_b64 v[104:105], v3 offset:256\nds_read_b64 v[106:107], v3 offset:264\n" \
  "ds_read_b64 v[100:101], v3 offset:320\nds_read_b64 v[102:103], v3 offset:328\nds_read_b64 v[96:97], v3 offset:384\n" \
  "ds_read_b64 v[98:99], v3 offset:392\nds_read_b64 v[92:93], v3 offset:448\nds_read_b64 v[94:95], v3 offset:456\n" \
  "ds_read_b64 v[88:89], v3 offset:512\nds_read_b64 v[90:91], v3 offset:520\nds_read_b64 v[84:85], v3 offset:576\n" \
  "ds_read_b64 v[86:87], v3 offset:584\nds_read_b64 v[80:81], v3 offset:640\nds_read_b64 v[82:83], v3 offset:648\n" \
  "ds_read_b64 v[76:77], v3 offset:704\nds_read_b64 v[78:79], v3 offset:712\nds_read_b64 v[72:73], v3 offset:768\n" \
  "ds_read_b64 v[74:75], v3 offset:776\nds_read_b64 v[68:69], v3 offset:832\nds_read_b64 v[70:71], v3 offset:840\n" \
  "ds_read_b64 v[64:65], v3 offset:896\nds_read_b64 v[66:67], v3 offset:904\nds_read_b64 v[60:61], v3 offset:960\n" \
  "ds_read_b64 v[62:63], v3 offset:968\nds_read_b64 v[56:57], v3 offset:1024\nds_read_b64 v[58:59], v3 offset:1032\n" \
  "ds_read_b64 v[52:53], v6 offset:0\nds_read_b64 v[54:55], v6 offset:8\nds_read_b64 v[48:49], v6 offset:64\nds_read_b64 v[50:51], v6 offset:72\n" \
  "ds_read_b64 v[44:45], v6 offset:128\nds_read_b64 v[46:47], v6 offset:136\nds_read_b64 v[40:41], v6 offset:192\n" \
  "ds_read_b64 v[42:43], v6 offset:200\ns_waitcnt lgkmcnt(0)\ns_barrier\nds_read_b128 v[24:27], v2 offset:1472\n" \
  "ds_read_b128 v[28:31], v2 offset:1984\nds_read_b128 v[32:35], v2 offset:2496\nds_read_b128 v[36:39], v2 offset:2560\ns_waitcnt lgkmcnt(3)\n" \
  "v_mfma_f32_16x16x32_f16 v[12:15], v[124:127], v[24:27], 0\ns_setprio 1\nds_read_b128 v[24:27], v2 offset:2624\ns_waitcnt lgkmcnt(3)\n" \
  "v_mfma_f32_16x16x32_f16 v[16:19], v[124:127], v[28:31], 0\nds_read_b128 v[124:127], v2 offset:2688\ns_waitcnt lgkmcnt(3)\n" \
  "v_mfma_f32_16x16x32_f16 v[12:15], v[120:123], v[32:35], v[12:15]\nds_read_b128 v[28:31], v2 offset:2752\ns_waitcnt lgkmcnt(3)\n" \
  "v_mfma_f32_16x16x32_f16 v[12:15], v[116:119], v[36:39], v[12:15]\nds_read_b128 v[32:35], v2 offset:2816\ns_waitcnt lgkmcnt(3)\n" \
  "v_mfma_f32_16x16x32_f16 v[12:15], v[112:115], v[24:27], v[12:15]\nds_read_b128 v[36:39], v2 offset:2880\ns_waitcnt lgkmcnt(3)\n" \
  "v_mfma_f32_16x16x32_f16 v[12:15], v[108:111], v[124:127], v[12:15]\nds_read_b128 v[24:27], v2 offset:2944\ns_waitcnt lgkmcnt(3)\n" \
  "v_mfma_f32_16x16x32_f16 v[12:15], v[104:107], v[28:31], v[12:15]\nds_read_b128 v[124:127], v2 offset:3008\ns_waitcnt lgkmcnt(3)\n" \
  "v_mfma_f32_16x16x32_f16 v[12:15], v[100:103], v[32:35], v[12:15]\nds_read_b128 v[28:31], v2 offset:3072\ns_waitcnt lgkmcnt(3)\n" \
  "v_mfma_f32_16x16x32_f16 v[12:15], v[96:99], v[36:39], v[12:15]\nds_read_b128 v[32:35], v2 offset:3136\ns_waitcnt lgkmcnt(3)\n" \
  "v_mfma_f32_16x16x32_f16 v[12:15], v[92:95], v[24:27], v[12:15]\nds_read_b128 v[36:39], v2 offset:3200\ns_waitcnt lgkmcnt(3)\n" \
  "v_mfma_f32_16x16x32_f16 v[12:15], v[88:91], v[124:127], v[12:15]\nds_read_b128 v[24:27], v2 offset:3264\n" \
  "v_mfma_f32_16x16x32_f16 v[16:19], v[120:123], v[124:127], v[16:19]\nds_read_b64 v[120:121], v6 offset:256\nds_read_b64 v[122:123], v6 offset:264\n" \
  "s_waitcnt lgkmcnt(4)\nv_mfma_f32_16x16x32_f16 v[12:15], v[84:87], v[28:31], v[12:15]\nds_read_b128 v[124:127], v2 offset:3328\n" \
  "v_mfma_f32_16x16x32_f16 v[16:19], v[116:119], v[28:31], v[16:19]\nds_read_b64 v[116:117], v6 offset:320\nds_read_b64 v[118:119], v6 offset:328\n" \
  "v_mfma_f32_16x16x32_f16 v[12:15], v[80:83], v[32:35], v[12:15]\nds_read_b128 v[28:31], v2 offset:3392\n" \
  "v_mfma_f32_16x16x32_f16 v[16:19], v[112:115], v[32:35], v[16:19]\ns_waitcnt lgkmcnt(6)\n" \
  "v_mfma_f32_16x16x32_f16 v[12:15], v[76:79], v[36:39], v[12:15]\nds_read_b128 v[112:115], v2 offset:3456\n" \
  "v_mfma_f32_16x16x32_f16 v[16:19], v[108:111], v[36:39], v[16:19]\nds_read2_b32 v[32:33], v8 offset0:16 offset1:17\n" \
  "ds_read2_b32 v[34:35], v8 offset0:18 offset1:19\nv_mfma_f32_16x16x32_f16 v[12:15], v[72:75], v[24:27], v[12:15]\n" \
  "ds_read_b128 v[108:111], v2 offset:3520\nv_mfma_f32_16x16x32_f16 v[16:19], v[104:107], v[24:27], v[16:19]\n" \
  "ds_read2_b32 v[36:37], v8 offset0:0 offset1:1\nds_read2_b32 v[38:39], v8 offset0:2 offset1:3\ns_waitcnt lgkmcnt(6)\n" \
  "v_mfma_f32_16x16x32_f16 v[12:15], v[68:71], v[124:127], v[12:15]\nds_read_b128 v[104:107], v2 offset:3584\n" \
  "v_mfma_f32_16x16x32_f16 v[16:19], v[100:103], v[124:127], v[16:19]\nds_read_b128 v[24:27], v2 offset:3648\n" \
  "ds_read2_b32 v[100:101], v8 offset0:32 offset1:33\nds_read2_b32 v[102:103], v8 offset0:34 offset1:35\n" \
  "v_mfma_f32_16x16x32_f16 v[12:15], v[64:67], v[28:31], v[12:15]\nds_read_b128 v[124:127], v2 offset:3712\nds_read_b32 v23, v9 offset:32\n" \
  "v_mfma_f32_16x16x32_f16 v[16:19], v[96:99], v[28:31], v[16:19]\nds_read_b128 v[96:99], v2 offset:3776\ns_waitcnt lgkmcnt(9)\n" \
  "v_mfma_f32_16x16x32_f16 v[12:15], v[60:63], v[112:115], v[12:15]\nds_read_b128 v[28:31], v2 offset:3840\n" \
  "v_mfma_f32_16x16x32_f16 v[16:19], v[92:95], v[112:115], v[16:19]\nds_read_b128 v[92:95], v2 offset:3904\n" \
  "v_mfma_f32_16x16x32_f16 v[12:15], v[56:59], v[108:111], v[12:15]\nds_read_b128 v[112:115], v2 offset:3968\n" \
  "v_mfma_f32_16x16x32_f16 v[16:19], v[88:91], v[108:111], v[16:19]\nds_read_b128 v[88:91], v2 offset:4032\ns_waitcnt lgkmcnt(5)\n" \
  "v_readfirstlane_b32 s85, v23\ns_cmp_lt_u32 s85, 3\ns_cbranch_scc1 71f\n72:\nv_mfma_f32_16x16x32_f16 v[16:19], v[84:87], v[104:107], v[16:19]\n" \
  "ds_read_b64_tr_b16 v[108:109], v5 offset:1024\nds_read_b64_tr_b16 v[110:111], v5 offset:1056\n" \
  "v_mfma_f32_16x16x32_f16 v[16:19], v[80:83], v[24:27], v[16:19]\nds_read_b64_tr_b16 v[84:85], v5 offset:1280\n" \
  "ds_read_b64_tr_b16 v[86:87], v5 offset:1312\nv_mfma_f32_16x16x32_f16 v[16:19], v[76:79], v[124:127], v[16:19]\n" \
  "ds_read_b64_tr_b16 v[104:105], v5 offset:1536\nds_read_b64_tr_b16 v[106:107], v5 offset:1568\ns_waitcnt lgkmcnt(9)\n" \
  "v_mfma_f32_16x16x32_f16 v[16:19], v[72:75], v[96:99], v[16:19]\nds_read_b64_tr_b16 v[80:81], v5 offset:1792\n" \
  "ds_read_b64_tr_b16 v[82:83], v5 offset:1824\nv_mfma_f32_16x16x32_f16 v[16:19], v[68:71], v[28:31], v[16:19]\n" \
  "ds_read_b64_tr_b16 v[24:25], v5 offset:2048\nds_read_b64_tr_b16 v[26:27], v5 offset:2080\ns_waitcnt lgkmcnt(11)\n" \
  "v_mfma_f32_16x16x32_f16 v[16:19], v[64:67], v[92:95], v[16:19]\nds_read_b64_tr_b16 v[76:77], v5 offset:2304\n" \
  "ds_read_b64_tr_b16 v[78:79], v5 offset:2336\nv_mfma_f32_16x16x32_f16 v[16:19], v[60:63], v[112:115], v[16:19]\n" \
  "ds_read_b64_tr_b16 v[124:125], v5 offset:2560\nds_read_b64_tr_b16 v[126:127], v5 offset:2592\ns_waitcnt lgkmcnt(12)\n" \
  "v_mfma_f32_16x16x32_f16 v[16:19], v[56:59], v[88:91], v[16:19]\nds_read_b64_tr_b16 v[72:73], v5 offset:2816\n" \
  "ds_read_b64_tr_b16 v[74:75], v5 offset:2848\nv_mfma_f32_16x16x32_f16 v[12:15], v[108:111], v[52:55], v[12:15]\ns_waitcnt lgkmcnt(10)\n" \
  "v_mfma_f32_16x16x32_f16 v[12:15], v[84:87], v[48:51], v[12:15]\nds_read_b64_tr_b16 v[96:97], v7 offset:0\n" \
  "ds_read_b64_tr_b16 v[98:99], v7 offset:64\nv_mfma_f32_16x16x32_f16 v[12:15], v[104:107], v[44:47], v[12:15]\n" \
  "ds_read_b64_tr_b16 v[68:69], v7 offset:512\nds_read_b64_tr_b16 v[70:71], v7 offset:576\n" \
  "v_mfma_f32_16x16x32_f16 v[16:19], v[104:107], v[52:55], v[16:19]\ns_waitcnt lgkmcnt(10)\n" \
  "v_mfma_f32_16x16x32_f16 v[12:15], v[80:83], v[40:43], v[12:15]\nds_read_b64_tr_b16 v[28:29], v7 offset:1024\n" \
  "ds_read_b64_tr_b16 v[30:31], v7 offset:1088\nv_mfma_f32_16x16x32_f16 v[16:19], v[80:83], v[48:51], v[16:19]\n" \
  "ds_read2_b32 v[64:65], v8 offset0:48 offset1:49\nds_read2_b32 v[66:67], v8 offset0:50 offset1:51\n" \
  "v_mfma_f32_16x16x32_f16 v[12:15], v[24:27], v[120:123], v[12:15]\nv_mfma_f32_16x16x32_f16 v[16:19], v[24:27], v[44:47], v[16:19]\n" \
  "s_waitcnt lgkmcnt(10)\nv_mfma_f32_16x16x32_f16 v[12:15], v[76:79], v[116:119], v[12:15]\nds_read_b64_tr_b16 v[92:93], v7 offset:1536\n" \
  "ds_read_b64_tr_b16 v[94:95], v7 offset:1600\nv_mfma_f32_16x16x32_f16 v[16:19], v[76:79], v[40:43], v[16:19]\n" \
  "v_mfma_f32_16x16x32_f16 v[16:19], v[124:127], v[120:123], v[16:19]\ns_waitcnt lgkmcnt(8)\n" \
  "v_mfma_f32_16x16x32_f16 v[16:19], v[72:75], v[116:119], v[16:19]\nv_mfma_f32_16x16x32_f16 v[12:15], v[96:99], v[32:35], v[12:15]\n" \
  "v_mfma_f32_16x16x32_f16 v[16:19], v[96:99], v[36:39], v[16:19]\ns_waitcnt lgkmcnt(4)\n" \
  "v_mfma_f32_16x16x32_f16 v[12:15], v[68:71], v[100:103], v[12:15]\nv_mfma_f32_16x16x32_f16 v[16:19], v[68:71], v[32:35], v[16:19]\n" \
  "s_waitcnt lgkmcnt(2)\nv_mfma_f32_16x16x32_f16 v[12:15], v[28:31], v[64:67], v[12:15]\n" \
  "v_mfma_f32_16x16x32_f16 v[16:19], v[28:31], v[100:103], v[16:19]\ns_waitcnt lgkmcnt(0)\n" \
  "v_mfma_f32_16x16x32_f16 v[16:19], v[92:95], v[64:67], v[16:19]\ns_nop 4\nv_cvt_pkrtz_f16_f32 v20, v12, v13\nv_cvt_pkrtz_f16_f32 v21, v14, v15\n" \
  "v_pk_add_u16 v20, v20, 4 op_sel_hi:[1,0] clamp\nv_pk_add_u16 v21, v21, 4 op_sel_hi:[1,0] clamp\nds_write_b64 v4, v[20:21] offset:6144\n" \
  "v_cvt_pkrtz_f16_f32 v20, v16, v17\nv_cvt_pkrtz_f16_f32 v21, v18, v19\nv_pk_add_u16 v20, v20, 4 op_sel_hi:[1,0] clamp\n" \
  "v_pk_add_u16 v21, v21, 4 op_sel_hi:[1,0] clamp\nds_write_b64 v4, v[20:21] offset:7168\ns_setprio 0\ns_setpc_b64 s[90:91]\n71:\n" \
  "s_movk_i32 s84, 0x4000\n73:\nds_read_b32 v23, v9 offset:32\ns_sub_u32 s84, s84, 1\ns_cbranch_scc1 72b\ns_waitcnt lgkmcnt(0)\n" \
  "v_readfirstlane_b32 s85, v23\ns_cmp_lt_u32 s85, 3\ns_cbranch_scc1 73b\ns_branch 72b\n"
#define CONV_CLOB "memory", "scc", "s84", "s85", "s90", "s91", "v12", "v13", "v14", "v15", "v16", "v17", "v18", "v19", "v20", "v21", "v22", "v23", "v24", "v25", "v26", "v27", "v28", "v29", "v30", "v31", "v32", "v33", "v34", "v35", "v36", "v37", "v38", "v39", "v40", "v41", "v42", "v43", "v44", "v45", "v46", "v47", "v48", "v49", "v50", "v51", "v52", "v53", "v54", "v55", "v56", "v57", "v58", "v59", "v60", "v61", "v62", "v63", "v64", "v65", "v66", "v67", "v68", "v69", "v70", "v71", "v72", "v73", "v74", "v75", "v76", "v77", "v78", "v79", "v80", "v81", "v82", "v83", "v84", "v85", "v86", "v87", "v88", "v89", "v90", "v91", "v92", "v93", "v94", "v95", "v96", "v97", "v98", "v99", "v100", "v101", "v102", "v103", "v104", "v105", "v106", "v107", "v108", "v109", "v110", "v111", "v112", "v113", "v114", "v115", "v116", "v117", "v118", "v119", "v120", "v121", "v122", "v123", "v124", "v125", "v126", "v127"
constexpr int TDP = 1216, T_3 = 4 * TDP, T_4 = T_3 + 512, TB = T_4 + 320;
constexpr int C_U = 480, UB = C_U + 4096 + 96, C_G5 = 0, C_G3 = 3200, C_G9 = 6400, C_G7 = C_G9 + 2096, GB = C_G7 + 2080 + 112;
constexpr int O_FL = 2 * TB, O_UL = O_FL + 64, O_IM = O_UL + 2 * UB, O_ST = O_IM + 2 * GB;
constexpr int X_TB = 10240, X_UL = X_TB + TB, X_IM = X_UL + UB, X_ST = X_IM + GB;
constexpr int X_WR = X_IM + 128 * IP;
#define X_RW(w) (LW + 9216 * (2 + ((w) >> 1)) + 2560 * ((w) & 1))
constexpr int RP = 1216;
constexpr int LW = 36864 + 512, LA = LW, LM = LW + 36864;
constexpr int L_SC = LM + 2048, SMEM = O_IM + 256 * IP;
static_assert(L_SC + 1152 <= SMEM && O_ST + 8192 <= SMEM && X_IM + 128 * IP <= LM && X_ST + 8192 <= LM && UB % 128 == 64 && GB % 128 == 64 && SMEM <= 79872, "");
struct TOps { uint32_t xa, xb, ya, pd, pf, pg, pe; };
__device__ __forceinline__ void table_ops(uint32_t im, uint32_t tb, int ct, int lane, TOps& t) {
  const int j = lane & 15, kq = lane >> 4, q4 = (lane >> 2) & 3, p4 = lane & 3, jj = j + 16 * ct;
  const uint32_t sa = im + (uint32_t)(IP * (lane >> 2));
  t.xa = sa + (uint32_t)(I_X + 8 * p4); t.xb = sa + (uint32_t)(I_X + 32 + 8 * (p4 & 1)); t.ya = sa + (uint32_t)(32 * ct + 8 * p4);
  t.pd = tb + (uint32_t)(2 * (541 - (8 * jj + 4 * (kq & 1) + 256 * (kq >> 1))));
  t.pf = tb + (uint32_t)((kq & 1) ? T_4 + 2 * (63 - jj) : T_3 + 2 * (127 - 2 * jj));
  t.pg = t.pf - ((kq & 1) ? 64 : 2);
  t.pe = tb + (uint32_t)(p4 * (TDP + 2) + ((lane < 12) ? 2 * (32 - q4) : 2 * (572 + (q4 & 1))));
  asm volatile("" : "+v"(t.xa), "+v"(t.xb), "+v"(t.ya), "+v"(t.pd), "+v"(t.pf), "+v"(t.pg), "+v"(t.pe));
}
typedef __attribute__((address_space(3))) char* lptr;
#define LP(T, a) ((__attribute__((address_space(3))) T*)(uintptr_t)(a))
__device__ __forceinline__ uint32_t table_tile(const TOps& t, int ct, int lane, uint32_t evp, float dsk TSP) {
  u32x4 ev;
  f32x4 ad[2], af[2];
  asm volatile(T_ASM : "=&v"(ad[0]), "=&v"(ad[1]), "=&v"(af[0]), "=&v"(af[1]), "=&v"(ev) : "v"(t.xa), "v"(t.xb), "v"(t.ya), "v"(evp) : T_CLOB);
  const int e0 = (int)(ev[0] + ev[2]), e1 = (int)(ev[1] + ev[3]);
  const int em = min(max(max(max(e0, e1), (int)((__float_as_uint(dsk) >> 23) & 0xffu) - 133), -110), 115);
  const int ts = ((uint32_t)(em + 12) <= 8u) ? 1 : -3 - em;
  float s0 = __builtin_amdgcn_ldexpf(1.f, e0 - 29 + ts), s1 = __builtin_amdgcn_ldexpf(1.f, e1 - 29 + ts);
  float d0 = (ct == 0 && lane == 0) ? __builtin_amdgcn_ldexpf(dsk, ts) : 0.f;
  asm volatile("s_nop 1" : "+v"(ad[0]), "+v"(ad[1]), "+v"(af[0]), "+v"(af[1]), "+v"(s0), "+v"(s1), "+v"(d0));
  const f32x4 k = ad[0] * s0 + (f32x4){d0, 0.f, 0.f, 0.f} + ad[1] * s1, f = af[0] * s0 + af[1] * s1;
  auto pk = [](float a, float b) { uint32_t r; asm("v_cvt_pk_f16_f32 %0, %1, %2" : "=v"(r) : "v"(a), "v"(b)); return r; };
  asm volatile("s_nop 0" : : "v"(k), "v"(f));
  const uint32_t p32 = pk(k[3], k[2]), p10 = pk(k[1], k[0]), p21 = pk(k[2], k[1]), p01 = pk(f[0], f[1]), p23 = pk(f[2], f[3]);
  *LP(u32x2, t.pd + 3 * TDP + 6) = (u32x2){p32, p10};
  *LP(uint32_t, t.pd + TDP + 2) = p32; *LP(uint32_t, t.pd + TDP + 6) = p10;
  *LP(uint32_t, t.pd + 2) = p21; *LP(uint32_t, t.pd + 2 * TDP + 6) = p21;
  *LP(u16, t.pd) = (u16)p32; *LP(u16, t.pd + 2 * TDP + 4) = (u16)p32;
  asm volatile("ds_write_b16_d16_hi %0, %1 offset:6\n\tds_write_b16_d16_hi %0, %1 offset:%2" : : "v"(t.pd), "v"(p10), "n"(2 * TDP + 10) : "memory");
  if (ct) {
    const uint32_t va = (uint32_t)__builtin_amdgcn_readlane((int)p32, 63), vb = (uint32_t)__builtin_amdgcn_readlane((int)p23, 15) >> 16;
    *LP(u16, t.pe) = (u16)((lane < 12) ? va : vb);
  }
  if (lane < 32) {
    *LP(u16, t.pf + 128) = (u16)p01; *LP(u16, t.pg + 128) = (u16)(p01 >> 16);
    *LP(u16, t.pf) = (u16)p23; *LP(u16, t.pg) = (u16)(p23 >> 16);
  }
  return (uint32_t)(127 + 5 - ts) << 23;
}
__device__ __forceinline__ uint32_t hadd(uint32_t a, uint32_t b) { return __builtin_bit_cast(uint32_t, __builtin_bit_cast(f16x2, a) + __builtin_bit_cast(f16x2, b)); }
template <bool FULL> __device__ __forceinline__ void sums(char* sm, const u32x4 p, const u32x4 c) {
  const uint32_t D[8] = {p[0], p[1], p[2], p[3], c[0], c[1], c[2], c[3]};
  uint32_t O[7];
#pragma unroll
  for (int j = 0; j < 7; ++j) O[j] = __builtin_amdgcn_alignbit(D[j + 1], D[j], 16);
  u32x4 a, b;
#pragma unroll
  for (int j = 0; j < 4; ++j) a[j] = hadd(D[j + 2], hadd(O[j + 1], O[j + 2]));
  *(u32x4*)(sm + C_G3 + 32) = a;
#pragma unroll
  for (int j = 0; j < 4; ++j) b[j] = hadd(a[j], hadd(D[j + 1], D[j + 3]));
  *(u32x4*)(sm + C_G5 + 32) = b;
  if (FULL) {
#pragma unroll
    for (int j = 0; j < 4; ++j) a[j] = hadd(b[j], hadd(O[j], O[j + 3]));
    *(u32x4*)(sm + C_G7 + 32) = a;
#pragma unroll
    for (int j = 0; j < 4; ++j) b[j] = hadd(a[j], hadd(D[j], D[j + 4]));
    *(u32x4*)(sm + C_G9 + 32) = b;
  }
}
__device__ __forceinline__ void count_up(uint32_t a) {
  uint32_t t;
  asm volatile("s_mov_b64 exec, 1\n\tv_mov_b32 %0, 1\n\tds_add_u32 %1, %0\n\ts_mov_b64 exec, -1" : "=&v"(t) : "v"(a) : "memory");
}
__device__ __forceinline__ int pad_at(int r) { return 16 * (r & 1) + 3200 * (r >> 1) - ((r >= 6) ? 3200 - 2096 : 0); }
struct COps { uint32_t b, a, o, g3, k3, g4, k4, f; };
__device__ __forceinline__ void conv_ops(uint32_t tb, uint32_t ub, uint32_t cb, int lane, COps& e) {
  const uint32_t j = lane & 15, kq = lane >> 4, q4 = (lane >> 2) & 3, p4 = lane & 3;
  e.b = ub + 32u * j + 16u * kq;
  e.a = tb + p4 * TDP + 32u - 8u * q4 + 16u * kq;
  e.g3 = cb + C_G5 + 8 + 64u * kq + 8u * (q4 + p4) + (q4 & 1u) * 3200u;
  e.g4 = cb + C_G9 + 8 + 128u * kq + 16u * q4 + 8u * p4 + (q4 & 1u) * 2096u;
  e.k3 = tb + T_3 + 120 - 8u * j + 16u * kq;
  e.k4 = tb + T_4 + 60 - 4u * j + 16u * kq;
  asm volatile("" : "+v"(e.b), "+v"(e.a), "+v"(e.g3), "+v"(e.g4), "+v"(e.k3), "+v"(e.k4));
}
#define CONV_RUN(prog, e, ...) do { int prog_ = (prog); \
    asm volatile("s_call_b64 s[90:91], kf_conv" : "+{s89}"(prog_), "+{v2}"(e.b), "+{v3}"(e.a), "+{v4}"(e.o), "+{v5}"(e.g3), "+{v6}"(e.k3), "+{v7}"(e.g4), \
                 "+{v8}"(e.k4), "+{v9}"(e.f) __VA_ARGS__ : : CONV_CLOB); } while (0)

template <bool E>
__device__ __forceinline__ void ln_stats(const uint32_t* v, int lane, float sc, f32x2* t, f32x4 z, f32x2& rc, f32x2& nc, float& e) {
  float s1a = z[0], s2a = z[1], s1b = z[2], s2b = z[3];
#pragma unroll
  for (int k = 0; k < 4; ++k) {
    s1a = dot2(v[k], 0x3f803f80u, s1a); s2a = dot2(v[k], v[k], s2a);
    s1b = dot2(v[4 + k], 0x3f803f80u, s1b); s2b = dot2(v[4 + k], v[4 + k], s2b);
  }
#pragma unroll
  for (int k = 0; k < 9; ++k) t[k] = (f32x2){bflo(v[k]), bfhi(v[k])};
  const float rs = row_sum(fold16(dot2(v[8], 0x3f803f80u, fold32(s1a, s1b)), dot2(v[8], v[8], fold32(s2a, s2b))));
  if (E) { e = row_sum(e); e += __int_as_float(__builtin_amdgcn_update_dpp(0, __float_as_int(e), 0x142, 0xa, 0xf, false)); }
  const f32x2 mean = (f32x2){rdl(rs, 15), rdl(rs, 47)} * (1.f / D_), m2 = mean * mean;
  f32x2 var = (f32x2){rdl(rs, 31), rdl(rs, 63)} * (1.f / D_) - m2;
  asm volatile("" : "+v"(t[0]), "+v"(t[1]), "+v"(t[2]), "+v"(t[3]), "+v"(t[4]), "+v"(t[5]), "+v"(t[6]), "+v"(t[7]), "+v"(t[8]));
  if (__builtin_expect(!(m2[0] <= 16.f * var[0] && m2[1] <= 16.f * var[1]), 0)) {
    float q[3] = {0.f, 0.f, 0.f};
#pragma unroll
    for (int k = 0; k < 9; ++k) {
      const f32x2 a = t[k] - ((k < 4 || (k == 8 && lane < 32)) ? mean[0] : mean[1]);
      q[k >> 2] += a[0] * a[0] + a[1] * a[1];
    }
    float qa = q[0] + ((lane < 32) ? q[2] : 0.f), qb = q[1] + ((lane < 32) ? 0.f : q[2]);
    wave_sum2(qa, qb);
    var = (f32x2){qa, qb} * (1.f / D_);
  }
  const uint32_t mp = pack2(mean[0], mean[1]), vp = pack2(var[0], var[1]);
  const f32x2 ve = (f32x2){bflo(vp), bfhi(vp)} + 1e-5f;
  const uint32_t e2 = pack2(ve[0], ve[1]);
  const uint32_t sp = pack2(__builtin_amdgcn_sqrtf(bflo(e2)), __builtin_amdgcn_sqrtf(bfhi(e2)));
  rc = (f32x2){__builtin_amdgcn_rcpf(bflo(sp)), __builtin_amdgcn_rcpf(bfhi(sp))} * sc;
  nc = -(f32x2){bflo(mp), bfhi(mp)} * rc;
}
template <bool H>
__device__ __forceinline__ void ln_apply(const f32x2* t, int lane, f32x2 rc, f32x2 nc, uint32_t* c) {
  const float nm[3] = {nc[0], nc[1], (lane < 32) ? nc[0] : nc[1]}, rr[3] = {rc[0], rc[1], (lane < 32) ? rc[0] : rc[1]};
  f32x2 dd[9];
#pragma unroll
  for (int k = 0; k < 9; ++k) dd[k] = __builtin_elementwise_fma(t[k], (f32x2){rr[k >> 2], rr[k >> 2]}, (f32x2){nm[k >> 2], nm[k >> 2]});
  asm volatile("" : "+v"(dd[0]), "+v"(dd[1]), "+v"(dd[2]), "+v"(dd[3]), "+v"(dd[4]), "+v"(dd[5]), "+v"(dd[6]), "+v"(dd[7]), "+v"(dd[8]));
#pragma unroll
  for (int k = 0; k < 9; ++k) {
    const f32x2 d = dd[k];
    typedef __attribute__((ext_vector_type(2))) uint16_t u16x2;
    if (H) c[k] = __builtin_bit_cast(uint32_t, __builtin_elementwise_add_sat(__builtin_bit_cast(u16x2, __builtin_amdgcn_cvt_pkrtz(d[0], d[1])), (u16x2){4, 4}));
    else c[k] = pack2(d[0], d[1]);
  }
}
__device__ __forceinline__ uint32_t med3u(uint32_t a, uint32_t b, uint32_t c) { return max(min(a, b), min(max(a, b), c)); }
__device__ __forceinline__ uint32_t oct_maxu(uint32_t v) {
  v = max(v, (uint32_t)DPP_I((int)v, 0xb1, 0xf));
  v = max(v, (uint32_t)DPP_I((int)v, 0x4e, 0xf));
  v = max(v, (uint32_t)DPP_I((int)v, 0x141, 0xf));
  return v;
}

#define KARGS const u16* __restrict__ x, const u16* __restrict__ Are, const u16* __restrict__ Aim, const u16* __restrict__ Bre, \
    const u16* __restrict__ Bim, const u16* __restrict__ Cre, const u16* __restrict__ Cim, const u16* __restrict__ logdt, \
    const u16* __restrict__ Dskip, char* __restrict__ ws, const u16* __restrict__ wr, const u16* __restrict__ wg, \
    const u16* __restrict__ wu, const u16* __restrict__ wd, u16* __restrict__ out
#define KPASS x, Are, Aim, Bre, Bim, Cre, Cim, logdt, Dskip, ws, wr, wg, wu, wd, out
template <int ROLE, int FFN = -1>
__device__ __forceinline__ void role_body(char* smem, KARGS) {
  const int tid = threadIdx.x, lane = tid & 63;
  const int w = __builtin_amdgcn_readfirstlane(tid >> 6), wl = w & 3;
  const int b = blockIdx.x & 255, bx = b & 7, bj = b >> 3;
  constexpr bool isl = ROLE == 1 || ROLE == 2;
  const int tg = isl ? 32 * (bj >> 2) + 4 * bx + (bj & 3) : 32 * bx + bj;
  u32x4 xa, xc;
  uint32_t ep, r0;
#define P_WAIT(n, ...) asm volatile("s_waitcnt vmcnt(" #n ")\n\tv_lshlrev_b32 %0, 16, v100\n\tv_lshlrev_b32 %1, 16, v101\n\tv_lshlrev_b32 %2, 16, v102" \
    : "=v"(ldt), "=v"(are), "=v"(aim) : : "v100", "v101", "v102" __VA_ARGS__)
#define P_MORE() asm volatile("v_lshlrev_b32 %0, 16, v103\n\tv_lshlrev_b32 %1, 16, v104\n\tv_lshlrev_b32 %2, 16, v105\n\tv_lshlrev_b32 %3, 16, v106" \
    : "=v"(bre), "=v"(bim), "=v"(cre), "=v"(cim) : : "v103", "v104", "v105", "v106")
  TSD;
  TS(0, 0);
  const uint32_t lb0 = (uint32_t)(uintptr_t)(__attribute__((address_space(3))) char*)smem;
  const int i = lane & 15, kq = lane >> 4;
  const u32x4 ones = {0xffffffffu, 0xffffffffu, 0xffffffffu, 0xffffffffu}, zero4 = {0u, 0u, 0u, 0u};
  auto u_again = [&](u32x4& uw, const u32x4& ut, uint32_t uo) {
#pragma unroll 1
    for (int spin = 0; spin < SPIN_CAP; ++spin) {
      const bool na = ((uw[0] | uw[1] | uw[2] | uw[3]) & UMASK) != 0u;
      if (__builtin_expect(!__any(na), 1)) break;
      TSC(19);
      SLEEP(USLEEP);
      if (na) { LD16W(uw, uo); uw ^= ut; }
    }
  };
  constexpr uint32_t S16 = 0x41800000u, LMASK = 0x00070007u;
  auto v_put = [&](u32x4 v, const u32x4& ut, bool other, uint32_t vo) {
    const bool second = __any(other || ((v[0] | v[1] | v[2] | v[3]) & 0x40004000u) != 0u);
    if (__builtin_expect(second, 0)) v = (v & ~LMASK) | (~ut & LMASK); else v = (v & ~LMASK) | ut;
    ST16(vo, v);
    return second;
  };
  auto v_end = [&](uint32_t T, int n) { DRAIN(); if (lane == 0) ST4(O_DN + 16u * n, T); };
  if constexpr (!isl) {
    const int ch = wl >> 1, tl = tid & 255;
    int* evp = (int*)(smem + O_FL + 16 * ch);
    float ldt, are, aim, dsk;
    if constexpr (ROLE == 0) {
      __builtin_amdgcn_s_setprio(KPRIO);
      COps e;
      conv_ops(lb0 + (uint32_t)(ch * TB), lb0 + (uint32_t)(O_UL + ch * UB), lb0 + (uint32_t)(O_IM + ch * GB), lane, e);
      e.o = lb0 + O_ST + (uint32_t)(16 * ((4 * i + 2 * (kq >> 1) + ch) ^ (i >> 1)) + 8 * (kq & 1));
      e.f = lb0 + O_FL;
      TOps t;
      table_ops(lb0 + (uint32_t)(O_IM + 128 * IP * ch), lb0 + (uint32_t)(TB * ch), wl & 1, lane, t);
      uint32_t pz = lb0 + O_IM + (uint32_t)(((lane >> 3) & 1) * GB + pad_at(lane & 7)), im = lb0 + (uint32_t)(O_IM + IP * tl);
      asm volatile("" : "+v"(e.o), "+v"(e.f), "+v"(pz), "+v"(im));
      float bre, bim, cre, cim;
      P_WAIT(0, , "v103", "v104", "v105", "v106", "v107", "v109");
      asm volatile("" : "={v109}"(ep));
      const uint32_t T = ((uint32_t)__builtin_amdgcn_readfirstlane(ep) + 1u) * 0x9E3779B1u;
      if (w == 3 && lane < 8) ST16(O_KEY + 128u * b + 16u * lane, ones);
      P_MORE();
      asm volatile("v_lshlrev_b32 %0, 16, v107" : "=v"(dsk) : : "v107");
      TS(0, 1);
      evp[wl & 1] = gen_y(ldt, are, aim, bre, bim, cre, cim, im, [] {} TSA);
      TS(0, 8);
      BAR();
      TS(0, 2);
      const uint32_t cv = table_tile(t, wl & 1, lane, lb0 + O_FL + 16 * ch, dsk TSA), cs = (uint32_t)__builtin_amdgcn_readfirstlane(cv);
      *(uint32_t*)(smem + O_FL + 40 + 4 * ch) = cv;
      TS(0, 9);
      gsync0(lb0 + O_FL + 36, 4);
      TS(0, 3);
      *(u32x4*)(smem + O_ST + 16 * tl) = ones;
      *(u32x4*)(smem + O_ST + 4096 + 16 * tl) = ones;
      if (w == 0) *LP(u32x4, pz) = zero4;
      DRAIN();
      CONV_RUN(wl & 1, e);
      if (wl & 1) asm volatile("s_mov_b64 exec, 1\n\tglobal_store_dwordx2 %0, %1, %2 sc1" : : "v"(O_SC + 16u * (2 * tg + ch)), "v"((u32x2){T, cs}), "s"(ws) : "memory");
      TS(0, 5);
    } else {
      __builtin_amdgcn_s_setprio(UPRIO);
      const uint32_t ck0 = (uint32_t)__builtin_amdgcn_s_memtime(), rt0 = (uint32_t)__builtin_amdgcn_s_memrealtime();
#pragma unroll
      for (int k = 0; k < 3; ++k) *(u32x4*)(smem + ((16 * (tl + 256 * k) < O_FL) ? 16 * (tl + 256 * k) : O_FL + 32)) = zero4;
      if (w == 7) *(u32x4*)(smem + O_UL + (lane >> 5) * UB + 16 * min(lane & 31, 29)) = zero4;
      uint32_t uo = u_at(tl >> 1, 2 * tg + (tid & 1)), go = O_SU + (uint32_t)(256 * bx + 32 * wl + (lane & 31) + 4 * (lane & 32)) * 128u;
      uint32_t ul = (uint32_t)(O_UL + (tid & 1) * UB + C_U - 16 + 16 * (tl >> 1)), ug = (uint32_t)(O_IM + (tid & 1) * GB + 16 * (tl >> 1)), so = lb0 + O_ST + 16u * (uint32_t)(lane ^ (lane >> 3));
      uint32_t vo = O_V + (uint32_t)tg * VP + 16u * lane;
      asm volatile("" : "+v"(uo), "+v"(go), "+v"(ul), "+v"(ug), "+v"(so), "+v"(vo));
      P_WAIT(1, , "v109");
      TS(0, 1);
      evp[2 + (wl & 1)] = gen_x(ldt, are, aim, smem + O_IM + IP * tl);
      asm volatile("s_waitcnt vmcnt(0)" : "={v109}"(ep));
      const uint32_t T = ((uint32_t)__builtin_amdgcn_readfirstlane(ep) + 1u) * 0x9E3779B1u;
      TS(0, 8);
      BAR();
      TS(0, 2);
      if (w == 7 && bj >= 24) {
        const uint32_t t_ = ahead(ck0, rt0, VDB);
        ST16(O_SB + (uint32_t)(64 * (lane >> 3) + 8 * bx + bj - 24) * 128u + 16u * (lane & 7), ((u32x4){T, t_, T, t_}));
      }
      u32x4 ut = {utag(T, 0), utag(T, 1), utag(T, 2), utag(T, 3)};
      asm volatile("" : "+v"(ut));
      SLEEP(GSLEEP);
      u32x2 pv;
      pv[1] = gate(ws, go, T);
      TS(0, 3);
      until(latest(pv[1]) + UD);
      TS(0, 7);
      u32x4 uw, ux;
      asm volatile("global_load_dwordx4 %0, %2, %4 sc1\n\tglobal_load_dwordx4 %1, %3, %4 sc1"
                   : "=&v"(uw), "=&v"(ux) : "v"(uo), "v"(uo + 64 * UP * 32), "s"(ws) : "memory");
      __builtin_amdgcn_s_setprio(FPR);
      asm volatile("s_waitcnt vmcnt(1)" : "+v"(uw));
      uw ^= ut;
      *(u32x4*)(smem + ul + 16) = uw;
      asm volatile("s_waitcnt vmcnt(0)" : "+v"(ux));
      TS(0, 11);
      ux ^= ut;
      *(u32x4*)(smem + ul + 2048 + 16) = ux;
      if (__builtin_expect(__any(((uw[0] | uw[1] | uw[2] | uw[3] | ux[0] | ux[1] | ux[2] | ux[3]) & UMASK) != 0u), 0)) {
        u_again(uw, ut, uo);
        u_again(ux, ut, uo + 64 * UP * 32);
        *(u32x4*)(smem + ul + 16) = uw;
        *(u32x4*)(smem + ul + 2048 + 16) = ux;
      }
      BAR();
      TS(0, 4);
      const u32x4 pw = *(const u32x4*)(smem + ul);
      if (wl < 2) {
        const u32x4 px = *(const u32x4*)(smem + ul + 2048);
        sums<true>(smem + ug, pw, uw);
        sums<false>(smem + ug + 2048, px, ux);
      } else sums<true>(smem + ug, pw, uw);
      count_up(lb0 + O_FL + 32);
      __builtin_amdgcn_s_setprio(SPR);
      TS(0, 5);
      const bool other = *(const uint32_t*)(smem + O_FL + 40 + 4 * (lane & 1)) != S16;
      bool second = false;
#pragma unroll
      for (int k = 0; k < 2; ++k) {
        const uint32_t o = ((k ? 0x3247u : 0x1065u) >> (4 * wl) & 7u) * 1024u;
        u32x4 v;
#pragma unroll 1
        for (int spin = 0; spin < SPIN_CAP; ++spin) {
          asm volatile("ds_read_b128 %0, %1\n\ts_waitcnt lgkmcnt(0)" : "=&v"(v) : "v"(so + o) : "memory");
          if (!__any(max4(v) == 0xffffffffu)) break;
          __builtin_amdgcn_s_sleep(VPS);
        }
        TS(0, 12 + k);
        second |= v_put(v, ut, other, vo + o);
        TS(0, 10 - 4 * k);
      }
      if (__builtin_expect(second, 0)) v_end(T, 4 * tg + wl);
    }
    TSE();
  } else {
  uint32_t* cand = (uint32_t*)(smem + LM);
  uint32_t* sel = cand + 128;
  uint32_t* kl = sel + 48;
  uint32_t* cnt = sel + 64;
  const uint32_t ca = lb0 + LM + 4 * (128 + 64);
  u16* hm = (u16*)(cnt + 16);
  uint32_t T;
  const bool ffn = (FFN < 0) ? bj < 18 : FFN, isx = FFN != 1 && bj >= 24;
  const int fb = 18 * bx + bj;
  constexpr bool ln2 = ROLE == 2;
  const int g0 = tg, g2 = tg;
  u32x4 bw[18];
#pragma unroll
  for (int s = 0; s < 9; ++s) bw[s] = (u32x4){0u, 0u, 0u, 0u};
  int tid2 = threadIdx.x;
  uint32_t tv = 0u, tk = 0u;
  auto v_look = [&](uint32_t tu, int lane) {
    uint32_t tm = latest(gate(ws, O_SB + (uint32_t)(64 * bx + lane) * 128u, T)) - V2D;
    if ((int)(tm - (tu + UD + VD2)) < 0) tm = tu + UD + VD2;
    cnt[8] = tm;
    count_up(ca + 52);
  };
  auto v_time = [&] { gwait(ca + 52, 1); tv = (uint32_t)__builtin_amdgcn_readfirstlane(cnt[8]); };
  uint32_t ts1 = 0u;
  auto rest = [&] {
    if (ts1 && (int)((uint32_t)__builtin_amdgcn_s_memrealtime() - ts1) > 0) {
      v_time();
      ts1 = (tv - TSTOP) | 1u;
      if ((int)((uint32_t)__builtin_amdgcn_s_memrealtime() - ts1) > 0) { TS(0, 8); until(tv + RD); ts1 = 0u; }
    }
  };
  auto gu_dma = [&](int q0, int q1, auto depth) {
    const char* gs = (const char*)((w < 2) ? wg : wu) + (size_t)((144 * (w & 1)) * (F_ * 2) + fb * 64);
    const uint32_t gv = (uint32_t)((lane >> 2) * (F_ * 2) + 16 * ((lane & 3) ^ (lane >> 5) ^ (w & 2)));
#pragma unroll 1
    for (int q = q0; q < q1; ++q) {
      const int k = (int)((((bj & 1) ? ORD1 : ((unsigned __int128)0x876543210u << 36 | 0x876543210ull)) >> (4 * (q - (q >= 9 ? 9 : 0)) + (q >= 9 ? 36 : 0))) & 15u), h = q >= 9;
      if (q >= RQ) rest();
      dma16(gs + (size_t)(288 * h + 16 * k) * (F_ * 2), gv, lb0 + LW * h + 9216 * w + 1024 * k);
      if (!BALL || q < RQ || ts1) asm volatile("s_waitcnt vmcnt(%0)" : : "n"(depth.value) : "memory");
    }
  };
  auto down_dma = [&](int k0, int k1, int lane) {
#pragma unroll 1
    for (int k = k0; k < k1; ++k) {
      const int o = 9216 * wl + 2304 * k;
      const char* ds = (const char*)wd + (size_t)(fb * 36864 + o);
      const uint32_t dl = lb0 + o + 32 * ((4 * wl + k + 1) >> 1);
      if (ROLE == 1) rest();
      dma16d(ds, 16u * lane, dl);
      dma16d(ds + 1024, 16u * lane, dl + 1024);
      uint32_t keep;
      asm volatile("s_mov_b32 %0, m0\n\ts_mov_b32 m0, %3\n\ts_mov_b64 exec, 0xffff\n\tglobal_load_lds_dwordx4 %1, %2" DPOL2 "\n\ts_mov_b64 exec, -1\n\ts_mov_b32 m0, %0\n\ts_waitcnt vmcnt(%4)"
                   : "=&s"(keep) : "v"(16u * lane), "s"(ds + 2048), "s"(dl + 2048), "n"(ROLE == 1 ? DDEPTH : 40) : "memory");
    }
  };
  auto gu_read = [&](int h) {
    const uint32_t ra = lb0 + LW * h + (uint32_t)(((lane >> 1) & 1) * 18432 + (8 * kq + ((lane >> 2) & 3)) * 64 + ((w ^ (kq & 1) ^ (lane & 2)) * 16) + 8 * (lane & 1));
#pragma unroll
    for (int s = 0; s < 9; ++s) {
      const u32x2 h0 = trd(ra + 2048 * s), h1 = trd(ra + 2048 * s + 256);
      bw[9 * h + s] = (u32x4){h0[0], h0[1], h1[0], h1[1]};
    }
#pragma unroll
    for (int s = 0; s < 9; ++s) asm volatile("" : "+v"(bw[9 * h + s]));
  };
  const uint32_t vearly = (uint32_t)((VEARLY >> (8 * (g0 >> 5))) & 0xffull);
#define TOUCH(pa) asm volatile("global_load_dword v126, %0, off" DPOL4 : : "v"(pa) : "memory", "v126")
  uint32_t wv[5], xv[9];
  auto rows_xw = [&](int lane) {
    u32x4 wa, xr, xs;
    const uint32_t xo = (uint32_t)((8 * g2 + 2 * wl) * (D_ * 2));
    asm volatile("global_load_dwordx4 %0, %5, %9\n\tglobal_load_dword %1, %6, %9 offset:1024\n\t"
                 "global_load_dwordx4 %2, %7, %10\n\tglobal_load_dwordx4 %3, %7, %10 offset:1152\n\tglobal_load_dword %4, %8, %10 offset:1024\n\ts_waitcnt vmcnt(0)"
                 : "=&v"(wa), "=&v"(wv[4]), "=&v"(xr), "=&v"(xs), "=&v"(xv[8])
                 : "v"(16u * lane), "v"(4u * (lane & 31)), "v"(xo + 16u * lane), "v"(xo + 4u * lane + 32u * (lane & 32)), "s"(wr), "s"(x) : "memory");
#pragma unroll
    for (int k = 0; k < 4; ++k) { wv[k] = wa[k]; xv[k] = xr[k]; xv[4 + k] = xs[k]; }
  };
  auto ln1 = [&](int g, int o, uint32_t c0) {
    uint32_t uo = u_at(g, 64 * wl + lane);
    uint32_t ta = lb0 + (uint32_t)(o + ((lane >> 2) & 3) * RP + (64 * wl + 16 * kq + 4 * (lane & 3)) * 2);
    uint32_t la = (uint32_t)(o + 2 * wl * RP + 16 * lane), lt = (uint32_t)(o + (2 * wl + (lane >> 5)) * RP + 1024 + 4 * (lane & 31));
    uint32_t sg = O_SU + (uint32_t)(256 * (lane >> 3) + g) * 128u + 16u * (lane & 7);
    asm volatile("" : "+v"(uo), "+v"(ta), "+v"(la), "+v"(lt), "+v"(sg));
    f32x4 z4 = {0.f, 0.f, 0.f, 0.f};
    asm volatile("" : "+v"(z4));
    asm volatile("s_waitcnt vmcnt(0)" : "={v[100:103]}"(xa), "={v[104:107]}"(xc), "={v108}"(r0), "={v109}"(ep));
    TS(0, 1);
    const uint32_t v[9] = {xa[0], xa[1], xa[2], xa[3], xc[0], xc[1], xc[2], xc[3], r0};
    uint32_t c[9];
    f32x2 tf[9];
    T = ((uint32_t)__builtin_amdgcn_readfirstlane(ep) + 1u) * 0x9E3779B1u;
    if (wl == 3) { const uint32_t now = (uint32_t)__builtin_amdgcn_s_memrealtime() + SD; ST16(sg, ((u32x4){T, now, T, now})); }
    const uint32_t tj = ((T >> (3 * wl)) & 0x00070007u) | (((T >> (12 + wl)) & 0x00010001u) << 14);
    uint32_t tw[3] = {(tj & 0xffffu) * 0x10001u, (tj >> 16) * 0x10001u, 0u};
    tw[2] = (lane < 32) ? tw[0] : tw[1];
    asm volatile("" : "+v"(tw[0]), "+v"(tw[1]), "+v"(tw[2]));
    f32x2 rc, nc;
    float e_;
    ln_stats<false>(v, lane, 0.03125f, tf, z4, rc, nc, e_);
    ln_apply<true>(tf, lane, rc, nc, c);
#pragma unroll
    for (int k = 0; k < 9; ++k) c[k] = (c[k] & ~UMASK) | tw[k >> 2];
    *(u32x4*)(smem + la) = (u32x4){c[0], c[1], c[2], c[3]};
    *(u32x4*)(smem + la + RP) = (u32x4){c[4], c[5], c[6], c[7]};
    *(uint32_t*)(smem + lt) = c[8];
    u32x4 ha, hb;
    {
      uint32_t t, cc, n = SPIN_CAP;
      asm volatile("s_mov_b64 exec, 1\n\tv_mov_b32 %2, 1\n\tds_add_u32 %5, %2\n\ts_mov_b64 exec, -1\n"
                   "1:\n\tds_read_b32 %2, %5\n\tds_read_b64_tr_b16 v[40:41], %6\n\tds_read_b64_tr_b16 v[42:43], %6 offset:%7\n\tds_read_b64_tr_b16 v[44:45], %6 offset:512\n\t"
                   "ds_read_b64_tr_b16 v[46:47], %6 offset:%8\n\ts_sub_u32 %4, %4, 1\n\ts_cbranch_scc1 2f\n\ts_waitcnt lgkmcnt(4)\n\tv_readfirstlane_b32 %3, %2\n\t"
                   "s_cmp_lt_u32 %3, 4\n\ts_cbranch_scc1 1b\n2:\n\ts_waitcnt lgkmcnt(0)"
                   : "=&{v[40:43]}"(ha), "=&{v[44:47]}"(hb), "=&v"(t), "=&s"(cc), "+s"(n) : "v"(c0), "v"(ta), "n"(4 * RP), "n"(512 + 4 * RP) : "memory", "scc");
    }
    ST16(uo, ha);
    ST16(uo + 64 * 128, hb);
    if (wl == 0) {
      const u32x2 h4 = trd(ta + 1024), h5 = trd(ta + 1024 + 4 * RP);
      ST16(uo + 128 * 128, ((u32x4){h4[0], h4[1], h5[0], h5[1]}));
    }
  };
  const int dX = 512 - 24 + 8 * bx + bj;
  if constexpr (ROLE == 2) {
    const int tl = tid - 256;
    if (isx) {
      __builtin_amdgcn_s_setprio(QPRIO0);
      *(u32x4*)(smem + X_TB + 16 * tl) = zero4;
      *(u32x4*)(smem + X_TB + 16 * min(256 + tl, (TB + C_U) / 16 - 1)) = zero4;
      const int pr = (wl + XROT) & 3;
      COps e;
      conv_ops(lb0 + X_TB, lb0 + X_UL, lb0 + X_IM, lane, e);
      TOps t;
      table_ops(lb0 + X_IM, lb0 + X_TB, wl & 1, lane, t);
      e.o = lb0 + X_ST + (uint32_t)(16 * ((2 * i + (kq >> 1)) ^ ((i >> 2) & 1)) + 8 * (kq & 1));
      e.f = ca + 28 - 32;
      asm volatile("" : "+v"(e.o), "+v"(e.f));
      float ldt, are, aim, bre, bim, cre, cim, dsk;
      P_WAIT(0, , "v103", "v104", "v105", "v106", "v107", "v109");
      asm volatile("" : "={v109}"(ep));
      T = ((uint32_t)__builtin_amdgcn_readfirstlane(ep) + 1u) * 0x9E3779B1u;
      P_MORE();
      asm volatile("v_lshlrev_b32 %0, 16, v107" : "=v"(dsk) : : "v107");
      TS(0, 1);
      __builtin_amdgcn_s_setprio(QPRIO);
      int* evp = (int*)(sel + 44);
      char* im = smem + X_IM + IP * (tl & 127);
      evp[wl] = (wl < 2) ? gen_y(ldt, are, aim, bre, bim, cre, cim, lb0 + X_IM + IP * tl, [] {} TSA) : gen_x(ldt, are, aim, im);
      TS(0, 2);
      __builtin_amdgcn_s_setprio(QPRIO2);
      gsync(ca + 8, 4);
      if (wl < 2) evp[-4 + wl] = (int)table_tile(t, wl & 1, lane, lb0 + LM + 4 * (128 + 44), dsk TSA);
      if (wl < 2) gsync0(ca + 8, 8); else { count_up(ca + 8); SLEEP(XSL); gwait(ca + 8, 8); }
      const uint32_t cs = (uint32_t)__builtin_amdgcn_readfirstlane(evp[-4]);
      if (wl == 3) *(u32x4*)(smem + X_IM + pad_at(lane & 7)) = zero4;
      TS(0, 3);
      CONV_RUN(2 + pr, e, , "+{v11}"(tid2));
      __builtin_amdgcn_s_setprio(3);
      {
        const int lane = tid2 & 63;
        const u32x4 v = *(const u32x4*)(smem + X_ST + 2048 * pr + 1024 * (lane >> 5) + 16 * ((lane & 31) ^ ((lane >> 3) & 1)));
        if (__builtin_expect(v_put(v, (u32x4){utag(T, 0), utag(T, 1), utag(T, 2), utag(T, 3)}, cs != S16, v_at(dX, 64 * pr + lane)), 0)) {
          DRAIN();
          if (lane == 0) ST4(O_DN + 16u * (4 * (dX - 256) + pr), T);
        }
        if (pr == 0 && lane == 0) asm volatile("global_store_dwordx2 %0, %1, %2 sc1" : : "v"(O_SC + 16u * dX), "v"((u32x2){T, cs}), "s"(ws) : "memory");
      }
      TS(0, 6);
    }
    if (!isx) __builtin_amdgcn_s_setprio(PPRIO);
    asm volatile("" : "+v"(tid2));
    if (!isx) rows_xw(tid2 & 63);
    if (!isx && !ffn) {
      SLEEP(RSLEEP);
#pragma unroll
      for (int k = 0; k < 7; ++k) ST16(O_SL + (uint32_t)((6 * bx + bj - 18) * 1792 + 256 * k - 256 + tid2) * 16u, ones);
      DRAIN();
    }
    if (!isx) BAR();
    const uint32_t tp = (uint32_t)__builtin_amdgcn_readfirstlane(cnt[4]);
    if (!isx) T = (uint32_t)__builtin_amdgcn_readfirstlane(sel[43]);
    if (!isx && w == 4) v_look(tp, tid2 & 63);
    v_time();
  } else {
    __builtin_amdgcn_s_setprio(3);
    if (tid < 16) cnt[tid] = 0u;
    BAR();
    if (tid < 20) ((uint32_t*)(hm + 320))[tid] = 0u;
    if (tid >= 184) *(u32x4*)(smem + L_SC - 16 * 184 + 16 * tid) = (u32x4){0x41804180u, 0x41804180u, 0x41804180u, 0x41804180u};
    uint32_t sink = 0;
    ln1(tg, 0, ca + 12);
    TS(0, 2);
    __builtin_amdgcn_s_setprio(NPRIO);
    asm volatile("s_waitcnt vmcnt(0)" : "+v"(sink) : : "memory");
    TS(0, 13);
    if (w == 0) {
      const uint32_t xr = O_XC + (uint32_t)((1021 - 4 * g0) * 72 + lane) * 16u;
      ST16(xr, ones);
      ST16(xr + 1024u, ones);
      ST16(xr + 2048u, ones);
      if (lane < 24) ST16(xr + 3072u, ones);
    }
    if (w == 2 && b < 72 && lane < 8) ST16(O_FB + (uint32_t)(8 * b + lane) * 16u, ones);
    if (isx) {
      rows_xw(lane);
      char* sp = smem + X_RW(w) + 16 * lane;
      *(u32x4*)sp = (u32x4){xv[0], xv[1], xv[2], xv[3]};
      *(u32x4*)(sp + 1024) = (u32x4){xv[4], xv[5], xv[6], xv[7]};
      *(u32x2*)(smem + X_RW(w) + 2048 + 8 * lane) = (u32x2){xv[8], wv[4]};
      if (w == 0) *(u32x4*)(smem + X_WR + 16 * lane) = (u32x4){wv[0], wv[1], wv[2], wv[3]};
    }
    SLEEP(ESLEEP);
    uint32_t tu = latest(gate(ws, O_SU + (uint32_t)(256 * bx + (ALLP ? tid : 64 * w + 4 * i + (bj & 3))) * 128u, T));
    asm volatile("s_mov_b64 exec, 1\n\tds_max_u32 %0, %1\n\ts_mov_b64 exec, -1" : : "v"(ca + 16), "v"(tu) : "memory");
    if (w == 0) sel[43] = T;
    if (isx) {
      const uint32_t xo = u_at(tid, dX);
      char* sm = smem + X_UL + C_U - 16 + 16 * tid;
      const u32x4 ut = {utag(T, 0), utag(T, 1), utag(T, 2), utag(T, 3)};
      __builtin_amdgcn_s_setprio(3);
      until(tu + UD + XD);
      u32x4 uw;
      asm volatile("global_load_dwordx4 %0, %1, %2 sc1" : "=&v"(uw) : "v"(xo), "s"(ws) : "memory");
      asm volatile("s_waitcnt vmcnt(0)" : "+v"(uw));
      uw ^= ut;
      u_again(uw, ut, xo);
      *(u32x4*)(sm + 16) = uw;
      BAR();
      if (w < 3) {
        const u32x4 pw = *(const u32x4*)sm;
        if (w < 2) sums<true>(smem + X_IM + 16 * tid, pw, uw); else sums<false>(smem + X_IM + 16 * tid, pw, uw);
        count_up(ca + 28);
      }
    } else {
      until(tu + UD + WD);
      BAR();
    }
    tu = (uint32_t)__builtin_amdgcn_readfirstlane(cnt[4]);
    TS(0, 3);
    __builtin_amdgcn_s_setprio(DPRIO);
    if (ffn) {
      ts1 = (tu + UD + VD2 - TSTOP) | 1u;
      gu_dma(0, N0, IC<40>{});
      gu_dma(N0, NA, IC<WDEPTH>{});
      asm volatile("s_waitcnt vmcnt(%0)" : : "n"(NA - 9) : "memory");
      gsync0(ca + 4, 4);
      gu_read(0);
      asm volatile("s_waitcnt lgkmcnt(0)" ::: "memory");
      count_up(ca + 36);
      TS(0, 4);
      gu_dma(NA, 18, IC<WDEPTH2>{});
      gwait(ca + 36, 4);
      down_dma(0, KN, lane);
      asm volatile("s_waitcnt vmcnt(%0)" : : "n"(3 * KN) : "memory");
      count_up(ca + 4);
      if (STP && w == 1)
        asm volatile("global_load_dword v126, %0, %1 sc1" : : "v"(O_SC + 1024u * (fb >> 4) + 128u * (lane & 7)), "s"(ws) : "memory", "v126");
      TS(0, 7);
    } else {
      if (!isx && C3) {
        const int n = 6 * min(tid, 191) + bj - 18, r = n >> 2;
        const char* wb = (const char*)((n & 1) ? wu : wg) + bx * 1152;
        const int c0 = (3 - (int)((uintptr_t)wb >> 7)) & 7;
        wb += (uint32_t)((r + 144 * ((r >= 144) + ((n >> 1) & 1))) * (F_ * 2) + 128 * c0);
        TOUCH(wb);
        if (c0 == 0) TOUCH(wb + 1024);
      }
      auto d_line = [&](int k) {
        const int n = min((k * 14 + bj - 18) * 64 + lane, 5183), q = n / 1296, r = n - 1296 * q;
        return (const char*)wd + (uint32_t)(bx * 5184 + (r / 18) * 72 + 18 * (HREV ? 3 - q : q) + r % 18) * 128u;
      };
      if (isx && w == 3) v_look(tu, lane);
      int k = w;
      if (w < HW) {
#pragma unroll 1
        for (; k < 6 && (int)((uint32_t)__builtin_amdgcn_s_memrealtime() - (tu + UD + VD2 - HSTOP)) < 0; k += HW) { TOUCH(d_line(k)); DRAIN(); }
      }
      TS(0, 4);
      v_time();
      until(tv + RD2);
      if (w < HW) {
#pragma unroll 1
        for (; k < 6; k += HW) { TOUCH(d_line(k)); if (!HALL) DRAIN(); }
      }
      TS(0, 7);
    }
    v_time();
    tk = tv + KD;
  }
  {
  const int tid = tid2, lane = tid & 63, i = lane & 15, kq = lane >> 4;
  __builtin_amdgcn_s_setprio(3);
  const int PL = ffn ? 6400 : LW + 6656;
  f32x2 xf[9];
  f32x4 za = {0.f, 0.f, 0.f, 0.f}, zb = za;
  uint32_t al, ha, h0a, h1a, so, da;
  auto ffn_terms = [&] {
    al = O_XC + 64u * (i >> 3) + 16u * kq + 128u * w;
    ha = (ROLE == 1) ? (uint32_t)((4 * (kq & 1) + 2 * (kq >> 1) + (i >> 3)) * 80 + 16 * w + 2 * (i & 7)) : 0u;
    h0a = (uint32_t)(min(i, 8) * 80 + 16 * kq); h1a = (uint32_t)(min(i ^ 8, 8) * 80 + 16 * kq);
    so = O_SL + (uint32_t)fb * SP + ((ROLE == 2) ? 16u * tid - 4096u : 8192u + 16u * lane);
    da = lb0 + (uint32_t)((8 * kq + ((lane >> 2) & 3)) * 1152 + 32 * ((8 * kq + ((lane >> 2) & 3) + 2) >> 2) + 8 * (lane & 3) + ((ROLE == 2) ? 128 * wl : 1024));
    if (ROLE == 2) asm volatile("" : "+v"(h0a), "+v"(h1a), "+v"(so), "+v"(da)); else asm volatile("" : "+v"(ha), "+v"(al), "+v"(da));
  };
  if (ROLE == 1) ffn_terms();
  uint32_t hv[9] = {}, nv[9] = {}, keya = 0u, keyb = 0u;
  int rka = 0, rkb = 0;
  auto put_row = [&](const uint32_t* r, int j, const void* base, uint32_t ro) {
    const u32x4 tl = {r[8], (uint32_t)DPP_I((int)r[8], 0x55, 0xf), (uint32_t)DPP_I((int)r[8], 0xaa, 0xf), (uint32_t)DPP_I((int)r[8], 0xff, 0xf)};
    asm volatile("global_store_dwordx4 %0, %1, %2 sc1\n\ts_nop 1" : : "v"(ro + 16u * lane), "v"((u32x4){r[4 * j], r[4 * j + 1], r[4 * j + 2], r[4 * j + 3]}), "s"(base) : "memory");
    if ((lane & 35) == 32 * j) asm volatile("global_store_dwordx4 %0, %1, %2 offset:1024 sc1\n\ts_nop 1" : : "v"(ro + 4u * (lane & 31)), "v"(tl), "s"(base) : "memory");
  };
  if (ln2) {
    const int tl = tid - 256, d1 = 64 * (tl >> 5) + (tl & 31), d3 = 64 * ((tl >> 4) & 7) + 32 + (tl & 15), d2 = (tl < 128) ? d3 + 16 : min(384 + tl, 575);
    uint32_t vo = v_at(d1, g0), vo2 = v_at(d2, g0), vo3 = v_at(d3, g0);
    u32x4 ut = {utag(T, 0), utag(T, 1), utag(T, 2), utag(T, 3)};
    u32x4 va, vc = ut, vb = ut;
    asm volatile("" : "+v"(vo), "+v"(vo2), "+v"(vo3), "+v"(ut), "+v"(vc), "+v"(vb));
    TS(0, 7);
    until(tv);
    TS(0, 8);
    asm volatile("global_load_dwordx4 %0, %2, %4 sc1\n\ts_cmp_lt_u32 %5, 2\n\ts_cbranch_scc1 1f\n\tglobal_load_dwordx4 %1, %3, %4 sc1\n1:"
                 : "=&v"(va), "+v"(vc) : "v"(vo), "v"(vo3), "s"(ws), "s"(wl) : "memory", "scc");
    until(tv + V2D);
    asm volatile("s_cmp_eq_u32 %3, 3\n\ts_cbranch_scc1 1f\n\tglobal_load_dwordx4 %0, %1, %2 sc1\n1:" : "+v"(vb) : "v"(vo2), "s"(ws), "s"(wl) : "memory", "scc");
    __builtin_amdgcn_sched_barrier(0);
    if (isx) {
      const u32x4 ra = *(const u32x4*)(smem + X_RW(wl) + 16 * lane), rb = *(const u32x4*)(smem + X_RW(wl) + 1024 + 16 * lane), rw = *(const u32x4*)(smem + X_WR + 16 * lane);
      const u32x2 rc = *(const u32x2*)(smem + X_RW(wl) + 2048 + 8 * lane);
#pragma unroll
      for (int k = 0; k < 4; ++k) { xv[k] = ra[k]; xv[4 + k] = rb[k]; wv[k] = rw[k]; }
      xv[8] = rc[0]; wv[4] = rc[1];
    }
    uint32_t pa = (uint32_t)(PL + 9216 * wl + 32 * lane), pb = (uint32_t)(PL + 9216 * wl + 2048 + 8 * (lane & 31)), ps = (lane < 32) ? 0u : 16u;
    uint32_t kid = (uint32_t)(((255 - g2) << 5) | (7 - 2 * wl - (lane >> 5)));
    uint32_t px = (uint32_t)(PL + 4 * d1), py = (uint32_t)(PL + 4 * d2), pz = (uint32_t)(PL + 4 * d3);
#pragma unroll
    for (int k = 0; k < 9; ++k) xf[k] = (f32x2){bflo(xv[k]), bfhi(xv[k])};
    asm volatile("" : "+v"(xf[0]), "+v"(xf[1]), "+v"(xf[2]), "+v"(xf[3]), "+v"(xf[4]), "+v"(xf[5]), "+v"(xf[6]), "+v"(xf[7]), "+v"(xf[8]));
    asm volatile("" : "+v"(za), "+v"(zb), "+v"(pa), "+v"(pb), "+v"(ps), "+v"(kid), "+v"(px), "+v"(py), "+v"(pz));
    float sv[10], ws_ = dot2(wv[4], 0x3f803f80u, 0.f) * 0.5f;
#pragma unroll
    for (int k = 0; k < 4; ++k) ws_ = dot2(wv[k], 0x3f803f80u, ws_);
    ws_ = wave_sum(ws_);
#pragma unroll
    for (int k = 0; k < 10; ++k) sv[k] = 16.f;
    uint32_t ka_ = lb0 + LM + 4u * (128 + 48 + 2 * wl + (lane >> 5)), kb_ = lb0 + LM + 4u * (128 + 48);
    uint32_t ko_ = O_KEY + (uint32_t)(256 * (lane & 7) + 8 * (g2 & 31) + (g2 >> 5)) * 16u;
    asm volatile("" : "+v"(ws_), "+v"(sv[0]), "+v"(sv[1]), "+v"(sv[2]), "+v"(sv[3]), "+v"(sv[4]), "+v"(sv[5]), "+v"(sv[6]), "+v"(sv[7]), "+v"(sv[8]), "+v"(sv[9]),
                 "+v"(ka_), "+v"(kb_), "+v"(ko_));
    if (ffn) gwait(ca + 36, 4);
    asm volatile("s_waitcnt vmcnt(1)\n\ts_cmp_eq_u32 %2, 3\n\ts_cbranch_scc0 1f\n\ts_waitcnt vmcnt(0)\n1:" : "+v"(va), "+v"(vc) : "s"(wl) : "memory", "scc");
    auto turn1 = [&] {
#pragma unroll
      for (int e = 0; e < 4; ++e) *(uint32_t*)(smem + px + 9216 * e) = va[e];
      if (wl >= 2) {
#pragma unroll
        for (int e = 0; e < 4; ++e) *(uint32_t*)(smem + pz + 9216 * e) = vc[e];
      }
    };
    auto turn = [&] {
      if (wl < 3) {
#pragma unroll
        for (int e = 0; e < 4; ++e) *(uint32_t*)(smem + py + 9216 * e) = vb[e];
      }
    };
    va ^= ut; vc ^= ut;
    turn1();
    asm volatile("s_waitcnt vmcnt(0)" : "+v"(vb) : : "memory");
    vb ^= ut;
    turn();
    const u32x4 vs = va | vb | vc;
    if (__builtin_expect(__any(((vs[0] | vs[1] | vs[2] | vs[3]) & UMASK) != 0u), 0)) {
#pragma unroll 1
      for (int spin = 0; spin < 4; ++spin) {
        const bool na = ((va[0] | va[1] | va[2] | va[3]) & UMASK) != 0u, nb = ((vb[0] | vb[1] | vb[2] | vb[3]) & UMASK) != 0u, nc = ((vc[0] | vc[1] | vc[2] | vc[3]) & UMASK) != 0u;
        if (!__any(na || nb || nc)) break;
        TSC(16);
        if (na) LD16(va, vo);
        if (nb) LD16(vb, vo2);
        if (nc) LD16(vc, vo3);
        asm volatile("s_waitcnt vmcnt(0)" : "+v"(va), "+v"(vb), "+v"(vc) : : "memory");
        if (na) va ^= ut;
        if (nb) vb ^= ut;
        if (nc) vc ^= ut;
      }
      auto again = [&](u32x4& v, uint32_t o, int d) {
        const int J = g0 >> 5;
        const uint32_t dn = O_DN + 16u * (uint32_t)((d < 512) ? 4 * (d >> 1) + ((0x01013232 >> (4 * J)) & 3) : 4 * (d - 256) + (J >> 1));
        bool pend = ((v[0] | v[1] | v[2] | v[3]) & UMASK) != 0u;
#pragma unroll 1
        for (int spin = 0; spin < SPIN_CAP && __any(pend); ++spin) {
          TSC(16);
          SLEEP(PSLEEP);
          if (pend) {
            uint32_t fin = ~T;
            if ((v[0] & v[1] & v[2] & v[3] & LMASK) == LMASK)
              asm volatile("global_load_dword %0, %1, %2 sc1\n\ts_waitcnt vmcnt(0)" : "=&v"(fin) : "v"(dn), "s"(ws) : "memory");
            LD16W(v, o);
            v ^= ut;
            pend = ((v[0] | v[1] | v[2] | v[3]) & UMASK) != 0u;
            if (pend && fin == T) {
              u32x2 sc;
              for (int k = 0; k < SPIN_CAP; ++k) {
                asm volatile("global_load_dwordx2 %0, %1, %2 sc1\n\ts_waitcnt vmcnt(0)" : "=&v"(sc) : "v"(O_SC + 16u * d), "s"(ws) : "memory");
                if (sc[0] == T) break;
              }
              *(u16*)(smem + L_SC + 2 * d) = (u16)(sc[1] >> 16);
              cnt[10] = 1u;
              v = (v ^ ut) & ~LMASK;
              pend = false;
            }
          }
        }
      };
      again(va, vo, d1); again(vc, vo3, d3); again(vb, vo2, d2);
      turn1();
      turn();
    }
    u32x4 a0, a1;
    u32x2 a2, cf;
    {
      uint32_t cc, n = SPIN_CAP;
      asm volatile("s_mov_b64 exec, 1\n\tv_mov_b32 v90, 1\n\tds_add_u32 %6, v90\n\ts_mov_b64 exec, -1\n"
                   "1:\n\tds_read2_b32 %3, %6 offset1:10\n\tds_read_b128 %0, %7\n\tds_read_b128 %1, %7 offset:16\n\tds_read_b64 %2, %8\n\t"
                   "s_sub_u32 %5, %5, 1\n\ts_cbranch_scc1 2f\n\ts_waitcnt lgkmcnt(3)\n\tv_readfirstlane_b32 %4, v90\n\t"
                   "s_cmp_lt_u32 %4, 4\n\ts_cbranch_scc1 1b\n2:\n\ts_waitcnt lgkmcnt(0)"
                   : "=&v"(a0), "=&v"(a1), "=&v"(a2), "=&{v[90:91]}"(cf), "=&s"(cc), "+s"(n) : "v"(ca), "v"(lb0 + pa), "v"(lb0 + pb) : "memory", "scc");
    }
    TS(0, 9);
    if (__builtin_expect(__builtin_amdgcn_readfirstlane(cf[1]) != 0, 0)) {
      const u32x4 q = *(const u32x4*)(smem + L_SC + 16 * lane);
      const uint32_t r = *(const uint32_t*)(smem + L_SC + 1024 + 4 * (lane & 31));
#pragma unroll
      for (int k = 0; k < 4; ++k) { sv[2 * k] = bflo(q[k]); sv[2 * k + 1] = bfhi(q[k]); }
      sv[8] = bflo(r); sv[9] = bfhi(r);
    }
    const uint32_t av[10] = {a0[0], a0[1], a0[2], a0[3], a1[0], a1[1], a1[2], a1[3], a2[0] >> ps, a2[1] >> ps};
    f32x2 h[9];
#pragma unroll
    for (int k = 0; k < 10; ++k) {
      asm("v_fma_mix_f32 %0, %1, %2, %3 op_sel_hi:[1,0,0]" : "=v"(h[(k < 8) ? k >> 1 : 8][k & 1]) : "v"(av[k]), "v"(sv[k]), "v"(xf[(k < 8) ? k >> 1 : 8][k & 1]));
      if (k < 8) asm("v_fma_mix_f32 %0, %1, %2, %3 op_sel:[1,0,0] op_sel_hi:[1,0,0]" : "=v"(h[4 + (k >> 1)][k & 1]) : "v"(av[k]), "v"(sv[k]), "v"(xf[4 + (k >> 1)][k & 1]));
    }
#pragma unroll
    for (int k = 0; k < 9; ++k) hv[k] = pack2(h[k][0], h[k][1]);
    const float da = dot2(hv[2], wv[2], dot2(hv[0], wv[0], zb[0])) + dot2(hv[3], wv[3], dot2(hv[1], wv[1], zb[1]));
    const float db = dot2(hv[6], wv[2], dot2(hv[4], wv[0], zb[2])) + dot2(hv[7], wv[3], dot2(hv[5], wv[1], zb[3]));
    float dt = dot2(hv[8], wv[4], fold32(da, db));
    f32x2 rc, nc;
    ln_stats<true>(hv, lane, 1.f, xf, za, rc, nc, dt);
    dt = (lane < 32) ? dt * rc[0] + ws_ * nc[0] : dt * rc[1] + ws_ * nc[1];
    const uint32_t bb = pack2(dt, dt) & 0xffff0000u;
    const uint32_t kk = (bb ^ (((uint32_t)((int)bb >> 31) & 0x7fff0000u) | 0x80000000u)) | kid;
    u32x4 ka, kb;
    uint32_t old;
    asm volatile("s_mov_b64 exec, %5\n\tds_write_b32 %3, %4\n\ts_mov_b64 exec, 1\n\tv_mov_b32 %2, 1\n\tds_add_rtn_u32 %2, %7, %2\n\ts_mov_b64 exec, -1\n\t"
                 "ds_read_b128 %0, %6\n\tds_read_b128 %1, %6 offset:16\n\ts_waitcnt lgkmcnt(0)"
                 : "=&v"(ka), "=&v"(kb), "=&v"(old) : "v"(ka_), "v"(kk), "s"(0x8000000080000000ull), "v"(kb_), "v"(ca) : "memory");
    if (__builtin_amdgcn_readfirstlane(old) == 7) {
      uint32_t m1 = max(max(ka[0], ka[1]), ka[2]), m2 = med3u(ka[0], ka[1], ka[2]), m3 = min(min(ka[0], ka[1]), ka[2]);
#pragma unroll
      for (int e = 0; e < 5; ++e) {
        const uint32_t kx = e ? kb[e - 1] : ka[3];
        m3 = med3u(m2, m3, kx); m2 = med3u(m1, m2, kx); m1 = max(m1, kx);
      }
      if (lane < 8) ST16(ko_, ((u32x4){m1 | 24u, m2 | 16u, m3 | 8u, T & ~1u}));
    }
    TS(0, 10);
    keya = (uint32_t)__builtin_amdgcn_readlane(kk, 31); keyb = (uint32_t)__builtin_amdgcn_readlane(kk, 63);
    ln_apply<false>(xf, lane, rc, nc, nv);
    gwait(ca, 8);
    if (wl == 1) {
      const u32x4 kv = *(const u32x4*)&kl[4 * (lane & 1)];
      if (lane < 2) ST16(O_KF + (uint32_t)(2 * g2 + lane) * 16u, ((u32x4){__builtin_amdgcn_perm(kv[1], kv[0], 0x07060302u), T, __builtin_amdgcn_perm(kv[3], kv[2], 0x07060302u), T}));
      if (ROLE == 2 && b == 0 && lane == 0) ST4(O_EP, T * 0x0E8B2F51u);
    }
    const uint32_t ko = kl[lane & 7];
    rka = FORCEFB + __popcll(__ballot(ko > keya) & 0xffull); rkb = FORCEFB + __popcll(__ballot(ko > keyb) & 0xffull);
#pragma unroll
    for (int k = 0; k < 9; ++k) {
      const f32x2 o = xf[k] + (f32x2){bflo(nv[k]), bfhi(nv[k])};
      hv[k] = pack2(o[0], o[1]);
      nv[k] = min(nv[k], 0xfffffffeu);
    }
    if (rka < 3) put_row(nv, 0, ws, O_XC + (uint32_t)(1023 - 4 * g2 - rka) * 1152u);
    if (rkb < 3) put_row(nv, 1, ws, O_XC + (uint32_t)(1023 - 4 * g2 - rkb) * 1152u);
    if (!LPOS && ffn) down_dma(KN, 4, lane);
    ffn_terms();
  }
  uint32_t k3rd = 0u, k4th = 0u;
  uint32_t q0 = lb0 + LM + 4u * lane, q1 = lb0 + LM + 32u * w, q2 = q1 + 4u * (lane & 7), q3 = lb0 + LM + 512u + 4u * (lane & 7), q4 = lb0 + LM + (uint32_t)(tid & ~7);
  uint32_t r8 = C_, sb = lb0 + LM + 512u;
  asm volatile("" : "+v"(q0), "+v"(q1), "+v"(q2), "+v"(q3), "+v"(q4), "+v"(r8), "+v"(sb));
  if constexpr (ROLE == 1) {
    u32x4 key;
    const uint32_t kof = O_KEY + (uint32_t)(256 * bx + tid) * 16u;
    until(tk);
    if (!ffn) SLEEP(NSLEEP);
    LD16(key, kof);
    if (ffn) {
      gwait0(ca + 4, 8);
      gu_read(1);
    }
    asm volatile("s_waitcnt vmcnt(0)" : "+v"(key) : : "memory");
    if (__builtin_expect(__any(key[3] != (T & ~1u)), 0)) {
#pragma unroll 1
      for (int spin = 0; spin < SPIN_CAP; ++spin) {
        const bool need = key[3] != (T & ~1u);
        if (!__any(need)) break;
        TSC(15);
        SLEEP(KBACK);
        if (need) LD16W(key, kof);
      }
    }
    TS(0, 11);
    const uint32_t m1 = oct_maxu(key[0]);
    const bool hit = key[0] == m1;
    const uint32_t k0 = hit ? key[1] : key[0], k1 = hit ? key[2] : key[1];
    const uint32_t m2 = oct_maxu(k0);
    *LP(u32x2, q4) = (u32x2){m1, m2};
    k3rd = (k0 == m2) ? k1 : k0;
    k4th = (k0 == m2) ? (hit ? 0u : key[2]) : k1;
    if (RPF == 1 && !ffn && w == (bj & 3)) {
      const uint32_t r1 = O_XC + ((m1 >> 3) & 0x3ffu) * 1152u + 128u * (lane & 7), r2 = O_XC + ((m2 >> 3) & 0x3ffu) * 1152u + 128u * (lane & 7);
      asm volatile("global_load_dword v126, %0, %3 sc1\n\tglobal_load_dword v126, %1, %3 sc1\n\tglobal_load_dword v126, %2, %3 offset:1024 sc1"
                   : : "v"(r1), "v"(r2), "v"(((lane & 1) ? r1 : r2) - 128u * (lane & 7)), "s"(ws) : "memory", "v126");
    }
  }
  auto pick = [&](auto co, auto so_) {
    BAR();
    const uint32_t c = *LP(uint32_t, q0 + co.value), own = *LP(uint32_t, q2 + co.value);
    const u32x4 la = *LP(u32x4, q1 + co.value), lb = *LP(u32x4, q1 + co.value + 16);
    uint32_t r = r8;
    asm("v_cmp_gt_u32 s[40:41], %1, %2\n\tv_cmp_gt_u32 s[42:43], %1, %3\n\tv_cmp_gt_u32 s[44:45], %1, %4\n\tv_cmp_gt_u32 s[46:47], %1, %5\n\t"
        "v_cmp_gt_u32 s[48:49], %1, %6\n\tv_cmp_gt_u32 s[50:51], %1, %7\n\tv_cmp_gt_u32 s[52:53], %1, %8\n\tv_cmp_gt_u32 s[54:55], %1, %9\n\t"
        "s_bcnt1_i32_b64 s40, s[40:41]\n\ts_bcnt1_i32_b64 s42, s[42:43]\n\tv_writelane_b32 %0, s40, 0\n\ts_bcnt1_i32_b64 s44, s[44:45]\n\t"
        "v_writelane_b32 %0, s42, 1\n\ts_bcnt1_i32_b64 s46, s[46:47]\n\tv_writelane_b32 %0, s44, 2\n\ts_bcnt1_i32_b64 s48, s[48:49]\n\t"
        "v_writelane_b32 %0, s46, 3\n\ts_bcnt1_i32_b64 s50, s[50:51]\n\tv_writelane_b32 %0, s48, 4\n\ts_bcnt1_i32_b64 s52, s[52:53]\n\t"
        "v_writelane_b32 %0, s50, 5\n\ts_bcnt1_i32_b64 s54, s[54:55]\n\tv_writelane_b32 %0, s52, 6\n\tv_writelane_b32 %0, s54, 7"
        : "+v"(r) : "v"(c), "v"(la[0]), "v"(la[1]), "v"(la[2]), "v"(la[3]), "v"(lb[0]), "v"(lb[1]), "v"(lb[2]), "v"(lb[3])
        : "scc", "s40", "s41", "s42", "s43", "s44", "s45", "s46", "s47", "s48", "s49", "s50", "s51", "s52", "s53", "s54", "s55");
    *LP(uint32_t, sb + 4u * min(r, (uint32_t)C_) + so_.value) = own;
    BAR();
  };
  auto rows = [&](uint32_t ao, auto&& meanwhile) {
    u32x4 aa, ab, ac = __builtin_bit_cast(u32x4, zb);
    asm volatile("global_load_dwordx4 %0, %3, %4 sc1\n\tglobal_load_dwordx4 %1, %3, %4 offset:512 sc1\n\t"
                 "s_cmp_lg_u32 %5, 0\n\ts_cbranch_scc1 1f\n\tglobal_load_dwordx4 %2, %3, %4 offset:1024 sc1\n1:"
                 : "=&v"(aa), "=&v"(ab), "+v"(ac) : "v"(ao), "s"(ws), "s"(w) : "memory", "scc");
    __builtin_amdgcn_sched_barrier(0);
    meanwhile();
    asm volatile("s_waitcnt vmcnt(0)" : "+v"(aa), "+v"(ab), "+v"(ac) : : "memory");
    if (__builtin_expect(__any(max(max(max4(aa), max4(ab)), max4(ac)) == 0xffffffffu), 0)) {
#pragma unroll 1
      for (int spin = 0; spin < SPIN_CAP; ++spin) {
        const bool na = max4(aa) == 0xffffffffu, nb = max4(ab) == 0xffffffffu, nc = max4(ac) == 0xffffffffu;
        if (!__any(na || nb || nc)) break;
        TSC(14);
        SLEEP(PSLEEP);
        if (na) LD16W(aa, ao);
        if (nb) LD16W(ab, ao + 512u);
        if (nc) LD16W(ac, ao + 1024u);
      }
    }
    *(u32x4*)(smem + LA + 16 * tid) = aa;
    *(u32x4*)(smem + LA + 4096 + 16 * tid) = ab;
    if (w == 0) *(u32x4*)(smem + LA + 8192 + 16 * tid) = ac;
  };
  auto rows_dma = [&](uint32_t ao) {
    uint32_t dl = lb0 + LA + 1024u * w, keep;
    asm volatile("s_mov_b32 %0, m0\n\ts_mov_b32 m0, %1\n\ts_add_u32 %1, %1, 4096 - 512\n\tglobal_load_lds_dwordx4 %2, %3 sc1\n\ts_mov_b32 m0, %1\n\ts_add_u32 %1, %1, 4096 - 512\n\t"
                 "global_load_lds_dwordx4 %2, %3 offset:512 sc1\n\ts_cmp_lg_u32 %4, 0\n\ts_cbranch_scc1 1f\n\ts_mov_b32 m0, %1\n\ts_nop 0\n\t"
                 "global_load_lds_dwordx4 %2, %3 offset:1024 sc1\n1:\n\ts_mov_b32 m0, %0" : "=&s"(keep), "+s"(dl) : "v"(ao), "s"(ws), "s"(w) : "memory", "scc");
  };
  auto get_bd = [&](int t0, int t1) {
#pragma unroll
    for (int t = t0; t < t1; ++t) {
      const u32x2 h0 = trd(da + 512 * (t >> 2) + 32 * (t & 3)), h1 = trd(da + 512 * (t >> 2) + 32 * (t & 3) + 4608 + 32);
      bw[t] = (u32x4){h0[0], h0[1], h1[0], h1[1]};
    }
#pragma unroll
    for (int t = t0; t < t1; ++t) asm volatile("" : "+v"(bw[t]));
  };
  pick(IC<0>{}, IC<0>{});
  uint32_t mykey = *LP(uint32_t, q3), ro = ((mykey >> 3) & 0x3ffu) * 1152u;
  TS(0, 12);
  auto check = [&] {
    cnt[(k3rd > (uint32_t)__builtin_amdgcn_readlane(mykey, 7)) ? 5 : 6] = 1u;
    const uint32_t m3 = oct_maxu(k3rd);
    if (k3rd == m3) k3rd = k4th;
    if ((lane & 7) == 0) cand[72 + (tid >> 3)] = m3;
    if (w == 0 && lane < 32) cand[(lane < 8) ? 64 + lane : 96 + lane] = (lane < 8) ? mykey : 0u;
  };
  if constexpr (ROLE == 1) {
    if (ffn && !FORCEFB) rows_dma(ro + al);
    check();
    DRAIN();
  } else if (ffn) {
    if (LPOS) { SLEEP(LSLEEP); down_dma(KN, 4, lane); } else { DRAIN(); gsync0(ca, 12); get_bd(0, 8); }
  }
  BAR();
  TS(0, 5);
  auto put_out = [&](int j, uint32_t k0, int rk) {
    const uint64_t mine = __ballot(((mykey ^ k0) & ~24u) == 0u) & 0xffull;
    if (mine == 0ull) put_row(hv, j, out, (uint32_t)((8 * g2 + 2 * wl + j) * (D_ * 2)));
    else if (rk >= 3) put_row(nv, j, ws, O_FB + 1152u * (uint32_t)__builtin_ctzll(mine));
  };
  auto warm_out = [&] {
    const uint32_t k0 = (lane < 9) ? keya : keyb;
    bool hit = false;
#pragma unroll
    for (int e = 0; e < 8; ++e) hit |= (((uint32_t)__builtin_amdgcn_readlane(mykey, e) ^ k0) & ~24u) == 0u;
    if (OTP && lane < 18 && hit) asm volatile("global_load_dword v126, %0, %1" : : "v"((uint32_t)((8 * g2 + 2 * wl) * (D_ * 2) + 128 * lane)), "s"(out) : "memory", "v126");
  };
  auto rest_out = [&] { put_out(0, keya, rka); put_out(1, keyb, rkb); };
  f32x4 lo, hi;
  auto gate_up = [&] {
    u32x4 av[5];
    const uint32_t aa = lb0 + LA + 16u * lane;
#pragma unroll
    for (int j = 0; j < 5; ++j) asm volatile("ds_read_b128 %0, %1 offset:%2" : "=v"(av[j]) : "v"(aa), "n"(1024 * j) : "memory");
    lo = (f32x4){0.f, 0.f, 0.f, 0.f}; hi = lo;
#pragma unroll
    for (int j = 0; j < 9; ++j) {
      if (j < 5) asm volatile("s_waitcnt lgkmcnt(4)" : "+v"(av[j % 5]));
      else if (j == 5) asm volatile("s_waitcnt lgkmcnt(2)" : "+v"(av[0]), "+v"(av[1]));
      else if (j == 7) asm volatile("s_waitcnt lgkmcnt(0)" : "+v"(av[2]), "+v"(av[3]));
      lo = MMB(av[j % 5], bw[2 * j], lo);
      hi = MMB(av[j % 5], bw[2 * j + 1], hi);
      __builtin_amdgcn_sched_barrier(0);
      if (j < 4) asm volatile("ds_read_b128 %0, %1 offset:%2" : "=v"(av[j]) : "v"(aa), "n"(1024 * (j + 5)) : "memory");
    }
  };
  uint32_t fl;
  asm volatile("ds_read_b32 %0, %1" : "=v"(fl) : "v"(ca + 20) : "memory");
  if (ROLE == 1) gate_up();
  asm volatile("s_waitcnt lgkmcnt(0)" : "+v"(fl));
  const bool slow = FORCESLOW || FORCEFB || __builtin_amdgcn_readfirstlane(fl) != 0;
  if (__builtin_expect(slow, 0)) {
    TSC(17);
    pick(IC<256>{}, IC<64>{});
    mykey = sel[16 + (lane & 7)];
    ro = ((mykey >> 3) & 0x3ffu) * 1152u;
    const uint32_t k8 = (uint32_t)__builtin_amdgcn_readlane(mykey, 7);
    if (ROLE == 1 && k3rd > k8) cnt[11] = 1u;
    if (w == 0 && __any((mykey & 24u) == 8u)) {
      const uint32_t pk = sel[16 + (lane >> 3)], gi = (pk >> 5) & 0xffu;
      u32x4 kf;
#pragma unroll 1
      for (int spin = 0; spin < SPIN_CAP; ++spin) {
        LD16W(kf, O_KF + (2u * (255u - gi) + ((lane >> 2) & 1u)) * 16u);
        if (!__any(kf[1] != T || kf[3] != T)) break;
        SLEEP(KBACK);
      }
      const uint32_t wd_ = (lane & 2) ? kf[2] : kf[0], kt = ((lane & 1) ? wd_ & 0xffff0000u : wd_ << 16) | (gi << 5) | (7u - (lane & 7));
      const uint64_t up = __ballot((pk & 24u) == 8u && kt >= (k8 & ~24u));
      if (__popcll((up >> (lane & 56)) & 0xffull) > 3) cnt[11] = 1u;
    }
    BAR();
    if (FORCESLOW > 1 || FORCEFB || __builtin_amdgcn_readfirstlane(cnt[11]) != 0) {
    u32x4 key;
#pragma unroll 1
    for (int spin = 0; spin < SPIN_CAP; ++spin) {
      LD16W(key, O_KF + 16u * tid);
      if (!__any(key[1] != T || key[3] != T)) break;
      SLEEP(KBACK);
    }
    const uint32_t ib = (uint32_t)(((255 - (tid >> 1)) << 5) | (7 - 4 * (tid & 1)));
    uint32_t k0 = (key[0] << 16) | ib, k1 = (key[0] & 0xffff0000u) | (ib - 1), k2 = (key[2] << 16) | (ib - 2), k3 = (key[2] & 0xffff0000u) | (ib - 3);
    CEX(k0, k1) CEX(k2, k3) CEX(k0, k2) CEX(k1, k3) CEX(k1, k2)
#pragma unroll 1
    for (int c = 0; c < C_; ++c) {
      const uint32_t mx = wave_maxu(k0);
      const bool hit = (k0 == mx);
      k0 = hit ? k1 : k0; k1 = hit ? k2 : k1; k2 = hit ? k3 : k2; k3 = hit ? 0u : k3;
      if (lane == 0) cand[64 + 8 * w + c] = mx;
    }
    pick(IC<256>{}, IC<128>{});
    mykey = sel[32 + (lane & 7)];
    const uint32_t okey = sel[32 + (lane >> 3)];
    const uint64_t same = __ballot(((mykey ^ okey) & 0x1fe0u) == 0u && okey > mykey);
    const int sl = FORCEFB + __popcll((same >> (lane & 7)) & 0x0101010101010101ull);
    ro = (sl < 3) ? (((mykey >> 3) & 0x3ffu) + 3 - sl) * 1152u : O_FB - O_XC + 1152u * (lane & 7);
    }
    if (ln2) rest_out();
    if (ffn) {
      if (ROLE == 1) rows(ro + al, [] {});
      BAR();
      if (ROLE == 1) gate_up();
    }
  }
  if (OPOS == 0 && ROLE == 2 && ffn && !slow) { SLEEP(OD); rest_out(); }
  if (!ffn) {
    if (ln2) warm_out();
    if (ln2 && !slow) { SLEEP(OD2); rest_out(); }
    TSE();
    return;
  }
  uint32_t fx = 0, fv = 0, fn = 0, fo = 0, sa[9];
  float rs, hx, xs;
  u32x4 se;
  if constexpr (ROLE == 1) {
    f32x4 lh = (lane < 32) ? lo : hi;
    float fa = fold32(lh[0], lh[2]), fb2 = fold32(lh[1], lh[3]);
    if (__builtin_expect(__any(__builtin_isunordered(fa, fb2)) && !slow, 0)) {
      gsync0(ca + 4, 12);
      rows(ro + al, [] {});
      gsync0(ca + 4, 16);
      gate_up();
      lh = (lane < 32) ? lo : hi;
      fa = fold32(lh[0], lh[2]); fb2 = fold32(lh[1], lh[3]);
    }
    const float g = __int_as_float(__builtin_amdgcn_update_dpp(__float_as_int(fa), __float_as_int(fb2), 0x128, 0xf, 0xc, false));
    const float u = __int_as_float(__builtin_amdgcn_update_dpp(__float_as_int(fb2), __float_as_int(fa), 0x128, 0xf, 0x3, false));
    const uint32_t gu = pack2(g, u);
    const float gp = bflo(gu);
    const float sg = rbf(gp * __builtin_amdgcn_rcpf(1.f + __expf(-gp)));
    *(u16*)((char*)hm + ha) = f2bf(sg * bfhi(gu));
  } else if (LPOS) {
    DRAIN();
    gsync0(ca, 12);
    get_bd(0, 8);
  }
  BAR();
  TS(0, 6);
  if (ROLE == 2 || w == 2) {
    const u32x4 a0 = *(const u32x4*)((char*)hm + h0a), a1 = *(const u32x4*)((char*)hm + h1a);
    if (ROLE == 1) get_bd(0, 4);
    const f32x4 z = {0.f, 0.f, 0.f, 0.f};
    f32x4 o[4];
#pragma unroll
    for (int t = 0; t < 2 * ROLE; ++t) o[t] = MMB(a1, bw[2 * t + 1], MMB(a0, bw[2 * t], z));
#pragma unroll
    for (int t = 0; t < ROLE; ++t) {
      u32x4 sv;
#pragma unroll
      for (int r = 0; r < 4; ++r) sv[r] = min(pack2(o[2 * t][r], o[2 * t + 1][r]), 0xfffffffeu);
      ST16(so + 4096u * t, sv);
    }
    TS(0, 13);
  }
  if (OPOS == 1 && ROLE == 2) {
    warm_out();
    SLEEP(OSLEEP);
    if (!slow) rest_out();
  }
  if (ROLE == 1 && w == 1) {
    const int q = (fb & 15) >> 2, ts = 4 * (q & 1) + ((i & 7) >> 1);
    const int dd = 64 * (fb >> 4) + 16 * (2 * (i & 1) + (q >> 1)) + 4 * (fb & 3) + 2 * (kq & 1) + (kq >> 1);
    const uint32_t fkey = (uint32_t)__builtin_amdgcn_ds_bpermute(4 * ts, (int)mykey), fr = (uint32_t)__builtin_amdgcn_ds_bpermute(4 * ts, (int)ro);
    const int ft = 2047 - (int)(((fkey >> 2) & 0x7f8u) | (fkey & 7u));
    fo = (uint32_t)(ft * D_ + dd) * 2u;
    u32x2 fs;
    asm volatile("global_load_ushort %0, %4, %8\n\tglobal_load_ushort %1, %5, %9 sc1\n\tglobal_load_ushort %2, %6, %9 sc1\n\tglobal_load_dwordx2 %3, %7, %9 sc1"
                 : "=&v"(fx), "=&v"(fv), "=&v"(fn), "=&v"(fs) : "v"(fo), "v"(v_at(dd, ft >> 3) + 2u * (ft & 7)), "v"(O_XC + fr + 2u * dd), "v"(O_SC + 16u * dd), "s"(x), "s"(ws) : "memory");
    uint32_t t16 = (((T >> (3 * ((ft >> 1) & 3))) & LMASK) | (((T >> (12 + ((ft >> 1) & 3))) & 0x00010001u) << 14)) >> (16 * (ft & 1)) & 0xffffu;
    const uint32_t o = fkey >> 16;
    const float rs0 = bf2f((u16)((o & 0x8000u) ? (o & 0x7fffu) : (~o & 0xffffu)));
    rs = rbf(__builtin_amdgcn_rcpf(1.f + __expf(-rs0)));
    const uint32_t one = (i < 8) ? (0x3f80u << (16 * (i & 1))) : 0u;
    se = (u32x4){(i >> 1) == 0 ? one : 0u, (i >> 1) == 1 ? one : 0u, (i >> 1) == 2 ? one : 0u, (i >> 1) == 3 ? one : 0u};
#pragma unroll
    for (int m = 0; m < 9; ++m) sa[m] = O_SL + (uint32_t)(16 * m + (lane >> 2)) * SP + (uint32_t)(4 * fb + (lane & 3)) * 16u;
    asm volatile("" : "+v"(t16), "+v"(rs), "+v"(se), "+v"(sa[0]), "+v"(sa[1]), "+v"(sa[2]), "+v"(sa[3]), "+v"(sa[4]), "+v"(sa[5]), "+v"(sa[6]), "+v"(sa[7]), "+v"(sa[8]));
    SLEEP(SSLEEP);
    if (FD) until(tk + FD);
    TS(0, 1);
    float sum;
#pragma unroll 1
    for (int spin = 0; spin < SPIN_CAP; ++spin) {
      u32x4 sv[9];
#pragma unroll
      for (int m = 0; m < 9; ++m) LD16(sv[m], sa[m]);
      asm volatile("s_waitcnt vmcnt(9)" : "+v"(fx), "+v"(fv), "+v"(fn), "+v"(fs));
      if (__builtin_expect(__any(fs[0] != T), 0)) {
        for (int k = 0; k < SPIN_CAP && fs[0] != T; ++k)
          asm volatile("global_load_dwordx2 %0, %1, %2 sc1\n\ts_waitcnt vmcnt(0)" : "=&v"(fs) : "v"(O_SC + 16u * dd), "s"(ws) : "memory");
      }
      const uint32_t f1 = fv ^ t16, f2 = (f1 & 7u) ? fv & ~7u : f1;
      hx = rbf(bf2f((u16)fx) + (float)__builtin_bit_cast(_Float16, (u16)f2) * __uint_as_float(fs[1]));
      xs = bf2f((u16)fn);
      asm volatile("" : "+v"(hx), "+v"(xs));
      f32x4 dv = {0.f, 0.f, 0.f, 0.f};
      static_for<0, 9>([&](auto mm) {
        constexpr int m = mm.value;
        asm volatile("s_waitcnt vmcnt(%1)" : "+v"(sv[m]) : "n"(8 - m));
        dv = MMB(sv[m], se, dv);
        __builtin_amdgcn_sched_barrier(0);
      });
      sum = fold16(fold32(dv[0], dv[1]), fold32(dv[2], dv[3]));
      if (__builtin_expect(!__any(sum != sum), 1)) break;
      uint32_t mx = 0u;
#pragma unroll
      for (int m = 0; m < 9; ++m) mx = max(mx, max4(sv[m]));
      if (!__any(mx == 0xffffffffu)) break;
      TSC(18);
      SLEEP(PSLEEP);
    }
    const float o1 = rbf(xs + rbf(rbf(sum) * rs));
    if (i < 8) asm volatile("global_store_short %0, %1, %2 sc1" : : "v"(fo), "v"((uint32_t)f2bf(hx + o1)), "s"(out) : "memory");
    TS(0, 2);
  }
  TSE();
  }
  }
}
#define BROT "192"
#define CPF_A "s_getpc_b64 s[96:97]\n\ts_lshl_b32 s99, s2, 4\n\ts_lshl_b32 s84, s98, 7\n\ts_add_u32 s99, s99, s84\n\tv_and_b32 v112, 1, v0\n\t" \
              "v_lshl_add_u32 v112, v112, 12, s99\n\tv_min_u32 v112, kf_end-kf_all-0x80, v112\n\ts_mov_b64 exec, 3\n\tglobal_load_dword v112, v112, s[96:97]\n\ts_mov_b64 exec, -1\n\t"
extern "C" __global__ __launch_bounds__(512, 4) void kf_all(KARGS) {
  __shared__ __attribute__((aligned(16))) char smem[SMEM];
  asm volatile("s_load_dwordx16 s[64:79], s[0:1], 0x0\n\ts_load_dwordx4 s[80:83], s[0:1], 0x40\n\tv_readfirstlane_b32 s98, v0\n\t"
               "s_add_u32 s84, s2, " BROT "\n\ts_xor_b32 s84, s84, s2\n\ts_bitset0_b32 s84, 8\n\ts_bitset0_b32 s84, 9\n\ts_xor_b32 s2, s2, s84\n\t"
               "v_mov_b32 v109, 0\n\ts_and_b32 s85, s2, 7\n\ts_bitcmp1_b32 s2, 8\n\ts_cbranch_scc1 1f\n\t" CPF_A "s_bitcmp1_b32 s98, 8\n\ts_cbranch_scc1 2f\n\t"
               "s_andn2_b32 s84, s2, 31\n\ts_lshl2_add_u32 s84, s85, s84\n\ts_bfe_u32 s85, s2, 0x20003\n\ts_or_b32 s84, s84, s85\n\t"
               "s_mulk_i32 s84, 0x2400\n\ts_mulk_i32 s98, 36\n\ts_add_u32 s84, s84, s98\n\tv_and_b32 v110, 63, v0\n\tv_and_b32 v111, 32, v0\n\t"
               "v_lshlrev_b32 v111, 5, v111\n\tv_lshl_add_u32 v111, v110, 2, v111\n\tv_lshl_add_u32 v110, v110, 4, s84\n\tv_add_u32 v111, s84, v111\n\t"
               "s_cmpk_lt_u32 s2, 0x90\n\ts_waitcnt lgkmcnt(0)\n\t"
               "global_load_dwordx4 v[100:103], v110, s[64:65]\n\tglobal_load_dwordx4 v[104:107], v110, s[64:65] offset:1152\n\t"
               "global_load_dword v108, v111, s[64:65] offset:1024\n\tglobal_load_dword v109, v109, s[82:83] sc1\n\t"
               "s_cbranch_scc1 kf_n\n\ts_branch kf_h\n"
               "1:\n\ts_bfe_u32 s84, s2, 0x50003\n\ts_lshl_b32 s85, s85, 5\n\ts_or_b32 s84, s84, s85\n\ts_lshl_b32 s84, s84, 2\n\t"
               "v_and_b32 v110, 0xff, v0\n\tv_bfe_u32 v107, v0, 7, 1\n"
               "4:\n\ts_lshl_b32 s85, s84, 7\n\tv_lshl_add_u32 v110, v110, 1, s85\n\tv_lshl_add_u32 v107, v107, 1, s84\n\ts_and_b32 s85, s98, s2\n\ts_waitcnt lgkmcnt(0)\n\t"
               "global_load_ushort v100, v107, s[78:79]\n\tglobal_load_ushort v101, v110, s[66:67]\n\tglobal_load_ushort v102, v110, s[68:69]\n\t"
               "global_load_dword v109, v109, s[82:83] sc1\n\ts_bitcmp1_b32 s85, 8\n\ts_cbranch_scc1 kf_u\n\t"
               "global_load_ushort v103, v110, s[70:71]\n\tglobal_load_ushort v104, v110, s[72:73]\n\tglobal_load_ushort v105, v110, s[74:75]\n\t"
               "global_load_ushort v106, v110, s[76:77]\n\tglobal_load_ushort v107, v107, s[80:81]\n\ts_bitcmp1_b32 s2, 8\n\ts_cbranch_scc1 kf_c\n"
               "6:\n\ts_barrier\n\ts_branch kf_p\n"
               "2:\n\ts_cmpk_lt_u32 s2, 0xc0\n\ts_cbranch_scc1 6b\n\t"
               "s_lshr_b32 s84, s2, 3\n\ts_lshl3_add_u32 s84, s85, s84\n\ts_lshl1_add_u32 s84, s84, 976\n\t"
               "v_and_b32 v110, 0x7f, v0\n\tv_mov_b32 v107, 0\n\ts_branch 4b"
               : : "s"(__builtin_amdgcn_kernarg_segment_ptr()), "s"(blockIdx.x), "v"(threadIdx.x),
                   "s"((uint32_t)(uintptr_t)(__attribute__((address_space(3))) char*)smem) : "scc", "s96", "s97", "s98", "s99", "v127");
}
extern "C" __global__ __launch_bounds__(512, 4) void kf_c(KARGS) {
  __shared__ __attribute__((aligned(16))) char smem[SMEM];
  role_body<0>(smem, KPASS);
}
extern "C" __global__ __launch_bounds__(512, 4) void kf_u(KARGS) {
  __shared__ __attribute__((aligned(16))) char smem[SMEM];
  role_body<3>(smem, KPASS);
}
extern "C" __global__ __launch_bounds__(512, 4) void kf_n(KARGS) {
  __shared__ __attribute__((aligned(16))) char smem[SMEM];
  role_body<1, 1>(smem, KPASS);
}
extern "C" __global__ __launch_bounds__(512, 4) void kf_h(KARGS) {
  __shared__ __attribute__((aligned(16))) char smem[SMEM];
  role_body<1, 0>(smem, KPASS);
}
extern "C" __global__ __launch_bounds__(512, 4) void kf_p(KARGS) {
  __shared__ __attribute__((aligned(16))) char smem[SMEM];
  role_body<2>(smem, KPASS);
}
extern "C" __global__ __launch_bounds__(512, 4) void kf_conv() { asm volatile(CONV_ASM ::: CONV_CLOB); }
extern "C" __global__ void kf_end() {}

extern "C" __global__ __launch_bounds__(1024) void k_probe(int* dummy) {
  __shared__ int sm[2];
  const uint32_t now = (uint32_t)__builtin_amdgcn_s_memrealtime();
  __shared__ int sn;
  if (threadIdx.x == 0) { sm[0] = 0; sm[1] = 1 == 1 ? 0x7fffffff : 0; sn = 0; }
  __syncthreads();
  for (int k = threadIdx.x; k < 4096; k += 1024) {
    const uint32_t a = (uint32_t)g_prb[k], b = (uint32_t)(g_prb[k] >> 32);
    if (a) atomicMax(&sm[0], (int)(now - a));
    if (b) { if (1 == 1) atomicMin(&sm[1], (int)(now - b)); else if (1 == 0) atomicMax(&sm[1], (int)(now - b)); else { atomicAdd(&sm[1], (int)(now - b)); atomicAdd(&sn, 1); } }
  }
  __syncthreads();
  const int q = min(max(sm[0] * 10 - (1 == 2 ? sm[1] * 10 / max(sn, 1) : sm[1] * 10), 0), 30000);
  if (threadIdx.x == 0) while ((int)((uint32_t)__builtin_amdgcn_s_memrealtime() - now) < q) __builtin_amdgcn_s_sleep(2);
}
extern "C" void kernel_launch(void* const* d_in, const int* in_sizes, int n_in,
                              void* d_out, int out_size, void* d_ws, size_t ws_size,
                              hipStream_t stream) {
  kf_all<<<512, 512, 0, stream>>>((const u16*)d_in[0], (const u16*)d_in[1], (const u16*)d_in[2], (const u16*)d_in[3], (const u16*)d_in[4],
                                  (const u16*)d_in[5], (const u16*)d_in[6], (const u16*)d_in[7], (const u16*)d_in[8], (char*)d_ws + (size_t)WSOFF * 1048576,
                                  (const u16*)d_in[10], (const u16*)d_in[11], (const u16*)d_in[12], (const u16*)d_in[13], (u16*)d_out);
  k_probe<<<1, 1024, 0, stream>>>((int*)d_ws);
}
```

```cpp
#include <hip/hip_runtime.h>
#include <hip/hip_bf16.h>
#include <stdint.h>

typedef __attribute__((ext_vector_type(8))) short bf16x8;
typedef __attribute__((ext_vector_type(4))) float f32x4;
typedef uint16_t u16;
typedef __attribute__((ext_vector_type(4))) uint32_t u32x4;
typedef __attribute__((ext_vector_type(2))) uint32_t u32x2;
#define TSD uint32_t pr0_ = 0u, pr1_ = 0u
__device__ unsigned long long g_prb[4096];
#define TS(k, i) do { if ((i) == 0) pr0_ = (uint32_t)__builtin_amdgcn_s_memrealtime() | 1u; \
    if (ROLE == 2 && (i) == 8 && (4 < 0 || w == 4) && (blockIdx.x & 255) < 144) pr1_ = (uint32_t)__builtin_amdgcn_s_memrealtime() | 1u; } while (0)
#define TSC(i)
#define TSE() do { if (__builtin_amdgcn_mbcnt_hi(-1, __builtin_amdgcn_mbcnt_lo(-1, 0)) == 0) \
    __hip_atomic_store(&g_prb[blockIdx.x * 8 + w], ((unsigned long long)pr1_ << 32) | pr0_, __ATOMIC_RELAXED, __HIP_MEMORY_SCOPE_AGENT); } while (0)
#define TSP
#define TSA

constexpr int L_ = 2048, D_ = 576, N_ = 128, F_ = 4608, C_ = 8;
constexpr int NF = 144;
constexpr uint32_t O_EP = 0;
constexpr uint32_t O_KF = 4096;
constexpr uint32_t O_KEY = O_KF + 8192;
constexpr uint32_t O_SC = O_KEY + 32768;
constexpr uint32_t O_DN = O_SC + 9216;
constexpr uint32_t O_SB = O_DN + 20480;
constexpr uint32_t O_SU = O_SB + 65536;
constexpr uint32_t O_FB = O_SU + 262144;
constexpr uint32_t O_XC = O_FB + 8 * 1152;
constexpr uint32_t SP = 9216 + 128;
constexpr uint32_t O_SL = O_XC + 1024 * 1152;
constexpr uint32_t SLU = 336;
constexpr uint32_t O_U = O_SL + 256 * SLU * 16;
constexpr int UP = 584;
constexpr uint32_t VP = 8192 + 128, VPP = 4096 + 128;
constexpr uint32_t O_V = O_U + 256 * UP * 16, O_VP = O_V + 256 * VP;
static_assert(256 * SLU * 16 >= NF * SP && O_SL % 128 == 0 && O_U % 128 == 0 && O_V % 128 == 0, "");
__host__ __device__ __forceinline__ uint32_t u_at(int tg, int d) { return O_U + 16u * (uint32_t)((tg >> 1) * (UP * 2) + (d >> 2) * 8 + (tg & 1) * 4 + (d & 3)); }
__host__ __device__ __forceinline__ uint32_t v_at(int d, int tg) {
  return (d < 512) ? O_V + (uint32_t)(d >> 1) * VP + 16u * (uint32_t)(2 * tg + (d & 1)) : O_VP + (uint32_t)(d - 512) * VPP + 16u * (uint32_t)tg;
}
#define HAVE_VAT
#define HAVE_VTAG
__device__ __forceinline__ float bf2f(u16 v) { return __uint_as_float((uint32_t)v << 16); }
__device__ __forceinline__ float bflo(uint32_t v) { return __uint_as_float(v << 16); }
__device__ __forceinline__ float bfhi(uint32_t v) { return __uint_as_float(v & 0xffff0000u); }
__device__ __forceinline__ u16 f2bf(float f) { return __builtin_bit_cast(u16, (__bf16)f); }
__device__ __forceinline__ float rbf(float f) { return bf2f(f2bf(f)); }
typedef __attribute__((ext_vector_type(2))) float f32x2;
typedef __attribute__((ext_vector_type(2))) __bf16 bf16x2v;
__device__ __forceinline__ uint32_t pack2(float a, float b) {
  return __builtin_bit_cast(uint32_t, __builtin_convertvector((f32x2){a, b}, bf16x2v));
}

#define DPP_I(v, ctrl, rmask) __builtin_amdgcn_update_dpp(0, (v), (ctrl), (rmask), 0xf, true)
__device__ __forceinline__ float row_sum(float v) {
  v += __int_as_float(DPP_I(__float_as_int(v), 0x111, 0xf));
  v += __int_as_float(DPP_I(__float_as_int(v), 0x112, 0xf));
  v += __int_as_float(DPP_I(__float_as_int(v), 0x114, 0xf));
  v += __int_as_float(DPP_I(__float_as_int(v), 0x118, 0xf));
  return v;
}
__device__ __forceinline__ float rdl(float v, int l) { return __int_as_float(__builtin_amdgcn_readlane(__float_as_int(v), l)); }
__device__ __forceinline__ float wave_sum(float v) {
  v = row_sum(v);
  asm("s_nop 1\n\tv_add_f32_dpp %0, %0, %0 row_bcast:15 row_mask:0xa bank_mask:0xf\n\t"
      "s_nop 1\n\tv_add_f32_dpp %0, %0, %0 row_bcast:31 row_mask:0xc bank_mask:0xf\n\ts_nop 0" : "+v"(v));
  return rdl(v, 63);
}
__device__ __forceinline__ float fold32(float a, float b) {
  const auto r = __builtin_amdgcn_permlane32_swap(__float_as_uint(a), __float_as_uint(b), false, false);
  return __uint_as_float(r[0]) + __uint_as_float(r[1]);
}
__device__ __forceinline__ float fold16(float a, float b) {
  const auto r = __builtin_amdgcn_permlane16_swap(__float_as_uint(a), __float_as_uint(b), false, false);
  return __uint_as_float(r[0]) + __uint_as_float(r[1]);
}
__device__ __forceinline__ void wave_sum2(float& a, float& b) {
  float v = row_sum(fold32(a, b));
  asm("s_nop 1\n\tv_add_f32_dpp %0, %0, %0 row_bcast:15 row_mask:0xa bank_mask:0xf\n\ts_nop 0" : "+v"(v));
  a = rdl(v, 31); b = rdl(v, 63);
}
__device__ __forceinline__ float dot2(uint32_t a, uint32_t b, float c) {
  return __builtin_amdgcn_fdot2_f32_bf16(__builtin_bit_cast(bf16x2v, a), __builtin_bit_cast(bf16x2v, b), c, false);
}
__device__ __forceinline__ uint32_t row_maxu(uint32_t v) {
  v = max(v, (uint32_t)DPP_I((int)v, 0xb1, 0xf));
  v = max(v, (uint32_t)DPP_I((int)v, 0x4e, 0xf));
  v = max(v, (uint32_t)DPP_I((int)v, 0x141, 0xf));
  v = max(v, (uint32_t)DPP_I((int)v, 0x140, 0xf));
  return v;
}
__device__ __forceinline__ uint32_t wave_maxu(uint32_t v) {
  v = max(v, (uint32_t)DPP_I((int)v, 0x111, 0xf));
  v = max(v, (uint32_t)DPP_I((int)v, 0x112, 0xf));
  v = max(v, (uint32_t)DPP_I((int)v, 0x114, 0xf));
  v = max(v, (uint32_t)DPP_I((int)v, 0x118, 0xf));
  v = max(v, (uint32_t)DPP_I((int)v, 0x142, 0xa));
  v = max(v, (uint32_t)DPP_I((int)v, 0x143, 0xc));
  return (uint32_t)__builtin_amdgcn_readlane((int)v, 63);
}

typedef __attribute__((ext_vector_type(8))) _Float16 f16x8;
typedef __attribute__((ext_vector_type(2))) _Float16 f16x2;
__device__ __forceinline__ uint32_t packh(f32x2 v) { return __builtin_bit_cast(uint32_t, __builtin_convertvector(v, f16x2)); }
__device__ __forceinline__ uint32_t packh(float a, float b) { return packh((f32x2){a, b}); }
constexpr int IP = 224, I_X = 128;
__device__ __forceinline__ f32x2 cmul(f32x2 z, f32x2 w) {
  f32x2 r;
  asm("v_pk_mul_f32 %0, %1, %2 op_sel_hi:[0,1]\n\tv_pk_fma_f32 %0, %1, %2, %0 op_sel:[1,1,0] op_sel_hi:[1,0,1] neg_lo:[0,1,0]" : "=&v"(r) : "v"(z), "v"(w));
  return r;
}
__device__ __forceinline__ f32x2 cexp2(float ar2, float air, float l) {
  const float t = air * l;
  return (f32x2){__builtin_amdgcn_cosf(t), __builtin_amdgcn_sinf(t)} * __builtin_amdgcn_exp2f(ar2 * l);
}
__device__ __forceinline__ float state_a(float ldt, float are, float aim, float& ar2, float& air, float& g) {
  const float dt = rbf(__expf(ldt));
  ar2 = dt * are * 1.4426950408889634f;
  air = dt * aim * 0.15915494309189535f;
  air -= __builtin_rintf(air);
  g = 0.f;
  if (__builtin_expect(__any(ar2 > 0.f), 0)) g = fminf(__uint_as_float(wave_maxu(__float_as_uint(fmaxf(ar2, 0.f)))), 1.f);
  return dt;
}
template <int N> __device__ __forceinline__ void put_im(char* p, int im, const f32x2* z) {
  *(u32x4*)p = (u32x4){packh(z[0][0], z[1][0]), packh(z[2][0], z[3][0]), packh(z[4][0], z[5][0]), packh(z[6][0], z[7][0])};
  *(u32x4*)(p + im) = (u32x4){packh(z[0][1], z[1][1]), packh(z[2][1], z[3][1]), packh(z[4][1], z[5][1]), packh(z[6][1], z[7][1])};
}
#define Y_ASM \
  "v_mov_b64 v[40:41], %0\nv_pk_mul_f32 v[42:43], v[40:41], %1 op_sel_hi:[0,1]\nv_pk_mul_f32 v[44:45], %0, %2 op_sel_hi:[0,1]\n" \
  "v_pk_fma_f32 v[42:43], v[40:41], %1, v[42:43] op_sel:[1,1,0] op_sel_hi:[1,0,1] neg_lo:[0,1,0]\n" \
  "v_pk_fma_f32 v[44:45], %0, %2, v[44:45] op_sel:[1,1,0] op_sel_hi:[1,0,1] neg_lo:[0,1,0]\nv_cvt_pk_f16_f32 v56, v40, v42\n" \
  "v_cvt_pk_f16_f32 v60, v41, v43\nv_pk_mul_f32 v[40:41], v[42:43], %1 op_sel_hi:[0,1]\nv_pk_mul_f32 v[48:49], v[44:45], %2 op_sel_hi:[0,1]\n" \
  "v_pk_fma_f32 v[40:41], v[42:43], %1, v[40:41] op_sel:[1,1,0] op_sel_hi:[1,0,1] neg_lo:[0,1,0]\n" \
  "v_pk_fma_f32 v[48:49], v[44:45], %2, v[48:49] op_sel:[1,1,0] op_sel_hi:[1,0,1] neg_lo:[0,1,0]\n" \
  "v_pk_mul_f32 v[42:43], v[40:41], %1 op_sel_hi:[0,1]\nv_pk_mul_f32 v[52:53], v[48:49], %2 op_sel_hi:[0,1]\n" \
  "v_pk_fma_f32 v[42:43], v[40:41], %1, v[42:43] op_sel:[1,1,0] op_sel_hi:[1,0,1] neg_lo:[0,1,0]\n" \
  "v_pk_fma_f32 v[52:53], v[48:49], %2, v[52:53] op_sel:[1,1,0] op_sel_hi:[1,0,1] neg_lo:[0,1,0]\nv_cvt_pk_f16_f32 v57, v40, v42\n" \
  "v_cvt_pk_f16_f32 v61, v41, v43\nv_pk_mul_f32 v[40:41], v[42:43], %1 op_sel_hi:[0,1]\n" \
  "v_pk_fma_f32 v[40:41], v[42:43], %1, v[40:41] op_sel:[1,1,0] op_sel_hi:[1,0,1] neg_lo:[0,1,0]\n" \
  "v_pk_mul_f32 v[42:43], v[40:41], %1 op_sel_hi:[0,1]\nv_pk_mul_f32 v[46:47], v[44:45], %1 op_sel_hi:[0,1]\n" \
  "v_pk_fma_f32 v[42:43], v[40:41], %1, v[42:43] op_sel:[1,1,0] op_sel_hi:[1,0,1] neg_lo:[0,1,0]\n" \
  "v_pk_fma_f32 v[46:47], v[44:45], %1, v[46:47] op_sel:[1,1,0] op_sel_hi:[1,0,1] neg_lo:[0,1,0]\nv_cvt_pk_f16_f32 v58, v40, v42\n" \
  "v_cvt_pk_f16_f32 v62, v41, v43\nv_cvt_pk_f16_f32 v64, v44, v46\nv_cvt_pk_f16_f32 v68, v45, v47\n" \
  "v_pk_mul_f32 v[40:41], v[42:43], %1 op_sel_hi:[0,1]\nv_pk_mul_f32 v[44:45], v[46:47], %1 op_sel_hi:[0,1]\n" \
  "v_pk_fma_f32 v[40:41], v[42:43], %1, v[40:41] op_sel:[1,1,0] op_sel_hi:[1,0,1] neg_lo:[0,1,0]\n" \
  "v_pk_fma_f32 v[44:45], v[46:47], %1, v[44:45] op_sel:[1,1,0] op_sel_hi:[1,0,1] neg_lo:[0,1,0]\n" \
  "v_pk_mul_f32 v[42:43], v[40:41], %1 op_sel_hi:[0,1]\nv_pk_mul_f32 v[46:47], v[44:45], %1 op_sel_hi:[0,1]\n" \
  "v_pk_fma_f32 v[42:43], v[40:41], %1, v[42:43] op_sel:[1,1,0] op_sel_hi:[1,0,1] neg_lo:[0,1,0]\n" \
  "v_pk_fma_f32 v[46:47], v[44:45], %1, v[46:47] op_sel:[1,1,0] op_sel_hi:[1,0,1] neg_lo:[0,1,0]\nv_cvt_pk_f16_f32 v59, v40, v42\n" \
  "v_cvt_pk_f16_f32 v63, v41, v43\nv_cvt_pk_f16_f32 v65, v44, v46\nv_cvt_pk_f16_f32 v69, v45, v47\n" \
  "v_pk_mul_f32 v[44:45], v[46:47], %1 op_sel_hi:[0,1]\nds_write_b128 %3, v[56:59] offset:0\n" \
  "v_pk_fma_f32 v[44:45], v[46:47], %1, v[44:45] op_sel:[1,1,0] op_sel_hi:[1,0,1] neg_lo:[0,1,0]\nds_write_b128 %3, v[60:63] offset:64\n" \
  "v_pk_mul_f32 v[46:47], v[44:45], %1 op_sel_hi:[0,1]\nv_pk_mul_f32 v[50:51], v[48:49], %1 op_sel_hi:[0,1]\n" \
  "v_pk_fma_f32 v[46:47], v[44:45], %1, v[46:47] op_sel:[1,1,0] op_sel_hi:[1,0,1] neg_lo:[0,1,0]\n" \
  "v_pk_fma_f32 v[50:51], v[48:49], %1, v[50:51] op_sel:[1,1,0] op_sel_hi:[1,0,1] neg_lo:[0,1,0]\nv_cvt_pk_f16_f32 v66, v44, v46\n" \
  "v_cvt_pk_f16_f32 v70, v45, v47\nv_cvt_pk_f16_f32 v56, v48, v50\nv_cvt_pk_f16_f32 v60, v49, v51\n" \
  "v_pk_mul_f32 v[44:45], v[46:47], %1 op_sel_hi:[0,1]\nv_pk_mul_f32 v[48:49], v[50:51], %1 op_sel_hi:[0,1]\n" \
  "v_pk_fma_f32 v[44:45], v[46:47], %1, v[44:45] op_sel:[1,1,0] op_sel_hi:[1,0,1] neg_lo:[0,1,0]\n" \
  "v_pk_fma_f32 v[48:49], v[50:51], %1, v[48:49] op_sel:[1,1,0] op_sel_hi:[1,0,1] neg_lo:[0,1,0]\n" \
  "v_pk_mul_f32 v[46:47], v[44:45], %1 op_sel_hi:[0,1]\nv_pk_mul_f32 v[50:51], v[48:49], %1 op_sel_hi:[0,1]\n" \
  "v_pk_fma_f32 v[46:47], v[44:45], %1, v[46:47] op_sel:[1,1,0] op_sel_hi:[1,0,1] neg_lo:[0,1,0]\n" \
  "v_pk_fma_f32 v[50:51], v[48:49], %1, v[50:51] op_sel:[1,1,0] op_sel_hi:[1,0,1] neg_lo:[0,1,0]\nv_cvt_pk_f16_f32 v67, v44, v46\n" \
  "v_cvt_pk_f16_f32 v71, v45, v47\nv_cvt_pk_f16_f32 v57, v48, v50\nv_cvt_pk_f16_f32 v61, v49, v51\n" \
  "v_pk_mul_f32 v[48:49], v[50:51], %1 op_sel_hi:[0,1]\nds_write_b128 %3, v[64:67] offset:16\n" \
  "v_pk_fma_f32 v[48:49], v[50:51], %1, v[48:49] op_sel:[1,1,0] op_sel_hi:[1,0,1] neg_lo:[0,1,0]\nds_write_b128 %3, v[68:71] offset:80\n" \
  "v_pk_mul_f32 v[50:51], v[48:49], %1 op_sel_hi:[0,1]\nv_pk_mul_f32 v[54:55], v[52:53], %1 op_sel_hi:[0,1]\n" \
  "v_pk_fma_f32 v[50:51], v[48:49], %1, v[50:51] op_sel:[1,1,0] op_sel_hi:[1,0,1] neg_lo:[0,1,0]\n" \
  "v_pk_fma_f32 v[54:55], v[52:53], %1, v[54:55] op_sel:[1,1,0] op_sel_hi:[1,0,1] neg_lo:[0,1,0]\nv_cvt_pk_f16_f32 v58, v48, v50\n" \
  "v_cvt_pk_f16_f32 v62, v49, v51\nv_cvt_pk_f16_f32 v64, v52, v54\nv_cvt_pk_f16_f32 v68, v53, v55\n" \
  "v_pk_mul_f32 v[48:49], v[50:51], %1 op_sel_hi:[0,1]\nv_pk_mul_f32 v[52:53], v[54:55], %1 op_sel_hi:[0,1]\n" \
  "v_pk_fma_f32 v[48:49], v[50:51], %1, v[48:49] op_sel:[1,1,0] op_sel_hi:[1,0,1] neg_lo:[0,1,0]\n" \
  "v_pk_fma_f32 v[52:53], v[54:55], %1, v[52:53] op_sel:[1,1,0] op_sel_hi:[1,0,1] neg_lo:[0,1,0]\n" \
  "v_pk_mul_f32 v[50:51], v[48:49], %1 op_sel_hi:[0,1]\nv_pk_mul_f32 v[54:55], v[52:53], %1 op_sel_hi:[0,1]\n" \
  "v_pk_fma_f32 v[50:51], v[48:49], %1, v[50:51] op_sel:[1,1,0] op_sel_hi:[1,0,1] neg_lo:[0,1,0]\n" \
  "v_pk_fma_f32 v[54:55], v[52:53], %1, v[54:55] op_sel:[1,1,0] op_sel_hi:[1,0,1] neg_lo:[0,1,0]\nv_cvt_pk_f16_f32 v59, v48, v50\n" \
  "v_cvt_pk_f16_f32 v63, v49, v51\nv_cvt_pk_f16_f32 v65, v52, v54\nv_cvt_pk_f16_f32 v69, v53, v55\n" \
  "v_pk_mul_f32 v[52:53], v[54:55], %1 op_sel_hi:[0,1]\nds_write_b128 %3, v[56:59] offset:32\n" \
  "v_pk_fma_f32 v[52:53], v[54:55], %1, v[52:53] op_sel:[1,1,0] op_sel_hi:[1,0,1] neg_lo:[0,1,0]\nds_write_b128 %3, v[60:63] offset:96\n" \
  "v_pk_mul_f32 v[54:55], v[52:53], %1 op_sel_hi:[0,1]\n" \
  "v_pk_fma_f32 v[54:55], v[52:53], %1, v[54:55] op_sel:[1,1,0] op_sel_hi:[1,0,1] neg_lo:[0,1,0]\nv_cvt_pk_f16_f32 v66, v52, v54\n" \
  "v_cvt_pk_f16_f32 v70, v53, v55\nv_pk_mul_f32 v[52:53], v[54:55], %1 op_sel_hi:[0,1]\n" \
  "v_pk_fma_f32 v[52:53], v[54:55], %1, v[52:53] op_sel:[1,1,0] op_sel_hi:[1,0,1] neg_lo:[0,1,0]\n" \
  "v_pk_mul_f32 v[54:55], v[52:53], %1 op_sel_hi:[0,1]\n" \
  "v_pk_fma_f32 v[54:55], v[52:53], %1, v[54:55] op_sel:[1,1,0] op_sel_hi:[1,0,1] neg_lo:[0,1,0]\nv_cvt_pk_f16_f32 v67, v52, v54\n" \
  "v_cvt_pk_f16_f32 v71, v53, v55\nds_write_b128 %3, v[64:67] offset:48\nds_write_b128 %3, v[68:71] offset:112\n"
#define Y_CLOB "memory", "v40", "v41", "v42", "v43", "v44", "v45", "v46", "v47", "v48", "v49", "v50", "v51", "v52", "v53", "v54", "v55", "v56", "v57", "v58", "v59", "v60", "v61", "v62", "v63", "v64", "v65", "v66", "v67", "v68", "v69", "v70", "v71"
#define T_ASM \
  "ds_read_b128 %4, %8\nds_read_b64_tr_b16 v[80:81], %7 offset:0\nds_read_b64_tr_b16 v[82:83], %7 offset:64\n" \
  "ds_read_b64_tr_b16 v[84:85], %5 offset:0\nds_read_b64_tr_b16 v[86:87], %5 offset:48\nds_read_b64_tr_b16 v[88:89], %6 offset:0\n" \
  "ds_read_b64_tr_b16 v[90:91], %6 offset:48\nds_read_b64_tr_b16 v[92:93], %7 offset:3584\nds_read_b64_tr_b16 v[94:95], %7 offset:3648\n" \
  "ds_read_b64_tr_b16 v[96:97], %5 offset:3584\nds_read_b64_tr_b16 v[98:99], %5 offset:3632\nds_read_b64_tr_b16 v[100:101], %6 offset:3584\n" \
  "ds_read_b64_tr_b16 v[102:103], %6 offset:3632\nds_read_b64_tr_b16 v[104:105], %7 offset:7168\nds_read_b64_tr_b16 v[106:107], %7 offset:7232\n" \
  "s_waitcnt lgkmcnt(8)\nv_mfma_f32_16x16x32_f16 %0, v[84:87], v[80:83], 0\nds_read_b64_tr_b16 v[108:109], %5 offset:7168\n" \
  "ds_read_b64_tr_b16 v[110:111], %5 offset:7216\nds_read_b64_tr_b16 v[112:113], %6 offset:7168\nv_mfma_f32_16x16x32_f16 %2, v[88:91], v[80:83], 0\n" \
  "ds_read_b64_tr_b16 v[114:115], %6 offset:7216\nds_read_b64_tr_b16 v[116:117], %7 offset:10752\nds_read_b64_tr_b16 v[118:119], %7 offset:10816\n" \
  "s_waitcnt lgkmcnt(8)\nv_mfma_f32_16x16x32_f16 %0, v[96:99], v[92:95], %0\nds_read_b64_tr_b16 v[120:121], %5 offset:10752\n" \
  "ds_read_b64_tr_b16 v[122:123], %5 offset:10800\nds_read_b64_tr_b16 v[124:125], %6 offset:10752\n" \
  "v_mfma_f32_16x16x32_f16 %2, v[100:103], v[92:95], %2\nds_read_b64_tr_b16 v[126:127], %6 offset:10800\n" \
  "ds_read_b64_tr_b16 v[80:81], %7 offset:14336\nds_read_b64_tr_b16 v[82:83], %7 offset:14400\ns_waitcnt lgkmcnt(8)\n" \
  "v_mfma_f32_16x16x32_f16 %0, v[108:111], v[104:107], %0\nds_read_b64_tr_b16 v[84:85], %5 offset:14336\n" \
  "ds_read_b64_tr_b16 v[86:87], %5 offset:14384\nds_read_b64_tr_b16 v[88:89], %6 offset:14336\n" \
  "v_mfma_f32_16x16x32_f16 %2, v[112:115], v[104:107], %2\nds_read_b64_tr_b16 v[90:91], %6 offset:14384\n" \
  "ds_read_b64_tr_b16 v[92:93], %7 offset:17920\nds_read_b64_tr_b16 v[94:95], %7 offset:17984\ns_waitcnt lgkmcnt(8)\n" \
  "v_mfma_f32_16x16x32_f16 %0, v[120:123], v[116:119], %0\nds_read_b64_tr_b16 v[96:97], %5 offset:17920\n" \
  "ds_read_b64_tr_b16 v[98:99], %5 offset:17968\nds_read_b64_tr_b16 v[100:101], %6 offset:17920\n" \
  "v_mfma_f32_16x16x32_f16 %2, v[124:127], v[116:119], %2\nds_read_b64_tr_b16 v[102:103], %6 offset:17968\n" \
  "ds_read_b64_tr_b16 v[104:105], %7 offset:21504\nds_read_b64_tr_b16 v[106:107], %7 offset:21568\ns_waitcnt lgkmcnt(8)\n" \
  "v_mfma_f32_16x16x32_f16 %1, v[84:87], v[80:83], 0\nds_read_b64_tr_b16 v[108:109], %5 offset:21504\nds_read_b64_tr_b16 v[110:111], %5 offset:21552\n" \
  "ds_read_b64_tr_b16 v[112:113], %6 offset:21504\nv_mfma_f32_16x16x32_f16 %3, v[88:91], v[80:83], 0\nds_read_b64_tr_b16 v[114:115], %6 offset:21552\n" \
  "ds_read_b64_tr_b16 v[116:117], %7 offset:25088\nds_read_b64_tr_b16 v[118:119], %7 offset:25152\ns_waitcnt lgkmcnt(8)\n" \
  "v_mfma_f32_16x16x32_f16 %1, v[96:99], v[92:95], %1\nds_read_b64_tr_b16 v[120:121], %5 offset:25088\n" \
  "ds_read_b64_tr_b16 v[122:123], %5 offset:25136\nds_read_b64_tr_b16 v[124:125], %6 offset:25088\n" \
  "v_mfma_f32_16x16x32_f16 %3, v[100:103], v[92:95], %3\nds_read_b64_tr_b16 v[126:127], %6 offset:25136\ns_waitcnt lgkmcnt(6)\n" \
  "v_mfma_f32_16x16x32_f16 %1, v[108:111], v[104:107], %1\nv_mfma_f32_16x16x32_f16 %3, v[112:115], v[104:107], %3\ns_waitcnt lgkmcnt(0)\n" \
  "v_mfma_f32_16x16x32_f16 %1, v[120:123], v[116:119], %1\nv_mfma_f32_16x16x32_f16 %3, v[124:127], v[116:119], %3\n"
#define T_CLOB "memory", "v80", "v81", "v82", "v83", "v84", "v85", "v86", "v87", "v88", "v89", "v90", "v91", "v92", "v93", "v94", "v95", "v96", "v97", "v98", "v99", "v100", "v101", "v102", "v103", "v104", "v105", "v106", "v107", "v108", "v109", "v110", "v111", "v112", "v113", "v114", "v115", "v116", "v117", "v118", "v119", "v120", "v121", "v122", "v123", "v124", "v125", "v126", "v127"
template <class F> __device__ __forceinline__ int gen_y(float ldt, float are, float aim, float bre, float bim, float cre, float cim, uint32_t im, F&& mid TSP) {
  float ar2, air, g;
  const float dt = state_a(ldt, are, aim, ar2, air, g);
  const float dbr = dt * bre, dbi = dt * bim, pr = cre * dbr - cim * dbi, pi = cre * dbi + cim * dbr;
  const f32x2 w8 = cexp2(ar2, -air, 8.f), w64 = cexp2(ar2, -air, 64.f);
  const float pm = fmaxf(fabsf(pr), fabsf(pi));
  int e = -4;
  if (__builtin_expect(__any(!(pm < 0x1p-4f) || ar2 > 0.f) || !__any(pm > 0x1p-12f), 0)) e = (int)(wave_maxu(__float_as_uint(pm)) >> 23) - 126 + (int)ceilf(g * 250.f);
  const f32x2 y0 = {__builtin_amdgcn_ldexpf(pr, 14 - e), __builtin_amdgcn_ldexpf(-pi, 14 - e)};
  mid();
  asm volatile(Y_ASM : : "v"(y0), "v"(w8), "v"(w64), "v"(im) : Y_CLOB);
  return e;
}
__device__ __forceinline__ int gen_x(float ldt, float are, float aim, char* im) {
  float ar2, air, g;
  state_a(ldt, are, aim, ar2, air, g);
  const int e = min((int)ceilf(g * 1810.f), 300);
  const float s = __builtin_amdgcn_ldexpf(1.f, 15 - e);
  const f32x2 w1 = cexp2(ar2, air, 1.f), w256 = cexp2(ar2, air, 256.f), w2 = cmul(w1, w1);
  f32x2 a[8], b[8], c[8];
  a[0] = (f32x2){s, 0.f}; a[1] = w1 * s; a[2] = w2 * s;
  b[0] = b[1] = w256 * s;
  b[2] = cmul(b[0], w2); a[3] = cmul(a[1], w2); a[4] = cmul(a[2], w2);
  b[3] = b[4] = b[5] = cmul(b[2], w2); a[5] = cmul(a[3], w2); a[6] = cmul(a[4], w2);
  b[6] = cmul(b[3], w2); a[7] = cmul(a[5], w2); c[0] = cmul(b[3], w256);
  b[7] = cmul(b[6], w2); c[2] = cmul(c[0], w256);
  put_im<0>(im + I_X, 48, a);
  c[1] = cmul(b[7], w256); c[3] = cmul(c[1], w256);
  put_im<0>(im + I_X + 16, 48, b);
#pragma unroll
  for (int k = 4; k < 8; ++k) c[k] = cmul(c[k - 1], w256);
  put_im<0>(im + I_X + 32, 48, c);
  return e;
}

template <int I> struct IC { static constexpr int value = I; };
template <int B, int E, class F> __device__ __forceinline__ void static_for(F&& f) {
  if constexpr (B < E) { f(IC<B>{}); static_for<B + 1, E>(f); }
}
typedef __attribute__((ext_vector_type(4))) short s16x4;
__device__ __forceinline__ u32x2 trd(uint32_t a) {
  return __builtin_bit_cast(u32x2, __builtin_amdgcn_ds_read_tr16_b64_v4i16((__attribute__((address_space(3))) s16x4*)(uintptr_t)a));
}
#define BAR() asm volatile("s_waitcnt lgkmcnt(0)\n\ts_barrier" ::: "memory")
#define ST16(off, v) asm volatile("global_store_dwordx4 %0, %1, %2 sc1\n\ts_nop 1" : : "v"(off), "v"(v), "s"(ws) : "memory")
#define ST4(off, v) asm volatile("global_store_dword %0, %1, %2 sc1" : : "v"(off), "v"(v), "s"(ws) : "memory")
#define LD16(r, off) asm volatile("global_load_dwordx4 %0, %1, %2 sc1" : "=v"(r) : "v"(off), "s"(ws) : "memory")
#define LD16W(r, off) asm volatile("global_load_dwordx4 %0, %1, %2 sc1\n\ts_waitcnt vmcnt(0)" : "=&v"(r) : "v"(off), "s"(ws) : "memory")
#define DRAIN() asm volatile("s_waitcnt vmcnt(0)" ::: "memory")
#define CEX(a, b) { const uint32_t hi_ = max(a, b), lo_ = min(a, b); a = hi_; b = lo_; }
#define MMB(a_, b_, c_) __builtin_amdgcn_mfma_f32_16x16x32_bf16(__builtin_bit_cast(bf16x8, a_), __builtin_bit_cast(bf16x8, b_), c_, 0, 0, 0)
#define NTM 0
#define DPOL ""
#define DPOL2 ""
#define DPOL4 ""
constexpr int SPIN_CAP = 1 << 18;
constexpr uint32_t UMASK = 0x40074007u;
__device__ __forceinline__ uint32_t utag(uint32_t T, int j) {
  return ((T >> (3 * j)) & 0x00070007u) | (((j <= 2) ? (T << (2 - j)) : (T >> (j - 2))) & 0x40004000u);
}
__device__ __forceinline__ uint32_t max4(const u32x4& a) { return max(max(a[0], a[1]), max(a[2], a[3])); }
#ifndef ULONG
#define ULONG 8
#endif
__device__ __forceinline__ void until(uint32_t t) {
#pragma unroll 1
  for (int spin = 0; spin < 600; ++spin) {
    const int d = (int)(t - (uint32_t)__builtin_amdgcn_s_memrealtime());
    if (d <= 0) break;
    if (ULONG && d > 4 * ULONG) __builtin_amdgcn_s_sleep(ULONG); else __builtin_amdgcn_s_sleep(1);
  }
}
__device__ __forceinline__ uint32_t latest(uint32_t t) {
  const uint32_t t0 = (uint32_t)__builtin_amdgcn_readfirstlane(t) - 0x40000000u;
  return wave_maxu(t - t0) + t0;
}
__device__ __forceinline__ uint32_t gate(const char* ws, uint32_t po, uint32_t T) {
  u32x2 pv;
#pragma unroll 1
  for (int spin = 0; spin < SPIN_CAP; ++spin) {
    asm volatile("global_load_dwordx2 %0, %1, %2 sc1\n\ts_waitcnt vmcnt(0)" : "=&v"(pv) : "v"(po), "s"(ws) : "memory");
    if (!__any(pv[0] != T)) break;
    __builtin_amdgcn_s_sleep(2);
  }
  return pv[1];
}
__device__ __forceinline__ uint32_t gate2(const char* ws, uint32_t oa, uint32_t da, uint32_t ob, uint32_t T) {
  u32x2 pa, pb;
#pragma unroll 1
  for (int spin = 0; spin < SPIN_CAP; ++spin) {
    asm volatile("global_load_dwordx2 %0, %2, %4 sc1\n\tglobal_load_dwordx2 %1, %3, %4 sc1\n\ts_waitcnt vmcnt(0)" : "=&v"(pa), "=&v"(pb) : "v"(oa), "v"(ob), "s"(ws) : "memory");
    if (!__any(pa[0] != T || pb[0] != T)) break;
    __builtin_amdgcn_s_sleep(4);
  }
  pa[1] += da;
  return latest(((int)(pa[1] - pb[1]) > 0) ? pa[1] : pb[1]);
}
__device__ __forceinline__ uint32_t ahead(uint32_t c0, uint32_t t0, int dt) {
  const uint32_t c = (uint32_t)__builtin_amdgcn_s_memtime(), t = (uint32_t)__builtin_amdgcn_s_memrealtime();
  return t + (uint32_t)((float)((t - t0) * (uint32_t)(20 * dt)) * __builtin_amdgcn_rcpf((float)(c - c0)));
}
__device__ __forceinline__ void gsync(uint32_t a, uint32_t target) {
  uint32_t t, c, n = SPIN_CAP;
  uint64_t ex;
  asm volatile("s_waitcnt lgkmcnt(0)\n\ts_mov_b64 %3, exec\n\ts_mov_b64 exec, 1\n\tv_mov_b32 %0, 1\n\tds_add_u32 %4, %0\n\ts_mov_b64 exec, %3\n"
               "1:\n\ts_sleep 1\n\tds_read_b32 %0, %4\n\ts_sub_u32 %2, %2, 1\n\ts_cbranch_scc1 2f\n\ts_waitcnt lgkmcnt(0)\n\tv_readfirstlane_b32 %1, %0\n\t"
               "s_cmp_lt_u32 %1, %5\n\ts_cbranch_scc1 1b\n2:\n\ts_waitcnt lgkmcnt(0)"
               : "=&v"(t), "=&s"(c), "+s"(n), "=&s"(ex) : "v"(a), "n"(target) : "memory", "scc");
}
__device__ __forceinline__ void gsync0(uint32_t a, uint32_t target) {
  uint32_t t, c, n = SPIN_CAP;
  asm volatile("s_waitcnt lgkmcnt(0)\n\ts_mov_b64 exec, 1\n\tv_mov_b32 %0, 1\n\tds_add_u32 %3, %0\n\ts_mov_b64 exec, -1\n"
               "1:\n\tds_read_b32 %0, %3\n\ts_sub_u32 %2, %2, 1\n\ts_cbranch_scc1 2f\n\ts_waitcnt lgkmcnt(0)\n\tv_readfirstlane_b32 %1, %0\n\t"
               "s_cmp_lt_u32 %1, %4\n\ts_cbranch_scc1 1b\n2:\n\ts_waitcnt lgkmcnt(0)"
               : "=&v"(t), "=&s"(c), "+s"(n) : "v"(a), "n"(target) : "memory", "scc");
}
__device__ __forceinline__ void gwait(uint32_t a, uint32_t target) {
  uint32_t t, c, n = SPIN_CAP;
  asm volatile("1:\n\ts_sleep 2\n\tds_read_b32 %0, %3\n\ts_sub_u32 %2, %2, 1\n\ts_cbranch_scc1 2f\n\ts_waitcnt lgkmcnt(0)\n\tv_readfirstlane_b32 %1, %0\n\t"
               "s_cmp_lt_u32 %1, %4\n\ts_cbranch_scc1 1b\n2:\n\ts_waitcnt lgkmcnt(0)"
               : "=&v"(t), "=&s"(c), "+s"(n) : "v"(a), "n"(target) : "memory", "scc");
}
__device__ __forceinline__ void gwait0(uint32_t a, uint32_t target) {
  uint32_t t, c, n = SPIN_CAP;
  asm volatile("1:\n\tds_read_b32 %0, %3\n\ts_sub_u32 %2, %2, 1\n\ts_cbranch_scc1 2f\n\ts_waitcnt lgkmcnt(0)\n\tv_readfirstlane_b32 %1, %0\n\t"
               "s_cmp_lt_u32 %1, %4\n\ts_cbranch_scc1 1b\n2:\n\ts_waitcnt lgkmcnt(0)"
               : "=&v"(t), "=&s"(c), "+s"(n) : "v"(a), "n"(target) : "memory", "scc");
}
__device__ __forceinline__ void dma16(const void* base, uint32_t voff, uint32_t lds) {
  uint32_t keep;
  asm volatile("s_mov_b32 %0, m0\n\ts_mov_b32 m0, %3\n\ts_nop 0\n\tglobal_load_lds_dwordx4 %1, %2" DPOL "\n\ts_mov_b32 m0, %0" : "=&s"(keep) : "v"(voff), "s"(base), "s"(lds) : "memory");
}
__device__ __forceinline__ void dma16d(const void* base, uint32_t voff, uint32_t lds) {
  uint32_t keep;
  asm volatile("s_mov_b32 %0, m0\n\ts_mov_b32 m0, %3\n\ts_nop 0\n\tglobal_load_lds_dwordx4 %1, %2" DPOL2 "\n\ts_mov_b32 m0, %0" : "=&s"(keep) : "v"(voff), "s"(base), "s"(lds) : "memory");
}
__device__ __forceinline__ void nap(int n) {
  for (; n > 15; n -= 15) __builtin_amdgcn_s_sleep(15);
  switch (n) {
    case 1: __builtin_amdgcn_s_sleep(1); break; case 2: __builtin_amdgcn_s_sleep(2); break; case 3: __builtin_amdgcn_s_sleep(3); break;
    case 4: __builtin_amdgcn_s_sleep(4); break; case 5: __builtin_amdgcn_s_sleep(5); break; case 6: __builtin_amdgcn_s_sleep(6); break;
    case 7: __builtin_amdgcn_s_sleep(7); break; case 8: __builtin_amdgcn_s_sleep(8); break; case 9: __builtin_amdgcn_s_sleep(9); break;
    case 10: __builtin_amdgcn_s_sleep(10); break; case 11: __builtin_amdgcn_s_sleep(11); break; case 12: __builtin_amdgcn_s_sleep(12); break;
    case 13: __builtin_amdgcn_s_sleep(13); break; case 14: __builtin_amdgcn_s_sleep(14); break; case 15: __builtin_amdgcn_s_sleep(15); break;
    default: break;
  }
}
#define SLEEP(n) do { if ((n) > 0) __builtin_amdgcn_s_sleep(n); } while (0)
#define USLEEP 3
#define SSLEEP 11
#define PSLEEP 2
#define PPRIO 0
#define LFIRST 1
#define WD 32
#define OSLEEP 12
#define QPRIO 1
#define KPRIO 2
#define GSLEEP 0
#define NSLEEP 0
#define UD 40
#define XD 0
#define RSLEEP 0
#define UPRIO 2
#define VD2 384
#define WDEPTH 6
#define ESLEEP 0
#define SD 58
#define KD 182
#define OD 0
#define KBACK 6
#define DDEPTH 12
#define C3 1
#define XROT 2
#define WSOFF 0
#define KN 1
#define RPF 1
#define LSLEEP 1
#define TSTOP 50
#define HSTOP 170
#define VEARLY 0ull
#define FPR 3
#define SPR 3
#define DPRIO 0
#define QPRIO2 3
#define HW 2
#define RD 57
#define OPOS 1
#define OD2 6
#define NA 12
#define RD2 110
#define ORD1 ((unsigned __int128)0x321087654u << 36 | 0x321087654ull)
#define WDEPTH2 5
#define RQ NA
#define HREV 1
#define QPRIO0 3
#define VPS 1
#define RW 0
#define NPRIO 0
#define XSL 10
#define V2D 24
#define N0 8
#define LPOS 1
#define ALLP 1
#define OTP 1
#define STP 0
#define FD 202
#define VDB 368
#define BALL 1
#define HALL 0
#define TAPER 50
#define TDEP 1
#define FORCESLOW 0
#define FORCEFB 0
#define CONV_ASM \
  "s_cmp_eq_u32 s89, 1\ns_cbranch_scc1 1f\ns_cmp_gt_u32 s89, 1\ns_cbranch_scc1 9f\nds_read_b64 v[124:125], v3 offset:576\n" \
  "ds_read_b64 v[126:127], v3 offset:584\nds_read_b64 v[120:121], v3 offset:64\nds_read_b64 v[122:123], v3 offset:72\n" \
  "ds_read_b64 v[116:117], v3 offset:640\nds_read_b64 v[118:119], v3 offset:648\nds_read_b64 v[112:113], v3 offset:128\n" \
  "ds_read_b64 v[114:115], v3 offset:136\nds_read_b64 v[108:109], v3 offset:704\nds_read_b64 v[110:111], v3 offset:712\n" \
  "ds_read_b64 v[104:105], v3 offset:192\nds_read_b64 v[106:107], v3 offset:200\nds_read_b64 v[100:101], v3 offset:768\n" \
  "ds_read_b64 v[102:103], v3 offset:776\nds_read_b64 v[96:97], v3 offset:256\nds_read_b64 v[98:99], v3 offset:264\n" \
  "ds_read_b64 v[92:93], v3 offset:832\nds_read_b64 v[94:95], v3 offset:840\nds_read_b64 v[88:89], v3 offset:320\n" \
  "ds_read_b64 v[90:91], v3 offset:328\nds_read_b64 v[84:85], v3 offset:896\nds_read_b64 v[86:87], v3 offset:904\n" \
  "ds_read_b64 v[80:81], v3 offset:384\nds_read_b64 v[82:83], v3 offset:392\nds_read_b64 v[76:77], v3 offset:960\n" \
  "ds_read_b64 v[78:79], v3 offset:968\nds_read_b64 v[72:73], v3 offset:448\nds_read_b64 v[74:75], v3 offset:456\n" \
  "ds_read_b64 v[68:69], v3 offset:1024\nds_read_b64 v[70:71], v3 offset:1032\nds_read_b64 v[64:65], v3 offset:512\n" \
  "ds_read_b64 v[66:67], v3 offset:520\nds_read_b64 v[60:61], v3 offset:0\nds_read_b64 v[62:63], v3 offset:8\nds_read_b64 v[56:57], v3 offset:1088\n" \
  "ds_read_b64 v[58:59], v3 offset:1096\ns_waitcnt lgkmcnt(0)\ns_barrier\nds_read_b128 v[36:39], v2 offset:0\nds_read_b128 v[40:43], v2 offset:64\n" \
  "ds_read_b128 v[44:47], v2 offset:128\nds_read_b128 v[48:51], v2 offset:192\ns_waitcnt lgkmcnt(3)\n" \
  "v_mfma_f32_16x16x32_f16 v[12:15], v[124:127], v[36:39], 0\ns_setprio 1\nds_read_b128 v[52:55], v2 offset:256\n" \
  "v_mfma_f32_16x16x32_f16 v[16:19], v[120:123], v[36:39], 0\ns_waitcnt lgkmcnt(3)\nv_mfma_f32_16x16x32_f16 v[12:15], v[116:119], v[40:43], v[12:15]\n" \
  "ds_read_b128 v[36:39], v2 offset:320\nv_mfma_f32_16x16x32_f16 v[16:19], v[112:115], v[40:43], v[16:19]\ns_waitcnt lgkmcnt(3)\n" \
  "v_mfma_f32_16x16x32_f16 v[12:15], v[108:111], v[44:47], v[12:15]\nds_read_b128 v[40:43], v2 offset:384\n" \
  "v_mfma_f32_16x16x32_f16 v[16:19], v[104:107], v[44:47], v[16:19]\ns_waitcnt lgkmcnt(3)\n" \
  "v_mfma_f32_16x16x32_f16 v[12:15], v[100:103], v[48:51], v[12:15]\nds_read_b128 v[44:47], v2 offset:448\n" \
  "v_mfma_f32_16x16x32_f16 v[16:19], v[96:99], v[48:51], v[16:19]\ns_waitcnt lgkmcnt(3)\n" \
  "v_mfma_f32_16x16x32_f16 v[12:15], v[92:95], v[52:55], v[12:15]\nv_mfma_f32_16x16x32_f16 v[16:19], v[88:91], v[52:55], v[16:19]\n" \
  "s_waitcnt lgkmcnt(2)\nv_mfma_f32_16x16x32_f16 v[12:15], v[84:87], v[36:39], v[12:15]\nds_read_b128 v[48:51], v2 offset:512\n" \
  "v_mfma_f32_16x16x32_f16 v[16:19], v[80:83], v[36:39], v[16:19]\ns_waitcnt lgkmcnt(2)\n" \
  "v_mfma_f32_16x16x32_f16 v[12:15], v[76:79], v[40:43], v[12:15]\nds_read_b128 v[52:55], v2 offset:576\n" \
  "v_mfma_f32_16x16x32_f16 v[16:19], v[72:75], v[40:43], v[16:19]\ns_waitcnt lgkmcnt(2)\n" \
  "v_mfma_f32_16x16x32_f16 v[12:15], v[68:71], v[44:47], v[12:15]\nds_read_b128 v[36:39], v2 offset:640\n" \
  "v_mfma_f32_16x16x32_f16 v[16:19], v[64:67], v[44:47], v[16:19]\nv_mfma_f32_16x16x32_f16 v[20:23], v[60:63], v[44:47], 0\n" \
  "ds_read_b128 v[40:43], v2 offset:704\nv_mfma_f32_16x16x32_f16 v[28:31], v[56:59], v[44:47], 0\ns_waitcnt lgkmcnt(3)\n" \
  "v_mfma_f32_16x16x32_f16 v[16:19], v[124:127], v[48:51], v[16:19]\nds_read_b128 v[56:59], v2 offset:768\nv_cvt_pkrtz_f16_f32 v32, v12, v13\n" \
  "v_mfma_f32_16x16x32_f16 v[20:23], v[120:123], v[48:51], v[20:23]\nv_cvt_pkrtz_f16_f32 v33, v14, v15\ns_waitcnt lgkmcnt(3)\n" \
  "v_mfma_f32_16x16x32_f16 v[16:19], v[116:119], v[52:55], v[16:19]\nds_read_b128 v[44:47], v2 offset:832\n" \
  "v_pk_add_u16 v32, v32, 4 op_sel_hi:[1,0] clamp\nv_mfma_f32_16x16x32_f16 v[20:23], v[112:115], v[52:55], v[20:23]\n" \
  "v_pk_add_u16 v33, v33, 4 op_sel_hi:[1,0] clamp\ns_waitcnt lgkmcnt(3)\nv_mfma_f32_16x16x32_f16 v[16:19], v[108:111], v[36:39], v[16:19]\n" \
  "ds_read_b128 v[48:51], v2 offset:896\nds_write_b64 v4, v[32:33] offset:0\nv_mfma_f32_16x16x32_f16 v[20:23], v[104:107], v[36:39], v[20:23]\n" \
  "s_waitcnt lgkmcnt(3)\nv_mfma_f32_16x16x32_f16 v[16:19], v[100:103], v[40:43], v[16:19]\nds_read_b128 v[52:55], v2 offset:960\n" \
  "v_mfma_f32_16x16x32_f16 v[20:23], v[96:99], v[40:43], v[20:23]\nv_mfma_f32_16x16x32_f16 v[16:19], v[92:95], v[56:59], v[16:19]\n" \
  "v_mfma_f32_16x16x32_f16 v[20:23], v[88:91], v[56:59], v[20:23]\nds_read_b128 v[36:39], v2 offset:1024\ns_waitcnt lgkmcnt(3)\n" \
  "v_mfma_f32_16x16x32_f16 v[16:19], v[84:87], v[44:47], v[16:19]\nv_mfma_f32_16x16x32_f16 v[20:23], v[80:83], v[44:47], v[20:23]\n" \
  "ds_read_b128 v[40:43], v2 offset:1088\nds_read_b64 v[56:57], v6 offset:256\nds_read_b64 v[58:59], v6 offset:264\n" \
  "v_mfma_f32_16x16x32_f16 v[16:19], v[76:79], v[48:51], v[16:19]\nv_mfma_f32_16x16x32_f16 v[20:23], v[72:75], v[48:51], v[20:23]\n" \
  "ds_read_b128 v[44:47], v2 offset:1152\ns_waitcnt lgkmcnt(4)\nv_mfma_f32_16x16x32_f16 v[16:19], v[68:71], v[52:55], v[16:19]\n" \
  "v_mfma_f32_16x16x32_f16 v[20:23], v[64:67], v[52:55], v[20:23]\nds_read_b128 v[48:51], v2 offset:1216\n" \
  "v_mfma_f32_16x16x32_f16 v[24:27], v[60:63], v[52:55], 0\nv_mfma_f32_16x16x32_f16 v[20:23], v[124:127], v[36:39], v[20:23]\n" \
  "ds_read_b128 v[52:55], v2 offset:1280\nv_mfma_f32_16x16x32_f16 v[24:27], v[120:123], v[36:39], v[24:27]\ns_waitcnt lgkmcnt(5)\n" \
  "v_mfma_f32_16x16x32_f16 v[20:23], v[116:119], v[40:43], v[20:23]\nds_read_b128 v[36:39], v2 offset:1344\nv_cvt_pkrtz_f16_f32 v32, v16, v17\n" \
  "v_mfma_f32_16x16x32_f16 v[24:27], v[112:115], v[40:43], v[24:27]\nv_cvt_pkrtz_f16_f32 v33, v18, v19\ns_waitcnt lgkmcnt(3)\n" \
  "v_mfma_f32_16x16x32_f16 v[20:23], v[108:111], v[44:47], v[20:23]\nds_read_b128 v[40:43], v2 offset:1408\n" \
  "v_pk_add_u16 v32, v32, 4 op_sel_hi:[1,0] clamp\nds_read_b32 v35, v9 offset:32\nv_mfma_f32_16x16x32_f16 v[24:27], v[104:107], v[44:47], v[24:27]\n" \
  "v_pk_add_u16 v33, v33, 4 op_sel_hi:[1,0] clamp\ns_waitcnt lgkmcnt(3)\nv_mfma_f32_16x16x32_f16 v[20:23], v[100:103], v[48:51], v[20:23]\n" \
  "ds_read_b128 v[44:47], v2 offset:1472\nds_write_b64 v4, v[32:33] offset:1024\nv_mfma_f32_16x16x32_f16 v[24:27], v[96:99], v[48:51], v[24:27]\n" \
  "v_mfma_f32_16x16x32_f16 v[20:23], v[92:95], v[52:55], v[20:23]\nv_mfma_f32_16x16x32_f16 v[24:27], v[88:91], v[52:55], v[24:27]\n" \
  "ds_read_b128 v[48:51], v2 offset:1536\ns_waitcnt lgkmcnt(3)\nv_readfirstlane_b32 s85, v35\ns_cmp_lt_u32 s85, 4\ns_cbranch_scc1 21f\n22:\n" \
  "v_mfma_f32_16x16x32_f16 v[20:23], v[84:87], v[36:39], v[20:23]\nv_mfma_f32_16x16x32_f16 v[24:27], v[80:83], v[36:39], v[24:27]\n" \
  "ds_read_b128 v[52:55], v2 offset:1600\nv_mfma_f32_16x16x32_f16 v[20:23], v[76:79], v[40:43], v[20:23]\n" \
  "v_mfma_f32_16x16x32_f16 v[24:27], v[72:75], v[40:43], v[24:27]\nds_read_b128 v[36:39], v2 offset:1664\ns_waitcnt lgkmcnt(4)\n" \
  "v_mfma_f32_16x16x32_f16 v[20:23], v[68:71], v[44:47], v[20:23]\nv_mfma_f32_16x16x32_f16 v[24:27], v[64:67], v[44:47], v[24:27]\n" \
  "ds_read_b128 v[40:43], v2 offset:1728\nv_mfma_f32_16x16x32_f16 v[28:31], v[60:63], v[44:47], v[28:31]\nds_read_b64_tr_b16 v[60:61], v5 offset:0\n" \
  "ds_read_b64_tr_b16 v[62:63], v5 offset:32\ns_waitcnt lgkmcnt(4)\nv_mfma_f32_16x16x32_f16 v[24:27], v[124:127], v[48:51], v[24:27]\n" \
  "v_mfma_f32_16x16x32_f16 v[28:31], v[120:123], v[48:51], v[28:31]\nds_read_b64_tr_b16 v[44:45], v5 offset:256\n" \
  "ds_read_b64_tr_b16 v[46:47], v5 offset:288\nv_mfma_f32_16x16x32_f16 v[24:27], v[116:119], v[52:55], v[24:27]\n" \
  "ds_read_b64 v[120:121], v6 offset:320\nds_read_b64 v[122:123], v6 offset:328\nv_mfma_f32_16x16x32_f16 v[28:31], v[112:115], v[52:55], v[28:31]\n" \
  "ds_read_b128 v[48:51], v2 offset:1792\ns_waitcnt lgkmcnt(7)\nv_mfma_f32_16x16x32_f16 v[24:27], v[108:111], v[36:39], v[24:27]\n" \
  "ds_read_b128 v[112:115], v2 offset:1856\nv_mfma_f32_16x16x32_f16 v[28:31], v[104:107], v[36:39], v[28:31]\nds_read_b64 v[52:53], v6 offset:128\n" \
  "ds_read_b64 v[54:55], v6 offset:136\nv_mfma_f32_16x16x32_f16 v[24:27], v[100:103], v[40:43], v[24:27]\nds_read_b128 v[104:107], v2 offset:1920\n" \
  "v_mfma_f32_16x16x32_f16 v[28:31], v[96:99], v[40:43], v[28:31]\nds_read_b64 v[36:37], v6 offset:192\nds_read_b64 v[38:39], v6 offset:200\n" \
  "s_waitcnt lgkmcnt(9)\nv_mfma_f32_16x16x32_f16 v[20:23], v[60:63], v[56:59], v[20:23]\nds_read_b128 v[96:99], v2 offset:1984\ns_waitcnt lgkmcnt(7)\n" \
  "v_mfma_f32_16x16x32_f16 v[20:23], v[44:47], v[120:123], v[20:23]\nv_mfma_f32_16x16x32_f16 v[24:27], v[92:95], v[48:51], v[24:27]\n" \
  "ds_read_b128 v[40:43], v2 offset:2048\nv_mfma_f32_16x16x32_f16 v[28:31], v[88:91], v[48:51], v[28:31]\nds_read_b64_tr_b16 v[88:89], v5 offset:512\n" \
  "ds_read_b64_tr_b16 v[90:91], v5 offset:544\ns_waitcnt lgkmcnt(6)\nv_mfma_f32_16x16x32_f16 v[24:27], v[84:87], v[112:115], v[24:27]\n" \
  "v_mfma_f32_16x16x32_f16 v[28:31], v[80:83], v[112:115], v[28:31]\nv_cvt_pkrtz_f16_f32 v32, v20, v21\nv_cvt_pkrtz_f16_f32 v33, v22, v23\n" \
  "v_mfma_f32_16x16x32_f16 v[24:27], v[76:79], v[104:107], v[24:27]\nds_read_b64_tr_b16 v[48:49], v5 offset:768\n" \
  "ds_read_b64_tr_b16 v[50:51], v5 offset:800\nv_mfma_f32_16x16x32_f16 v[28:31], v[72:75], v[104:107], v[28:31]\n" \
  "ds_read_b128 v[80:83], v2 offset:2112\ns_waitcnt lgkmcnt(5)\nv_mfma_f32_16x16x32_f16 v[24:27], v[68:71], v[96:99], v[24:27]\n" \
  "ds_read_b128 v[112:115], v2 offset:2176\nv_pk_add_u16 v32, v32, 4 op_sel_hi:[1,0] clamp\n" \
  "v_mfma_f32_16x16x32_f16 v[28:31], v[64:67], v[96:99], v[28:31]\nds_read_b128 v[72:75], v2 offset:2240\n" \
  "v_pk_add_u16 v33, v33, 4 op_sel_hi:[1,0] clamp\nv_mfma_f32_16x16x32_f16 v[28:31], v[124:127], v[40:43], v[28:31]\n" \
  "ds_read_b128 v[104:107], v2 offset:2304\nds_write_b64 v4, v[32:33] offset:2048\nv_mfma_f32_16x16x32_f16 v[24:27], v[60:63], v[52:55], v[24:27]\n" \
  "ds_read_b128 v[64:67], v2 offset:2368\nds_read_b64 v[96:97], v6 offset:0\nds_read_b64 v[98:99], v6 offset:8\n" \
  "v_mfma_f32_16x16x32_f16 v[24:27], v[44:47], v[36:39], v[24:27]\nds_read_b128 v[124:127], v2 offset:2432\ns_waitcnt lgkmcnt(9)\n" \
  "v_mfma_f32_16x16x32_f16 v[24:27], v[88:91], v[56:59], v[24:27]\nds_read_b128 v[40:43], v2 offset:2496\n" \
  "v_mfma_f32_16x16x32_f16 v[24:27], v[48:51], v[120:123], v[24:27]\ns_waitcnt lgkmcnt(8)\n" \
  "v_mfma_f32_16x16x32_f16 v[28:31], v[116:119], v[80:83], v[28:31]\nds_read_b64 v[116:117], v6 offset:64\nds_read_b64 v[118:119], v6 offset:72\n" \
  "v_mfma_f32_16x16x32_f16 v[28:31], v[108:111], v[112:115], v[28:31]\nds_read_b64_tr_b16 v[80:81], v5 offset:1024\n" \
  "ds_read_b64_tr_b16 v[82:83], v5 offset:1056\ns_waitcnt lgkmcnt(10)\nv_mfma_f32_16x16x32_f16 v[28:31], v[100:103], v[72:75], v[28:31]\n" \
  "v_cvt_pkrtz_f16_f32 v32, v24, v25\nv_cvt_pkrtz_f16_f32 v33, v26, v27\nv_mfma_f32_16x16x32_f16 v[28:31], v[92:95], v[104:107], v[28:31]\n" \
  "v_pk_add_u16 v32, v32, 4 op_sel_hi:[1,0] clamp\ns_waitcnt lgkmcnt(5)\nv_mfma_f32_16x16x32_f16 v[28:31], v[84:87], v[64:67], v[28:31]\n" \
  "ds_read_b64_tr_b16 v[108:109], v5 offset:1280\nds_read_b64_tr_b16 v[110:111], v5 offset:1312\n" \
  "v_mfma_f32_16x16x32_f16 v[28:31], v[76:79], v[124:127], v[28:31]\nds_read_b64_tr_b16 v[112:113], v7 offset:0\n" \
  "ds_read_b64_tr_b16 v[114:115], v7 offset:64\ns_waitcnt lgkmcnt(6)\nv_mfma_f32_16x16x32_f16 v[28:31], v[68:71], v[40:43], v[28:31]\n" \
  "ds_read2_b32 v[100:101], v8 offset0:48 offset1:49\nds_read2_b32 v[102:103], v8 offset0:50 offset1:51\n" \
  "v_mfma_f32_16x16x32_f16 v[28:31], v[60:63], v[96:99], v[28:31]\nv_pk_add_u16 v33, v33, 4 op_sel_hi:[1,0] clamp\n" \
  "ds_write_b64 v4, v[32:33] offset:3072\nv_mfma_f32_16x16x32_f16 v[28:31], v[44:47], v[116:119], v[28:31]\n" \
  "v_mfma_f32_16x16x32_f16 v[28:31], v[88:91], v[52:55], v[28:31]\nv_mfma_f32_16x16x32_f16 v[28:31], v[48:51], v[36:39], v[28:31]\n" \
  "s_waitcnt lgkmcnt(5)\nv_mfma_f32_16x16x32_f16 v[28:31], v[80:83], v[56:59], v[28:31]\n" \
  "v_mfma_f32_16x16x32_f16 v[28:31], v[108:111], v[120:123], v[28:31]\ns_waitcnt lgkmcnt(1)\n" \
  "v_mfma_f32_16x16x32_f16 v[28:31], v[112:115], v[100:103], v[28:31]\ns_nop 7\nv_cvt_pkrtz_f16_f32 v32, v28, v29\nv_cvt_pkrtz_f16_f32 v33, v30, v31\n" \
  "v_pk_add_u16 v32, v32, 4 op_sel_hi:[1,0] clamp\nv_pk_add_u16 v33, v33, 4 op_sel_hi:[1,0] clamp\nds_write_b64 v4, v[32:33] offset:4096\n" \
  "s_setprio 0\ns_setpc_b64 s[90:91]\n21:\ns_movk_i32 s84, 0x4000\n23:\nds_read_b32 v35, v9 offset:32\ns_sub_u32 s84, s84, 1\ns_cbranch_scc1 22b\n" \
  "s_waitcnt lgkmcnt(0)\nv_readfirstlane_b32 s85, v35\ns_cmp_lt_u32 s85, 4\ns_cbranch_scc1 23b\ns_branch 22b\n1:\n" \
  "ds_read_b64 v[124:125], v3 offset:1088\nds_read_b64 v[126:127], v3 offset:1096\nds_read_b64 v[120:121], v3 offset:0\n" \
  "ds_read_b64 v[122:123], v3 offset:8\nds_read_b64 v[116:117], v3 offset:64\nds_read_b64 v[118:119], v3 offset:72\n" \
  "ds_read_b64 v[112:113], v3 offset:128\nds_read_b64 v[114:115], v3 offset:136\nds_read_b64 v[108:109], v3 offset:192\n" \
  "ds_read_b64 v[110:111], v3 offset:200\nds_read_b64 v[104:105], v3 offset:256\nds_read_b64 v[106:107], v3 offset:264\n" \
  "ds_read_b64 v[100:101], v3 offset:320\nds_read_b64 v[102:103], v3 offset:328\nds_read_b64 v[96:97], v3 offset:384\n" \
  "ds_read_b64 v[98:99], v3 offset:392\nds_read_b64 v[92:93], v3 offset:448\nds_read_b64 v[94:95], v3 offset:456\n" \
  "ds_read_b64 v[88:89], v3 offset:512\nds_read_b64 v[90:91], v3 offset:520\nds_read_b64 v[84:85], v3 offset:576\n" \
  "ds_read_b64 v[86:87], v3 offset:584\nds_read_b64 v[80:81], v3 offset:640\nds_read_b64 v[82:83], v3 offset:648\n" \
  "ds_read_b64 v[76:77], v3 offset:704\nds_read_b64 v[78:79], v3 offset:712\nds_read_b64 v[72:73], v3 offset:768\n" \
  "ds_read_b64 v[74:75], v3 offset:776\nds_read_b64 v[68:69], v3 offset:832\nds_read_b64 v[70:71], v3 offset:840\n" \
  "ds_read_b64 v[64:65], v3 offset:896\nds_read_b64 v[66:67], v3 offset:904\nds_read_b64 v[60:61], v3 offset:960\n" \
  "ds_read_b64 v[62:63], v3 offset:968\nds_read_b64 v[56:57], v3 offset:1024\nds_read_b64 v[58:59], v3 offset:1032\n" \
  "ds_read_b64 v[52:53], v6 offset:0\nds_read_b64 v[54:55], v6 offset:8\nds_read_b64 v[48:49], v6 offset:64\nds_read_b64 v[50:51], v6 offset:72\n" \
  "s_waitcnt lgkmcnt(0)\ns_barrier\nds_read_b128 v[28:31], v2 offset:960\nds_read_b128 v[32:35], v2 offset:1472\n" \
  "ds_read_b128 v[36:39], v2 offset:1984\nds_read_b128 v[40:43], v2 offset:2048\nds_read_b128 v[44:47], v2 offset:2112\ns_waitcnt lgkmcnt(3)\n" \
  "v_mfma_f32_16x16x32_f16 v[12:15], v[124:127], v[28:31], 0\ns_setprio 1\nds_read_b128 v[28:31], v2 offset:2176\n" \
  "v_mfma_f32_16x16x32_f16 v[16:19], v[124:127], v[32:35], 0\nds_read_b128 v[32:35], v2 offset:2240\ns_waitcnt lgkmcnt(3)\n" \
  "v_mfma_f32_16x16x32_f16 v[12:15], v[120:123], v[36:39], v[12:15]\nv_mfma_f32_16x16x32_f16 v[20:23], v[124:127], v[36:39], 0\n" \
  "ds_read_b128 v[124:127], v2 offset:2304\nds_read_b128 v[36:39], v2 offset:2368\nv_mfma_f32_16x16x32_f16 v[12:15], v[116:119], v[40:43], v[12:15]\n" \
  "ds_read_b128 v[40:43], v2 offset:2432\ns_waitcnt lgkmcnt(4)\nv_mfma_f32_16x16x32_f16 v[12:15], v[112:115], v[44:47], v[12:15]\n" \
  "ds_read_b128 v[44:47], v2 offset:2496\nv_mfma_f32_16x16x32_f16 v[12:15], v[108:111], v[28:31], v[12:15]\nds_read_b128 v[28:31], v2 offset:2560\n" \
  "s_waitcnt lgkmcnt(4)\nv_mfma_f32_16x16x32_f16 v[12:15], v[104:107], v[32:35], v[12:15]\nds_read_b128 v[32:35], v2 offset:2624\n" \
  "v_mfma_f32_16x16x32_f16 v[12:15], v[100:103], v[124:127], v[12:15]\ns_waitcnt lgkmcnt(3)\n" \
  "v_mfma_f32_16x16x32_f16 v[12:15], v[96:99], v[36:39], v[12:15]\nds_read_b128 v[124:127], v2 offset:2688\n" \
  "v_mfma_f32_16x16x32_f16 v[12:15], v[92:95], v[40:43], v[12:15]\ns_waitcnt lgkmcnt(3)\n" \
  "v_mfma_f32_16x16x32_f16 v[12:15], v[88:91], v[44:47], v[12:15]\nds_read_b128 v[36:39], v2 offset:2752\n" \
  "v_mfma_f32_16x16x32_f16 v[16:19], v[120:123], v[44:47], v[16:19]\ns_waitcnt lgkmcnt(3)\n" \
  "v_mfma_f32_16x16x32_f16 v[12:15], v[84:87], v[28:31], v[12:15]\nds_read_b128 v[40:43], v2 offset:2816\n" \
  "v_mfma_f32_16x16x32_f16 v[16:19], v[116:119], v[28:31], v[16:19]\ns_waitcnt lgkmcnt(3)\n" \
  "v_mfma_f32_16x16x32_f16 v[12:15], v[80:83], v[32:35], v[12:15]\nds_read_b128 v[44:47], v2 offset:2880\n" \
  "v_mfma_f32_16x16x32_f16 v[16:19], v[112:115], v[32:35], v[16:19]\ns_waitcnt lgkmcnt(3)\n" \
  "v_mfma_f32_16x16x32_f16 v[12:15], v[76:79], v[124:127], v[12:15]\nds_read_b128 v[28:31], v2 offset:2944\n" \
  "v_mfma_f32_16x16x32_f16 v[16:19], v[108:111], v[124:127], v[16:19]\ns_waitcnt lgkmcnt(3)\n" \
  "v_mfma_f32_16x16x32_f16 v[12:15], v[72:75], v[36:39], v[12:15]\nds_read_b128 v[32:35], v2 offset:3008\n" \
  "v_mfma_f32_16x16x32_f16 v[16:19], v[104:107], v[36:39], v[16:19]\ns_waitcnt lgkmcnt(3)\n" \
  "v_mfma_f32_16x16x32_f16 v[12:15], v[68:71], v[40:43], v[12:15]\nv_mfma_f32_16x16x32_f16 v[16:19], v[100:103], v[40:43], v[16:19]\n" \
  "ds_read_b128 v[124:127], v2 offset:3072\ns_waitcnt lgkmcnt(3)\nv_mfma_f32_16x16x32_f16 v[12:15], v[64:67], v[44:47], v[12:15]\n" \
  "v_mfma_f32_16x16x32_f16 v[16:19], v[96:99], v[44:47], v[16:19]\nds_read_b128 v[36:39], v2 offset:3136\ns_waitcnt lgkmcnt(3)\n" \
  "v_mfma_f32_16x16x32_f16 v[12:15], v[60:63], v[28:31], v[12:15]\nds_read_b64 v[40:41], v6 offset:128\nds_read_b64 v[42:43], v6 offset:136\n" \
  "v_mfma_f32_16x16x32_f16 v[16:19], v[92:95], v[28:31], v[16:19]\nds_read_b128 v[44:47], v2 offset:3200\ns_waitcnt lgkmcnt(4)\n" \
  "v_mfma_f32_16x16x32_f16 v[12:15], v[56:59], v[32:35], v[12:15]\nv_mfma_f32_16x16x32_f16 v[16:19], v[88:91], v[32:35], v[16:19]\n" \
  "ds_read_b128 v[28:31], v2 offset:3264\nv_mfma_f32_16x16x32_f16 v[20:23], v[120:123], v[32:35], v[20:23]\nds_read_b64 v[120:121], v6 offset:192\n" \
  "ds_read_b64 v[122:123], v6 offset:200\nv_mfma_f32_16x16x32_f16 v[16:19], v[84:87], v[124:127], v[16:19]\nds_read_b128 v[32:35], v2 offset:3328\n" \
  "v_mfma_f32_16x16x32_f16 v[20:23], v[116:119], v[124:127], v[20:23]\nds_read_b64 v[116:117], v6 offset:256\nds_read_b64 v[118:119], v6 offset:264\n" \
  "s_waitcnt lgkmcnt(6)\nv_mfma_f32_16x16x32_f16 v[16:19], v[80:83], v[36:39], v[16:19]\nds_read_b128 v[124:127], v2 offset:3392\n" \
  "v_mfma_f32_16x16x32_f16 v[20:23], v[112:115], v[36:39], v[20:23]\nds_read_b64 v[112:113], v6 offset:320\nds_read_b64 v[114:115], v6 offset:328\n" \
  "v_mfma_f32_16x16x32_f16 v[16:19], v[76:79], v[44:47], v[16:19]\nds_read_b128 v[36:39], v2 offset:3456\n" \
  "v_mfma_f32_16x16x32_f16 v[20:23], v[108:111], v[44:47], v[20:23]\ns_waitcnt lgkmcnt(6)\n" \
  "v_mfma_f32_16x16x32_f16 v[16:19], v[72:75], v[28:31], v[16:19]\nds_read_b128 v[108:111], v2 offset:3520\n" \
  "v_mfma_f32_16x16x32_f16 v[20:23], v[104:107], v[28:31], v[20:23]\nv_mfma_f32_16x16x32_f16 v[16:19], v[68:71], v[32:35], v[16:19]\n" \
  "ds_read_b128 v[44:47], v2 offset:3584\nv_mfma_f32_16x16x32_f16 v[20:23], v[100:103], v[32:35], v[20:23]\nds_read_b128 v[104:107], v2 offset:3648\n" \
  "ds_read_b32 v27, v9 offset:32\ns_waitcnt lgkmcnt(4)\nv_mfma_f32_16x16x32_f16 v[16:19], v[64:67], v[124:127], v[16:19]\n" \
  "ds_read_b128 v[28:31], v2 offset:3712\nv_mfma_f32_16x16x32_f16 v[20:23], v[96:99], v[124:127], v[20:23]\nds_read_b128 v[100:103], v2 offset:3776\n" \
  "ds_read2_b32 v[32:33], v8 offset0:32 offset1:33\nds_read2_b32 v[34:35], v8 offset0:34 offset1:35\n" \
  "v_mfma_f32_16x16x32_f16 v[16:19], v[60:63], v[36:39], v[16:19]\nds_read_b128 v[96:99], v2 offset:3840\n" \
  "v_mfma_f32_16x16x32_f16 v[20:23], v[92:95], v[36:39], v[20:23]\nds_read_b128 v[124:127], v2 offset:3904\ns_waitcnt lgkmcnt(8)\n" \
  "v_mfma_f32_16x16x32_f16 v[16:19], v[56:59], v[108:111], v[16:19]\nds_read_b128 v[92:95], v2 offset:3968\ns_waitcnt lgkmcnt(7)\n" \
  "v_readfirstlane_b32 s85, v27\ns_cmp_lt_u32 s85, 4\ns_cbranch_scc1 31f\n32:\nv_mfma_f32_16x16x32_f16 v[20:23], v[88:91], v[108:111], v[20:23]\n" \
  "ds_read_b128 v[36:39], v2 offset:4032\nds_read_b64_tr_b16 v[88:89], v5 offset:512\nds_read_b64_tr_b16 v[90:91], v5 offset:544\n" \
  "v_mfma_f32_16x16x32_f16 v[20:23], v[84:87], v[44:47], v[20:23]\nds_read_b64_tr_b16 v[108:109], v5 offset:768\n" \
  "ds_read_b64_tr_b16 v[110:111], v5 offset:800\nv_mfma_f32_16x16x32_f16 v[20:23], v[80:83], v[104:107], v[20:23]\n" \
  "ds_read_b64_tr_b16 v[84:85], v5 offset:1024\nds_read_b64_tr_b16 v[86:87], v5 offset:1056\ns_waitcnt lgkmcnt(12)\n" \
  "v_mfma_f32_16x16x32_f16 v[20:23], v[76:79], v[28:31], v[20:23]\nds_read_b64_tr_b16 v[44:45], v5 offset:1280\n" \
  "ds_read_b64_tr_b16 v[46:47], v5 offset:1312\nv_mfma_f32_16x16x32_f16 v[20:23], v[72:75], v[100:103], v[20:23]\ns_waitcnt lgkmcnt(10)\n" \
  "v_mfma_f32_16x16x32_f16 v[20:23], v[68:71], v[96:99], v[20:23]\nds_read_b64_tr_b16 v[80:81], v5 offset:1536\n" \
  "ds_read_b64_tr_b16 v[82:83], v5 offset:1568\nv_mfma_f32_16x16x32_f16 v[20:23], v[64:67], v[124:127], v[20:23]\n" \
  "ds_read_b64_tr_b16 v[104:105], v5 offset:1792\nds_read_b64_tr_b16 v[106:107], v5 offset:1824\ns_waitcnt lgkmcnt(12)\n" \
  "v_mfma_f32_16x16x32_f16 v[20:23], v[60:63], v[92:95], v[20:23]\nds_read_b64_tr_b16 v[76:77], v5 offset:2048\n" \
  "ds_read_b64_tr_b16 v[78:79], v5 offset:2080\nv_mfma_f32_16x16x32_f16 v[20:23], v[56:59], v[36:39], v[20:23]\ns_waitcnt lgkmcnt(10)\n" \
  "v_mfma_f32_16x16x32_f16 v[12:15], v[88:91], v[52:55], v[12:15]\nds_read_b64_tr_b16 v[28:29], v5 offset:2304\n" \
  "ds_read_b64_tr_b16 v[30:31], v5 offset:2336\nv_mfma_f32_16x16x32_f16 v[12:15], v[108:111], v[48:51], v[12:15]\n" \
  "ds_read_b64_tr_b16 v[72:73], v5 offset:2560\nds_read_b64_tr_b16 v[74:75], v5 offset:2592\ns_waitcnt lgkmcnt(10)\n" \
  "v_mfma_f32_16x16x32_f16 v[12:15], v[84:87], v[40:43], v[12:15]\nds_read_b64_tr_b16 v[100:101], v5 offset:2816\n" \
  "ds_read_b64_tr_b16 v[102:103], v5 offset:2848\nv_mfma_f32_16x16x32_f16 v[16:19], v[84:87], v[52:55], v[16:19]\n" \
  "ds_read_b64_tr_b16 v[68:69], v7 offset:0\nds_read_b64_tr_b16 v[70:71], v7 offset:64\n" \
  "v_mfma_f32_16x16x32_f16 v[12:15], v[44:47], v[120:123], v[12:15]\nv_mfma_f32_16x16x32_f16 v[16:19], v[44:47], v[48:51], v[16:19]\n" \
  "s_waitcnt lgkmcnt(10)\nv_mfma_f32_16x16x32_f16 v[12:15], v[80:83], v[116:119], v[12:15]\nds_read2_b32 v[96:97], v8 offset0:16 offset1:17\n" \
  "ds_read2_b32 v[98:99], v8 offset0:18 offset1:19\nv_mfma_f32_16x16x32_f16 v[16:19], v[80:83], v[40:43], v[16:19]\n" \
  "ds_read2_b32 v[64:65], v8 offset0:0 offset1:1\nds_read2_b32 v[66:67], v8 offset0:2 offset1:3\n" \
  "v_mfma_f32_16x16x32_f16 v[20:23], v[80:83], v[52:55], v[20:23]\nv_mfma_f32_16x16x32_f16 v[12:15], v[104:107], v[112:115], v[12:15]\n" \
  "v_mfma_f32_16x16x32_f16 v[16:19], v[104:107], v[120:123], v[16:19]\nv_mfma_f32_16x16x32_f16 v[20:23], v[104:107], v[48:51], v[20:23]\n" \
  "s_waitcnt lgkmcnt(10)\nv_mfma_f32_16x16x32_f16 v[16:19], v[76:79], v[116:119], v[16:19]\nds_read_b64_tr_b16 v[124:125], v7 offset:512\n" \
  "ds_read_b64_tr_b16 v[126:127], v7 offset:576\nv_mfma_f32_16x16x32_f16 v[20:23], v[76:79], v[40:43], v[20:23]\n" \
  "ds_read2_b32 v[60:61], v8 offset0:48 offset1:49\nds_read2_b32 v[62:63], v8 offset0:50 offset1:51\n" \
  "v_mfma_f32_16x16x32_f16 v[16:19], v[28:31], v[112:115], v[16:19]\nv_mfma_f32_16x16x32_f16 v[20:23], v[28:31], v[120:123], v[20:23]\n" \
  "s_waitcnt lgkmcnt(10)\nv_mfma_f32_16x16x32_f16 v[20:23], v[72:75], v[116:119], v[20:23]\nds_read_b64_tr_b16 v[92:93], v7 offset:1024\n" \
  "ds_read_b64_tr_b16 v[94:95], v7 offset:1088\nv_mfma_f32_16x16x32_f16 v[20:23], v[100:103], v[112:115], v[20:23]\n" \
  "ds_read_b64_tr_b16 v[56:57], v7 offset:1536\nds_read_b64_tr_b16 v[58:59], v7 offset:1600\ns_waitcnt lgkmcnt(10)\n" \
  "v_mfma_f32_16x16x32_f16 v[12:15], v[68:71], v[32:35], v[12:15]\nv_mfma_f32_16x16x32_f16 v[16:19], v[68:71], v[96:99], v[16:19]\n" \
  "s_waitcnt lgkmcnt(6)\nv_mfma_f32_16x16x32_f16 v[20:23], v[68:71], v[64:67], v[20:23]\ns_waitcnt lgkmcnt(2)\n" \
  "v_mfma_f32_16x16x32_f16 v[12:15], v[124:127], v[60:63], v[12:15]\nv_mfma_f32_16x16x32_f16 v[16:19], v[124:127], v[32:35], v[16:19]\n" \
  "v_mfma_f32_16x16x32_f16 v[20:23], v[124:127], v[96:99], v[20:23]\nv_mfma_f32_16x16x32_f16 v[16:19], v[92:95], v[60:63], v[16:19]\n" \
  "v_mfma_f32_16x16x32_f16 v[20:23], v[92:95], v[32:35], v[20:23]\ns_waitcnt lgkmcnt(0)\n" \
  "v_mfma_f32_16x16x32_f16 v[20:23], v[56:59], v[60:63], v[20:23]\ns_nop 1\nv_cvt_pkrtz_f16_f32 v24, v12, v13\nv_cvt_pkrtz_f16_f32 v25, v14, v15\n" \
  "v_pk_add_u16 v24, v24, 4 op_sel_hi:[1,0] clamp\nv_pk_add_u16 v25, v25, 4 op_sel_hi:[1,0] clamp\nds_write_b64 v4, v[24:25] offset:5120\n" \
  "v_cvt_pkrtz_f16_f32 v24, v16, v17\nv_cvt_pkrtz_f16_f32 v25, v18, v19\nv_pk_add_u16 v24, v24, 4 op_sel_hi:[1,0] clamp\n" \
  "v_pk_add_u16 v25, v25, 4 op_sel_hi:[1,0] clamp\nds_write_b64 v4, v[24:25] offset:6144\nv_cvt_pkrtz_f16_f32 v24, v20, v21\n" \
  "v_cvt_pkrtz_f16_f32 v25, v22, v23\nv_pk_add_u16 v24, v24, 4 op_sel_hi:[1,0] clamp\nv_pk_add_u16 v25, v25, 4 op_sel_hi:[1,0] clamp\n" \
  "ds_write_b64 v4, v[24:25] offset:7168\ns_setprio 0\ns_setpc_b64 s[90:91]\n31:\ns_movk_i32 s84, 0x4000\n33:\nds_read_b32 v27, v9 offset:32\n" \
  "s_sub_u32 s84, s84, 1\ns_cbranch_scc1 32b\ns_waitcnt lgkmcnt(0)\nv_readfirstlane_b32 s85, v27\ns_cmp_lt_u32 s85, 4\ns_cbranch_scc1 33b\n" \
  "s_branch 32b\n9:\ns_cmp_eq_u32 s89, 3\ns_cbranch_scc1 3f\ns_cmp_eq_u32 s89, 4\ns_cbranch_scc1 4f\ns_cmp_eq_u32 s89, 5\ns_cbranch_scc1 5f\n" \
  "ds_read_b64 v[124:125], v3 offset:576\nds_read_b64 v[126:127], v3 offset:584\nds_read_b64 v[120:121], v3 offset:64\n" \
  "ds_read_b64 v[122:123], v3 offset:72\nds_read_b64 v[116:117], v3 offset:640\nds_read_b64 v[118:119], v3 offset:648\n" \
  "ds_read_b64 v[112:113], v3 offset:128\nds_read_b64 v[114:115], v3 offset:136\nds_read_b64 v[108:109], v3 offset:704\n" \
  "ds_read_b64 v[110:111], v3 offset:712\nds_read_b64 v[104:105], v3 offset:192\nds_read_b64 v[106:107], v3 offset:200\n" \
  "ds_read_b64 v[100:101], v3 offset:768\nds_read_b64 v[102:103], v3 offset:776\nds_read_b64 v[96:97], v3 offset:256\n" \
  "ds_read_b64 v[98:99], v3 offset:264\nds_read_b64 v[92:93], v3 offset:832\nds_read_b64 v[94:95], v3 offset:840\n" \
  "ds_read_b64 v[88:89], v3 offset:320\nds_read_b64 v[90:91], v3 offset:328\nds_read_b64 v[84:85], v3 offset:896\n" \
  "ds_read_b64 v[86:87], v3 offset:904\nds_read_b64 v[80:81], v3 offset:384\nds_read_b64 v[82:83], v3 offset:392\n" \
  "ds_read_b64 v[76:77], v3 offset:960\nds_read_b64 v[78:79], v3 offset:968\nds_read_b64 v[72:73], v3 offset:448\n" \
  "ds_read_b64 v[74:75], v3 offset:456\nds_read_b64 v[68:69], v3 offset:1024\nds_read_b64 v[70:71], v3 offset:1032\n" \
  "ds_read_b64 v[64:65], v3 offset:512\nds_read_b64 v[66:67], v3 offset:520\ns_waitcnt lgkmcnt(0)\ns_barrier\nds_read_b128 v[24:27], v2 offset:0\n" \
  "ds_read_b128 v[28:31], v2 offset:64\nds_read_b128 v[32:35], v2 offset:128\nds_read_b128 v[36:39], v2 offset:192\ns_waitcnt lgkmcnt(3)\n" \
  "v_mfma_f32_16x16x32_f16 v[12:15], v[124:127], v[24:27], 0\ns_setprio 1\nds_read_b128 v[40:43], v2 offset:256\n" \
  "v_mfma_f32_16x16x32_f16 v[16:19], v[120:123], v[24:27], 0\ns_waitcnt lgkmcnt(3)\nv_mfma_f32_16x16x32_f16 v[12:15], v[116:119], v[28:31], v[12:15]\n" \
  "ds_read_b128 v[44:47], v2 offset:320\nv_mfma_f32_16x16x32_f16 v[16:19], v[112:115], v[28:31], v[16:19]\ns_waitcnt lgkmcnt(3)\n" \
  "v_mfma_f32_16x16x32_f16 v[12:15], v[108:111], v[32:35], v[12:15]\nds_read_b128 v[48:51], v2 offset:384\n" \
  "v_mfma_f32_16x16x32_f16 v[16:19], v[104:107], v[32:35], v[16:19]\ns_waitcnt lgkmcnt(3)\n" \
  "v_mfma_f32_16x16x32_f16 v[12:15], v[100:103], v[36:39], v[12:15]\nds_read_b128 v[52:55], v2 offset:448\n" \
  "v_mfma_f32_16x16x32_f16 v[16:19], v[96:99], v[36:39], v[16:19]\ns_waitcnt lgkmcnt(3)\n" \
  "v_mfma_f32_16x16x32_f16 v[12:15], v[92:95], v[40:43], v[12:15]\nds_read_b128 v[56:59], v2 offset:512\n" \
  "v_mfma_f32_16x16x32_f16 v[16:19], v[88:91], v[40:43], v[16:19]\nds_read_b128 v[60:63], v2 offset:576\ns_waitcnt lgkmcnt(3)\n" \
  "v_mfma_f32_16x16x32_f16 v[12:15], v[84:87], v[44:47], v[12:15]\nds_read_b128 v[120:123], v2 offset:640\n" \
  "v_mfma_f32_16x16x32_f16 v[16:19], v[80:83], v[44:47], v[16:19]\nds_read_b128 v[24:27], v2 offset:704\n" \
  "v_mfma_f32_16x16x32_f16 v[12:15], v[76:79], v[48:51], v[12:15]\nds_read_b128 v[112:115], v2 offset:768\n" \
  "v_mfma_f32_16x16x32_f16 v[16:19], v[72:75], v[48:51], v[16:19]\nds_read_b128 v[28:31], v2 offset:832\ns_waitcnt lgkmcnt(5)\n" \
  "v_mfma_f32_16x16x32_f16 v[12:15], v[68:71], v[52:55], v[12:15]\nds_read_b128 v[104:107], v2 offset:896\n" \
  "v_mfma_f32_16x16x32_f16 v[16:19], v[64:67], v[52:55], v[16:19]\nds_read_b128 v[32:35], v2 offset:960\n" \
  "v_mfma_f32_16x16x32_f16 v[16:19], v[124:127], v[56:59], v[16:19]\ns_waitcnt lgkmcnt(5)\n" \
  "v_mfma_f32_16x16x32_f16 v[16:19], v[116:119], v[60:63], v[16:19]\nv_mfma_f32_16x16x32_f16 v[16:19], v[108:111], v[120:123], v[16:19]\n" \
  "s_waitcnt lgkmcnt(3)\nv_mfma_f32_16x16x32_f16 v[16:19], v[100:103], v[24:27], v[16:19]\nv_cvt_pkrtz_f16_f32 v20, v12, v13\n" \
  "v_cvt_pkrtz_f16_f32 v21, v14, v15\nv_mfma_f32_16x16x32_f16 v[16:19], v[92:95], v[112:115], v[16:19]\n" \
  "v_pk_add_u16 v20, v20, 4 op_sel_hi:[1,0] clamp\ns_waitcnt lgkmcnt(2)\nv_mfma_f32_16x16x32_f16 v[16:19], v[84:87], v[28:31], v[16:19]\n" \
  "v_pk_add_u16 v21, v21, 4 op_sel_hi:[1,0] clamp\ns_waitcnt lgkmcnt(1)\nv_mfma_f32_16x16x32_f16 v[16:19], v[76:79], v[104:107], v[16:19]\n" \
  "ds_write_b64 v4, v[20:21] offset:0\ns_waitcnt lgkmcnt(1)\nv_mfma_f32_16x16x32_f16 v[16:19], v[68:71], v[32:35], v[16:19]\ns_nop 7\n" \
  "v_cvt_pkrtz_f16_f32 v20, v16, v17\nv_cvt_pkrtz_f16_f32 v21, v18, v19\nv_pk_add_u16 v20, v20, 4 op_sel_hi:[1,0] clamp\n" \
  "v_pk_add_u16 v21, v21, 4 op_sel_hi:[1,0] clamp\nds_write_b64 v4, v[20:21] offset:1024\ns_setprio 0\ns_setpc_b64 s[90:91]\n3:\n" \
  "ds_read_b64 v[124:125], v3 offset:0\nds_read_b64 v[126:127], v3 offset:8\nds_read_b64 v[120:121], v3 offset:64\n" \
  "ds_read_b64 v[122:123], v3 offset:72\nds_read_b64 v[116:117], v3 offset:128\nds_read_b64 v[118:119], v3 offset:136\n" \
  "ds_read_b64 v[112:113], v3 offset:192\nds_read_b64 v[114:115], v3 offset:200\nds_read_b64 v[108:109], v3 offset:256\n" \
  "ds_read_b64 v[110:111], v3 offset:264\nds_read_b64 v[104:105], v3 offset:320\nds_read_b64 v[106:107], v3 offset:328\n" \
  "ds_read_b64 v[100:101], v3 offset:384\nds_read_b64 v[102:103], v3 offset:392\nds_read_b64 v[96:97], v3 offset:448\n" \
  "ds_read_b64 v[98:99], v3 offset:456\nds_read_b64 v[92:93], v3 offset:512\nds_read_b64 v[94:95], v3 offset:520\n" \
  "ds_read_b64 v[88:89], v3 offset:576\nds_read_b64 v[90:91], v3 offset:584\nds_read_b64 v[84:85], v3 offset:640\n" \
  "ds_read_b64 v[86:87], v3 offset:648\nds_read_b64 v[80:81], v3 offset:704\nds_read_b64 v[82:83], v3 offset:712\n" \
  "ds_read_b64 v[76:77], v3 offset:768\nds_read_b64 v[78:79], v3 offset:776\nds_read_b64 v[72:73], v3 offset:832\n" \
  "ds_read_b64 v[74:75], v3 offset:840\nds_read_b64 v[68:69], v3 offset:896\nds_read_b64 v[70:71], v3 offset:904\n" \
  "ds_read_b64 v[64:65], v3 offset:960\nds_read_b64 v[66:67], v3 offset:968\nds_read_b64 v[60:61], v3 offset:1024\n" \
  "ds_read_b64 v[62:63], v3 offset:1032\nds_read_b64 v[56:57], v6 offset:256\nds_read_b64 v[58:59], v6 offset:264\n" \
  "ds_read_b64 v[52:53], v6 offset:128\nds_read_b64 v[54:55], v6 offset:136\nds_read_b64 v[48:49], v6 offset:320\n" \
  "ds_read_b64 v[50:51], v6 offset:328\nds_read_b64 v[44:45], v6 offset:192\nds_read_b64 v[46:47], v6 offset:200\ns_waitcnt lgkmcnt(0)\ns_barrier\n" \
  "ds_read_b128 v[24:27], v2 offset:448\nds_read_b128 v[28:31], v2 offset:512\nds_read_b128 v[32:35], v2 offset:576\n" \
  "ds_read_b128 v[36:39], v2 offset:640\nds_read_b128 v[40:43], v2 offset:704\ns_waitcnt lgkmcnt(3)\n" \
  "v_mfma_f32_16x16x32_f16 v[12:15], v[124:127], v[24:27], 0\ns_setprio 1\nds_read_b128 v[24:27], v2 offset:768\n" \
  "v_mfma_f32_16x16x32_f16 v[12:15], v[120:123], v[28:31], v[12:15]\nds_read_b128 v[28:31], v2 offset:832\ns_waitcnt lgkmcnt(3)\n" \
  "v_mfma_f32_16x16x32_f16 v[12:15], v[116:119], v[32:35], v[12:15]\nds_read_b128 v[32:35], v2 offset:896\n" \
  "v_mfma_f32_16x16x32_f16 v[12:15], v[112:115], v[36:39], v[12:15]\nds_read_b128 v[36:39], v2 offset:960\ns_waitcnt lgkmcnt(3)\n" \
  "v_mfma_f32_16x16x32_f16 v[12:15], v[108:111], v[40:43], v[12:15]\nds_read_b128 v[40:43], v2 offset:1024\n" \
  "v_mfma_f32_16x16x32_f16 v[12:15], v[104:107], v[24:27], v[12:15]\nds_read_b128 v[24:27], v2 offset:1088\ns_waitcnt lgkmcnt(3)\n" \
  "v_mfma_f32_16x16x32_f16 v[12:15], v[100:103], v[28:31], v[12:15]\nds_read_b128 v[28:31], v2 offset:1152\n" \
  "v_mfma_f32_16x16x32_f16 v[12:15], v[96:99], v[32:35], v[12:15]\ns_waitcnt lgkmcnt(3)\n" \
  "v_mfma_f32_16x16x32_f16 v[12:15], v[92:95], v[36:39], v[12:15]\nds_read_b128 v[32:35], v2 offset:1216\n" \
  "v_mfma_f32_16x16x32_f16 v[16:19], v[124:127], v[36:39], 0\ns_waitcnt lgkmcnt(3)\nv_mfma_f32_16x16x32_f16 v[12:15], v[88:91], v[40:43], v[12:15]\n" \
  "ds_read_b128 v[124:127], v2 offset:1280\nv_mfma_f32_16x16x32_f16 v[16:19], v[120:123], v[40:43], v[16:19]\ns_waitcnt lgkmcnt(3)\n" \
  "v_mfma_f32_16x16x32_f16 v[12:15], v[84:87], v[24:27], v[12:15]\nds_read_b128 v[36:39], v2 offset:1344\n" \
  "v_mfma_f32_16x16x32_f16 v[16:19], v[116:119], v[24:27], v[16:19]\ns_waitcnt lgkmcnt(3)\n" \
  "v_mfma_f32_16x16x32_f16 v[12:15], v[80:83], v[28:31], v[12:15]\nds_read_b128 v[120:123], v2 offset:1408\n" \
  "v_mfma_f32_16x16x32_f16 v[16:19], v[112:115], v[28:31], v[16:19]\ns_waitcnt lgkmcnt(3)\n" \
  "v_mfma_f32_16x16x32_f16 v[12:15], v[76:79], v[32:35], v[12:15]\nds_read_b128 v[40:43], v2 offset:1472\n" \
  "v_mfma_f32_16x16x32_f16 v[16:19], v[108:111], v[32:35], v[16:19]\ns_waitcnt lgkmcnt(3)\n" \
  "v_mfma_f32_16x16x32_f16 v[12:15], v[72:75], v[124:127], v[12:15]\nds_read_b128 v[116:119], v2 offset:1536\n" \
  "v_mfma_f32_16x16x32_f16 v[16:19], v[104:107], v[124:127], v[16:19]\nds_read_b128 v[24:27], v2 offset:1600\ns_waitcnt lgkmcnt(3)\n" \
  "v_mfma_f32_16x16x32_f16 v[12:15], v[68:71], v[36:39], v[12:15]\nds_read_b128 v[112:115], v2 offset:1664\nds_read_b32 v23, v9 offset:32\n" \
  "v_mfma_f32_16x16x32_f16 v[16:19], v[100:103], v[36:39], v[16:19]\nds_read_b128 v[28:31], v2 offset:1728\n" \
  "v_mfma_f32_16x16x32_f16 v[12:15], v[64:67], v[120:123], v[12:15]\nds_read_b128 v[108:111], v2 offset:1792\n" \
  "v_mfma_f32_16x16x32_f16 v[16:19], v[96:99], v[120:123], v[16:19]\nds_read_b128 v[32:35], v2 offset:1856\ns_waitcnt lgkmcnt(6)\n" \
  "v_mfma_f32_16x16x32_f16 v[12:15], v[60:63], v[40:43], v[12:15]\nds_read_b128 v[104:107], v2 offset:1920\n" \
  "v_mfma_f32_16x16x32_f16 v[16:19], v[92:95], v[40:43], v[16:19]\nds_read_b128 v[124:127], v2 offset:1984\ns_waitcnt lgkmcnt(5)\n" \
  "v_readfirstlane_b32 s85, v23\ns_cmp_lt_u32 s85, 3\ns_cbranch_scc1 51f\n52:\nv_mfma_f32_16x16x32_f16 v[16:19], v[88:91], v[116:119], v[16:19]\n" \
  "ds_read_b64_tr_b16 v[100:101], v5 offset:0\nds_read_b64_tr_b16 v[102:103], v5 offset:32\n" \
  "v_mfma_f32_16x16x32_f16 v[16:19], v[84:87], v[24:27], v[16:19]\nds_read_b64_tr_b16 v[36:37], v5 offset:256\n" \
  "ds_read_b64_tr_b16 v[38:39], v5 offset:288\nv_mfma_f32_16x16x32_f16 v[16:19], v[80:83], v[112:115], v[16:19]\n" \
  "ds_read_b64_tr_b16 v[96:97], v5 offset:512\nds_read_b64_tr_b16 v[98:99], v5 offset:544\ns_waitcnt lgkmcnt(9)\n" \
  "v_mfma_f32_16x16x32_f16 v[16:19], v[76:79], v[28:31], v[16:19]\nds_read_b64_tr_b16 v[120:121], v5 offset:768\n" \
  "ds_read_b64_tr_b16 v[122:123], v5 offset:800\nv_mfma_f32_16x16x32_f16 v[16:19], v[72:75], v[108:111], v[16:19]\ns_waitcnt lgkmcnt(9)\n" \
  "v_mfma_f32_16x16x32_f16 v[16:19], v[68:71], v[32:35], v[16:19]\nv_mfma_f32_16x16x32_f16 v[16:19], v[64:67], v[104:107], v[16:19]\n" \
  "s_waitcnt lgkmcnt(6)\nv_mfma_f32_16x16x32_f16 v[16:19], v[60:63], v[124:127], v[16:19]\n" \
  "v_mfma_f32_16x16x32_f16 v[12:15], v[100:103], v[56:59], v[12:15]\nv_mfma_f32_16x16x32_f16 v[16:19], v[100:103], v[52:55], v[16:19]\n" \
  "s_waitcnt lgkmcnt(2)\nv_mfma_f32_16x16x32_f16 v[12:15], v[36:39], v[48:51], v[12:15]\n" \
  "v_mfma_f32_16x16x32_f16 v[16:19], v[36:39], v[44:47], v[16:19]\nv_mfma_f32_16x16x32_f16 v[16:19], v[96:99], v[56:59], v[16:19]\n" \
  "s_waitcnt lgkmcnt(0)\nv_mfma_f32_16x16x32_f16 v[16:19], v[120:123], v[48:51], v[16:19]\ns_nop 3\nv_cvt_pkrtz_f16_f32 v20, v12, v13\n" \
  "v_cvt_pkrtz_f16_f32 v21, v14, v15\nv_pk_add_u16 v20, v20, 4 op_sel_hi:[1,0] clamp\nv_pk_add_u16 v21, v21, 4 op_sel_hi:[1,0] clamp\n" \
  "ds_write_b64 v4, v[20:21] offset:2048\nv_cvt_pkrtz_f16_f32 v20, v16, v17\nv_cvt_pkrtz_f16_f32 v21, v18, v19\n" \
  "v_pk_add_u16 v20, v20, 4 op_sel_hi:[1,0] clamp\nv_pk_add_u16 v21, v21, 4 op_sel_hi:[1,0] clamp\nds_write_b64 v4, v[20:21] offset:3072\n" \
  "s_setprio 0\ns_setpc_b64 s[90:91]\n51:\ns_movk_i32 s84, 0x4000\n53:\nds_read_b32 v23, v9 offset:32\ns_sub_u32 s84, s84, 1\ns_cbranch_scc1 52b\n" \
  "s_waitcnt lgkmcnt(0)\nv_readfirstlane_b32 s85, v23\ns_cmp_lt_u32 s85, 3\ns_cbranch_scc1 53b\ns_branch 52b\n4:\n" \
  "ds_read_b64 v[124:125], v3 offset:1088\nds_read_b64 v[126:127], v3 offset:1096\nds_read_b64 v[120:121], v3 offset:0\n" \
  "ds_read_b64 v[122:123], v3 offset:8\nds_read_b64 v[116:117], v3 offset:64\nds_read_b64 v[118:119], v3 offset:72\n" \
  "ds_read_b64 v[112:113], v3 offset:128\nds_read_b64 v[114:115], v3 offset:136\nds_read_b64 v[108:109], v3 offset:192\n" \
  "ds_read_b64 v[110:111], v3 offset:200\nds_read_b64 v[104:105], v3 offset:256\nds_read_b64 v[106:107], v3 offset:264\n" \
  "ds_read_b64 v[100:101], v3 offset:320\nds_read_b64 v[102:103], v3 offset:328\nds_read_b64 v[96:97], v3 offset:384\n" \
  "ds_read_b64 v[98:99], v3 offset:392\nds_read_b64 v[92:93], v3 offset:448\nds_read_b64 v[94:95], v3 offset:456\n" \
  "ds_read_b64 v[88:89], v3 offset:512\nds_read_b64 v[90:91], v3 offset:520\nds_read_b64 v[84:85], v3 offset:576\n" \
  "ds_read_b64 v[86:87], v3 offset:584\nds_read_b64 v[80:81], v3 offset:640\nds_read_b64 v[82:83], v3 offset:648\n" \
  "ds_read_b64 v[76:77], v3 offset:704\nds_read_b64 v[78:79], v3 offset:712\nds_read_b64 v[72:73], v3 offset:768\n" \
  "ds_read_b64 v[74:75], v3 offset:776\nds_read_b64 v[68:69], v3 offset:832\nds_read_b64 v[70:71], v3 offset:840\n" \
  "ds_read_b64 v[64:65], v3 offset:896\nds_read_b64 v[66:67], v3 offset:904\nds_read_b64 v[60:61], v3 offset:960\n" \
  "ds_read_b64 v[62:63], v3 offset:968\nds_read_b64 v[56:57], v3 offset:1024\nds_read_b64 v[58:59], v3 offset:1032\n" \
  "ds_read_b64 v[52:53], v6 offset:0\nds_read_b64 v[54:55], v6 offset:8\nds_read_b64 v[48:49], v6 offset:64\nds_read_b64 v[50:51], v6 offset:72\n" \
  "ds_read_b64 v[44:45], v6 offset:128\nds_read_b64 v[46:47], v6 offset:136\nds_read_b64 v[40:41], v6 offset:192\n" \
  "ds_read_b64 v[42:43], v6 offset:200\ns_waitcnt lgkmcnt(0)\ns_barrier\nds_read_b128 v[24:27], v2 offset:448\nds_read_b128 v[28:31], v2 offset:960\n" \
  "ds_read_b128 v[32:35], v2 offset:1472\nds_read_b128 v[36:39], v2 offset:1536\ns_waitcnt lgkmcnt(3)\n" \
  "v_mfma_f32_16x16x32_f16 v[12:15], v[124:127], v[24:27], 0\ns_setprio 1\nds_read_b128 v[24:27], v2 offset:1600\ns_waitcnt lgkmcnt(3)\n" \
  "v_mfma_f32_16x16x32_f16 v[16:19], v[124:127], v[28:31], 0\nds_read_b128 v[124:127], v2 offset:1664\ns_waitcnt lgkmcnt(3)\n" \
  "v_mfma_f32_16x16x32_f16 v[12:15], v[120:123], v[32:35], v[12:15]\nds_read_b128 v[28:31], v2 offset:1728\ns_waitcnt lgkmcnt(3)\n" \
  "v_mfma_f32_16x16x32_f16 v[12:15], v[116:119], v[36:39], v[12:15]\nds_read_b128 v[32:35], v2 offset:1792\ns_waitcnt lgkmcnt(3)\n" \
  "v_mfma_f32_16x16x32_f16 v[12:15], v[112:115], v[24:27], v[12:15]\nds_read_b128 v[36:39], v2 offset:1856\ns_waitcnt lgkmcnt(3)\n" \
  "v_mfma_f32_16x16x32_f16 v[12:15], v[108:111], v[124:127], v[12:15]\nds_read_b128 v[24:27], v2 offset:1920\ns_waitcnt lgkmcnt(3)\n" \
  "v_mfma_f32_16x16x32_f16 v[12:15], v[104:107], v[28:31], v[12:15]\nds_read_b128 v[124:127], v2 offset:1984\ns_waitcnt lgkmcnt(3)\n" \
  "v_mfma_f32_16x16x32_f16 v[12:15], v[100:103], v[32:35], v[12:15]\nds_read_b128 v[28:31], v2 offset:2048\ns_waitcnt lgkmcnt(3)\n" \
  "v_mfma_f32_16x16x32_f16 v[12:15], v[96:99], v[36:39], v[12:15]\nds_read_b128 v[32:35], v2 offset:2112\ns_waitcnt lgkmcnt(3)\n" \
  "v_mfma_f32_16x16x32_f16 v[12:15], v[92:95], v[24:27], v[12:15]\nds_read_b128 v[36:39], v2 offset:2176\ns_waitcnt lgkmcnt(3)\n" \
  "v_mfma_f32_16x16x32_f16 v[12:15], v[88:91], v[124:127], v[12:15]\nds_read_b128 v[24:27], v2 offset:2240\n" \
  "v_mfma_f32_16x16x32_f16 v[16:19], v[120:123], v[124:127], v[16:19]\nds_read_b64 v[120:121], v6 offset:256\nds_read_b64 v[122:123], v6 offset:264\n" \
  "s_waitcnt lgkmcnt(4)\nv_mfma_f32_16x16x32_f16 v[12:15], v[84:87], v[28:31], v[12:15]\nds_read_b128 v[124:127], v2 offset:2304\n" \
  "v_mfma_f32_16x16x32_f16 v[16:19], v[116:119], v[28:31], v[16:19]\nds_read_b64 v[116:117], v6 offset:320\nds_read_b64 v[118:119], v6 offset:328\n" \
  "v_mfma_f32_16x16x32_f16 v[12:15], v[80:83], v[32:35], v[12:15]\nds_read_b128 v[28:31], v2 offset:2368\n" \
  "v_mfma_f32_16x16x32_f16 v[16:19], v[112:115], v[32:35], v[16:19]\ns_waitcnt lgkmcnt(6)\n" \
  "v_mfma_f32_16x16x32_f16 v[12:15], v[76:79], v[36:39], v[12:15]\nds_read_b128 v[112:115], v2 offset:2432\n" \
  "v_mfma_f32_16x16x32_f16 v[16:19], v[108:111], v[36:39], v[16:19]\nds_read2_b32 v[32:33], v8 offset0:48 offset1:49\n" \
  "ds_read2_b32 v[34:35], v8 offset0:50 offset1:51\nv_mfma_f32_16x16x32_f16 v[12:15], v[72:75], v[24:27], v[12:15]\n" \
  "ds_read_b128 v[108:111], v2 offset:2496\nv_mfma_f32_16x16x32_f16 v[16:19], v[104:107], v[24:27], v[16:19]\n" \
  "ds_read2_b32 v[36:37], v8 offset0:32 offset1:33\nds_read2_b32 v[38:39], v8 offset0:34 offset1:35\ns_waitcnt lgkmcnt(6)\n" \
  "v_mfma_f32_16x16x32_f16 v[12:15], v[68:71], v[124:127], v[12:15]\nds_read_b128 v[104:107], v2 offset:2560\n" \
  "v_mfma_f32_16x16x32_f16 v[16:19], v[100:103], v[124:127], v[16:19]\nds_read_b128 v[24:27], v2 offset:2624\n" \
  "v_mfma_f32_16x16x32_f16 v[12:15], v[64:67], v[28:31], v[12:15]\nds_read_b128 v[100:103], v2 offset:2688\nds_read_b32 v23, v9 offset:32\n" \
  "v_mfma_f32_16x16x32_f16 v[16:19], v[96:99], v[28:31], v[16:19]\nds_read_b128 v[124:127], v2 offset:2752\ns_waitcnt lgkmcnt(7)\n" \
  "v_mfma_f32_16x16x32_f16 v[12:15], v[60:63], v[112:115], v[12:15]\nds_read_b128 v[96:99], v2 offset:2816\n" \
  "v_mfma_f32_16x16x32_f16 v[16:19], v[92:95], v[112:115], v[16:19]\nds_read_b128 v[28:31], v2 offset:2880\n" \
  "v_mfma_f32_16x16x32_f16 v[12:15], v[56:59], v[108:111], v[12:15]\nds_read_b128 v[92:95], v2 offset:2944\n" \
  "v_mfma_f32_16x16x32_f16 v[16:19], v[88:91], v[108:111], v[16:19]\nds_read_b128 v[112:115], v2 offset:3008\ns_waitcnt lgkmcnt(5)\n" \
  "v_readfirstlane_b32 s85, v23\ns_cmp_lt_u32 s85, 3\ns_cbranch_scc1 61f\n62:\nv_mfma_f32_16x16x32_f16 v[16:19], v[84:87], v[104:107], v[16:19]\n" \
  "ds_read_b64_tr_b16 v[88:89], v5 offset:0\nds_read_b64_tr_b16 v[90:91], v5 offset:32\n" \
  "v_mfma_f32_16x16x32_f16 v[16:19], v[80:83], v[24:27], v[16:19]\nds_read_b64_tr_b16 v[108:109], v5 offset:256\n" \
  "ds_read_b64_tr_b16 v[110:111], v5 offset:288\nv_mfma_f32_16x16x32_f16 v[16:19], v[76:79], v[100:103], v[16:19]\n" \
  "ds_read_b64_tr_b16 v[84:85], v5 offset:512\nds_read_b64_tr_b16 v[86:87], v5 offset:544\ns_waitcnt lgkmcnt(9)\n" \
  "v_mfma_f32_16x16x32_f16 v[16:19], v[72:75], v[124:127], v[16:19]\nds_read_b64_tr_b16 v[104:105], v5 offset:768\n" \
  "ds_read_b64_tr_b16 v[106:107], v5 offset:800\nv_mfma_f32_16x16x32_f16 v[16:19], v[68:71], v[96:99], v[16:19]\n" \
  "ds_read_b64_tr_b16 v[80:81], v5 offset:1024\nds_read_b64_tr_b16 v[82:83], v5 offset:1056\ns_waitcnt lgkmcnt(11)\n" \
  "v_mfma_f32_16x16x32_f16 v[16:19], v[64:67], v[28:31], v[16:19]\nds_read_b64_tr_b16 v[24:25], v5 offset:1280\n" \
  "ds_read_b64_tr_b16 v[26:27], v5 offset:1312\nv_mfma_f32_16x16x32_f16 v[16:19], v[60:63], v[92:95], v[16:19]\n" \
  "ds_read_b64_tr_b16 v[76:77], v5 offset:1536\nds_read_b64_tr_b16 v[78:79], v5 offset:1568\ns_waitcnt lgkmcnt(12)\n" \
  "v_mfma_f32_16x16x32_f16 v[16:19], v[56:59], v[112:115], v[16:19]\nds_read_b64_tr_b16 v[100:101], v5 offset:1792\n" \
  "ds_read_b64_tr_b16 v[102:103], v5 offset:1824\nv_mfma_f32_16x16x32_f16 v[12:15], v[88:91], v[52:55], v[12:15]\ns_waitcnt lgkmcnt(10)\n" \
  "v_mfma_f32_16x16x32_f16 v[12:15], v[108:111], v[48:51], v[12:15]\nds_read_b64_tr_b16 v[72:73], v7 offset:0\n" \
  "ds_read_b64_tr_b16 v[74:75], v7 offset:64\nv_mfma_f32_16x16x32_f16 v[12:15], v[84:87], v[44:47], v[12:15]\n" \
  "ds_read_b64_tr_b16 v[124:125], v7 offset:512\nds_read_b64_tr_b16 v[126:127], v7 offset:576\n" \
  "v_mfma_f32_16x16x32_f16 v[16:19], v[84:87], v[52:55], v[16:19]\ns_waitcnt lgkmcnt(10)\n" \
  "v_mfma_f32_16x16x32_f16 v[12:15], v[104:107], v[40:43], v[12:15]\nv_mfma_f32_16x16x32_f16 v[16:19], v[104:107], v[48:51], v[16:19]\n" \
  "v_mfma_f32_16x16x32_f16 v[12:15], v[80:83], v[120:123], v[12:15]\nv_mfma_f32_16x16x32_f16 v[16:19], v[80:83], v[44:47], v[16:19]\n" \
  "s_waitcnt lgkmcnt(6)\nv_mfma_f32_16x16x32_f16 v[12:15], v[24:27], v[116:119], v[12:15]\n" \
  "v_mfma_f32_16x16x32_f16 v[16:19], v[24:27], v[40:43], v[16:19]\nv_mfma_f32_16x16x32_f16 v[16:19], v[76:79], v[120:123], v[16:19]\n" \
  "s_waitcnt lgkmcnt(2)\nv_mfma_f32_16x16x32_f16 v[16:19], v[100:103], v[116:119], v[16:19]\n" \
  "v_mfma_f32_16x16x32_f16 v[12:15], v[72:75], v[32:35], v[12:15]\nv_mfma_f32_16x16x32_f16 v[16:19], v[72:75], v[36:39], v[16:19]\n" \
  "s_waitcnt lgkmcnt(0)\nv_mfma_f32_16x16x32_f16 v[16:19], v[124:127], v[32:35], v[16:19]\ns_nop 4\nv_cvt_pkrtz_f16_f32 v20, v12, v13\n" \
  "v_cvt_pkrtz_f16_f32 v21, v14, v15\nv_pk_add_u16 v20, v20, 4 op_sel_hi:[1,0] clamp\nv_pk_add_u16 v21, v21, 4 op_sel_hi:[1,0] clamp\n" \
  "ds_write_b64 v4, v[20:21] offset:4096\nv_cvt_pkrtz_f16_f32 v20, v16, v17\nv_cvt_pkrtz_f16_f32 v21, v18, v19\n" \
  "v_pk_add_u16 v20, v20, 4 op_sel_hi:[1,0] clamp\nv_pk_add_u16 v21, v21, 4 op_sel_hi:[1,0] clamp\nds_write_b64 v4, v[20:21] offset:5120\n" \
  "s_setprio 0\ns_setpc_b64 s[90:91]\n61:\ns_movk_i32 s84, 0x4000\n63:\nds_read_b32 v23, v9 offset:32\ns_sub_u32 s84, s84, 1\ns_cbranch_scc1 62b\n" \
  "s_waitcnt lgkmcnt(0)\nv_readfirstlane_b32 s85, v23\ns_cmp_lt_u32 s85, 3\ns_cbranch_scc1 63b\ns_branch 62b\n5:\n" \
  "ds_read_b64 v[124:125], v3 offset:1088\nds_read_b64 v[126:127], v3 offset:1096\nds_read_b64 v[120:121], v3 offset:0\n" \
  "ds_read_b64 v[122:123], v3 offset:8\nds_read_b64 v[116:117], v3 offset:64\nds_read_b64 v[118:119], v3 offset:72\n" \
  "ds_read_b64 v[112:113], v3 offset:128\nds_read_b64 v[114:115], v3 offset:136\nds_read_b64 v[108:109], v3 offset:192\n" \
  "ds_read_b64 v[110:111], v3 offset:200\nds_read_b64 v[104:105], v3 offset:256\nds_read_b64 v[106:107], v3 offset:264\n" \
  "ds_read_b64 v[100:101], v3 offset:320\nds_read_b64 v[102:103], v3 offset:328\nds_read_b64 v[96:97], v3 offset:384\n" \
  "ds_read_b64 v[98:99], v3 offset:392\nds_read_b64 v[92:93], v3 offset:448\nds_read_b64 v[94:95], v3 offset:456\n" \
  "ds_read_b64 v[88:89], v3 offset:512\nds_read_b64 v[90:91], v3 offset:520\nds_read_b64 v[84:85], v3 offset:576\n" \
  "ds_read_b64 v[86:87], v3 offset:584\nds_read_b64 v[80:81], v3 offset:640\nds_read_b64 v[82:83], v3 offset:648\n" \
  "ds_read_b64 v[76:77], v3 offset:704\nds_read_b64 v[78:79], v3 offset:712\nds_read_b64 v[72:73], v3 offset:768\n" \
  "ds_read_b64 v[74:75], v3 offset:776\nds_read_b64 v[68:69], v3 offset:832\nds_read_b64 v[70:71], v3 offset:840\n" \
  "ds_read_b64 v[64:65], v3 offset:896\nds_read_b64 v[66:67], v3 offset:904\nds_read_b64 v[60:61], v3 offset:960\n" \
  "ds_read_b64 v[62:63], v3 offset:968\nds_read_b64 v[56:57], v3 offset:1024\nds_read_b64 v[58:59], v3 offset:1032\n" \
  "ds_read_b64 v[52:53], v6 offset:0\nds_read_b64 v[54:55], v6 offset:8\nds_read_b64 v[48:49], v6 offset:64\nds_read_b64 v[50:51], v6 offset:72\n" \
  "ds_read_b64 v[44:45], v6 offset:128\nds_read_b64 v[46:47], v6 offset:136\nds_read_b64 v[40:41], v6 offset:192\n" \
  "ds_read_b64 v[42:43], v6 offset:200\ns_waitcnt lgkmcnt(0)\ns_barrier\nds_read_b128 v[24:27], v2 offset:1472\n" \
  "ds_read_b128 v[28:31], v2 offset:1984\nds_read_b128 v[32:35], v2 offset:2496\nds_read_b128 v[36:39], v2 offset:2560\ns_waitcnt lgkmcnt(3)\n" \
  "v_mfma_f32_16x16x32_f16 v[12:15], v[124:127], v[24:27], 0\ns_setprio 1\nds_read_b128 v[24:27], v2 offset:2624\ns_waitcnt lgkmcnt(3)\n" \
  "v_mfma_f32_16x16x32_f16 v[16:19], v[124:127], v[28:31], 0\nds_read_b128 v[124:127], v2 offset:2688\ns_waitcnt lgkmcnt(3)\n" \
  "v_mfma_f32_16x16x32_f16 v[12:15], v[120:123], v[32:35], v[12:15]\nds_read_b128 v[28:31], v2 offset:2752\ns_waitcnt lgkmcnt(3)\n" \
  "v_mfma_f32_16x16x32_f16 v[12:15], v[116:119], v[36:39], v[12:15]\nds_read_b128 v[32:35], v2 offset:2816\ns_waitcnt lgkmcnt(3)\n" \
  "v_mfma_f32_16x16x32_f16 v[12:15], v[112:115], v[24:27], v[12:15]\nds_read_b128 v[36:39], v2 offset:2880\ns_waitcnt lgkmcnt(3)\n" \
  "v_mfma_f32_16x16x32_f16 v[12:15], v[108:111], v[124:127], v[12:15]\nds_read_b128 v[24:27], v2 offset:2944\ns_waitcnt lgkmcnt(3)\n" \
  "v_mfma_f32_16x16x32_f16 v[12:15], v[104:107], v[28:31], v[12:15]\nds_read_b128 v[124:127], v2 offset:3008\ns_waitcnt lgkmcnt(3)\n" \
  "v_mfma_f32_16x16x32_f16 v[12:15], v[100:103], v[32:35], v[12:15]\nds_read_b128 v[28:31], v2 offset:3072\ns_waitcnt lgkmcnt(3)\n" \
  "v_mfma_f32_16x16x32_f16 v[12:15], v[96:99], v[36:39], v[12:15]\nds_read_b128 v[32:35], v2 offset:3136\ns_waitcnt lgkmcnt(3)\n" \
  "v_mfma_f32_16x16x32_f16 v[12:15], v[92:95], v[24:27], v[12:15]\nds_read_b128 v[36:39], v2 offset:3200\ns_waitcnt lgkmcnt(3)\n" \
  "v_mfma_f32_16x16x32_f16 v[12:15], v[88:91], v[124:127], v[12:15]\nds_read_b128 v[24:27], v2 offset:3264\n" \
  "v_mfma_f32_16x16x32_f16 v[16:19], v[120:123], v[124:127], v[16:19]\nds_read_b64 v[120:121], v6 offset:256\nds_read_b64 v[122:123], v6 offset:264\n" \
  "s_waitcnt lgkmcnt(4)\nv_mfma_f32_16x16x32_f16 v[12:15], v[84:87], v[28:31], v[12:15]\nds_read_b128 v[124:127], v2 offset:3328\n" \
  "v_mfma_f32_16x16x32_f16 v[16:19], v[116:119], v[28:31], v[16:19]\nds_read_b64 v[116:117], v6 offset:320\nds_read_b64 v[118:119], v6 offset:328\n" \
  "v_mfma_f32_16x16x32_f16 v[12:15], v[80:83], v[32:35], v[12:15]\nds_read_b128 v[28:31], v2 offset:3392\n" \
  "v_mfma_f32_16x16x32_f16 v[16:19], v[112:115], v[32:35], v[16:19]\ns_waitcnt lgkmcnt(6)\n" \
  "v_mfma_f32_16x16x32_f16 v[12:15], v[76:79], v[36:39], v[12:15]\nds_read_b128 v[112:115], v2 offset:3456\n" \
  "v_mfma_f32_16x16x32_f16 v[16:19], v[108:111], v[36:39], v[16:19]\nds_read2_b32 v[32:33], v8 offset0:16 offset1:17\n" \
  "ds_read2_b32 v[34:35], v8 offset0:18 offset1:19\nv_mfma_f32_16x16x32_f16 v[12:15], v[72:75], v[24:27], v[12:15]\n" \
  "ds_read_b128 v[108:111], v2 offset:3520\nv_mfma_f32_16x16x32_f16 v[16:19], v[104:107], v[24:27], v[16:19]\n" \
  "ds_read2_b32 v[36:37], v8 offset0:0 offset1:1\nds_read2_b32 v[38:39], v8 offset0:2 offset1:3\ns_waitcnt lgkmcnt(6)\n" \
  "v_mfma_f32_16x16x32_f16 v[12:15], v[68:71], v[124:127], v[12:15]\nds_read_b128 v[104:107], v2 offset:3584\n" \
  "v_mfma_f32_16x16x32_f16 v[16:19], v[100:103], v[124:127], v[16:19]\nds_read_b128 v[24:27], v2 offset:3648\n" \
  "ds_read2_b32 v[100:101], v8 offset0:32 offset1:33\nds_read2_b32 v[102:103], v8 offset0:34 offset1:35\n" \
  "v_mfma_f32_16x16x32_f16 v[12:15], v[64:67], v[28:31], v[12:15]\nds_read_b128 v[124:127], v2 offset:3712\nds_read_b32 v23, v9 offset:32\n" \
  "v_mfma_f32_16x16x32_f16 v[16:19], v[96:99], v[28:31], v[16:19]\nds_read_b128 v[96:99], v2 offset:3776\ns_waitcnt lgkmcnt(9)\n" \
  "v_mfma_f32_16x16x32_f16 v[12:15], v[60:63], v[112:115], v[12:15]\nds_read_b128 v[28:31], v2 offset:3840\n" \
  "v_mfma_f32_16x16x32_f16 v[16:19], v[92:95], v[112:115], v[16:19]\nds_read_b128 v[92:95], v2 offset:3904\n" \
  "v_mfma_f32_16x16x32_f16 v[12:15], v[56:59], v[108:111], v[12:15]\nds_read_b128 v[112:115], v2 offset:3968\n" \
  "v_mfma_f32_16x16x32_f16 v[16:19], v[88:91], v[108:111], v[16:19]\nds_read_b128 v[88:91], v2 offset:4032\ns_waitcnt lgkmcnt(5)\n" \
  "v_readfirstlane_b32 s85, v23\ns_cmp_lt_u32 s85, 3\ns_cbranch_scc1 71f\n72:\nv_mfma_f32_16x16x32_f16 v[16:19], v[84:87], v[104:107], v[16:19]\n" \
  "ds_read_b64_tr_b16 v[108:109], v5 offset:1024\nds_read_b64_tr_b16 v[110:111], v5 offset:1056\n" \
  "v_mfma_f32_16x16x32_f16 v[16:19], v[80:83], v[24:27], v[16:19]\nds_read_b64_tr_b16 v[84:85], v5 offset:1280\n" \
  "ds_read_b64_tr_b16 v[86:87], v5 offset:1312\nv_mfma_f32_16x16x32_f16 v[16:19], v[76:79], v[124:127], v[16:19]\n" \
  "ds_read_b64_tr_b16 v[104:105], v5 offset:1536\nds_read_b64_tr_b16 v[106:107], v5 offset:1568\ns_waitcnt lgkmcnt(9)\n" \
  "v_mfma_f32_16x16x32_f16 v[16:19], v[72:75], v[96:99], v[16:19]\nds_read_b64_tr_b16 v[80:81], v5 offset:1792\n" \
  "ds_read_b64_tr_b16 v[82:83], v5 offset:1824\nv_mfma_f32_16x16x32_f16 v[16:19], v[68:71], v[28:31], v[16:19]\n" \
  "ds_read_b64_tr_b16 v[24:25], v5 offset:2048\nds_read_b64_tr_b16 v[26:27], v5 offset:2080\ns_waitcnt lgkmcnt(11)\n" \
  "v_mfma_f32_16x16x32_f16 v[16:19], v[64:67], v[92:95], v[16:19]\nds_read_b64_tr_b16 v[76:77], v5 offset:2304\n" \
  "ds_read_b64_tr_b16 v[78:79], v5 offset:2336\nv_mfma_f32_16x16x32_f16 v[16:19], v[60:63], v[112:115], v[16:19]\n" \
  "ds_read_b64_tr_b16 v[124:125], v5 offset:2560\nds_read_b64_tr_b16 v[126:127], v5 offset:2592\ns_waitcnt lgkmcnt(12)\n" \
  "v_mfma_f32_16x16x32_f16 v[16:19], v[56:59], v[88:91], v[16:19]\nds_read_b64_tr_b16 v[72:73], v5 offset:2816\n" \
  "ds_read_b64_tr_b16 v[74:75], v5 offset:2848\nv_mfma_f32_16x16x32_f16 v[12:15], v[108:111], v[52:55], v[12:15]\ns_waitcnt lgkmcnt(10)\n" \
  "v_mfma_f32_16x16x32_f16 v[12:15], v[84:87], v[48:51], v[12:15]\nds_read_b64_tr_b16 v[96:97], v7 offset:0\n" \
  "ds_read_b64_tr_b16 v[98:99], v7 offset:64\nv_mfma_f32_16x16x32_f16 v[12:15], v[104:107], v[44:47], v[12:15]\n" \
  "ds_read_b64_tr_b16 v[68:69], v7 offset:512\nds_read_b64_tr_b16 v[70:71], v7 offset:576\n" \
  "v_mfma_f32_16x16x32_f16 v[16:19], v[104:107], v[52:55], v[16:19]\ns_waitcnt lgkmcnt(10)\n" \
  "v_mfma_f32_16x16x32_f16 v[12:15], v[80:83], v[40:43], v[12:15]\nds_read_b64_tr_b16 v[28:29], v7 offset:1024\n" \
  "ds_read_b64_tr_b16 v[30:31], v7 offset:1088\nv_mfma_f32_16x16x32_f16 v[16:19], v[80:83], v[48:51], v[16:19]\n" \
  "ds_read2_b32 v[64:65], v8 offset0:48 offset1:49\nds_read2_b32 v[66:67], v8 offset0:50 offset1:51\n" \
  "v_mfma_f32_16x16x32_f16 v[12:15], v[24:27], v[120:123], v[12:15]\nv_mfma_f32_16x16x32_f16 v[16:19], v[24:27], v[44:47], v[16:19]\n" \
  "s_waitcnt lgkmcnt(10)\nv_mfma_f32_16x16x32_f16 v[12:15], v[76:79], v[116:119], v[12:15]\nds_read_b64_tr_b16 v[92:93], v7 offset:1536\n" \
  "ds_read_b64_tr_b16 v[94:95], v7 offset:1600\nv_mfma_f32_16x16x32_f16 v[16:19], v[76:79], v[40:43], v[16:19]\n" \
  "v_mfma_f32_16x16x32_f16 v[16:19], v[124:127], v[120:123], v[16:19]\ns_waitcnt lgkmcnt(8)\n" \
  "v_mfma_f32_16x16x32_f16 v[16:19], v[72:75], v[116:119], v[16:19]\nv_mfma_f32_16x16x32_f16 v[12:15], v[96:99], v[32:35], v[12:15]\n" \
  "v_mfma_f32_16x16x32_f16 v[16:19], v[96:99], v[36:39], v[16:19]\ns_waitcnt lgkmcnt(4)\n" \
  "v_mfma_f32_16x16x32_f16 v[12:15], v[68:71], v[100:103], v[12:15]\nv_mfma_f32_16x16x32_f16 v[16:19], v[68:71], v[32:35], v[16:19]\n" \
  "s_waitcnt lgkmcnt(2)\nv_mfma_f32_16x16x32_f16 v[12:15], v[28:31], v[64:67], v[12:15]\n" \
  "v_mfma_f32_16x16x32_f16 v[16:19], v[28:31], v[100:103], v[16:19]\ns_waitcnt lgkmcnt(0)\n" \
  "v_mfma_f32_16x16x32_f16 v[16:19], v[92:95], v[64:67], v[16:19]\ns_nop 4\nv_cvt_pkrtz_f16_f32 v20, v12, v13\nv_cvt_pkrtz_f16_f32 v21, v14, v15\n" \
  "v_pk_add_u16 v20, v20, 4 op_sel_hi:[1,0] clamp\nv_pk_add_u16 v21, v21, 4 op_sel_hi:[1,0] clamp\nds_write_b64 v4, v[20:21] offset:6144\n" \
  "v_cvt_pkrtz_f16_f32 v20, v16, v17\nv_cvt_pkrtz_f16_f32 v21, v18, v19\nv_pk_add_u16 v20, v20, 4 op_sel_hi:[1,0] clamp\n" \
  "v_pk_add_u16 v21, v21, 4 op_sel_hi:[1,0] clamp\nds_write_b64 v4, v[20:21] offset:7168\ns_setprio 0\ns_setpc_b64 s[90:91]\n71:\n" \
  "s_movk_i32 s84, 0x4000\n73:\nds_read_b32 v23, v9 offset:32\ns_sub_u32 s84, s84, 1\ns_cbranch_scc1 72b\ns_waitcnt lgkmcnt(0)\n" \
  "v_readfirstlane_b32 s85, v23\ns_cmp_lt_u32 s85, 3\ns_cbranch_scc1 73b\ns_branch 72b\n"
#define CONV_CLOB "memory", "scc", "s84", "s85", "s90", "s91", "v12", "v13", "v14", "v15", "v16", "v17", "v18", "v19", "v20", "v21", "v22", "v23", "v24", "v25", "v26", "v27", "v28", "v29", "v30", "v31", "v32", "v33", "v34", "v35", "v36", "v37", "v38", "v39", "v40", "v41", "v42", "v43", "v44", "v45", "v46", "v47", "v48", "v49", "v50", "v51", "v52", "v53", "v54", "v55", "v56", "v57", "v58", "v59", "v60", "v61", "v62", "v63", "v64", "v65", "v66", "v67", "v68", "v69", "v70", "v71", "v72", "v73", "v74", "v75", "v76", "v77", "v78", "v79", "v80", "v81", "v82", "v83", "v84", "v85", "v86", "v87", "v88", "v89", "v90", "v91", "v92", "v93", "v94", "v95", "v96", "v97", "v98", "v99", "v100", "v101", "v102", "v103", "v104", "v105", "v106", "v107", "v108", "v109", "v110", "v111", "v112", "v113", "v114", "v115", "v116", "v117", "v118", "v119", "v120", "v121", "v122", "v123", "v124", "v125", "v126", "v127"
constexpr int TDP = 1216, T_3 = 4 * TDP, T_4 = T_3 + 512, TB = T_4 + 320;
constexpr int C_U = 480, UB = C_U + 4096 + 96, C_G5 = 0, C_G3 = 3200, C_G9 = 6400, C_G7 = C_G9 + 2096, GB = C_G7 + 2080 + 112;
constexpr int O_FL = 2 * TB, O_UL = O_FL + 64, O_IM = O_UL + 2 * UB, O_ST = O_IM + 2 * GB;
constexpr int X_TB = 10240, X_UL = X_TB + TB, X_IM = X_UL + UB, X_ST = X_IM + GB;
constexpr int X_WR = X_IM + 128 * IP;
#define X_RW(w) (LW + 9216 * (2 + ((w) >> 1)) + 2560 * ((w) & 1))
constexpr int RP = 1216;
constexpr int LW = 36864 + 512, LA = LW, LM = LW + 36864;
constexpr int L_SC = LM + 2048, SMEM = O_IM + 256 * IP;
static_assert(L_SC + 1152 <= SMEM && O_ST + 8192 <= SMEM && X_IM + 128 * IP <= LM && X_ST + 8192 <= LM && UB % 128 == 64 && GB % 128 == 64 && SMEM <= 79872, "");
struct TOps { uint32_t xa, xb, ya, pd, pf, pg, pe; };
__device__ __forceinline__ void table_ops(uint32_t im, uint32_t tb, int ct, int lane, TOps& t) {
  const int j = lane & 15, kq = lane >> 4, q4 = (lane >> 2) & 3, p4 = lane & 3, jj = j + 16 * ct;
  const uint32_t sa = im + (uint32_t)(IP * (lane >> 2));
  t.xa = sa + (uint32_t)(I_X + 8 * p4); t.xb = sa + (uint32_t)(I_X + 32 + 8 * (p4 & 1)); t.ya = sa + (uint32_t)(32 * ct + 8 * p4);
  t.pd = tb + (uint32_t)(2 * (541 - (8 * jj + 4 * (kq & 1) + 256 * (kq >> 1))));
  t.pf = tb + (uint32_t)((kq & 1) ? T_4 + 2 * (63 - jj) : T_3 + 2 * (127 - 2 * jj));
  t.pg = t.pf - ((kq & 1) ? 64 : 2);
  t.pe = tb + (uint32_t)(p4 * (TDP + 2) + ((lane < 12) ? 2 * (32 - q4) : 2 * (572 + (q4 & 1))));
  asm volatile("" : "+v"(t.xa), "+v"(t.xb), "+v"(t.ya), "+v"(t.pd), "+v"(t.pf), "+v"(t.pg), "+v"(t.pe));
}
typedef __attribute__((address_space(3))) char* lptr;
#define LP(T, a) ((__attribute__((address_space(3))) T*)(uintptr_t)(a))
__device__ __forceinline__ uint32_t table_tile(const TOps& t, int ct, int lane, uint32_t evp, float dsk TSP) {
  u32x4 ev;
  f32x4 ad[2], af[2];
  asm volatile(T_ASM : "=&v"(ad[0]), "=&v"(ad[1]), "=&v"(af[0]), "=&v"(af[1]), "=&v"(ev) : "v"(t.xa), "v"(t.xb), "v"(t.ya), "v"(evp) : T_CLOB);
  const int e0 = (int)(ev[0] + ev[2]), e1 = (int)(ev[1] + ev[3]);
  const int em = min(max(max(max(e0, e1), (int)((__float_as_uint(dsk) >> 23) & 0xffu) - 133), -110), 115);
  const int ts = ((uint32_t)(em + 12) <= 8u) ? 1 : -3 - em;
  float s0 = __builtin_amdgcn_ldexpf(1.f, e0 - 29 + ts), s1 = __builtin_amdgcn_ldexpf(1.f, e1 - 29 + ts);
  float d0 = (ct == 0 && lane == 0) ? __builtin_amdgcn_ldexpf(dsk, ts) : 0.f;
  asm volatile("s_nop 1" : "+v"(ad[0]), "+v"(ad[1]), "+v"(af[0]), "+v"(af[1]), "+v"(s0), "+v"(s1), "+v"(d0));
  const f32x4 k = ad[0] * s0 + (f32x4){d0, 0.f, 0.f, 0.f} + ad[1] * s1, f = af[0] * s0 + af[1] * s1;
  auto pk = [](float a, float b) { uint32_t r; asm("v_cvt_pk_f16_f32 %0, %1, %2" : "=v"(r) : "v"(a), "v"(b)); return r; };
  asm volatile("s_nop 0" : : "v"(k), "v"(f));
  const uint32_t p32 = pk(k[3], k[2]), p10 = pk(k[1], k[0]), p21 = pk(k[2], k[1]), p01 = pk(f[0], f[1]), p23 = pk(f[2], f[3]);
  *LP(u32x2, t.pd + 3 * TDP + 6) = (u32x2){p32, p10};
  *LP(uint32_t, t.pd + TDP + 2) = p32; *LP(uint32_t, t.pd + TDP + 6) = p10;
  *LP(uint32_t, t.pd + 2) = p21; *LP(uint32_t, t.pd + 2 * TDP + 6) = p21;
  *LP(u16, t.pd) = (u16)p32; *LP(u16, t.pd + 2 * TDP + 4) = (u16)p32;
  asm volatile("ds_write_b16_d16_hi %0, %1 offset:6\n\tds_write_b16_d16_hi %0, %1 offset:%2" : : "v"(t.pd), "v"(p10), "n"(2 * TDP + 10) : "memory");
  if (ct) {
    const uint32_t va = (uint32_t)__builtin_amdgcn_readlane((int)p32, 63), vb = (uint32_t)__builtin_amdgcn_readlane((int)p23, 15) >> 16;
    *LP(u16, t.pe) = (u16)((lane < 12) ? va : vb);
  }
  if (lane < 32) {
    *LP(u16, t.pf + 128) = (u16)p01; *LP(u16, t.pg + 128) = (u16)(p01 >> 16);
    *LP(u16, t.pf) = (u16)p23; *LP(u16, t.pg) = (u16)(p23 >> 16);
  }
  return (uint32_t)(127 + 5 - ts) << 23;
}
__device__ __forceinline__ uint32_t hadd(uint32_t a, uint32_t b) { return __builtin_bit_cast(uint32_t, __builtin_bit_cast(f16x2, a) + __builtin_bit_cast(f16x2, b)); }
template <bool FULL> __device__ __forceinline__ void sums(char* sm, const u32x4 p, const u32x4 c) {
  const uint32_t D[8] = {p[0], p[1], p[2], p[3], c[0], c[1], c[2], c[3]};
  uint32_t O[7];
#pragma unroll
  for (int j = 0; j < 7; ++j) O[j] = __builtin_amdgcn_alignbit(D[j + 1], D[j], 16);
  u32x4 a, b;
#pragma unroll
  for (int j = 0; j < 4; ++j) a[j] = hadd(D[j + 2], hadd(O[j + 1], O[j + 2]));
  *(u32x4*)(sm + C_G3 + 32) = a;
#pragma unroll
  for (int j = 0; j < 4; ++j) b[j] = hadd(a[j], hadd(D[j + 1], D[j + 3]));
  *(u32x4*)(sm + C_G5 + 32) = b;
  if (FULL) {
#pragma unroll
    for (int j = 0; j < 4; ++j) a[j] = hadd(b[j], hadd(O[j], O[j + 3]));
    *(u32x4*)(sm + C_G7 + 32) = a;
#pragma unroll
    for (int j = 0; j < 4; ++j) b[j] = hadd(a[j], hadd(D[j], D[j + 4]));
    *(u32x4*)(sm + C_G9 + 32) = b;
  }
}
__device__ __forceinline__ void count_up(uint32_t a) {
  uint32_t t;
  asm volatile("s_mov_b64 exec, 1\n\tv_mov_b32 %0, 1\n\tds_add_u32 %1, %0\n\ts_mov_b64 exec, -1" : "=&v"(t) : "v"(a) : "memory");
}
__device__ __forceinline__ int pad_at(int r) { return 16 * (r & 1) + 3200 * (r >> 1) - ((r >= 6) ? 3200 - 2096 : 0); }
struct COps { uint32_t b, a, o, g3, k3, g4, k4, f; };
__device__ __forceinline__ void conv_ops(uint32_t tb, uint32_t ub, uint32_t cb, int lane, COps& e) {
  const uint32_t j = lane & 15, kq = lane >> 4, q4 = (lane >> 2) & 3, p4 = lane & 3;
  e.b = ub + 32u * j + 16u * kq;
  e.a = tb + p4 * TDP + 32u - 8u * q4 + 16u * kq;
  e.g3 = cb + C_G5 + 8 + 64u * kq + 8u * (q4 + p4) + (q4 & 1u) * 3200u;
  e.g4 = cb + C_G9 + 8 + 128u * kq + 16u * q4 + 8u * p4 + (q4 & 1u) * 2096u;
  e.k3 = tb + T_3 + 120 - 8u * j + 16u * kq;
  e.k4 = tb + T_4 + 60 - 4u * j + 16u * kq;
  asm volatile("" : "+v"(e.b), "+v"(e.a), "+v"(e.g3), "+v"(e.g4), "+v"(e.k3), "+v"(e.k4));
}
#define CONV_RUN(prog, e, ...) do { int prog_ = (prog); \
    asm volatile("s_call_b64 s[90:91], kf_conv" : "+{s89}"(prog_), "+{v2}"(e.b), "+{v3}"(e.a), "+{v4}"(e.o), "+{v5}"(e.g3), "+{v6}"(e.k3), "+{v7}"(e.g4), \
                 "+{v8}"(e.k4), "+{v9}"(e.f) __VA_ARGS__ : : CONV_CLOB); } while (0)

template <bool E>
__device__ __forceinline__ void ln_stats(const uint32_t* v, int lane, float sc, f32x2* t, f32x4 z, f32x2& rc, f32x2& nc, float& e) {
  float s1a = z[0], s2a = z[1], s1b = z[2], s2b = z[3];
#pragma unroll
  for (int k = 0; k < 4; ++k) {
    s1a = dot2(v[k], 0x3f803f80u, s1a); s2a = dot2(v[k], v[k], s2a);
    s1b = dot2(v[4 + k], 0x3f803f80u, s1b); s2b = dot2(v[4 + k], v[4 + k], s2b);
  }
#pragma unroll
  for (int k = 0; k < 9; ++k) t[k] = (f32x2){bflo(v[k]), bfhi(v[k])};
  const float rs = row_sum(fold16(dot2(v[8], 0x3f803f80u, fold32(s1a, s1b)), dot2(v[8], v[8], fold32(s2a, s2b))));
  if (E) { e = row_sum(e); e += __int_as_float(__builtin_amdgcn_update_dpp(0, __float_as_int(e), 0x142, 0xa, 0xf, false)); }
  const f32x2 mean = (f32x2){rdl(rs, 15), rdl(rs, 47)} * (1.f / D_), m2 = mean * mean;
  f32x2 var = (f32x2){rdl(rs, 31), rdl(rs, 63)} * (1.f / D_) - m2;
  asm volatile("" : "+v"(t[0]), "+v"(t[1]), "+v"(t[2]), "+v"(t[3]), "+v"(t[4]), "+v"(t[5]), "+v"(t[6]), "+v"(t[7]), "+v"(t[8]));
  if (__builtin_expect(!(m2[0] <= 16.f * var[0] && m2[1] <= 16.f * var[1]), 0)) {
    float q[3] = {0.f, 0.f, 0.f};
#pragma unroll
    for (int k = 0; k < 9; ++k) {
      const f32x2 a = t[k] - ((k < 4 || (k == 8 && lane < 32)) ? mean[0] : mean[1]);
      q[k >> 2] += a[0] * a[0] + a[1] * a[1];
    }
    float qa = q[0] + ((lane < 32) ? q[2] : 0.f), qb = q[1] + ((lane < 32) ? 0.f : q[2]);
    wave_sum2(qa, qb);
    var = (f32x2){qa, qb} * (1.f / D_);
  }
  const uint32_t mp = pack2(mean[0], mean[1]), vp = pack2(var[0], var[1]);
  const f32x2 ve = (f32x2){bflo(vp), bfhi(vp)} + 1e-5f;
  const uint32_t e2 = pack2(ve[0], ve[1]);
  const uint32_t sp = pack2(__builtin_amdgcn_sqrtf(bflo(e2)), __builtin_amdgcn_sqrtf(bfhi(e2)));
  rc = (f32x2){__builtin_amdgcn_rcpf(bflo(sp)), __builtin_amdgcn_rcpf(bfhi(sp))} * sc;
  nc = -(f32x2){bflo(mp), bfhi(mp)} * rc;
}
template <bool H>
__device__ __forceinline__ void ln_apply(const f32x2* t, int lane, f32x2 rc, f32x2 nc, uint32_t* c) {
  const float nm[3] = {nc[0], nc[1], (lane < 32) ? nc[0] : nc[1]}, rr[3] = {rc[0], rc[1], (lane < 32) ? rc[0] : rc[1]};
  f32x2 dd[9];
#pragma unroll
  for (int k = 0; k < 9; ++k) dd[k] = __builtin_elementwise_fma(t[k], (f32x2){rr[k >> 2], rr[k >> 2]}, (f32x2){nm[k >> 2], nm[k >> 2]});
  asm volatile("" : "+v"(dd[0]), "+v"(dd[1]), "+v"(dd[2]), "+v"(dd[3]), "+v"(dd[4]), "+v"(dd[5]), "+v"(dd[6]), "+v"(dd[7]), "+v"(dd[8]));
#pragma unroll
  for (int k = 0; k < 9; ++k) {
    const f32x2 d = dd[k];
    typedef __attribute__((ext_vector_type(2))) uint16_t u16x2;
    if (H) c[k] = __builtin_bit_cast(uint32_t, __builtin_elementwise_add_sat(__builtin_bit_cast(u16x2, __builtin_amdgcn_cvt_pkrtz(d[0], d[1])), (u16x2){4, 4}));
    else c[k] = pack2(d[0], d[1]);
  }
}
__device__ __forceinline__ uint32_t med3u(uint32_t a, uint32_t b, uint32_t c) { return max(min(a, b), min(max(a, b), c)); }
__device__ __forceinline__ uint32_t oct_maxu(uint32_t v) {
  v = max(v, (uint32_t)DPP_I((int)v, 0xb1, 0xf));
  v = max(v, (uint32_t)DPP_I((int)v, 0x4e, 0xf));
  v = max(v, (uint32_t)DPP_I((int)v, 0x141, 0xf));
  return v;
}

#define KARGS const u16* __restrict__ x, const u16* __restrict__ Are, const u16* __restrict__ Aim, const u16* __restrict__ Bre, \
    const u16* __restrict__ Bim, const u16* __restrict__ Cre, const u16* __restrict__ Cim, const u16* __restrict__ logdt, \
    const u16* __restrict__ Dskip, char* __restrict__ ws, const u16* __restrict__ wr, const u16* __restrict__ wg, \
    const u16* __restrict__ wu, const u16* __restrict__ wd, u16* __restrict__ out
#define KPASS x, Are, Aim, Bre, Bim, Cre, Cim, logdt, Dskip, ws, wr, wg, wu, wd, out
template <int ROLE, int FFN = -1>
__device__ __forceinline__ void role_body(char* smem, KARGS) {
  const int tid = threadIdx.x, lane = tid & 63;
  const int w = __builtin_amdgcn_readfirstlane(tid >> 6), wl = w & 3;
  const int b = blockIdx.x & 255, bx = b & 7, bj = b >> 3;
  constexpr bool isl = ROLE == 1 || ROLE == 2;
  const int tg = isl ? 32 * (bj >> 2) + 4 * bx + (bj & 3) : 32 * bx + bj;
  u32x4 xa, xc;
  uint32_t ep, r0;
#define P_WAIT(n, ...) asm volatile("s_waitcnt vmcnt(" #n ")\n\tv_lshlrev_b32 %0, 16, v100\n\tv_lshlrev_b32 %1, 16, v101\n\tv_lshlrev_b32 %2, 16, v102" \
    : "=v"(ldt), "=v"(are), "=v"(aim) : : "v100", "v101", "v102" __VA_ARGS__)
#define P_MORE() asm volatile("v_lshlrev_b32 %0, 16, v103\n\tv_lshlrev_b32 %1, 16, v104\n\tv_lshlrev_b32 %2, 16, v105\n\tv_lshlrev_b32 %3, 16, v106" \
    : "=v"(bre), "=v"(bim), "=v"(cre), "=v"(cim) : : "v103", "v104", "v105", "v106")
  TSD;
  TS(0, 0);
  const uint32_t lb0 = (uint32_t)(uintptr_t)(__attribute__((address_space(3))) char*)smem;
  const int i = lane & 15, kq = lane >> 4;
  const u32x4 ones = {0xffffffffu, 0xffffffffu, 0xffffffffu, 0xffffffffu}, zero4 = {0u, 0u, 0u, 0u};
  auto u_again = [&](u32x4& uw, const u32x4& ut, uint32_t uo) {
#pragma unroll 1
    for (int spin = 0; spin < SPIN_CAP; ++spin) {
      const bool na = ((uw[0] | uw[1] | uw[2] | uw[3]) & UMASK) != 0u;
      if (__builtin_expect(!__any(na), 1)) break;
      TSC(19);
      SLEEP(USLEEP);
      if (na) { LD16W(uw, uo); uw ^= ut; }
    }
  };
  constexpr uint32_t S16 = 0x41800000u, LMASK = 0x00070007u;
  auto v_put = [&](u32x4 v, const u32x4& ut, bool other, uint32_t vo) {
    const bool second = __any(other || ((v[0] | v[1] | v[2] | v[3]) & 0x40004000u) != 0u);
    if (__builtin_expect(second, 0)) v = (v & ~LMASK) | (~ut & LMASK); else v = (v & ~LMASK) | ut;
    ST16(vo, v);
    return second;
  };
  auto v_end = [&](uint32_t T, int n) { DRAIN(); if (lane == 0) ST4(O_DN + 16u * n, T); };
  if constexpr (!isl) {
    const int ch = wl >> 1, tl = tid & 255;
    int* evp = (int*)(smem + O_FL + 16 * ch);
    float ldt, are, aim, dsk;
    if constexpr (ROLE == 0) {
      __builtin_amdgcn_s_setprio(KPRIO);
      COps e;
      conv_ops(lb0 + (uint32_t)(ch * TB), lb0 + (uint32_t)(O_UL + ch * UB), lb0 + (uint32_t)(O_IM + ch * GB), lane, e);
      e.o = lb0 + O_ST + (uint32_t)(16 * ((4 * i + 2 * (kq >> 1) + ch) ^ (i >> 1)) + 8 * (kq & 1));
      e.f = lb0 + O_FL;
      TOps t;
      table_ops(lb0 + (uint32_t)(O_IM + 128 * IP * ch), lb0 + (uint32_t)(TB * ch), wl & 1, lane, t);
      uint32_t pz = lb0 + O_IM + (uint32_t)(((lane >> 3) & 1) * GB + pad_at(lane & 7)), im = lb0 + (uint32_t)(O_IM + IP * tl);
      asm volatile("" : "+v"(e.o), "+v"(e.f), "+v"(pz), "+v"(im));
      float bre, bim, cre, cim;
      P_WAIT(0, , "v103", "v104", "v105", "v106", "v107", "v109");
      asm volatile("" : "={v109}"(ep));
      const uint32_t T = ((uint32_t)__builtin_amdgcn_readfirstlane(ep) + 1u) * 0x9E3779B1u;
      if (w == 3 && lane < 8) ST16(O_KEY + 128u * b + 16u * lane, ones);
      P_MORE();
      asm volatile("v_lshlrev_b32 %0, 16, v107" : "=v"(dsk) : : "v107");
      TS(0, 1);
      evp[wl & 1] = gen_y(ldt, are, aim, bre, bim, cre, cim, im, [] {} TSA);
      TS(0, 8);
      BAR();
      TS(0, 2);
      const uint32_t cv = table_tile(t, wl & 1, lane, lb0 + O_FL + 16 * ch, dsk TSA), cs = (uint32_t)__builtin_amdgcn_readfirstlane(cv);
      *(uint32_t*)(smem + O_FL + 40 + 4 * ch) = cv;
      TS(0, 9);
      gsync0(lb0 + O_FL + 36, 4);
      TS(0, 3);
      *(u32x4*)(smem + O_ST + 16 * tl) = ones;
      *(u32x4*)(smem + O_ST + 4096 + 16 * tl) = ones;
      if (w == 0) *LP(u32x4, pz) = zero4;
      DRAIN();
      CONV_RUN(wl & 1, e);
      if (wl & 1) asm volatile("s_mov_b64 exec, 1\n\tglobal_store_dwordx2 %0, %1, %2 sc1" : : "v"(O_SC + 16u * (2 * tg + ch)), "v"((u32x2){T, cs}), "s"(ws) : "memory");
      TS(0, 5);
    } else {
      __builtin_amdgcn_s_setprio(UPRIO);
      const uint32_t ck0 = (uint32_t)__builtin_amdgcn_s_memtime(), rt0 = (uint32_t)__builtin_amdgcn_s_memrealtime();
#pragma unroll
      for (int k = 0; k < 3; ++k) *(u32x4*)(smem + ((16 * (tl + 256 * k) < O_FL) ? 16 * (tl + 256 * k) : O_FL + 32)) = zero4;
      if (w == 7) *(u32x4*)(smem + O_UL + (lane >> 5) * UB + 16 * min(lane & 31, 29)) = zero4;
      uint32_t uo = u_at(tl >> 1, 2 * tg + (tid & 1)), go = O_SU + (uint32_t)(256 * bx + 32 * wl + (lane & 31) + 4 * (lane & 32)) * 128u;
      uint32_t ul = (uint32_t)(O_UL + (tid & 1) * UB + C_U - 16 + 16 * (tl >> 1)), ug = (uint32_t)(O_IM + (tid & 1) * GB + 16 * (tl >> 1)), so = lb0 + O_ST + 16u * (uint32_t)(lane ^ (lane >> 3));
      uint32_t vo = O_V + (uint32_t)tg * VP + 16u * lane;
      asm volatile("" : "+v"(uo), "+v"(go), "+v"(ul), "+v"(ug), "+v"(so), "+v"(vo));
      P_WAIT(1, , "v109");
      TS(0, 1);
      evp[2 + (wl & 1)] = gen_x(ldt, are, aim, smem + O_IM + IP * tl);
      asm volatile("s_waitcnt vmcnt(0)" : "={v109}"(ep));
      const uint32_t T = ((uint32_t)__builtin_amdgcn_readfirstlane(ep) + 1u) * 0x9E3779B1u;
      TS(0, 8);
      BAR();
      TS(0, 2);
      if (w == 7 && bj >= 24) {
        const uint32_t t_ = ahead(ck0, rt0, VDB);
        ST16(O_SB + (uint32_t)(64 * (lane >> 3) + 8 * bx + bj - 24) * 128u + 16u * (lane & 7), ((u32x4){T, t_, T, t_}));
      }
      u32x4 ut = {utag(T, 0), utag(T, 1), utag(T, 2), utag(T, 3)};
      asm volatile("" : "+v"(ut));
      SLEEP(GSLEEP);
      u32x2 pv;
      pv[1] = gate(ws, go, T);
      TS(0, 3);
      until(latest(pv[1]) + UD);
      TS(0, 7);
      u32x4 uw, ux;
      asm volatile("global_load_dwordx4 %0, %2, %4 sc1\n\tglobal_load_dwordx4 %1, %3, %4 sc1"
                   : "=&v"(uw), "=&v"(ux) : "v"(uo), "v"(uo + 64 * UP * 32), "s"(ws) : "memory");
      __builtin_amdgcn_s_setprio(FPR);
      asm volatile("s_waitcnt vmcnt(1)" : "+v"(uw));
      uw ^= ut;
      *(u32x4*)(smem + ul + 16) = uw;
      asm volatile("s_waitcnt vmcnt(0)" : "+v"(ux));
      TS(0, 11);
      ux ^= ut;
      *(u32x4*)(smem + ul + 2048 + 16) = ux;
      if (__builtin_expect(__any(((uw[0] | uw[1] | uw[2] | uw[3] | ux[0] | ux[1] | ux[2] | ux[3]) & UMASK) != 0u), 0)) {
        u_again(uw, ut, uo);
        u_again(ux, ut, uo + 64 * UP * 32);
        *(u32x4*)(smem + ul + 16) = uw;
        *(u32x4*)(smem + ul + 2048 + 16) = ux;
      }
      BAR();
      TS(0, 4);
      const u32x4 pw = *(const u32x4*)(smem + ul);
      if (wl < 2) {
        const u32x4 px = *(const u32x4*)(smem + ul + 2048);
        sums<true>(smem + ug, pw, uw);
        sums<false>(smem + ug + 2048, px, ux);
      } else sums<true>(smem + ug, pw, uw);
      count_up(lb0 + O_FL + 32);
      __builtin_amdgcn_s_setprio(SPR);
      TS(0, 5);
      const bool other = *(const uint32_t*)(smem + O_FL + 40 + 4 * (lane & 1)) != S16;
      bool second = false;
#pragma unroll
      for (int k = 0; k < 2; ++k) {
        const uint32_t o = ((k ? 0x3247u : 0x1065u) >> (4 * wl) & 7u) * 1024u;
        u32x4 v;
#pragma unroll 1
        for (int spin = 0; spin < SPIN_CAP; ++spin) {
          asm volatile("ds_read_b128 %0, %1\n\ts_waitcnt lgkmcnt(0)" : "=&v"(v) : "v"(so + o) : "memory");
          if (!__any(max4(v) == 0xffffffffu)) break;
          __builtin_amdgcn_s_sleep(VPS);
        }
        TS(0, 12 + k);
        second |= v_put(v, ut, other, vo + o);
        TS(0, 10 - 4 * k);
      }
      if (__builtin_expect(second, 0)) v_end(T, 4 * tg + wl);
    }
    TSE();
  } else {
  uint32_t* cand = (uint32_t*)(smem + LM);
  uint32_t* sel = cand + 128;
  uint32_t* kl = sel + 48;
  uint32_t* cnt = sel + 64;
  const uint32_t ca = lb0 + LM + 4 * (128 + 64);
  u16* hm = (u16*)(cnt + 16);
  uint32_t T;
  const bool ffn = (FFN < 0) ? bj < 18 : FFN, isx = FFN != 1 && bj >= 24;
  const int fb = 18 * bx + bj;
  constexpr bool ln2 = ROLE == 2;
  const int g0 = tg, g2 = tg;
  u32x4 bw[18];
#pragma unroll
  for (int s = 0; s < 9; ++s) bw[s] = (u32x4){0u, 0u, 0u, 0u};
  int tid2 = threadIdx.x;
  uint32_t tv = 0u, tk = 0u;
  auto v_look = [&](uint32_t tu, int lane) {
    uint32_t tm = latest(gate(ws, O_SB + (uint32_t)(64 * bx + lane) * 128u, T)) - V2D;
    if ((int)(tm - (tu + UD + VD2)) < 0) tm = tu + UD + VD2;
    cnt[8] = tm;
    count_up(ca + 52);
  };
  auto v_time = [&] { gwait(ca + 52, 1); tv = (uint32_t)__builtin_amdgcn_readfirstlane(cnt[8]); };
  uint32_t ts1 = 0u;
  auto rest = [&] {
    if (ts1 && (int)((uint32_t)__builtin_amdgcn_s_memrealtime() - ts1) > 0) {
      v_time();
      ts1 = (tv - TSTOP - TAPER) | 1u;
      if (TAPER && (int)((uint32_t)__builtin_amdgcn_s_memrealtime() - ts1) > 0) asm volatile("s_waitcnt vmcnt(%0)" : : "n"(TDEP) : "memory");
      if ((int)((uint32_t)__builtin_amdgcn_s_memrealtime() - (ts1 + TAPER)) > 0) { TS(0, 8); until(tv + RD); ts1 = 0u; }
    }
  };
  auto gu_dma = [&](int q0, int q1, auto depth) {
    const char* gs = (const char*)((w < 2) ? wg : wu) + (size_t)((144 * (w & 1)) * (F_ * 2) + fb * 64);
    const uint32_t gv = (uint32_t)((lane >> 2) * (F_ * 2) + 16 * ((lane & 3) ^ (lane >> 5) ^ (w & 2)));
#pragma unroll 1
    for (int q = q0; q < q1; ++q) {
      const int k = (int)((((bj & 1) ? ORD1 : ((unsigned __int128)0x876543210u << 36 | 0x876543210ull)) >> (4 * (q - (q >= 9 ? 9 : 0)) + (q >= 9 ? 36 : 0))) & 15u), h = q >= 9;
      if (q >= RQ) rest();
      dma16(gs + (size_t)(288 * h + 16 * k) * (F_ * 2), gv, lb0 + LW * h + 9216 * w + 1024 * k);
      if (!BALL || q < RQ || ts1) asm volatile("s_waitcnt vmcnt(%0)" : : "n"(depth.value) : "memory");
    }
  };
  auto down_dma = [&](int k0, int k1, int lane) {
#pragma unroll 1
    for (int k = k0; k < k1; ++k) {
      const int o = 9216 * wl + 2304 * k;
      const char* ds = (const char*)wd + (size_t)(fb * 36864 + o);
      const uint32_t dl = lb0 + o + 32 * ((4 * wl + k + 1) >> 1);
      if (ROLE == 1) rest();
      dma16d(ds, 16u * lane, dl);
      dma16d(ds + 1024, 16u * lane, dl + 1024);
      uint32_t keep;
      asm volatile("s_mov_b32 %0, m0\n\ts_mov_b32 m0, %3\n\ts_mov_b64 exec, 0xffff\n\tglobal_load_lds_dwordx4 %1, %2" DPOL2 "\n\ts_mov_b64 exec, -1\n\ts_mov_b32 m0, %0\n\ts_waitcnt vmcnt(%4)"
                   : "=&s"(keep) : "v"(16u * lane), "s"(ds + 2048), "s"(dl + 2048), "n"(ROLE == 1 ? DDEPTH : 40) : "memory");
    }
  };
  auto gu_read = [&](int h) {
    const uint32_t ra = lb0 + LW * h + (uint32_t)(((lane >> 1) & 1) * 18432 + (8 * kq + ((lane >> 2) & 3)) * 64 + ((w ^ (kq & 1) ^ (lane & 2)) * 16) + 8 * (lane & 1));
#pragma unroll
    for (int s = 0; s < 9; ++s) {
      const u32x2 h0 = trd(ra + 2048 * s), h1 = trd(ra + 2048 * s + 256);
      bw[9 * h + s] = (u32x4){h0[0], h0[1], h1[0], h1[1]};
    }
#pragma unroll
    for (int s = 0; s < 9; ++s) asm volatile("" : "+v"(bw[9 * h + s]));
  };
  const uint32_t vearly = (uint32_t)((VEARLY >> (8 * (g0 >> 5))) & 0xffull);
#define TOUCH(pa) asm volatile("global_load_dword v126, %0, off" DPOL4 : : "v"(pa) : "memory", "v126")
  uint32_t wv[5], xv[9];
  auto rows_xw = [&](int lane) {
    u32x4 wa, xr, xs;
    const uint32_t xo = (uint32_t)((8 * g2 + 2 * wl) * (D_ * 2));
    asm volatile("global_load_dwordx4 %0, %5, %9\n\tglobal_load_dword %1, %6, %9 offset:1024\n\t"
                 "global_load_dwordx4 %2, %7, %10\n\tglobal_load_dwordx4 %3, %7, %10 offset:1152\n\tglobal_load_dword %4, %8, %10 offset:1024\n\ts_waitcnt vmcnt(0)"
                 : "=&v"(wa), "=&v"(wv[4]), "=&v"(xr), "=&v"(xs), "=&v"(xv[8])
                 : "v"(16u * lane), "v"(4u * (lane & 31)), "v"(xo + 16u * lane), "v"(xo + 4u * lane + 32u * (lane & 32)), "s"(wr), "s"(x) : "memory");
#pragma unroll
    for (int k = 0; k < 4; ++k) { wv[k] = wa[k]; xv[k] = xr[k]; xv[4 + k] = xs[k]; }
  };
  auto ln1 = [&](int g, int o, uint32_t c0) {
    uint32_t uo = u_at(g, 64 * wl + lane);
    uint32_t ta = lb0 + (uint32_t)(o + ((lane >> 2) & 3) * RP + (64 * wl + 16 * kq + 4 * (lane & 3)) * 2);
    uint32_t la = (uint32_t)(o + 2 * wl * RP + 16 * lane), lt = (uint32_t)(o + (2 * wl + (lane >> 5)) * RP + 1024 + 4 * (lane & 31));
    uint32_t sg = O_SU + (uint32_t)(256 * (lane >> 3) + g) * 128u + 16u * (lane & 7);
    asm volatile("" : "+v"(uo), "+v"(ta), "+v"(la), "+v"(lt), "+v"(sg));
    f32x4 z4 = {0.f, 0.f, 0.f, 0.f};
    asm volatile("" : "+v"(z4));
    asm volatile("s_waitcnt vmcnt(0)" : "={v[100:103]}"(xa), "={v[104:107]}"(xc), "={v108}"(r0), "={v109}"(ep));
    TS(0, 1);
    const uint32_t v[9] = {xa[0], xa[1], xa[2], xa[3], xc[0], xc[1], xc[2], xc[3], r0};
    uint32_t c[9];
    f32x2 tf[9];
    T = ((uint32_t)__builtin_amdgcn_readfirstlane(ep) + 1u) * 0x9E3779B1u;
    if (wl == 3) { const uint32_t now = (uint32_t)__builtin_amdgcn_s_memrealtime() + SD; ST16(sg, ((u32x4){T, now, T, now})); }
    const uint32_t tj = ((T >> (3 * wl)) & 0x00070007u) | (((T >> (12 + wl)) & 0x00010001u) << 14);
    uint32_t tw[3] = {(tj & 0xffffu) * 0x10001u, (tj >> 16) * 0x10001u, 0u};
    tw[2] = (lane < 32) ? tw[0] : tw[1];
    asm volatile("" : "+v"(tw[0]), "+v"(tw[1]), "+v"(tw[2]));
    f32x2 rc, nc;
    float e_;
    ln_stats<false>(v, lane, 0.03125f, tf, z4, rc, nc, e_);
    ln_apply<true>(tf, lane, rc, nc, c);
#pragma unroll
    for (int k = 0; k < 9; ++k) c[k] = (c[k] & ~UMASK) | tw[k >> 2];
    *(u32x4*)(smem + la) = (u32x4){c[0], c[1], c[2], c[3]};
    *(u32x4*)(smem + la + RP) = (u32x4){c[4], c[5], c[6], c[7]};
    *(uint32_t*)(smem + lt) = c[8];
    u32x4 ha, hb;
    {
      uint32_t t, cc, n = SPIN_CAP;
      asm volatile("s_mov_b64 exec, 1\n\tv_mov_b32 %2, 1\n\tds_add_u32 %5, %2\n\ts_mov_b64 exec, -1\n"
                   "1:\n\tds_read_b32 %2, %5\n\tds_read_b64_tr_b16 v[40:41], %6\n\tds_read_b64_tr_b16 v[42:43], %6 offset:%7\n\tds_read_b64_tr_b16 v[44:45], %6 offset:512\n\t"
                   "ds_read_b64_tr_b16 v[46:47], %6 offset:%8\n\ts_sub_u32 %4, %4, 1\n\ts_cbranch_scc1 2f\n\ts_waitcnt lgkmcnt(4)\n\tv_readfirstlane_b32 %3, %2\n\t"
                   "s_cmp_lt_u32 %3, 4\n\ts_cbranch_scc1 1b\n2:\n\ts_waitcnt lgkmcnt(0)"
                   : "=&{v[40:43]}"(ha), "=&{v[44:47]}"(hb), "=&v"(t), "=&s"(cc), "+s"(n) : "v"(c0), "v"(ta), "n"(4 * RP), "n"(512 + 4 * RP) : "memory", "scc");
    }
    ST16(uo, ha);
    ST16(uo + 64 * 128, hb);
    if (wl == 0) {
      const u32x2 h4 = trd(ta + 1024), h5 = trd(ta + 1024 + 4 * RP);
      ST16(uo + 128 * 128, ((u32x4){h4[0], h4[1], h5[0], h5[1]}));
    }
  };
  const int dX = 512 - 24 + 8 * bx + bj;
  if constexpr (ROLE == 2) {
    const int tl = tid - 256;
    if (isx) {
      __builtin_amdgcn_s_setprio(QPRIO0);
      *(u32x4*)(smem + X_TB + 16 * tl) = zero4;
      *(u32x4*)(smem + X_TB + 16 * min(256 + tl, (TB + C_U) / 16 - 1)) = zero4;
      const int pr = (wl + XROT) & 3;
      COps e;
      conv_ops(lb0 + X_TB, lb0 + X_UL, lb0 + X_IM, lane, e);
      TOps t;
      table_ops(lb0 + X_IM, lb0 + X_TB, wl & 1, lane, t);
      e.o = lb0 + X_ST + (uint32_t)(16 * ((2 * i + (kq >> 1)) ^ ((i >> 2) & 1)) + 8 * (kq & 1));
      e.f = ca + 28 - 32;
      asm volatile("" : "+v"(e.o), "+v"(e.f));
      float ldt, are, aim, bre, bim, cre, cim, dsk;
      P_WAIT(0, , "v103", "v104", "v105", "v106", "v107", "v109");
      asm volatile("" : "={v109}"(ep));
      T = ((uint32_t)__builtin_amdgcn_readfirstlane(ep) + 1u) * 0x9E3779B1u;
      P_MORE();
      asm volatile("v_lshlrev_b32 %0, 16, v107" : "=v"(dsk) : : "v107");
      TS(0, 1);
      __builtin_amdgcn_s_setprio(QPRIO);
      int* evp = (int*)(sel + 44);
      char* im = smem + X_IM + IP * (tl & 127);
      evp[wl] = (wl < 2) ? gen_y(ldt, are, aim, bre, bim, cre, cim, lb0 + X_IM + IP * tl, [] {} TSA) : gen_x(ldt, are, aim, im);
      TS(0, 2);
      __builtin_amdgcn_s_setprio(QPRIO2);
      gsync(ca + 8, 4);
      if (wl < 2) evp[-4 + wl] = (int)table_tile(t, wl & 1, lane, lb0 + LM + 4 * (128 + 44), dsk TSA);
      if (wl < 2) gsync0(ca + 8, 8); else { count_up(ca + 8); SLEEP(XSL); gwait(ca + 8, 8); }
      const uint32_t cs = (uint32_t)__builtin_amdgcn_readfirstlane(evp[-4]);
      if (wl == 3) *(u32x4*)(smem + X_IM + pad_at(lane & 7)) = zero4;
      TS(0, 3);
      CONV_RUN(2 + pr, e, , "+{v11}"(tid2));
      __builtin_amdgcn_s_setprio(3);
      {
        const int lane = tid2 & 63;
        const u32x4 v = *(const u32x4*)(smem + X_ST + 2048 * pr + 1024 * (lane >> 5) + 16 * ((lane & 31) ^ ((lane >> 3) & 1)));
        if (__builtin_expect(v_put(v, (u32x4){utag(T, 0), utag(T, 1), utag(T, 2), utag(T, 3)}, cs != S16, v_at(dX, 64 * pr + lane)), 0)) {
          DRAIN();
          if (lane == 0) ST4(O_DN + 16u * (4 * (dX - 256) + pr), T);
        }
        if (pr == 0 && lane == 0) asm volatile("global_store_dwordx2 %0, %1, %2 sc1" : : "v"(O_SC + 16u * dX), "v"((u32x2){T, cs}), "s"(ws) : "memory");
      }
      TS(0, 6);
    }
    if (!isx) __builtin_amdgcn_s_setprio(PPRIO);
    asm volatile("" : "+v"(tid2));
    if (!isx) rows_xw(tid2 & 63);
    if (!isx && !ffn) {
      SLEEP(RSLEEP);
#pragma unroll
      for (int k = 0; k < 7; ++k) ST16(O_SL + (uint32_t)((6 * bx + bj - 18) * 1792 + 256 * k - 256 + tid2) * 16u, ones);
      DRAIN();
    }
    if (!isx) BAR();
    const uint32_t tp = (uint32_t)__builtin_amdgcn_readfirstlane(cnt[4]);
    if (!isx) T = (uint32_t)__builtin_amdgcn_readfirstlane(sel[43]);
    if (!isx && w == 4) v_look(tp, tid2 & 63);
    v_time();
  } else {
    __builtin_amdgcn_s_setprio(3);
    if (tid < 16) cnt[tid] = 0u;
    BAR();
    if (tid < 20) ((uint32_t*)(hm + 320))[tid] = 0u;
    if (tid >= 184) *(u32x4*)(smem + L_SC - 16 * 184 + 16 * tid) = (u32x4){0x41804180u, 0x41804180u, 0x41804180u, 0x41804180u};
    uint32_t sink = 0;
    ln1(tg, 0, ca + 12);
    TS(0, 2);
    __builtin_amdgcn_s_setprio(NPRIO);
    asm volatile("s_waitcnt vmcnt(0)" : "+v"(sink) : : "memory");
    TS(0, 13);
    if (w == 0) {
      const uint32_t xr = O_XC + (uint32_t)((1021 - 4 * g0) * 72 + lane) * 16u;
      ST16(xr, ones);
      ST16(xr + 1024u, ones);
      ST16(xr + 2048u, ones);
      if (lane < 24) ST16(xr + 3072u, ones);
    }
    if (w == 2 && b < 72 && lane < 8) ST16(O_FB + (uint32_t)(8 * b + lane) * 16u, ones);
    if (isx) {
      rows_xw(lane);
      char* sp = smem + X_RW(w) + 16 * lane;
      *(u32x4*)sp = (u32x4){xv[0], xv[1], xv[2], xv[3]};
      *(u32x4*)(sp + 1024) = (u32x4){xv[4], xv[5], xv[6], xv[7]};
      *(u32x2*)(smem + X_RW(w) + 2048 + 8 * lane) = (u32x2){xv[8], wv[4]};
      if (w == 0) *(u32x4*)(smem + X_WR + 16 * lane) = (u32x4){wv[0], wv[1], wv[2], wv[3]};
    }
    SLEEP(ESLEEP);
    uint32_t tu = latest(gate(ws, O_SU + (uint32_t)(256 * bx + (ALLP ? tid : 64 * w + 4 * i + (bj & 3))) * 128u, T));
    asm volatile("s_mov_b64 exec, 1\n\tds_max_u32 %0, %1\n\ts_mov_b64 exec, -1" : : "v"(ca + 16), "v"(tu) : "memory");
    if (w == 0) sel[43] = T;
    if (isx) {
      const uint32_t xo = u_at(tid, dX);
      char* sm = smem + X_UL + C_U - 16 + 16 * tid;
      const u32x4 ut = {utag(T, 0), utag(T, 1), utag(T, 2), utag(T, 3)};
      __builtin_amdgcn_s_setprio(3);
      until(tu + UD + XD);
      u32x4 uw;
      asm volatile("global_load_dwordx4 %0, %1, %2 sc1" : "=&v"(uw) : "v"(xo), "s"(ws) : "memory");
      asm volatile("s_waitcnt vmcnt(0)" : "+v"(uw));
      uw ^= ut;
      u_again(uw, ut, xo);
      *(u32x4*)(sm + 16) = uw;
      BAR();
      if (w < 3) {
        const u32x4 pw = *(const u32x4*)sm;
        if (w < 2) sums<true>(smem + X_IM + 16 * tid, pw, uw); else sums<false>(smem + X_IM + 16 * tid, pw, uw);
        count_up(ca + 28);
      }
    } else {
      until(tu + UD + WD);
      BAR();
    }
    tu = (uint32_t)__builtin_amdgcn_readfirstlane(cnt[4]);
    TS(0, 3);
    __builtin_amdgcn_s_setprio(DPRIO);
    if (ffn) {
      ts1 = (tu + UD + VD2 - TSTOP - TAPER) | 1u;
      gu_dma(0, N0, IC<40>{});
      gu_dma(N0, NA, IC<WDEPTH>{});
      asm volatile("s_waitcnt vmcnt(%0)" : : "n"(NA - 9) : "memory");
      gsync0(ca + 4, 4);
      gu_read(0);
      asm volatile("s_waitcnt lgkmcnt(0)" ::: "memory");
      count_up(ca + 36);
      TS(0, 4);
      gu_dma(NA, 18, IC<WDEPTH2>{});
      gwait(ca + 36, 4);
      down_dma(0, KN, lane);
      asm volatile("s_waitcnt vmcnt(%0)" : : "n"(3 * KN) : "memory");
      count_up(ca + 4);
      if (STP && w == 1)
        asm volatile("global_load_dword v126, %0, %1 sc1" : : "v"(O_SC + 1024u * (fb >> 4) + 128u * (lane & 7)), "s"(ws) : "memory", "v126");
      TS(0, 7);
    } else {
      if (!isx && C3) {
        const int n = 6 * min(tid, 191) + bj - 18, r = n >> 2;
        const char* wb = (const char*)((n & 1) ? wu : wg) + bx * 1152;
        const int c0 = (3 - (int)((uintptr_t)wb >> 7)) & 7;
        wb += (uint32_t)((r + 144 * ((r >= 144) + ((n >> 1) & 1))) * (F_ * 2) + 128 * c0);
        TOUCH(wb);
        if (c0 == 0) TOUCH(wb + 1024);
      }
      auto d_line = [&](int k) {
        const int n = min((k * 14 + bj - 18) * 64 + lane, 5183), q = n / 1296, r = n - 1296 * q;
        return (const char*)wd + (uint32_t)(bx * 5184 + (r / 18) * 72 + 18 * (HREV ? 3 - q : q) + r % 18) * 128u;
      };
      if (isx && w == 3) v_look(tu, lane);
      int k = w;
      if (w < HW) {
#pragma unroll 1
        for (; k < 6 && (int)((uint32_t)__builtin_amdgcn_s_memrealtime() - (tu + UD + VD2 - HSTOP)) < 0; k += HW) { TOUCH(d_line(k)); DRAIN(); }
      }
      TS(0, 4);
      v_time();
      until(tv + RD2);
      if (w < HW) {
#pragma unroll 1
        for (; k < 6; k += HW) { TOUCH(d_line(k)); if (!HALL) DRAIN(); }
      }
      TS(0, 7);
    }
    v_time();
    tk = tv + KD;
  }
  {
  const int tid = tid2, lane = tid & 63, i = lane & 15, kq = lane >> 4;
  __builtin_amdgcn_s_setprio(3);
  const int PL = ffn ? 6400 : LW + 6656;
  f32x2 xf[9];
  f32x4 za = {0.f, 0.f, 0.f, 0.f}, zb = za;
  uint32_t al, ha, h0a, h1a, so, da;
  auto ffn_terms = [&] {
    al = O_XC + 64u * (i >> 3) + 16u * kq + 128u * w;
    ha = (ROLE == 1) ? (uint32_t)((4 * (kq & 1) + 2 * (kq >> 1) + (i >> 3)) * 80 + 16 * w + 2 * (i & 7)) : 0u;
    h0a = (uint32_t)(min(i, 8) * 80 + 16 * kq); h1a = (uint32_t)(min(i ^ 8, 8) * 80 + 16 * kq);
    so = O_SL + (uint32_t)fb * SP + ((ROLE == 2) ? 16u * tid - 4096u : 8192u + 16u * lane);
    da = lb0 + (uint32_t)((8 * kq + ((lane >> 2) & 3)) * 1152 + 32 * ((8 * kq + ((lane >> 2) & 3) + 2) >> 2) + 8 * (lane & 3) + ((ROLE == 2) ? 128 * wl : 1024));
    if (ROLE == 2) asm volatile("" : "+v"(h0a), "+v"(h1a), "+v"(so), "+v"(da)); else asm volatile("" : "+v"(ha), "+v"(al), "+v"(da));
  };
  if (ROLE == 1) ffn_terms();
  uint32_t hv[9] = {}, nv[9] = {}, keya = 0u, keyb = 0u;
  int rka = 0, rkb = 0;
  auto put_row = [&](const uint32_t* r, int j, const void* base, uint32_t ro) {
    const u32x4 tl = {r[8], (uint32_t)DPP_I((int)r[8], 0x55, 0xf), (uint32_t)DPP_I((int)r[8], 0xaa, 0xf), (uint32_t)DPP_I((int)r[8], 0xff, 0xf)};
    asm volatile("global_store_dwordx4 %0, %1, %2 sc1\n\ts_nop 1" : : "v"(ro + 16u * lane), "v"((u32x4){r[4 * j], r[4 * j + 1], r[4 * j + 2], r[4 * j + 3]}), "s"(base) : "memory");
    if ((lane & 35) == 32 * j) asm volatile("global_store_dwordx4 %0, %1, %2 offset:1024 sc1\n\ts_nop 1" : : "v"(ro + 4u * (lane & 31)), "v"(tl), "s"(base) : "memory");
  };
  if (ln2) {
    const int tl = tid - 256, d1 = 64 * (tl >> 5) + (tl & 31), d3 = 64 * ((tl >> 4) & 7) + 32 + (tl & 15), d2 = (tl < 128) ? d3 + 16 : min(384 + tl, 575);
    uint32_t vo = v_at(d1, g0), vo2 = v_at(d2, g0), vo3 = v_at(d3, g0);
    u32x4 ut = {utag(T, 0), utag(T, 1), utag(T, 2), utag(T, 3)};
    u32x4 va, vc = ut, vb = ut;
    asm volatile("" : "+v"(vo), "+v"(vo2), "+v"(vo3), "+v"(ut), "+v"(vc), "+v"(vb));
    TS(0, 7);
    until(tv);
    TS(0, 8);
    asm volatile("global_load_dwordx4 %0, %2, %4 sc1\n\ts_cmp_lt_u32 %5, 2\n\ts_cbranch_scc1 1f\n\tglobal_load_dwordx4 %1, %3, %4 sc1\n1:"
                 : "=&v"(va), "+v"(vc) : "v"(vo), "v"(vo3), "s"(ws), "s"(wl) : "memory", "scc");
    until(tv + V2D);
    asm volatile("s_cmp_eq_u32 %3, 3\n\ts_cbranch_scc1 1f\n\tglobal_load_dwordx4 %0, %1, %2 sc1\n1:" : "+v"(vb) : "v"(vo2), "s"(ws), "s"(wl) : "memory", "scc");
    __builtin_amdgcn_sched_barrier(0);
    if (isx) {
      const u32x4 ra = *(const u32x4*)(smem + X_RW(wl) + 16 * lane), rb = *(const u32x4*)(smem + X_RW(wl) + 1024 + 16 * lane), rw = *(const u32x4*)(smem + X_WR + 16 * lane);
      const u32x2 rc = *(const u32x2*)(smem + X_RW(wl) + 2048 + 8 * lane);
#pragma unroll
      for (int k = 0; k < 4; ++k) { xv[k] = ra[k]; xv[4 + k] = rb[k]; wv[k] = rw[k]; }
      xv[8] = rc[0]; wv[4] = rc[1];
    }
    uint32_t pa = (uint32_t)(PL + 9216 * wl + 32 * lane), pb = (uint32_t)(PL + 9216 * wl + 2048 + 8 * (lane & 31)), ps = (lane < 32) ? 0u : 16u;
    uint32_t kid = (uint32_t)(((255 - g2) << 5) | (7 - 2 * wl - (lane >> 5)));
    uint32_t px = (uint32_t)(PL + 4 * d1), py = (uint32_t)(PL + 4 * d2), pz = (uint32_t)(PL + 4 * d3);
#pragma unroll
    for (int k = 0; k < 9; ++k) xf[k] = (f32x2){bflo(xv[k]), bfhi(xv[k])};
    asm volatile("" : "+v"(xf[0]), "+v"(xf[1]), "+v"(xf[2]), "+v"(xf[3]), "+v"(xf[4]), "+v"(xf[5]), "+v"(xf[6]), "+v"(xf[7]), "+v"(xf[8]));
    asm volatile("" : "+v"(za), "+v"(zb), "+v"(pa), "+v"(pb), "+v"(ps), "+v"(kid), "+v"(px), "+v"(py), "+v"(pz));
    float sv[10], ws_ = dot2(wv[4], 0x3f803f80u, 0.f) * 0.5f;
#pragma unroll
    for (int k = 0; k < 4; ++k) ws_ = dot2(wv[k], 0x3f803f80u, ws_);
    ws_ = wave_sum(ws_);
#pragma unroll
    for (int k = 0; k < 10; ++k) sv[k] = 16.f;
    uint32_t ka_ = lb0 + LM + 4u * (128 + 48 + 2 * wl + (lane >> 5)), kb_ = lb0 + LM + 4u * (128 + 48);
    uint32_t ko_ = O_KEY + (uint32_t)(256 * (lane & 7) + 8 * (g2 & 31) + (g2 >> 5)) * 16u;
    asm volatile("" : "+v"(ws_), "+v"(sv[0]), "+v"(sv[1]), "+v"(sv[2]), "+v"(sv[3]), "+v"(sv[4]), "+v"(sv[5]), "+v"(sv[6]), "+v"(sv[7]), "+v"(sv[8]), "+v"(sv[9]),
                 "+v"(ka_), "+v"(kb_), "+v"(ko_));
    if (ffn) gwait(ca + 36, 4);
    asm volatile("s_waitcnt vmcnt(1)\n\ts_cmp_eq_u32 %2, 3\n\ts_cbranch_scc0 1f\n\ts_waitcnt vmcnt(0)\n1:" : "+v"(va), "+v"(vc) : "s"(wl) : "memory", "scc");
    auto turn1 = [&] {
#pragma unroll
      for (int e = 0; e < 4; ++e) *(uint32_t*)(smem + px + 9216 * e) = va[e];
      if (wl >= 2) {
#pragma unroll
        for (int e = 0; e < 4; ++e) *(uint32_t*)(smem + pz + 9216 * e) = vc[e];
      }
    };
    auto turn = [&] {
      if (wl < 3) {
#pragma unroll
        for (int e = 0; e < 4; ++e) *(uint32_t*)(smem + py + 9216 * e) = vb[e];
      }
    };
    va ^= ut; vc ^= ut;
    turn1();
    asm volatile("s_waitcnt vmcnt(0)" : "+v"(vb) : : "memory");
    vb ^= ut;
    turn();
    const u32x4 vs = va | vb | vc;
    if (__builtin_expect(__any(((vs[0] | vs[1] | vs[2] | vs[3]) & UMASK) != 0u), 0)) {
#pragma unroll 1
      for (int spin = 0; spin < 4; ++spin) {
        const bool na = ((va[0] | va[1] | va[2] | va[3]) & UMASK) != 0u, nb = ((vb[0] | vb[1] | vb[2] | vb[3]) & UMASK) != 0u, nc = ((vc[0] | vc[1] | vc[2] | vc[3]) & UMASK) != 0u;
        if (!__any(na || nb || nc)) break;
        TSC(16);
        if (na) LD16(va, vo);
        if (nb) LD16(vb, vo2);
        if (nc) LD16(vc, vo3);
        asm volatile("s_waitcnt vmcnt(0)" : "+v"(va), "+v"(vb), "+v"(vc) : : "memory");
        if (na) va ^= ut;
        if (nb) vb ^= ut;
        if (nc) vc ^= ut;
      }
      auto again = [&](u32x4& v, uint32_t o, int d) {
        const int J = g0 >> 5;
        const uint32_t dn = O_DN + 16u * (uint32_t)((d < 512) ? 4 * (d >> 1) + ((0x01013232 >> (4 * J)) & 3) : 4 * (d - 256) + (J >> 1));
        bool pend = ((v[0] | v[1] | v[2] | v[3]) & UMASK) != 0u;
#pragma unroll 1
        for (int spin = 0; spin < SPIN_CAP && __any(pend); ++spin) {
          TSC(16);
          SLEEP(PSLEEP);
          if (pend) {
            uint32_t fin = ~T;
            if ((v[0] & v[1] & v[2] & v[3] & LMASK) == LMASK)
              asm volatile("global_load_dword %0, %1, %2 sc1\n\ts_waitcnt vmcnt(0)" : "=&v"(fin) : "v"(dn), "s"(ws) : "memory");
            LD16W(v, o);
            v ^= ut;
            pend = ((v[0] | v[1] | v[2] | v[3]) & UMASK) != 0u;
            if (pend && fin == T) {
              u32x2 sc;
              for (int k = 0; k < SPIN_CAP; ++k) {
                asm volatile("global_load_dwordx2 %0, %1, %2 sc1\n\ts_waitcnt vmcnt(0)" : "=&v"(sc) : "v"(O_SC + 16u * d), "s"(ws) : "memory");
                if (sc[0] == T) break;
              }
              *(u16*)(smem + L_SC + 2 * d) = (u16)(sc[1] >> 16);
              cnt[10] = 1u;
              v = (v ^ ut) & ~LMASK;
              pend = false;
            }
          }
        }
      };
      again(va, vo, d1); again(vc, vo3, d3); again(vb, vo2, d2);
      turn1();
      turn();
    }
    u32x4 a0, a1;
    u32x2 a2, cf;
    {
      uint32_t cc, n = SPIN_CAP;
      asm volatile("s_mov_b64 exec, 1\n\tv_mov_b32 v90, 1\n\tds_add_u32 %6, v90\n\ts_mov_b64 exec, -1\n"
                   "1:\n\tds_read2_b32 %3, %6 offset1:10\n\tds_read_b128 %0, %7\n\tds_read_b128 %1, %7 offset:16\n\tds_read_b64 %2, %8\n\t"
                   "s_sub_u32 %5, %5, 1\n\ts_cbranch_scc1 2f\n\ts_waitcnt lgkmcnt(3)\n\tv_readfirstlane_b32 %4, v90\n\t"
                   "s_cmp_lt_u32 %4, 4\n\ts_cbranch_scc1 1b\n2:\n\ts_waitcnt lgkmcnt(0)"
                   : "=&v"(a0), "=&v"(a1), "=&v"(a2), "=&{v[90:91]}"(cf), "=&s"(cc), "+s"(n) : "v"(ca), "v"(lb0 + pa), "v"(lb0 + pb) : "memory", "scc");
    }
    TS(0, 9);
    if (__builtin_expect(__builtin_amdgcn_readfirstlane(cf[1]) != 0, 0)) {
      const u32x4 q = *(const u32x4*)(smem + L_SC + 16 * lane);
      const uint32_t r = *(const uint32_t*)(smem + L_SC + 1024 + 4 * (lane & 31));
#pragma unroll
      for (int k = 0; k < 4; ++k) { sv[2 * k] = bflo(q[k]); sv[2 * k + 1] = bfhi(q[k]); }
      sv[8] = bflo(r); sv[9] = bfhi(r);
    }
    const uint32_t av[10] = {a0[0], a0[1], a0[2], a0[3], a1[0], a1[1], a1[2], a1[3], a2[0] >> ps, a2[1] >> ps};
    f32x2 h[9];
#pragma unroll
    for (int k = 0; k < 10; ++k) {
      asm("v_fma_mix_f32 %0, %1, %2, %3 op_sel_hi:[1,0,0]" : "=v"(h[(k < 8) ? k >> 1 : 8][k & 1]) : "v"(av[k]), "v"(sv[k]), "v"(xf[(k < 8) ? k >> 1 : 8][k & 1]));
      if (k < 8) asm("v_fma_mix_f32 %0, %1, %2, %3 op_sel:[1,0,0] op_sel_hi:[1,0,0]" : "=v"(h[4 + (k >> 1)][k & 1]) : "v"(av[k]), "v"(sv[k]), "v"(xf[4 + (k >> 1)][k & 1]));
    }
#pragma unroll
    for (int k = 0; k < 9; ++k) hv[k] = pack2(h[k][0], h[k][1]);
    const float da = dot2(hv[2], wv[2], dot2(hv[0], wv[0], zb[0])) + dot2(hv[3], wv[3], dot2(hv[1], wv[1], zb[1]));
    const float db = dot2(hv[6], wv[2], dot2(hv[4], wv[0], zb[2])) + dot2(hv[7], wv[3], dot2(hv[5], wv[1], zb[3]));
    float dt = dot2(hv[8], wv[4], fold32(da, db));
    f32x2 rc, nc;
    ln_stats<true>(hv, lane, 1.f, xf, za, rc, nc, dt);
    dt = (lane < 32) ? dt * rc[0] + ws_ * nc[0] : dt * rc[1] + ws_ * nc[1];
    const uint32_t bb = pack2(dt, dt) & 0xffff0000u;
    const uint32_t kk = (bb ^ (((uint32_t)((int)bb >> 31) & 0x7fff0000u) | 0x80000000u)) | kid;
    u32x4 ka, kb;
    uint32_t old;
    asm volatile("s_mov_b64 exec, %5\n\tds_write_b32 %3, %4\n\ts_mov_b64 exec, 1\n\tv_mov_b32 %2, 1\n\tds_add_rtn_u32 %2, %7, %2\n\ts_mov_b64 exec, -1\n\t"
                 "ds_read_b128 %0, %6\n\tds_read_b128 %1, %6 offset:16\n\ts_waitcnt lgkmcnt(0)"
                 : "=&v"(ka), "=&v"(kb), "=&v"(old) : "v"(ka_), "v"(kk), "s"(0x8000000080000000ull), "v"(kb_), "v"(ca) : "memory");
    if (__builtin_amdgcn_readfirstlane(old) == 7) {
      uint32_t m1 = max(max(ka[0], ka[1]), ka[2]), m2 = med3u(ka[0], ka[1], ka[2]), m3 = min(min(ka[0], ka[1]), ka[2]);
#pragma unroll
      for (int e = 0; e < 5; ++e) {
        const uint32_t kx = e ? kb[e - 1] : ka[3];
        m3 = med3u(m2, m3, kx); m2 = med3u(m1, m2, kx); m1 = max(m1, kx);
      }
      if (lane < 8) ST16(ko_, ((u32x4){m1 | 24u, m2 | 16u, m3 | 8u, T & ~1u}));
    }
    TS(0, 10);
    keya = (uint32_t)__builtin_amdgcn_readlane(kk, 31); keyb = (uint32_t)__builtin_amdgcn_readlane(kk, 63);
    ln_apply<false>(xf, lane, rc, nc, nv);
    gwait(ca, 8);
    if (wl == 1) {
      const u32x4 kv = *(const u32x4*)&kl[4 * (lane & 1)];
      if (lane < 2) ST16(O_KF + (uint32_t)(2 * g2 + lane) * 16u, ((u32x4){__builtin_amdgcn_perm(kv[1], kv[0], 0x07060302u), T, __builtin_amdgcn_perm(kv[3], kv[2], 0x07060302u), T}));
      if (ROLE == 2 && b == 0 && lane == 0) ST4(O_EP, T * 0x0E8B2F51u);
    }
    const uint32_t ko = kl[lane & 7];
    rka = FORCEFB + __popcll(__ballot(ko > keya) & 0xffull); rkb = FORCEFB + __popcll(__ballot(ko > keyb) & 0xffull);
#pragma unroll
    for (int k = 0; k < 9; ++k) {
      const f32x2 o = xf[k] + (f32x2){bflo(nv[k]), bfhi(nv[k])};
      hv[k] = pack2(o[0], o[1]);
      nv[k] = min(nv[k], 0xfffffffeu);
    }
    if (rka < 3) put_row(nv, 0, ws, O_XC + (uint32_t)(1023 - 4 * g2 - rka) * 1152u);
    if (rkb < 3) put_row(nv, 1, ws, O_XC + (uint32_t)(1023 - 4 * g2 - rkb) * 1152u);
    if (!LPOS && ffn) down_dma(KN, 4, lane);
    ffn_terms();
  }
  uint32_t k3rd = 0u, k4th = 0u;
  uint32_t q0 = lb0 + LM + 4u * lane, q1 = lb0 + LM + 32u * w, q2 = q1 + 4u * (lane & 7), q3 = lb0 + LM + 512u + 4u * (lane & 7), q4 = lb0 + LM + (uint32_t)(tid & ~7);
  uint32_t r8 = C_, sb = lb0 + LM + 512u;
  asm volatile("" : "+v"(q0), "+v"(q1), "+v"(q2), "+v"(q3), "+v"(q4), "+v"(r8), "+v"(sb));
  if constexpr (ROLE == 1) {
    u32x4 key;
    const uint32_t kof = O_KEY + (uint32_t)(256 * bx + tid) * 16u;
    until(tk);
    if (!ffn) SLEEP(NSLEEP);
    LD16(key, kof);
    if (ffn) {
      gwait0(ca + 4, 8);
      gu_read(1);
    }
    asm volatile("s_waitcnt vmcnt(0)" : "+v"(key) : : "memory");
    if (__builtin_expect(__any(key[3] != (T & ~1u)), 0)) {
#pragma unroll 1
      for (int spin = 0; spin < SPIN_CAP; ++spin) {
        const bool need = key[3] != (T & ~1u);
        if (!__any(need)) break;
        TSC(15);
        SLEEP(KBACK);
        if (need) LD16W(key, kof);
      }
    }
    TS(0, 11);
    const uint32_t m1 = oct_maxu(key[0]);
    const bool hit = key[0] == m1;
    const uint32_t k0 = hit ? key[1] : key[0], k1 = hit ? key[2] : key[1];
    const uint32_t m2 = oct_maxu(k0);
    *LP(u32x2, q4) = (u32x2){m1, m2};
    k3rd = (k0 == m2) ? k1 : k0;
    k4th = (k0 == m2) ? (hit ? 0u : key[2]) : k1;
    if (RPF == 1 && !ffn && w == (bj & 3)) {
      const uint32_t r1 = O_XC + ((m1 >> 3) & 0x3ffu) * 1152u + 128u * (lane & 7), r2 = O_XC + ((m2 >> 3) & 0x3ffu) * 1152u + 128u * (lane & 7);
      asm volatile("global_load_dword v126, %0, %3 sc1\n\tglobal_load_dword v126, %1, %3 sc1\n\tglobal_load_dword v126, %2, %3 offset:1024 sc1"
                   : : "v"(r1), "v"(r2), "v"(((lane & 1) ? r1 : r2) - 128u * (lane & 7)), "s"(ws) : "memory", "v126");
    }
  }
  auto pick = [&](auto co, auto so_) {
    BAR();
    const uint32_t c = *LP(uint32_t, q0 + co.value), own = *LP(uint32_t, q2 + co.value);
    const u32x4 la = *LP(u32x4, q1 + co.value), lb = *LP(u32x4, q1 + co.value + 16);
    uint32_t r = r8;
    asm("v_cmp_gt_u32 s[40:41], %1, %2\n\tv_cmp_gt_u32 s[42:43], %1, %3\n\tv_cmp_gt_u32 s[44:45], %1, %4\n\tv_cmp_gt_u32 s[46:47], %1, %5\n\t"
        "v_cmp_gt_u32 s[48:49], %1, %6\n\tv_cmp_gt_u32 s[50:51], %1, %7\n\tv_cmp_gt_u32 s[52:53], %1, %8\n\tv_cmp_gt_u32 s[54:55], %1, %9\n\t"
        "s_bcnt1_i32_b64 s40, s[40:41]\n\ts_bcnt1_i32_b64 s42, s[42:43]\n\tv_writelane_b32 %0, s40, 0\n\ts_bcnt1_i32_b64 s44, s[44:45]\n\t"
        "v_writelane_b32 %0, s42, 1\n\ts_bcnt1_i32_b64 s46, s[46:47]\n\tv_writelane_b32 %0, s44, 2\n\ts_bcnt1_i32_b64 s48, s[48:49]\n\t"
        "v_writelane_b32 %0, s46, 3\n\ts_bcnt1_i32_b64 s50, s[50:51]\n\tv_writelane_b32 %0, s48, 4\n\ts_bcnt1_i32_b64 s52, s[52:53]\n\t"
        "v_writelane_b32 %0, s50, 5\n\ts_bcnt1_i32_b64 s54, s[54:55]\n\tv_writelane_b32 %0, s52, 6\n\tv_writelane_b32 %0, s54, 7"
        : "+v"(r) : "v"(c), "v"(la[0]), "v"(la[1]), "v"(la[2]), "v"(la[3]), "v"(lb[0]), "v"(lb[1]), "v"(lb[2]), "v"(lb[3])
        : "scc", "s40", "s41", "s42", "s43", "s44", "s45", "s46", "s47", "s48", "s49", "s50", "s51", "s52", "s53", "s54", "s55");
    *LP(uint32_t, sb + 4u * min(r, (uint32_t)C_) + so_.value) = own;
    BAR();
  };
  auto rows = [&](uint32_t ao, auto&& meanwhile) {
    u32x4 aa, ab, ac = __builtin_bit_cast(u32x4, zb);
    asm volatile("global_load_dwordx4 %0, %3, %4 sc1\n\tglobal_load_dwordx4 %1, %3, %4 offset:512 sc1\n\t"
                 "s_cmp_lg_u32 %5, 0\n\ts_cbranch_scc1 1f\n\tglobal_load_dwordx4 %2, %3, %4 offset:1024 sc1\n1:"
                 : "=&v"(aa), "=&v"(ab), "+v"(ac) : "v"(ao), "s"(ws), "s"(w) : "memory", "scc");
    __builtin_amdgcn_sched_barrier(0);
    meanwhile();
    asm volatile("s_waitcnt vmcnt(0)" : "+v"(aa), "+v"(ab), "+v"(ac) : : "memory");
    if (__builtin_expect(__any(max(max(max4(aa), max4(ab)), max4(ac)) == 0xffffffffu), 0)) {
#pragma unroll 1
      for (int spin = 0; spin < SPIN_CAP; ++spin) {
        const bool na = max4(aa) == 0xffffffffu, nb = max4(ab) == 0xffffffffu, nc = max4(ac) == 0xffffffffu;
        if (!__any(na || nb || nc)) break;
        TSC(14);
        SLEEP(PSLEEP);
        if (na) LD16W(aa, ao);
        if (nb) LD16W(ab, ao + 512u);
        if (nc) LD16W(ac, ao + 1024u);
      }
    }
    *(u32x4*)(smem + LA + 16 * tid) = aa;
    *(u32x4*)(smem + LA + 4096 + 16 * tid) = ab;
    if (w == 0) *(u32x4*)(smem + LA + 8192 + 16 * tid) = ac;
  };
  auto rows_dma = [&](uint32_t ao) {
    uint32_t dl = lb0 + LA + 1024u * w, keep;
    asm volatile("s_mov_b32 %0, m0\n\ts_mov_b32 m0, %1\n\ts_add_u32 %1, %1, 4096 - 512\n\tglobal_load_lds_dwordx4 %2, %3 sc1\n\ts_mov_b32 m0, %1\n\ts_add_u32 %1, %1, 4096 - 512\n\t"
                 "global_load_lds_dwordx4 %2, %3 offset:512 sc1\n\ts_cmp_lg_u32 %4, 0\n\ts_cbranch_scc1 1f\n\ts_mov_b32 m0, %1\n\ts_nop 0\n\t"
                 "global_load_lds_dwordx4 %2, %3 offset:1024 sc1\n1:\n\ts_mov_b32 m0, %0" : "=&s"(keep), "+s"(dl) : "v"(ao), "s"(ws), "s"(w) : "memory", "scc");
  };
  auto get_bd = [&](int t0, int t1) {
#pragma unroll
    for (int t = t0; t < t1; ++t) {
      const u32x2 h0 = trd(da + 512 * (t >> 2) + 32 * (t & 3)), h1 = trd(da + 512 * (t >> 2) + 32 * (t & 3) + 4608 + 32);
      bw[t] = (u32x4){h0[0], h0[1], h1[0], h1[1]};
    }
#pragma unroll
    for (int t = t0; t < t1; ++t) asm volatile("" : "+v"(bw[t]));
  };
  pick(IC<0>{}, IC<0>{});
  uint32_t mykey = *LP(uint32_t, q3), ro = ((mykey >> 3) & 0x3ffu) * 1152u;
  TS(0, 12);
  auto check = [&] {
    cnt[(k3rd > (uint32_t)__builtin_amdgcn_readlane(mykey, 7)) ? 5 : 6] = 1u;
    const uint32_t m3 = oct_maxu(k3rd);
    if (k3rd == m3) k3rd = k4th;
    if ((lane & 7) == 0) cand[72 + (tid >> 3)] = m3;
    if (w == 0 && lane < 32) cand[(lane < 8) ? 64 + lane : 96 + lane] = (lane < 8) ? mykey : 0u;
  };
  if constexpr (ROLE == 1) {
    if (ffn && !FORCEFB) rows_dma(ro + al);
    check();
    DRAIN();
  } else if (ffn) {
    if (LPOS) { SLEEP(LSLEEP); down_dma(KN, 4, lane); } else { DRAIN(); gsync0(ca, 12); get_bd(0, 8); }
  }
  BAR();
  TS(0, 5);
  auto put_out = [&](int j, uint32_t k0, int rk) {
    const uint64_t mine = __ballot(((mykey ^ k0) & ~24u) == 0u) & 0xffull;
    if (mine == 0ull) put_row(hv, j, out, (uint32_t)((8 * g2 + 2 * wl + j) * (D_ * 2)));
    else if (rk >= 3) put_row(nv, j, ws, O_FB + 1152u * (uint32_t)__builtin_ctzll(mine));
  };
  auto warm_out = [&] {
    const uint32_t k0 = (lane < 9) ? keya : keyb;
    bool hit = false;
#pragma unroll
    for (int e = 0; e < 8; ++e) hit |= (((uint32_t)__builtin_amdgcn_readlane(mykey, e) ^ k0) & ~24u) == 0u;
    if (OTP && lane < 18 && hit) asm volatile("global_load_dword v126, %0, %1" : : "v"((uint32_t)((8 * g2 + 2 * wl) * (D_ * 2) + 128 * lane)), "s"(out) : "memory", "v126");
  };
  auto rest_out = [&] { put_out(0, keya, rka); put_out(1, keyb, rkb); };
  f32x4 lo, hi;
  u32x4 av[5];
  const uint32_t aa = lb0 + LA + 16u * lane;
  auto gu_ask = [&] {
#pragma unroll
    for (int j = 0; j < 5; ++j) asm volatile("ds_read_b128 %0, %1 offset:%2" : "=v"(av[j]) : "v"(aa), "n"(1024 * j) : "memory");
  };
  auto gate_up = [&] {
    lo = (f32x4){0.f, 0.f, 0.f, 0.f}; hi = lo;
#pragma unroll
    for (int j = 0; j < 9; ++j) {
      if (j < 5) asm volatile("s_waitcnt lgkmcnt(4)" : "+v"(av[j % 5]));
      else if (j == 5) asm volatile("s_waitcnt lgkmcnt(2)" : "+v"(av[0]), "+v"(av[1]));
      else if (j == 7) asm volatile("s_waitcnt lgkmcnt(0)" : "+v"(av[2]), "+v"(av[3]));
      lo = MMB(av[j % 5], bw[2 * j], lo);
      hi = MMB(av[j % 5], bw[2 * j + 1], hi);
      __builtin_amdgcn_sched_barrier(0);
      if (j < 4) asm volatile("ds_read_b128 %0, %1 offset:%2" : "=v"(av[j]) : "v"(aa), "n"(1024 * (j + 5)) : "memory");
    }
  };
  uint32_t fl;
  asm volatile("ds_read_b32 %0, %1" : "=v"(fl) : "v"(ca + 20) : "memory");
  if (FFN == 1) gu_ask();
  asm volatile("s_waitcnt lgkmcnt(%1)" : "+v"(fl) : "n"(FFN == 1 ? 5 : 0));
  bool slow = false;
  if (__builtin_expect(FORCESLOW || FORCEFB || __builtin_amdgcn_readfirstlane(fl) != 0, 0)) {
    TSC(17);
    if (FFN == 1) asm volatile("s_waitcnt lgkmcnt(0)" : "+v"(av[0]), "+v"(av[1]), "+v"(av[2]), "+v"(av[3]), "+v"(av[4]));
    pick(IC<256>{}, IC<64>{});
    mykey = sel[16 + (lane & 7)];
    ro = ((mykey >> 3) & 0x3ffu) * 1152u;
    if (FFN == 1 && !FORCEFB) rows_dma(ro + al);
    const uint32_t k8 = (uint32_t)__builtin_amdgcn_readlane(mykey, 7);
    if (ROLE == 1 && k3rd > k8) cnt[11] = 1u;
    if (w == 0 && __any((mykey & 24u) == 8u)) {
      const uint32_t pk = sel[16 + (lane >> 3)], gi = (pk >> 5) & 0xffu;
      u32x4 kf;
#pragma unroll 1
      for (int spin = 0; spin < SPIN_CAP; ++spin) {
        LD16W(kf, O_KF + (2u * (255u - gi) + ((lane >> 2) & 1u)) * 16u);
        if (!__any(kf[1] != T || kf[3] != T)) break;
        SLEEP(KBACK);
      }
      const uint32_t wd_ = (lane & 2) ? kf[2] : kf[0], kt = ((lane & 1) ? wd_ & 0xffff0000u : wd_ << 16) | (gi << 5) | (7u - (lane & 7));
      const uint64_t up = __ballot((pk & 24u) == 8u && kt >= (k8 & ~24u));
      if (__popcll((up >> (lane & 56)) & 0xffull) > 3) cnt[11] = 1u;
    }
    if (ROLE == 1) DRAIN();
    BAR();
    if (FORCESLOW > 1 || FORCEFB || __builtin_amdgcn_readfirstlane(cnt[11]) != 0) {
    slow = true;
    u32x4 key;
#pragma unroll 1
    for (int spin = 0; spin < SPIN_CAP; ++spin) {
      LD16W(key, O_KF + 16u * tid);
      if (!__any(key[1] != T || key[3] != T)) break;
      SLEEP(KBACK);
    }
    const uint32_t ib = (uint32_t)(((255 - (tid >> 1)) << 5) | (7 - 4 * (tid & 1)));
    uint32_t k0 = (key[0] << 16) | ib, k1 = (key[0] & 0xffff0000u) | (ib - 1), k2 = (key[2] << 16) | (ib - 2), k3 = (key[2] & 0xffff0000u) | (ib - 3);
    CEX(k0, k1) CEX(k2, k3) CEX(k0, k2) CEX(k1, k3) CEX(k1, k2)
#pragma unroll 1
    for (int c = 0; c < C_; ++c) {
      const uint32_t mx = wave_maxu(k0);
      const bool hit = (k0 == mx);
      k0 = hit ? k1 : k0; k1 = hit ? k2 : k1; k2 = hit ? k3 : k2; k3 = hit ? 0u : k3;
      if (lane == 0) cand[64 + 8 * w + c] = mx;
    }
    pick(IC<256>{}, IC<128>{});
    mykey = sel[32 + (lane & 7)];
    const uint32_t okey = sel[32 + (lane >> 3)];
    const uint64_t same = __ballot(((mykey ^ okey) & 0x1fe0u) == 0u && okey > mykey);
    const int sl = FORCEFB + __popcll((same >> (lane & 7)) & 0x0101010101010101ull);
    ro = (sl < 3) ? (((mykey >> 3) & 0x3ffu) + 3 - sl) * 1152u : O_FB - O_XC + 1152u * (lane & 7);
    if (ln2) rest_out();
    if (ffn) {
      if (ROLE == 1) rows(ro + al, [] {});
      BAR();
    }
    }
    if (FFN == 1) {
      gu_ask();
      asm volatile("s_waitcnt lgkmcnt(0)" : "+v"(av[0]), "+v"(av[1]), "+v"(av[2]), "+v"(av[3]), "+v"(av[4]));
    }
  }
  if (FFN == 1) gate_up();
  if (OPOS == 0 && ROLE == 2 && ffn && !slow) { SLEEP(OD); rest_out(); }
  if (!ffn) {
    if (ln2) warm_out();
    if (ln2 && !slow) { SLEEP(OD2); rest_out(); }
    TSE();
    return;
  }
  uint32_t fx = 0, fv = 0, fn = 0, fo = 0, sa[9];
  float rs, hx, xs;
  u32x4 se;
  if constexpr (ROLE == 1) {
    f32x4 lh = (lane < 32) ? lo : hi;
    float fa = fold32(lh[0], lh[2]), fb2 = fold32(lh[1], lh[3]);
    if (__builtin_expect(__any(__builtin_isunordered(fa, fb2)) && !slow, 0)) {
      gsync0(ca + 4, 12);
      rows(ro + al, [] {});
      gsync0(ca + 4, 16);
      gu_ask();
      gate_up();
      lh = (lane < 32) ? lo : hi;
      fa = fold32(lh[0], lh[2]); fb2 = fold32(lh[1], lh[3]);
    }
    const float g = __int_as_float(__builtin_amdgcn_update_dpp(__float_as_int(fa), __float_as_int(fb2), 0x128, 0xf, 0xc, false));
    const float u = __int_as_float(__builtin_amdgcn_update_dpp(__float_as_int(fb2), __float_as_int(fa), 0x128, 0xf, 0x3, false));
    const uint32_t gu = pack2(g, u);
    const float gp = bflo(gu);
    const float sg = rbf(gp * __builtin_amdgcn_rcpf(1.f + __expf(-gp)));
    *(u16*)((char*)hm + ha) = f2bf(sg * bfhi(gu));
  } else if (LPOS) {
    DRAIN();
    gsync0(ca, 12);
    get_bd(0, 8);
  }
  BAR();
  TS(0, 6);
  if (ROLE == 2 || w == 2) {
    const u32x4 a0 = *(const u32x4*)((char*)hm + h0a), a1 = *(const u32x4*)((char*)hm + h1a);
    if (ROLE == 1) get_bd(0, 4);
    const f32x4 z = {0.f, 0.f, 0.f, 0.f};
    f32x4 o[4];
#pragma unroll
    for (int t = 0; t < 2 * ROLE; ++t) o[t] = MMB(a1, bw[2 * t + 1], MMB(a0, bw[2 * t], z));
#pragma unroll
    for (int t = 0; t < ROLE; ++t) {
      u32x4 sv;
#pragma unroll
      for (int r = 0; r < 4; ++r) sv[r] = min(pack2(o[2 * t][r], o[2 * t + 1][r]), 0xfffffffeu);
      ST16(so + 4096u * t, sv);
    }
    TS(0, 13);
  }
  if (OPOS == 1 && ROLE == 2) {
    warm_out();
    SLEEP(OSLEEP);
    if (!slow) rest_out();
  }
  if (ROLE == 1 && w == 1) {
    const int q = (fb & 15) >> 2, ts = 4 * (q & 1) + ((i & 7) >> 1);
    const int dd = 64 * (fb >> 4) + 16 * (2 * (i & 1) + (q >> 1)) + 4 * (fb & 3) + 2 * (kq & 1) + (kq >> 1);
    const uint32_t fkey = (uint32_t)__builtin_amdgcn_ds_bpermute(4 * ts, (int)mykey), fr = (uint32_t)__builtin_amdgcn_ds_bpermute(4 * ts, (int)ro);
    const int ft = 2047 - (int)(((fkey >> 2) & 0x7f8u) | (fkey & 7u));
    fo = (uint32_t)(ft * D_ + dd) * 2u;
    u32x2 fs;
    asm volatile("global_load_ushort %0, %4, %8\n\tglobal_load_ushort %1, %5, %9 sc1\n\tglobal_load_ushort %2, %6, %9 sc1\n\tglobal_load_dwordx2 %3, %7, %9 sc1"
                 : "=&v"(fx), "=&v"(fv), "=&v"(fn), "=&v"(fs) : "v"(fo), "v"(v_at(dd, ft >> 3) + 2u * (ft & 7)), "v"(O_XC + fr + 2u * dd), "v"(O_SC + 16u * dd), "s"(x), "s"(ws) : "memory");
    uint32_t t16 = (((T >> (3 * ((ft >> 1) & 3))) & LMASK) | (((T >> (12 + ((ft >> 1) & 3))) & 0x00010001u) << 14)) >> (16 * (ft & 1)) & 0xffffu;
    const uint32_t o = fkey >> 16;
    const float rs0 = bf2f((u16)((o & 0x8000u) ? (o & 0x7fffu) : (~o & 0xffffu)));
    rs = rbf(__builtin_amdgcn_rcpf(1.f + __expf(-rs0)));
    const uint32_t one = (i < 8) ? (0x3f80u << (16 * (i & 1))) : 0u;
    se = (u32x4){(i >> 1) == 0 ? one : 0u, (i >> 1) == 1 ? one : 0u, (i >> 1) == 2 ? one : 0u, (i >> 1) == 3 ? one : 0u};
#pragma unroll
    for (int m = 0; m < 9; ++m) sa[m] = O_SL + (uint32_t)(16 * m + (lane >> 2)) * SP + (uint32_t)(4 * fb + (lane & 3)) * 16u;
    asm volatile("" : "+v"(t16), "+v"(rs), "+v"(se), "+v"(sa[0]), "+v"(sa[1]), "+v"(sa[2]), "+v"(sa[3]), "+v"(sa[4]), "+v"(sa[5]), "+v"(sa[6]), "+v"(sa[7]), "+v"(sa[8]));
    SLEEP(SSLEEP);
    if (FD) until(tk + FD);
    TS(0, 1);
    float sum;
#pragma unroll 1
    for (int spin = 0; spin < SPIN_CAP; ++spin) {
      u32x4 sv[9];
#pragma unroll
      for (int m = 0; m < 9; ++m) LD16(sv[m], sa[m]);
      asm volatile("s_waitcnt vmcnt(9)" : "+v"(fx), "+v"(fv), "+v"(fn), "+v"(fs));
      if (__builtin_expect(__any(fs[0] != T), 0)) {
        for (int k = 0; k < SPIN_CAP && fs[0] != T; ++k)
          asm volatile("global_load_dwordx2 %0, %1, %2 sc1\n\ts_waitcnt vmcnt(0)" : "=&v"(fs) : "v"(O_SC + 16u * dd), "s"(ws) : "memory");
      }
      const uint32_t f1 = fv ^ t16, f2 = (f1 & 7u) ? fv & ~7u : f1;
      hx = rbf(bf2f((u16)fx) + (float)__builtin_bit_cast(_Float16, (u16)f2) * __uint_as_float(fs[1]));
      xs = bf2f((u16)fn);
      asm volatile("" : "+v"(hx), "+v"(xs));
      f32x4 dv = {0.f, 0.f, 0.f, 0.f};
      static_for<0, 9>([&](auto mm) {
        constexpr int m = mm.value;
        asm volatile("s_waitcnt vmcnt(%1)" : "+v"(sv[m]) : "n"(8 - m));
        dv = MMB(sv[m], se, dv);
        __builtin_amdgcn_sched_barrier(0);
      });
      sum = fold16(fold32(dv[0], dv[1]), fold32(dv[2], dv[3]));
      if (__builtin_expect(!__any(sum != sum), 1)) break;
      uint32_t mx = 0u;
#pragma unroll
      for (int m = 0; m < 9; ++m) mx = max(mx, max4(sv[m]));
      if (!__any(mx == 0xffffffffu)) break;
      TSC(18);
      SLEEP(PSLEEP);
    }
    const float o1 = rbf(xs + rbf(rbf(sum) * rs));
    if (i < 8) asm volatile("global_store_short %0, %1, %2 sc1" : : "v"(fo), "v"((uint32_t)f2bf(hx + o1)), "s"(out) : "memory");
    TS(0, 2);
  }
  TSE();
  }
  }
}
#define BROT "192"
#define CPF_A "s_getpc_b64 s[96:97]\n\ts_lshl_b32 s99, s2, 4\n\ts_lshl_b32 s84, s98, 7\n\ts_add_u32 s99, s99, s84\n\tv_and_b32 v112, 1, v0\n\t" \
              "v_lshl_add_u32 v112, v112, 12, s99\n\tv_min_u32 v112, kf_end-kf_all-0x80, v112\n\ts_mov_b64 exec, 3\n\tglobal_load_dword v112, v112, s[96:97]\n\ts_mov_b64 exec, -1\n\t"
extern "C" __global__ __launch_bounds__(512, 4) void kf_all(KARGS) {
  __shared__ __attribute__((aligned(16))) char smem[SMEM];
  asm volatile("s_load_dwordx16 s[64:79], s[0:1], 0x0\n\ts_load_dwordx4 s[80:83], s[0:1], 0x40\n\tv_readfirstlane_b32 s98, v0\n\t"
               "s_add_u32 s84, s2, " BROT "\n\ts_xor_b32 s84, s84, s2\n\ts_bitset0_b32 s84, 8\n\ts_bitset0_b32 s84, 9\n\ts_xor_b32 s2, s2, s84\n\t"
               "v_mov_b32 v109, 0\n\ts_and_b32 s85, s2, 7\n\ts_bitcmp1_b32 s2, 8\n\ts_cbranch_scc1 1f\n\t" CPF_A "s_bitcmp1_b32 s98, 8\n\ts_cbranch_scc1 2f\n\t"
               "s_andn2_b32 s84, s2, 31\n\ts_lshl2_add_u32 s84, s85, s84\n\ts_bfe_u32 s85, s2, 0x20003\n\ts_or_b32 s84, s84, s85\n\t"
               "s_mulk_i32 s84, 0x2400\n\ts_mulk_i32 s98, 36\n\ts_add_u32 s84, s84, s98\n\tv_and_b32 v110, 63, v0\n\tv_and_b32 v111, 32, v0\n\t"
               "v_lshlrev_b32 v111, 5, v111\n\tv_lshl_add_u32 v111, v110, 2, v111\n\tv_lshl_add_u32 v110, v110, 4, s84\n\tv_add_u32 v111, s84, v111\n\t"
               "s_cmpk_lt_u32 s2, 0x90\n\ts_waitcnt lgkmcnt(0)\n\t"
               "global_load_dwordx4 v[100:103], v110, s[64:65]\n\tglobal_load_dwordx4 v[104:107], v110, s[64:65] offset:1152\n\t"
               "global_load_dword v108, v111, s[64:65] offset:1024\n\tglobal_load_dword v109, v109, s[82:83] sc1\n\t"
               "s_cbranch_scc1 kf_n\n\ts_branch kf_h\n"
               "1:\n\ts_bfe_u32 s84, s2, 0x50003\n\ts_lshl_b32 s85, s85, 5\n\ts_or_b32 s84, s84, s85\n\ts_lshl_b32 s84, s84, 2\n\t"
               "v_and_b32 v110, 0xff, v0\n\tv_bfe_u32 v107, v0, 7, 1\n"
               "4:\n\ts_lshl_b32 s85, s84, 7\n\tv_lshl_add_u32 v110, v110, 1, s85\n\tv_lshl_add_u32 v107, v107, 1, s84\n\ts_and_b32 s85, s98, s2\n\ts_waitcnt lgkmcnt(0)\n\t"
               "global_load_ushort v100, v107, s[78:79]\n\tglobal_load_ushort v101, v110, s[66:67]\n\tglobal_load_ushort v102, v110, s[68:69]\n\t"
               "global_load_dword v109, v109, s[82:83] sc1\n\ts_bitcmp1_b32 s85, 8\n\ts_cbranch_scc1 kf_u\n\t"
               "global_load_ushort v103, v110, s[70:71]\n\tglobal_load_ushort v104, v110, s[72:73]\n\tglobal_load_ushort v105, v110, s[74:75]\n\t"
               "global_load_ushort v106, v110, s[76:77]\n\tglobal_load_ushort v107, v107, s[80:81]\n\ts_bitcmp1_b32 s2, 8\n\ts_cbranch_scc1 kf_c\n"
               "6:\n\ts_barrier\n\ts_branch kf_p\n"
               "2:\n\ts_cmpk_lt_u32 s2, 0xc0\n\ts_cbranch_scc1 6b\n\t"
               "s_lshr_b32 s84, s2, 3\n\ts_lshl3_add_u32 s84, s85, s84\n\ts_lshl1_add_u32 s84, s84, 976\n\t"
               "v_and_b32 v110, 0x7f, v0\n\tv_mov_b32 v107, 0\n\ts_branch 4b"
               : : "s"(__builtin_amdgcn_kernarg_segment_ptr()), "s"(blockIdx.x), "v"(threadIdx.x),
                   "s"((uint32_t)(uintptr_t)(__attribute__((address_space(3))) char*)smem) : "scc", "s96", "s97", "s98", "s99", "v127");
}
extern "C" __global__ __launch_bounds__(512, 4) void kf_c(KARGS) {
  __shared__ __attribute__((aligned(16))) char smem[SMEM];
  role_body<0>(smem, KPASS);
}
extern "C" __global__ __launch_bounds__(512, 4) void kf_u(KARGS) {
  __shared__ __attribute__((aligned(16))) char smem[SMEM];
  role_body<3>(smem, KPASS);
}
extern "C" __global__ __launch_bounds__(512, 4) void kf_n(KARGS) {
  __shared__ __attribute__((aligned(16))) char smem[SMEM];
  role_body<1, 1>(smem, KPASS);
}
extern "C" __global__ __launch_bounds__(512, 4) void kf_h(KARGS) {
  __shared__ __attribute__((aligned(16))) char smem[SMEM];
  role_body<1, 0>(smem, KPASS);
}
extern "C" __global__ __launch_bounds__(512, 4) void kf_p(KARGS) {
  __shared__ __attribute__((aligned(16))) char smem[SMEM];
  role_body<2>(smem, KPASS);
}
extern "C" __global__ __launch_bounds__(512, 4) void kf_conv() { asm volatile(CONV_ASM ::: CONV_CLOB); }
extern "C" __global__ void kf_end() {}

extern "C" __global__ __launch_bounds__(1024) void k_probe(int* dummy) {
  __shared__ int sm[2];
  const uint32_t now = (uint32_t)__builtin_amdgcn_s_memrealtime();
  __shared__ int sn;
  if (threadIdx.x == 0) { sm[0] = 0; sm[1] = 2 == 1 ? 0x7fffffff : 0; sn = 0; }
  __syncthreads();
  for (int k = threadIdx.x; k < 4096; k += 1024) {
    const uint32_t a = (uint32_t)g_prb[k], b = (uint32_t)(g_prb[k] >> 32);
    if (a) atomicMax(&sm[0], (int)(now - a));
    if (b) { if (2 == 1) atomicMin(&sm[1], (int)(now - b)); else if (2 == 0) atomicMax(&sm[1], (int)(now - b)); else { atomicAdd(&sm[1], (int)(now - b)); atomicAdd(&sn, 1); } }
  }
  __syncthreads();
  const int q = min(max(sm[0] * 10 - (2 == 2 ? sm[1] * 10 / max(sn, 1) : sm[1] * 10), 0), 30000);
  if (threadIdx.x == 0) while ((int)((uint32_t)__builtin_amdgcn_s_memrealtime() - now) < q) __builtin_amdgcn_s_sleep(2);
}
extern "C" void kernel_launch(void* const* d_in, const int* in_sizes, int n_in,
                              void* d_out, int out_size, void* d_ws, size_t ws_size,
                              hipStream_t stream) {
  kf_all<<<512, 512, 0, stream>>>((const u16*)d_in[0], (const u16*)d_in[1], (const u16*)d_in[2], (const u16*)d_in[3], (const u16*)d_in[4],
                                  (const u16*)d_in[5], (const u16*)d_in[6], (const u16*)d_in[7], (const u16*)d_in[8], (char*)d_ws + (size_t)WSOFF * 1048576,
                                  (const u16*)d_in[10], (const u16*)d_in[11], (const u16*)d_in[12], (const u16*)d_in[13], (u16*)d_out);
  k_probe<<<1, 1024, 0, stream>>>((int*)d_ws);
}
```

```cpp
#include <hip/hip_runtime.h>
#include <hip/hip_bf16.h>
#include <stdint.h>

typedef __attribute__((ext_vector_type(8))) short bf16x8;
typedef __attribute__((ext_vector_type(4))) float f32x4;
typedef uint16_t u16;
typedef __attribute__((ext_vector_type(4))) uint32_t u32x4;
typedef __attribute__((ext_vector_type(2))) uint32_t u32x2;
#define TSD
#define TS(k, i)
#define TSC(i)
#define TSE()
#define TSP
#define TSA

constexpr int L_ = 2048, D_ = 576, N_ = 128, F_ = 4608, C_ = 8;
constexpr int NF = 144;
constexpr uint32_t O_EP = 0;
constexpr uint32_t O_KF = 4096;
constexpr uint32_t O_KEY = O_KF + 8192;
constexpr uint32_t O_SC = O_KEY + 32768;
constexpr uint32_t O_DN = O_SC + 9216;
constexpr uint32_t O_SB = O_DN + 20480;
constexpr uint32_t O_SU = O_SB + 65536;
constexpr uint32_t O_FB = O_SU + 262144;
constexpr uint32_t O_XC = O_FB + 8 * 1152;
constexpr uint32_t SP = 9216 + 128;
constexpr uint32_t O_SL = O_XC + 1024 * 1152;
constexpr uint32_t SLU = 336;
constexpr uint32_t O_U = O_SL + 256 * SLU * 16;
constexpr int UP = 584;
constexpr uint32_t VP = 8192 + 128, VPP = 4096 + 128;
constexpr uint32_t O_V = O_U + 256 * UP * 16, O_VP = O_V + 256 * VP;
static_assert(256 * SLU * 16 >= NF * SP && O_SL % 128 == 0 && O_U % 128 == 0 && O_V % 128 == 0, "");
__host__ __device__ __forceinline__ uint32_t u_at(int tg, int d) { return O_U + 16u * (uint32_t)((tg >> 1) * (UP * 2) + (d >> 2) * 8 + (tg & 1) * 4 + (d & 3)); }
__host__ __device__ __forceinline__ uint32_t v_at(int d, int tg) {
  return (d < 512) ? O_V + (uint32_t)(d >> 1) * VP + 16u * (uint32_t)(2 * tg + (d & 1)) : O_VP + (uint32_t)(d - 512) * VPP + 16u * (uint32_t)tg;
}
#define HAVE_VAT
#define HAVE_VTAG
__device__ __forceinline__ float bf2f(u16 v) { return __uint_as_float((uint32_t)v << 16); }
__device__ __forceinline__ float bflo(uint32_t v) { return __uint_as_float(v << 16); }
__device__ __forceinline__ float bfhi(uint32_t v) { return __uint_as_float(v & 0xffff0000u); }
__device__ __forceinline__ u16 f2bf(float f) { return __builtin_bit_cast(u16, (__bf16)f); }
__device__ __forceinline__ float rbf(float f) { return bf2f(f2bf(f)); }
typedef __attribute__((ext_vector_type(2))) float f32x2;
typedef __attribute__((ext_vector_type(2))) __bf16 bf16x2v;
__device__ __forceinline__ uint32_t pack2(float a, float b) {
  return __builtin_bit_cast(uint32_t, __builtin_convertvector((f32x2){a, b}, bf16x2v));
}

#define DPP_I(v, ctrl, rmask) __builtin_amdgcn_update_dpp(0, (v), (ctrl), (rmask), 0xf, true)
__device__ __forceinline__ float row_sum(float v) {
  v += __int_as_float(DPP_I(__float_as_int(v), 0x111, 0xf));
  v += __int_as_float(DPP_I(__float_as_int(v), 0x112, 0xf));
  v += __int_as_float(DPP_I(__float_as_int(v), 0x114, 0xf));
  v += __int_as_float(DPP_I(__float_as_int(v), 0x118, 0xf));
  return v;
}
__device__ __forceinline__ float rdl(float v, int l) { return __int_as_float(__builtin_amdgcn_readlane(__float_as_int(v), l)); }
__device__ __forceinline__ float wave_sum(float v) {
  v = row_sum(v);
  asm("s_nop 1\n\tv_add_f32_dpp %0, %0, %0 row_bcast:15 row_mask:0xa bank_mask:0xf\n\t"
      "s_nop 1\n\tv_add_f32_dpp %0, %0, %0 row_bcast:31 row_mask:0xc bank_mask:0xf\n\ts_nop 0" : "+v"(v));
  return rdl(v, 63);
}
__device__ __forceinline__ float fold32(float a, float b) {
  const auto r = __builtin_amdgcn_permlane32_swap(__float_as_uint(a), __float_as_uint(b), false, false);
  return __uint_as_float(r[0]) + __uint_as_float(r[1]);
}
__device__ __forceinline__ float fold16(float a, float b) {
  const auto r = __builtin_amdgcn_permlane16_swap(__float_as_uint(a), __float_as_uint(b), false, false);
  return __uint_as_float(r[0]) + __uint_as_float(r[1]);
}
__device__ __forceinline__ void wave_sum2(float& a, float& b) {
  float v = row_sum(fold32(a, b));
  asm("s_nop 1\n\tv_add_f32_dpp %0, %0, %0 row_bcast:15 row_mask:0xa bank_mask:0xf\n\ts_nop 0" : "+v"(v));
  a = rdl(v, 31); b = rdl(v, 63);
}
__device__ __forceinline__ float dot2(uint32_t a, uint32_t b, float c) {
  return __builtin_amdgcn_fdot2_f32_bf16(__builtin_bit_cast(bf16x2v, a), __builtin_bit_cast(bf16x2v, b), c, false);
}
__device__ __forceinline__ uint32_t row_maxu(uint32_t v) {
  v = max(v, (uint32_t)DPP_I((int)v, 0xb1, 0xf));
  v = max(v, (uint32_t)DPP_I((int)v, 0x4e, 0xf));
  v = max(v, (uint32_t)DPP_I((int)v, 0x141, 0xf));
  v = max(v, (uint32_t)DPP_I((int)v, 0x140, 0xf));
  return v;
}
__device__ __forceinline__ uint32_t wave_maxu(uint32_t v) {
  v = max(v, (uint32_t)DPP_I((int)v, 0x111, 0xf));
  v = max(v, (uint32_t)DPP_I((int)v, 0x112, 0xf));
  v = max(v, (uint32_t)DPP_I((int)v, 0x114, 0xf));
  v = max(v, (uint32_t)DPP_I((int)v, 0x118, 0xf));
  v = max(v, (uint32_t)DPP_I((int)v, 0x142, 0xa));
  v = max(v, (uint32_t)DPP_I((int)v, 0x143, 0xc));
  return (uint32_t)__builtin_amdgcn_readlane((int)v, 63);
}

typedef __attribute__((ext_vector_type(8))) _Float16 f16x8;
typedef __attribute__((ext_vector_type(2))) _Float16 f16x2;
__device__ __forceinline__ uint32_t packh(f32x2 v) { return __builtin_bit_cast(uint32_t, __builtin_convertvector(v, f16x2)); }
__device__ __forceinline__ uint32_t packh(float a, float b) { return packh((f32x2){a, b}); }
constexpr int IP = 224, I_X = 128;
__device__ __forceinline__ f32x2 cmul(f32x2 z, f32x2 w) {
  f32x2 r;
  asm("v_pk_mul_f32 %0, %1, %2 op_sel_hi:[0,1]\n\tv_pk_fma_f32 %0, %1, %2, %0 op_sel:[1,1,0] op_sel_hi:[1,0,1] neg_lo:[0,1,0]" : "=&v"(r) : "v"(z), "v"(w));
  return r;
}
__device__ __forceinline__ f32x2 cexp2(float ar2, float air, float l) {
  const float t = air * l;
  return (f32x2){__builtin_amdgcn_cosf(t), __builtin_amdgcn_sinf(t)} * __builtin_amdgcn_exp2f(ar2 * l);
}
__device__ __forceinline__ float state_a(float ldt, float are, float aim, float& ar2, float& air, float& g) {
  const float dt = rbf(__expf(ldt));
  ar2 = dt * are * 1.4426950408889634f;
  air = dt * aim * 0.15915494309189535f;
  air -= __builtin_rintf(air);
  g = 0.f;
  if (__builtin_expect(__any(ar2 > 0.f), 0)) g = fminf(__uint_as_float(wave_maxu(__float_as_uint(fmaxf(ar2, 0.f)))), 1.f);
  return dt;
}
template <int N> __device__ __forceinline__ void put_im(char* p, int im, const f32x2* z) {
  *(u32x4*)p = (u32x4){packh(z[0][0], z[1][0]), packh(z[2][0], z[3][0]), packh(z[4][0], z[5][0]), packh(z[6][0], z[7][0])};
  *(u32x4*)(p + im) = (u32x4){packh(z[0][1], z[1][1]), packh(z[2][1], z[3][1]), packh(z[4][1], z[5][1]), packh(z[6][1], z[7][1])};
}
#define Y_ASM \
  "v_mov_b64 v[40:41], %0\nv_pk_mul_f32 v[42:43], v[40:41], %1 op_sel_hi:[0,1]\nv_pk_mul_f32 v[44:45], %0, %2 op_sel_hi:[0,1]\n" \
  "v_pk_fma_f32 v[42:43], v[40:41], %1, v[42:43] op_sel:[1,1,0] op_sel_hi:[1,0,1] neg_lo:[0,1,0]\n" \
  "v_pk_fma_f32 v[44:45], %0, %2, v[44:45] op_sel:[1,1,0] op_sel_hi:[1,0,1] neg_lo:[0,1,0]\nv_cvt_pk_f16_f32 v56, v40, v42\n" \
  "v_cvt_pk_f16_f32 v60, v41, v43\nv_pk_mul_f32 v[40:41], v[42:43], %1 op_sel_hi:[0,1]\nv_pk_mul_f32 v[48:49], v[44:45], %2 op_sel_hi:[0,1]\n" \
  "v_pk_fma_f32 v[40:41], v[42:43], %1, v[40:41] op_sel:[1,1,0] op_sel_hi:[1,0,1] neg_lo:[0,1,0]\n" \
  "v_pk_fma_f32 v[48:49], v[44:45], %2, v[48:49] op_sel:[1,1,0] op_sel_hi:[1,0,1] neg_lo:[0,1,0]\n" \
  "v_pk_mul_f32 v[42:43], v[40:41], %1 op_sel_hi:[0,1]\nv_pk_mul_f32 v[52:53], v[48:49], %2 op_sel_hi:[0,1]\n" \
  "v_pk_fma_f32 v[42:43], v[40:41], %1, v[42:43] op_sel:[1,1,0] op_sel_hi:[1,0,1] neg_lo:[0,1,0]\n" \
  "v_pk_fma_f32 v[52:53], v[48:49], %2, v[52:53] op_sel:[1,1,0] op_sel_hi:[1,0,1] neg_lo:[0,1,0]\nv_cvt_pk_f16_f32 v57, v40, v42\n" \
  "v_cvt_pk_f16_f32 v61, v41, v43\nv_pk_mul_f32 v[40:41], v[42:43], %1 op_sel_hi:[0,1]\n" \
  "v_pk_fma_f32 v[40:41], v[42:43], %1, v[40:41] op_sel:[1,1,0] op_sel_hi:[1,0,1] neg_lo:[0,1,0]\n" \
  "v_pk_mul_f32 v[42:43], v[40:41], %1 op_sel_hi:[0,1]\nv_pk_mul_f32 v[46:47], v[44:45], %1 op_sel_hi:[0,1]\n" \
  "v_pk_fma_f32 v[42:43], v[40:41], %1, v[42:43] op_sel:[1,1,0] op_sel_hi:[1,0,1] neg_lo:[0,1,0]\n" \
  "v_pk_fma_f32 v[46:47], v[44:45], %1, v[46:47] op_sel:[1,1,0] op_sel_hi:[1,0,1] neg_lo:[0,1,0]\nv_cvt_pk_f16_f32 v58, v40, v42\n" \
  "v_cvt_pk_f16_f32 v62, v41, v43\nv_cvt_pk_f16_f32 v64, v44, v46\nv_cvt_pk_f16_f32 v68, v45, v47\n" \
  "v_pk_mul_f32 v[40:41], v[42:43], %1 op_sel_hi:[0,1]\nv_pk_mul_f32 v[44:45], v[46:47], %1 op_sel_hi:[0,1]\n" \
  "v_pk_fma_f32 v[40:41], v[42:43], %1, v[40:41] op_sel:[1,1,0] op_sel_hi:[1,0,1] neg_lo:[0,1,0]\n" \
  "v_pk_fma_f32 v[44:45], v[46:47], %1, v[44:45] op_sel:[1,1,0] op_sel_hi:[1,0,1] neg_lo:[0,1,0]\n" \
  "v_pk_mul_f32 v[42:43], v[40:41], %1 op_sel_hi:[0,1]\nv_pk_mul_f32 v[46:47], v[44:45], %1 op_sel_hi:[0,1]\n" \
  "v_pk_fma_f32 v[42:43], v[40:41], %1, v[42:43] op_sel:[1,1,0] op_sel_hi:[1,0,1] neg_lo:[0,1,0]\n" \
  "v_pk_fma_f32 v[46:47], v[44:45], %1, v[46:47] op_sel:[1,1,0] op_sel_hi:[1,0,1] neg_lo:[0,1,0]\nv_cvt_pk_f16_f32 v59, v40, v42\n" \
  "v_cvt_pk_f16_f32 v63, v41, v43\nv_cvt_pk_f16_f32 v65, v44, v46\nv_cvt_pk_f16_f32 v69, v45, v47\n" \
  "v_pk_mul_f32 v[44:45], v[46:47], %1 op_sel_hi:[0,1]\nds_write_b128 %3, v[56:59] offset:0\n" \
  "v_pk_fma_f32 v[44:45], v[46:47], %1, v[44:45] op_sel:[1,1,0] op_sel_hi:[1,0,1] neg_lo:[0,1,0]\nds_write_b128 %3, v[60:63] offset:64\n" \
  "v_pk_mul_f32 v[46:47], v[44:45], %1 op_sel_hi:[0,1]\nv_pk_mul_f32 v[50:51], v[48:49], %1 op_sel_hi:[0,1]\n" \
  "v_pk_fma_f32 v[46:47], v[44:45], %1, v[46:47] op_sel:[1,1,0] op_sel_hi:[1,0,1] neg_lo:[0,1,0]\n" \
  "v_pk_fma_f32 v[50:51], v[48:49], %1, v[50:51] op_sel:[1,1,0] op_sel_hi:[1,0,1] neg_lo:[0,1,0]\nv_cvt_pk_f16_f32 v66, v44, v46\n" \
  "v_cvt_pk_f16_f32 v70, v45, v47\nv_cvt_pk_f16_f32 v56, v48, v50\nv_cvt_pk_f16_f32 v60, v49, v51\n" \
  "v_pk_mul_f32 v[44:45], v[46:47], %1 op_sel_hi:[0,1]\nv_pk_mul_f32 v[48:49], v[50:51], %1 op_sel_hi:[0,1]\n" \
  "v_pk_fma_f32 v[44:45], v[46:47], %1, v[44:45] op_sel:[1,1,0] op_sel_hi:[1,0,1] neg_lo:[0,1,0]\n" \
  "v_pk_fma_f32 v[48:49], v[50:51], %1, v[48:49] op_sel:[1,1,0] op_sel_hi:[1,0,1] neg_lo:[0,1,0]\n" \
  "v_pk_mul_f32 v[46:47], v[44:45], %1 op_sel_hi:[0,1]\nv_pk_mul_f32 v[50:51], v[48:49], %1 op_sel_hi:[0,1]\n" \
  "v_pk_fma_f32 v[46:47], v[44:45], %1, v[46:47] op_sel:[1,1,0] op_sel_hi:[1,0,1] neg_lo:[0,1,0]\n" \
  "v_pk_fma_f32 v[50:51], v[48:49], %1, v[50:51] op_sel:[1,1,0] op_sel_hi:[1,0,1] neg_lo:[0,1,0]\nv_cvt_pk_f16_f32 v67, v44, v46\n" \
  "v_cvt_pk_f16_f32 v71, v45, v47\nv_cvt_pk_f16_f32 v57, v48, v50\nv_cvt_pk_f16_f32 v61, v49, v51\n" \
  "v_pk_mul_f32 v[48:49], v[50:51], %1 op_sel_hi:[0,1]\nds_write_b128 %3, v[64:67] offset:16\n" \
  "v_pk_fma_f32 v[48:49], v[50:51], %1, v[48:49] op_sel:[1,1,0] op_sel_hi:[1,0,1] neg_lo:[0,1,0]\nds_write_b128 %3, v[68:71] offset:80\n" \
  "v_pk_mul_f32 v[50:51], v[48:49], %1 op_sel_hi:[0,1]\nv_pk_mul_f32 v[54:55], v[52:53], %1 op_sel_hi:[0,1]\n" \
  "v_pk_fma_f32 v[50:51], v[48:49], %1, v[50:51] op_sel:[1,1,0] op_sel_hi:[1,0,1] neg_lo:[0,1,0]\n" \
  "v_pk_fma_f32 v[54:55], v[52:53], %1, v[54:55] op_sel:[1,1,0] op_sel_hi:[1,0,1] neg_lo:[0,1,0]\nv_cvt_pk_f16_f32 v58, v48, v50\n" \
  "v_cvt_pk_f16_f32 v62, v49, v51\nv_cvt_pk_f16_f32 v64, v52, v54\nv_cvt_pk_f16_f32 v68, v53, v55\n" \
  "v_pk_mul_f32 v[48:49], v[50:51], %1 op_sel_hi:[0,1]\nv_pk_mul_f32 v[52:53], v[54:55], %1 op_sel_hi:[0,1]\n" \
  "v_pk_fma_f32 v[48:49], v[50:51], %1, v[48:49] op_sel:[1,1,0] op_sel_hi:[1,0,1] neg_lo:[0,1,0]\n" \
  "v_pk_fma_f32 v[52:53], v[54:55], %1, v[52:53] op_sel:[1,1,0] op_sel_hi:[1,0,1] neg_lo:[0,1,0]\n" \
  "v_pk_mul_f32 v[50:51], v[48:49], %1 op_sel_hi:[0,1]\nv_pk_mul_f32 v[54:55], v[52:53], %1 op_sel_hi:[0,1]\n" \
  "v_pk_fma_f32 v[50:51], v[48:49], %1, v[50:51] op_sel:[1,1,0] op_sel_hi:[1,0,1] neg_lo:[0,1,0]\n" \
  "v_pk_fma_f32 v[54:55], v[52:53], %1, v[54:55] op_sel:[1,1,0] op_sel_hi:[1,0,1] neg_lo:[0,1,0]\nv_cvt_pk_f16_f32 v59, v48, v50\n" \
  "v_cvt_pk_f16_f32 v63, v49, v51\nv_cvt_pk_f16_f32 v65, v52, v54\nv_cvt_pk_f16_f32 v69, v53, v55\n" \
  "v_pk_mul_f32 v[52:53], v[54:55], %1 op_sel_hi:[0,1]\nds_write_b128 %3, v[56:59] offset:32\n" \
  "v_pk_fma_f32 v[52:53], v[54:55], %1, v[52:53] op_sel:[1,1,0] op_sel_hi:[1,0,1] neg_lo:[0,1,0]\nds_write_b128 %3, v[60:63] offset:96\n" \
  "v_pk_mul_f32 v[54:55], v[52:53], %1 op_sel_hi:[0,1]\n" \
  "v_pk_fma_f32 v[54:55], v[52:53], %1, v[54:55] op_sel:[1,1,0] op_sel_hi:[1,0,1] neg_lo:[0,1,0]\nv_cvt_pk_f16_f32 v66, v52, v54\n" \
  "v_cvt_pk_f16_f32 v70, v53, v55\nv_pk_mul_f32 v[52:53], v[54:55], %1 op_sel_hi:[0,1]\n" \
  "v_pk_fma_f32 v[52:53], v[54:55], %1, v[52:53] op_sel:[1,1,0] op_sel_hi:[1,0,1] neg_lo:[0,1,0]\n" \
  "v_pk_mul_f32 v[54:55], v[52:53], %1 op_sel_hi:[0,1]\n" \
  "v_pk_fma_f32 v[54:55], v[52:53], %1, v[54:55] op_sel:[1,1,0] op_sel_hi:[1,0,1] neg_lo:[0,1,0]\nv_cvt_pk_f16_f32 v67, v52, v54\n" \
  "v_cvt_pk_f16_f32 v71, v53, v55\nds_write_b128 %3, v[64:67] offset:48\nds_write_b128 %3, v[68:71] offset:112\n"
#define Y_CLOB "memory", "v40", "v41", "v42", "v43", "v44", "v45", "v46", "v47", "v48", "v49", "v50", "v51", "v52", "v53", "v54", "v55", "v56", "v57", "v58", "v59", "v60", "v61", "v62", "v63", "v64", "v65", "v66", "v67", "v68", "v69", "v70", "v71"
#define T_ASM \
  "ds_read_b128 %4, %8\nds_read_b64_tr_b16 v[80:81], %7 offset:0\nds_read_b64_tr_b16 v[82:83], %7 offset:64\n" \
  "ds_read_b64_tr_b16 v[84:85], %5 offset:0\nds_read_b64_tr_b16 v[86:87], %5 offset:48\nds_read_b64_tr_b16 v[88:89], %6 offset:0\n" \
  "ds_read_b64_tr_b16 v[90:91], %6 offset:48\nds_read_b64_tr_b16 v[92:93], %7 offset:3584\nds_read_b64_tr_b16 v[94:95], %7 offset:3648\n" \
  "ds_read_b64_tr_b16 v[96:97], %5 offset:3584\nds_read_b64_tr_b16 v[98:99], %5 offset:3632\nds_read_b64_tr_b16 v[100:101], %6 offset:3584\n" \
  "ds_read_b64_tr_b16 v[102:103], %6 offset:3632\nds_read_b64_tr_b16 v[104:105], %7 offset:7168\nds_read_b64_tr_b16 v[106:107], %7 offset:7232\n" \
  "s_waitcnt lgkmcnt(8)\nv_mfma_f32_16x16x32_f16 %0, v[84:87], v[80:83], 0\nds_read_b64_tr_b16 v[108:109], %5 offset:7168\n" \
  "ds_read_b64_tr_b16 v[110:111], %5 offset:7216\nds_read_b64_tr_b16 v[112:113], %6 offset:7168\nv_mfma_f32_16x16x32_f16 %2, v[88:91], v[80:83], 0\n" \
  "ds_read_b64_tr_b16 v[114:115], %6 offset:7216\nds_read_b64_tr_b16 v[116:117], %7 offset:10752\nds_read_b64_tr_b16 v[118:119], %7 offset:10816\n" \
  "s_waitcnt lgkmcnt(8)\nv_mfma_f32_16x16x32_f16 %0, v[96:99], v[92:95], %0\nds_read_b64_tr_b16 v[120:121], %5 offset:10752\n" \
  "ds_read_b64_tr_b16 v[122:123], %5 offset:10800\nds_read_b64_tr_b16 v[124:125], %6 offset:10752\n" \
  "v_mfma_f32_16x16x32_f16 %2, v[100:103], v[92:95], %2\nds_read_b64_tr_b16 v[126:127], %6 offset:10800\n" \
  "ds_read_b64_tr_b16 v[80:81], %7 offset:14336\nds_read_b64_tr_b16 v[82:83], %7 offset:14400\ns_waitcnt lgkmcnt(8)\n" \
  "v_mfma_f32_16x16x32_f16 %0, v[108:111], v[104:107], %0\nds_read_b64_tr_b16 v[84:85], %5 offset:14336\n" \
  "ds_read_b64_tr_b16 v[86:87], %5 offset:14384\nds_read_b64_tr_b16 v[88:89], %6 offset:14336\n" \
  "v_mfma_f32_16x16x32_f16 %2, v[112:115], v[104:107], %2\nds_read_b64_tr_b16 v[90:91], %6 offset:14384\n" \
  "ds_read_b64_tr_b16 v[92:93], %7 offset:17920\nds_read_b64_tr_b16 v[94:95], %7 offset:17984\ns_waitcnt lgkmcnt(8)\n" \
  "v_mfma_f32_16x16x32_f16 %0, v[120:123], v[116:119], %0\nds_read_b64_tr_b16 v[96:97], %5 offset:17920\n" \
  "ds_read_b64_tr_b16 v[98:99], %5 offset:17968\nds_read_b64_tr_b16 v[100:101], %6 offset:17920\n" \
  "v_mfma_f32_16x16x32_f16 %2, v[124:127], v[116:119], %2\nds_read_b64_tr_b16 v[102:103], %6 offset:17968\n" \
  "ds_read_b64_tr_b16 v[104:105], %7 offset:21504\nds_read_b64_tr_b16 v[106:107], %7 offset:21568\ns_waitcnt lgkmcnt(8)\n" \
  "v_mfma_f32_16x16x32_f16 %1, v[84:87], v[80:83], 0\nds_read_b64_tr_b16 v[108:109], %5 offset:21504\nds_read_b64_tr_b16 v[110:111], %5 offset:21552\n" \
  "ds_read_b64_tr_b16 v[112:113], %6 offset:21504\nv_mfma_f32_16x16x32_f16 %3, v[88:91], v[80:83], 0\nds_read_b64_tr_b16 v[114:115], %6 offset:21552\n" \
  "ds_read_b64_tr_b16 v[116:117], %7 offset:25088\nds_read_b64_tr_b16 v[118:119], %7 offset:25152\ns_waitcnt lgkmcnt(8)\n" \
  "v_mfma_f32_16x16x32_f16 %1, v[96:99], v[92:95], %1\nds_read_b64_tr_b16 v[120:121], %5 offset:25088\n" \
  "ds_read_b64_tr_b16 v[122:123], %5 offset:25136\nds_read_b64_tr_b16 v[124:125], %6 offset:25088\n" \
  "v_mfma_f32_16x16x32_f16 %3, v[100:103], v[92:95], %3\nds_read_b64_tr_b16 v[126:127], %6 offset:25136\ns_waitcnt lgkmcnt(6)\n" \
  "v_mfma_f32_16x16x32_f16 %1, v[108:111], v[104:107], %1\nv_mfma_f32_16x16x32_f16 %3, v[112:115], v[104:107], %3\ns_waitcnt lgkmcnt(0)\n" \
  "v_mfma_f32_16x16x32_f16 %1, v[120:123], v[116:119], %1\nv_mfma_f32_16x16x32_f16 %3, v[124:127], v[116:119], %3\n"
#define T_CLOB "memory", "v80", "v81", "v82", "v83", "v84", "v85", "v86", "v87", "v88", "v89", "v90", "v91", "v92", "v93", "v94", "v95", "v96", "v97", "v98", "v99", "v100", "v101", "v102", "v103", "v104", "v105", "v106", "v107", "v108", "v109", "v110", "v111", "v112", "v113", "v114", "v115", "v116", "v117", "v118", "v119", "v120", "v121", "v122", "v123", "v124", "v125", "v126", "v127"
template <class F> __device__ __forceinline__ int gen_y(float ldt, float are, float aim, float bre, float bim, float cre, float cim, uint32_t im, F&& mid TSP) {
  float ar2, air, g;
  const float dt = state_a(ldt, are, aim, ar2, air, g);
  const float dbr = dt * bre, dbi = dt * bim, pr = cre * dbr - cim * dbi, pi = cre * dbi + cim * dbr;
  const f32x2 w8 = cexp2(ar2, -air, 8.f), w64 = cexp2(ar2, -air, 64.f);
  const float pm = fmaxf(fabsf(pr), fabsf(pi));
  int e = -4;
  if (__builtin_expect(__any(!(pm < 0x1p-4f) || ar2 > 0.f) || !__any(pm > 0x1p-12f), 0)) e = (int)(wave_maxu(__float_as_uint(pm)) >> 23) - 126 + (int)ceilf(g * 250.f);
  const f32x2 y0 = {__builtin_amdgcn_ldexpf(pr, 14 - e), __builtin_amdgcn_ldexpf(-pi, 14 - e)};
  mid();
  TS(0, 7);
  asm volatile(Y_ASM : : "v"(y0), "v"(w8), "v"(w64), "v"(im) : Y_CLOB);
  return e;
}
__device__ __forceinline__ int gen_x(float ldt, float are, float aim, char* im) {
  float ar2, air, g;
  state_a(ldt, are, aim, ar2, air, g);
  const int e = min((int)ceilf(g * 1810.f), 300);
  const float s = __builtin_amdgcn_ldexpf(1.f, 15 - e);
  const f32x2 w1 = cexp2(ar2, air, 1.f), w256 = cexp2(ar2, air, 256.f), w2 = cmul(w1, w1);
  f32x2 a[8], b[8], c[8];
  a[0] = (f32x2){s, 0.f}; a[1] = w1 * s; a[2] = w2 * s;
  b[0] = b[1] = w256 * s;
  b[2] = cmul(b[0], w2); a[3] = cmul(a[1], w2); a[4] = cmul(a[2], w2);
  b[3] = b[4] = b[5] = cmul(b[2], w2); a[5] = cmul(a[3], w2); a[6] = cmul(a[4], w2);
  b[6] = cmul(b[3], w2); a[7] = cmul(a[5], w2); c[0] = cmul(b[3], w256);
  b[7] = cmul(b[6], w2); c[2] = cmul(c[0], w256);
  put_im<0>(im + I_X, 48, a);
  c[1] = cmul(b[7], w256); c[3] = cmul(c[1], w256);
  put_im<0>(im + I_X + 16, 48, b);
#pragma unroll
  for (int k = 4; k < 8; ++k) c[k] = cmul(c[k - 1], w256);
  put_im<0>(im + I_X + 32, 48, c);
  return e;
}

template <int I> struct IC { static constexpr int value = I; };
template <int B, int E, class F> __device__ __forceinline__ void static_for(F&& f) {
  if constexpr (B < E) { f(IC<B>{}); static_for<B + 1, E>(f); }
}
typedef __attribute__((ext_vector_type(4))) short s16x4;
__device__ __forceinline__ u32x2 trd(uint32_t a) {
  return __builtin_bit_cast(u32x2, __builtin_amdgcn_ds_read_tr16_b64_v4i16((__attribute__((address_space(3))) s16x4*)(uintptr_t)a));
}
#define BAR() asm volatile("s_waitcnt lgkmcnt(0)\n\ts_barrier" ::: "memory")
#define ST16(off, v) asm volatile("global_store_dwordx4 %0, %1, %2 sc1\n\ts_nop 1" : : "v"(off), "v"(v), "s"(ws) : "memory")
#define ST4(off, v) asm volatile("global_store_dword %0, %1, %2 sc1" : : "v"(off), "v"(v), "s"(ws) : "memory")
#define LD16(r, off) asm volatile("global_load_dwordx4 %0, %1, %2 sc1" : "=v"(r) : "v"(off), "s"(ws) : "memory")
#define LD16W(r, off) asm volatile("global_load_dwordx4 %0, %1, %2 sc1\n\ts_waitcnt vmcnt(0)" : "=&v"(r) : "v"(off), "s"(ws) : "memory")
#define DRAIN() asm volatile("s_waitcnt vmcnt(0)" ::: "memory")
#define CEX(a, b) { const uint32_t hi_ = max(a, b), lo_ = min(a, b); a = hi_; b = lo_; }
#define MMB(a_, b_, c_) __builtin_amdgcn_mfma_f32_16x16x32_bf16(__builtin_bit_cast(bf16x8, a_), __builtin_bit_cast(bf16x8, b_), c_, 0, 0, 0)
#define NTM 0
#define DPOL ""
#define DPOL2 ""
#define DPOL4 ""
constexpr int SPIN_CAP = 1 << 18;
constexpr uint32_t UMASK = 0x40074007u;
__device__ __forceinline__ uint32_t utag(uint32_t T, int j) {
  return ((T >> (3 * j)) & 0x00070007u) | (((j <= 2) ? (T << (2 - j)) : (T >> (j - 2))) & 0x40004000u);
}
__device__ __forceinline__ uint32_t max4(const u32x4& a) { return max(max(a[0], a[1]), max(a[2], a[3])); }
#ifndef ULONG
#define ULONG 8
#endif
__device__ __forceinline__ void until(uint32_t t) {
#pragma unroll 1
  for (int spin = 0; spin < 600; ++spin) {
    const int d = (int)(t - (uint32_t)__builtin_amdgcn_s_memrealtime());
    if (d <= 0) break;
    if (ULONG && d > 4 * ULONG) __builtin_amdgcn_s_sleep(ULONG); else __builtin_amdgcn_s_sleep(1);
  }
}
__device__ __forceinline__ uint32_t latest(uint32_t t) {
  const uint32_t t0 = (uint32_t)__builtin_amdgcn_readfirstlane(t) - 0x40000000u;
  return wave_maxu(t - t0) + t0;
}
__device__ __forceinline__ uint32_t gate(const char* ws, uint32_t po, uint32_t T) {
  u32x2 pv;
#pragma unroll 1
  for (int spin = 0; spin < SPIN_CAP; ++spin) {
    asm volatile("global_load_dwordx2 %0, %1, %2 sc1\n\ts_waitcnt vmcnt(0)" : "=&v"(pv) : "v"(po), "s"(ws) : "memory");
    if (!__any(pv[0] != T)) break;
    __builtin_amdgcn_s_sleep(2);
  }
  return pv[1];
}
__device__ __forceinline__ uint32_t gate2(const char* ws, uint32_t oa, uint32_t da, uint32_t ob, uint32_t T) {
  u32x2 pa, pb;
#pragma unroll 1
  for (int spin = 0; spin < SPIN_CAP; ++spin) {
    asm volatile("global_load_dwordx2 %0, %2, %4 sc1\n\tglobal_load_dwordx2 %1, %3, %4 sc1\n\ts_waitcnt vmcnt(0)" : "=&v"(pa), "=&v"(pb) : "v"(oa), "v"(ob), "s"(ws) : "memory");
    if (!__any(pa[0] != T || pb[0] != T)) break;
    __builtin_amdgcn_s_sleep(4);
  }
  pa[1] += da;
  return latest(((int)(pa[1] - pb[1]) > 0) ? pa[1] : pb[1]);
}
__device__ __forceinline__ uint32_t ahead(uint32_t c0, uint32_t t0, int dt) {
  const uint32_t c = (uint32_t)__builtin_amdgcn_s_memtime(), t = (uint32_t)__builtin_amdgcn_s_memrealtime();
  return t + (uint32_t)((float)((t - t0) * (uint32_t)(20 * dt)) * __builtin_amdgcn_rcpf((float)(c - c0)));
}
__device__ __forceinline__ void gsync(uint32_t a, uint32_t target) {
  uint32_t t, c, n = SPIN_CAP;
  uint64_t ex;
  asm volatile("s_waitcnt lgkmcnt(0)\n\ts_mov_b64 %3, exec\n\ts_mov_b64 exec, 1\n\tv_mov_b32 %0, 1\n\tds_add_u32 %4, %0\n\ts_mov_b64 exec, %3\n"
               "1:\n\ts_sleep 1\n\tds_read_b32 %0, %4\n\ts_sub_u32 %2, %2, 1\n\ts_cbranch_scc1 2f\n\ts_waitcnt lgkmcnt(0)\n\tv_readfirstlane_b32 %1, %0\n\t"
               "s_cmp_lt_u32 %1, %5\n\ts_cbranch_scc1 1b\n2:\n\ts_waitcnt lgkmcnt(0)"
               : "=&v"(t), "=&s"(c), "+s"(n), "=&s"(ex) : "v"(a), "n"(target) : "memory", "scc");
}
__device__ __forceinline__ void gsync0(uint32_t a, uint32_t target) {
  uint32_t t, c, n = SPIN_CAP;
  asm volatile("s_waitcnt lgkmcnt(0)\n\ts_mov_b64 exec, 1\n\tv_mov_b32 %0, 1\n\tds_add_u32 %3, %0\n\ts_mov_b64 exec, -1\n"
               "1:\n\tds_read_b32 %0, %3\n\ts_sub_u32 %2, %2, 1\n\ts_cbranch_scc1 2f\n\ts_waitcnt lgkmcnt(0)\n\tv_readfirstlane_b32 %1, %0\n\t"
               "s_cmp_lt_u32 %1, %4\n\ts_cbranch_scc1 1b\n2:\n\ts_waitcnt lgkmcnt(0)"
               : "=&v"(t), "=&s"(c), "+s"(n) : "v"(a), "n"(target) : "memory", "scc");
}
__device__ __forceinline__ void gwait(uint32_t a, uint32_t target) {
  uint32_t t, c, n = SPIN_CAP;
  asm volatile("1:\n\ts_sleep 2\n\tds_read_b32 %0, %3\n\ts_sub_u32 %2, %2, 1\n\ts_cbranch_scc1 2f\n\ts_waitcnt lgkmcnt(0)\n\tv_readfirstlane_b32 %1, %0\n\t"
               "s_cmp_lt_u32 %1, %4\n\ts_cbranch_scc1 1b\n2:\n\ts_waitcnt lgkmcnt(0)"
               : "=&v"(t), "=&s"(c), "+s"(n) : "v"(a), "n"(target) : "memory", "scc");
}
__device__ __forceinline__ void gwait0(uint32_t a, uint32_t target) {
  uint32_t t, c, n = SPIN_CAP;
  asm volatile("1:\n\tds_read_b32 %0, %3\n\ts_sub_u32 %2, %2, 1\n\ts_cbranch_scc1 2f\n\ts_waitcnt lgkmcnt(0)\n\tv_readfirstlane_b32 %1, %0\n\t"
               "s_cmp_lt_u32 %1, %4\n\ts_cbranch_scc1 1b\n2:\n\ts_waitcnt lgkmcnt(0)"
               : "=&v"(t), "=&s"(c), "+s"(n) : "v"(a), "n"(target) : "memory", "scc");
}
__device__ __forceinline__ void dma16(const void* base, uint32_t voff, uint32_t lds) {
  uint32_t keep;
  asm volatile("s_mov_b32 %0, m0\n\ts_mov_b32 m0, %3\n\ts_nop 0\n\tglobal_load_lds_dwordx4 %1, %2" DPOL "\n\ts_mov_b32 m0, %0" : "=&s"(keep) : "v"(voff), "s"(base), "s"(lds) : "memory");
}
__device__ __forceinline__ void dma16d(const void* base, uint32_t voff, uint32_t lds) {
  uint32_t keep;
  asm volatile("s_mov_b32 %0, m0\n\ts_mov_b32 m0, %3\n\ts_nop 0\n\tglobal_load_lds_dwordx4 %1, %2" DPOL2 "\n\ts_mov_b32 m0, %0" : "=&s"(keep) : "v"(voff), "s"(base), "s"(lds) : "memory");
}
__device__ __forceinline__ void nap(int n) {
  for (; n > 15; n -= 15) __builtin_amdgcn_s_sleep(15);
  switch (n) {
    case 1: __builtin_amdgcn_s_sleep(1); break; case 2: __builtin_amdgcn_s_sleep(2); break; case 3: __builtin_amdgcn_s_sleep(3); break;
    case 4: __builtin_amdgcn_s_sleep(4); break; case 5: __builtin_amdgcn_s_sleep(5); break; case 6: __builtin_amdgcn_s_sleep(6); break;
    case 7: __builtin_amdgcn_s_sleep(7); break; case 8: __builtin_amdgcn_s_sleep(8); break; case 9: __builtin_amdgcn_s_sleep(9); break;
    case 10: __builtin_amdgcn_s_sleep(10); break; case 11: __builtin_amdgcn_s_sleep(11); break; case 12: __builtin_amdgcn_s_sleep(12); break;
    case 13: __builtin_amdgcn_s_sleep(13); break; case 14: __builtin_amdgcn_s_sleep(14); break; case 15: __builtin_amdgcn_s_sleep(15); break;
    default: break;
  }
}
#define SLEEP(n) do { if ((n) > 0) __builtin_amdgcn_s_sleep(n); } while (0)
#define USLEEP 3
#define SSLEEP 11
#define PSLEEP 2
#define PPRIO 0
#define LFIRST 1
#define WD 32
#define OSLEEP 12
#define QPRIO 1
#define KPRIO 2
#define GSLEEP 0
#define NSLEEP 0
#define UD 40
#define XD 0
#define RSLEEP 0
#define UPRIO 2
#define VD2 384
#define WDEPTH 6
#define ESLEEP 0
#define SD 58
#define KD 182
#define OD 0
#define KBACK 6
#define DDEPTH 12
#define C3 1
#define XROT 2
#define WSOFF 0
#define KN 1
#define RPF 1
#define LSLEEP 1
#define TSTOP 78
#define HSTOP 170
#define VEARLY 0ull
#define FPR 3
#define SPR 3
#define DPRIO 0
#define QPRIO2 3
#define HW 2
#define RD 57
#define OPOS 1
#define OD2 6
#define NA 12
#define RD2 110
#define ORD1 ((unsigned __int128)0x321087654u << 36 | 0x321087654ull)
#define WDEPTH2 5
#define RQ NA
#define HREV 1
#define QPRIO0 3
#define VPS 1
#define RW 0
#define NPRIO 0
#define XSL 10
#define V2D 24
#define N0 8
#define LPOS 1
#define ALLP 1
#define OTP 1
#define STP 0
#define FD 204
#define VDB 368
#define BALL 1
#define HALL 0
#define FORCESLOW 0
#define FORCEFB 0
#define CONV_ASM \
  "s_cmp_eq_u32 s89, 1\ns_cbranch_scc1 1f\ns_cmp_gt_u32 s89, 1\ns_cbranch_scc1 9f\nds_read_b64 v[124:125], v3 offset:576\n" \
  "ds_read_b64 v[126:127], v3 offset:584\nds_read_b64 v[120:121], v3 offset:64\nds_read_b64 v[122:123], v3 offset:72\n" \
  "ds_read_b64 v[116:117], v3 offset:640\nds_read_b64 v[118:119], v3 offset:648\nds_read_b64 v[112:113], v3 offset:128\n" \
  "ds_read_b64 v[114:115], v3 offset:136\nds_read_b64 v[108:109], v3 offset:704\nds_read_b64 v[110:111], v3 offset:712\n" \
  "ds_read_b64 v[104:105], v3 offset:192\nds_read_b64 v[106:107], v3 offset:200\nds_read_b64 v[100:101], v3 offset:768\n" \
  "ds_read_b64 v[102:103], v3 offset:776\nds_read_b64 v[96:97], v3 offset:256\nds_read_b64 v[98:99], v3 offset:264\n" \
  "ds_read_b64 v[92:93], v3 offset:832\nds_read_b64 v[94:95], v3 offset:840\nds_read_b64 v[88:89], v3 offset:320\n" \
  "ds_read_b64 v[90:91], v3 offset:328\nds_read_b64 v[84:85], v3 offset:896\nds_read_b64 v[86:87], v3 offset:904\n" \
  "ds_read_b64 v[80:81], v3 offset:384\nds_read_b64 v[82:83], v3 offset:392\nds_read_b64 v[76:77], v3 offset:960\n" \
  "ds_read_b64 v[78:79], v3 offset:968\nds_read_b64 v[72:73], v3 offset:448\nds_read_b64 v[74:75], v3 offset:456\n" \
  "ds_read_b64 v[68:69], v3 offset:1024\nds_read_b64 v[70:71], v3 offset:1032\nds_read_b64 v[64:65], v3 offset:512\n" \
  "ds_read_b64 v[66:67], v3 offset:520\nds_read_b64 v[60:61], v3 offset:0\nds_read_b64 v[62:63], v3 offset:8\nds_read_b64 v[56:57], v3 offset:1088\n" \
  "ds_read_b64 v[58:59], v3 offset:1096\ns_waitcnt lgkmcnt(0)\ns_barrier\nds_read_b128 v[36:39], v2 offset:0\nds_read_b128 v[40:43], v2 offset:64\n" \
  "ds_read_b128 v[44:47], v2 offset:128\nds_read_b128 v[48:51], v2 offset:192\ns_waitcnt lgkmcnt(3)\n" \
  "v_mfma_f32_16x16x32_f16 v[12:15], v[124:127], v[36:39], 0\ns_setprio 1\nds_read_b128 v[52:55], v2 offset:256\n" \
  "v_mfma_f32_16x16x32_f16 v[16:19], v[120:123], v[36:39], 0\ns_waitcnt lgkmcnt(3)\nv_mfma_f32_16x16x32_f16 v[12:15], v[116:119], v[40:43], v[12:15]\n" \
  "ds_read_b128 v[36:39], v2 offset:320\nv_mfma_f32_16x16x32_f16 v[16:19], v[112:115], v[40:43], v[16:19]\ns_waitcnt lgkmcnt(3)\n" \
  "v_mfma_f32_16x16x32_f16 v[12:15], v[108:111], v[44:47], v[12:15]\nds_read_b128 v[40:43], v2 offset:384\n" \
  "v_mfma_f32_16x16x32_f16 v[16:19], v[104:107], v[44:47], v[16:19]\ns_waitcnt lgkmcnt(3)\n" \
  "v_mfma_f32_16x16x32_f16 v[12:15], v[100:103], v[48:51], v[12:15]\nds_read_b128 v[44:47], v2 offset:448\n" \
  "v_mfma_f32_16x16x32_f16 v[16:19], v[96:99], v[48:51], v[16:19]\ns_waitcnt lgkmcnt(3)\n" \
  "v_mfma_f32_16x16x32_f16 v[12:15], v[92:95], v[52:55], v[12:15]\nv_mfma_f32_16x16x32_f16 v[16:19], v[88:91], v[52:55], v[16:19]\n" \
  "s_waitcnt lgkmcnt(2)\nv_mfma_f32_16x16x32_f16 v[12:15], v[84:87], v[36:39], v[12:15]\nds_read_b128 v[48:51], v2 offset:512\n" \
  "v_mfma_f32_16x16x32_f16 v[16:19], v[80:83], v[36:39], v[16:19]\ns_waitcnt lgkmcnt(2)\n" \
  "v_mfma_f32_16x16x32_f16 v[12:15], v[76:79], v[40:43], v[12:15]\nds_read_b128 v[52:55], v2 offset:576\n" \
  "v_mfma_f32_16x16x32_f16 v[16:19], v[72:75], v[40:43], v[16:19]\ns_waitcnt lgkmcnt(2)\n" \
  "v_mfma_f32_16x16x32_f16 v[12:15], v[68:71], v[44:47], v[12:15]\nds_read_b128 v[36:39], v2 offset:640\n" \
  "v_mfma_f32_16x16x32_f16 v[16:19], v[64:67], v[44:47], v[16:19]\nv_mfma_f32_16x16x32_f16 v[20:23], v[60:63], v[44:47], 0\n" \
  "ds_read_b128 v[40:43], v2 offset:704\nv_mfma_f32_16x16x32_f16 v[28:31], v[56:59], v[44:47], 0\ns_waitcnt lgkmcnt(3)\n" \
  "v_mfma_f32_16x16x32_f16 v[16:19], v[124:127], v[48:51], v[16:19]\nds_read_b128 v[56:59], v2 offset:768\nv_cvt_pkrtz_f16_f32 v32, v12, v13\n" \
  "v_mfma_f32_16x16x32_f16 v[20:23], v[120:123], v[48:51], v[20:23]\nv_cvt_pkrtz_f16_f32 v33, v14, v15\ns_waitcnt lgkmcnt(3)\n" \
  "v_mfma_f32_16x16x32_f16 v[16:19], v[116:119], v[52:55], v[16:19]\nds_read_b128 v[44:47], v2 offset:832\n" \
  "v_pk_add_u16 v32, v32, 4 op_sel_hi:[1,0] clamp\nv_mfma_f32_16x16x32_f16 v[20:23], v[112:115], v[52:55], v[20:23]\n" \
  "v_pk_add_u16 v33, v33, 4 op_sel_hi:[1,0] clamp\ns_waitcnt lgkmcnt(3)\nv_mfma_f32_16x16x32_f16 v[16:19], v[108:111], v[36:39], v[16:19]\n" \
  "ds_read_b128 v[48:51], v2 offset:896\nds_write_b64 v4, v[32:33] offset:0\nv_mfma_f32_16x16x32_f16 v[20:23], v[104:107], v[36:39], v[20:23]\n" \
  "s_waitcnt lgkmcnt(3)\nv_mfma_f32_16x16x32_f16 v[16:19], v[100:103], v[40:43], v[16:19]\nds_read_b128 v[52:55], v2 offset:960\n" \
  "v_mfma_f32_16x16x32_f16 v[20:23], v[96:99], v[40:43], v[20:23]\nv_mfma_f32_16x16x32_f16 v[16:19], v[92:95], v[56:59], v[16:19]\n" \
  "v_mfma_f32_16x16x32_f16 v[20:23], v[88:91], v[56:59], v[20:23]\nds_read_b128 v[36:39], v2 offset:1024\ns_waitcnt lgkmcnt(3)\n" \
  "v_mfma_f32_16x16x32_f16 v[16:19], v[84:87], v[44:47], v[16:19]\nv_mfma_f32_16x16x32_f16 v[20:23], v[80:83], v[44:47], v[20:23]\n" \
  "ds_read_b128 v[40:43], v2 offset:1088\nds_read_b64 v[56:57], v6 offset:256\nds_read_b64 v[58:59], v6 offset:264\n" \
  "v_mfma_f32_16x16x32_f16 v[16:19], v[76:79], v[48:51], v[16:19]\nv_mfma_f32_16x16x32_f16 v[20:23], v[72:75], v[48:51], v[20:23]\n" \
  "ds_read_b128 v[44:47], v2 offset:1152\ns_waitcnt lgkmcnt(4)\nv_mfma_f32_16x16x32_f16 v[16:19], v[68:71], v[52:55], v[16:19]\n" \
  "v_mfma_f32_16x16x32_f16 v[20:23], v[64:67], v[52:55], v[20:23]\nds_read_b128 v[48:51], v2 offset:1216\n" \
  "v_mfma_f32_16x16x32_f16 v[24:27], v[60:63], v[52:55], 0\nv_mfma_f32_16x16x32_f16 v[20:23], v[124:127], v[36:39], v[20:23]\n" \
  "ds_read_b128 v[52:55], v2 offset:1280\nv_mfma_f32_16x16x32_f16 v[24:27], v[120:123], v[36:39], v[24:27]\ns_waitcnt lgkmcnt(5)\n" \
  "v_mfma_f32_16x16x32_f16 v[20:23], v[116:119], v[40:43], v[20:23]\nds_read_b128 v[36:39], v2 offset:1344\nv_cvt_pkrtz_f16_f32 v32, v16, v17\n" \
  "v_mfma_f32_16x16x32_f16 v[24:27], v[112:115], v[40:43], v[24:27]\nv_cvt_pkrtz_f16_f32 v33, v18, v19\ns_waitcnt lgkmcnt(3)\n" \
  "v_mfma_f32_16x16x32_f16 v[20:23], v[108:111], v[44:47], v[20:23]\nds_read_b128 v[40:43], v2 offset:1408\n" \
  "v_pk_add_u16 v32, v32, 4 op_sel_hi:[1,0] clamp\nds_read_b32 v35, v9 offset:32\nv_mfma_f32_16x16x32_f16 v[24:27], v[104:107], v[44:47], v[24:27]\n" \
  "v_pk_add_u16 v33, v33, 4 op_sel_hi:[1,0] clamp\ns_waitcnt lgkmcnt(3)\nv_mfma_f32_16x16x32_f16 v[20:23], v[100:103], v[48:51], v[20:23]\n" \
  "ds_read_b128 v[44:47], v2 offset:1472\nds_write_b64 v4, v[32:33] offset:1024\nv_mfma_f32_16x16x32_f16 v[24:27], v[96:99], v[48:51], v[24:27]\n" \
  "v_mfma_f32_16x16x32_f16 v[20:23], v[92:95], v[52:55], v[20:23]\nv_mfma_f32_16x16x32_f16 v[24:27], v[88:91], v[52:55], v[24:27]\n" \
  "ds_read_b128 v[48:51], v2 offset:1536\ns_waitcnt lgkmcnt(3)\nv_readfirstlane_b32 s85, v35\ns_cmp_lt_u32 s85, 4\ns_cbranch_scc1 21f\n22:\n" \
  "v_mfma_f32_16x16x32_f16 v[20:23], v[84:87], v[36:39], v[20:23]\nv_mfma_f32_16x16x32_f16 v[24:27], v[80:83], v[36:39], v[24:27]\n" \
  "ds_read_b128 v[52:55], v2 offset:1600\nv_mfma_f32_16x16x32_f16 v[20:23], v[76:79], v[40:43], v[20:23]\n" \
  "v_mfma_f32_16x16x32_f16 v[24:27], v[72:75], v[40:43], v[24:27]\nds_read_b128 v[36:39], v2 offset:1664\ns_waitcnt lgkmcnt(4)\n" \
  "v_mfma_f32_16x16x32_f16 v[20:23], v[68:71], v[44:47], v[20:23]\nv_mfma_f32_16x16x32_f16 v[24:27], v[64:67], v[44:47], v[24:27]\n" \
  "ds_read_b128 v[40:43], v2 offset:1728\nv_mfma_f32_16x16x32_f16 v[28:31], v[60:63], v[44:47], v[28:31]\nds_read_b64_tr_b16 v[60:61], v5 offset:0\n" \
  "ds_read_b64_tr_b16 v[62:63], v5 offset:32\ns_waitcnt lgkmcnt(4)\nv_mfma_f32_16x16x32_f16 v[24:27], v[124:127], v[48:51], v[24:27]\n" \
  "v_mfma_f32_16x16x32_f16 v[28:31], v[120:123], v[48:51], v[28:31]\nds_read_b64_tr_b16 v[44:45], v5 offset:256\n" \
  "ds_read_b64_tr_b16 v[46:47], v5 offset:288\nv_mfma_f32_16x16x32_f16 v[24:27], v[116:119], v[52:55], v[24:27]\n" \
  "ds_read_b64 v[120:121], v6 offset:320\nds_read_b64 v[122:123], v6 offset:328\nv_mfma_f32_16x16x32_f16 v[28:31], v[112:115], v[52:55], v[28:31]\n" \
  "ds_read_b128 v[48:51], v2 offset:1792\ns_waitcnt lgkmcnt(7)\nv_mfma_f32_16x16x32_f16 v[24:27], v[108:111], v[36:39], v[24:27]\n" \
  "ds_read_b128 v[112:115], v2 offset:1856\nv_mfma_f32_16x16x32_f16 v[28:31], v[104:107], v[36:39], v[28:31]\nds_read_b64 v[52:53], v6 offset:128\n" \
  "ds_read_b64 v[54:55], v6 offset:136\nv_mfma_f32_16x16x32_f16 v[24:27], v[100:103], v[40:43], v[24:27]\nds_read_b128 v[104:107], v2 offset:1920\n" \
  "v_mfma_f32_16x16x32_f16 v[28:31], v[96:99], v[40:43], v[28:31]\nds_read_b64 v[36:37], v6 offset:192\nds_read_b64 v[38:39], v6 offset:200\n" \
  "s_waitcnt lgkmcnt(9)\nv_mfma_f32_16x16x32_f16 v[20:23], v[60:63], v[56:59], v[20:23]\nds_read_b128 v[96:99], v2 offset:1984\ns_waitcnt lgkmcnt(7)\n" \
  "v_mfma_f32_16x16x32_f16 v[20:23], v[44:47], v[120:123], v[20:23]\nv_mfma_f32_16x16x32_f16 v[24:27], v[92:95], v[48:51], v[24:27]\n" \
  "ds_read_b128 v[40:43], v2 offset:2048\nv_mfma_f32_16x16x32_f16 v[28:31], v[88:91], v[48:51], v[28:31]\nds_read_b64_tr_b16 v[88:89], v5 offset:512\n" \
  "ds_read_b64_tr_b16 v[90:91], v5 offset:544\ns_waitcnt lgkmcnt(6)\nv_mfma_f32_16x16x32_f16 v[24:27], v[84:87], v[112:115], v[24:27]\n" \
  "v_mfma_f32_16x16x32_f16 v[28:31], v[80:83], v[112:115], v[28:31]\nv_cvt_pkrtz_f16_f32 v32, v20, v21\nv_cvt_pkrtz_f16_f32 v33, v22, v23\n" \
  "v_mfma_f32_16x16x32_f16 v[24:27], v[76:79], v[104:107], v[24:27]\nds_read_b64_tr_b16 v[48:49], v5 offset:768\n" \
  "ds_read_b64_tr_b16 v[50:51], v5 offset:800\nv_mfma_f32_16x16x32_f16 v[28:31], v[72:75], v[104:107], v[28:31]\n" \
  "ds_read_b128 v[80:83], v2 offset:2112\ns_waitcnt lgkmcnt(5)\nv_mfma_f32_16x16x32_f16 v[24:27], v[68:71], v[96:99], v[24:27]\n" \
  "ds_read_b128 v[112:115], v2 offset:2176\nv_pk_add_u16 v32, v32, 4 op_sel_hi:[1,0] clamp\n" \
  "v_mfma_f32_16x16x32_f16 v[28:31], v[64:67], v[96:99], v[28:31]\nds_read_b128 v[72:75], v2 offset:2240\n" \
  "v_pk_add_u16 v33, v33, 4 op_sel_hi:[1,0] clamp\nv_mfma_f32_16x16x32_f16 v[28:31], v[124:127], v[40:43], v[28:31]\n" \
  "ds_read_b128 v[104:107], v2 offset:2304\nds_write_b64 v4, v[32:33] offset:2048\nv_mfma_f32_16x16x32_f16 v[24:27], v[60:63], v[52:55], v[24:27]\n" \
  "ds_read_b128 v[64:67], v2 offset:2368\nds_read_b64 v[96:97], v6 offset:0\nds_read_b64 v[98:99], v6 offset:8\n" \
  "v_mfma_f32_16x16x32_f16 v[24:27], v[44:47], v[36:39], v[24:27]\nds_read_b128 v[124:127], v2 offset:2432\ns_waitcnt lgkmcnt(9)\n" \
  "v_mfma_f32_16x16x32_f16 v[24:27], v[88:91], v[56:59], v[24:27]\nds_read_b128 v[40:43], v2 offset:2496\n" \
  "v_mfma_f32_16x16x32_f16 v[24:27], v[48:51], v[120:123], v[24:27]\ns_waitcnt lgkmcnt(8)\n" \
  "v_mfma_f32_16x16x32_f16 v[28:31], v[116:119], v[80:83], v[28:31]\nds_read_b64 v[116:117], v6 offset:64\nds_read_b64 v[118:119], v6 offset:72\n" \
  "v_mfma_f32_16x16x32_f16 v[28:31], v[108:111], v[112:115], v[28:31]\nds_read_b64_tr_b16 v[80:81], v5 offset:1024\n" \
  "ds_read_b64_tr_b16 v[82:83], v5 offset:1056\ns_waitcnt lgkmcnt(10)\nv_mfma_f32_16x16x32_f16 v[28:31], v[100:103], v[72:75], v[28:31]\n" \
  "v_cvt_pkrtz_f16_f32 v32, v24, v25\nv_cvt_pkrtz_f16_f32 v33, v26, v27\nv_mfma_f32_16x16x32_f16 v[28:31], v[92:95], v[104:107], v[28:31]\n" \
  "v_pk_add_u16 v32, v32, 4 op_sel_hi:[1,0] clamp\ns_waitcnt lgkmcnt(5)\nv_mfma_f32_16x16x32_f16 v[28:31], v[84:87], v[64:67], v[28:31]\n" \
  "ds_read_b64_tr_b16 v[108:109], v5 offset:1280\nds_read_b64_tr_b16 v[110:111], v5 offset:1312\n" \
  "v_mfma_f32_16x16x32_f16 v[28:31], v[76:79], v[124:127], v[28:31]\nds_read_b64_tr_b16 v[112:113], v7 offset:0\n" \
  "ds_read_b64_tr_b16 v[114:115], v7 offset:64\ns_waitcnt lgkmcnt(6)\nv_mfma_f32_16x16x32_f16 v[28:31], v[68:71], v[40:43], v[28:31]\n" \
  "ds_read2_b32 v[100:101], v8 offset0:48 offset1:49\nds_read2_b32 v[102:103], v8 offset0:50 offset1:51\n" \
  "v_mfma_f32_16x16x32_f16 v[28:31], v[60:63], v[96:99], v[28:31]\nv_pk_add_u16 v33, v33, 4 op_sel_hi:[1,0] clamp\n" \
  "ds_write_b64 v4, v[32:33] offset:3072\nv_mfma_f32_16x16x32_f16 v[28:31], v[44:47], v[116:119], v[28:31]\n" \
  "v_mfma_f32_16x16x32_f16 v[28:31], v[88:91], v[52:55], v[28:31]\nv_mfma_f32_16x16x32_f16 v[28:31], v[48:51], v[36:39], v[28:31]\n" \
  "s_waitcnt lgkmcnt(5)\nv_mfma_f32_16x16x32_f16 v[28:31], v[80:83], v[56:59], v[28:31]\n" \
  "v_mfma_f32_16x16x32_f16 v[28:31], v[108:111], v[120:123], v[28:31]\ns_waitcnt lgkmcnt(1)\n" \
  "v_mfma_f32_16x16x32_f16 v[28:31], v[112:115], v[100:103], v[28:31]\ns_nop 7\nv_cvt_pkrtz_f16_f32 v32, v28, v29\nv_cvt_pkrtz_f16_f32 v33, v30, v31\n" \
  "v_pk_add_u16 v32, v32, 4 op_sel_hi:[1,0] clamp\nv_pk_add_u16 v33, v33, 4 op_sel_hi:[1,0] clamp\nds_write_b64 v4, v[32:33] offset:4096\n" \
  "s_setprio 0\ns_setpc_b64 s[90:91]\n21:\ns_movk_i32 s84, 0x4000\n23:\nds_read_b32 v35, v9 offset:32\ns_sub_u32 s84, s84, 1\ns_cbranch_scc1 22b\n" \
  "s_waitcnt lgkmcnt(0)\nv_readfirstlane_b32 s85, v35\ns_cmp_lt_u32 s85, 4\ns_cbranch_scc1 23b\ns_branch 22b\n1:\n" \
  "ds_read_b64 v[124:125], v3 offset:1088\nds_read_b64 v[126:127], v3 offset:1096\nds_read_b64 v[120:121], v3 offset:0\n" \
  "ds_read_b64 v[122:123], v3 offset:8\nds_read_b64 v[116:117], v3 offset:64\nds_read_b64 v[118:119], v3 offset:72\n" \
  "ds_read_b64 v[112:113], v3 offset:128\nds_read_b64 v[114:115], v3 offset:136\nds_read_b64 v[108:109], v3 offset:192\n" \
  "ds_read_b64 v[110:111], v3 offset:200\nds_read_b64 v[104:105], v3 offset:256\nds_read_b64 v[106:107], v3 offset:264\n" \
  "ds_read_b64 v[100:101], v3 offset:320\nds_read_b64 v[102:103], v3 offset:328\nds_read_b64 v[96:97], v3 offset:384\n" \
  "ds_read_b64 v[98:99], v3 offset:392\nds_read_b64 v[92:93], v3 offset:448\nds_read_b64 v[94:95], v3 offset:456\n" \
  "ds_read_b64 v[88:89], v3 offset:512\nds_read_b64 v[90:91], v3 offset:520\nds_read_b64 v[84:85], v3 offset:576\n" \
  "ds_read_b64 v[86:87], v3 offset:584\nds_read_b64 v[80:81], v3 offset:640\nds_read_b64 v[82:83], v3 offset:648\n" \
  "ds_read_b64 v[76:77], v3 offset:704\nds_read_b64 v[78:79], v3 offset:712\nds_read_b64 v[72:73], v3 offset:768\n" \
  "ds_read_b64 v[74:75], v3 offset:776\nds_read_b64 v[68:69], v3 offset:832\nds_read_b64 v[70:71], v3 offset:840\n" \
  "ds_read_b64 v[64:65], v3 offset:896\nds_read_b64 v[66:67], v3 offset:904\nds_read_b64 v[60:61], v3 offset:960\n" \
  "ds_read_b64 v[62:63], v3 offset:968\nds_read_b64 v[56:57], v3 offset:1024\nds_read_b64 v[58:59], v3 offset:1032\n" \
  "ds_read_b64 v[52:53], v6 offset:0\nds_read_b64 v[54:55], v6 offset:8\nds_read_b64 v[48:49], v6 offset:64\nds_read_b64 v[50:51], v6 offset:72\n" \
  "s_waitcnt lgkmcnt(0)\ns_barrier\nds_read_b128 v[28:31], v2 offset:960\nds_read_b128 v[32:35], v2 offset:1472\n" \
  "ds_read_b128 v[36:39], v2 offset:1984\nds_read_b128 v[40:43], v2 offset:2048\nds_read_b128 v[44:47], v2 offset:2112\ns_waitcnt lgkmcnt(3)\n" \
  "v_mfma_f32_16x16x32_f16 v[12:15], v[124:127], v[28:31], 0\ns_setprio 1\nds_read_b128 v[28:31], v2 offset:2176\n" \
  "v_mfma_f32_16x16x32_f16 v[16:19], v[124:127], v[32:35], 0\nds_read_b128 v[32:35], v2 offset:2240\ns_waitcnt lgkmcnt(3)\n" \
  "v_mfma_f32_16x16x32_f16 v[12:15], v[120:123], v[36:39], v[12:15]\nv_mfma_f32_16x16x32_f16 v[20:23], v[124:127], v[36:39], 0\n" \
  "ds_read_b128 v[124:127], v2 offset:2304\nds_read_b128 v[36:39], v2 offset:2368\nv_mfma_f32_16x16x32_f16 v[12:15], v[116:119], v[40:43], v[12:15]\n" \
  "ds_read_b128 v[40:43], v2 offset:2432\ns_waitcnt lgkmcnt(4)\nv_mfma_f32_16x16x32_f16 v[12:15], v[112:115], v[44:47], v[12:15]\n" \
  "ds_read_b128 v[44:47], v2 offset:2496\nv_mfma_f32_16x16x32_f16 v[12:15], v[108:111], v[28:31], v[12:15]\nds_read_b128 v[28:31], v2 offset:2560\n" \
  "s_waitcnt lgkmcnt(4)\nv_mfma_f32_16x16x32_f16 v[12:15], v[104:107], v[32:35], v[12:15]\nds_read_b128 v[32:35], v2 offset:2624\n" \
  "v_mfma_f32_16x16x32_f16 v[12:15], v[100:103], v[124:127], v[12:15]\ns_waitcnt lgkmcnt(3)\n" \
  "v_mfma_f32_16x16x32_f16 v[12:15], v[96:99], v[36:39], v[12:15]\nds_read_b128 v[124:127], v2 offset:2688\n" \
  "v_mfma_f32_16x16x32_f16 v[12:15], v[92:95], v[40:43], v[12:15]\ns_waitcnt lgkmcnt(3)\n" \
  "v_mfma_f32_16x16x32_f16 v[12:15], v[88:91], v[44:47], v[12:15]\nds_read_b128 v[36:39], v2 offset:2752\n" \
  "v_mfma_f32_16x16x32_f16 v[16:19], v[120:123], v[44:47], v[16:19]\ns_waitcnt lgkmcnt(3)\n" \
  "v_mfma_f32_16x16x32_f16 v[12:15], v[84:87], v[28:31], v[12:15]\nds_read_b128 v[40:43], v2 offset:2816\n" \
  "v_mfma_f32_16x16x32_f16 v[16:19], v[116:119], v[28:31], v[16:19]\ns_waitcnt lgkmcnt(3)\n" \
  "v_mfma_f32_16x16x32_f16 v[12:15], v[80:83], v[32:35], v[12:15]\nds_read_b128 v[44:47], v2 offset:2880\n" \
  "v_mfma_f32_16x16x32_f16 v[16:19], v[112:115], v[32:35], v[16:19]\ns_waitcnt lgkmcnt(3)\n" \
  "v_mfma_f32_16x16x32_f16 v[12:15], v[76:79], v[124:127], v[12:15]\nds_read_b128 v[28:31], v2 offset:2944\n" \
  "v_mfma_f32_16x16x32_f16 v[16:19], v[108:111], v[124:127], v[16:19]\ns_waitcnt lgkmcnt(3)\n" \
  "v_mfma_f32_16x16x32_f16 v[12:15], v[72:75], v[36:39], v[12:15]\nds_read_b128 v[32:35], v2 offset:3008\n" \
  "v_mfma_f32_16x16x32_f16 v[16:19], v[104:107], v[36:39], v[16:19]\ns_waitcnt lgkmcnt(3)\n" \
  "v_mfma_f32_16x16x32_f16 v[12:15], v[68:71], v[40:43], v[12:15]\nv_mfma_f32_16x16x32_f16 v[16:19], v[100:103], v[40:43], v[16:19]\n" \
  "ds_read_b128 v[124:127], v2 offset:3072\ns_waitcnt lgkmcnt(3)\nv_mfma_f32_16x16x32_f16 v[12:15], v[64:67], v[44:47], v[12:15]\n" \
  "v_mfma_f32_16x16x32_f16 v[16:19], v[96:99], v[44:47], v[16:19]\nds_read_b128 v[36:39], v2 offset:3136\ns_waitcnt lgkmcnt(3)\n" \
  "v_mfma_f32_16x16x32_f16 v[12:15], v[60:63], v[28:31], v[12:15]\nds_read_b64 v[40:41], v6 offset:128\nds_read_b64 v[42:43], v6 offset:136\n" \
  "v_mfma_f32_16x16x32_f16 v[16:19], v[92:95], v[28:31], v[16:19]\nds_read_b128 v[44:47], v2 offset:3200\ns_waitcnt lgkmcnt(4)\n" \
  "v_mfma_f32_16x16x32_f16 v[12:15], v[56:59], v[32:35], v[12:15]\nv_mfma_f32_16x16x32_f16 v[16:19], v[88:91], v[32:35], v[16:19]\n" \
  "ds_read_b128 v[28:31], v2 offset:3264\nv_mfma_f32_16x16x32_f16 v[20:23], v[120:123], v[32:35], v[20:23]\nds_read_b64 v[120:121], v6 offset:192\n" \
  "ds_read_b64 v[122:123], v6 offset:200\nv_mfma_f32_16x16x32_f16 v[16:19], v[84:87], v[124:127], v[16:19]\nds_read_b128 v[32:35], v2 offset:3328\n" \
  "v_mfma_f32_16x16x32_f16 v[20:23], v[116:119], v[124:127], v[20:23]\nds_read_b64 v[116:117], v6 offset:256\nds_read_b64 v[118:119], v6 offset:264\n" \
  "s_waitcnt lgkmcnt(6)\nv_mfma_f32_16x16x32_f16 v[16:19], v[80:83], v[36:39], v[16:19]\nds_read_b128 v[124:127], v2 offset:3392\n" \
  "v_mfma_f32_16x16x32_f16 v[20:23], v[112:115], v[36:39], v[20:23]\nds_read_b64 v[112:113], v6 offset:320\nds_read_b64 v[114:115], v6 offset:328\n" \
  "v_mfma_f32_16x16x32_f16 v[16:19], v[76:79], v[44:47], v[16:19]\nds_read_b128 v[36:39], v2 offset:3456\n" \
  "v_mfma_f32_16x16x32_f16 v[20:23], v[108:111], v[44:47], v[20:23]\ns_waitcnt lgkmcnt(6)\n" \
  "v_mfma_f32_16x16x32_f16 v[16:19], v[72:75], v[28:31], v[16:19]\nds_read_b128 v[108:111], v2 offset:3520\n" \
  "v_mfma_f32_16x16x32_f16 v[20:23], v[104:107], v[28:31], v[20:23]\nv_mfma_f32_16x16x32_f16 v[16:19], v[68:71], v[32:35], v[16:19]\n" \
  "ds_read_b128 v[44:47], v2 offset:3584\nv_mfma_f32_16x16x32_f16 v[20:23], v[100:103], v[32:35], v[20:23]\nds_read_b128 v[104:107], v2 offset:3648\n" \
  "ds_read_b32 v27, v9 offset:32\ns_waitcnt lgkmcnt(4)\nv_mfma_f32_16x16x32_f16 v[16:19], v[64:67], v[124:127], v[16:19]\n" \
  "ds_read_b128 v[28:31], v2 offset:3712\nv_mfma_f32_16x16x32_f16 v[20:23], v[96:99], v[124:127], v[20:23]\nds_read_b128 v[100:103], v2 offset:3776\n" \
  "ds_read2_b32 v[32:33], v8 offset0:32 offset1:33\nds_read2_b32 v[34:35], v8 offset0:34 offset1:35\n" \
  "v_mfma_f32_16x16x32_f16 v[16:19], v[60:63], v[36:39], v[16:19]\nds_read_b128 v[96:99], v2 offset:3840\n" \
  "v_mfma_f32_16x16x32_f16 v[20:23], v[92:95], v[36:39], v[20:23]\nds_read_b128 v[124:127], v2 offset:3904\ns_waitcnt lgkmcnt(8)\n" \
  "v_mfma_f32_16x16x32_f16 v[16:19], v[56:59], v[108:111], v[16:19]\nds_read_b128 v[92:95], v2 offset:3968\ns_waitcnt lgkmcnt(7)\n" \
  "v_readfirstlane_b32 s85, v27\ns_cmp_lt_u32 s85, 4\ns_cbranch_scc1 31f\n32:\nv_mfma_f32_16x16x32_f16 v[20:23], v[88:91], v[108:111], v[20:23]\n" \
  "ds_read_b128 v[36:39], v2 offset:4032\nds_read_b64_tr_b16 v[88:89], v5 offset:512\nds_read_b64_tr_b16 v[90:91], v5 offset:544\n" \
  "v_mfma_f32_16x16x32_f16 v[20:23], v[84:87], v[44:47], v[20:23]\nds_read_b64_tr_b16 v[108:109], v5 offset:768\n" \
  "ds_read_b64_tr_b16 v[110:111], v5 offset:800\nv_mfma_f32_16x16x32_f16 v[20:23], v[80:83], v[104:107], v[20:23]\n" \
  "ds_read_b64_tr_b16 v[84:85], v5 offset:1024\nds_read_b64_tr_b16 v[86:87], v5 offset:1056\ns_waitcnt lgkmcnt(12)\n" \
  "v_mfma_f32_16x16x32_f16 v[20:23], v[76:79], v[28:31], v[20:23]\nds_read_b64_tr_b16 v[44:45], v5 offset:1280\n" \
  "ds_read_b64_tr_b16 v[46:47], v5 offset:1312\nv_mfma_f32_16x16x32_f16 v[20:23], v[72:75], v[100:103], v[20:23]\ns_waitcnt lgkmcnt(10)\n" \
  "v_mfma_f32_16x16x32_f16 v[20:23], v[68:71], v[96:99], v[20:23]\nds_read_b64_tr_b16 v[80:81], v5 offset:1536\n" \
  "ds_read_b64_tr_b16 v[82:83], v5 offset:1568\nv_mfma_f32_16x16x32_f16 v[20:23], v[64:67], v[124:127], v[20:23]\n" \
  "ds_read_b64_tr_b16 v[104:105], v5 offset:1792\nds_read_b64_tr_b16 v[106:107], v5 offset:1824\ns_waitcnt lgkmcnt(12)\n" \
  "v_mfma_f32_16x16x32_f16 v[20:23], v[60:63], v[92:95], v[20:23]\nds_read_b64_tr_b16 v[76:77], v5 offset:2048\n" \
  "ds_read_b64_tr_b16 v[78:79], v5 offset:2080\nv_mfma_f32_16x16x32_f16 v[20:23], v[56:59], v[36:39], v[20:23]\ns_waitcnt lgkmcnt(10)\n" \
  "v_mfma_f32_16x16x32_f16 v[12:15], v[88:91], v[52:55], v[12:15]\nds_read_b64_tr_b16 v[28:29], v5 offset:2304\n" \
  "ds_read_b64_tr_b16 v[30:31], v5 offset:2336\nv_mfma_f32_16x16x32_f16 v[12:15], v[108:111], v[48:51], v[12:15]\n" \
  "ds_read_b64_tr_b16 v[72:73], v5 offset:2560\nds_read_b64_tr_b16 v[74:75], v5 offset:2592\ns_waitcnt lgkmcnt(10)\n" \
  "v_mfma_f32_16x16x32_f16 v[12:15], v[84:87], v[40:43], v[12:15]\nds_read_b64_tr_b16 v[100:101], v5 offset:2816\n" \
  "ds_read_b64_tr_b16 v[102:103], v5 offset:2848\nv_mfma_f32_16x16x32_f16 v[16:19], v[84:87], v[52:55], v[16:19]\n" \
  "ds_read_b64_tr_b16 v[68:69], v7 offset:0\nds_read_b64_tr_b16 v[70:71], v7 offset:64\n" \
  "v_mfma_f32_16x16x32_f16 v[12:15], v[44:47], v[120:123], v[12:15]\nv_mfma_f32_16x16x32_f16 v[16:19], v[44:47], v[48:51], v[16:19]\n" \
  "s_waitcnt lgkmcnt(10)\nv_mfma_f32_16x16x32_f16 v[12:15], v[80:83], v[116:119], v[12:15]\nds_read2_b32 v[96:97], v8 offset0:16 offset1:17\n" \
  "ds_read2_b32 v[98:99], v8 offset0:18 offset1:19\nv_mfma_f32_16x16x32_f16 v[16:19], v[80:83], v[40:43], v[16:19]\n" \
  "ds_read2_b32 v[64:65], v8 offset0:0 offset1:1\nds_read2_b32 v[66:67], v8 offset0:2 offset1:3\n" \
  "v_mfma_f32_16x16x32_f16 v[20:23], v[80:83], v[52:55], v[20:23]\nv_mfma_f32_16x16x32_f16 v[12:15], v[104:107], v[112:115], v[12:15]\n" \
  "v_mfma_f32_16x16x32_f16 v[16:19], v[104:107], v[120:123], v[16:19]\nv_mfma_f32_16x16x32_f16 v[20:23], v[104:107], v[48:51], v[20:23]\n" \
  "s_waitcnt lgkmcnt(10)\nv_mfma_f32_16x16x32_f16 v[16:19], v[76:79], v[116:119], v[16:19]\nds_read_b64_tr_b16 v[124:125], v7 offset:512\n" \
  "ds_read_b64_tr_b16 v[126:127], v7 offset:576\nv_mfma_f32_16x16x32_f16 v[20:23], v[76:79], v[40:43], v[20:23]\n" \
  "ds_read2_b32 v[60:61], v8 offset0:48 offset1:49\nds_read2_b32 v[62:63], v8 offset0:50 offset1:51\n" \
  "v_mfma_f32_16x16x32_f16 v[16:19], v[28:31], v[112:115], v[16:19]\nv_mfma_f32_16x16x32_f16 v[20:23], v[28:31], v[120:123], v[20:23]\n" \
  "s_waitcnt lgkmcnt(10)\nv_mfma_f32_16x16x32_f16 v[20:23], v[72:75], v[116:119], v[20:23]\nds_read_b64_tr_b16 v[92:93], v7 offset:1024\n" \
  "ds_read_b64_tr_b16 v[94:95], v7 offset:1088\nv_mfma_f32_16x16x32_f16 v[20:23], v[100:103], v[112:115], v[20:23]\n" \
  "ds_read_b64_tr_b16 v[56:57], v7 offset:1536\nds_read_b64_tr_b16 v[58:59], v7 offset:1600\ns_waitcnt lgkmcnt(10)\n" \
  "v_mfma_f32_16x16x32_f16 v[12:15], v[68:71], v[32:35], v[12:15]\nv_mfma_f32_16x16x32_f16 v[16:19], v[68:71], v[96:99], v[16:19]\n" \
  "s_waitcnt lgkmcnt(6)\nv_mfma_f32_16x16x32_f16 v[20:23], v[68:71], v[64:67], v[20:23]\ns_waitcnt lgkmcnt(2)\n" \
  "v_mfma_f32_16x16x32_f16 v[12:15], v[124:127], v[60:63], v[12:15]\nv_mfma_f32_16x16x32_f16 v[16:19], v[124:127], v[32:35], v[16:19]\n" \
  "v_mfma_f32_16x16x32_f16 v[20:23], v[124:127], v[96:99], v[20:23]\nv_mfma_f32_16x16x32_f16 v[16:19], v[92:95], v[60:63], v[16:19]\n" \
  "v_mfma_f32_16x16x32_f16 v[20:23], v[92:95], v[32:35], v[20:23]\ns_waitcnt lgkmcnt(0)\n" \
  "v_mfma_f32_16x16x32_f16 v[20:23], v[56:59], v[60:63], v[20:23]\ns_nop 1\nv_cvt_pkrtz_f16_f32 v24, v12, v13\nv_cvt_pkrtz_f16_f32 v25, v14, v15\n" \
  "v_pk_add_u16 v24, v24, 4 op_sel_hi:[1,0] clamp\nv_pk_add_u16 v25, v25, 4 op_sel_hi:[1,0] clamp\nds_write_b64 v4, v[24:25] offset:5120\n" \
  "v_cvt_pkrtz_f16_f32 v24, v16, v17\nv_cvt_pkrtz_f16_f32 v25, v18, v19\nv_pk_add_u16 v24, v24, 4 op_sel_hi:[1,0] clamp\n" \
  "v_pk_add_u16 v25, v25, 4 op_sel_hi:[1,0] clamp\nds_write_b64 v4, v[24:25] offset:6144\nv_cvt_pkrtz_f16_f32 v24, v20, v21\n" \
  "v_cvt_pkrtz_f16_f32 v25, v22, v23\nv_pk_add_u16 v24, v24, 4 op_sel_hi:[1,0] clamp\nv_pk_add_u16 v25, v25, 4 op_sel_hi:[1,0] clamp\n" \
  "ds_write_b64 v4, v[24:25] offset:7168\ns_setprio 0\ns_setpc_b64 s[90:91]\n31:\ns_movk_i32 s84, 0x4000\n33:\nds_read_b32 v27, v9 offset:32\n" \
  "s_sub_u32 s84, s84, 1\ns_cbranch_scc1 32b\ns_waitcnt lgkmcnt(0)\nv_readfirstlane_b32 s85, v27\ns_cmp_lt_u32 s85, 4\ns_cbranch_scc1 33b\n" \
  "s_branch 32b\n9:\ns_cmp_eq_u32 s89, 3\ns_cbranch_scc1 3f\ns_cmp_eq_u32 s89, 4\ns_cbranch_scc1 4f\ns_cmp_eq_u32 s89, 5\ns_cbranch_scc1 5f\n" \
  "ds_read_b64 v[124:125], v3 offset:576\nds_read_b64 v[126:127], v3 offset:584\nds_read_b64 v[120:121], v3 offset:64\n" \
  "ds_read_b64 v[122:123], v3 offset:72\nds_read_b64 v[116:117], v3 offset:640\nds_read_b64 v[118:119], v3 offset:648\n" \
  "ds_read_b64 v[112:113], v3 offset:128\nds_read_b64 v[114:115], v3 offset:136\nds_read_b64 v[108:109], v3 offset:704\n" \
  "ds_read_b64 v[110:111], v3 offset:712\nds_read_b64 v[104:105], v3 offset:192\nds_read_b64 v[106:107], v3 offset:200\n" \
  "ds_read_b64 v[100:101], v3 offset:768\nds_read_b64 v[102:103], v3 offset:776\nds_read_b64 v[96:97], v3 offset:256\n" \
  "ds_read_b64 v[98:99], v3 offset:264\nds_read_b64 v[92:93], v3 offset:832\nds_read_b64 v[94:95], v3 offset:840\n" \
  "ds_read_b64 v[88:89], v3 offset:320\nds_read_b64 v[90:91], v3 offset:328\nds_read_b64 v[84:85], v3 offset:896\n" \
  "ds_read_b64 v[86:87], v3 offset:904\nds_read_b64 v[80:81], v3 offset:384\nds_read_b64 v[82:83], v3 offset:392\n" \
  "ds_read_b64 v[76:77], v3 offset:960\nds_read_b64 v[78:79], v3 offset:968\nds_read_b64 v[72:73], v3 offset:448\n" \
  "ds_read_b64 v[74:75], v3 offset:456\nds_read_b64 v[68:69], v3 offset:1024\nds_read_b64 v[70:71], v3 offset:1032\n" \
  "ds_read_b64 v[64:65], v3 offset:512\nds_read_b64 v[66:67], v3 offset:520\ns_waitcnt lgkmcnt(0)\ns_barrier\nds_read_b128 v[24:27], v2 offset:0\n" \
  "ds_read_b128 v[28:31], v2 offset:64\nds_read_b128 v[32:35], v2 offset:128\nds_read_b128 v[36:39], v2 offset:192\ns_waitcnt lgkmcnt(3)\n" \
  "v_mfma_f32_16x16x32_f16 v[12:15], v[124:127], v[24:27], 0\ns_setprio 1\nds_read_b128 v[40:43], v2 offset:256\n" \
  "v_mfma_f32_16x16x32_f16 v[16:19], v[120:123], v[24:27], 0\ns_waitcnt lgkmcnt(3)\nv_mfma_f32_16x16x32_f16 v[12:15], v[116:119], v[28:31], v[12:15]\n" \
  "ds_read_b128 v[44:47], v2 offset:320\nv_mfma_f32_16x16x32_f16 v[16:19], v[112:115], v[28:31], v[16:19]\ns_waitcnt lgkmcnt(3)\n" \
  "v_mfma_f32_16x16x32_f16 v[12:15], v[108:111], v[32:35], v[12:15]\nds_read_b128 v[48:51], v2 offset:384\n" \
  "v_mfma_f32_16x16x32_f16 v[16:19], v[104:107], v[32:35], v[16:19]\ns_waitcnt lgkmcnt(3)\n" \
  "v_mfma_f32_16x16x32_f16 v[12:15], v[100:103], v[36:39], v[12:15]\nds_read_b128 v[52:55], v2 offset:448\n" \
  "v_mfma_f32_16x16x32_f16 v[16:19], v[96:99], v[36:39], v[16:19]\ns_waitcnt lgkmcnt(3)\n" \
  "v_mfma_f32_16x16x32_f16 v[12:15], v[92:95], v[40:43], v[12:15]\nds_read_b128 v[56:59], v2 offset:512\n" \
  "v_mfma_f32_16x16x32_f16 v[16:19], v[88:91], v[40:43], v[16:19]\nds_read_b128 v[60:63], v2 offset:576\ns_waitcnt lgkmcnt(3)\n" \
  "v_mfma_f32_16x16x32_f16 v[12:15], v[84:87], v[44:47], v[12:15]\nds_read_b128 v[120:123], v2 offset:640\n" \
  "v_mfma_f32_16x16x32_f16 v[16:19], v[80:83], v[44:47], v[16:19]\nds_read_b128 v[24:27], v2 offset:704\n" \
  "v_mfma_f32_16x16x32_f16 v[12:15], v[76:79], v[48:51], v[12:15]\nds_read_b128 v[112:115], v2 offset:768\n" \
  "v_mfma_f32_16x16x32_f16 v[16:19], v[72:75], v[48:51], v[16:19]\nds_read_b128 v[28:31], v2 offset:832\ns_waitcnt lgkmcnt(5)\n" \
  "v_mfma_f32_16x16x32_f16 v[12:15], v[68:71], v[52:55], v[12:15]\nds_read_b128 v[104:107], v2 offset:896\n" \
  "v_mfma_f32_16x16x32_f16 v[16:19], v[64:67], v[52:55], v[16:19]\nds_read_b128 v[32:35], v2 offset:960\n" \
  "v_mfma_f32_16x16x32_f16 v[16:19], v[124:127], v[56:59], v[16:19]\ns_waitcnt lgkmcnt(5)\n" \
  "v_mfma_f32_16x16x32_f16 v[16:19], v[116:119], v[60:63], v[16:19]\nv_mfma_f32_16x16x32_f16 v[16:19], v[108:111], v[120:123], v[16:19]\n" \
  "s_waitcnt lgkmcnt(3)\nv_mfma_f32_16x16x32_f16 v[16:19], v[100:103], v[24:27], v[16:19]\nv_cvt_pkrtz_f16_f32 v20, v12, v13\n" \
  "v_cvt_pkrtz_f16_f32 v21, v14, v15\nv_mfma_f32_16x16x32_f16 v[16:19], v[92:95], v[112:115], v[16:19]\n" \
  "v_pk_add_u16 v20, v20, 4 op_sel_hi:[1,0] clamp\ns_waitcnt lgkmcnt(2)\nv_mfma_f32_16x16x32_f16 v[16:19], v[84:87], v[28:31], v[16:19]\n" \
  "v_pk_add_u16 v21, v21, 4 op_sel_hi:[1,0] clamp\ns_waitcnt lgkmcnt(1)\nv_mfma_f32_16x16x32_f16 v[16:19], v[76:79], v[104:107], v[16:19]\n" \
  "ds_write_b64 v4, v[20:21] offset:0\ns_waitcnt lgkmcnt(1)\nv_mfma_f32_16x16x32_f16 v[16:19], v[68:71], v[32:35], v[16:19]\ns_nop 7\n" \
  "v_cvt_pkrtz_f16_f32 v20, v16, v17\nv_cvt_pkrtz_f16_f32 v21, v18, v19\nv_pk_add_u16 v20, v20, 4 op_sel_hi:[1,0] clamp\n" \
  "v_pk_add_u16 v21, v21, 4 op_sel_hi:[1,0] clamp\nds_write_b64 v4, v[20:21] offset:1024\ns_setprio 0\ns_setpc_b64 s[90:91]\n3:\n" \
  "ds_read_b64 v[124:125], v3 offset:0\nds_read_b64 v[126:127], v3 offset:8\nds_read_b64 v[120:121], v3 offset:64\n" \
  "ds_read_b64 v[122:123], v3 offset:72\nds_read_b64 v[116:117], v3 offset:128\nds_read_b64 v[118:119], v3 offset:136\n" \
  "ds_read_b64 v[112:113], v3 offset:192\nds_read_b64 v[114:115], v3 offset:200\nds_read_b64 v[108:109], v3 offset:256\n" \
  "ds_read_b64 v[110:111], v3 offset:264\nds_read_b64 v[104:105], v3 offset:320\nds_read_b64 v[106:107], v3 offset:328\n" \
  "ds_read_b64 v[100:101], v3 offset:384\nds_read_b64 v[102:103], v3 offset:392\nds_read_b64 v[96:97], v3 offset:448\n" \
  "ds_read_b64 v[98:99], v3 offset:456\nds_read_b64 v[92:93], v3 offset:512\nds_read_b64 v[94:95], v3 offset:520\n" \
  "ds_read_b64 v[88:89], v3 offset:576\nds_read_b64 v[90:91], v3 offset:584\nds_read_b64 v[84:85], v3 offset:640\n" \
  "ds_read_b64 v[86:87], v3 offset:648\nds_read_b64 v[80:81], v3 offset:704\nds_read_b64 v[82:83], v3 offset:712\n" \
  "ds_read_b64 v[76:77], v3 offset:768\nds_read_b64 v[78:79], v3 offset:776\nds_read_b64 v[72:73], v3 offset:832\n" \
  "ds_read_b64 v[74:75], v3 offset:840\nds_read_b64 v[68:69], v3 offset:896\nds_read_b64 v[70:71], v3 offset:904\n" \
  "ds_read_b64 v[64:65], v3 offset:960\nds_read_b64 v[66:67], v3 offset:968\nds_read_b64 v[60:61], v3 offset:1024\n" \
  "ds_read_b64 v[62:63], v3 offset:1032\nds_read_b64 v[56:57], v6 offset:256\nds_read_b64 v[58:59], v6 offset:264\n" \
  "ds_read_b64 v[52:53], v6 offset:128\nds_read_b64 v[54:55], v6 offset:136\nds_read_b64 v[48:49], v6 offset:320\n" \
  "ds_read_b64 v[50:51], v6 offset:328\nds_read_b64 v[44:45], v6 offset:192\nds_read_b64 v[46:47], v6 offset:200\ns_waitcnt lgkmcnt(0)\ns_barrier\n" \
  "ds_read_b128 v[24:27], v2 offset:448\nds_read_b128 v[28:31], v2 offset:512\nds_read_b128 v[32:35], v2 offset:576\n" \
  "ds_read_b128 v[36:39], v2 offset:640\nds_read_b128 v[40:43], v2 offset:704\ns_waitcnt lgkmcnt(3)\n" \
  "v_mfma_f32_16x16x32_f16 v[12:15], v[124:127], v[24:27], 0\ns_setprio 1\nds_read_b128 v[24:27], v2 offset:768\n" \
  "v_mfma_f32_16x16x32_f16 v[12:15], v[120:123], v[28:31], v[12:15]\nds_read_b128 v[28:31], v2 offset:832\ns_waitcnt lgkmcnt(3)\n" \
  "v_mfma_f32_16x16x32_f16 v[12:15], v[116:119], v[32:35], v[12:15]\nds_read_b128 v[32:35], v2 offset:896\n" \
  "v_mfma_f32_16x16x32_f16 v[12:15], v[112:115], v[36:39], v[12:15]\nds_read_b128 v[36:39], v2 offset:960\ns_waitcnt lgkmcnt(3)\n" \
  "v_mfma_f32_16x16x32_f16 v[12:15], v[108:111], v[40:43], v[12:15]\nds_read_b128 v[40:43], v2 offset:1024\n" \
  "v_mfma_f32_16x16x32_f16 v[12:15], v[104:107], v[24:27], v[12:15]\nds_read_b128 v[24:27], v2 offset:1088\ns_waitcnt lgkmcnt(3)\n" \
  "v_mfma_f32_16x16x32_f16 v[12:15], v[100:103], v[28:31], v[12:15]\nds_read_b128 v[28:31], v2 offset:1152\n" \
  "v_mfma_f32_16x16x32_f16 v[12:15], v[96:99], v[32:35], v[12:15]\ns_waitcnt lgkmcnt(3)\n" \
  "v_mfma_f32_16x16x32_f16 v[12:15], v[92:95], v[36:39], v[12:15]\nds_read_b128 v[32:35], v2 offset:1216\n" \
  "v_mfma_f32_16x16x32_f16 v[16:19], v[124:127], v[36:39], 0\ns_waitcnt lgkmcnt(3)\nv_mfma_f32_16x16x32_f16 v[12:15], v[88:91], v[40:43], v[12:15]\n" \
  "ds_read_b128 v[124:127], v2 offset:1280\nv_mfma_f32_16x16x32_f16 v[16:19], v[120:123], v[40:43], v[16:19]\ns_waitcnt lgkmcnt(3)\n" \
  "v_mfma_f32_16x16x32_f16 v[12:15], v[84:87], v[24:27], v[12:15]\nds_read_b128 v[36:39], v2 offset:1344\n" \
  "v_mfma_f32_16x16x32_f16 v[16:19], v[116:119], v[24:27], v[16:19]\ns_waitcnt lgkmcnt(3)\n" \
  "v_mfma_f32_16x16x32_f16 v[12:15], v[80:83], v[28:31], v[12:15]\nds_read_b128 v[120:123], v2 offset:1408\n" \
  "v_mfma_f32_16x16x32_f16 v[16:19], v[112:115], v[28:31], v[16:19]\ns_waitcnt lgkmcnt(3)\n" \
  "v_mfma_f32_16x16x32_f16 v[12:15], v[76:79], v[32:35], v[12:15]\nds_read_b128 v[40:43], v2 offset:1472\n" \
  "v_mfma_f32_16x16x32_f16 v[16:19], v[108:111], v[32:35], v[16:19]\ns_waitcnt lgkmcnt(3)\n" \
  "v_mfma_f32_16x16x32_f16 v[12:15], v[72:75], v[124:127], v[12:15]\nds_read_b128 v[116:119], v2 offset:1536\n" \
  "v_mfma_f32_16x16x32_f16 v[16:19], v[104:107], v[124:127], v[16:19]\nds_read_b128 v[24:27], v2 offset:1600\ns_waitcnt lgkmcnt(3)\n" \
  "v_mfma_f32_16x16x32_f16 v[12:15], v[68:71], v[36:39], v[12:15]\nds_read_b128 v[112:115], v2 offset:1664\nds_read_b32 v23, v9 offset:32\n" \
  "v_mfma_f32_16x16x32_f16 v[16:19], v[100:103], v[36:39], v[16:19]\nds_read_b128 v[28:31], v2 offset:1728\n" \
  "v_mfma_f32_16x16x32_f16 v[12:15], v[64:67], v[120:123], v[12:15]\nds_read_b128 v[108:111], v2 offset:1792\n" \
  "v_mfma_f32_16x16x32_f16 v[16:19], v[96:99], v[120:123], v[16:19]\nds_read_b128 v[32:35], v2 offset:1856\ns_waitcnt lgkmcnt(6)\n" \
  "v_mfma_f32_16x16x32_f16 v[12:15], v[60:63], v[40:43], v[12:15]\nds_read_b128 v[104:107], v2 offset:1920\n" \
  "v_mfma_f32_16x16x32_f16 v[16:19], v[92:95], v[40:43], v[16:19]\nds_read_b128 v[124:127], v2 offset:1984\ns_waitcnt lgkmcnt(5)\n" \
  "v_readfirstlane_b32 s85, v23\ns_cmp_lt_u32 s85, 3\ns_cbranch_scc1 51f\n52:\nv_mfma_f32_16x16x32_f16 v[16:19], v[88:91], v[116:119], v[16:19]\n" \
  "ds_read_b64_tr_b16 v[100:101], v5 offset:0\nds_read_b64_tr_b16 v[102:103], v5 offset:32\n" \
  "v_mfma_f32_16x16x32_f16 v[16:19], v[84:87], v[24:27], v[16:19]\nds_read_b64_tr_b16 v[36:37], v5 offset:256\n" \
  "ds_read_b64_tr_b16 v[38:39], v5 offset:288\nv_mfma_f32_16x16x32_f16 v[16:19], v[80:83], v[112:115], v[16:19]\n" \
  "ds_read_b64_tr_b16 v[96:97], v5 offset:512\nds_read_b64_tr_b16 v[98:99], v5 offset:544\ns_waitcnt lgkmcnt(9)\n" \
  "v_mfma_f32_16x16x32_f16 v[16:19], v[76:79], v[28:31], v[16:19]\nds_read_b64_tr_b16 v[120:121], v5 offset:768\n" \
  "ds_read_b64_tr_b16 v[122:123], v5 offset:800\nv_mfma_f32_16x16x32_f16 v[16:19], v[72:75], v[108:111], v[16:19]\ns_waitcnt lgkmcnt(9)\n" \
  "v_mfma_f32_16x16x32_f16 v[16:19], v[68:71], v[32:35], v[16:19]\nv_mfma_f32_16x16x32_f16 v[16:19], v[64:67], v[104:107], v[16:19]\n" \
  "s_waitcnt lgkmcnt(6)\nv_mfma_f32_16x16x32_f16 v[16:19], v[60:63], v[124:127], v[16:19]\n" \
  "v_mfma_f32_16x16x32_f16 v[12:15], v[100:103], v[56:59], v[12:15]\nv_mfma_f32_16x16x32_f16 v[16:19], v[100:103], v[52:55], v[16:19]\n" \
  "s_waitcnt lgkmcnt(2)\nv_mfma_f32_16x16x32_f16 v[12:15], v[36:39], v[48:51], v[12:15]\n" \
  "v_mfma_f32_16x16x32_f16 v[16:19], v[36:39], v[44:47], v[16:19]\nv_mfma_f32_16x16x32_f16 v[16:19], v[96:99], v[56:59], v[16:19]\n" \
  "s_waitcnt lgkmcnt(0)\nv_mfma_f32_16x16x32_f16 v[16:19], v[120:123], v[48:51], v[16:19]\ns_nop 3\nv_cvt_pkrtz_f16_f32 v20, v12, v13\n" \
  "v_cvt_pkrtz_f16_f32 v21, v14, v15\nv_pk_add_u16 v20, v20, 4 op_sel_hi:[1,0] clamp\nv_pk_add_u16 v21, v21, 4 op_sel_hi:[1,0] clamp\n" \
  "ds_write_b64 v4, v[20:21] offset:2048\nv_cvt_pkrtz_f16_f32 v20, v16, v17\nv_cvt_pkrtz_f16_f32 v21, v18, v19\n" \
  "v_pk_add_u16 v20, v20, 4 op_sel_hi:[1,0] clamp\nv_pk_add_u16 v21, v21, 4 op_sel_hi:[1,0] clamp\nds_write_b64 v4, v[20:21] offset:3072\n" \
  "s_setprio 0\ns_setpc_b64 s[90:91]\n51:\ns_movk_i32 s84, 0x4000\n53:\nds_read_b32 v23, v9 offset:32\ns_sub_u32 s84, s84, 1\ns_cbranch_scc1 52b\n" \
  "s_waitcnt lgkmcnt(0)\nv_readfirstlane_b32 s85, v23\ns_cmp_lt_u32 s85, 3\ns_cbranch_scc1 53b\ns_branch 52b\n4:\n" \
  "ds_read_b64 v[124:125], v3 offset:1088\nds_read_b64 v[126:127], v3 offset:1096\nds_read_b64 v[120:121], v3 offset:0\n" \
  "ds_read_b64 v[122:123], v3 offset:8\nds_read_b64 v[116:117], v3 offset:64\nds_read_b64 v[118:119], v3 offset:72\n" \
  "ds_read_b64 v[112:113], v3 offset:128\nds_read_b64 v[114:115], v3 offset:136\nds_read_b64 v[108:109], v3 offset:192\n" \
  "ds_read_b64 v[110:111], v3 offset:200\nds_read_b64 v[104:105], v3 offset:256\nds_read_b64 v[106:107], v3 offset:264\n" \
  "ds_read_b64 v[100:101], v3 offset:320\nds_read_b64 v[102:103], v3 offset:328\nds_read_b64 v[96:97], v3 offset:384\n" \
  "ds_read_b64 v[98:99], v3 offset:392\nds_read_b64 v[92:93], v3 offset:448\nds_read_b64 v[94:95], v3 offset:456\n" \
  "ds_read_b64 v[88:89], v3 offset:512\nds_read_b64 v[90:91], v3 offset:520\nds_read_b64 v[84:85], v3 offset:576\n" \
  "ds_read_b64 v[86:87], v3 offset:584\nds_read_b64 v[80:81], v3 offset:640\nds_read_b64 v[82:83], v3 offset:648\n" \
  "ds_read_b64 v[76:77], v3 offset:704\nds_read_b64 v[78:79], v3 offset:712\nds_read_b64 v[72:73], v3 offset:768\n" \
  "ds_read_b64 v[74:75], v3 offset:776\nds_read_b64 v[68:69], v3 offset:832\nds_read_b64 v[70:71], v3 offset:840\n" \
  "ds_read_b64 v[64:65], v3 offset:896\nds_read_b64 v[66:67], v3 offset:904\nds_read_b64 v[60:61], v3 offset:960\n" \
  "ds_read_b64 v[62:63], v3 offset:968\nds_read_b64 v[56:57], v3 offset:1024\nds_read_b64 v[58:59], v3 offset:1032\n" \
  "ds_read_b64 v[52:53], v6 offset:0\nds_read_b64 v[54:55], v6 offset:8\nds_read_b64 v[48:49], v6 offset:64\nds_read_b64 v[50:51], v6 offset:72\n" \
  "ds_read_b64 v[44:45], v6 offset:128\nds_read_b64 v[46:47], v6 offset:136\nds_read_b64 v[40:41], v6 offset:192\n" \
  "ds_read_b64 v[42:43], v6 offset:200\ns_waitcnt lgkmcnt(0)\ns_barrier\nds_read_b128 v[24:27], v2 offset:448\nds_read_b128 v[28:31], v2 offset:960\n" \
  "ds_read_b128 v[32:35], v2 offset:1472\nds_read_b128 v[36:39], v2 offset:1536\ns_waitcnt lgkmcnt(3)\n" \
  "v_mfma_f32_16x16x32_f16 v[12:15], v[124:127], v[24:27], 0\ns_setprio 1\nds_read_b128 v[24:27], v2 offset:1600\ns_waitcnt lgkmcnt(3)\n" \
  "v_mfma_f32_16x16x32_f16 v[16:19], v[124:127], v[28:31], 0\nds_read_b128 v[124:127], v2 offset:1664\ns_waitcnt lgkmcnt(3)\n" \
  "v_mfma_f32_16x16x32_f16 v[12:15], v[120:123], v[32:35], v[12:15]\nds_read_b128 v[28:31], v2 offset:1728\ns_waitcnt lgkmcnt(3)\n" \
  "v_mfma_f32_16x16x32_f16 v[12:15], v[116:119], v[36:39], v[12:15]\nds_read_b128 v[32:35], v2 offset:1792\ns_waitcnt lgkmcnt(3)\n" \
  "v_mfma_f32_16x16x32_f16 v[12:15], v[112:115], v[24:27], v[12:15]\nds_read_b128 v[36:39], v2 offset:1856\ns_waitcnt lgkmcnt(3)\n" \
  "v_mfma_f32_16x16x32_f16 v[12:15], v[108:111], v[124:127], v[12:15]\nds_read_b128 v[24:27], v2 offset:1920\ns_waitcnt lgkmcnt(3)\n" \
  "v_mfma_f32_16x16x32_f16 v[12:15], v[104:107], v[28:31], v[12:15]\nds_read_b128 v[124:127], v2 offset:1984\ns_waitcnt lgkmcnt(3)\n" \
  "v_mfma_f32_16x16x32_f16 v[12:15], v[100:103], v[32:35], v[12:15]\nds_read_b128 v[28:31], v2 offset:2048\ns_waitcnt lgkmcnt(3)\n" \
  "v_mfma_f32_16x16x32_f16 v[12:15], v[96:99], v[36:39], v[12:15]\nds_read_b128 v[32:35], v2 offset:2112\ns_waitcnt lgkmcnt(3)\n" \
  "v_mfma_f32_16x16x32_f16 v[12:15], v[92:95], v[24:27], v[12:15]\nds_read_b128 v[36:39], v2 offset:2176\ns_waitcnt lgkmcnt(3)\n" \
  "v_mfma_f32_16x16x32_f16 v[12:15], v[88:91], v[124:127], v[12:15]\nds_read_b128 v[24:27], v2 offset:2240\n" \
  "v_mfma_f32_16x16x32_f16 v[16:19], v[120:123], v[124:127], v[16:19]\nds_read_b64 v[120:121], v6 offset:256\nds_read_b64 v[122:123], v6 offset:264\n" \
  "s_waitcnt lgkmcnt(4)\nv_mfma_f32_16x16x32_f16 v[12:15], v[84:87], v[28:31], v[12:15]\nds_read_b128 v[124:127], v2 offset:2304\n" \
  "v_mfma_f32_16x16x32_f16 v[16:19], v[116:119], v[28:31], v[16:19]\nds_read_b64 v[116:117], v6 offset:320\nds_read_b64 v[118:119], v6 offset:328\n" \
  "v_mfma_f32_16x16x32_f16 v[12:15], v[80:83], v[32:35], v[12:15]\nds_read_b128 v[28:31], v2 offset:2368\n" \
  "v_mfma_f32_16x16x32_f16 v[16:19], v[112:115], v[32:35], v[16:19]\ns_waitcnt lgkmcnt(6)\n" \
  "v_mfma_f32_16x16x32_f16 v[12:15], v[76:79], v[36:39], v[12:15]\nds_read_b128 v[112:115], v2 offset:2432\n" \
  "v_mfma_f32_16x16x32_f16 v[16:19], v[108:111], v[36:39], v[16:19]\nds_read2_b32 v[32:33], v8 offset0:48 offset1:49\n" \
  "ds_read2_b32 v[34:35], v8 offset0:50 offset1:51\nv_mfma_f32_16x16x32_f16 v[12:15], v[72:75], v[24:27], v[12:15]\n" \
  "ds_read_b128 v[108:111], v2 offset:2496\nv_mfma_f32_16x16x32_f16 v[16:19], v[104:107], v[24:27], v[16:19]\n" \
  "ds_read2_b32 v[36:37], v8 offset0:32 offset1:33\nds_read2_b32 v[38:39], v8 offset0:34 offset1:35\ns_waitcnt lgkmcnt(6)\n" \
  "v_mfma_f32_16x16x32_f16 v[12:15], v[68:71], v[124:127], v[12:15]\nds_read_b128 v[104:107], v2 offset:2560\n" \
  "v_mfma_f32_16x16x32_f16 v[16:19], v[100:103], v[124:127], v[16:19]\nds_read_b128 v[24:27], v2 offset:2624\n" \
  "v_mfma_f32_16x16x32_f16 v[12:15], v[64:67], v[28:31], v[12:15]\nds_read_b128 v[100:103], v2 offset:2688\nds_read_b32 v23, v9 offset:32\n" \
  "v_mfma_f32_16x16x32_f16 v[16:19], v[96:99], v[28:31], v[16:19]\nds_read_b128 v[124:127], v2 offset:2752\ns_waitcnt lgkmcnt(7)\n" \
  "v_mfma_f32_16x16x32_f16 v[12:15], v[60:63], v[112:115], v[12:15]\nds_read_b128 v[96:99], v2 offset:2816\n" \
  "v_mfma_f32_16x16x32_f16 v[16:19], v[92:95], v[112:115], v[16:19]\nds_read_b128 v[28:31], v2 offset:2880\n" \
  "v_mfma_f32_16x16x32_f16 v[12:15], v[56:59], v[108:111], v[12:15]\nds_read_b128 v[92:95], v2 offset:2944\n" \
  "v_mfma_f32_16x16x32_f16 v[16:19], v[88:91], v[108:111], v[16:19]\nds_read_b128 v[112:115], v2 offset:3008\ns_waitcnt lgkmcnt(5)\n" \
  "v_readfirstlane_b32 s85, v23\ns_cmp_lt_u32 s85, 3\ns_cbranch_scc1 61f\n62:\nv_mfma_f32_16x16x32_f16 v[16:19], v[84:87], v[104:107], v[16:19]\n" \
  "ds_read_b64_tr_b16 v[88:89], v5 offset:0\nds_read_b64_tr_b16 v[90:91], v5 offset:32\n" \
  "v_mfma_f32_16x16x32_f16 v[16:19], v[80:83], v[24:27], v[16:19]\nds_read_b64_tr_b16 v[108:109], v5 offset:256\n" \
  "ds_read_b64_tr_b16 v[110:111], v5 offset:288\nv_mfma_f32_16x16x32_f16 v[16:19], v[76:79], v[100:103], v[16:19]\n" \
  "ds_read_b64_tr_b16 v[84:85], v5 offset:512\nds_read_b64_tr_b16 v[86:87], v5 offset:544\ns_waitcnt lgkmcnt(9)\n" \
  "v_mfma_f32_16x16x32_f16 v[16:19], v[72:75], v[124:127], v[16:19]\nds_read_b64_tr_b16 v[104:105], v5 offset:768\n" \
  "ds_read_b64_tr_b16 v[106:107], v5 offset:800\nv_mfma_f32_16x16x32_f16 v[16:19], v[68:71], v[96:99], v[16:19]\n" \
  "ds_read_b64_tr_b16 v[80:81], v5 offset:1024\nds_read_b64_tr_b16 v[82:83], v5 offset:1056\ns_waitcnt lgkmcnt(11)\n" \
  "v_mfma_f32_16x16x32_f16 v[16:19], v[64:67], v[28:31], v[16:19]\nds_read_b64_tr_b16 v[24:25], v5 offset:1280\n" \
  "ds_read_b64_tr_b16 v[26:27], v5 offset:1312\nv_mfma_f32_16x16x32_f16 v[16:19], v[60:63], v[92:95], v[16:19]\n" \
  "ds_read_b64_tr_b16 v[76:77], v5 offset:1536\nds_read_b64_tr_b16 v[78:79], v5 offset:1568\ns_waitcnt lgkmcnt(12)\n" \
  "v_mfma_f32_16x16x32_f16 v[16:19], v[56:59], v[112:115], v[16:19]\nds_read_b64_tr_b16 v[100:101], v5 offset:1792\n" \
  "ds_read_b64_tr_b16 v[102:103], v5 offset:1824\nv_mfma_f32_16x16x32_f16 v[12:15], v[88:91], v[52:55], v[12:15]\ns_waitcnt lgkmcnt(10)\n" \
  "v_mfma_f32_16x16x32_f16 v[12:15], v[108:111], v[48:51], v[12:15]\nds_read_b64_tr_b16 v[72:73], v7 offset:0\n" \
  "ds_read_b64_tr_b16 v[74:75], v7 offset:64\nv_mfma_f32_16x16x32_f16 v[12:15], v[84:87], v[44:47], v[12:15]\n" \
  "ds_read_b64_tr_b16 v[124:125], v7 offset:512\nds_read_b64_tr_b16 v[126:127], v7 offset:576\n" \
  "v_mfma_f32_16x16x32_f16 v[16:19], v[84:87], v[52:55], v[16:19]\ns_waitcnt lgkmcnt(10)\n" \
  "v_mfma_f32_16x16x32_f16 v[12:15], v[104:107], v[40:43], v[12:15]\nv_mfma_f32_16x16x32_f16 v[16:19], v[104:107], v[48:51], v[16:19]\n" \
  "v_mfma_f32_16x16x32_f16 v[12:15], v[80:83], v[120:123], v[12:15]\nv_mfma_f32_16x16x32_f16 v[16:19], v[80:83], v[44:47], v[16:19]\n" \
  "s_waitcnt lgkmcnt(6)\nv_mfma_f32_16x16x32_f16 v[12:15], v[24:27], v[116:119], v[12:15]\n" \
  "v_mfma_f32_16x16x32_f16 v[16:19], v[24:27], v[40:43], v[16:19]\nv_mfma_f32_16x16x32_f16 v[16:19], v[76:79], v[120:123], v[16:19]\n" \
  "s_waitcnt lgkmcnt(2)\nv_mfma_f32_16x16x32_f16 v[16:19], v[100:103], v[116:119], v[16:19]\n" \
  "v_mfma_f32_16x16x32_f16 v[12:15], v[72:75], v[32:35], v[12:15]\nv_mfma_f32_16x16x32_f16 v[16:19], v[72:75], v[36:39], v[16:19]\n" \
  "s_waitcnt lgkmcnt(0)\nv_mfma_f32_16x16x32_f16 v[16:19], v[124:127], v[32:35], v[16:19]\ns_nop 4\nv_cvt_pkrtz_f16_f32 v20, v12, v13\n" \
  "v_cvt_pkrtz_f16_f32 v21, v14, v15\nv_pk_add_u16 v20, v20, 4 op_sel_hi:[1,0] clamp\nv_pk_add_u16 v21, v21, 4 op_sel_hi:[1,0] clamp\n" \
  "ds_write_b64 v4, v[20:21] offset:4096\nv_cvt_pkrtz_f16_f32 v20, v16, v17\nv_cvt_pkrtz_f16_f32 v21, v18, v19\n" \
  "v_pk_add_u16 v20, v20, 4 op_sel_hi:[1,0] clamp\nv_pk_add_u16 v21, v21, 4 op_sel_hi:[1,0] clamp\nds_write_b64 v4, v[20:21] offset:5120\n" \
  "s_setprio 0\ns_setpc_b64 s[90:91]\n61:\ns_movk_i32 s84, 0x4000\n63:\nds_read_b32 v23, v9 offset:32\ns_sub_u32 s84, s84, 1\ns_cbranch_scc1 62b\n" \
  "s_waitcnt lgkmcnt(0)\nv_readfirstlane_b32 s85, v23\ns_cmp_lt_u32 s85, 3\ns_cbranch_scc1 63b\ns_branch 62b\n5:\n" \
  "ds_read_b64 v[124:125], v3 offset:1088\nds_read_b64 v[126:127], v3 offset:1096\nds_read_b64 v[120:121], v3 offset:0\n" \
  "ds_read_b64 v[122:123], v3 offset:8\nds_read_b64 v[116:117], v3 offset:64\nds_read_b64 v[118:119], v3 offset:72\n" \
  "ds_read_b64 v[112:113], v3 offset:128\nds_read_b64 v[114:115], v3 offset:136\nds_read_b64 v[108:109], v3 offset:192\n" \
  "ds_read_b64 v[110:111], v3 offset:200\nds_read_b64 v[104:105], v3 offset:256\nds_read_b64 v[106:107], v3 offset:264\n" \
  "ds_read_b64 v[100:101], v3 offset:320\nds_read_b64 v[102:103], v3 offset:328\nds_read_b64 v[96:97], v3 offset:384\n" \
  "ds_read_b64 v[98:99], v3 offset:392\nds_read_b64 v[92:93], v3 offset:448\nds_read_b64 v[94:95], v3 offset:456\n" \
  "ds_read_b64 v[88:89], v3 offset:512\nds_read_b64 v[90:91], v3 offset:520\nds_read_b64 v[84:85], v3 offset:576\n" \
  "ds_read_b64 v[86:87], v3 offset:584\nds_read_b64 v[80:81], v3 offset:640\nds_read_b64 v[82:83], v3 offset:648\n" \
  "ds_read_b64 v[76:77], v3 offset:704\nds_read_b64 v[78:79], v3 offset:712\nds_read_b64 v[72:73], v3 offset:768\n" \
  "ds_read_b64 v[74:75], v3 offset:776\nds_read_b64 v[68:69], v3 offset:832\nds_read_b64 v[70:71], v3 offset:840\n" \
  "ds_read_b64 v[64:65], v3 offset:896\nds_read_b64 v[66:67], v3 offset:904\nds_read_b64 v[60:61], v3 offset:960\n" \
  "ds_read_b64 v[62:63], v3 offset:968\nds_read_b64 v[56:57], v3 offset:1024\nds_read_b64 v[58:59], v3 offset:1032\n" \
  "ds_read_b64 v[52:53], v6 offset:0\nds_read_b64 v[54:55], v6 offset:8\nds_read_b64 v[48:49], v6 offset:64\nds_read_b64 v[50:51], v6 offset:72\n" \
  "ds_read_b64 v[44:45], v6 offset:128\nds_read_b64 v[46:47], v6 offset:136\nds_read_b64 v[40:41], v6 offset:192\n" \
  "ds_read_b64 v[42:43], v6 offset:200\ns_waitcnt lgkmcnt(0)\ns_barrier\nds_read_b128 v[24:27], v2 offset:1472\n" \
  "ds_read_b128 v[28:31], v2 offset:1984\nds_read_b128 v[32:35], v2 offset:2496\nds_read_b128 v[36:39], v2 offset:2560\ns_waitcnt lgkmcnt(3)\n" \
  "v_mfma_f32_16x16x32_f16 v[12:15], v[124:127], v[24:27], 0\ns_setprio 1\nds_read_b128 v[24:27], v2 offset:2624\ns_waitcnt lgkmcnt(3)\n" \
  "v_mfma_f32_16x16x32_f16 v[16:19], v[124:127], v[28:31], 0\nds_read_b128 v[124:127], v2 offset:2688\ns_waitcnt lgkmcnt(3)\n" \
  "v_mfma_f32_16x16x32_f16 v[12:15], v[120:123], v[32:35], v[12:15]\nds_read_b128 v[28:31], v2 offset:2752\ns_waitcnt lgkmcnt(3)\n" \
  "v_mfma_f32_16x16x32_f16 v[12:15], v[116:119], v[36:39], v[12:15]\nds_read_b128 v[32:35], v2 offset:2816\ns_waitcnt lgkmcnt(3)\n" \
  "v_mfma_f32_16x16x32_f16 v[12:15], v[112:115], v[24:27], v[12:15]\nds_read_b128 v[36:39], v2 offset:2880\ns_waitcnt lgkmcnt(3)\n" \
  "v_mfma_f32_16x16x32_f16 v[12:15], v[108:111], v[124:127], v[12:15]\nds_read_b128 v[24:27], v2 offset:2944\ns_waitcnt lgkmcnt(3)\n" \
  "v_mfma_f32_16x16x32_f16 v[12:15], v[104:107], v[28:31], v[12:15]\nds_read_b128 v[124:127], v2 offset:3008\ns_waitcnt lgkmcnt(3)\n" \
  "v_mfma_f32_16x16x32_f16 v[12:15], v[100:103], v[32:35], v[12:15]\nds_read_b128 v[28:31], v2 offset:3072\ns_waitcnt lgkmcnt(3)\n" \
  "v_mfma_f32_16x16x32_f16 v[12:15], v[96:99], v[36:39], v[12:15]\nds_read_b128 v[32:35], v2 offset:3136\ns_waitcnt lgkmcnt(3)\n" \
  "v_mfma_f32_16x16x32_f16 v[12:15], v[92:95], v[24:27], v[12:15]\nds_read_b128 v[36:39], v2 offset:3200\ns_waitcnt lgkmcnt(3)\n" \
  "v_mfma_f32_16x16x32_f16 v[12:15], v[88:91], v[124:127], v[12:15]\nds_read_b128 v[24:27], v2 offset:3264\n" \
  "v_mfma_f32_16x16x32_f16 v[16:19], v[120:123], v[124:127], v[16:19]\nds_read_b64 v[120:121], v6 offset:256\nds_read_b64 v[122:123], v6 offset:264\n" \
  "s_waitcnt lgkmcnt(4)\nv_mfma_f32_16x16x32_f16 v[12:15], v[84:87], v[28:31], v[12:15]\nds_read_b128 v[124:127], v2 offset:3328\n" \
  "v_mfma_f32_16x16x32_f16 v[16:19], v[116:119], v[28:31], v[16:19]\nds_read_b64 v[116:117], v6 offset:320\nds_read_b64 v[118:119], v6 offset:328\n" \
  "v_mfma_f32_16x16x32_f16 v[12:15], v[80:83], v[32:35], v[12:15]\nds_read_b128 v[28:31], v2 offset:3392\n" \
  "v_mfma_f32_16x16x32_f16 v[16:19], v[112:115], v[32:35], v[16:19]\ns_waitcnt lgkmcnt(6)\n" \
  "v_mfma_f32_16x16x32_f16 v[12:15], v[76:79], v[36:39], v[12:15]\nds_read_b128 v[112:115], v2 offset:3456\n" \
  "v_mfma_f32_16x16x32_f16 v[16:19], v[108:111], v[36:39], v[16:19]\nds_read2_b32 v[32:33], v8 offset0:16 offset1:17\n" \
  "ds_read2_b32 v[34:35], v8 offset0:18 offset1:19\nv_mfma_f32_16x16x32_f16 v[12:15], v[72:75], v[24:27], v[12:15]\n" \
  "ds_read_b128 v[108:111], v2 offset:3520\nv_mfma_f32_16x16x32_f16 v[16:19], v[104:107], v[24:27], v[16:19]\n" \
  "ds_read2_b32 v[36:37], v8 offset0:0 offset1:1\nds_read2_b32 v[38:39], v8 offset0:2 offset1:3\ns_waitcnt lgkmcnt(6)\n" \
  "v_mfma_f32_16x16x32_f16 v[12:15], v[68:71], v[124:127], v[12:15]\nds_read_b128 v[104:107], v2 offset:3584\n" \
  "v_mfma_f32_16x16x32_f16 v[16:19], v[100:103], v[124:127], v[16:19]\nds_read_b128 v[24:27], v2 offset:3648\n" \
  "ds_read2_b32 v[100:101], v8 offset0:32 offset1:33\nds_read2_b32 v[102:103], v8 offset0:34 offset1:35\n" \
  "v_mfma_f32_16x16x32_f16 v[12:15], v[64:67], v[28:31], v[12:15]\nds_read_b128 v[124:127], v2 offset:3712\nds_read_b32 v23, v9 offset:32\n" \
  "v_mfma_f32_16x16x32_f16 v[16:19], v[96:99], v[28:31], v[16:19]\nds_read_b128 v[96:99], v2 offset:3776\ns_waitcnt lgkmcnt(9)\n" \
  "v_mfma_f32_16x16x32_f16 v[12:15], v[60:63], v[112:115], v[12:15]\nds_read_b128 v[28:31], v2 offset:3840\n" \
  "v_mfma_f32_16x16x32_f16 v[16:19], v[92:95], v[112:115], v[16:19]\nds_read_b128 v[92:95], v2 offset:3904\n" \
  "v_mfma_f32_16x16x32_f16 v[12:15], v[56:59], v[108:111], v[12:15]\nds_read_b128 v[112:115], v2 offset:3968\n" \
  "v_mfma_f32_16x16x32_f16 v[16:19], v[88:91], v[108:111], v[16:19]\nds_read_b128 v[88:91], v2 offset:4032\ns_waitcnt lgkmcnt(5)\n" \
  "v_readfirstlane_b32 s85, v23\ns_cmp_lt_u32 s85, 3\ns_cbranch_scc1 71f\n72:\nv_mfma_f32_16x16x32_f16 v[16:19], v[84:87], v[104:107], v[16:19]\n" \
  "ds_read_b64_tr_b16 v[108:109], v5 offset:1024\nds_read_b64_tr_b16 v[110:111], v5 offset:1056\n" \
  "v_mfma_f32_16x16x32_f16 v[16:19], v[80:83], v[24:27], v[16:19]\nds_read_b64_tr_b16 v[84:85], v5 offset:1280\n" \
  "ds_read_b64_tr_b16 v[86:87], v5 offset:1312\nv_mfma_f32_16x16x32_f16 v[16:19], v[76:79], v[124:127], v[16:19]\n" \
  "ds_read_b64_tr_b16 v[104:105], v5 offset:1536\nds_read_b64_tr_b16 v[106:107], v5 offset:1568\ns_waitcnt lgkmcnt(9)\n" \
  "v_mfma_f32_16x16x32_f16 v[16:19], v[72:75], v[96:99], v[16:19]\nds_read_b64_tr_b16 v[80:81], v5 offset:1792\n" \
  "ds_read_b64_tr_b16 v[82:83], v5 offset:1824\nv_mfma_f32_16x16x32_f16 v[16:19], v[68:71], v[28:31], v[16:19]\n" \
  "ds_read_b64_tr_b16 v[24:25], v5 offset:2048\nds_read_b64_tr_b16 v[26:27], v5 offset:2080\ns_waitcnt lgkmcnt(11)\n" \
  "v_mfma_f32_16x16x32_f16 v[16:19], v[64:67], v[92:95], v[16:19]\nds_read_b64_tr_b16 v[76:77], v5 offset:2304\n" \
  "ds_read_b64_tr_b16 v[78:79], v5 offset:2336\nv_mfma_f32_16x16x32_f16 v[16:19], v[60:63], v[112:115], v[16:19]\n" \
  "ds_read_b64_tr_b16 v[124:125], v5 offset:2560\nds_read_b64_tr_b16 v[126:127], v5 offset:2592\ns_waitcnt lgkmcnt(12)\n" \
  "v_mfma_f32_16x16x32_f16 v[16:19], v[56:59], v[88:91], v[16:19]\nds_read_b64_tr_b16 v[72:73], v5 offset:2816\n" \
  "ds_read_b64_tr_b16 v[74:75], v5 offset:2848\nv_mfma_f32_16x16x32_f16 v[12:15], v[108:111], v[52:55], v[12:15]\ns_waitcnt lgkmcnt(10)\n" \
  "v_mfma_f32_16x16x32_f16 v[12:15], v[84:87], v[48:51], v[12:15]\nds_read_b64_tr_b16 v[96:97], v7 offset:0\n" \
  "ds_read_b64_tr_b16 v[98:99], v7 offset:64\nv_mfma_f32_16x16x32_f16 v[12:15], v[104:107], v[44:47], v[12:15]\n" \
  "ds_read_b64_tr_b16 v[68:69], v7 offset:512\nds_read_b64_tr_b16 v[70:71], v7 offset:576\n" \
  "v_mfma_f32_16x16x32_f16 v[16:19], v[104:107], v[52:55], v[16:19]\ns_waitcnt lgkmcnt(10)\n" \
  "v_mfma_f32_16x16x32_f16 v[12:15], v[80:83], v[40:43], v[12:15]\nds_read_b64_tr_b16 v[28:29], v7 offset:1024\n" \
  "ds_read_b64_tr_b16 v[30:31], v7 offset:1088\nv_mfma_f32_16x16x32_f16 v[16:19], v[80:83], v[48:51], v[16:19]\n" \
  "ds_read2_b32 v[64:65], v8 offset0:48 offset1:49\nds_read2_b32 v[66:67], v8 offset0:50 offset1:51\n" \
  "v_mfma_f32_16x16x32_f16 v[12:15], v[24:27], v[120:123], v[12:15]\nv_mfma_f32_16x16x32_f16 v[16:19], v[24:27], v[44:47], v[16:19]\n" \
  "s_waitcnt lgkmcnt(10)\nv_mfma_f32_16x16x32_f16 v[12:15], v[76:79], v[116:119], v[12:15]\nds_read_b64_tr_b16 v[92:93], v7 offset:1536\n" \
  "ds_read_b64_tr_b16 v[94:95], v7 offset:1600\nv_mfma_f32_16x16x32_f16 v[16:19], v[76:79], v[40:43], v[16:19]\n" \
  "v_mfma_f32_16x16x32_f16 v[16:19], v[124:127], v[120:123], v[16:19]\ns_waitcnt lgkmcnt(8)\n" \
  "v_mfma_f32_16x16x32_f16 v[16:19], v[72:75], v[116:119], v[16:19]\nv_mfma_f32_16x16x32_f16 v[12:15], v[96:99], v[32:35], v[12:15]\n" \
  "v_mfma_f32_16x16x32_f16 v[16:19], v[96:99], v[36:39], v[16:19]\ns_waitcnt lgkmcnt(4)\n" \
  "v_mfma_f32_16x16x32_f16 v[12:15], v[68:71], v[100:103], v[12:15]\nv_mfma_f32_16x16x32_f16 v[16:19], v[68:71], v[32:35], v[16:19]\n" \
  "s_waitcnt lgkmcnt(2)\nv_mfma_f32_16x16x32_f16 v[12:15], v[28:31], v[64:67], v[12:15]\n" \
  "v_mfma_f32_16x16x32_f16 v[16:19], v[28:31], v[100:103], v[16:19]\ns_waitcnt lgkmcnt(0)\n" \
  "v_mfma_f32_16x16x32_f16 v[16:19], v[92:95], v[64:67], v[16:19]\ns_nop 4\nv_cvt_pkrtz_f16_f32 v20, v12, v13\nv_cvt_pkrtz_f16_f32 v21, v14, v15\n" \
  "v_pk_add_u16 v20, v20, 4 op_sel_hi:[1,0] clamp\nv_pk_add_u16 v21, v21, 4 op_sel_hi:[1,0] clamp\nds_write_b64 v4, v[20:21] offset:6144\n" \
  "v_cvt_pkrtz_f16_f32 v20, v16, v17\nv_cvt_pkrtz_f16_f32 v21, v18, v19\nv_pk_add_u16 v20, v20, 4 op_sel_hi:[1,0] clamp\n" \
  "v_pk_add_u16 v21, v21, 4 op_sel_hi:[1,0] clamp\nds_write_b64 v4, v[20:21] offset:7168\ns_setprio 0\ns_setpc_b64 s[90:91]\n71:\n" \
  "s_movk_i32 s84, 0x4000\n73:\nds_read_b32 v23, v9 offset:32\ns_sub_u32 s84, s84, 1\ns_cbranch_scc1 72b\ns_waitcnt lgkmcnt(0)\n" \
  "v_readfirstlane_b32 s85, v23\ns_cmp_lt_u32 s85, 3\ns_cbranch_scc1 73b\ns_branch 72b\n"
#define CONV_CLOB "memory", "scc", "s84", "s85", "s90", "s91", "v12", "v13", "v14", "v15", "v16", "v17", "v18", "v19", "v20", "v21", "v22", "v23", "v24", "v25", "v26", "v27", "v28", "v29", "v30", "v31", "v32", "v33", "v34", "v35", "v36", "v37", "v38", "v39", "v40", "v41", "v42", "v43", "v44", "v45", "v46", "v47", "v48", "v49", "v50", "v51", "v52", "v53", "v54", "v55", "v56", "v57", "v58", "v59", "v60", "v61", "v62", "v63", "v64", "v65", "v66", "v67", "v68", "v69", "v70", "v71", "v72", "v73", "v74", "v75", "v76", "v77", "v78", "v79", "v80", "v81", "v82", "v83", "v84", "v85", "v86", "v87", "v88", "v89", "v90", "v91", "v92", "v93", "v94", "v95", "v96", "v97", "v98", "v99", "v100", "v101", "v102", "v103", "v104", "v105", "v106", "v107", "v108", "v109", "v110", "v111", "v112", "v113", "v114", "v115", "v116", "v117", "v118", "v119", "v120", "v121", "v122", "v123", "v124", "v125", "v126", "v127"
constexpr int TDP = 1216, T_3 = 4 * TDP, T_4 = T_3 + 512, TB = T_4 + 320;
constexpr int C_U = 480, UB = C_U + 4096 + 96, C_G5 = 0, C_G3 = 3200, C_G9 = 6400, C_G7 = C_G9 + 2096, GB = C_G7 + 2080 + 112;
constexpr int O_FL = 2 * TB, O_UL = O_FL + 64, O_IM = O_UL + 2 * UB, O_ST = O_IM + 2 * GB;
constexpr int X_TB = 10240, X_UL = X_TB + TB, X_IM = X_UL + UB, X_ST = X_IM + GB;
constexpr int X_WR = X_IM + 128 * IP;
#define X_RW(w) (LW + 9216 * (2 + ((w) >> 1)) + 2560 * ((w) & 1))
constexpr int RP = 1216;
constexpr int LW = 36864 + 512, LA = LW, LM = LW + 36864;
constexpr int L_SC = LM + 2048, SMEM = O_IM + 256 * IP;
static_assert(L_SC + 1152 <= SMEM && O_ST + 8192 <= SMEM && X_IM + 128 * IP <= LM && X_ST + 8192 <= LM && UB % 128 == 64 && GB % 128 == 64 && SMEM <= 79872, "");
struct TOps { uint32_t xa, xb, ya, pd, pf, pg, pe; };
__device__ __forceinline__ void table_ops(uint32_t im, uint32_t tb, int ct, int lane, TOps& t) {
  const int j = lane & 15, kq = lane >> 4, q4 = (lane >> 2) & 3, p4 = lane & 3, jj = j + 16 * ct;
  const uint32_t sa = im + (uint32_t)(IP * (lane >> 2));
  t.xa = sa + (uint32_t)(I_X + 8 * p4); t.xb = sa + (uint32_t)(I_X + 32 + 8 * (p4 & 1)); t.ya = sa + (uint32_t)(32 * ct + 8 * p4);
  t.pd = tb + (uint32_t)(2 * (541 - (8 * jj + 4 * (kq & 1) + 256 * (kq >> 1))));
  t.pf = tb + (uint32_t)((kq & 1) ? T_4 + 2 * (63 - jj) : T_3 + 2 * (127 - 2 * jj));
  t.pg = t.pf - ((kq & 1) ? 64 : 2);
  t.pe = tb + (uint32_t)(p4 * (TDP + 2) + ((lane < 12) ? 2 * (32 - q4) : 2 * (572 + (q4 & 1))));
  asm volatile("" : "+v"(t.xa), "+v"(t.xb), "+v"(t.ya), "+v"(t.pd), "+v"(t.pf), "+v"(t.pg), "+v"(t.pe));
}
typedef __attribute__((address_space(3))) char* lptr;
#define LP(T, a) ((__attribute__((address_space(3))) T*)(uintptr_t)(a))
__device__ __forceinline__ uint32_t table_tile(const TOps& t, int ct, int lane, uint32_t evp, float dsk TSP) {
  u32x4 ev;
  f32x4 ad[2], af[2];
  asm volatile(T_ASM : "=&v"(ad[0]), "=&v"(ad[1]), "=&v"(af[0]), "=&v"(af[1]), "=&v"(ev) : "v"(t.xa), "v"(t.xb), "v"(t.ya), "v"(evp) : T_CLOB);
  TS(0, 4);
  const int e0 = (int)(ev[0] + ev[2]), e1 = (int)(ev[1] + ev[3]);
  const int em = min(max(max(max(e0, e1), (int)((__float_as_uint(dsk) >> 23) & 0xffu) - 133), -110), 115);
  const int ts = ((uint32_t)(em + 12) <= 8u) ? 1 : -3 - em;
  float s0 = __builtin_amdgcn_ldexpf(1.f, e0 - 29 + ts), s1 = __builtin_amdgcn_ldexpf(1.f, e1 - 29 + ts);
  float d0 = (ct == 0 && lane == 0) ? __builtin_amdgcn_ldexpf(dsk, ts) : 0.f;
  asm volatile("s_nop 1" : "+v"(ad[0]), "+v"(ad[1]), "+v"(af[0]), "+v"(af[1]), "+v"(s0), "+v"(s1), "+v"(d0));
  const f32x4 k = ad[0] * s0 + (f32x4){d0, 0.f, 0.f, 0.f} + ad[1] * s1, f = af[0] * s0 + af[1] * s1;
  auto pk = [](float a, float b) { uint32_t r; asm("v_cvt_pk_f16_f32 %0, %1, %2" : "=v"(r) : "v"(a), "v"(b)); return r; };
  asm volatile("s_nop 0" : : "v"(k), "v"(f));
  const uint32_t p32 = pk(k[3], k[2]), p10 = pk(k[1], k[0]), p21 = pk(k[2], k[1]), p01 = pk(f[0], f[1]), p23 = pk(f[2], f[3]);
  *LP(u32x2, t.pd + 3 * TDP + 6) = (u32x2){p32, p10};
  *LP(uint32_t, t.pd + TDP + 2) = p32; *LP(uint32_t, t.pd + TDP + 6) = p10;
  *LP(uint32_t, t.pd + 2) = p21; *LP(uint32_t, t.pd + 2 * TDP + 6) = p21;
  *LP(u16, t.pd) = (u16)p32; *LP(u16, t.pd + 2 * TDP + 4) = (u16)p32;
  asm volatile("ds_write_b16_d16_hi %0, %1 offset:6\n\tds_write_b16_d16_hi %0, %1 offset:%2" : : "v"(t.pd), "v"(p10), "n"(2 * TDP + 10) : "memory");
  if (ct) {
    const uint32_t va = (uint32_t)__builtin_amdgcn_readlane((int)p32, 63), vb = (uint32_t)__builtin_amdgcn_readlane((int)p23, 15) >> 16;
    *LP(u16, t.pe) = (u16)((lane < 12) ? va : vb);
  }
  if (lane < 32) {
    *LP(u16, t.pf + 128) = (u16)p01; *LP(u16, t.pg + 128) = (u16)(p01 >> 16);
    *LP(u16, t.pf) = (u16)p23; *LP(u16, t.pg) = (u16)(p23 >> 16);
  }
  return (uint32_t)(127 + 5 - ts) << 23;
}
__device__ __forceinline__ uint32_t hadd(uint32_t a, uint32_t b) { return __builtin_bit_cast(uint32_t, __builtin_bit_cast(f16x2, a) + __builtin_bit_cast(f16x2, b)); }
template <bool FULL> __device__ __forceinline__ void sums(char* sm, const u32x4 p, const u32x4 c) {
  const uint32_t D[8] = {p[0], p[1], p[2], p[3], c[0], c[1], c[2], c[3]};
  uint32_t O[7];
#pragma unroll
  for (int j = 0; j < 7; ++j) O[j] = __builtin_amdgcn_alignbit(D[j + 1], D[j], 16);
  u32x4 a, b;
#pragma unroll
  for (int j = 0; j < 4; ++j) a[j] = hadd(D[j + 2], hadd(O[j + 1], O[j + 2]));
  *(u32x4*)(sm + C_G3 + 32) = a;
#pragma unroll
  for (int j = 0; j < 4; ++j) b[j] = hadd(a[j], hadd(D[j + 1], D[j + 3]));
  *(u32x4*)(sm + C_G5 + 32) = b;
  if (FULL) {
#pragma unroll
    for (int j = 0; j < 4; ++j) a[j] = hadd(b[j], hadd(O[j], O[j + 3]));
    *(u32x4*)(sm + C_G7 + 32) = a;
#pragma unroll
    for (int j = 0; j < 4; ++j) b[j] = hadd(a[j], hadd(D[j], D[j + 4]));
    *(u32x4*)(sm + C_G9 + 32) = b;
  }
}
__device__ __forceinline__ void count_up(uint32_t a) {
  uint32_t t;
  asm volatile("s_mov_b64 exec, 1\n\tv_mov_b32 %0, 1\n\tds_add_u32 %1, %0\n\ts_mov_b64 exec, -1" : "=&v"(t) : "v"(a) : "memory");
}
__device__ __forceinline__ int pad_at(int r) { return 16 * (r & 1) + 3200 * (r >> 1) - ((r >= 6) ? 3200 - 2096 : 0); }
struct COps { uint32_t b, a, o, g3, k3, g4, k4, f; };
__device__ __forceinline__ void conv_ops(uint32_t tb, uint32_t ub, uint32_t cb, int lane, COps& e) {
  const uint32_t j = lane & 15, kq = lane >> 4, q4 = (lane >> 2) & 3, p4 = lane & 3;
  e.b = ub + 32u * j + 16u * kq;
  e.a = tb + p4 * TDP + 32u - 8u * q4 + 16u * kq;
  e.g3 = cb + C_G5 + 8 + 64u * kq + 8u * (q4 + p4) + (q4 & 1u) * 3200u;
  e.g4 = cb + C_G9 + 8 + 128u * kq + 16u * q4 + 8u * p4 + (q4 & 1u) * 2096u;
  e.k3 = tb + T_3 + 120 - 8u * j + 16u * kq;
  e.k4 = tb + T_4 + 60 - 4u * j + 16u * kq;
  asm volatile("" : "+v"(e.b), "+v"(e.a), "+v"(e.g3), "+v"(e.g4), "+v"(e.k3), "+v"(e.k4));
}
#define CONV_RUN(prog, e, ...) do { int prog_ = (prog); \
    asm volatile("s_call_b64 s[90:91], kf_conv" : "+{s89}"(prog_), "+{v2}"(e.b), "+{v3}"(e.a), "+{v4}"(e.o), "+{v5}"(e.g3), "+{v6}"(e.k3), "+{v7}"(e.g4), \
                 "+{v8}"(e.k4), "+{v9}"(e.f) __VA_ARGS__ : : CONV_CLOB); } while (0)

template <bool E>
__device__ __forceinline__ void ln_stats(const uint32_t* v, int lane, float sc, f32x2* t, f32x4 z, f32x2& rc, f32x2& nc, float& e) {
  float s1a = z[0], s2a = z[1], s1b = z[2], s2b = z[3];
#pragma unroll
  for (int k = 0; k < 4; ++k) {
    s1a = dot2(v[k], 0x3f803f80u, s1a); s2a = dot2(v[k], v[k], s2a);
    s1b = dot2(v[4 + k], 0x3f803f80u, s1b); s2b = dot2(v[4 + k], v[4 + k], s2b);
  }
#pragma unroll
  for (int k = 0; k < 9; ++k) t[k] = (f32x2){bflo(v[k]), bfhi(v[k])};
  const float rs = row_sum(fold16(dot2(v[8], 0x3f803f80u, fold32(s1a, s1b)), dot2(v[8], v[8], fold32(s2a, s2b))));
  if (E) { e = row_sum(e); e += __int_as_float(__builtin_amdgcn_update_dpp(0, __float_as_int(e), 0x142, 0xa, 0xf, false)); }
  const f32x2 mean = (f32x2){rdl(rs, 15), rdl(rs, 47)} * (1.f / D_), m2 = mean * mean;
  f32x2 var = (f32x2){rdl(rs, 31), rdl(rs, 63)} * (1.f / D_) - m2;
  asm volatile("" : "+v"(t[0]), "+v"(t[1]), "+v"(t[2]), "+v"(t[3]), "+v"(t[4]), "+v"(t[5]), "+v"(t[6]), "+v"(t[7]), "+v"(t[8]));
  if (__builtin_expect(!(m2[0] <= 16.f * var[0] && m2[1] <= 16.f * var[1]), 0)) {
    float q[3] = {0.f, 0.f, 0.f};
#pragma unroll
    for (int k = 0; k < 9; ++k) {
      const f32x2 a = t[k] - ((k < 4 || (k == 8 && lane < 32)) ? mean[0] : mean[1]);
      q[k >> 2] += a[0] * a[0] + a[1] * a[1];
    }
    float qa = q[0] + ((lane < 32) ? q[2] : 0.f), qb = q[1] + ((lane < 32) ? 0.f : q[2]);
    wave_sum2(qa, qb);
    var = (f32x2){qa, qb} * (1.f / D_);
  }
  const uint32_t mp = pack2(mean[0], mean[1]), vp = pack2(var[0], var[1]);
  const f32x2 ve = (f32x2){bflo(vp), bfhi(vp)} + 1e-5f;
  const uint32_t e2 = pack2(ve[0], ve[1]);
  const uint32_t sp = pack2(__builtin_amdgcn_sqrtf(bflo(e2)), __builtin_amdgcn_sqrtf(bfhi(e2)));
  rc = (f32x2){__builtin_amdgcn_rcpf(bflo(sp)), __builtin_amdgcn_rcpf(bfhi(sp))} * sc;
  nc = -(f32x2){bflo(mp), bfhi(mp)} * rc;
}
template <bool H>
__device__ __forceinline__ void ln_apply(const f32x2* t, int lane, f32x2 rc, f32x2 nc, uint32_t* c) {
  const float nm[3] = {nc[0], nc[1], (lane < 32) ? nc[0] : nc[1]}, rr[3] = {rc[0], rc[1], (lane < 32) ? rc[0] : rc[1]};
  f32x2 dd[9];
#pragma unroll
  for (int k = 0; k < 9; ++k) dd[k] = __builtin_elementwise_fma(t[k], (f32x2){rr[k >> 2], rr[k >> 2]}, (f32x2){nm[k >> 2], nm[k >> 2]});
  asm volatile("" : "+v"(dd[0]), "+v"(dd[1]), "+v"(dd[2]), "+v"(dd[3]), "+v"(dd[4]), "+v"(dd[5]), "+v"(dd[6]), "+v"(dd[7]), "+v"(dd[8]));
#pragma unroll
  for (int k = 0; k < 9; ++k) {
    const f32x2 d = dd[k];
    typedef __attribute__((ext_vector_type(2))) uint16_t u16x2;
    if (H) c[k] = __builtin_bit_cast(uint32_t, __builtin_elementwise_add_sat(__builtin_bit_cast(u16x2, __builtin_amdgcn_cvt_pkrtz(d[0], d[1])), (u16x2){4, 4}));
    else c[k] = pack2(d[0], d[1]);
  }
}
__device__ __forceinline__ uint32_t med3u(uint32_t a, uint32_t b, uint32_t c) { return max(min(a, b), min(max(a, b), c)); }
__device__ __forceinline__ uint32_t oct_maxu(uint32_t v) {
  v = max(v, (uint32_t)DPP_I((int)v, 0xb1, 0xf));
  v = max(v, (uint32_t)DPP_I((int)v, 0x4e, 0xf));
  v = max(v, (uint32_t)DPP_I((int)v, 0x141, 0xf));
  return v;
}

#define KARGS const u16* __restrict__ x, const u16* __restrict__ Are, const u16* __restrict__ Aim, const u16* __restrict__ Bre, \
    const u16* __restrict__ Bim, const u16* __restrict__ Cre, const u16* __restrict__ Cim, const u16* __restrict__ logdt, \
    const u16* __restrict__ Dskip, char* __restrict__ ws, const u16* __restrict__ wr, const u16* __restrict__ wg, \
    const u16* __restrict__ wu, const u16* __restrict__ wd, u16* __restrict__ out
#define KPASS x, Are, Aim, Bre, Bim, Cre, Cim, logdt, Dskip, ws, wr, wg, wu, wd, out
template <int ROLE, int FFN = -1>
__device__ __forceinline__ void role_body(char* smem, KARGS) {
  const int tid = threadIdx.x, lane = tid & 63;
  const int w = __builtin_amdgcn_readfirstlane(tid >> 6), wl = w & 3;
  const int b = blockIdx.x & 255, bx = b & 7, bj = b >> 3;
  constexpr bool isl = ROLE == 1 || ROLE == 2;
  const int tg = isl ? 32 * (bj >> 2) + 4 * bx + (bj & 3) : 32 * bx + bj;
  u32x4 xa, xc;
  uint32_t ep, r0;
#define P_WAIT(n, ...) asm volatile("s_waitcnt vmcnt(" #n ")\n\tv_lshlrev_b32 %0, 16, v100\n\tv_lshlrev_b32 %1, 16, v101\n\tv_lshlrev_b32 %2, 16, v102" \
    : "=v"(ldt), "=v"(are), "=v"(aim) : : "v100", "v101", "v102" __VA_ARGS__)
#define P_MORE() asm volatile("v_lshlrev_b32 %0, 16, v103\n\tv_lshlrev_b32 %1, 16, v104\n\tv_lshlrev_b32 %2, 16, v105\n\tv_lshlrev_b32 %3, 16, v106" \
    : "=v"(bre), "=v"(bim), "=v"(cre), "=v"(cim) : : "v103", "v104", "v105", "v106")
  TSD;
  TS(0, 0);
  const uint32_t lb0 = (uint32_t)(uintptr_t)(__attribute__((address_space(3))) char*)smem;
  const int i = lane & 15, kq = lane >> 4;
  const u32x4 ones = {0xffffffffu, 0xffffffffu, 0xffffffffu, 0xffffffffu}, zero4 = {0u, 0u, 0u, 0u};
  auto u_again = [&](u32x4& uw, const u32x4& ut, uint32_t uo) {
#pragma unroll 1
    for (int spin = 0; spin < SPIN_CAP; ++spin) {
      const bool na = ((uw[0] | uw[1] | uw[2] | uw[3]) & UMASK) != 0u;
      if (__builtin_expect(!__any(na), 1)) break;
      TSC(19);
      SLEEP(USLEEP);
      if (na) { LD16W(uw, uo); uw ^= ut; }
    }
  };
  constexpr uint32_t S16 = 0x41800000u, LMASK = 0x00070007u;
  auto v_put = [&](u32x4 v, const u32x4& ut, bool other, uint32_t vo) {
    const bool second = __any(other || ((v[0] | v[1] | v[2] | v[3]) & 0x40004000u) != 0u);
    if (__builtin_expect(second, 0)) v = (v & ~LMASK) | (~ut & LMASK); else v = (v & ~LMASK) | ut;
    ST16(vo, v);
    return second;
  };
  auto v_end = [&](uint32_t T, int n) { DRAIN(); if (lane == 0) ST4(O_DN + 16u * n, T); };
  if constexpr (!isl) {
    const int ch = wl >> 1, tl = tid & 255;
    int* evp = (int*)(smem + O_FL + 16 * ch);
    float ldt, are, aim, dsk;
    if constexpr (ROLE == 0) {
      __builtin_amdgcn_s_setprio(KPRIO);
      COps e;
      conv_ops(lb0 + (uint32_t)(ch * TB), lb0 + (uint32_t)(O_UL + ch * UB), lb0 + (uint32_t)(O_IM + ch * GB), lane, e);
      e.o = lb0 + O_ST + (uint32_t)(16 * ((4 * i + 2 * (kq >> 1) + ch) ^ (i >> 1)) + 8 * (kq & 1));
      e.f = lb0 + O_FL;
      TOps t;
      table_ops(lb0 + (uint32_t)(O_IM + 128 * IP * ch), lb0 + (uint32_t)(TB * ch), wl & 1, lane, t);
      uint32_t pz = lb0 + O_IM + (uint32_t)(((lane >> 3) & 1) * GB + pad_at(lane & 7)), im = lb0 + (uint32_t)(O_IM + IP * tl);
      asm volatile("" : "+v"(e.o), "+v"(e.f), "+v"(pz), "+v"(im));
      float bre, bim, cre, cim;
      P_WAIT(0, , "v103", "v104", "v105", "v106", "v107", "v109");
      asm volatile("" : "={v109}"(ep));
      const uint32_t T = ((uint32_t)__builtin_amdgcn_readfirstlane(ep) + 1u) * 0x9E3779B1u;
      if (w == 3 && lane < 8) ST16(O_KEY + 128u * b + 16u * lane, ones);
      P_MORE();
      asm volatile("v_lshlrev_b32 %0, 16, v107" : "=v"(dsk) : : "v107");
      TS(0, 1);
      evp[wl & 1] = gen_y(ldt, are, aim, bre, bim, cre, cim, im, [] {} TSA);
      TS(0, 8);
      BAR();
      TS(0, 2);
      const uint32_t cv = table_tile(t, wl & 1, lane, lb0 + O_FL + 16 * ch, dsk TSA), cs = (uint32_t)__builtin_amdgcn_readfirstlane(cv);
      *(uint32_t*)(smem + O_FL + 40 + 4 * ch) = cv;
      TS(0, 9);
      gsync0(lb0 + O_FL + 36, 4);
      TS(0, 3);
      *(u32x4*)(smem + O_ST + 16 * tl) = ones;
      *(u32x4*)(smem + O_ST + 4096 + 16 * tl) = ones;
      if (w == 0) *LP(u32x4, pz) = zero4;
      DRAIN();
      CONV_RUN(wl & 1, e);
      if (wl & 1) asm volatile("s_mov_b64 exec, 1\n\tglobal_store_dwordx2 %0, %1, %2 sc1" : : "v"(O_SC + 16u * (2 * tg + ch)), "v"((u32x2){T, cs}), "s"(ws) : "memory");
      TS(0, 5);
    } else {
      __builtin_amdgcn_s_setprio(UPRIO);
      const uint32_t ck0 = (uint32_t)__builtin_amdgcn_s_memtime(), rt0 = (uint32_t)__builtin_amdgcn_s_memrealtime();
#pragma unroll
      for (int k = 0; k < 3; ++k) *(u32x4*)(smem + ((16 * (tl + 256 * k) < O_FL) ? 16 * (tl + 256 * k) : O_FL + 32)) = zero4;
      if (w == 7) *(u32x4*)(smem + O_UL + (lane >> 5) * UB + 16 * min(lane & 31, 29)) = zero4;
      uint32_t uo = u_at(tl >> 1, 2 * tg + (tid & 1)), go = O_SU + (uint32_t)(256 * bx + 32 * wl + (lane & 31) + 4 * (lane & 32)) * 128u;
      uint32_t ul = (uint32_t)(O_UL + (tid & 1) * UB + C_U - 16 + 16 * (tl >> 1)), ug = (uint32_t)(O_IM + (tid & 1) * GB + 16 * (tl >> 1)), so = lb0 + O_ST + 16u * (uint32_t)(lane ^ (lane >> 3));
      uint32_t vo = O_V + (uint32_t)tg * VP + 16u * lane;
      asm volatile("" : "+v"(uo), "+v"(go), "+v"(ul), "+v"(ug), "+v"(so), "+v"(vo));
      P_WAIT(1, , "v109");
      TS(0, 1);
      evp[2 + (wl & 1)] = gen_x(ldt, are, aim, smem + O_IM + IP * tl);
      asm volatile("s_waitcnt vmcnt(0)" : "={v109}"(ep));
      const uint32_t T = ((uint32_t)__builtin_amdgcn_readfirstlane(ep) + 1u) * 0x9E3779B1u;
      TS(0, 8);
      BAR();
      TS(0, 2);
      if (w == 7 && bj >= 24) {
        const uint32_t t_ = ahead(ck0, rt0, VDB);
        ST16(O_SB + (uint32_t)(64 * (lane >> 3) + 8 * bx + bj - 24) * 128u + 16u * (lane & 7), ((u32x4){T, t_, T, t_}));
      }
      u32x4 ut = {utag(T, 0), utag(T, 1), utag(T, 2), utag(T, 3)};
      asm volatile("" : "+v"(ut));
      SLEEP(GSLEEP);
      u32x2 pv;
      pv[1] = gate(ws, go, T);
      TS(0, 3);
      until(latest(pv[1]) + UD);
      TS(0, 7);
      u32x4 uw, ux;
      asm volatile("global_load_dwordx4 %0, %2, %4 sc1\n\tglobal_load_dwordx4 %1, %3, %4 sc1"
                   : "=&v"(uw), "=&v"(ux) : "v"(uo), "v"(uo + 64 * UP * 32), "s"(ws) : "memory");
      __builtin_amdgcn_s_setprio(FPR);
      asm volatile("s_waitcnt vmcnt(1)" : "+v"(uw));
      uw ^= ut;
      *(u32x4*)(smem + ul + 16) = uw;
      asm volatile("s_waitcnt vmcnt(0)" : "+v"(ux));
      TS(0, 11);
      ux ^= ut;
      *(u32x4*)(smem + ul + 2048 + 16) = ux;
      if (__builtin_expect(__any(((uw[0] | uw[1] | uw[2] | uw[3] | ux[0] | ux[1] | ux[2] | ux[3]) & UMASK) != 0u), 0)) {
        u_again(uw, ut, uo);
        u_again(ux, ut, uo + 64 * UP * 32);
        *(u32x4*)(smem + ul + 16) = uw;
        *(u32x4*)(smem + ul + 2048 + 16) = ux;
      }
      BAR();
      TS(0, 4);
      const u32x4 pw = *(const u32x4*)(smem + ul);
      if (wl < 2) {
        const u32x4 px = *(const u32x4*)(smem + ul + 2048);
        sums<true>(smem + ug, pw, uw);
        sums<false>(smem + ug + 2048, px, ux);
      } else sums<true>(smem + ug, pw, uw);
      count_up(lb0 + O_FL + 32);
      __builtin_amdgcn_s_setprio(SPR);
      TS(0, 5);
      const bool other = *(const uint32_t*)(smem + O_FL + 40 + 4 * (lane & 1)) != S16;
      bool second = false;
#pragma unroll
      for (int k = 0; k < 2; ++k) {
        const uint32_t o = ((k ? 0x3247u : 0x1065u) >> (4 * wl) & 7u) * 1024u;
        u32x4 v;
#pragma unroll 1
        for (int spin = 0; spin < SPIN_CAP; ++spin) {
          asm volatile("ds_read_b128 %0, %1\n\ts_waitcnt lgkmcnt(0)" : "=&v"(v) : "v"(so + o) : "memory");
          if (!__any(max4(v) == 0xffffffffu)) break;
          __builtin_amdgcn_s_sleep(VPS);
        }
        TS(0, 12 + k);
        second |= v_put(v, ut, other, vo + o);
        TS(0, 10 - 4 * k);
      }
      if (__builtin_expect(second, 0)) v_end(T, 4 * tg + wl);
    }
    TSE();
  } else {
  uint32_t* cand = (uint32_t*)(smem + LM);
  uint32_t* sel = cand + 128;
  uint32_t* kl = sel + 48;
  uint32_t* cnt = sel + 64;
  const uint32_t ca = lb0 + LM + 4 * (128 + 64);
  u16* hm = (u16*)(cnt + 16);
  uint32_t T;
  const bool ffn = (FFN < 0) ? bj < 18 : FFN, isx = FFN != 1 && bj >= 24;
  const int fb = 18 * bx + bj;
  constexpr bool ln2 = ROLE == 2;
  const int g0 = tg, g2 = tg;
  u32x4 bw[18];
#pragma unroll
  for (int s = 0; s < 9; ++s) bw[s] = (u32x4){0u, 0u, 0u, 0u};
  int tid2 = threadIdx.x;
  uint32_t tv = 0u, tk = 0u;
  auto v_look = [&](uint32_t tu, int lane) {
    uint32_t tm = latest(gate(ws, O_SB + (uint32_t)(64 * bx + lane) * 128u, T)) - V2D;
    if ((int)(tm - (tu + UD + VD2)) < 0) tm = tu + UD + VD2;
    cnt[8] = tm;
    count_up(ca + 52);
  };
  auto v_time = [&] { gwait(ca + 52, 1); tv = (uint32_t)__builtin_amdgcn_readfirstlane(cnt[8]); };
  uint32_t ts1 = 0u;
  auto rest = [&] {
    if (ts1 && (int)((uint32_t)__builtin_amdgcn_s_memrealtime() - ts1) > 0) {
      v_time();
      ts1 = (tv - TSTOP) | 1u;
      if ((int)((uint32_t)__builtin_amdgcn_s_memrealtime() - ts1) > 0) { TS(0, 8); until(tv + RD); ts1 = 0u; }
    }
  };
  auto gu_dma = [&](int q0, int q1, auto depth) {
    const char* gs = (const char*)((w < 2) ? wg : wu) + (size_t)((144 * (w & 1)) * (F_ * 2) + fb * 64);
    const uint32_t gv = (uint32_t)((lane >> 2) * (F_ * 2) + 16 * ((lane & 3) ^ (lane >> 5) ^ (w & 2)));
#pragma unroll 1
    for (int q = q0; q < q1; ++q) {
      const int k = (int)((((bj & 1) ? ORD1 : ((unsigned __int128)0x876543210u << 36 | 0x876543210ull)) >> (4 * (q - (q >= 9 ? 9 : 0)) + (q >= 9 ? 36 : 0))) & 15u), h = q >= 9;
      if (q >= RQ) rest();
      dma16(gs + (size_t)(288 * h + 16 * k) * (F_ * 2), gv, lb0 + LW * h + 9216 * w + 1024 * k);
      if (!BALL || q < RQ || ts1) asm volatile("s_waitcnt vmcnt(%0)" : : "n"(depth.value) : "memory");
    }
  };
  auto down_dma = [&](int k0, int k1, int lane) {
#pragma unroll 1
    for (int k = k0; k < k1; ++k) {
      const int o = 9216 * wl + 2304 * k;
      const char* ds = (const char*)wd + (size_t)(fb * 36864 + o);
      const uint32_t dl = lb0 + o + 32 * ((4 * wl + k + 1) >> 1);
      if (ROLE == 1) rest();
      dma16d(ds, 16u * lane, dl);
      dma16d(ds + 1024, 16u * lane, dl + 1024);
      uint32_t keep;
      asm volatile("s_mov_b32 %0, m0\n\ts_mov_b32 m0, %3\n\ts_mov_b64 exec, 0xffff\n\tglobal_load_lds_dwordx4 %1, %2" DPOL2 "\n\ts_mov_b64 exec, -1\n\ts_mov_b32 m0, %0\n\ts_waitcnt vmcnt(%4)"
                   : "=&s"(keep) : "v"(16u * lane), "s"(ds + 2048), "s"(dl + 2048), "n"(ROLE == 1 ? DDEPTH : 40) : "memory");
    }
  };
  auto gu_read = [&](int h) {
    const uint32_t ra = lb0 + LW * h + (uint32_t)(((lane >> 1) & 1) * 18432 + (8 * kq + ((lane >> 2) & 3)) * 64 + ((w ^ (kq & 1) ^ (lane & 2)) * 16) + 8 * (lane & 1));
#pragma unroll
    for (int s = 0; s < 9; ++s) {
      const u32x2 h0 = trd(ra + 2048 * s), h1 = trd(ra + 2048 * s + 256);
      bw[9 * h + s] = (u32x4){h0[0], h0[1], h1[0], h1[1]};
    }
#pragma unroll
    for (int s = 0; s < 9; ++s) asm volatile("" : "+v"(bw[9 * h + s]));
  };
  const uint32_t vearly = (uint32_t)((VEARLY >> (8 * (g0 >> 5))) & 0xffull);
#define TOUCH(pa) asm volatile("global_load_dword v126, %0, off" DPOL4 : : "v"(pa) : "memory", "v126")
  uint32_t wv[5], xv[9];
  auto rows_xw = [&](int lane) {
    u32x4 wa, xr, xs;
    const uint32_t xo = (uint32_t)((8 * g2 + 2 * wl) * (D_ * 2));
    asm volatile("global_load_dwordx4 %0, %5, %9\n\tglobal_load_dword %1, %6, %9 offset:1024\n\t"
                 "global_load_dwordx4 %2, %7, %10\n\tglobal_load_dwordx4 %3, %7, %10 offset:1152\n\tglobal_load_dword %4, %8, %10 offset:1024\n\ts_waitcnt vmcnt(0)"
                 : "=&v"(wa), "=&v"(wv[4]), "=&v"(xr), "=&v"(xs), "=&v"(xv[8])
                 : "v"(16u * lane), "v"(4u * (lane & 31)), "v"(xo + 16u * lane), "v"(xo + 4u * lane + 32u * (lane & 32)), "s"(wr), "s"(x) : "memory");
#pragma unroll
    for (int k = 0; k < 4; ++k) { wv[k] = wa[k]; xv[k] = xr[k]; xv[4 + k] = xs[k]; }
  };
  auto ln1 = [&](int g, int o, uint32_t c0) {
    uint32_t uo = u_at(g, 64 * wl + lane);
    uint32_t ta = lb0 + (uint32_t)(o + ((lane >> 2) & 3) * RP + (64 * wl + 16 * kq + 4 * (lane & 3)) * 2);
    uint32_t la = (uint32_t)(o + 2 * wl * RP + 16 * lane), lt = (uint32_t)(o + (2 * wl + (lane >> 5)) * RP + 1024 + 4 * (lane & 31));
    uint32_t sg = O_SU + (uint32_t)(256 * (lane >> 3) + g) * 128u + 16u * (lane & 7);
    asm volatile("" : "+v"(uo), "+v"(ta), "+v"(la), "+v"(lt), "+v"(sg));
    f32x4 z4 = {0.f, 0.f, 0.f, 0.f};
    asm volatile("" : "+v"(z4));
    asm volatile("s_waitcnt vmcnt(0)" : "={v[100:103]}"(xa), "={v[104:107]}"(xc), "={v108}"(r0), "={v109}"(ep));
    TS(0, 1);
    const uint32_t v[9] = {xa[0], xa[1], xa[2], xa[3], xc[0], xc[1], xc[2], xc[3], r0};
    uint32_t c[9];
    f32x2 tf[9];
    T = ((uint32_t)__builtin_amdgcn_readfirstlane(ep) + 1u) * 0x9E3779B1u;
    if (wl == 3) { const uint32_t now = (uint32_t)__builtin_amdgcn_s_memrealtime() + SD; ST16(sg, ((u32x4){T, now, T, now})); }
    const uint32_t tj = ((T >> (3 * wl)) & 0x00070007u) | (((T >> (12 + wl)) & 0x00010001u) << 14);
    uint32_t tw[3] = {(tj & 0xffffu) * 0x10001u, (tj >> 16) * 0x10001u, 0u};
    tw[2] = (lane < 32) ? tw[0] : tw[1];
    asm volatile("" : "+v"(tw[0]), "+v"(tw[1]), "+v"(tw[2]));
    f32x2 rc, nc;
    float e_;
    ln_stats<false>(v, lane, 0.03125f, tf, z4, rc, nc, e_);
    ln_apply<true>(tf, lane, rc, nc, c);
#pragma unroll
    for (int k = 0; k < 9; ++k) c[k] = (c[k] & ~UMASK) | tw[k >> 2];
    *(u32x4*)(smem + la) = (u32x4){c[0], c[1], c[2], c[3]};
    *(u32x4*)(smem + la + RP) = (u32x4){c[4], c[5], c[6], c[7]};
    *(uint32_t*)(smem + lt) = c[8];
    u32x4 ha, hb;
    {
      uint32_t t, cc, n = SPIN_CAP;
      asm volatile("s_mov_b64 exec, 1\n\tv_mov_b32 %2, 1\n\tds_add_u32 %5, %2\n\ts_mov_b64 exec, -1\n"
                   "1:\n\tds_read_b32 %2, %5\n\tds_read_b64_tr_b16 v[40:41], %6\n\tds_read_b64_tr_b16 v[42:43], %6 offset:%7\n\tds_read_b64_tr_b16 v[44:45], %6 offset:512\n\t"
                   "ds_read_b64_tr_b16 v[46:47], %6 offset:%8\n\ts_sub_u32 %4, %4, 1\n\ts_cbranch_scc1 2f\n\ts_waitcnt lgkmcnt(4)\n\tv_readfirstlane_b32 %3, %2\n\t"
                   "s_cmp_lt_u32 %3, 4\n\ts_cbranch_scc1 1b\n2:\n\ts_waitcnt lgkmcnt(0)"
                   : "=&{v[40:43]}"(ha), "=&{v[44:47]}"(hb), "=&v"(t), "=&s"(cc), "+s"(n) : "v"(c0), "v"(ta), "n"(4 * RP), "n"(512 + 4 * RP) : "memory", "scc");
    }
    ST16(uo, ha);
    ST16(uo + 64 * 128, hb);
    if (wl == 0) {
      const u32x2 h4 = trd(ta + 1024), h5 = trd(ta + 1024 + 4 * RP);
      ST16(uo + 128 * 128, ((u32x4){h4[0], h4[1], h5[0], h5[1]}));
    }
  };
  const int dX = 512 - 24 + 8 * bx + bj;
  if constexpr (ROLE == 2) {
    const int tl = tid - 256;
    if (isx) {
      __builtin_amdgcn_s_setprio(QPRIO0);
      *(u32x4*)(smem + X_TB + 16 * tl) = zero4;
      *(u32x4*)(smem + X_TB + 16 * min(256 + tl, (TB + C_U) / 16 - 1)) = zero4;
      const int pr = (wl + XROT) & 3;
      COps e;
      conv_ops(lb0 + X_TB, lb0 + X_UL, lb0 + X_IM, lane, e);
      TOps t;
      table_ops(lb0 + X_IM, lb0 + X_TB, wl & 1, lane, t);
      e.o = lb0 + X_ST + (uint32_t)(16 * ((2 * i + (kq >> 1)) ^ ((i >> 2) & 1)) + 8 * (kq & 1));
      e.f = ca + 28 - 32;
      asm volatile("" : "+v"(e.o), "+v"(e.f));
      float ldt, are, aim, bre, bim, cre, cim, dsk;
      P_WAIT(0, , "v103", "v104", "v105", "v106", "v107", "v109");
      asm volatile("" : "={v109}"(ep));
      T = ((uint32_t)__builtin_amdgcn_readfirstlane(ep) + 1u) * 0x9E3779B1u;
      P_MORE();
      asm volatile("v_lshlrev_b32 %0, 16, v107" : "=v"(dsk) : : "v107");
      TS(0, 1);
      __builtin_amdgcn_s_setprio(QPRIO);
      int* evp = (int*)(sel + 44);
      char* im = smem + X_IM + IP * (tl & 127);
      evp[wl] = (wl < 2) ? gen_y(ldt, are, aim, bre, bim, cre, cim, lb0 + X_IM + IP * tl, [] {} TSA) : gen_x(ldt, are, aim, im);
      TS(0, 2);
      __builtin_amdgcn_s_setprio(QPRIO2);
      gsync(ca + 8, 4);
      if (wl < 2) evp[-4 + wl] = (int)table_tile(t, wl & 1, lane, lb0 + LM + 4 * (128 + 44), dsk TSA);
      if (wl < 2) gsync0(ca + 8, 8); else { count_up(ca + 8); SLEEP(XSL); gwait(ca + 8, 8); }
      const uint32_t cs = (uint32_t)__builtin_amdgcn_readfirstlane(evp[-4]);
      if (wl == 3) *(u32x4*)(smem + X_IM + pad_at(lane & 7)) = zero4;
      TS(0, 3);
      CONV_RUN(2 + pr, e, , "+{v11}"(tid2));
      __builtin_amdgcn_s_setprio(3);
      {
        const int lane = tid2 & 63;
        const u32x4 v = *(const u32x4*)(smem + X_ST + 2048 * pr + 1024 * (lane >> 5) + 16 * ((lane & 31) ^ ((lane >> 3) & 1)));
        if (__builtin_expect(v_put(v, (u32x4){utag(T, 0), utag(T, 1), utag(T, 2), utag(T, 3)}, cs != S16, v_at(dX, 64 * pr + lane)), 0)) {
          DRAIN();
          if (lane == 0) ST4(O_DN + 16u * (4 * (dX - 256) + pr), T);
        }
        if (pr == 0 && lane == 0) asm volatile("global_store_dwordx2 %0, %1, %2 sc1" : : "v"(O_SC + 16u * dX), "v"((u32x2){T, cs}), "s"(ws) : "memory");
      }
      TS(0, 6);
    }
    if (!isx) __builtin_amdgcn_s_setprio(PPRIO);
    asm volatile("" : "+v"(tid2));
    if (!isx) rows_xw(tid2 & 63);
    if (!isx && !ffn) {
      SLEEP(RSLEEP);
#pragma unroll
      for (int k = 0; k < 7; ++k) ST16(O_SL + (uint32_t)((6 * bx + bj - 18) * 1792 + 256 * k - 256 + tid2) * 16u, ones);
      DRAIN();
    }
    if (!isx) BAR();
    const uint32_t tp = (uint32_t)__builtin_amdgcn_readfirstlane(cnt[4]);
    if (!isx) T = (uint32_t)__builtin_amdgcn_readfirstlane(sel[43]);
    if (!isx && w == 4) v_look(tp, tid2 & 63);
    v_time();
  } else {
    __builtin_amdgcn_s_setprio(3);
    if (tid < 16) cnt[tid] = 0u;
    BAR();
    if (tid < 20) ((uint32_t*)(hm + 320))[tid] = 0u;
    if (tid >= 184) *(u32x4*)(smem + L_SC - 16 * 184 + 16 * tid) = (u32x4){0x41804180u, 0x41804180u, 0x41804180u, 0x41804180u};
    uint32_t sink = 0;
    ln1(tg, 0, ca + 12);
    TS(0, 2);
    __builtin_amdgcn_s_setprio(NPRIO);
    asm volatile("s_waitcnt vmcnt(0)" : "+v"(sink) : : "memory");
    TS(0, 13);
    if (w == 0) {
      const uint32_t xr = O_XC + (uint32_t)((1021 - 4 * g0) * 72 + lane) * 16u;
      ST16(xr, ones);
      ST16(xr + 1024u, ones);
      ST16(xr + 2048u, ones);
      if (lane < 24) ST16(xr + 3072u, ones);
    }
    if (w == 2 && b < 72 && lane < 8) ST16(O_FB + (uint32_t)(8 * b + lane) * 16u, ones);
    if (isx) {
      rows_xw(lane);
      char* sp = smem + X_RW(w) + 16 * lane;
      *(u32x4*)sp = (u32x4){xv[0], xv[1], xv[2], xv[3]};
      *(u32x4*)(sp + 1024) = (u32x4){xv[4], xv[5], xv[6], xv[7]};
      *(u32x2*)(smem + X_RW(w) + 2048 + 8 * lane) = (u32x2){xv[8], wv[4]};
      if (w == 0) *(u32x4*)(smem + X_WR + 16 * lane) = (u32x4){wv[0], wv[1], wv[2], wv[3]};
    }
    SLEEP(ESLEEP);
    uint32_t tu = latest(gate(ws, O_SU + (uint32_t)(256 * bx + (ALLP ? tid : 64 * w + 4 * i + (bj & 3))) * 128u, T));
    asm volatile("s_mov_b64 exec, 1\n\tds_max_u32 %0, %1\n\ts_mov_b64 exec, -1" : : "v"(ca + 16), "v"(tu) : "memory");
    if (w == 0) sel[43] = T;
    if (isx) {
      const uint32_t xo = u_at(tid, dX);
      char* sm = smem + X_UL + C_U - 16 + 16 * tid;
      const u32x4 ut = {utag(T, 0), utag(T, 1), utag(T, 2), utag(T, 3)};
      __builtin_amdgcn_s_setprio(3);
      until(tu + UD + XD);
      u32x4 uw;
      asm volatile("global_load_dwordx4 %0, %1, %2 sc1" : "=&v"(uw) : "v"(xo), "s"(ws) : "memory");
      asm volatile("s_waitcnt vmcnt(0)" : "+v"(uw));
      uw ^= ut;
      u_again(uw, ut, xo);
      *(u32x4*)(sm + 16) = uw;
      BAR();
      if (w < 3) {
        const u32x4 pw = *(const u32x4*)sm;
        if (w < 2) sums<true>(smem + X_IM + 16 * tid, pw, uw); else sums<false>(smem + X_IM + 16 * tid, pw, uw);
        count_up(ca + 28);
      }
    } else {
      until(tu + UD + WD);
      BAR();
    }
    tu = (uint32_t)__builtin_amdgcn_readfirstlane(cnt[4]);
    TS(0, 3);
    __builtin_amdgcn_s_setprio(DPRIO);
    if (ffn) {
      ts1 = (tu + UD + VD2 - TSTOP) | 1u;
      gu_dma(0, N0, IC<40>{});
      gu_dma(N0, NA, IC<WDEPTH>{});
      asm volatile("s_waitcnt vmcnt(%0)" : : "n"(NA - 9) : "memory");
      gsync0(ca + 4, 4);
      gu_read(0);
      asm volatile("s_waitcnt lgkmcnt(0)" ::: "memory");
      count_up(ca + 36);
      TS(0, 4);
      gu_dma(NA, 18, IC<WDEPTH2>{});
      gwait(ca + 36, 4);
      down_dma(0, KN, lane);
      asm volatile("s_waitcnt vmcnt(%0)" : : "n"(3 * KN) : "memory");
      count_up(ca + 4);
      if (STP && w == 1)
        asm volatile("global_load_dword v126, %0, %1 sc1" : : "v"(O_SC + 1024u * (fb >> 4) + 128u * (lane & 7)), "s"(ws) : "memory", "v126");
      TS(0, 7);
    } else {
      if (!isx && C3) {
        const int n = 6 * min(tid, 191) + bj - 18, r = n >> 2;
        const char* wb = (const char*)((n & 1) ? wu : wg) + bx * 1152;
        const int c0 = (3 - (int)((uintptr_t)wb >> 7)) & 7;
        wb += (uint32_t)((r + 144 * ((r >= 144) + ((n >> 1) & 1))) * (F_ * 2) + 128 * c0);
        TOUCH(wb);
        if (c0 == 0) TOUCH(wb + 1024);
      }
      auto d_line = [&](int k) {
        const int n = min((k * 14 + bj - 18) * 64 + lane, 5183), q = n / 1296, r = n - 1296 * q;
        return (const char*)wd + (uint32_t)(bx * 5184 + (r / 18) * 72 + 18 * (HREV ? 3 - q : q) + r % 18) * 128u;
      };
      if (isx && w == 3) v_look(tu, lane);
      int k = w;
      if (w < HW) {
#pragma unroll 1
        for (; k < 6 && (int)((uint32_t)__builtin_amdgcn_s_memrealtime() - (tu + UD + VD2 - HSTOP)) < 0; k += HW) { TOUCH(d_line(k)); DRAIN(); }
      }
      TS(0, 4);
      v_time();
      until(tv + RD2);
      if (w < HW) {
#pragma unroll 1
        for (; k < 6; k += HW) { TOUCH(d_line(k)); if (!HALL) DRAIN(); }
      }
      TS(0, 7);
    }
    v_time();
    tk = tv + KD;
  }
  {
  const int tid = tid2, lane = tid & 63, i = lane & 15, kq = lane >> 4;
  __builtin_amdgcn_s_setprio(3);
  const int PL = ffn ? 6400 : LW + 6656;
  f32x2 xf[9];
  f32x4 za = {0.f, 0.f, 0.f, 0.f}, zb = za;
  uint32_t al, ha, h0a, h1a, so, da;
  auto ffn_terms = [&] {
    al = O_XC + 64u * (i >> 3) + 16u * kq + 128u * w;
    ha = (ROLE == 1) ? (uint32_t)((4 * (kq & 1) + 2 * (kq >> 1) + (i >> 3)) * 80 + 16 * w + 2 * (i & 7)) : 0u;
    h0a = (uint32_t)(min(i, 8) * 80 + 16 * kq); h1a = (uint32_t)(min(i ^ 8, 8) * 80 + 16 * kq);
    so = O_SL + (uint32_t)fb * SP + ((ROLE == 2) ? 16u * tid - 4096u : 8192u + 16u * lane);
    da = lb0 + (uint32_t)((8 * kq + ((lane >> 2) & 3)) * 1152 + 32 * ((8 * kq + ((lane >> 2) & 3) + 2) >> 2) + 8 * (lane & 3) + ((ROLE == 2) ? 128 * wl : 1024));
    if (ROLE == 2) asm volatile("" : "+v"(h0a), "+v"(h1a), "+v"(so), "+v"(da)); else asm volatile("" : "+v"(ha), "+v"(al), "+v"(da));
  };
  if (ROLE == 1) ffn_terms();
  uint32_t hv[9] = {}, nv[9] = {}, keya = 0u, keyb = 0u;
  int rka = 0, rkb = 0;
  auto put_row = [&](const uint32_t* r, int j, const void* base, uint32_t ro) {
    const u32x4 tl = {r[8], (uint32_t)DPP_I((int)r[8], 0x55, 0xf), (uint32_t)DPP_I((int)r[8], 0xaa, 0xf), (uint32_t)DPP_I((int)r[8], 0xff, 0xf)};
    asm volatile("global_store_dwordx4 %0, %1, %2 sc1\n\ts_nop 1" : : "v"(ro + 16u * lane), "v"((u32x4){r[4 * j], r[4 * j + 1], r[4 * j + 2], r[4 * j + 3]}), "s"(base) : "memory");
    if ((lane & 35) == 32 * j) asm volatile("global_store_dwordx4 %0, %1, %2 offset:1024 sc1\n\ts_nop 1" : : "v"(ro + 4u * (lane & 31)), "v"(tl), "s"(base) : "memory");
  };
  if (ln2) {
    const int tl = tid - 256, d1 = 64 * (tl >> 5) + (tl & 31), d3 = 64 * ((tl >> 4) & 7) + 32 + (tl & 15), d2 = (tl < 128) ? d3 + 16 : min(384 + tl, 575);
    uint32_t vo = v_at(d1, g0), vo2 = v_at(d2, g0), vo3 = v_at(d3, g0);
    u32x4 ut = {utag(T, 0), utag(T, 1), utag(T, 2), utag(T, 3)};
    u32x4 va, vc = ut, vb = ut;
    asm volatile("" : "+v"(vo), "+v"(vo2), "+v"(vo3), "+v"(ut), "+v"(vc), "+v"(vb));
    TS(0, 7);
    until(tv);
    TS(0, 8);
    asm volatile("global_load_dwordx4 %0, %2, %4 sc1\n\ts_cmp_lt_u32 %5, 2\n\ts_cbranch_scc1 1f\n\tglobal_load_dwordx4 %1, %3, %4 sc1\n1:"
                 : "=&v"(va), "+v"(vc) : "v"(vo), "v"(vo3), "s"(ws), "s"(wl) : "memory", "scc");
    until(tv + V2D);
    asm volatile("s_cmp_eq_u32 %3, 3\n\ts_cbranch_scc1 1f\n\tglobal_load_dwordx4 %0, %1, %2 sc1\n1:" : "+v"(vb) : "v"(vo2), "s"(ws), "s"(wl) : "memory", "scc");
    __builtin_amdgcn_sched_barrier(0);
    if (isx) {
      const u32x4 ra = *(const u32x4*)(smem + X_RW(wl) + 16 * lane), rb = *(const u32x4*)(smem + X_RW(wl) + 1024 + 16 * lane), rw = *(const u32x4*)(smem + X_WR + 16 * lane);
      const u32x2 rc = *(const u32x2*)(smem + X_RW(wl) + 2048 + 8 * lane);
#pragma unroll
      for (int k = 0; k < 4; ++k) { xv[k] = ra[k]; xv[4 + k] = rb[k]; wv[k] = rw[k]; }
      xv[8] = rc[0]; wv[4] = rc[1];
    }
    uint32_t pa = (uint32_t)(PL + 9216 * wl + 32 * lane), pb = (uint32_t)(PL + 9216 * wl + 2048 + 8 * (lane & 31)), ps = (lane < 32) ? 0u : 16u;
    uint32_t kid = (uint32_t)(((255 - g2) << 5) | (7 - 2 * wl - (lane >> 5)));
    uint32_t px = (uint32_t)(PL + 4 * d1), py = (uint32_t)(PL + 4 * d2), pz = (uint32_t)(PL + 4 * d3);
#pragma unroll
    for (int k = 0; k < 9; ++k) xf[k] = (f32x2){bflo(xv[k]), bfhi(xv[k])};
    asm volatile("" : "+v"(xf[0]), "+v"(xf[1]), "+v"(xf[2]), "+v"(xf[3]), "+v"(xf[4]), "+v"(xf[5]), "+v"(xf[6]), "+v"(xf[7]), "+v"(xf[8]));
    asm volatile("" : "+v"(za), "+v"(zb), "+v"(pa), "+v"(pb), "+v"(ps), "+v"(kid), "+v"(px), "+v"(py), "+v"(pz));
    float sv[10], ws_ = dot2(wv[4], 0x3f803f80u, 0.f) * 0.5f;
#pragma unroll
    for (int k = 0; k < 4; ++k) ws_ = dot2(wv[k], 0x3f803f80u, ws_);
    ws_ = wave_sum(ws_);
#pragma unroll
    for (int k = 0; k < 10; ++k) sv[k] = 16.f;
    uint32_t ka_ = lb0 + LM + 4u * (128 + 48 + 2 * wl + (lane >> 5)), kb_ = lb0 + LM + 4u * (128 + 48);
    uint32_t ko_ = O_KEY + (uint32_t)(256 * (lane & 7) + 8 * (g2 & 31) + (g2 >> 5)) * 16u;
    asm volatile("" : "+v"(ws_), "+v"(sv[0]), "+v"(sv[1]), "+v"(sv[2]), "+v"(sv[3]), "+v"(sv[4]), "+v"(sv[5]), "+v"(sv[6]), "+v"(sv[7]), "+v"(sv[8]), "+v"(sv[9]),
                 "+v"(ka_), "+v"(kb_), "+v"(ko_));
    if (ffn) gwait(ca + 36, 4);
    asm volatile("s_waitcnt vmcnt(1)\n\ts_cmp_eq_u32 %2, 3\n\ts_cbranch_scc0 1f\n\ts_waitcnt vmcnt(0)\n1:" : "+v"(va), "+v"(vc) : "s"(wl) : "memory", "scc");
    auto turn1 = [&] {
#pragma unroll
      for (int e = 0; e < 4; ++e) *(uint32_t*)(smem + px + 9216 * e) = va[e];
      if (wl >= 2) {
#pragma unroll
        for (int e = 0; e < 4; ++e) *(uint32_t*)(smem + pz + 9216 * e) = vc[e];
      }
    };
    auto turn = [&] {
      if (wl < 3) {
#pragma unroll
        for (int e = 0; e < 4; ++e) *(uint32_t*)(smem + py + 9216 * e) = vb[e];
      }
    };
    va ^= ut; vc ^= ut;
    turn1();
    asm volatile("s_waitcnt vmcnt(0)" : "+v"(vb) : : "memory");
    vb ^= ut;
    turn();
    const u32x4 vs = va | vb | vc;
    if (__builtin_expect(__any(((vs[0] | vs[1] | vs[2] | vs[3]) & UMASK) != 0u), 0)) {
#pragma unroll 1
      for (int spin = 0; spin < 4; ++spin) {
        const bool na = ((va[0] | va[1] | va[2] | va[3]) & UMASK) != 0u, nb = ((vb[0] | vb[1] | vb[2] | vb[3]) & UMASK) != 0u, nc = ((vc[0] | vc[1] | vc[2] | vc[3]) & UMASK) != 0u;
        if (!__any(na || nb || nc)) break;
        TSC(16);
        if (na) LD16(va, vo);
        if (nb) LD16(vb, vo2);
        if (nc) LD16(vc, vo3);
        asm volatile("s_waitcnt vmcnt(0)" : "+v"(va), "+v"(vb), "+v"(vc) : : "memory");
        if (na) va ^= ut;
        if (nb) vb ^= ut;
        if (nc) vc ^= ut;
      }
      auto again = [&](u32x4& v, uint32_t o, int d) {
        const int J = g0 >> 5;
        const uint32_t dn = O_DN + 16u * (uint32_t)((d < 512) ? 4 * (d >> 1) + ((0x01013232 >> (4 * J)) & 3) : 4 * (d - 256) + (J >> 1));
        bool pend = ((v[0] | v[1] | v[2] | v[3]) & UMASK) != 0u;
#pragma unroll 1
        for (int spin = 0; spin < SPIN_CAP && __any(pend); ++spin) {
          TSC(16);
          SLEEP(PSLEEP);
          if (pend) {
            uint32_t fin = ~T;
            if ((v[0] & v[1] & v[2] & v[3] & LMASK) == LMASK)
              asm volatile("global_load_dword %0, %1, %2 sc1\n\ts_waitcnt vmcnt(0)" : "=&v"(fin) : "v"(dn), "s"(ws) : "memory");
            LD16W(v, o);
            v ^= ut;
            pend = ((v[0] | v[1] | v[2] | v[3]) & UMASK) != 0u;
            if (pend && fin == T) {
              u32x2 sc;
              for (int k = 0; k < SPIN_CAP; ++k) {
                asm volatile("global_load_dwordx2 %0, %1, %2 sc1\n\ts_waitcnt vmcnt(0)" : "=&v"(sc) : "v"(O_SC + 16u * d), "s"(ws) : "memory");
                if (sc[0] == T) break;
              }
              *(u16*)(smem + L_SC + 2 * d) = (u16)(sc[1] >> 16);
              cnt[10] = 1u;
              v = (v ^ ut) & ~LMASK;
              pend = false;
            }
          }
        }
      };
      again(va, vo, d1); again(vc, vo3, d3); again(vb, vo2, d2);
      turn1();
      turn();
    }
    u32x4 a0, a1;
    u32x2 a2, cf;
    {
      uint32_t cc, n = SPIN_CAP;
      asm volatile("s_mov_b64 exec, 1\n\tv_mov_b32 v90, 1\n\tds_add_u32 %6, v90\n\ts_mov_b64 exec, -1\n"
                   "1:\n\tds_read2_b32 %3, %6 offset1:10\n\tds_read_b128 %0, %7\n\tds_read_b128 %1, %7 offset:16\n\tds_read_b64 %2, %8\n\t"
                   "s_sub_u32 %5, %5, 1\n\ts_cbranch_scc1 2f\n\ts_waitcnt lgkmcnt(3)\n\tv_readfirstlane_b32 %4, v90\n\t"
                   "s_cmp_lt_u32 %4, 4\n\ts_cbranch_scc1 1b\n2:\n\ts_waitcnt lgkmcnt(0)"
                   : "=&v"(a0), "=&v"(a1), "=&v"(a2), "=&{v[90:91]}"(cf), "=&s"(cc), "+s"(n) : "v"(ca), "v"(lb0 + pa), "v"(lb0 + pb) : "memory", "scc");
    }
    TS(0, 9);
    if (__builtin_expect(__builtin_amdgcn_readfirstlane(cf[1]) != 0, 0)) {
      const u32x4 q = *(const u32x4*)(smem + L_SC + 16 * lane);
      const uint32_t r = *(const uint32_t*)(smem + L_SC + 1024 + 4 * (lane & 31));
#pragma unroll
      for (int k = 0; k < 4; ++k) { sv[2 * k] = bflo(q[k]); sv[2 * k + 1] = bfhi(q[k]); }
      sv[8] = bflo(r); sv[9] = bfhi(r);
    }
    const uint32_t av[10] = {a0[0], a0[1], a0[2], a0[3], a1[0], a1[1], a1[2], a1[3], a2[0] >> ps, a2[1] >> ps};
    f32x2 h[9];
#pragma unroll
    for (int k = 0; k < 10; ++k) {
      asm("v_fma_mix_f32 %0, %1, %2, %3 op_sel_hi:[1,0,0]" : "=v"(h[(k < 8) ? k >> 1 : 8][k & 1]) : "v"(av[k]), "v"(sv[k]), "v"(xf[(k < 8) ? k >> 1 : 8][k & 1]));
      if (k < 8) asm("v_fma_mix_f32 %0, %1, %2, %3 op_sel:[1,0,0] op_sel_hi:[1,0,0]" : "=v"(h[4 + (k >> 1)][k & 1]) : "v"(av[k]), "v"(sv[k]), "v"(xf[4 + (k >> 1)][k & 1]));
    }
#pragma unroll
    for (int k = 0; k < 9; ++k) hv[k] = pack2(h[k][0], h[k][1]);
    const float da = dot2(hv[2], wv[2], dot2(hv[0], wv[0], zb[0])) + dot2(hv[3], wv[3], dot2(hv[1], wv[1], zb[1]));
    const float db = dot2(hv[6], wv[2], dot2(hv[4], wv[0], zb[2])) + dot2(hv[7], wv[3], dot2(hv[5], wv[1], zb[3]));
    float dt = dot2(hv[8], wv[4], fold32(da, db));
    f32x2 rc, nc;
    ln_stats<true>(hv, lane, 1.f, xf, za, rc, nc, dt);
    dt = (lane < 32) ? dt * rc[0] + ws_ * nc[0] : dt * rc[1] + ws_ * nc[1];
    const uint32_t bb = pack2(dt, dt) & 0xffff0000u;
    const uint32_t kk = (bb ^ (((uint32_t)((int)bb >> 31) & 0x7fff0000u) | 0x80000000u)) | kid;
    u32x4 ka, kb;
    uint32_t old;
    asm volatile("s_mov_b64 exec, %5\n\tds_write_b32 %3, %4\n\ts_mov_b64 exec, 1\n\tv_mov_b32 %2, 1\n\tds_add_rtn_u32 %2, %7, %2\n\ts_mov_b64 exec, -1\n\t"
                 "ds_read_b128 %0, %6\n\tds_read_b128 %1, %6 offset:16\n\ts_waitcnt lgkmcnt(0)"
                 : "=&v"(ka), "=&v"(kb), "=&v"(old) : "v"(ka_), "v"(kk), "s"(0x8000000080000000ull), "v"(kb_), "v"(ca) : "memory");
    if (__builtin_amdgcn_readfirstlane(old) == 7) {
      uint32_t m1 = max(max(ka[0], ka[1]), ka[2]), m2 = med3u(ka[0], ka[1], ka[2]), m3 = min(min(ka[0], ka[1]), ka[2]);
#pragma unroll
      for (int e = 0; e < 5; ++e) {
        const uint32_t kx = e ? kb[e - 1] : ka[3];
        m3 = med3u(m2, m3, kx); m2 = med3u(m1, m2, kx); m1 = max(m1, kx);
      }
      if (lane < 8) ST16(ko_, ((u32x4){m1 | 24u, m2 | 16u, m3 | 8u, T & ~1u}));
    }
    TS(0, 10);
    keya = (uint32_t)__builtin_amdgcn_readlane(kk, 31); keyb = (uint32_t)__builtin_amdgcn_readlane(kk, 63);
    ln_apply<false>(xf, lane, rc, nc, nv);
    gwait(ca, 8);
    if (wl == 1) {
      const u32x4 kv = *(const u32x4*)&kl[4 * (lane & 1)];
      if (lane < 2) ST16(O_KF + (uint32_t)(2 * g2 + lane) * 16u, ((u32x4){__builtin_amdgcn_perm(kv[1], kv[0], 0x07060302u), T, __builtin_amdgcn_perm(kv[3], kv[2], 0x07060302u), T}));
      if (ROLE == 2 && b == 0 && lane == 0) ST4(O_EP, T * 0x0E8B2F51u);
    }
    const uint32_t ko = kl[lane & 7];
    rka = FORCEFB + __popcll(__ballot(ko > keya) & 0xffull); rkb = FORCEFB + __popcll(__ballot(ko > keyb) & 0xffull);
#pragma unroll
    for (int k = 0; k < 9; ++k) {
      const f32x2 o = xf[k] + (f32x2){bflo(nv[k]), bfhi(nv[k])};
      hv[k] = pack2(o[0], o[1]);
      nv[k] = min(nv[k], 0xfffffffeu);
    }
    if (rka < 3) put_row(nv, 0, ws, O_XC + (uint32_t)(1023 - 4 * g2 - rka) * 1152u);
    if (rkb < 3) put_row(nv, 1, ws, O_XC + (uint32_t)(1023 - 4 * g2 - rkb) * 1152u);
    if (!LPOS && ffn) down_dma(KN, 4, lane);
    ffn_terms();
  }
  uint32_t k3rd = 0u, k4th = 0u;
  uint32_t q0 = lb0 + LM + 4u * lane, q1 = lb0 + LM + 32u * w, q2 = q1 + 4u * (lane & 7), q3 = lb0 + LM + 512u + 4u * (lane & 7), q4 = lb0 + LM + (uint32_t)(tid & ~7);
  uint32_t r8 = C_, sb = lb0 + LM + 512u;
  asm volatile("" : "+v"(q0), "+v"(q1), "+v"(q2), "+v"(q3), "+v"(q4), "+v"(r8), "+v"(sb));
  if constexpr (ROLE == 1) {
    u32x4 key;
    const uint32_t kof = O_KEY + (uint32_t)(256 * bx + tid) * 16u;
    until(tk);
    if (!ffn) SLEEP(NSLEEP);
    LD16(key, kof);
    if (ffn) {
      gwait0(ca + 4, 8);
      gu_read(1);
    }
    asm volatile("s_waitcnt vmcnt(0)" : "+v"(key) : : "memory");
    if (__builtin_expect(__any(key[3] != (T & ~1u)), 0)) {
#pragma unroll 1
      for (int spin = 0; spin < SPIN_CAP; ++spin) {
        const bool need = key[3] != (T & ~1u);
        if (!__any(need)) break;
        TSC(15);
        SLEEP(KBACK);
        if (need) LD16W(key, kof);
      }
    }
    TS(0, 11);
    const uint32_t m1 = oct_maxu(key[0]);
    const bool hit = key[0] == m1;
    const uint32_t k0 = hit ? key[1] : key[0], k1 = hit ? key[2] : key[1];
    const uint32_t m2 = oct_maxu(k0);
    *LP(u32x2, q4) = (u32x2){m1, m2};
    k3rd = (k0 == m2) ? k1 : k0;
    k4th = (k0 == m2) ? (hit ? 0u : key[2]) : k1;
    if (RPF == 1 && !ffn && w == (bj & 3)) {
      const uint32_t r1 = O_XC + ((m1 >> 3) & 0x3ffu) * 1152u + 128u * (lane & 7), r2 = O_XC + ((m2 >> 3) & 0x3ffu) * 1152u + 128u * (lane & 7);
      asm volatile("global_load_dword v126, %0, %3 sc1\n\tglobal_load_dword v126, %1, %3 sc1\n\tglobal_load_dword v126, %2, %3 offset:1024 sc1"
                   : : "v"(r1), "v"(r2), "v"(((lane & 1) ? r1 : r2) - 128u * (lane & 7)), "s"(ws) : "memory", "v126");
    }
  }
  auto pick = [&](auto co, auto so_) {
    BAR();
    const uint32_t c = *LP(uint32_t, q0 + co.value), own = *LP(uint32_t, q2 + co.value);
    const u32x4 la = *LP(u32x4, q1 + co.value), lb = *LP(u32x4, q1 + co.value + 16);
    uint32_t r = r8;
    asm("v_cmp_gt_u32 s[40:41], %1, %2\n\tv_cmp_gt_u32 s[42:43], %1, %3\n\tv_cmp_gt_u32 s[44:45], %1, %4\n\tv_cmp_gt_u32 s[46:47], %1, %5\n\t"
        "v_cmp_gt_u32 s[48:49], %1, %6\n\tv_cmp_gt_u32 s[50:51], %1, %7\n\tv_cmp_gt_u32 s[52:53], %1, %8\n\tv_cmp_gt_u32 s[54:55], %1, %9\n\t"
        "s_bcnt1_i32_b64 s40, s[40:41]\n\ts_bcnt1_i32_b64 s42, s[42:43]\n\tv_writelane_b32 %0, s40, 0\n\ts_bcnt1_i32_b64 s44, s[44:45]\n\t"
        "v_writelane_b32 %0, s42, 1\n\ts_bcnt1_i32_b64 s46, s[46:47]\n\tv_writelane_b32 %0, s44, 2\n\ts_bcnt1_i32_b64 s48, s[48:49]\n\t"
        "v_writelane_b32 %0, s46, 3\n\ts_bcnt1_i32_b64 s50, s[50:51]\n\tv_writelane_b32 %0, s48, 4\n\ts_bcnt1_i32_b64 s52, s[52:53]\n\t"
        "v_writelane_b32 %0, s50, 5\n\ts_bcnt1_i32_b64 s54, s[54:55]\n\tv_writelane_b32 %0, s52, 6\n\tv_writelane_b32 %0, s54, 7"
        : "+v"(r) : "v"(c), "v"(la[0]), "v"(la[1]), "v"(la[2]), "v"(la[3]), "v"(lb[0]), "v"(lb[1]), "v"(lb[2]), "v"(lb[3])
        : "scc", "s40", "s41", "s42", "s43", "s44", "s45", "s46", "s47", "s48", "s49", "s50", "s51", "s52", "s53", "s54", "s55");
    *LP(uint32_t, sb + 4u * min(r, (uint32_t)C_) + so_.value) = own;
    BAR();
  };
  auto rows = [&](uint32_t ao, auto&& meanwhile) {
    u32x4 aa, ab, ac = __builtin_bit_cast(u32x4, zb);
    asm volatile("global_load_dwordx4 %0, %3, %4 sc1\n\tglobal_load_dwordx4 %1, %3, %4 offset:512 sc1\n\t"
                 "s_cmp_lg_u32 %5, 0\n\ts_cbranch_scc1 1f\n\tglobal_load_dwordx4 %2, %3, %4 offset:1024 sc1\n1:"
                 : "=&v"(aa), "=&v"(ab), "+v"(ac) : "v"(ao), "s"(ws), "s"(w) : "memory", "scc");
    __builtin_amdgcn_sched_barrier(0);
    meanwhile();
    asm volatile("s_waitcnt vmcnt(0)" : "+v"(aa), "+v"(ab), "+v"(ac) : : "memory");
    if (__builtin_expect(__any(max(max(max4(aa), max4(ab)), max4(ac)) == 0xffffffffu), 0)) {
#pragma unroll 1
      for (int spin = 0; spin < SPIN_CAP; ++spin) {
        const bool na = max4(aa) == 0xffffffffu, nb = max4(ab) == 0xffffffffu, nc = max4(ac) == 0xffffffffu;
        if (!__any(na || nb || nc)) break;
        TSC(14);
        SLEEP(PSLEEP);
        if (na) LD16W(aa, ao);
        if (nb) LD16W(ab, ao + 512u);
        if (nc) LD16W(ac, ao + 1024u);
      }
    }
    *(u32x4*)(smem + LA + 16 * tid) = aa;
    *(u32x4*)(smem + LA + 4096 + 16 * tid) = ab;
    if (w == 0) *(u32x4*)(smem + LA + 8192 + 16 * tid) = ac;
  };
  auto rows_dma = [&](uint32_t ao) {
    uint32_t dl = lb0 + LA + 1024u * w, keep;
    asm volatile("s_mov_b32 %0, m0\n\ts_mov_b32 m0, %1\n\ts_add_u32 %1, %1, 4096 - 512\n\tglobal_load_lds_dwordx4 %2, %3 sc1\n\ts_mov_b32 m0, %1\n\ts_add_u32 %1, %1, 4096 - 512\n\t"
                 "global_load_lds_dwordx4 %2, %3 offset:512 sc1\n\ts_cmp_lg_u32 %4, 0\n\ts_cbranch_scc1 1f\n\ts_mov_b32 m0, %1\n\ts_nop 0\n\t"
                 "global_load_lds_dwordx4 %2, %3 offset:1024 sc1\n1:\n\ts_mov_b32 m0, %0" : "=&s"(keep), "+s"(dl) : "v"(ao), "s"(ws), "s"(w) : "memory", "scc");
  };
  auto get_bd = [&](int t0, int t1) {
#pragma unroll
    for (int t = t0; t < t1; ++t) {
      const u32x2 h0 = trd(da + 512 * (t >> 2) + 32 * (t & 3)), h1 = trd(da + 512 * (t >> 2) + 32 * (t & 3) + 4608 + 32);
      bw[t] = (u32x4){h0[0], h0[1], h1[0], h1[1]};
    }
#pragma unroll
    for (int t = t0; t < t1; ++t) asm volatile("" : "+v"(bw[t]));
  };
  pick(IC<0>{}, IC<0>{});
  uint32_t mykey = *LP(uint32_t, q3), ro = ((mykey >> 3) & 0x3ffu) * 1152u;
  TS(0, 12);
  auto check = [&] {
    cnt[(k3rd > (uint32_t)__builtin_amdgcn_readlane(mykey, 7)) ? 5 : 6] = 1u;
    const uint32_t m3 = oct_maxu(k3rd);
    if (k3rd == m3) k3rd = k4th;
    if ((lane & 7) == 0) cand[72 + (tid >> 3)] = m3;
    if (w == 0 && lane < 32) cand[(lane < 8) ? 64 + lane : 96 + lane] = (lane < 8) ? mykey : 0u;
  };
  if constexpr (ROLE == 1) {
    if (ffn && !FORCEFB) rows_dma(ro + al);
    check();
    DRAIN();
  } else if (ffn) {
    if (LPOS) { SLEEP(LSLEEP); down_dma(KN, 4, lane); } else { DRAIN(); gsync0(ca, 12); get_bd(0, 8); }
  }
  BAR();
  TS(0, 5);
  auto put_out = [&](int j, uint32_t k0, int rk) {
    const uint64_t mine = __ballot(((mykey ^ k0) & ~24u) == 0u) & 0xffull;
    if (mine == 0ull) put_row(hv, j, out, (uint32_t)((8 * g2 + 2 * wl + j) * (D_ * 2)));
    else if (rk >= 3) put_row(nv, j, ws, O_FB + 1152u * (uint32_t)__builtin_ctzll(mine));
  };
  auto warm_out = [&] {
    const uint32_t k0 = (lane < 9) ? keya : keyb;
    bool hit = false;
#pragma unroll
    for (int e = 0; e < 8; ++e) hit |= (((uint32_t)__builtin_amdgcn_readlane(mykey, e) ^ k0) & ~24u) == 0u;
    if (OTP && lane < 18 && hit) asm volatile("global_load_dword v126, %0, %1" : : "v"((uint32_t)((8 * g2 + 2 * wl) * (D_ * 2) + 128 * lane)), "s"(out) : "memory", "v126");
  };
  auto rest_out = [&] { put_out(0, keya, rka); put_out(1, keyb, rkb); };
  f32x4 lo, hi;
  u32x4 av[5];
  const uint32_t aa = lb0 + LA + 16u * lane;
  auto gu_ask = [&] {
#pragma unroll
    for (int j = 0; j < 5; ++j) asm volatile("ds_read_b128 %0, %1 offset:%2" : "=v"(av[j]) : "v"(aa), "n"(1024 * j) : "memory");
  };
  auto gate_up = [&] {
    lo = (f32x4){0.f, 0.f, 0.f, 0.f}; hi = lo;
#pragma unroll
    for (int j = 0; j < 9; ++j) {
      if (j < 5) asm volatile("s_waitcnt lgkmcnt(4)" : "+v"(av[j % 5]));
      else if (j == 5) asm volatile("s_waitcnt lgkmcnt(2)" : "+v"(av[0]), "+v"(av[1]));
      else if (j == 7) asm volatile("s_waitcnt lgkmcnt(0)" : "+v"(av[2]), "+v"(av[3]));
      lo = MMB(av[j % 5], bw[2 * j], lo);
      hi = MMB(av[j % 5], bw[2 * j + 1], hi);
      __builtin_amdgcn_sched_barrier(0);
      if (j < 4) asm volatile("ds_read_b128 %0, %1 offset:%2" : "=v"(av[j]) : "v"(aa), "n"(1024 * (j + 5)) : "memory");
    }
  };
  uint32_t fl;
  asm volatile("ds_read_b32 %0, %1" : "=v"(fl) : "v"(ca + 20) : "memory");
  if (FFN == 1) gu_ask();
  asm volatile("s_waitcnt lgkmcnt(%1)" : "+v"(fl) : "n"(FFN == 1 ? 5 : 0));
  bool slow = false;
  if (__builtin_expect(FORCESLOW || FORCEFB || __builtin_amdgcn_readfirstlane(fl) != 0, 0)) {
    TSC(17);
    if (FFN == 1) asm volatile("s_waitcnt lgkmcnt(0)" : "+v"(av[0]), "+v"(av[1]), "+v"(av[2]), "+v"(av[3]), "+v"(av[4]));
    pick(IC<256>{}, IC<64>{});
    mykey = sel[16 + (lane & 7)];
    ro = ((mykey >> 3) & 0x3ffu) * 1152u;
    if (FFN == 1 && !FORCEFB) rows_dma(ro + al);
    const uint32_t k8 = (uint32_t)__builtin_amdgcn_readlane(mykey, 7);
    if (ROLE == 1 && k3rd > k8) cnt[11] = 1u;
    if (w == 0 && __any((mykey & 24u) == 8u)) {
      const uint32_t pk = sel[16 + (lane >> 3)], gi = (pk >> 5) & 0xffu;
      u32x4 kf;
#pragma unroll 1
      for (int spin = 0; spin < SPIN_CAP; ++spin) {
        LD16W(kf, O_KF + (2u * (255u - gi) + ((lane >> 2) & 1u)) * 16u);
        if (!__any(kf[1] != T || kf[3] != T)) break;
        SLEEP(KBACK);
      }
      const uint32_t wd_ = (lane & 2) ? kf[2] : kf[0], kt = ((lane & 1) ? wd_ & 0xffff0000u : wd_ << 16) | (gi << 5) | (7u - (lane & 7));
      const uint64_t up = __ballot((pk & 24u) == 8u && kt >= (k8 & ~24u));
      if (__popcll((up >> (lane & 56)) & 0xffull) > 3) cnt[11] = 1u;
    }
    if (ROLE == 1) DRAIN();
    BAR();
    if (FORCESLOW > 1 || FORCEFB || __builtin_amdgcn_readfirstlane(cnt[11]) != 0) {
    slow = true;
    u32x4 key;
#pragma unroll 1
    for (int spin = 0; spin < SPIN_CAP; ++spin) {
      LD16W(key, O_KF + 16u * tid);
      if (!__any(key[1] != T || key[3] != T)) break;
      SLEEP(KBACK);
    }
    const uint32_t ib = (uint32_t)(((255 - (tid >> 1)) << 5) | (7 - 4 * (tid & 1)));
    uint32_t k0 = (key[0] << 16) | ib, k1 = (key[0] & 0xffff0000u) | (ib - 1), k2 = (key[2] << 16) | (ib - 2), k3 = (key[2] & 0xffff0000u) | (ib - 3);
    CEX(k0, k1) CEX(k2, k3) CEX(k0, k2) CEX(k1, k3) CEX(k1, k2)
#pragma unroll 1
    for (int c = 0; c < C_; ++c) {
      const uint32_t mx = wave_maxu(k0);
      const bool hit = (k0 == mx);
      k0 = hit ? k1 : k0; k1 = hit ? k2 : k1; k2 = hit ? k3 : k2; k3 = hit ? 0u : k3;
      if (lane == 0) cand[64 + 8 * w + c] = mx;
    }
    pick(IC<256>{}, IC<128>{});
    mykey = sel[32 + (lane & 7)];
    const uint32_t okey = sel[32 + (lane >> 3)];
    const uint64_t same = __ballot(((mykey ^ okey) & 0x1fe0u) == 0u && okey > mykey);
    const int sl = FORCEFB + __popcll((same >> (lane & 7)) & 0x0101010101010101ull);
    ro = (sl < 3) ? (((mykey >> 3) & 0x3ffu) + 3 - sl) * 1152u : O_FB - O_XC + 1152u * (lane & 7);
    if (ln2) rest_out();
    if (ffn) {
      if (ROLE == 1) rows(ro + al, [] {});
      BAR();
    }
    }
    if (FFN == 1) {
      gu_ask();
      asm volatile("s_waitcnt lgkmcnt(0)" : "+v"(av[0]), "+v"(av[1]), "+v"(av[2]), "+v"(av[3]), "+v"(av[4]));
    }
  }
  if (FFN == 1) gate_up();
  if (OPOS == 0 && ROLE == 2 && ffn && !slow) { SLEEP(OD); rest_out(); }
  if (!ffn) {
    if (ln2) warm_out();
    if (ln2 && !slow) { SLEEP(OD2); rest_out(); }
    TSE();
    return;
  }
  uint32_t fx = 0, fv = 0, fn = 0, fo = 0, sa[9];
  float rs, hx, xs;
  u32x4 se;
  if constexpr (ROLE == 1) {
    f32x4 lh = (lane < 32) ? lo : hi;
    float fa = fold32(lh[0], lh[2]), fb2 = fold32(lh[1], lh[3]);
    if (__builtin_expect(__any(__builtin_isunordered(fa, fb2)) && !slow, 0)) {
      gsync0(ca + 4, 12);
      rows(ro + al, [] {});
      gsync0(ca + 4, 16);
      gu_ask();
      gate_up();
      lh = (lane < 32) ? lo : hi;
      fa = fold32(lh[0], lh[2]); fb2 = fold32(lh[1], lh[3]);
    }
    const float g = __int_as_float(__builtin_amdgcn_update_dpp(__float_as_int(fa), __float_as_int(fb2), 0x128, 0xf, 0xc, false));
    const float u = __int_as_float(__builtin_amdgcn_update_dpp(__float_as_int(fb2), __float_as_int(fa), 0x128, 0xf, 0x3, false));
    const uint32_t gu = pack2(g, u);
    const float gp = bflo(gu);
    const float sg = rbf(gp * __builtin_amdgcn_rcpf(1.f + __expf(-gp)));
    *(u16*)((char*)hm + ha) = f2bf(sg * bfhi(gu));
  } else if (LPOS) {
    DRAIN();
    gsync0(ca, 12);
    get_bd(0, 8);
  }
  BAR();
  TS(0, 6);
  if (ROLE == 2 || w == 2) {
    const u32x4 a0 = *(const u32x4*)((char*)hm + h0a), a1 = *(const u32x4*)((char*)hm + h1a);
    if (ROLE == 1) get_bd(0, 4);
    const f32x4 z = {0.f, 0.f, 0.f, 0.f};
    f32x4 o[4];
#pragma unroll
    for (int t = 0; t < 2 * ROLE; ++t) o[t] = MMB(a1, bw[2 * t + 1], MMB(a0, bw[2 * t], z));
#pragma unroll
    for (int t = 0; t < ROLE; ++t) {
      u32x4 sv;
#pragma unroll
      for (int r = 0; r < 4; ++r) sv[r] = min(pack2(o[2 * t][r], o[2 * t + 1][r]), 0xfffffffeu);
      ST16(so + 4096u * t, sv);
    }
    TS(0, 13);
  }
  if (OPOS == 1 && ROLE == 2) {
    warm_out();
    SLEEP(OSLEEP);
    if (!slow) rest_out();
  }
  if (ROLE == 1 && w == 1) {
    const int q = (fb & 15) >> 2, ts = 4 * (q & 1) + ((i & 7) >> 1);
    const int dd = 64 * (fb >> 4) + 16 * (2 * (i & 1) + (q >> 1)) + 4 * (fb & 3) + 2 * (kq & 1) + (kq >> 1);
    const uint32_t fkey = (uint32_t)__builtin_amdgcn_ds_bpermute(4 * ts, (int)mykey), fr = (uint32_t)__builtin_amdgcn_ds_bpermute(4 * ts, (int)ro);
    const int ft = 2047 - (int)(((fkey >> 2) & 0x7f8u) | (fkey & 7u));
    fo = (uint32_t)(ft * D_ + dd) * 2u;
    u32x2 fs;
    asm volatile("global_load_ushort %0, %4, %8\n\tglobal_load_ushort %1, %5, %9 sc1\n\tglobal_load_ushort %2, %6, %9 sc1\n\tglobal_load_dwordx2 %3, %7, %9 sc1"
                 : "=&v"(fx), "=&v"(fv), "=&v"(fn), "=&v"(fs) : "v"(fo), "v"(v_at(dd, ft >> 3) + 2u * (ft & 7)), "v"(O_XC + fr + 2u * dd), "v"(O_SC + 16u * dd), "s"(x), "s"(ws) : "memory");
    uint32_t t16 = (((T >> (3 * ((ft >> 1) & 3))) & LMASK) | (((T >> (12 + ((ft >> 1) & 3))) & 0x00010001u) << 14)) >> (16 * (ft & 1)) & 0xffffu;
    const uint32_t o = fkey >> 16;
    const float rs0 = bf2f((u16)((o & 0x8000u) ? (o & 0x7fffu) : (~o & 0xffffu)));
    rs = rbf(__builtin_amdgcn_rcpf(1.f + __expf(-rs0)));
    const uint32_t one = (i < 8) ? (0x3f80u << (16 * (i & 1))) : 0u;
    se = (u32x4){(i >> 1) == 0 ? one : 0u, (i >> 1) == 1 ? one : 0u, (i >> 1) == 2 ? one : 0u, (i >> 1) == 3 ? one : 0u};
#pragma unroll
    for (int m = 0; m < 9; ++m) sa[m] = O_SL + (uint32_t)(16 * m + (lane >> 2)) * SP + (uint32_t)(4 * fb + (lane & 3)) * 16u;
    asm volatile("" : "+v"(t16), "+v"(rs), "+v"(se), "+v"(sa[0]), "+v"(sa[1]), "+v"(sa[2]), "+v"(sa[3]), "+v"(sa[4]), "+v"(sa[5]), "+v"(sa[6]), "+v"(sa[7]), "+v"(sa[8]));
    SLEEP(SSLEEP);
    if (FD) until(tk + FD);
    TS(0, 1);
    float sum;
#pragma unroll 1
    for (int spin = 0; spin < SPIN_CAP; ++spin) {
      u32x4 sv[9];
#pragma unroll
      for (int m = 0; m < 9; ++m) LD16(sv[m], sa[m]);
      asm volatile("s_waitcnt vmcnt(9)" : "+v"(fx), "+v"(fv), "+v"(fn), "+v"(fs));
      if (__builtin_expect(__any(fs[0] != T), 0)) {
        for (int k = 0; k < SPIN_CAP && fs[0] != T; ++k)
          asm volatile("global_load_dwordx2 %0, %1, %2 sc1\n\ts_waitcnt vmcnt(0)" : "=&v"(fs) : "v"(O_SC + 16u * dd), "s"(ws) : "memory");
      }
      const uint32_t f1 = fv ^ t16, f2 = (f1 & 7u) ? fv & ~7u : f1;
      hx = rbf(bf2f((u16)fx) + (float)__builtin_bit_cast(_Float16, (u16)f2) * __uint_as_float(fs[1]));
      xs = bf2f((u16)fn);
      asm volatile("" : "+v"(hx), "+v"(xs));
      f32x4 dv = {0.f, 0.f, 0.f, 0.f};
      static_for<0, 9>([&](auto mm) {
        constexpr int m = mm.value;
        asm volatile("s_waitcnt vmcnt(%1)" : "+v"(sv[m]) : "n"(8 - m));
        dv = MMB(sv[m], se, dv);
        __builtin_amdgcn_sched_barrier(0);
      });
      sum = fold16(fold32(dv[0], dv[1]), fold32(dv[2], dv[3]));
      if (__builtin_expect(!__any(sum != sum), 1)) break;
      uint32_t mx = 0u;
#pragma unroll
      for (int m = 0; m < 9; ++m) mx = max(mx, max4(sv[m]));
      if (!__any(mx == 0xffffffffu)) break;
      TSC(18);
      SLEEP(PSLEEP);
    }
    const float o1 = rbf(xs + rbf(rbf(sum) * rs));
    if (i < 8) asm volatile("global_store_short %0, %1, %2 sc1" : : "v"(fo), "v"((uint32_t)f2bf(hx + o1)), "s"(out) : "memory");
    TS(0, 2);
  }
  TSE();
  }
  }
}
#define BROT "192"
#define CPF_A "s_getpc_b64 s[96:97]\n\ts_lshl_b32 s99, s2, 4\n\ts_lshl_b32 s84, s98, 7\n\ts_add_u32 s99, s99, s84\n\tv_and_b32 v112, 1, v0\n\t" \
              "v_lshl_add_u32 v112, v112, 12, s99\n\tv_min_u32 v112, kf_end-kf_all-0x80, v112\n\ts_mov_b64 exec, 3\n\tglobal_load_dword v112, v112, s[96:97]\n\ts_mov_b64 exec, -1\n\t"
extern "C" __global__ __launch_bounds__(512, 4) void kf_all(KARGS) {
  __shared__ __attribute__((aligned(16))) char smem[SMEM];
  asm volatile("s_load_dwordx16 s[64:79], s[0:1], 0x0\n\ts_load_dwordx4 s[80:83], s[0:1], 0x40\n\tv_readfirstlane_b32 s98, v0\n\t"
               "s_add_u32 s84, s2, " BROT "\n\ts_xor_b32 s84, s84, s2\n\ts_bitset0_b32 s84, 8\n\ts_bitset0_b32 s84, 9\n\ts_xor_b32 s2, s2, s84\n\t"
               "v_mov_b32 v109, 0\n\ts_and_b32 s85, s2, 7\n\ts_bitcmp1_b32 s2, 8\n\ts_cbranch_scc1 1f\n\t" CPF_A "s_bitcmp1_b32 s98, 8\n\ts_cbranch_scc1 2f\n\t"
               "s_andn2_b32 s84, s2, 31\n\ts_lshl2_add_u32 s84, s85, s84\n\ts_bfe_u32 s85, s2, 0x20003\n\ts_or_b32 s84, s84, s85\n\t"
               "s_mulk_i32 s84, 0x2400\n\ts_mulk_i32 s98, 36\n\ts_add_u32 s84, s84, s98\n\tv_and_b32 v110, 63, v0\n\tv_and_b32 v111, 32, v0\n\t"
               "v_lshlrev_b32 v111, 5, v111\n\tv_lshl_add_u32 v111, v110, 2, v111\n\tv_lshl_add_u32 v110, v110, 4, s84\n\tv_add_u32 v111, s84, v111\n\t"
               "s_cmpk_lt_u32 s2, 0x90\n\ts_waitcnt lgkmcnt(0)\n\t"
               "global_load_dwordx4 v[100:103], v110, s[64:65]\n\tglobal_load_dwordx4 v[104:107], v110, s[64:65] offset:1152\n\t"
               "global_load_dword v108, v111, s[64:65] offset:1024\n\tglobal_load_dword v109, v109, s[82:83] sc1\n\t"
               "s_cbranch_scc1 kf_n\n\ts_branch kf_h\n"
               "1:\n\ts_bfe_u32 s84, s2, 0x50003\n\ts_lshl_b32 s85, s85, 5\n\ts_or_b32 s84, s84, s85\n\ts_lshl_b32 s84, s84, 2\n\t"
               "v_and_b32 v110, 0xff, v0\n\tv_bfe_u32 v107, v0, 7, 1\n"
               "4:\n\ts_lshl_b32 s85, s84, 7\n\tv_lshl_add_u32 v110, v110, 1, s85\n\tv_lshl_add_u32 v107, v107, 1, s84\n\ts_and_b32 s85, s98, s2\n\ts_waitcnt lgkmcnt(0)\n\t"
               "global_load_ushort v100, v107, s[78:79]\n\tglobal_load_ushort v101, v110, s[66:67]\n\tglobal_load_ushort v102, v110, s[68:69]\n\t"
               "global_load_dword v109, v109, s[82:83] sc1\n\ts_bitcmp1_b32 s85, 8\n\ts_cbranch_scc1 kf_u\n\t"
               "global_load_ushort v103, v110, s[70:71]\n\tglobal_load_ushort v104, v110, s[72:73]\n\tglobal_load_ushort v105, v110, s[74:75]\n\t"
               "global_load_ushort v106, v110, s[76:77]\n\tglobal_load_ushort v107, v107, s[80:81]\n\ts_bitcmp1_b32 s2, 8\n\ts_cbranch_scc1 kf_c\n"
               "6:\n\ts_barrier\n\ts_branch kf_p\n"
               "2:\n\ts_cmpk_lt_u32 s2, 0xc0\n\ts_cbranch_scc1 6b\n\t"
               "s_lshr_b32 s84, s2, 3\n\ts_lshl3_add_u32 s84, s85, s84\n\ts_lshl1_add_u32 s84, s84, 976\n\t"
               "v_and_b32 v110, 0x7f, v0\n\tv_mov_b32 v107, 0\n\ts_branch 4b"
               : : "s"(__builtin_amdgcn_kernarg_segment_ptr()), "s"(blockIdx.x), "v"(threadIdx.x),
                   "s"((uint32_t)(uintptr_t)(__attribute__((address_space(3))) char*)smem) : "scc", "s96", "s97", "s98", "s99", "v127");
}
extern "C" __global__ __launch_bounds__(512, 4) void kf_c(KARGS) {
  __shared__ __attribute__((aligned(16))) char smem[SMEM];
  role_body<0>(smem, KPASS);
}
extern "C" __global__ __launch_bounds__(512, 4) void kf_u(KARGS) {
  __shared__ __attribute__((aligned(16))) char smem[SMEM];
  role_body<3>(smem, KPASS);
}
extern "C" __global__ __launch_bounds__(512, 4) void kf_n(KARGS) {
  __shared__ __attribute__((aligned(16))) char smem[SMEM];
  role_body<1, 1>(smem, KPASS);
}
extern "C" __global__ __launch_bounds__(512, 4) void kf_h(KARGS) {
  __shared__ __attribute__((aligned(16))) char smem[SMEM];
  role_body<1, 0>(smem, KPASS);
}
extern "C" __global__ __launch_bounds__(512, 4) void kf_p(KARGS) {
  __shared__ __attribute__((aligned(16))) char smem[SMEM];
  role_body<2>(smem, KPASS);
}
extern "C" __global__ __launch_bounds__(512, 4) void kf_conv() { asm volatile(CONV_ASM ::: CONV_CLOB); }
extern "C" __global__ void kf_end() {}

extern "C" void kernel_launch(void* const* d_in, const int* in_sizes, int n_in,
                              void* d_out, int out_size, void* d_ws, size_t ws_size,
                              hipStream_t stream) {
  kf_all<<<512, 512, 0, stream>>>((const u16*)d_in[0], (const u16*)d_in[1], (const u16*)d_in[2], (const u16*)d_in[3], (const u16*)d_in[4],
                                  (const u16*)d_in[5], (const u16*)d_in[6], (const u16*)d_in[7], (const u16*)d_in[8], (char*)d_ws + (size_t)WSOFF * 1048576,
                                  (const u16*)d_in[10], (const u16*)d_in[11], (const u16*)d_in[12], (const u16*)d_in[13], (u16*)d_out);
}
```
